```python
import torch

def _softmax(x, axis=-1):
    return torch.softmax(x, dim=axis)

def _gelu(x):
    return torch.nn.functional.gelu(x, approximate='tanh')

def _silu(x):
    return torch.nn.functional.silu(x)

def _sigmoid(x):
    return torch.sigmoid(x)

def _layernorm(x, axis=-1, eps=1e-05):
    m = x.mean(dim=axis, keepdim=True)
    v = x.var(dim=axis, keepdim=True, unbiased=False)
    return (x - m) / torch.sqrt(v + eps)

def _scatter_add(tgt, idx, src, axis=0):
    return tgt.clone().index_add_(axis, idx, src)

def _segment_sum(src, seg, n):
    out = torch.zeros((n,) + src.shape[1:], dtype=src.dtype)
    return out.index_add_(0, seg, src)

def _pad(x, pw):
    flat = tuple((v for pair in reversed(pw) for v in pair))
    return torch.nn.functional.pad(x, flat)
kernel_name = 'standalone__window_attn_davit__vision'

def setup_inputs():

    _w = lambda a: (lambda t: t.to(torch.bfloat16) if t.is_floating_point() else t)(torch.as_tensor(a))
    HW = 512
    C0 = 192
    ws = 7
    nh = 4
    H = W = max(HW // ws * ws, ws)
    C = max(C0 // nh * nh, nh)
    k = 3
    return {'x': _w((lambda *s: torch.randn(*s, dtype=torch.bfloat16) * 1.6)(1, H, W, C)), 'dw_pe': _w((lambda *s: torch.randn(*s, dtype=torch.bfloat16) * 1.6)(k, k, C) * 0.1), 'w_qkv': _w((lambda *s: torch.randn(*s, dtype=torch.bfloat16) * 1.6)(C, 3 * C) * C ** (-0.5)), 'w_o': _w((lambda *s: torch.randn(*s, dtype=torch.bfloat16) * 0.4)(C, C) * C ** (-0.5))}

def reference(x, dw_pe, w_qkv, w_o):
    B, H, W, C = x.shape
    ws = 7
    nh = 4
    dh = C // nh
    kk = dw_pe.shape[0]
    pad = kk // 2
    xp = _pad(x, ((0, 0), (pad, pad), (pad, pad), (0, 0)))
    pe = torch.zeros_like(x)
    for i in range(kk):
        for j in range(kk):
            pe = pe + xp[:, i:i + H, j:j + W, :] * dw_pe[i, j][None, None, None, :]
    x0 = x + pe
    h = _layernorm(x0, axis=-1)
    hw = h.reshape(B, H // ws, ws, W // ws, ws, C)
    hw = (lambda x, *d: x.permute(*d))(hw, 0, 1, 3, 2, 4, 5)
    hw = hw.reshape(-1, ws * ws, C)
    qkv = (hw @ w_qkv).reshape(-1, ws * ws, 3, nh, dh)
    q = qkv[:, :, 0].clone()
    k = qkv[:, :, 1].clone()
    v = qkv[:, :, 2].clone()
    scale = dh ** (-0.5)
    attn = torch.einsum('wnhd,wmhd->whnm', q, k) * scale
    attn = _softmax(attn, axis=-1)
    out = torch.einsum('whnm,wmhd->wnhd', attn, v)
    out = out.reshape(-1, ws * ws, C) @ w_o
    out = out.reshape(B, H // ws, W // ws, ws, ws, C)
    out = (lambda x, *d: x.permute(*d))(out, 0, 1, 3, 2, 4, 5)
    out = out.reshape(B, H, W, C)
    return x0 + out
```

```cpp
#include <hip/hip_runtime.h>
#include <hip/hip_bf16.h>
#include <stdint.h>

typedef short bf16x8 __attribute__((ext_vector_type(8)));
typedef float f32x4 __attribute__((ext_vector_type(4)));
typedef float f32x2 __attribute__((ext_vector_type(2)));
typedef unsigned u32x2 __attribute__((ext_vector_type(2)));
typedef unsigned u32x4 __attribute__((ext_vector_type(4)));

constexpr int IMG = 511;
constexpr int CH = 192;
constexpr int NWX = 73;
constexpr int NWIN = NWX * NWX;
constexpr int GRID = 256;
constexpr int ROWBYTES = IMG * CH * 2;
constexpr int XBIAS = (IMG + 1) * CH * 2;
constexpr unsigned OOB = 0x40000000u;

constexpr int FLAG_OFF = 7856;
constexpr int X_OFF = 32768;
constexpr int X1_OFF = 53632;
constexpr int O_OFF = 74496;
constexpr int V_OFF = 92928;
constexpr int KQL_OFF = 111360;
constexpr int OL_OFF = 128256;
constexpr int AT_OFF = 137472;
constexpr int CT_OFF = AT_OFF + 19584;
constexpr int RS_OFF = CT_OFF + 4096;
constexpr int WL_OFF = RS_OFF + 2304 + 64;
constexpr int LDS_BYTES = WL_OFF + 128;
#define RS_BASE (RS_OFF + (wave < 4 ? wave * 384 : 768 + wave * 192))
constexpr int STL_OFF = X1_OFF + 18816;
#define LB_OFF(b) ((b) < 7 ? V_OFF + (b) * 6160 : X1_OFF + ((b) - 7) * 6160)
#define LPOS(wc) (((wc) & 1) ? 0 : 6)
constexpr unsigned CROW = 0x060a0230u;
constexpr unsigned CLANE = 0x00132C3Fu;

constexpr unsigned long long TOKMAP[6] = {0x20001c190f2a2202ull, 0x052517121f061a13ull, 0x181e08151d0b260cull, 0x1b2b2903102d0901ull, 0x04162e0e2824142cull, 0x110a072127232f0dull};
constexpr unsigned long long ROWMAP[8] = {0x08321b240f1c0006ull, 0x23310e2a09170d14ull, 0x210c2f030a273418ull, 0x1d2b11221f043739ull, 0x3234363a103e192dull, 0x291a20022c3f1605ull, 0x303a3c2628122e15ull, 0x39253d1e01070b13ull};

constexpr int WA = 12, WB = 4;
constexpr int WF_BYTES = 8 * 36 * 1024;
constexpr int TB_OFF = WF_BYTES + 8192;
constexpr int WS_BYTES = TB_OFF + 24576;

#define MFMA32(a, b, c) __builtin_amdgcn_mfma_f32_16x16x32_bf16((a), (b), (c), 0, 0, 0)
#define BC8(v) __builtin_bit_cast(bf16x8, (v))
#define BAR() asm volatile("s_waitcnt lgkmcnt(0)\n\ts_barrier" ::: "memory")
#define SB() __builtin_amdgcn_sched_barrier(0)
#define OPQ(x) asm volatile("" : "+v"(x))
#define ZERO4 ((f32x4){0.f, 0.f, 0.f, 0.f})

typedef __bf16 nbf16x2 __attribute__((ext_vector_type(2)));
__device__ __forceinline__ unsigned pk2(float a, float b) {
    f32x2 v = {a, b};
    nbf16x2 r = __builtin_convertvector(v, nbf16x2);
    return __builtin_bit_cast(unsigned, r);
}
__device__ __forceinline__ float bflo(unsigned u) { return __uint_as_float(u << 16); }
__device__ __forceinline__ float bfhi(unsigned u) { return __uint_as_float(u & 0xffff0000u); }

__device__ __forceinline__ float bf2f(unsigned short v) { return __uint_as_float((unsigned)v << 16); }
__device__ __forceinline__ unsigned byte_of(unsigned long long a, unsigned long long b, int i) {
    return (unsigned)(((i & 8) ? b : a) >> (8 * (i & 7))) & 0xFFu;
}
__device__ __forceinline__ void seq_to_win(int seq, int& wy, int& wx) {
    const bool last = seq >= 5110;
    const int s = last ? 35 : seq / 146, r = seq - 146 * s;
    const int kx = last ? r / 6 : r >> 2, pos = r - (last ? 6 : 4) * kx;
    const bool low = last && pos >= (kx == 36 ? 2 : 4);
    wy = low ? 72 : 2 * s + (pos & 1);
    wx = 2 * kx + (low ? (kx == 36 ? 0 : pos - 4) : pos >> 1);
}
__device__ __forceinline__ int win_to_seq(int wy, int wx) {
    const int kx = wx >> 1;
    return wy == 72 ? 5110 + 6 * kx + (kx == 36 ? 2 : 4 + (wx & 1)) : 146 * (wy >> 1) + (wy >= 70 ? 6 : 4) * kx + 2 * (wx & 1) + (wy & 1);
}
__device__ __forceinline__ int block_of(int wy, int wx) { return wy >= 70 ? 1295 + (wx & ~1) + (wy == 72) : 37 * (wy >> 1) + (wx >> 1); }
__device__ __forceinline__ void block_pos(int b, int& ky, int& kx) {
    if (b >= 1295) { kx = (b - 1295) >> 1; ky = 70 + 2 * ((b - 1295) & 1); }
    else { const int s = b / 37; kx = b - 37 * s; ky = 2 * s; }
}
__device__ __forceinline__ unsigned wl_entry(int seq) {
    int wy, wx;
    seq_to_win(seq, wy, wx);
    return (unsigned)((wy * 7 * IMG + wx * 7) * (CH * 2)) | (wy == 0 ? 0x08000000u : 0u) | (wx == 0 ? 0x10000000u : 0u) |
           (wx == NWX - 1 ? 0x20000000u : 0u) | ((unsigned)(wx & 1) << 30) | ((unsigned)(wy & 1) << 31);
}
__device__ __forceinline__ unsigned dma_off(int lane) {
    const int col = (lane * 43) >> 8;
    return (unsigned)(col * (CH * 2 - 96) + 16 * lane) | (col == 0 ? 0x10000000u : 0u) | (col == 8 ? 0x20000000u : 0u);
}

__global__ __launch_bounds__(384) void prep_weights(const unsigned short* __restrict__ wqkv, const unsigned short* __restrict__ wo,
                                                    const unsigned short* __restrict__ dw, u32x4* __restrict__ wf) {
    __shared__ float red[24][16];
    const int tid = threadIdx.x;
    const int blk = blockIdx.x;
    if (blk == 48) return;
    if (blk >= 49) {
        const int i = (blk - 49) * 384 + tid;
        const int b = i * 4;
        unsigned v = 0;
        if (b < 18688) {
            const int dy = b < 6144 ? 0 : (b < 12544 ? 1 : 2);
            const int r = b - (dy == 0 ? 0 : (dy == 1 ? 6208 : 12544));
            if (r >= 0 && r < 6144) {
                const int dxo = r / 3072, ch = (r % 3072) >> 4, dx = ((r >> 2) & 3) - dxo;
                if (dx >= 0 && dx < 3) v = dw[(dy * 3 + dx) * 192 + ch] | ((dy == 1 && dx == 1) ? 0x3F800000u : 0u);
            }
        } else if (b >= 19712 && b < 23808) {
            const int k = (b - 19712) >> 10, lane = ((b - 19712) >> 4) & 63, e = i & 3;
            if (k == 0) {
                if (e < 3) {
                    const int l15 = lane & 15, g = lane >> 4;
                    const int tk = byte_of(e == 0 ? TOKMAP[0] : (e == 1 ? TOKMAP[2] : TOKMAP[4]), e == 0 ? TOKMAP[1] : (e == 1 ? TOKMAP[3] : TOKMAP[5]), l15);
                    const int ty = tk / 7, tx = tk - 7 * ty;
                    v = (unsigned)(ty * ROWBYTES + tx * (CH * 2)) + 16 * g;
                }
            } else v = dma_off(lane);
        }
        ((unsigned*)((char*)wf + TB_OFF))[i] = v;
        return;
    }
    const int wave = blk / 6, mdt = blk % 6;
    const int m = mdt / 3, dt = mdt % 3;
    const int ks = tid >> 6, lane = tid & 63;
    const int n = lane & 15, g = lane >> 4;
    const int w = wave & 3;
    const bool isO = wave >= 6;
    float v[8];
    if (!isO) {
        int col = (wave < 4) ? (m == 0 ? 192 : 0) + 48 * w + 16 * dt + n : 384 + 96 * w + 16 * mdt + n;
#pragma unroll
        for (int j = 0; j < 8; ++j) {
            int pos = 32 * ks + 8 * g + j;
            int row = 48 * ((pos >> 4) & 3) + 16 * (pos >> 6) + (pos & 15);
            v[j] = bf2f(wqkv[row * 576 + col]);
        }
    } else {
        int col = 96 * (w - 2) + 32 * (mdt >> 1) + 8 * (n >> 2) + 4 * (mdt & 1) + (n & 3);
#pragma unroll
        for (int j = 0; j < 8; ++j) {
            int pos = 32 * ks + 8 * g + j;
            int row = 48 * ((pos >> 4) & 3) + 16 * (pos >> 6) + (pos & 15);
            v[j] = bf2f(wo[row * 192 + col]);
        }
    }
    float s = 0.f;
#pragma unroll
    for (int j = 0; j < 8; ++j) s += v[j];
    red[ks * 4 + g][n] = s;
    __syncthreads();
    float mean = 0.f;
    if (!isO) {
#pragma unroll
        for (int i = 0; i < 24; ++i) mean += red[i][n];
        mean *= (1.f / 192.f);
    }
    u32x4 o;
#pragma unroll
    for (int j = 0; j < 4; ++j) o[j] = pk2(v[2 * j] - mean, v[2 * j + 1] - mean);
    wf[(wave * 36 + mdt * 6 + ks) * 64 + lane] = o;
}

__device__ __forceinline__ float rstd_of(const char* p) {
    f32x4 p0 = *(const f32x4*)(p);
    f32x4 p1 = *(const f32x4*)(p + 1024);
    float s = (p0[0] + p0[2]) + (p1[0] + p1[2]);
    float q = (p0[1] + p0[3]) + (p1[1] + p1[3]);
    float mean = s * (1.f / 192.f);
    float var = __builtin_fmaf(q, 1.f / 192.f, -mean * mean);
    var = __builtin_fmaxf(var, 0.f);
    return __builtin_amdgcn_rsqf(var + 1e-5f);
}

#if defined(__HIP_DEVICE_COMPILE__)
#define NOPK __attribute__((target("no-packed-fp32-ops")))
#else
#define NOPK
#endif
__global__ __launch_bounds__(512, 2) NOPK void win_attn_kernel(const unsigned short* __restrict__ x,
                                                          const u32x4* wf,
                                                          unsigned short* __restrict__ out) {
    __shared__ __attribute__((aligned(16))) char smem[LDS_BYTES];

    asm volatile("wk_code_begin:");
    const int tid = threadIdx.x;
    const int wave = __builtin_amdgcn_readfirstlane(tid >> 6);
    const int w = wave & 3;
    const int q0 = (blockIdx.x & 7) * 32 + (blockIdx.x >> 3);
    const int start = (q0 * NWIN) >> 8;
    const int cnt = (((q0 + 1) * NWIN) >> 8) - start;

    const unsigned en0 = __builtin_amdgcn_readfirstlane(wl_entry(start));
    const auto wrsrc = __builtin_amdgcn_make_buffer_rsrc((void*)wf, 0, WS_BYTES, 0x00020000);
    const auto xrsrc = __builtin_amdgcn_make_buffer_rsrc((void*)((const char*)x - XBIAS), 0, IMG * IMG * CH * 2 + XBIAS, 0x00020000);
    bf16x8 W[36];
#define LOAD_W(j0, j1, k0, k1)                                                                     \
    _Pragma("unroll") for (int ks_ = (k0); ks_ < (k1); ++ks_)                                      \
        _Pragma("unroll") for (int j_ = (j0); j_ < (j1); ++j_)                                     \
            W[j_ * 6 + ks_] = BC8(__builtin_amdgcn_raw_buffer_load_b128(wrsrc, lane * 16, wave * 36864 + (j_ * 6 + ks_) * 1024, 0));

#define LOAD_WI(i0, i1)                                                                            \
    _Pragma("unroll") for (int i_ = (i0); i_ < (i1); ++i_)                                         \
        W[i_] = BC8(__builtin_amdgcn_raw_buffer_load_b128(wrsrc, lane * 16, wave * 36864 + i_ * 1024, 0));

#define LOAD_WL(i0, i1)                                                                            \
    _Pragma("unroll") for (int i_ = (i0); i_ < (i1); ++i_)                                         \
        W[i_] = BC8(__builtin_amdgcn_raw_buffer_load_b128(wrsrc, lane * 16, wl_ + i_ * 1024, 0));

#define LANE_SETUP                                                                                 \
    int lane;                                                                                      \
    asm volatile("v_mbcnt_lo_u32_b32 %0, -1, 0\n\tv_mbcnt_hi_u32_b32 %0, -1, %0" : "=v"(lane));    \
    __builtin_assume(lane >= 0 && lane < 64);                                                      \
    const int g = lane >> 4, l15 = lane & 15, sw = l15 >> 1;                                       \
    int fE = X_OFF + l15 * 384 + ((g ^ sw) << 4);                                                  \
    int fO = fE ^ 64;                                                                              \
    OPQ(fE); OPQ(fO);
#define FRAG(base, t, ks) (*(const bf16x8*)(smem + (((ks) & 1) ? fO : fE) + ((base) - X_OFF) + (t) * 6144 + ((ks) >> 1) * 128))
#define RSTD_PUBLISH(ln_, stoff)                                                                   \
    *(float*)(smem + ((ln_) < 48 ? RS_BASE : RS_OFF + 2304 - 192) + (ln_) * 4) = rstd_of(smem + (stoff) + (ln_) * 16);

    const unsigned ldsb = (unsigned)(size_t)smem;
#define DMA_SLICE(en)                                                                              \
        {                                                                                          \
            const unsigned vo_ = dma_off(lane) & (0x401fffffu | ((en) & 0x38000000u));             \
            const unsigned so_ = ((en) & 0x7ffffffu) + 96 * w;                                     \
            _Pragma("unroll") for (int r = 0; r < 9; ++r) {                                        \
                unsigned m0_ = ldsb + w * 8192 + r * 872;                                          \
                unsigned sr_ = (r == 0 && ((en) & 0x08000000u)) ? OOB : so_ + r * ROWBYTES;        \
                asm volatile("s_mov_b32 m0, %0\n\ts_mov_b32 exec_hi, 0x3fffff\n\tbuffer_load_dwordx4 %1, %2, %3 offen lds\n\ts_mov_b32 exec_hi, -1" \
                             :: "s"(m0_), "v"(vo_), "s"(xrsrc), "s"(sr_) : "m0", "memory");        \
            }                                                                                      \
        }
#define XPAT(r) (((r) < 48 ? (r) : 48) * 384 + (((2 * w) ^ (((r) >> 1) & 7)) << 4))
#define SPAT(r) ((w >> 1) * 1024 + (w & 1) * 8 + (r) * 16)
#define CONV_CONSTS(var) \
            const unsigned long long rlo = g == 0 ? ROWMAP[0] : (g == 1 ? ROWMAP[2] : (g == 2 ? ROWMAP[4] : ROWMAP[6])); \
            const unsigned long long rhi = g == 0 ? ROWMAP[1] : (g == 1 ? ROWMAP[3] : (g == 2 ? ROWMAP[5] : ROWMAP[7])); \
            int xrow = byte_of(rlo, rhi, l15); \
            if (lane == (int)((CLANE >> (8 * (var))) & 0xFF)) xrow = 48; \
            if (lane == 63) xrow = (CROW >> (8 * (var))) & 0xFF; \
            const unsigned pXw_ = XPAT(xrow); \
            const unsigned pSTw_ = (w >> 1) * 1024 + (w & 1) * 8 + xrow * 16; \
            const int oy = (0x5320 >> (4 * (l15 >> 2))) & 15, ox = (0x5320 >> (4 * (l15 & 3))) & 15; \
            const unsigned aB_ = ldsb + w * 8192 + (oy + (g >> 1)) * 872 + (ox + 2 * (g & 1)) * 96; \
            const int dyi = g - (l15 >> 3); \
            const unsigned aA_ = ldsb + AT_OFF + (l15 & 3) * 16 + \
                                 ((dyi < 0 || dyi > 2) ? 18816 : (dyi == 0 ? 0 : (dyi == 1 ? 6208 : 12544)) + ((l15 >> 2) & 1) * 3072 + w * 768); \
            const unsigned idx_ = (l15 & 3) * 0x5555u; \
            const unsigned se = (l15 == 4 * g) ? 0x3F803F80u : 0u; \
            const u32x4 sel4 = {se, se, se, se};

#define LANE_IDS int ln_ = lane; OPQ(ln_); const int l15_ = ln_ & 15;
#define B_PROLOGUE(ISV) \
        LANE_SETUP \
        const auto orsrc = __builtin_amdgcn_make_buffer_rsrc((void*)out, 0, IMG * IMG * CH * 2, 0x00020000); \
        __builtin_amdgcn_s_setprio(1); \
        const unsigned en1 = __builtin_amdgcn_readfirstlane(wl_entry(start + 1)); \
        int key0, nblk; \
        { \
            int wy, wx; \
            seq_to_win(start, wy, wx); \
            key0 = block_of(wy, wx); \
            seq_to_win(start + cnt - 1, wy, wx); \
            nblk = block_of(wy, wx) - key0 + 1; \
            asm volatile("s_barrier" ::: "memory"); \
            { \
                unsigned long long pc; \
                unsigned len, rel; \
                asm volatile("s_getpc_b64 %0\n.Lwk_here%=:\n\ts_mov_b32 %1, wk_code_end-wk_code_begin\n\ts_mov_b32 %2, .Lwk_here%=-wk_code_begin" : "=s"(pc), "=s"(len), "=s"(rel)); \
                pc -= rel; \
                const u32x4 cr = {(unsigned)pc, (unsigned)(pc >> 32) & 0xffffu, len, 0x00020000u}; \
                const int ci = (blockIdx.x >> 3) * 4 + w; \
                unsigned m0_ = ldsb + O_OFF + 12288 + w * 1024, vo_ = lane * 16; \
                asm volatile("s_mov_b32 m0, %0\n\ts_nop 0\n\tbuffer_load_dwordx4 %1, %2, %3 offen lds\n\tbuffer_load_dwordx4 %1, %4, %3 offen lds\n\t" \
                             "buffer_load_dwordx4 %1, %4, %5 offen lds\n\tbuffer_load_dwordx4 %1, %4, %6 offen lds" \
                             :: "s"(m0_), "v"(vo_), "s"(cr), "s"(ci * 1024), "s"(wrsrc), "s"(131072 + ci * 1024), "s"(262144 + ci * 1024) : "m0", "memory"); \
            } \
            _Pragma("unroll") for (int k = 0; k < 3; ++k) { \
                const int b = w + 4 * k; \
                if (b < nblk) { \
                    int ky, kx; \
                    block_pos(key0 + b, ky, kx); \
                    const unsigned org = (unsigned)(((7 * ky + 5) * IMG + 14 * kx + 5) * (CH * 2) + XBIAS); \
                    const unsigned org2 = (kx == 36) ? OOB : org; \
                    const unsigned vo_ = lane * 16; \
                    _Pragma("unroll") for (int r = 0; r < 4; ++r) { \
                        unsigned m0_ = ldsb + LB_OFF(b) + r * 1536; \
                        asm volatile("s_mov_b32 m0, %0\n\ts_mov_b64 exec, %5\n\tbuffer_load_dwordx4 %1, %2, %3 offen lds\n\t" \
                                     "buffer_load_dwordx4 %1, %2, %4 offen offset:768 lds\n\ts_mov_b64 exec, -1" \
                                     :: "s"(m0_), "v"(vo_), "s"(xrsrc), "s"(org + r * ROWBYTES), "s"(org2 + r * ROWBYTES), "s"(0xFFFFFFFFFFFFull) : "m0", "memory"); \
                    } \
                } \
            } \
        } \
        const int wl_ = (ISV) ? wave * 36864 : (wave - 2) * 36864 + 18432; \
        LOAD_WL(0, WB) \
        u32x4 xl[9]; \
        { \
            CONV_CONSTS(0) \
            const int b = l15 < nblk ? l15 : 0; \
            int ky, kx; \
            block_pos(key0 + b, ky, kx); \
            const int wy = ky + (g >> 1), wx = 2 * kx + (g & 1), i = win_to_seq(wy, wx) - start; \
            const bool ok = wy < NWX && wx < NWX && i >= 0 && i < cnt && l15 < nblk; \
            const int row = ok ? i : 47; \
            const unsigned lB = ldsb + LB_OFF(b) + (g >> 1) * 1536 + (g & 1) * 768 + 96 * w; \
            const unsigned lX = XPAT(row), lS = SPAT(row) + (unsigned)(STL_OFF - O_OFF - 18816); \
            const unsigned sol = ok ? (unsigned)(((7 * wy + LPOS(wy)) * IMG + 7 * wx + LPOS(wx)) * (CH * 2) + 96 * w) : OOB; \
            xl[6] = (u32x4){sol, 0u, 0u, 0u}; xl[7] = xl[8] = (u32x4){0u, 0u, 0u, 0u}; \
            asm volatile("s_waitcnt vmcnt(%0)" :: "n"(WB) : "memory"); \
            asm volatile( \
            "s_waitcnt lgkmcnt(0)\n\t" \
            "s_barrier\n\t" \
            "s_nop 7\n\t" \
            "ds_read_b128 v[168:171], v254\n\t" \
            "ds_read_b64 v[144:145], v253\n\t" \
            "ds_read_b64 v[146:147], v253 offset:384\n\t" \
            "ds_read_b64 v[148:149], v253 offset:3072\n\t" \
            "ds_read_b64 v[150:151], v253 offset:3456\n\t" \
            "ds_read_b128 v[172:175], v254 offset:64\n\t" \
            "ds_read_b64 v[152:153], v253 offset:8\n\t" \
            "ds_read_b64 v[154:155], v253 offset:392\n\t" \
            "ds_read_b64 v[156:157], v253 offset:3080\n\t" \
            "ds_read_b64 v[158:159], v253 offset:3464\n\t" \
            "ds_read_b64 v[160:161], v253 offset:16\n\t" \
            "ds_read_b64 v[162:163], v253 offset:400\n\t" \
            "ds_read_b64 v[164:165], v253 offset:3088\n\t" \
            "ds_read_b64 v[166:167], v253 offset:3472\n\t" \
            "v_mov_b64 v[176:177], 0\n\t" \
            "v_mov_b64 v[178:179], 0\n\t" \
            "v_mov_b64 v[180:181], 0\n\t" \
            "v_mov_b64 v[182:183], 0\n\t" \
            "v_mov_b64 v[184:185], 0\n\t" \
            "v_mov_b64 v[186:187], 0\n\t" \
            "v_mov_b64 v[188:189], 0\n\t" \
            "v_mov_b64 v[190:191], 0\n\t" \
            "v_add_u32 v236, s67, v250\n\t" \
            "v_xor_b32 v237, 16, v236\n\t" \
            "v_add_u32 v248, s67, v251\n\t" \
            "s_waitcnt lgkmcnt(9)\n\t" \
            "v_smfmac_f32_16x16x64_bf16 v[176:179], v[168:171], v[144:151], v255\n\t" \
            "ds_read_b128 v[168:171], v254 offset:128\n\t" \
            "ds_read_b64 v[144:145], v253 offset:24\n\t" \
            "s_waitcnt lgkmcnt(6)\n\t" \
            "v_smfmac_f32_16x16x64_bf16 v[180:183], v[172:175], v[152:159], v255\n\t" \
            "ds_read_b64 v[146:147], v253 offset:408\n\t" \
            "ds_read_b64 v[148:149], v253 offset:3096\n\t" \
            "ds_read_b64 v[150:151], v253 offset:3480\n\t" \
            "ds_read_b128 v[172:175], v254 offset:192\n\t" \
            "ds_read_b64 v[152:153], v253 offset:32\n\t" \
            "ds_read_b64 v[154:155], v253 offset:416\n\t" \
            "ds_read_b64 v[156:157], v253 offset:3104\n\t" \
            "ds_read_b64 v[158:159], v253 offset:3488\n\t" \
            "v_cvt_pk_bf16_f32 v200, v176, v177\n\t" \
            "v_cvt_pk_bf16_f32 v201, v178, v179\n\t" \
            "v_mov_b64 v[176:177], 0\n\t" \
            "v_mov_b64 v[178:179], 0\n\t" \
            "v_mfma_f32_4x4x4_16b_bf16 v[196:199], v[200:201], v[200:201], 0\n\t" \
            "v_cvt_pk_bf16_f32 v202, v180, v181\n\t" \
            "v_cvt_pk_bf16_f32 v203, v182, v183\n\t" \
            "v_mov_b64 v[180:181], 0\n\t" \
            "v_mov_b64 v[182:183], 0\n\t" \
            "v_mfma_f32_4x4x4_16b_bf16 v[196:199], v[202:203], v[202:203], v[196:199]\n\t" \
            "ds_write_b128 v236, v[200:203]\n\t" \
            "v_mfma_f32_16x16x32_bf16 v[192:195], v[238:241], v[200:203], 0\n\t" \
            "s_waitcnt lgkmcnt(10)\n\t" \
            "v_smfmac_f32_16x16x64_bf16 v[184:187], v[168:171], v[160:167], v255\n\t" \
            "ds_read_b128 v[168:171], v254 offset:256\n\t" \
            "ds_read_b64 v[160:161], v253 offset:40\n\t" \
            "ds_read_b64 v[162:163], v253 offset:424\n\t" \
            "ds_read_b64 v[164:165], v253 offset:3112\n\t" \
            "s_waitcnt lgkmcnt(9)\n\t" \
            "v_smfmac_f32_16x16x64_bf16 v[188:191], v[172:175], v[144:151], v255\n\t" \
            "ds_read_b64 v[166:167], v253 offset:3496\n\t" \
            "ds_read_b128 v[172:175], v254 offset:320\n\t" \
            "ds_read_b64 v[144:145], v253 offset:48\n\t" \
            "ds_read_b64 v[146:147], v253 offset:432\n\t" \
            "ds_read_b64 v[148:149], v253 offset:3120\n\t" \
            "ds_read_b64 v[150:151], v253 offset:3504\n\t" \
            "v_cvt_pk_bf16_f32 v204, v184, v185\n\t" \
            "v_cvt_pk_bf16_f32 v205, v186, v187\n\t" \
            "v_mov_b64 v[184:185], 0\n\t" \
            "v_mov_b64 v[186:187], 0\n\t" \
            "v_mfma_f32_4x4x4_16b_bf16 v[196:199], v[204:205], v[204:205], v[196:199]\n\t" \
            "v_cvt_pk_bf16_f32 v206, v188, v189\n\t" \
            "v_cvt_pk_bf16_f32 v207, v190, v191\n\t" \
            "v_mov_b64 v[188:189], 0\n\t" \
            "v_mov_b64 v[190:191], 0\n\t" \
            "v_mfma_f32_4x4x4_16b_bf16 v[196:199], v[206:207], v[206:207], v[196:199]\n\t" \
            "s_waitcnt lgkmcnt(14)\n\t" \
            "ds_write_b128 v237, v[204:207]\n\t" \
            "v_mfma_f32_16x16x32_bf16 v[192:195], v[238:241], v[204:207], v[192:195]\n\t" \
            "s_waitcnt lgkmcnt(10)\n\t" \
            "v_smfmac_f32_16x16x64_bf16 v[176:179], v[168:171], v[152:159], v255\n\t" \
            "ds_read_b128 v[168:171], v254 offset:384\n\t" \
            "ds_read_b64 v[152:153], v253 offset:56\n\t" \
            "ds_read_b64 v[154:155], v253 offset:440\n\t" \
            "ds_read_b64 v[156:157], v253 offset:3128\n\t" \
            "s_waitcnt lgkmcnt(9)\n\t" \
            "v_smfmac_f32_16x16x64_bf16 v[180:183], v[172:175], v[160:167], v255\n\t" \
            "ds_read_b64 v[158:159], v253 offset:3512\n\t" \
            "ds_read_b128 v[172:175], v254 offset:448\n\t" \
            "ds_read_b64 v[160:161], v253 offset:64\n\t" \
            "ds_read_b64 v[162:163], v253 offset:448\n\t" \
            "ds_read_b64 v[164:165], v253 offset:3136\n\t" \
            "ds_read_b64 v[166:167], v253 offset:3520\n\t" \
            "v_cvt_pk_bf16_f32 v208, v176, v177\n\t" \
            "v_cvt_pk_bf16_f32 v209, v178, v179\n\t" \
            "v_mov_b64 v[176:177], 0\n\t" \
            "v_mov_b64 v[178:179], 0\n\t" \
            "v_mfma_f32_4x4x4_16b_bf16 v[196:199], v[208:209], v[208:209], v[196:199]\n\t" \
            "v_cvt_pk_bf16_f32 v210, v180, v181\n\t" \
            "v_cvt_pk_bf16_f32 v211, v182, v183\n\t" \
            "v_mov_b64 v[180:181], 0\n\t" \
            "v_mov_b64 v[182:183], 0\n\t" \
            "v_mfma_f32_4x4x4_16b_bf16 v[196:199], v[210:211], v[210:211], v[196:199]\n\t" \
            "s_waitcnt lgkmcnt(14)\n\t" \
            "ds_write_b128 v236, v[208:211] offset:128\n\t" \
            "v_mfma_f32_16x16x32_bf16 v[192:195], v[238:241], v[208:211], v[192:195]\n\t" \
            "s_waitcnt lgkmcnt(10)\n\t" \
            "v_smfmac_f32_16x16x64_bf16 v[184:187], v[168:171], v[144:151], v255\n\t" \
            "ds_read_b128 v[168:171], v254 offset:512\n\t" \
            "ds_read_b64 v[144:145], v253 offset:72\n\t" \
            "ds_read_b64 v[146:147], v253 offset:456\n\t" \
            "ds_read_b64 v[148:149], v253 offset:3144\n\t" \
            "s_waitcnt lgkmcnt(9)\n\t" \
            "v_smfmac_f32_16x16x64_bf16 v[188:191], v[172:175], v[152:159], v255\n\t" \
            "ds_read_b64 v[150:151], v253 offset:3528\n\t" \
            "ds_read_b128 v[172:175], v254 offset:576\n\t" \
            "ds_read_b64 v[152:153], v253 offset:80\n\t" \
            "ds_read_b64 v[154:155], v253 offset:464\n\t" \
            "ds_read_b64 v[156:157], v253 offset:3152\n\t" \
            "ds_read_b64 v[158:159], v253 offset:3536\n\t" \
            "v_cvt_pk_bf16_f32 v212, v184, v185\n\t" \
            "v_cvt_pk_bf16_f32 v213, v186, v187\n\t" \
            "v_mov_b64 v[184:185], 0\n\t" \
            "v_mov_b64 v[186:187], 0\n\t" \
            "v_mfma_f32_4x4x4_16b_bf16 v[196:199], v[212:213], v[212:213], v[196:199]\n\t" \
            "v_cvt_pk_bf16_f32 v214, v188, v189\n\t" \
            "v_cvt_pk_bf16_f32 v215, v190, v191\n\t" \
            "v_mov_b64 v[188:189], 0\n\t" \
            "v_mov_b64 v[190:191], 0\n\t" \
            "v_mfma_f32_4x4x4_16b_bf16 v[196:199], v[214:215], v[214:215], v[196:199]\n\t" \
            "s_waitcnt lgkmcnt(14)\n\t" \
            "ds_write_b128 v237, v[212:215] offset:128\n\t" \
            "v_mfma_f32_16x16x32_bf16 v[192:195], v[238:241], v[212:215], v[192:195]\n\t" \
            "s_waitcnt lgkmcnt(10)\n\t" \
            "v_smfmac_f32_16x16x64_bf16 v[176:179], v[168:171], v[160:167], v255\n\t" \
            "ds_read_b128 v[168:171], v254 offset:640\n\t" \
            "ds_read_b64 v[160:161], v253 offset:88\n\t" \
            "ds_read_b64 v[162:163], v253 offset:472\n\t" \
            "ds_read_b64 v[164:165], v253 offset:3160\n\t" \
            "s_waitcnt lgkmcnt(9)\n\t" \
            "v_smfmac_f32_16x16x64_bf16 v[180:183], v[172:175], v[144:151], v255\n\t" \
            "ds_read_b64 v[166:167], v253 offset:3544\n\t" \
            "ds_read_b128 v[172:175], v254 offset:704\n\t" \
            "v_cvt_pk_bf16_f32 v216, v176, v177\n\t" \
            "v_cvt_pk_bf16_f32 v217, v178, v179\n\t" \
            "s_nop 1\n\t" \
            "v_mfma_f32_4x4x4_16b_bf16 v[196:199], v[216:217], v[216:217], v[196:199]\n\t" \
            "s_nop 0\n\t" \
            "v_cvt_pk_bf16_f32 v218, v180, v181\n\t" \
            "v_cvt_pk_bf16_f32 v219, v182, v183\n\t" \
            "s_nop 1\n\t" \
            "v_mfma_f32_4x4x4_16b_bf16 v[196:199], v[218:219], v[218:219], v[196:199]\n\t" \
            "ds_write_b128 v236, v[216:219] offset:256\n\t" \
            "v_mfma_f32_16x16x32_bf16 v[192:195], v[238:241], v[216:219], v[192:195]\n\t" \
            "s_waitcnt lgkmcnt(6)\n\t" \
            "v_smfmac_f32_16x16x64_bf16 v[184:187], v[168:171], v[152:159], v255\n\t" \
            "s_waitcnt lgkmcnt(1)\n\t" \
            "v_smfmac_f32_16x16x64_bf16 v[188:191], v[172:175], v[160:167], v255\n\t" \
            "s_nop 5\n\t" \
            "v_cvt_pk_bf16_f32 v220, v184, v185\n\t" \
            "v_cvt_pk_bf16_f32 v221, v186, v187\n\t" \
            "s_nop 1\n\t" \
            "v_mfma_f32_4x4x4_16b_bf16 v[196:199], v[220:221], v[220:221], v[196:199]\n\t" \
            "v_cvt_pk_bf16_f32 v222, v188, v189\n\t" \
            "v_cvt_pk_bf16_f32 v223, v190, v191\n\t" \
            "s_nop 1\n\t" \
            "v_mfma_f32_4x4x4_16b_bf16 v[196:199], v[222:223], v[222:223], v[196:199]\n\t" \
            "ds_write_b128 v237, v[220:223] offset:256\n\t" \
            "v_mfma_f32_16x16x32_bf16 v[192:195], v[238:241], v[220:223], v[192:195]\n\t" \
            "s_nop 7\n\t" \
            "v_cndmask_b32_e64 v193, v196, v197, s[60:61]\n\t" \
            "v_cndmask_b32_e64 v193, v193, v198, s[62:63]\n\t" \
            "v_cndmask_b32_e64 v193, v193, v199, s[64:65]\n\t" \
            "ds_write_b64 v248, v[192:193] offset:18816\n\t" \
            "s_waitcnt lgkmcnt(0)\n\t" \
            "s_nop 7\n\t" \
            : "={v[200:203]}"(xl[0]), "={v[204:207]}"(xl[1]), "={v[208:211]}"(xl[2]), "={v[212:215]}"(xl[3]), "={v[216:219]}"(xl[4]), "={v[220:223]}"(xl[5]) \
            : "{v250}"(lX), "{v251}"(lS), "{v253}"(lB), "{v254}"(aA_), "{v255}"(idx_), "{v[238:241]}"(sel4), "{s67}"(ldsb + O_OFF), \
              "{s[60:61]}"(0x2222222222222222ull), "{s[62:63]}"(0x4444444444444444ull), "{s[64:65]}"(0x8888888888888888ull) \
            : "v144", "v145", "v146", "v147", "v148", "v149", "v150", "v151", "v152", "v153", "v154", "v155", "v156", "v157", "v158", "v159", "v160", "v161", "v162", "v163", "v164", "v165", "v166", "v167", "v168", "v169", "v170", "v171", "v172", "v173", "v174", "v175", "v176", "v177", "v178", "v179", "v180", "v181", "v182", "v183", "v184", "v185", "v186", "v187", "v188", "v189", "v190", "v191", "v192", "v193", "v194", "v195", "v196", "v197", "v198", "v199", "v236", "v237", "v248", "v249", "memory"); \
        } \
        LOAD_WL(WB, 18) \
        SB(); \
        BAR(); \
        { \
            LANE_IDS \
            const int hd = (ISV) ? 2 * w : 2 * w - 3; \
            bf16x8 hl[12]; \
            _Pragma("unroll") for (int i = 0; i < 12; ++i) hl[i] = FRAG(O_OFF, i / 6, i % 6); \
            RSTD_PUBLISH(ln_, STL_OFF) \
            float rsl[2]; \
            _Pragma("unroll") for (int lt = 0; lt < 2; ++lt) rsl[lt] = *(const float*)(smem + RS_BASE + l15_ * 4 + lt * 64); \
            __builtin_amdgcn_s_waitcnt(0x0F70); \
            if (ISV) { LOAD_WI(18, 36) } \
            DMA_SLICE(en1) \
            SB(); \
            _Pragma("unroll") for (int lt = 0; lt < 2; ++lt) { \
                f32x4 acc[3]; \
                _Pragma("unroll") for (int ks = 0; ks < 6; ++ks) \
                    _Pragma("unroll") for (int j = 0; j < 3; ++j) acc[j] = MFMA32(W[j * 6 + ks], hl[lt * 6 + ks], ks == 0 ? ZERO4 : acc[j]); \
                const int i = 16 * lt + l15_, g_ = ln_ >> 4; \
                const int vp = i < cnt ? OL_OFF + i * 384 + (((2 * hd + (g_ >> 1)) ^ ((i >> 1) & 7)) << 4) + 8 * (g_ & 1) : OL_OFF + 22 * 384 + g_ * 8; \
                _Pragma("unroll") for (int j = 0; j < 3; ++j) \
                    *(u32x2*)(smem + vp + j * 128) = (u32x2){pk2(acc[j][0] * rsl[lt], acc[j][1] * rsl[lt]), pk2(acc[j][2] * rsl[lt], acc[j][3] * rsl[lt])}; \
            } \
            SB(); \
        } \
        BAR(); \
        _Pragma("unroll") for (int p = 0; p < 6; ++p) __builtin_amdgcn_raw_buffer_store_b128(xl[p], orsrc, xl[6][0] + 16 * p, 0, 0);
#define B_SCALARS \
            CONV_CONSTS(0) \
            const unsigned aCA_ = ldsb + CT_OFF + lane * 16; \
            const unsigned long long ob = (unsigned long long)out, xb = (unsigned long long)((const char*)x - XBIAS), wb = (unsigned long long)wf; \
            const u32x4 orr = {(unsigned)ob, (unsigned)(ob >> 32) & 0xffffu, (unsigned)(IMG * IMG * CH * 2), 0x00020000u}; \
            const u32x4 xr = {(unsigned)xb, (unsigned)(xb >> 32) & 0xffffu, (unsigned)(IMG * IMG * CH * 2 + XBIAS), 0x00020000u}; \
            const u32x4 wr = {(unsigned)wb, (unsigned)(wb >> 32) & 0xffffu, (unsigned)WS_BYTES, 0x00020000u}; \
            unsigned s45 = OOB, s46 = (en0 & 0x7ffffffu) + 96 * w, s47 = (en1 & 0x7ffffffu) + 96 * w; \
            unsigned s48 = ldsb + X_OFF, s67 = ldsb + X1_OFF, s76 = ldsb + WL_OFF + 8, s70 = 4; \
            unsigned s56 = __builtin_amdgcn_readfirstlane((en0 >> 30) * 4 + (en1 >> 30)); \
            unsigned long long xp = 0, sp = 0; \
            _Pragma("unroll") for (int v = 0; v < 4; ++v) { \
                const unsigned r = (CROW >> (8 * v)) & 0xFF; \
                xp |= (unsigned long long)XPAT(r) << (16 * v); \
                sp |= (unsigned long long)SPAT(r) << (16 * v); \
            }
#define B_SIO "+{s45}"(s45), "+{s46}"(s46), "+{s47}"(s47), "+{s48}"(s48), "+{s67}"(s67), "+{s76}"(s76), "+{s56}"(s56), "+{s70}"(s70)
#define B_SIN "{v[238:241]}"(sel4), "{s[40:43]}"(orr), "{s[72:75]}"(xr), "{s39}"(0x401fffffu), "{s[28:31]}"(wr), "{s33}"(wave * 36864), "{s78}"(96 * w), \
              "{s27}"(96 * w - 384), "{s[50:51]}"(xp), "{s[52:53]}"(sp), "{s54}"(CLANE), "{s55}"(CROW & 0x0F0F0F0Fu), "{s[34:35]}"(0x0001000100010001ull), \
              "{s[60:61]}"(0x2222222222222222ull), "{s[62:63]}"(0x4444444444444444ull), "{s[64:65]}"(0x8888888888888888ull), \
              "{s66}"(ldsb + w * 8192), "{s71}"((ldsb + X_OFF) ^ (ldsb + X1_OFF))
#define B_SCLOB "s26", "s77", "s68", "s69", "s79", "s36", "s37", "s38", "s57", "s58", "s59", "s49", "vcc", "scc", "m0", "memory"

    if (wave < 4) {
        LANE_SETUP
        __builtin_amdgcn_s_setprio(3);
        {
            DMA_SLICE(en0)
            asm volatile("s_barrier" ::: "memory");
#pragma unroll
            for (int i = 0; i < 6; ++i) {
                const int ro = (i < 2 ? 0 : (i < 4 ? 6208 : 12544)) + (i & 1) * 3072 + w * 768;
                unsigned m0_ = ldsb + AT_OFF + ro, vo_ = lane * 16;
                asm volatile("s_mov_b32 m0, %0\n\ts_mov_b64 exec, %4\n\tbuffer_load_dwordx4 %1, %2, %3 offen lds\n\ts_mov_b64 exec, -1"
                             :: "s"(m0_), "v"(vo_), "s"(wrsrc), "s"(TB_OFF + ro), "s"(0xFFFFFFFFFFFFull) : "m0", "memory");
            }
            {
                unsigned m0_ = ldsb + CT_OFF + w * 1024, vo_ = lane * 16;
                asm volatile("s_mov_b32 m0, %0\n\ts_nop 0\n\tbuffer_load_dwordx4 %1, %2, %3 offen lds"
                             :: "s"(m0_), "v"(vo_), "s"(wrsrc), "s"(TB_OFF + 19712 + w * 1024) : "m0", "memory");
            }
            if (lane < 48) *(u32x4*)(smem + AT_OFF + 18816 + lane * 16) = (u32x4){0u, 0u, 0u, 0u};
            if (w == 0 && lane < 32) *(unsigned*)(smem + WL_OFF + lane * 4) = lane < cnt ? wl_entry(start + lane) : 0x7ffffffu;
            if (w == 1 && lane == 0) *(unsigned*)(smem + FLAG_OFF) = 0u;
            CONV_CONSTS(en0 >> 30)
            asm volatile(
            "s_waitcnt vmcnt(0) lgkmcnt(0)\n\t"
            "s_nop 7\n\t"
            "ds_read_b128 v[168:171], v254\n\t"
            "ds_read_b64 v[144:145], v253\n\t"
            "ds_read_b64 v[146:147], v253 offset:96\n\t"
            "ds_read_b64 v[148:149], v253 offset:1744\n\t"
            "ds_read_b64 v[150:151], v253 offset:1840\n\t"
            "ds_read_b128 v[172:175], v254 offset:64\n\t"
            "ds_read_b64 v[152:153], v253 offset:8\n\t"
            "ds_read_b64 v[154:155], v253 offset:104\n\t"
            "ds_read_b64 v[156:157], v253 offset:1752\n\t"
            "ds_read_b64 v[158:159], v253 offset:1848\n\t"
            "ds_read_b64 v[160:161], v253 offset:16\n\t"
            "ds_read_b64 v[162:163], v253 offset:112\n\t"
            "ds_read_b64 v[164:165], v253 offset:1760\n\t"
            "ds_read_b64 v[166:167], v253 offset:1856\n\t"
            "v_mov_b64 v[176:177], 0\n\t"
            "v_mov_b64 v[178:179], 0\n\t"
            "v_mov_b64 v[180:181], 0\n\t"
            "v_mov_b64 v[182:183], 0\n\t"
            "v_mov_b64 v[184:185], 0\n\t"
            "v_mov_b64 v[186:187], 0\n\t"
            "v_mov_b64 v[188:189], 0\n\t"
            "v_mov_b64 v[190:191], 0\n\t"
            "v_add_u32 v236, s67, v250\n\t"
            "v_xor_b32 v237, 16, v236\n\t"
            "v_add_u32 v248, s67, v251\n\t"
            "s_waitcnt lgkmcnt(9)\n\t"
            "v_smfmac_f32_16x16x64_bf16 v[176:179], v[168:171], v[144:151], v255\n\t"
            "ds_read_b128 v[168:171], v254 offset:128\n\t"
            "ds_read_b64 v[144:145], v253 offset:24\n\t"
            "s_waitcnt lgkmcnt(6)\n\t"
            "v_smfmac_f32_16x16x64_bf16 v[180:183], v[172:175], v[152:159], v255\n\t"
            "ds_read_b64 v[146:147], v253 offset:120\n\t"
            "ds_read_b64 v[148:149], v253 offset:1768\n\t"
            "ds_read_b64 v[150:151], v253 offset:1864\n\t"
            "ds_read_b128 v[172:175], v254 offset:192\n\t"
            "ds_read_b64 v[152:153], v253 offset:32\n\t"
            "ds_read_b64 v[154:155], v253 offset:128\n\t"
            "ds_read_b64 v[156:157], v253 offset:1776\n\t"
            "ds_read_b64 v[158:159], v253 offset:1872\n\t"
            "v_cvt_pk_bf16_f32 v176, v176, v177\n\t"
            "v_cvt_pk_bf16_f32 v177, v178, v179\n\t"
            "v_cvt_pk_bf16_f32 v178, v180, v181\n\t"
            "v_cvt_pk_bf16_f32 v179, v182, v183\n\t"
            "v_mfma_f32_4x4x4_16b_bf16 v[196:199], v[176:177], v[176:177], 0\n\t"
            "ds_write_b128 v236, v[176:179]\n\t"
            "v_mfma_f32_16x16x32_bf16 v[192:195], v[238:241], v[176:179], 0\n\t"
            "v_mov_b64 v[176:177], 0\n\t"
            "v_mov_b64 v[180:181], 0\n\t"
            "v_mfma_f32_4x4x4_16b_bf16 v[196:199], v[178:179], v[178:179], v[196:199]\n\t"
            "v_mov_b64 v[178:179], 0\n\t"
            "v_mov_b64 v[182:183], 0\n\t"
            "s_waitcnt lgkmcnt(10)\n\t"
            "v_smfmac_f32_16x16x64_bf16 v[184:187], v[168:171], v[160:167], v255\n\t"
            "ds_read_b128 v[168:171], v254 offset:256\n\t"
            "ds_read_b64 v[160:161], v253 offset:40\n\t"
            "ds_read_b64 v[162:163], v253 offset:136\n\t"
            "ds_read_b64 v[164:165], v253 offset:1784\n\t"
            "ds_read_b64 v[166:167], v253 offset:1880\n\t"
            "s_waitcnt lgkmcnt(10)\n\t"
            "v_smfmac_f32_16x16x64_bf16 v[188:191], v[172:175], v[144:151], v255\n\t"
            "ds_read_b128 v[172:175], v254 offset:320\n\t"
            "ds_read_b64 v[144:145], v253 offset:48\n\t"
            "ds_read_b64 v[146:147], v253 offset:144\n\t"
            "ds_read_b64 v[148:149], v253 offset:1792\n\t"
            "ds_read_b64 v[150:151], v253 offset:1888\n\t"
            "v_cvt_pk_bf16_f32 v184, v184, v185\n\t"
            "v_cvt_pk_bf16_f32 v185, v186, v187\n\t"
            "s_nop 0\n\t"
            "v_cvt_pk_bf16_f32 v186, v188, v189\n\t"
            "v_cvt_pk_bf16_f32 v187, v190, v191\n\t"
            "v_mfma_f32_4x4x4_16b_bf16 v[196:199], v[184:185], v[184:185], v[196:199]\n\t"
            "s_waitcnt lgkmcnt(14)\n\t"
            "ds_write_b128 v237, v[184:187]\n\t"
            "v_mfma_f32_16x16x32_bf16 v[192:195], v[238:241], v[184:187], v[192:195]\n\t"
            "v_mov_b64 v[184:185], 0\n\t"
            "v_mov_b64 v[188:189], 0\n\t"
            "v_mfma_f32_4x4x4_16b_bf16 v[196:199], v[186:187], v[186:187], v[196:199]\n\t"
            "v_mov_b64 v[186:187], 0\n\t"
            "v_mov_b64 v[190:191], 0\n\t"
            "s_waitcnt lgkmcnt(10)\n\t"
            "v_smfmac_f32_16x16x64_bf16 v[176:179], v[168:171], v[152:159], v255\n\t"
            "ds_read_b128 v[168:171], v254 offset:384\n\t"
            "ds_read_b64 v[152:153], v253 offset:56\n\t"
            "ds_read_b64 v[154:155], v253 offset:152\n\t"
            "ds_read_b64 v[156:157], v253 offset:1800\n\t"
            "ds_read_b64 v[158:159], v253 offset:1896\n\t"
            "s_waitcnt lgkmcnt(10)\n\t"
            "v_smfmac_f32_16x16x64_bf16 v[180:183], v[172:175], v[160:167], v255\n\t"
            "ds_read_b128 v[172:175], v254 offset:448\n\t"
            "ds_read_b64 v[160:161], v253 offset:64\n\t"
            "ds_read_b64 v[162:163], v253 offset:160\n\t"
            "ds_read_b64 v[164:165], v253 offset:1808\n\t"
            "ds_read_b64 v[166:167], v253 offset:1904\n\t"
            "v_cvt_pk_bf16_f32 v176, v176, v177\n\t"
            "v_cvt_pk_bf16_f32 v177, v178, v179\n\t"
            "s_nop 0\n\t"
            "v_cvt_pk_bf16_f32 v178, v180, v181\n\t"
            "v_cvt_pk_bf16_f32 v179, v182, v183\n\t"
            "v_mfma_f32_4x4x4_16b_bf16 v[196:199], v[176:177], v[176:177], v[196:199]\n\t"
            "s_waitcnt lgkmcnt(14)\n\t"
            "ds_write_b128 v236, v[176:179] offset:128\n\t"
            "v_mfma_f32_16x16x32_bf16 v[192:195], v[238:241], v[176:179], v[192:195]\n\t"
            "v_mov_b64 v[176:177], 0\n\t"
            "v_mov_b64 v[180:181], 0\n\t"
            "v_mfma_f32_4x4x4_16b_bf16 v[196:199], v[178:179], v[178:179], v[196:199]\n\t"
            "v_mov_b64 v[178:179], 0\n\t"
            "v_mov_b64 v[182:183], 0\n\t"
            "s_waitcnt lgkmcnt(10)\n\t"
            "v_smfmac_f32_16x16x64_bf16 v[184:187], v[168:171], v[144:151], v255\n\t"
            "ds_read_b128 v[168:171], v254 offset:512\n\t"
            "ds_read_b64 v[144:145], v253 offset:72\n\t"
            "ds_read_b64 v[146:147], v253 offset:168\n\t"
            "ds_read_b64 v[148:149], v253 offset:1816\n\t"
            "ds_read_b64 v[150:151], v253 offset:1912\n\t"
            "s_waitcnt lgkmcnt(10)\n\t"
            "v_smfmac_f32_16x16x64_bf16 v[188:191], v[172:175], v[152:159], v255\n\t"
            "ds_read_b128 v[172:175], v254 offset:576\n\t"
            "ds_read_b64 v[152:153], v253 offset:80\n\t"
            "ds_read_b64 v[154:155], v253 offset:176\n\t"
            "ds_read_b64 v[156:157], v253 offset:1824\n\t"
            "ds_read_b64 v[158:159], v253 offset:1920\n\t"
            "v_cvt_pk_bf16_f32 v184, v184, v185\n\t"
            "v_cvt_pk_bf16_f32 v185, v186, v187\n\t"
            "s_nop 0\n\t"
            "v_cvt_pk_bf16_f32 v186, v188, v189\n\t"
            "v_cvt_pk_bf16_f32 v187, v190, v191\n\t"
            "v_mfma_f32_4x4x4_16b_bf16 v[196:199], v[184:185], v[184:185], v[196:199]\n\t"
            "s_waitcnt lgkmcnt(14)\n\t"
            "ds_write_b128 v237, v[184:187] offset:128\n\t"
            "v_mfma_f32_16x16x32_bf16 v[192:195], v[238:241], v[184:187], v[192:195]\n\t"
            "v_mov_b64 v[184:185], 0\n\t"
            "v_mov_b64 v[188:189], 0\n\t"
            "v_mfma_f32_4x4x4_16b_bf16 v[196:199], v[186:187], v[186:187], v[196:199]\n\t"
            "v_mov_b64 v[186:187], 0\n\t"
            "v_mov_b64 v[190:191], 0\n\t"
            "s_waitcnt lgkmcnt(10)\n\t"
            "v_smfmac_f32_16x16x64_bf16 v[176:179], v[168:171], v[160:167], v255\n\t"
            "ds_read_b128 v[168:171], v254 offset:640\n\t"
            "ds_read_b64 v[160:161], v253 offset:88\n\t"
            "ds_read_b64 v[162:163], v253 offset:184\n\t"
            "ds_read_b64 v[164:165], v253 offset:1832\n\t"
            "ds_read_b64 v[166:167], v253 offset:1928\n\t"
            "s_waitcnt lgkmcnt(10)\n\t"
            "v_smfmac_f32_16x16x64_bf16 v[180:183], v[172:175], v[144:151], v255\n\t"
            "ds_read_b128 v[172:175], v254 offset:704\n\t"
            "v_cvt_pk_bf16_f32 v176, v176, v177\n\t"
            "v_cvt_pk_bf16_f32 v177, v178, v179\n\t"
            "s_nop 1\n\t"
            "v_mfma_f32_4x4x4_16b_bf16 v[196:199], v[176:177], v[176:177], v[196:199]\n\t"
            "s_nop 1\n\t"
            "v_cvt_pk_bf16_f32 v178, v180, v181\n\t"
            "v_cvt_pk_bf16_f32 v179, v182, v183\n\t"
            "s_nop 1\n\t"
            "v_mfma_f32_4x4x4_16b_bf16 v[196:199], v[178:179], v[178:179], v[196:199]\n\t"
            "ds_write_b128 v236, v[176:179] offset:256\n\t"
            "s_waitcnt lgkmcnt(6)\n\t"
            "v_smfmac_f32_16x16x64_bf16 v[184:187], v[168:171], v[152:159], v255\n\t"
            "v_mfma_f32_16x16x32_bf16 v[192:195], v[238:241], v[176:179], v[192:195]\n\t"
            "s_waitcnt lgkmcnt(1)\n\t"
            "v_smfmac_f32_16x16x64_bf16 v[188:191], v[172:175], v[160:167], v255\n\t"
            "s_nop 4\n\t"
            "v_cvt_pk_bf16_f32 v184, v184, v185\n\t"
            "v_cvt_pk_bf16_f32 v185, v186, v187\n\t"
            "s_nop 1\n\t"
            "v_mfma_f32_4x4x4_16b_bf16 v[196:199], v[184:185], v[184:185], v[196:199]\n\t"
            "v_cvt_pk_bf16_f32 v186, v188, v189\n\t"
            "v_cvt_pk_bf16_f32 v187, v190, v191\n\t"
            "s_nop 1\n\t"
            "v_mfma_f32_4x4x4_16b_bf16 v[196:199], v[186:187], v[186:187], v[196:199]\n\t"
            "ds_write_b128 v237, v[184:187] offset:256\n\t"
            "v_mfma_f32_16x16x32_bf16 v[192:195], v[238:241], v[184:187], v[192:195]\n\t"
            "s_nop 7\n\t"
            "v_cndmask_b32_e64 v193, v196, v197, s[60:61]\n\t"
            "v_cndmask_b32_e64 v193, v193, v198, s[62:63]\n\t"
            "v_cndmask_b32_e64 v193, v193, v199, s[64:65]\n\t"
            "ds_write_b64 v248, v[192:193] offset:18816\n\t"
            "s_waitcnt lgkmcnt(0)\n\t"
            "s_nop 7\n\t"
            :: "{v250}"(pXw_), "{v251}"(pSTw_), "{v253}"(aB_), "{v254}"(aA_), "{v255}"(idx_), "{v[238:241]}"(sel4), "{s67}"(ldsb + X_OFF), "{s[60:61]}"(0x2222222222222222ull),
               "{s[62:63]}"(0x4444444444444444ull), "{s[64:65]}"(0x8888888888888888ull)
            : "v144", "v145", "v146", "v147", "v148", "v149", "v150", "v151", "v152", "v153", "v154", "v155", "v156", "v157", "v158", "v159", "v160", "v161", "v162", "v163", "v164", "v165", "v166", "v167", "v168", "v169", "v170", "v171", "v172", "v173", "v174", "v175", "v176", "v177", "v178", "v179", "v180", "v181", "v182", "v183", "v184", "v185", "v186", "v187", "v188", "v189", "v190", "v191", "v192", "v193", "v194", "v195", "v196", "v197", "v198", "v199", "v236", "v237", "v248", "v249", "memory");
        }
        __builtin_amdgcn_s_setprio(0);
        LOAD_WI(0, WA)
        BAR();
        LOAD_WI(WA, 36)
        __builtin_amdgcn_s_setprio(2);
        int dW = X_OFF + l15 * 384 + (((2 * w + (g >> 1)) ^ sw) << 4) + 8 * (g & 1);
        int vA = V_OFF + w * 4608 + lane * 16, vB = V_OFF + w * 4608 + 1024 + lane * 8;
        int rsR = RS_BASE + l15 * 4;
        OPQ(dW); OPQ(vA); OPQ(vB); OPQ(rsR);
        const float SC = 0.14433756729740643f * 1.4426950408889634f;
        BAR();
        {
            bf16x8 hl[12];
#pragma unroll
            for (int i = 0; i < 12; ++i) hl[i] = FRAG(O_OFF, i / 6, i % 6);
            RSTD_PUBLISH(lane, STL_OFF)
            float rsl[2];
#pragma unroll
            for (int lt = 0; lt < 2; ++lt) rsl[lt] = *(const float*)(smem + rsR + lt * 64);
            SB();
            f32x4 acc[2][6];
#pragma unroll
            for (int lt = 0; lt < 2; ++lt)
#pragma unroll
                for (int ks = 0; ks < 6; ++ks)
#pragma unroll
                    for (int j = 0; j < 6; ++j) acc[lt][j] = MFMA32(W[j * 6 + ks], hl[lt * 6 + ks], ks == 0 ? ZERO4 : acc[lt][j]);
#pragma unroll
            for (int lt = 0; lt < 2; ++lt) {
                const int i = 16 * lt + l15;
                const float rs = rsl[lt];
                char* p = smem + (i < cnt ? KQL_OFF + i * 800 + w * 192 : X1_OFF + w * 192);
                const u32x4 k01 = {pk2(acc[lt][0][0] * rs, acc[lt][0][1] * rs), pk2(acc[lt][0][2] * rs, acc[lt][0][3] * rs),
                                   pk2(acc[lt][1][0] * rs, acc[lt][1][1] * rs), pk2(acc[lt][1][2] * rs, acc[lt][1][3] * rs)};
                const u32x4 q01 = {pk2(acc[lt][3][0], acc[lt][3][1]), pk2(acc[lt][3][2], acc[lt][3][3]),
                                   pk2(acc[lt][4][0], acc[lt][4][1]), pk2(acc[lt][4][2], acc[lt][4][3])};
                const u32x4 k2 = {pk2(acc[lt][2][0] * rs, acc[lt][2][1] * rs), pk2(acc[lt][2][2] * rs, acc[lt][2][3] * rs), 0u, 0u};
                const u32x4 q2 = {pk2(acc[lt][5][0], acc[lt][5][1]), pk2(acc[lt][5][2], acc[lt][5][3]), 0u, 0u};
                *(u32x4*)(p + g * 16) = k01;
                *(u32x4*)(p + 64 + g * 16) = q01;
                *(u32x2*)(p + 128 + g * 8) = (u32x2){k2[0], k2[1]};
                *(u32x2*)(p + 160 + g * 8) = (u32x2){q2[0], q2[1]};
                f32x4 dd = MFMA32(BC8(k2), BC8(q2), ZERO4);
                dd = MFMA32(BC8(k01), BC8(q01), dd);
                float sll = (l15 & 3) == 1 ? dd[1] : dd[0];
                sll = (l15 & 3) == 2 ? dd[2] : sll;
                sll = (l15 & 3) == 3 ? dd[3] : sll;
                const bool dg = g == (l15 >> 2);
                if (dg && i < cnt) { *(float*)(smem + KQL_OFF + i * 800 + 768 + w * 4) = sll; *(float*)(smem + KQL_OFF + i * 800 + 784) = rs * SC; }
            }
        }

        {
            const unsigned rsb = ldsb + RS_BASE;
            const unsigned pE_ = fE - X_OFF, aO_ = ldsb + dW + (O_OFF - X_OFF), sel_ = (l15 & 3) ? 0u : 1u, aVB_ = ldsb + vB;
            const unsigned pST_ = lane * 16, aRSw_ = rsb + lane * 4, aPLw_ = rsb + lane * 2, aRSr_ = ldsb + rsR;
            const unsigned g16_ = (l15 & 3) ? ldsb + AT_OFF + 18816 : g * 16, g8_ = (l15 & 3) ? ldsb + AT_OFF + 18816 : g * 8;
            const unsigned loVL_ = ((2 * w + (l15 >> 3)) << 4) + (l15 & 7) * 2, loOL_ = g * 128;
            const unsigned on = (l15 & 3) ? 0u : 0x3F803F80u;
            const u32x4 ones4 = {on, on, on, on};
            unsigned s44 = cnt, s48 = ldsb + X_OFF, s70 = 0;
            unsigned s72 = ldsb + KQL_OFF + w * 192, s73 = ldsb + OL_OFF, s74 = ldsb + KQL_OFF + 784, s77 = ldsb + OL_OFF, s76 = ldsb + KQL_OFF + 768 + w * 4;
            unsigned long long s68 = 0;
            asm volatile(
            "s_waitcnt lgkmcnt(0)\n\t"
            "s_nop 7\n\t"
            "s_branch .Lwa_in%=\n\t"
            ".Lwa_loop%=:\n\t"
            "s_waitcnt lgkmcnt(2)\n\t"
            "v_mfma_f32_16x16x32_bf16 v[192:195], v[0:3], v[212:215], 0\n\t"
            "s_bfe_u32 s49, s70, 0x30001\n\t"
            "s_lshl_b32 s49, s49, 4\n\t"
            "s_or_b32 s73, s77, s49\n\t"
            "v_mfma_f32_16x16x32_bf16 v[192:195], v[4:7], v[224:227], v[192:195]\n\t"
            "v_mad_u32_u24 v220, v246, s72, v253\n\t"
            "v_mad_u32_u24 v216, v246, s72, v252\n\t"
            "v_mfma_f32_16x16x32_bf16 v[148:151], v[28:31], v[224:227], 0\n\t"
            "ds_read_b64 v[222:223], v220 offset:160\n\t"
            "ds_read_b64 v[220:221], v220 offset:128\n\t"
            "v_mfma_f32_16x16x32_bf16 v[148:151], v[24:27], v[212:215], v[148:151]\n\t"
            "v_mul_f32 v176, v237, v176\n\t"
            "v_mul_f32 v177, v237, v177\n\t"
            "v_mfma_f32_16x16x32_bf16 v[196:199], v[48:51], v[212:215], 0\n\t"
            "v_mul_f32 v178, v237, v178\n\t"
            "v_mul_f32 v179, v237, v179\n\t"
            "v_mfma_f32_16x16x32_bf16 v[196:199], v[52:55], v[224:227], v[196:199]\n\t"
            "v_cvt_pk_bf16_f32 v176, v176, v177\n\t"
            "v_mul_f32 v168, v237, v168\n\t"
            "v_mfma_f32_16x16x32_bf16 v[200:203], v[76:79], v[224:227], 0\n\t"
            "v_mul_f32 v169, v237, v169\n\t"
            "v_cvt_pk_bf16_f32 v177, v178, v179\n\t"
            "v_mfma_f32_16x16x32_bf16 v[200:203], v[72:75], v[212:215], v[200:203]\n\t"
            "v_mul_f32 v170, v237, v170\n\t"
            "v_mul_f32 v171, v237, v171\n\t"
            "v_mfma_f32_16x16x32_bf16 v[160:163], v[96:99], v[212:215], 0\n\t"
            "v_cvt_pk_bf16_f32 v168, v168, v169\n\t"
            "v_cvt_pk_bf16_f32 v180, v180, v181\n\t"
            "v_mfma_f32_16x16x32_bf16 v[160:163], v[100:103], v[224:227], v[160:163]\n\t"
            "v_cvt_pk_bf16_f32 v178, v188, v189\n\t"
            "v_cvt_pk_bf16_f32 v179, v190, v191\n\t"
            "v_mfma_f32_16x16x32_bf16 v[164:167], v[124:127], v[224:227], 0\n\t"
            "ds_read_b128 v[224:227], v233 offset:12544\n\t"
            "v_mul_f32 v172, v237, v172\n\t"
            "v_mfma_f32_16x16x32_bf16 v[164:167], v[120:123], v[212:215], v[164:167]\n\t"
            "ds_read_b128 v[212:215], v232 offset:12544\n\t"
            "v_mul_f32 v173, v237, v173\n\t"
            "s_waitcnt lgkmcnt(4)\n\t"
            "v_mfma_f32_16x16x32_bf16 v[192:195], v[8:11], v[204:207], v[192:195]\n\t"
            "v_mul_f32 v174, v237, v174\n\t"
            "v_mul_f32 v175, v237, v175\n\t"
            "v_mfma_f32_16x16x32_bf16 v[192:195], v[12:15], v[208:211], v[192:195]\n\t"
            "v_cvt_pk_bf16_f32 v169, v170, v171\n\t"
            "v_cvt_pk_bf16_f32 v181, v182, v183\n\t"
            "v_mfma_f32_16x16x32_bf16 v[196:199], v[56:59], v[204:207], v[196:199]\n\t"
            "v_cvt_pk_bf16_f32 v170, v172, v173\n\t"
            "v_cvt_pk_bf16_f32 v171, v174, v175\n\t"
            "v_mfma_f32_16x16x32_bf16 v[196:199], v[60:63], v[208:211], v[196:199]\n\t"
            "v_cvt_pk_bf16_f32 v182, v184, v185\n\t"
            "v_cvt_pk_bf16_f32 v183, v186, v187\n\t"
            "v_mfma_f32_16x16x32_bf16 v[160:163], v[104:107], v[204:207], v[160:163]\n\t"
            "v_mov_b32 v230, s76\n\t"
            "v_mov_b32 v239, s74\n\t"
            "v_mfma_f32_16x16x32_bf16 v[160:163], v[108:111], v[208:211], v[160:163]\n\t"
            "s_add_u32 s70, s70, 1\n\t"
            "s_add_u32 s72, s72, 800\n\t"
            "s_add_u32 s74, s74, 800\n\t"
            "s_add_u32 s76, s76, 800\n\t"
            "v_mfma_f32_16x16x32_bf16 v[148:151], v[36:39], v[208:211], v[148:151]\n\t"
            "s_add_u32 s77, s77, 384\n\t"
            "s_xor_b32 s48, s48, s47\n\t"
            "s_sub_u32 s44, s44, 1\n\t"
            "v_mfma_f32_16x16x32_bf16 v[148:151], v[32:35], v[204:207], v[148:151]\n\t"
            "s_mov_b32 s68, -1\n\t"
            "s_mov_b32 s69, 0xffff\n\t"
            "ds_read_b32 v230, v230\n\t"
            "v_mfma_f32_16x16x32_bf16 v[200:203], v[84:87], v[208:211], v[200:203]\n\t"
            "ds_read_b32 v239, v239\n\t"
            "v_mul_f32 v237, 0x3e553b94, v237\n\t"
            "v_mfma_f32_16x16x32_bf16 v[200:203], v[80:83], v[204:207], v[200:203]\n\t"
            "v_mfma_f32_16x16x32_bf16 v[164:167], v[132:135], v[208:211], v[164:167]\n\t"
            "v_mfma_f32_16x16x32_bf16 v[164:167], v[128:131], v[204:207], v[164:167]\n\t"
            "s_waitcnt lgkmcnt(2)\n\t"
            "v_mfma_f32_16x16x32_bf16 v[192:195], v[16:19], v[212:215], v[192:195]\n\t"
            "v_mfma_f32_16x16x32_bf16 v[192:195], v[20:23], v[224:227], v[192:195]\n\t"
            "v_mfma_f32_16x16x32_bf16 v[196:199], v[64:67], v[212:215], v[196:199]\n\t"
            "v_mfma_f32_16x16x32_bf16 v[196:199], v[68:71], v[224:227], v[196:199]\n\t"
            "v_mfma_f32_16x16x32_bf16 v[160:163], v[112:115], v[212:215], v[160:163]\n\t"
            "v_mfma_f32_16x16x32_bf16 v[160:163], v[116:119], v[224:227], v[160:163]\n\t"
            "v_mfma_f32_16x16x32_bf16 v[148:151], v[44:47], v[224:227], v[148:151]\n\t"
            "v_mfma_f32_16x16x32_bf16 v[148:151], v[40:43], v[212:215], v[148:151]\n\t"
            "s_nop 1\n\t"
            "v_mul_f32 v192, v238, v192\n\t"
            "v_mfma_f32_16x16x32_bf16 v[200:203], v[92:95], v[224:227], v[200:203]\n\t"
            "v_mul_f32 v193, v238, v193\n\t"
            "v_mul_f32 v194, v238, v194\n\t"
            "v_mfma_f32_16x16x32_bf16 v[200:203], v[88:91], v[212:215], v[200:203]\n\t"
            "v_mul_f32 v195, v238, v195\n\t"
            "v_cvt_pk_bf16_f32 v192, v192, v193\n\t"
            "v_mfma_f32_16x16x32_bf16 v[164:167], v[140:143], v[224:227], v[164:167]\n\t"
            "ds_read_b128 v[224:227], v216 offset:64\n\t"
            "ds_read_b128 v[216:219], v216\n\t"
            "v_mfma_f32_16x16x32_bf16 v[164:167], v[136:139], v[212:215], v[164:167]\n\t"
            "v_mul_f32 v148, v238, v148\n\t"
            "v_mul_f32 v149, v238, v149\n\t"
            "v_mfma_f32_16x16x16_bf16 v[172:175], v[220:221], v[154:155], 0\n\t"
            "v_mul_f32 v150, v238, v150\n\t"
            "v_mul_f32 v151, v238, v151\n\t"
            "v_cvt_pk_bf16_f32 v193, v194, v195\n\t"
            "v_mfma_f32_16x16x16_bf16 v[184:187], v[152:153], v[178:179], 0\n\t"
            "v_mul_f32 v196, v238, v196\n\t"
            "v_mul_f32 v197, v238, v197\n\t"
            "v_cvt_pk_bf16_f32 v194, v148, v149\n\t"
            "v_mfma_f32_16x16x16_bf16 v[208:211], v[220:221], v[178:179], 0\n\t"
            "v_cvt_pk_bf16_f32 v195, v150, v151\n\t"
            "v_cvt_pk_bf16_f32 v200, v200, v201\n\t"
            "v_mul_f32 v198, v238, v198\n\t"
            "v_mfma_f32_16x16x16_bf16 v[148:151], v[152:153], v[154:155], 0\n\t"
            "v_mul_f32 v199, v238, v199\n\t"
            "v_cvt_pk_bf16_f32 v196, v196, v197\n\t"
            "v_cvt_pk_bf16_f32 v201, v202, v203\n\t"
            "v_mfma_f32_16x16x16_bf16 v[204:207], v[176:177], v[178:179], 0\n\t"
            "v_cvt_pk_bf16_f32 v197, v198, v199\n\t"
            "v_cvt_pk_bf16_f32 v202, v160, v161\n\t"
            "v_cvt_pk_bf16_f32 v203, v162, v163\n\t"
            "v_mfma_f32_16x16x16_bf16 v[160:163], v[176:177], v[154:155], 0\n\t"
            "v_cvt_pk_bf16_f32 v198, v164, v165\n\t"
            "v_cvt_pk_bf16_f32 v199, v166, v167\n\t"
            "s_setprio 0\n\t"
            "s_setprio 3\n\t"
            "v_mfma_f32_16x16x16_bf16 v[164:167], v[196:197], v[154:155], 0\n\t"
            "v_mul_f32 v238, 0x3e553b94, v238\n\t"
            "v_mfma_f32_16x16x16_bf16 v[188:191], v[196:197], v[178:179], 0\n\t"
            "v_mfma_f32_16x16x32_bf16 v[148:151], v[144:147], v[156:159], v[148:151]\n\t"
            "s_waitcnt lgkmcnt(0)\n\t"
            "v_mfma_f32_16x16x32_bf16 v[172:175], v[216:219], v[156:159], v[172:175]\n\t"
            "v_mfma_f32_16x16x32_bf16 v[184:187], v[144:147], v[180:183], v[184:187]\n\t"
            "v_mfma_f32_16x16x32_bf16 v[208:211], v[216:219], v[180:183], v[208:211]\n\t"
            "v_mfma_f32_16x16x32_bf16 v[160:163], v[168:171], v[156:159], v[160:163]\n\t"
            "v_mfma_f32_16x16x32_bf16 v[204:207], v[168:171], v[180:183], v[204:207]\n\t"
            "v_mfma_f32_16x16x32_bf16 v[164:167], v[192:195], v[156:159], v[164:167]\n\t"
            "v_mfma_f32_16x16x32_bf16 v[188:191], v[192:195], v[180:183], v[188:191]\n\t"
            "s_nop 1\n\t"
            "v_max3_f32 v228, v172, v148, v149\n\t"
            "v_mfma_f32_16x16x16_bf16 v[212:215], v[222:223], v[152:153], 0\n\t"
            "v_max3_f32 v228, v228, v150, v151\n\t"
            "v_max3_f32 v229, v208, v184, v185\n\t"
            "v_mfma_f32_16x16x16_bf16 v[156:159], v[222:223], v[196:197], 0\n\t"
            "v_max3_f32 v229, v229, v186, v187\n\t"
            "v_max3_f32 v228, v228, v160, v161\n\t"
            "v_max3_f32 v229, v229, v204, v205\n\t"
            "v_mfma_f32_16x16x32_bf16 v[212:215], v[224:227], v[144:147], v[212:215]\n\t"
            "v_max3_f32 v228, v228, v162, v163\n\t"
            "v_max3_f32 v229, v229, v206, v207\n\t"
            "v_mfma_f32_16x16x32_bf16 v[156:159], v[224:227], v[192:195], v[156:159]\n\t"
            "v_max3_f32 v228, v228, v164, v165\n\t"
            "v_max3_f32 v229, v229, v188, v189\n\t"
            "v_mfma_f32_16x16x16_bf16 v[180:183], v[152:153], v[198:199], 0\n\t"
            "s_setprio 0\n\t"
            "v_max3_f32 v228, v228, v166, v167\n\t"
            "v_max3_f32 v229, v229, v190, v191\n\t"
            "s_nop 1\n\t"
            "v_permlane32_swap_b32 v228, v229\n\t"
            "v_max_f32 v228, v228, v229\n\t"
            "v_mov_b32 v231, v228\n\t"
            "s_setprio 3\n\t"
            "v_mfma_f32_16x16x32_bf16 v[180:183], v[144:147], v[200:203], v[180:183]\n\t"
            "s_setprio 0\n\t"
            "v_permlane16_swap_b32 v228, v231\n\t"
            "v_max_f32 v228, v228, v231\n\t"
            "v_mov_b32 v229, v228\n\t"
            "s_nop 1\n\t"
            "v_permlane32_swap_b32 v228, v229\n\t"
            "v_mul_f32_e64 v228, -v228, v236\n\t"
            "v_fma_f32 v148, v148, v236, v228\n\t"
            "v_fma_f32 v149, v149, v236, v228\n\t"
            "v_exp_f32 v148, v148\n\t"
            "v_exp_f32 v149, v149\n\t"
            "v_fma_f32 v150, v150, v236, v228\n\t"
            "v_fma_f32 v151, v151, v236, v228\n\t"
            "v_exp_f32 v150, v150\n\t"
            "v_exp_f32 v151, v151\n\t"
            "v_fma_f32 v160, v160, v236, v228\n\t"
            "v_fma_f32 v161, v161, v236, v228\n\t"
            "v_fma_f32 v162, v162, v236, v228\n\t"
            "v_fma_f32 v163, v163, v236, v228\n\t"
            "v_exp_f32 v160, v160\n\t"
            "v_exp_f32 v161, v161\n\t"
            "v_exp_f32 v162, v162\n\t"
            "v_exp_f32 v163, v163\n\t"
            "v_cvt_pk_bf16_f32 v148, v148, v149\n\t"
            "v_cvt_pk_bf16_f32 v149, v150, v151\n\t"
            "v_cvt_pk_bf16_f32 v150, v160, v161\n\t"
            "v_cvt_pk_bf16_f32 v151, v162, v163\n\t"
            "s_setprio 3\n\t"
            "v_mfma_f32_16x16x16_bf16 v[160:163], v[222:223], v[176:177], 0\n\t"
            "s_setprio 0\n\t"
            "v_mul_f32_e64 v229, -v229, v237\n\t"
            "v_fma_f32 v188, v188, v237, v229\n\t"
            "v_fma_f32 v189, v189, v237, v229\n\t"
            "v_exp_f32 v188, v188\n\t"
            "v_exp_f32 v189, v189\n\t"
            "v_fma_f32 v190, v190, v237, v229\n\t"
            "s_setprio 3\n\t"
            "v_mfma_f32_16x16x32_bf16 v[160:163], v[224:227], v[168:171], v[160:163]\n\t"
            "s_setprio 0\n\t"
            "v_fma_f32 v191, v191, v237, v229\n\t"
            "v_fma_f32 v208, v208, v237, v229\n\t"
            "v_fma_f32 v184, v184, v237, v229\n\t"
            "v_fma_f32 v185, v185, v237, v229\n\t"
            "v_exp_f32 v190, v190\n\t"
            "v_exp_f32 v191, v191\n\t"
            "v_exp_f32 v208, v208\n\t"
            "v_fma_f32 v164, v164, v236, v228\n\t"
            "v_max3_f32 v234, v212, v160, v156\n\t"
            "v_max_f32 v234, v234, v230\n\t"
            "v_fma_f32 v165, v165, v236, v228\n\t"
            "v_exp_f32 v184, v184\n\t"
            "v_max_f32_dpp v234, v234, v234 row_ror:8 row_mask:0xf bank_mask:0xf\n\t"
            "v_exp_f32 v185, v185\n\t"
            "v_fma_f32 v186, v186, v237, v229\n\t"
            "v_max_f32_dpp v234, v234, v234 row_ror:4 row_mask:0xf bank_mask:0xf\n\t"
            "v_fma_f32 v187, v187, v237, v229\n\t"
            "v_exp_f32 v164, v164\n\t"
            "v_exp_f32 v165, v165\n\t"
            "v_fma_f32 v166, v166, v236, v228\n\t"
            "v_fma_f32 v167, v167, v236, v228\n\t"
            "v_fma_f32 v172, v172, v236, v228\n\t"
            "v_exp_f32 v186, v186\n\t"
            "v_exp_f32 v187, v187\n\t"
            "v_fma_f32 v204, v204, v237, v229\n\t"
            "v_fma_f32 v205, v205, v237, v229\n\t"
            "v_fma_f32 v206, v206, v237, v229\n\t"
            "v_fma_f32 v207, v207, v237, v229\n\t"
            "v_cvt_pk_bf16_f32 v188, v188, v189\n\t"
            "v_exp_f32 v166, v166\n\t"
            "v_exp_f32 v167, v167\n\t"
            "v_exp_f32 v172, v172\n\t"
            "v_exp_f32 v204, v204\n\t"
            "v_exp_f32 v205, v205\n\t"
            "v_exp_f32 v206, v206\n\t"
            "v_exp_f32 v207, v207\n\t"
            "v_cvt_pk_bf16_f32 v189, v190, v191\n\t"
            "v_cvt_pk_bf16_f32 v208, v208, 0\n\t"
            "v_max_f32_dpp v234, v234, v234 row_ror:2 row_mask:0xf bank_mask:0xf\n\t"
            "v_cvt_pk_bf16_f32 v184, v184, v185\n\t"
            "v_cndmask_b32_e64 v190, 0, v208, s[58:59]\n\t"
            "s_setprio 3\n\t"
            "v_mfma_f32_16x16x16_bf16 v[208:211], v[220:221], v[198:199], 0\n\t"
            "s_setprio 0\n\t"
            "v_cvt_pk_bf16_f32 v164, v164, v165\n\t"
            "v_cndmask_b32_e64 v235, v212, v160, s[60:61]\n\t"
            "v_cvt_pk_bf16_f32 v185, v186, v187\n\t"
            "v_cvt_pk_bf16_f32 v165, v166, v167\n\t"
            "v_cvt_pk_bf16_f32 v172, v172, 0\n\t"
            "v_cndmask_b32_e64 v235, v235, v156, s[62:63]\n\t"
            "v_cvt_pk_bf16_f32 v186, v204, v205\n\t"
            "s_setprio 3\n\t"
            "v_mfma_f32_16x16x32_bf16 v[208:211], v[216:219], v[200:203], v[208:211]\n\t"
            "v_cvt_pk_bf16_f32 v187, v206, v207\n\t"
            "v_cndmask_b32_e64 v166, 0, v172, s[58:59]\n\t"
            "v_mfma_f32_16x16x16_bf16 v[204:207], v[176:177], v[198:199], 0\n\t"
            "v_max_f32_dpp v234, v234, v234 row_ror:1 row_mask:0xf bank_mask:0xf\n\t"
            "v_cndmask_b32_e64 v235, v235, v230, s[64:65]\n\t"
            "v_sub_f32 v235, v235, v234\n\t"
            "v_mfma_f32_16x16x16_bf16 v[172:175], v[196:197], v[198:199], 0\n\t"
            "v_mul_f32 v235, v239, v235\n\t"
            "v_exp_f32 v235, v235\n\t"
            "v_mfma_f32_16x16x32_bf16 v[204:207], v[168:171], v[200:203], v[204:207]\n\t"
            "v_cvt_pk_bf16_f32 v235, v235, 0\n\t"
            "v_cndmask_b32_e64 v235, 0, v235, s[66:67]\n\t"
            "v_max3_f32 v230, v208, v180, v181\n\t"
            "v_mfma_f32_16x16x32_bf16 v[172:175], v[192:195], v[200:203], v[172:175]\n\t"
            "s_setprio 0\n\t"
            "ds_write_b16 v250, v235 offset:256\n\t"
            "v_max3_f32 v230, v230, v182, v183\n\t"
            "v_mov_b32 v167, 0\n\t"
            "v_mov_b32 v191, 0\n\t"
            "v_max3_f32 v230, v230, v204, v205\n\t"
            "v_max3_f32 v230, v230, v206, v207\n\t"
            "s_nop 0\n\t"
            "v_max3_f32 v230, v230, v172, v173\n\t"
            "v_max3_f32 v230, v230, v174, v175\n\t"
            "v_mov_b32 v231, v230\n\t"
            "s_nop 1\n\t"
            "v_permlane32_swap_b32 v230, v231\n\t"
            "v_max_f32 v230, v230, v231\n\t"
            "v_mov_b32 v231, v230\n\t"
            "s_nop 1\n\t"
            "v_permlane16_swap_b32 v230, v231\n\t"
            "v_max_f32 v230, v230, v231\n\t"
            "v_mul_f32_e64 v230, -v230, v238\n\t"
            "v_fma_f32 v180, v180, v238, v230\n\t"
            "v_fma_f32 v181, v181, v238, v230\n\t"
            "v_fma_f32 v172, v172, v238, v230\n\t"
            "v_fma_f32 v173, v173, v238, v230\n\t"
            "v_exp_f32 v180, v180\n\t"
            "v_exp_f32 v181, v181\n\t"
            "v_fma_f32 v182, v182, v238, v230\n\t"
            "v_fma_f32 v183, v183, v238, v230\n\t"
            "v_exp_f32 v172, v172\n\t"
            "v_exp_f32 v173, v173\n\t"
            "v_fma_f32 v174, v174, v238, v230\n\t"
            "v_fma_f32 v175, v175, v238, v230\n\t"
            "v_fma_f32 v208, v208, v238, v230\n\t"
            "v_exp_f32 v182, v182\n\t"
            "v_exp_f32 v183, v183\n\t"
            "v_fma_f32 v204, v204, v238, v230\n\t"
            "v_fma_f32 v205, v205, v238, v230\n\t"
            "v_fma_f32 v206, v206, v238, v230\n\t"
            "v_fma_f32 v207, v207, v238, v230\n\t"
            "v_exp_f32 v174, v174\n\t"
            "v_exp_f32 v175, v175\n\t"
            "v_exp_f32 v208, v208\n\t"
            "v_exp_f32 v204, v204\n\t"
            "v_exp_f32 v205, v205\n\t"
            "v_exp_f32 v206, v206\n\t"
            "v_exp_f32 v207, v207\n\t"
            "v_cvt_pk_bf16_f32 v180, v180, v181\n\t"
            "v_cvt_pk_bf16_f32 v172, v172, v173\n\t"
            "v_cvt_pk_bf16_f32 v181, v182, v183\n\t"
            "v_cvt_pk_bf16_f32 v173, v174, v175\n\t"
            "v_cvt_pk_bf16_f32 v208, v208, 0\n\t"
            "v_cvt_pk_bf16_f32 v182, v204, v205\n\t"
            "v_cvt_pk_bf16_f32 v183, v206, v207\n\t"
            "v_cndmask_b32_e64 v174, 0, v208, s[58:59]\n\t"
            "v_mov_b32 v175, 0\n\t"
            ".Lwa_in%=:\n\t"
            "s_barrier\n\t"
            "s_setprio 2\n\t"
            "v_mfma_f32_16x16x32_bf16 v[200:203], v[240:243], v[148:151], 0\n\t"
            "ds_read_b64 v[176:177], v247\n\t"
            "ds_read_b64 v[196:197], v247 offset:1536\n\t"
            "v_mfma_f32_16x16x32_bf16 v[200:203], v[240:243], v[164:167], v[200:203]\n\t"
            "ds_read_b64 v[220:221], v247 offset:3072\n\t"
            "v_xor_b32 v234, s73, v254\n\t"
            "v_add_u32 v235, s45, v248\n\t"
            "v_mov_b32 v179, 0\n\t"
            "v_mov_b32 v199, 0\n\t"
            "v_add_u32 v232, s48, v244\n\t"
            "v_xor_b32 v233, 64, v232\n\t"
            "v_mov_b32 v223, 0\n\t"
            "v_rcp_f32 v201, v200\n\t"
            "ds_read_b128 v[168:171], v235\n\t"
            "ds_read_u16 v178, v234\n\t"
            "ds_read_b128 v[192:195], v235 offset:1536\n\t"
            "ds_read_u16 v198, v234 offset:128\n\t"
            "ds_read_b128 v[216:219], v235 offset:3072\n\t"
            "ds_read_u16 v222, v234 offset:256\n\t"
            "ds_read_b128 v[204:207], v232\n\t"
            "ds_read_b128 v[208:211], v233\n\t"
            "v_add_u32 v235, s48, v248\n\t"
            "v_mad_u32_u24 v234, v246, s75, v253\n\t"
            "ds_read_b128 v[228:231], v235 offset:18816\n\t"
            "s_waitcnt lgkmcnt(7)\n\t"
            "v_mfma_f32_16x16x32_bf16 v[224:227], v[168:171], v[148:151], 0\n\t"
            "v_mfma_f32_16x16x32_bf16 v[224:227], v[176:179], v[164:167], v[224:227]\n\t"
            "s_waitcnt lgkmcnt(5)\n\t"
            "v_mfma_f32_16x16x32_bf16 v[212:215], v[192:195], v[148:151], 0\n\t"
            "s_waitcnt lgkmcnt(0)\n\t"
            "v_add_f32 v236, v228, v230\n\t"
            "v_mfma_f32_16x16x32_bf16 v[212:215], v[196:199], v[164:167], v[212:215]\n\t"
            "v_add_f32 v237, v229, v231\n\t"
            "v_mfma_f32_16x16x32_bf16 v[148:151], v[216:219], v[148:151], 0\n\t"
            "ds_read_b128 v[228:231], v235 offset:19840\n\t"
            "v_mfma_f32_16x16x32_bf16 v[148:151], v[220:223], v[164:167], v[148:151]\n\t"
            "v_mul_f32 v224, v201, v224\n\t"
            "v_mul_f32 v225, v201, v225\n\t"
            "v_mfma_f32_16x16x32_bf16 v[144:147], v[0:3], v[204:207], 0\n\t"
            "s_nop 0\n\t"
            "v_mul_f32 v212, v201, v212\n\t"
            "v_mul_f32 v213, v201, v213\n\t"
            "v_mfma_f32_16x16x32_bf16 v[144:147], v[4:7], v[208:211], v[144:147]\n\t"
            "v_mul_f32 v226, v201, v226\n\t"
            "v_mul_f32 v227, v201, v227\n\t"
            "v_mfma_f32_16x16x32_bf16 v[152:155], v[48:51], v[204:207], 0\n\t"
            "v_cvt_pk_bf16_f32 v224, v224, v225\n\t"
            "v_mul_f32 v214, v201, v214\n\t"
            "v_mfma_f32_16x16x32_bf16 v[152:155], v[52:55], v[208:211], v[152:155]\n\t"
            "v_mul_f32 v215, v201, v215\n\t"
            "v_cvt_pk_bf16_f32 v212, v212, v213\n\t"
            "v_mfma_f32_16x16x32_bf16 v[156:159], v[76:79], v[208:211], 0\n\t"
            "v_cvt_pk_bf16_f32 v225, v226, v227\n\t"
            "v_cvt_pk_bf16_f32 v213, v214, v215\n\t"
            "v_mfma_f32_16x16x32_bf16 v[156:159], v[72:75], v[204:207], v[156:159]\n\t"
            "ds_write_b64 v245, v[224:225]\n\t"
            "v_mfma_f32_16x16x32_bf16 v[224:227], v[168:171], v[184:187], 0\n\t"
            "v_mul_f32 v148, v201, v148\n\t"
            "v_mul_f32 v149, v201, v149\n\t"
            "v_mfma_f32_16x16x32_bf16 v[224:227], v[176:179], v[188:191], v[224:227]\n\t"
            "ds_write_b64 v245, v[212:213] offset:128\n\t"
            "v_mfma_f32_16x16x32_bf16 v[212:215], v[192:195], v[184:187], 0\n\t"
            "v_mul_f32 v150, v201, v150\n\t"
            "v_mul_f32 v151, v201, v151\n\t"
            "v_mfma_f32_16x16x32_bf16 v[212:215], v[196:199], v[188:191], v[212:215]\n\t"
            "v_cvt_pk_bf16_f32 v148, v148, v149\n\t"
            "v_cvt_pk_bf16_f32 v149, v150, v151\n\t"
            "v_mfma_f32_16x16x32_bf16 v[200:203], v[240:243], v[184:187], 0\n\t"
            "s_waitcnt lgkmcnt(2)\n\t"
            "v_add_f32 v228, v228, v230\n\t"
            "v_add_f32 v236, v236, v228\n\t"
            "v_mfma_f32_16x16x32_bf16 v[200:203], v[240:243], v[188:191], v[200:203]\n\t"
            "ds_write_b64 v245, v[148:149] offset:256\n\t"
            "v_mfma_f32_16x16x32_bf16 v[184:187], v[216:219], v[184:187], 0\n\t"
            "v_add_f32 v229, v229, v231\n\t"
            "v_mul_f32 v236, 0x3baaaaab, v236\n\t"
            "v_mfma_f32_16x16x32_bf16 v[184:187], v[220:223], v[188:191], v[184:187]\n\t"
            "v_add_f32 v237, v237, v229\n\t"
            "v_mul_f32_e64 v236, v236, -v236\n\t"
            "v_mfma_f32_16x16x32_bf16 v[148:151], v[28:31], v[208:211], 0\n\t"
            "v_rcp_f32 v201, v200\n\t"
            "v_mfma_f32_16x16x32_bf16 v[148:151], v[24:27], v[204:207], v[148:151]\n\t"
            "v_mul_f32 v224, v201, v224\n\t"
            "v_mul_f32 v225, v201, v225\n\t"
            "v_mfma_f32_16x16x32_bf16 v[160:163], v[96:99], v[204:207], 0\n\t"
            "v_mul_f32 v226, v201, v226\n\t"
            "v_mul_f32 v227, v201, v227\n\t"
            "v_mfma_f32_16x16x32_bf16 v[160:163], v[100:103], v[208:211], v[160:163]\n\t"
            "v_mul_f32 v212, v201, v212\n\t"
            "v_mul_f32 v213, v201, v213\n\t"
            "v_mfma_f32_16x16x32_bf16 v[164:167], v[124:127], v[208:211], 0\n\t"
            "ds_read_b128 v[208:211], v233 offset:128\n\t"
            "v_mul_f32 v214, v201, v214\n\t"
            "v_mfma_f32_16x16x32_bf16 v[164:167], v[120:123], v[204:207], v[164:167]\n\t"
            "ds_read_b128 v[204:207], v232 offset:128\n\t"
            "v_mul_f32 v215, v201, v215\n\t"
            "v_mfma_f32_16x16x32_bf16 v[188:191], v[192:195], v[180:183], 0\n\t"
            "v_mul_f32 v184, v201, v184\n\t"
            "v_mul_f32 v185, v201, v185\n\t"
            "v_mfma_f32_16x16x32_bf16 v[188:191], v[196:199], v[172:175], v[188:191]\n\t"
            "v_mul_f32 v186, v201, v186\n\t"
            "v_mul_f32 v187, v201, v187\n\t"
            "v_mfma_f32_16x16x32_bf16 v[200:203], v[240:243], v[180:183], 0\n\t"
            "v_cvt_pk_bf16_f32 v224, v224, v225\n\t"
            "v_cvt_pk_bf16_f32 v225, v226, v227\n\t"
            "v_mfma_f32_16x16x32_bf16 v[200:203], v[240:243], v[172:175], v[200:203]\n\t"
            "ds_write_b64 v245, v[224:225] offset:6144\n\t"
            "v_mfma_f32_16x16x32_bf16 v[224:227], v[168:171], v[180:183], 0\n\t"
            "v_cvt_pk_bf16_f32 v184, v184, v185\n\t"
            "v_cvt_pk_bf16_f32 v185, v186, v187\n\t"
            "v_mfma_f32_16x16x32_bf16 v[224:227], v[176:179], v[172:175], v[224:227]\n\t"
            "ds_read_b64 v[186:187], v234 offset:288\n\t"
            "ds_write_b64 v245, v[184:185] offset:6400\n\t"
            "v_mfma_f32_16x16x32_bf16 v[180:183], v[216:219], v[180:183], 0\n\t"
            "ds_read_b64 v[184:185], v234 offset:256\n\t"
            "v_cvt_pk_bf16_f32 v212, v212, v213\n\t"
            "v_mfma_f32_16x16x32_bf16 v[180:183], v[220:223], v[172:175], v[180:183]\n\t"
            "v_rcp_f32 v201, v200\n\t"
            "s_waitcnt lgkmcnt(4)\n\t"
            "v_mfma_f32_16x16x32_bf16 v[144:147], v[8:11], v[204:207], v[144:147]\n\t"
            "v_mul_f32 v188, v201, v188\n\t"
            "v_mul_f32 v189, v201, v189\n\t"
            "v_mfma_f32_16x16x32_bf16 v[144:147], v[12:15], v[208:211], v[144:147]\n\t"
            "v_mul_f32 v190, v201, v190\n\t"
            "v_mul_f32 v191, v201, v191\n\t"
            "v_mfma_f32_16x16x32_bf16 v[148:151], v[36:39], v[208:211], v[148:151]\n\t"
            "v_cvt_pk_bf16_f32 v188, v188, v189\n\t"
            "v_cvt_pk_bf16_f32 v189, v190, v191\n\t"
            "v_mfma_f32_16x16x32_bf16 v[148:151], v[32:35], v[204:207], v[148:151]\n\t"
            "ds_read_b64 v[190:191], v234 offset:352\n\t"
            "ds_write_b64 v245, v[188:189] offset:12416\n\t"
            "v_mfma_f32_16x16x32_bf16 v[152:155], v[56:59], v[204:207], v[152:155]\n\t"
            "ds_read_b64 v[188:189], v234 offset:320\n\t"
            "v_mul_f32 v224, v201, v224\n\t"
            "v_mfma_f32_16x16x32_bf16 v[152:155], v[60:63], v[208:211], v[152:155]\n\t"
            "v_mul_f32 v225, v201, v225\n\t"
            "v_mul_f32 v226, v201, v226\n\t"
            "v_mfma_f32_16x16x32_bf16 v[156:159], v[84:87], v[208:211], v[156:159]\n\t"
            "v_mul_f32 v227, v201, v227\n\t"
            "v_cvt_pk_bf16_f32 v224, v224, v225\n\t"
            "v_mfma_f32_16x16x32_bf16 v[156:159], v[80:83], v[204:207], v[156:159]\n\t"
            "v_cvt_pk_bf16_f32 v213, v214, v215\n\t"
            "v_cvt_pk_bf16_f32 v225, v226, v227\n\t"
            "v_mfma_f32_16x16x32_bf16 v[160:163], v[104:107], v[204:207], v[160:163]\n\t"
            "v_mul_f32 v180, v201, v180\n\t"
            "v_mul_f32 v181, v201, v181\n\t"
            "v_mfma_f32_16x16x32_bf16 v[160:163], v[108:111], v[208:211], v[160:163]\n\t"
            "ds_write_b64 v245, v[212:213] offset:6272\n\t"
            "v_mfma_f32_16x16x32_bf16 v[164:167], v[132:135], v[208:211], v[164:167]\n\t"
            "ds_read_b128 v[212:215], v232 offset:256\n\t"
            "ds_read_b128 v[208:211], v233 offset:6144\n\t"
            "v_mfma_f32_16x16x32_bf16 v[164:167], v[128:131], v[204:207], v[164:167]\n\t"
            "ds_read_b128 v[204:207], v232 offset:6144\n\t"
            "ds_write_b64 v245, v[224:225] offset:12288\n\t"
            "s_waitcnt lgkmcnt(5)\n\t"
            "v_mfma_f32_16x16x32_bf16 v[168:171], v[184:187], v[168:171], 0\n\t"
            "ds_read_b128 v[224:227], v233 offset:256\n\t"
            "v_mul_f32 v182, v201, v182\n\t"
            "v_mfma_f32_16x16x32_bf16 v[168:171], v[188:191], v[176:179], v[168:171]\n\t"
            "v_mul_f32 v183, v201, v183\n\t"
            "v_cvt_pk_bf16_f32 v180, v180, v181\n\t"
            "v_mfma_f32_16x16x32_bf16 v[200:203], v[240:243], v[184:187], 0\n\t"
            "v_cvt_pk_bf16_f32 v181, v182, v183\n\t"
            "v_fmac_f32 v236, 0x3baaaaab, v237\n\t"
            "v_mfma_f32_16x16x32_bf16 v[200:203], v[240:243], v[188:191], v[200:203]\n\t"
            "ds_write_b64 v245, v[180:181] offset:12544\n\t"
            "v_mfma_f32_16x16x32_bf16 v[192:195], v[184:187], v[192:195], 0\n\t"
            "v_max_f32 v236, 0, v236\n\t"
            "v_add_f32 v236, 0x3727c5ac, v236\n\t"
            "v_mfma_f32_16x16x32_bf16 v[192:195], v[188:191], v[196:199], v[192:195]\n\t"
            "v_rsq_f32 v236, v236\n\t"
            "v_mfma_f32_16x16x32_bf16 v[216:219], v[184:187], v[216:219], 0\n\t"
            "s_nop 0\n\t"
            "v_rcp_f32 v201, v200\n\t"
            "v_mfma_f32_16x16x32_bf16 v[216:219], v[188:191], v[220:223], v[216:219]\n\t"
            "ds_write_b32 v249, v236\n\t"
            "s_waitcnt lgkmcnt(2)\n\t"
            "v_mfma_f32_16x16x32_bf16 v[144:147], v[16:19], v[212:215], v[144:147]\n\t"
            "ds_read_b32 v236, v251\n\t"
            "ds_read_b32 v237, v251 offset:64\n\t"
            "v_mfma_f32_16x16x32_bf16 v[144:147], v[20:23], v[224:227], v[144:147]\n\t"
            "ds_read_b32 v238, v251 offset:128\n\t"
            "v_cndmask_b32_e64 v203, v168, v192, s[60:61]\n\t"
            "v_mfma_f32_16x16x32_bf16 v[148:151], v[44:47], v[224:227], v[148:151]\n\t"
            "v_cndmask_b32_e64 v203, v203, v216, s[62:63]\n\t"
            "v_xad_u32 v202, s73, v254, v255\n\t"
            "v_mfma_f32_16x16x32_bf16 v[148:151], v[40:43], v[212:215], v[148:151]\n\t"
            "v_mul_f32_dpp v203, v201, v203 quad_perm:[0,0,0,0] row_mask:0xf bank_mask:0xf\n\t"
            "v_cvt_pk_bf16_f32 v203, v203, 0\n\t"
            "v_mfma_f32_16x16x32_bf16 v[152:155], v[64:67], v[212:215], v[152:155]\n\t"
            "v_mfma_f32_16x16x32_bf16 v[152:155], v[68:71], v[224:227], v[152:155]\n\t"
            "s_mov_b64 exec, s[68:69]\n\t"
            "ds_write_b16 v202, v203\n\t"
            "s_mov_b64 exec, 1\n\t"
            "ds_add_u32 v253, v246 offset:7856\n\t"
            "s_mov_b64 exec, -1\n\t"
            "v_mfma_f32_16x16x32_bf16 v[156:159], v[92:95], v[224:227], v[156:159]\n\t"
            "s_waitcnt lgkmcnt(4)\n\t"
            "v_mul_f32 v144, v236, v144\n\t"
            "v_mfma_f32_16x16x32_bf16 v[156:159], v[88:91], v[212:215], v[156:159]\n\t"
            "s_cmp_eq_u32 s44, 0\n\t"
            "s_cbranch_scc1 .Lwa_done%=\n\t"
            "v_mul_f32 v145, v236, v145\n\t"
            "v_mul_f32 v152, v236, v152\n\t"
            "v_mfma_f32_16x16x32_bf16 v[160:163], v[112:115], v[212:215], v[160:163]\n\t"
            "v_mul_f32 v153, v236, v153\n\t"
            "v_mul_f32 v146, v236, v146\n\t"
            "v_mfma_f32_16x16x32_bf16 v[160:163], v[116:119], v[224:227], v[160:163]\n\t"
            "v_mul_f32 v147, v236, v147\n\t"
            "v_mul_f32 v154, v236, v154\n\t"
            "v_mfma_f32_16x16x32_bf16 v[164:167], v[140:143], v[224:227], v[164:167]\n\t"
            "ds_read_b128 v[224:227], v233 offset:6272\n\t"
            "v_mul_f32 v155, v236, v155\n\t"
            "v_mfma_f32_16x16x32_bf16 v[164:167], v[136:139], v[212:215], v[164:167]\n\t"
            "ds_read_b128 v[212:215], v232 offset:6272\n\t"
            "v_cvt_pk_bf16_f32 v144, v144, v145\n\t"
            "v_mfma_f32_16x16x32_bf16 v[168:171], v[0:3], v[204:207], 0\n\t"
            "v_cvt_pk_bf16_f32 v152, v152, v153\n\t"
            "v_cvt_pk_bf16_f32 v156, v156, v157\n\t"
            "v_mfma_f32_16x16x32_bf16 v[168:171], v[4:7], v[208:211], v[168:171]\n\t"
            "v_mul_f32 v148, v236, v148\n\t"
            "v_mul_f32 v149, v236, v149\n\t"
            "v_mfma_f32_16x16x32_bf16 v[172:175], v[28:31], v[208:211], 0\n\t"
            "v_mul_f32 v150, v236, v150\n\t"
            "v_mul_f32 v151, v236, v151\n\t"
            "v_mfma_f32_16x16x32_bf16 v[172:175], v[24:27], v[204:207], v[172:175]\n\t"
            "v_cvt_pk_bf16_f32 v145, v146, v147\n\t"
            "v_cvt_pk_bf16_f32 v153, v154, v155\n\t"
            "v_mfma_f32_16x16x32_bf16 v[176:179], v[48:51], v[204:207], 0\n\t"
            "v_cvt_pk_bf16_f32 v157, v158, v159\n\t"
            "v_cvt_pk_bf16_f32 v146, v148, v149\n\t"
            "v_mfma_f32_16x16x32_bf16 v[176:179], v[52:55], v[208:211], v[176:179]\n\t"
            "v_cvt_pk_bf16_f32 v147, v150, v151\n\t"
            "v_cvt_pk_bf16_f32 v158, v160, v161\n\t"
            "v_mfma_f32_16x16x32_bf16 v[180:183], v[76:79], v[208:211], 0\n\t"
            "v_cvt_pk_bf16_f32 v159, v162, v163\n\t"
            "v_cvt_pk_bf16_f32 v154, v164, v165\n\t"
            "v_mfma_f32_16x16x32_bf16 v[180:183], v[72:75], v[204:207], v[180:183]\n\t"
            "v_cvt_pk_bf16_f32 v155, v166, v167\n\t"
            "v_mul_f32 v236, 0x3e553b94, v236\n\t"
            "v_mfma_f32_16x16x32_bf16 v[184:187], v[96:99], v[204:207], 0\n\t"
            "v_mfma_f32_16x16x32_bf16 v[184:187], v[100:103], v[208:211], v[184:187]\n\t"
            "v_mfma_f32_16x16x32_bf16 v[188:191], v[124:127], v[208:211], 0\n\t"
            "ds_read_b128 v[208:211], v233 offset:6400\n\t"
            "v_mfma_f32_16x16x32_bf16 v[188:191], v[120:123], v[204:207], v[188:191]\n\t"
            "ds_read_b128 v[204:207], v232 offset:6400\n\t"
            "s_waitcnt lgkmcnt(2)\n\t"
            "v_mfma_f32_16x16x32_bf16 v[168:171], v[8:11], v[212:215], v[168:171]\n\t"
            "v_mfma_f32_16x16x32_bf16 v[168:171], v[12:15], v[224:227], v[168:171]\n\t"
            "v_mfma_f32_16x16x32_bf16 v[172:175], v[36:39], v[224:227], v[172:175]\n\t"
            "v_mfma_f32_16x16x32_bf16 v[172:175], v[32:35], v[212:215], v[172:175]\n\t"
            "v_mfma_f32_16x16x32_bf16 v[176:179], v[56:59], v[212:215], v[176:179]\n\t"
            "v_mfma_f32_16x16x32_bf16 v[176:179], v[60:63], v[224:227], v[176:179]\n\t"
            "v_mfma_f32_16x16x32_bf16 v[180:183], v[84:87], v[224:227], v[180:183]\n\t"
            "v_mfma_f32_16x16x32_bf16 v[180:183], v[80:83], v[212:215], v[180:183]\n\t"
            "v_mfma_f32_16x16x32_bf16 v[184:187], v[104:107], v[212:215], v[184:187]\n\t"
            "v_mfma_f32_16x16x32_bf16 v[184:187], v[108:111], v[224:227], v[184:187]\n\t"
            "v_mfma_f32_16x16x32_bf16 v[188:191], v[132:135], v[224:227], v[188:191]\n\t"
            "ds_read_b128 v[224:227], v233 offset:12288\n\t"
            "v_mfma_f32_16x16x32_bf16 v[188:191], v[128:131], v[212:215], v[188:191]\n\t"
            "ds_read_b128 v[212:215], v232 offset:12288\n\t"
            "s_waitcnt lgkmcnt(2)\n\t"
            "v_mfma_f32_16x16x32_bf16 v[168:171], v[16:19], v[204:207], v[168:171]\n\t"
            "v_mfma_f32_16x16x32_bf16 v[168:171], v[20:23], v[208:211], v[168:171]\n\t"
            "v_mfma_f32_16x16x32_bf16 v[172:175], v[44:47], v[208:211], v[172:175]\n\t"
            "v_mfma_f32_16x16x32_bf16 v[172:175], v[40:43], v[204:207], v[172:175]\n\t"
            "v_mfma_f32_16x16x32_bf16 v[176:179], v[64:67], v[204:207], v[176:179]\n\t"
            "v_mfma_f32_16x16x32_bf16 v[176:179], v[68:71], v[208:211], v[176:179]\n\t"
            "v_mfma_f32_16x16x32_bf16 v[180:183], v[92:95], v[208:211], v[180:183]\n\t"
            "v_mfma_f32_16x16x32_bf16 v[180:183], v[88:91], v[204:207], v[180:183]\n\t"
            "v_mfma_f32_16x16x32_bf16 v[184:187], v[112:115], v[204:207], v[184:187]\n\t"
            "v_mfma_f32_16x16x32_bf16 v[184:187], v[116:119], v[208:211], v[184:187]\n\t"
            "v_mfma_f32_16x16x32_bf16 v[188:191], v[140:143], v[208:211], v[188:191]\n\t"
            "ds_read_b128 v[208:211], v233 offset:12416\n\t"
            "v_mfma_f32_16x16x32_bf16 v[188:191], v[136:139], v[204:207], v[188:191]\n\t"
            "ds_read_b128 v[204:207], v232 offset:12416\n\t"
            "s_branch .Lwa_loop%=\n\t"
            ".Lwa_done%=:\n\t"
            "s_waitcnt lgkmcnt(0)\n\t"
            "s_nop 7\n\t"
            "s_barrier\n\t"
            : "+{v[0:3]}"(W[0]), "+{v[4:7]}"(W[1]), "+{v[8:11]}"(W[2]), "+{v[12:15]}"(W[3]), "+{v[16:19]}"(W[4]), "+{v[20:23]}"(W[5]), "+{v[24:27]}"(W[6]), "+{v[28:31]}"(W[7]), "+{v[32:35]}"(W[8]), "+{v[36:39]}"(W[9]), "+{v[40:43]}"(W[10]), "+{v[44:47]}"(W[11]), "+{v[48:51]}"(W[12]), "+{v[52:55]}"(W[13]), "+{v[56:59]}"(W[14]), "+{v[60:63]}"(W[15]), "+{v[64:67]}"(W[16]), "+{v[68:71]}"(W[17]), "+{v[72:75]}"(W[18]), "+{v[76:79]}"(W[19]), "+{v[80:83]}"(W[20]), "+{v[84:87]}"(W[21]), "+{v[88:91]}"(W[22]), "+{v[92:95]}"(W[23]), "+{v[96:99]}"(W[24]), "+{v[100:103]}"(W[25]), "+{v[104:107]}"(W[26]), "+{v[108:111]}"(W[27]), "+{v[112:115]}"(W[28]), "+{v[116:119]}"(W[29]), "+{v[120:123]}"(W[30]), "+{v[124:127]}"(W[31]), "+{v[128:131]}"(W[32]), "+{v[132:135]}"(W[33]), "+{v[136:139]}"(W[34]), "+{v[140:143]}"(W[35]),
              "+{s44}"(s44), "+{s48}"(s48), "+{s70}"(s70), "+{s72}"(s72), "+{s73}"(s73), "+{s74}"(s74), "+{s76}"(s76), "+{s77}"(s77), "+{s[68:69]}"(s68)
            : "{v244}"(pE_), "{v245}"(aO_), "{v246}"(sel_), "{v247}"(aVB_), "{v248}"(pST_), "{v249}"(aRSw_), "{v250}"(aPLw_), "{v251}"(aRSr_), "{v252}"(g16_), "{v253}"(g8_), "{v254}"(loVL_), "{v255}"(loOL_), "{v[240:243]}"(ones4), "{s47}"((ldsb + X_OFF) ^ (ldsb + X1_OFF)),
              "{s[58:59]}"(0xFFFFull), "{s[60:61]}"(0xFFFF0000ull), "{s[62:63]}"(0xFFFF00000000ull), "{s[64:65]}"(0xFFFF000000000000ull),
              "{s[66:67]}"(0x1FFFFFFFFFFFFull), "{s75}"(rsb), "{s45}"(ldsb + V_OFF + w * 4608)
            : "v144", "v145", "v146", "v147", "v148", "v149", "v150", "v151", "v152", "v153", "v154", "v155", "v156", "v157", "v158", "v159", "v160", "v161", "v162", "v163", "v164", "v165", "v166", "v167", "v168", "v169", "v170", "v171", "v172", "v173", "v174", "v175", "v176", "v177", "v178", "v179", "v180", "v181", "v182", "v183", "v184", "v185", "v186", "v187", "v188", "v189", "v190", "v191", "v192", "v193", "v194", "v195", "v196", "v197", "v198", "v199", "v200", "v201", "v202", "v203", "v204", "v205", "v206", "v207", "v208", "v209", "v210", "v211", "v212", "v213", "v214", "v215", "v216", "v217", "v218", "v219", "v220", "v221", "v222", "v223", "v224", "v225", "v226", "v227", "v228", "v229", "v230", "v231", "v232", "v233", "v234", "v235", "v236", "v237", "v238", "v239",  "s49", "vcc", "scc", "memory");

        }
        asm volatile("wk_code_end:");
    } else if (wave < 6) {
        B_PROLOGUE(true)
        {
            LANE_SETUP
            const unsigned rsb = ldsb + RS_BASE;
            const unsigned pE_ = fE - X_OFF;
            const unsigned aVAw_ = ldsb + V_OFF + w * 9216 + lane * 16, aVBw_ = ldsb + V_OFF + w * 9216 + 1024 + lane * 8;
            const unsigned pST_ = lane * 16, aRSw_ = (lane < 48 ? rsb : ldsb + RS_OFF + 2304 - 192) + lane * 4, aRSr_ = rsb + (lane & 48);
            B_SCALARS
            asm volatile(
            "s_waitcnt lgkmcnt(0)\n\t"
            "s_nop 7\n\t"
            "ds_read_b128 v[168:171], v254\n\t"
            "ds_read_b128 v[172:175], v254 offset:64\n\t"
            "s_waitcnt vmcnt(0)\n\t"
            "ds_read_b64 v[144:145], v253\n\t"
            "ds_read_b64 v[146:147], v253 offset:96\n\t"
            "ds_read_b64 v[148:149], v253 offset:1744\n\t"
            "ds_read_b64 v[150:151], v253 offset:1840\n\t"
            "ds_read_b64 v[152:153], v253 offset:8\n\t"
            "ds_read_b64 v[154:155], v253 offset:104\n\t"
            "ds_read_b64 v[156:157], v253 offset:1752\n\t"
            "ds_read_b64 v[158:159], v253 offset:1848\n\t"
            "ds_read_b64 v[160:161], v253 offset:16\n\t"
            "ds_read_b64 v[162:163], v253 offset:112\n\t"
            "ds_read_b64 v[164:165], v253 offset:1760\n\t"
            "ds_read_b64 v[166:167], v253 offset:1856\n\t"
            "s_waitcnt lgkmcnt(0)\n\t"
            "s_nop 7\n\t"
            ".Lwc_loop%=:\n\t"
            "s_setprio 1\n\t"
            "v_mov_b64 v[176:177], 0\n\t"
            "v_mov_b64 v[178:179], 0\n\t"
            "v_mov_b64 v[180:181], 0\n\t"
            "v_mov_b64 v[182:183], 0\n\t"
            "s_setprio 3\n\t"
            "v_smfmac_f32_16x16x64_bf16 v[176:179], v[168:171], v[144:151], v255\n\t"
            "ds_read_b128 v[168:171], v254 offset:128\n\t"
            "v_smfmac_f32_16x16x64_bf16 v[180:183], v[172:175], v[152:159], v255\n\t"
            "s_setprio 1\n\t"
            "ds_read_b64 v[144:145], v253 offset:24\n\t"
            "ds_read_b64 v[146:147], v253 offset:120\n\t"
            "ds_read_b64 v[148:149], v253 offset:1768\n\t"
            "ds_read_b64 v[150:151], v253 offset:1864\n\t"
            "ds_read_b128 v[172:175], v254 offset:192\n\t"
            "ds_read_b64 v[152:153], v253 offset:32\n\t"
            "ds_read_b64 v[154:155], v253 offset:128\n\t"
            "ds_read_b64 v[156:157], v253 offset:1776\n\t"
            "ds_read_b64 v[158:159], v253 offset:1872\n\t"
            "v_mov_b32 v249, s76\n\t"
            "v_mov_b64 v[184:185], 0\n\t"
            "v_mov_b64 v[186:187], 0\n\t"
            "v_mov_b64 v[188:189], 0\n\t"
            "v_mov_b64 v[190:191], 0\n\t"
            "v_cvt_pk_bf16_f32 v176, v176, v177\n\t"
            "v_add_u32 v236, s67, v250\n\t"
            "v_cvt_pk_bf16_f32 v177, v178, v179\n\t"
            "v_cvt_pk_bf16_f32 v178, v180, v181\n\t"
            "v_cvt_pk_bf16_f32 v179, v182, v183\n\t"
            "s_setprio 3\n\t"
            "v_mfma_f32_4x4x4_16b_bf16 v[196:199], v[176:177], v[176:177], 0\n\t"
            "ds_read_b32 v249, v249\n\t"
            "v_mfma_f32_16x16x32_bf16 v[192:195], v[238:241], v[176:179], 0\n\t"
            "v_mov_b64 v[180:181], 0\n\t"
            "v_mov_b64 v[182:183], 0\n\t"
            "v_mfma_f32_4x4x4_16b_bf16 v[196:199], v[178:179], v[178:179], v[196:199]\n\t"
            "v_add_u32 v234, s48, v245\n\t"
            "s_waitcnt lgkmcnt(10)\n\t"
            "v_smfmac_f32_16x16x64_bf16 v[184:187], v[168:171], v[160:167], v255\n\t"
            "ds_read_b128 v[168:171], v254 offset:256\n\t"
            "ds_read_b64 v[160:161], v253 offset:40\n\t"
            "ds_read_b64 v[162:163], v253 offset:136\n\t"
            "ds_read_b64 v[164:165], v253 offset:1784\n\t"
            "ds_read_b64 v[166:167], v253 offset:1880\n\t"
            "s_waitcnt lgkmcnt(10)\n\t"
            "v_smfmac_f32_16x16x64_bf16 v[188:191], v[172:175], v[144:151], v255\n\t"
            "s_setprio 1\n\t"
            "ds_read_b128 v[172:175], v254 offset:320\n\t"
            "ds_read_b64 v[144:145], v253 offset:48\n\t"
            "ds_read_b64 v[146:147], v253 offset:144\n\t"
            "ds_read_b64 v[148:149], v253 offset:1792\n\t"
            "ds_read_b64 v[150:151], v253 offset:1888\n\t"
            "s_waitcnt lgkmcnt(14)\n\t"
            "ds_read_b128 v[200:203], v234 offset:18816\n\t"
            "s_waitcnt lgkmcnt(14)\n\t"
            "ds_read_b128 v[204:207], v234 offset:19840\n\t"
            "v_cvt_pk_bf16_f32 v184, v184, v185\n\t"
            "v_cvt_pk_bf16_f32 v185, v186, v187\n\t"
            "v_cvt_pk_bf16_f32 v186, v188, v189\n\t"
            "v_cvt_pk_bf16_f32 v187, v190, v191\n\t"
            "s_setprio 3\n\t"
            "v_mfma_f32_4x4x4_16b_bf16 v[196:199], v[184:185], v[184:185], v[196:199]\n\t"
            "v_mov_b64 v[188:189], 0\n\t"
            "v_mfma_f32_16x16x32_bf16 v[192:195], v[238:241], v[184:187], v[192:195]\n\t"
            "s_waitcnt lgkmcnt(12)\n\t"
            "v_readfirstlane_b32 s26, v249\n\t"
            "v_mov_b64 v[190:191], 0\n\t"
            "v_mfma_f32_4x4x4_16b_bf16 v[196:199], v[186:187], v[186:187], v[196:199]\n\t"
            "ds_read_b32 v249, v252 offset:1024\n\t"
            "s_and_b32 s68, s26, 0x7ffffff\n\t"
            "s_add_u32 s68, s68, s78\n\t"
            "s_and_b32 s69, s26, 0x38000000\n\t"
            "s_or_b32 s69, s69, s39\n\t"
            "s_bitcmp1_b32 s26, 27\n\t"
            "s_cselect_b32 s79, 2.0, s68\n\t"
            "s_and_b32 s57, s56, 3\n\t"
            "s_lshl_b32 s58, s57, 3\n\t"
            "s_lshr_b32 m0, s54, s58\n\t"
            "s_lshl_b32 s58, s57, 4\n\t"
            "s_lshr_b64 s[36:37], s[50:51], s58\n\t"
            "s_and_b32 s57, s36, 0xffff\n\t"
            "s_add_u32 s57, s57, s67\n\t"
            "s_waitcnt lgkmcnt(7)\n\t"
            "v_smfmac_f32_16x16x64_bf16 v[180:183], v[172:175], v[160:167], v255\n\t"
            "s_setprio 1\n\t"
            "s_lshr_b64 s[36:37], s[52:53], s58\n\t"
            "s_and_b32 s58, s36, 0xffff\n\t"
            "s_add_u32 s58, s58, s67\n\t"
            "ds_read_b128 v[172:175], v254 offset:448\n\t"
            "ds_read_b64 v[160:161], v253 offset:64\n\t"
            "ds_read_b64 v[162:163], v253 offset:160\n\t"
            "ds_read_b64 v[164:165], v253 offset:1808\n\t"
            "ds_read_b64 v[166:167], v253 offset:1904\n\t"
            "s_and_b32 s59, s50, 0xffff\n\t"
            "s_add_u32 s59, s59, s67\n\t"
            "s_and_b32 s38, s52, 0xffff\n\t"
            "s_add_u32 s38, s38, s67\n\t"
            "v_writelane_b32 v236, s59, m0\n\t"
            "v_writelane_b32 v236, s57, 63\n\t"
            "v_xor_b32 v237, 16, v236\n\t"
            "v_add_u32 v248, s67, v251\n\t"
            "v_writelane_b32 v248, s38, m0\n\t"
            "s_waitcnt lgkmcnt(5)\n\t"
            "v_and_b32 v249, s69, v249\n\t"
            "v_add_u32 v232, s48, v242\n\t"
            "v_xor_b32 v233, 64, v232\n\t"
            "v_add_f32 v200, v200, v202\n\t"
            "v_add_f32 v204, v204, v206\n\t"
            "v_add_f32 v200, v200, v204\n\t"
            "v_add_f32 v201, v201, v203\n\t"
            "v_add_f32 v205, v205, v207\n\t"
            "v_mul_f32 v200, 0x3baaaaab, v200\n\t"
            "v_add_f32 v201, v201, v205\n\t"
            "v_mul_f32_e64 v200, v200, -v200\n\t"
            "v_fmac_f32 v200, 0x3baaaaab, v201\n\t"
            "v_max_f32 v200, 0, v200\n\t"
            "v_writelane_b32 v248, s58, 63\n\t"
            "v_add_f32 v200, 0x3727c5ac, v200\n\t"
            "v_rsq_f32 v200, v200\n\t"
            "ds_read_b128 v[224:227], v232\n\t"
            "ds_read_b128 v[228:231], v233\n\t"
            "ds_write_b128 v236, v[176:179]\n\t"
            "v_mov_b64 v[176:177], 0\n\t"
            "v_mov_b64 v[178:179], 0\n\t"
            "s_setprio 3\n\t"
            "s_nop 0\n\t"
            "v_smfmac_f32_16x16x64_bf16 v[176:179], v[168:171], v[152:159], v255\n\t"
            "s_setprio 1\n\t"
            "ds_read_b128 v[168:171], v254 offset:384\n\t"
            "ds_read_b64 v[152:153], v253 offset:56\n\t"
            "ds_read_b64 v[154:155], v253 offset:152\n\t"
            "ds_read_b64 v[156:157], v253 offset:1800\n\t"
            "ds_read_b64 v[158:159], v253 offset:1896\n\t"
            "ds_write_b128 v237, v[184:187]\n\t"
            "v_mov_b64 v[184:185], 0\n\t"
            "v_mov_b64 v[186:187], 0\n\t"
            "v_cvt_pk_bf16_f32 v176, v176, v177\n\t"
            "v_cvt_pk_bf16_f32 v177, v178, v179\n\t"
            "v_cvt_pk_bf16_f32 v178, v180, v181\n\t"
            "v_cvt_pk_bf16_f32 v179, v182, v183\n\t"
            "s_setprio 3\n\t"
            "v_mfma_f32_4x4x4_16b_bf16 v[196:199], v[176:177], v[176:177], v[196:199]\n\t"
            "ds_write_b128 v236, v[176:179] offset:128\n\t"
            "v_mfma_f32_16x16x32_bf16 v[192:195], v[238:241], v[176:179], v[192:195]\n\t"
            "v_mov_b64 v[176:177], 0\n\t"
            "v_mov_b64 v[180:181], 0\n\t"
            "v_mfma_f32_4x4x4_16b_bf16 v[196:199], v[178:179], v[178:179], v[196:199]\n\t"
            "v_mov_b64 v[178:179], 0\n\t"
            "s_waitcnt lgkmcnt(8)\n\t"
            "v_mfma_f32_16x16x32_bf16 v[204:207], v[228:231], v[28:31], 0\n\t"
            "v_mov_b64 v[182:183], 0\n\t"
            "v_mfma_f32_16x16x32_bf16 v[204:207], v[224:227], v[24:27], v[204:207]\n\t"
            "ds_write_b32 v246, v200\n\t"
            "s_waitcnt lgkmcnt(7)\n\t"
            "v_smfmac_f32_16x16x64_bf16 v[184:187], v[168:171], v[144:151], v255\n\t"
            "ds_read_b128 v[168:171], v254 offset:512\n\t"
            "s_waitcnt lgkmcnt(4)\n\t"
            "v_smfmac_f32_16x16x64_bf16 v[188:191], v[172:175], v[152:159], v255\n\t"
            "ds_read_b64 v[144:145], v253 offset:72\n\t"
            "v_mfma_f32_16x16x32_bf16 v[200:203], v[224:227], v[0:3], 0\n\t"
            "ds_read_b64 v[146:147], v253 offset:168\n\t"
            "ds_read_b64 v[148:149], v253 offset:1816\n\t"
            "v_mfma_f32_16x16x32_bf16 v[200:203], v[228:231], v[4:7], v[200:203]\n\t"
            "ds_read_b64 v[150:151], v253 offset:1912\n\t"
            "ds_read_b128 v[172:175], v254 offset:576\n\t"
            "v_mfma_f32_16x16x32_bf16 v[208:211], v[224:227], v[48:51], 0\n\t"
            "ds_read_b64 v[152:153], v253 offset:80\n\t"
            "ds_read_b64 v[154:155], v253 offset:176\n\t"
            "v_mfma_f32_16x16x32_bf16 v[208:211], v[228:231], v[52:55], v[208:211]\n\t"
            "ds_read_b64 v[156:157], v253 offset:1824\n\t"
            "ds_read_b64 v[158:159], v253 offset:1920\n\t"
            "v_mfma_f32_16x16x32_bf16 v[212:215], v[228:231], v[76:79], 0\n\t"
            "v_cvt_pk_bf16_f32 v184, v184, v185\n\t"
            "v_cvt_pk_bf16_f32 v185, v186, v187\n\t"
            "v_mfma_f32_16x16x32_bf16 v[212:215], v[224:227], v[72:75], v[212:215]\n\t"
            "v_cvt_pk_bf16_f32 v186, v188, v189\n\t"
            "v_cvt_pk_bf16_f32 v187, v190, v191\n\t"
            "v_mfma_f32_4x4x4_16b_bf16 v[196:199], v[184:185], v[184:185], v[196:199]\n\t"
            "ds_write_b128 v237, v[184:187] offset:128\n\t"
            "s_waitcnt lgkmcnt(10)\n\t"
            "v_smfmac_f32_16x16x64_bf16 v[176:179], v[168:171], v[160:167], v255\n\t"
            "ds_read_b128 v[168:171], v254 offset:640\n\t"
            "v_mfma_f32_16x16x32_bf16 v[192:195], v[238:241], v[184:187], v[192:195]\n\t"
            "ds_read_b64 v[160:161], v253 offset:88\n\t"
            "ds_read_b64 v[162:163], v253 offset:184\n\t"
            "s_waitcnt lgkmcnt(8)\n\t"
            "v_smfmac_f32_16x16x64_bf16 v[180:183], v[172:175], v[144:151], v255\n\t"
            "ds_read_b64 v[164:165], v253 offset:1832\n\t"
            "v_mfma_f32_4x4x4_16b_bf16 v[196:199], v[186:187], v[186:187], v[196:199]\n\t"
            "ds_read_b64 v[166:167], v253 offset:1928\n\t"
            "v_mfma_f32_16x16x32_bf16 v[216:219], v[224:227], v[96:99], 0\n\t"
            "ds_read_b128 v[172:175], v254 offset:704\n\t"
            "v_mov_b64 v[184:185], 0\n\t"
            "v_mfma_f32_16x16x32_bf16 v[216:219], v[228:231], v[100:103], v[216:219]\n\t"
            "v_mov_b64 v[186:187], 0\n\t"
            "v_mov_b64 v[188:189], 0\n\t"
            "v_mfma_f32_16x16x32_bf16 v[220:223], v[228:231], v[124:127], 0\n\t"
            "ds_read_b128 v[228:231], v233 offset:256\n\t"
            "v_mov_b64 v[190:191], 0\n\t"
            "v_mfma_f32_16x16x32_bf16 v[220:223], v[224:227], v[120:123], v[220:223]\n\t"
            "s_setprio 1\n\t"
            "ds_read_b128 v[224:227], v232 offset:256\n\t"
            "v_cvt_pk_bf16_f32 v176, v176, v177\n\t"
            "v_cvt_pk_bf16_f32 v177, v178, v179\n\t"
            "v_cvt_pk_bf16_f32 v178, v180, v181\n\t"
            "v_cvt_pk_bf16_f32 v179, v182, v183\n\t"
            "s_setprio 3\n\t"
            "v_mfma_f32_4x4x4_16b_bf16 v[196:199], v[176:177], v[176:177], v[196:199]\n\t"
            "ds_write_b128 v236, v[176:179] offset:256\n\t"
            "s_waitcnt lgkmcnt(8)\n\t"
            "v_smfmac_f32_16x16x64_bf16 v[184:187], v[168:171], v[152:159], v255\n\t"
            "v_mfma_f32_4x4x4_16b_bf16 v[196:199], v[178:179], v[178:179], v[196:199]\n\t"
            "v_mfma_f32_16x16x32_bf16 v[192:195], v[238:241], v[176:179], v[192:195]\n\t"
            "s_waitcnt lgkmcnt(3)\n\t"
            "v_smfmac_f32_16x16x64_bf16 v[188:191], v[172:175], v[160:167], v255\n\t"
            "s_setprio 1\n\t"
            "s_mov_b32 exec_hi, 0x3fffff\n\t"
            "s_add_u32 m0, s66, 0\n\t"
            "s_add_u32 s49, s79, 0\n\t"
            "buffer_load_dwordx4 v249, s[72:75], s49 offen lds\n\t"
            "s_mov_b32 exec_hi, -1\n\t"
            "s_mov_b32 exec_hi, 0x3fffff\n\t"
            "s_add_u32 m0, s66, 872\n\t"
            "s_add_u32 s49, s68, 196224\n\t"
            "buffer_load_dwordx4 v249, s[72:75], s49 offen lds\n\t"
            "s_mov_b32 exec_hi, -1\n\t"
            "v_cvt_pk_bf16_f32 v184, v184, v185\n\t"
            "v_cvt_pk_bf16_f32 v185, v186, v187\n\t"
            "v_cvt_pk_bf16_f32 v186, v188, v189\n\t"
            "v_cvt_pk_bf16_f32 v187, v190, v191\n\t"
            "s_setprio 3\n\t"
            "v_mfma_f32_4x4x4_16b_bf16 v[196:199], v[184:185], v[184:185], v[196:199]\n\t"
            "s_mov_b32 exec_hi, 0x3fffff\n\t"
            "s_add_u32 m0, s66, 1744\n\t"
            "s_add_u32 s49, s68, 392448\n\t"
            "buffer_load_dwordx4 v249, s[72:75], s49 offen lds\n\t"
            "s_mov_b32 exec_hi, -1\n\t"
            "v_mfma_f32_4x4x4_16b_bf16 v[196:199], v[186:187], v[186:187], v[196:199]\n\t"
            "s_mov_b32 exec_hi, 0x3fffff\n\t"
            "s_add_u32 m0, s66, 2616\n\t"
            "s_add_u32 s49, s68, 588672\n\t"
            "buffer_load_dwordx4 v249, s[72:75], s49 offen lds\n\t"
            "s_mov_b32 exec_hi, -1\n\t"
            "v_mfma_f32_16x16x32_bf16 v[192:195], v[238:241], v[184:187], v[192:195]\n\t"
            "s_setprio 1\n\t"
            "s_mov_b32 exec_hi, 0x3fffff\n\t"
            "s_add_u32 m0, s66, 3488\n\t"
            "s_add_u32 s49, s68, 784896\n\t"
            "buffer_load_dwordx4 v249, s[72:75], s49 offen lds\n\t"
            "s_mov_b32 exec_hi, -1\n\t"
            "s_mov_b32 exec_hi, 0x3fffff\n\t"
            "s_add_u32 m0, s66, 4360\n\t"
            "s_add_u32 s49, s68, 981120\n\t"
            "buffer_load_dwordx4 v249, s[72:75], s49 offen lds\n\t"
            "s_mov_b32 exec_hi, -1\n\t"
            "v_cndmask_b32_e64 v193, v196, v197, s[60:61]\n\t"
            "v_cndmask_b32_e64 v193, v193, v198, s[62:63]\n\t"
            "v_cndmask_b32_e64 v193, v193, v199, s[64:65]\n\t"
            "s_setprio 1\n\t"
            "ds_read_b128 v[196:199], v233 offset:128\n\t"
            "ds_write_b64 v248, v[192:193] offset:18816\n\t"
            "ds_read_b128 v[192:195], v232 offset:128\n\t"
            "s_mov_b32 exec_hi, 0x3fffff\n\t"
            "s_add_u32 m0, s66, 5232\n\t"
            "s_add_u32 s49, s68, 1177344\n\t"
            "buffer_load_dwordx4 v249, s[72:75], s49 offen lds\n\t"
            "s_mov_b32 exec_hi, -1\n\t"
            "s_mov_b32 exec_hi, 0x3fffff\n\t"
            "s_add_u32 m0, s66, 6104\n\t"
            "s_add_u32 s49, s68, 1373568\n\t"
            "buffer_load_dwordx4 v249, s[72:75], s49 offen lds\n\t"
            "s_mov_b32 exec_hi, -1\n\t"
            "s_mov_b32 exec_hi, 0x3fffff\n\t"
            "s_add_u32 m0, s66, 6976\n\t"
            "s_add_u32 s49, s68, 1569792\n\t"
            "buffer_load_dwordx4 v249, s[72:75], s49 offen lds\n\t"
            "s_mov_b32 exec_hi, -1\n\t"
            "s_waitcnt lgkmcnt(0)\n\t"
            "v_mfma_f32_16x16x32_bf16 v[200:203], v[192:195], v[8:11], v[200:203]\n\t"
            "ds_read_b64 v[248:249], v247 offset:8\n\t"
            "v_mfma_f32_16x16x32_bf16 v[200:203], v[196:199], v[12:15], v[200:203]\n\t"
            "ds_write_b128 v237, v[184:187] offset:256\n\t"
            "v_mfma_f32_16x16x32_bf16 v[204:207], v[196:199], v[36:39], v[204:207]\n\t"
            "ds_read_b64 v[236:237], v247\n\t"
            "v_mfma_f32_16x16x32_bf16 v[204:207], v[192:195], v[32:35], v[204:207]\n\t"
            "ds_read_b32 v235, v255 offset:7856\n\t"
            "v_mfma_f32_16x16x32_bf16 v[208:211], v[192:195], v[56:59], v[208:211]\n\t"
            "v_mfma_f32_16x16x32_bf16 v[208:211], v[196:199], v[60:63], v[208:211]\n\t"
            "v_mfma_f32_16x16x32_bf16 v[212:215], v[196:199], v[84:87], v[212:215]\n\t"
            "v_mfma_f32_16x16x32_bf16 v[212:215], v[192:195], v[80:83], v[212:215]\n\t"
            "v_mfma_f32_16x16x32_bf16 v[216:219], v[192:195], v[104:107], v[216:219]\n\t"
            "v_mfma_f32_16x16x32_bf16 v[216:219], v[196:199], v[108:111], v[216:219]\n\t"
            "v_mfma_f32_16x16x32_bf16 v[220:223], v[196:199], v[132:135], v[220:223]\n\t"
            "ds_read_b128 v[196:199], v233 offset:6144\n\t"
            "v_mfma_f32_16x16x32_bf16 v[220:223], v[192:195], v[128:131], v[220:223]\n\t"
            "ds_read_b128 v[192:195], v232 offset:6144\n\t"
            "s_waitcnt lgkmcnt(2)\n\t"
            "v_readfirstlane_b32 s77, v235\n\t"
            "s_cmp_ge_u32 s77, s70\n\t"
            "s_cbranch_scc0 .Lwc_spin%=\n\t"
            ".Lwc_go%=:\n\t"
            "v_mfma_f32_16x16x32_bf16 v[200:203], v[224:227], v[16:19], v[200:203]\n\t"
            "s_add_u32 s45, s46, s27\n\t"
            "s_mov_b32 s46, s47\n\t"
            "s_mov_b32 s47, s68\n\t"
            "v_mfma_f32_16x16x32_bf16 v[200:203], v[228:231], v[20:23], v[200:203]\n\t"
            "s_xor_b32 s48, s48, s71\n\t"
            "s_xor_b32 s67, s67, s71\n\t"
            "s_add_u32 s76, s76, 4\n\t"
            "s_add_u32 s70, s70, 4\n\t"
            "v_mfma_f32_16x16x32_bf16 v[204:207], v[228:231], v[44:47], v[204:207]\n\t"
            "s_lshl_b32 s56, s56, 2\n\t"
            "s_lshr_b32 s49, s26, 30\n\t"
            "s_or_b32 s56, s56, s49\n\t"
            "v_mfma_f32_16x16x32_bf16 v[204:207], v[224:227], v[40:43], v[204:207]\n\t"
            "v_mul_f32 v200, v236, v200\n\t"
            "v_mul_f32 v201, v237, v201\n\t"
            "v_mfma_f32_16x16x32_bf16 v[208:211], v[224:227], v[64:67], v[208:211]\n\t"
            "v_mul_f32 v202, v248, v202\n\t"
            "v_mul_f32 v203, v249, v203\n\t"
            "v_mfma_f32_16x16x32_bf16 v[208:211], v[228:231], v[68:71], v[208:211]\n\t"
            "v_cvt_pk_bf16_f32 v200, v200, v201\n\t"
            "v_cvt_pk_bf16_f32 v201, v202, v203\n\t"
            "v_mfma_f32_16x16x32_bf16 v[212:215], v[228:231], v[92:95], v[212:215]\n\t"
            "v_mul_f32 v204, v236, v204\n\t"
            "v_mul_f32 v205, v237, v205\n\t"
            "v_mfma_f32_16x16x32_bf16 v[212:215], v[224:227], v[88:91], v[212:215]\n\t"
            "v_mul_f32 v206, v248, v206\n\t"
            "v_mul_f32 v207, v249, v207\n\t"
            "v_mfma_f32_16x16x32_bf16 v[216:219], v[224:227], v[112:115], v[216:219]\n\t"
            "v_mul_f32 v208, v236, v208\n\t"
            "v_mul_f32 v209, v237, v209\n\t"
            "v_mfma_f32_16x16x32_bf16 v[216:219], v[228:231], v[116:119], v[216:219]\n\t"
            "v_mul_f32 v210, v248, v210\n\t"
            "v_mul_f32 v211, v249, v211\n\t"
            "v_mfma_f32_16x16x32_bf16 v[220:223], v[228:231], v[140:143], v[220:223]\n\t"
            "ds_read_b128 v[228:231], v233 offset:6272\n\t"
            "v_mul_f32 v212, v236, v212\n\t"
            "v_mfma_f32_16x16x32_bf16 v[220:223], v[224:227], v[136:139], v[220:223]\n\t"
            "ds_read_b128 v[224:227], v232 offset:6272\n\t"
            "v_mul_f32 v213, v237, v213\n\t"
            "s_waitcnt lgkmcnt(2)\n\t"
            "v_mfma_f32_16x16x32_bf16 v[144:147], v[192:195], v[0:3], 0\n\t"
            "v_mul_f32 v216, v236, v216\n\t"
            "v_mul_f32 v217, v237, v217\n\t"
            "v_mfma_f32_16x16x32_bf16 v[144:147], v[196:199], v[4:7], v[144:147]\n\t"
            "v_mul_f32 v214, v248, v214\n\t"
            "v_mul_f32 v215, v249, v215\n\t"
            "v_mfma_f32_16x16x32_bf16 v[148:151], v[196:199], v[28:31], 0\n\t"
            "v_mul_f32 v220, v236, v220\n\t"
            "v_mul_f32 v221, v237, v221\n\t"
            "v_mfma_f32_16x16x32_bf16 v[148:151], v[192:195], v[24:27], v[148:151]\n\t"
            "ds_read_b64 v[236:237], v247 offset:64\n\t"
            "v_mul_f32 v218, v248, v218\n\t"
            "v_mfma_f32_16x16x32_bf16 v[168:171], v[192:195], v[48:51], 0\n\t"
            "v_mul_f32 v219, v249, v219\n\t"
            "v_mul_f32 v222, v248, v222\n\t"
            "v_mfma_f32_16x16x32_bf16 v[168:171], v[196:199], v[52:55], v[168:171]\n\t"
            "v_mul_f32 v223, v249, v223\n\t"
            "v_cvt_pk_bf16_f32 v204, v204, v205\n\t"
            "v_mfma_f32_16x16x32_bf16 v[172:175], v[196:199], v[76:79], 0\n\t"
            "ds_read_b64 v[248:249], v247 offset:72\n\t"
            "v_cvt_pk_bf16_f32 v208, v208, v209\n\t"
            "v_mfma_f32_16x16x32_bf16 v[172:175], v[192:195], v[72:75], v[172:175]\n\t"
            "v_cvt_pk_bf16_f32 v212, v212, v213\n\t"
            "v_cvt_pk_bf16_f32 v216, v216, v217\n\t"
            "v_mfma_f32_16x16x32_bf16 v[152:155], v[192:195], v[96:99], 0\n\t"
            "v_cvt_pk_bf16_f32 v220, v220, v221\n\t"
            "v_cvt_pk_bf16_f32 v205, v206, v207\n\t"
            "v_mfma_f32_16x16x32_bf16 v[152:155], v[196:199], v[100:103], v[152:155]\n\t"
            "v_cvt_pk_bf16_f32 v209, v210, v211\n\t"
            "v_cvt_pk_bf16_f32 v213, v214, v215\n\t"
            "v_mfma_f32_16x16x32_bf16 v[156:159], v[196:199], v[124:127], 0\n\t"
            "ds_read_b128 v[196:199], v233 offset:6400\n\t"
            "v_cvt_pk_bf16_f32 v217, v218, v219\n\t"
            "v_mfma_f32_16x16x32_bf16 v[156:159], v[192:195], v[120:123], v[156:159]\n\t"
            "ds_read_b128 v[192:195], v232 offset:6400\n\t"
            "v_cvt_pk_bf16_f32 v221, v222, v223\n\t"
            "s_waitcnt lgkmcnt(4)\n\t"
            "v_mfma_f32_16x16x32_bf16 v[144:147], v[224:227], v[8:11], v[144:147]\n\t"
            "v_mfma_f32_16x16x32_bf16 v[144:147], v[228:231], v[12:15], v[144:147]\n\t"
            "v_mfma_f32_16x16x32_bf16 v[148:151], v[228:231], v[36:39], v[148:151]\n\t"
            "v_mfma_f32_16x16x32_bf16 v[148:151], v[224:227], v[32:35], v[148:151]\n\t"
            "v_mfma_f32_16x16x32_bf16 v[168:171], v[224:227], v[56:59], v[168:171]\n\t"
            "v_mfma_f32_16x16x32_bf16 v[168:171], v[228:231], v[60:63], v[168:171]\n\t"
            "v_mfma_f32_16x16x32_bf16 v[172:175], v[228:231], v[84:87], v[172:175]\n\t"
            "v_mfma_f32_16x16x32_bf16 v[172:175], v[224:227], v[80:83], v[172:175]\n\t"
            "v_mfma_f32_16x16x32_bf16 v[152:155], v[224:227], v[104:107], v[152:155]\n\t"
            "v_mfma_f32_16x16x32_bf16 v[152:155], v[228:231], v[108:111], v[152:155]\n\t"
            "v_mfma_f32_16x16x32_bf16 v[156:159], v[228:231], v[132:135], v[156:159]\n\t"
            "ds_read_b128 v[228:231], v233 offset:12288\n\t"
            "v_mfma_f32_16x16x32_bf16 v[156:159], v[224:227], v[128:131], v[156:159]\n\t"
            "ds_read_b128 v[224:227], v232 offset:12288\n\t"
            "s_waitcnt lgkmcnt(2)\n\t"
            "v_mfma_f32_16x16x32_bf16 v[144:147], v[192:195], v[16:19], v[144:147]\n\t"
            "v_mfma_f32_16x16x32_bf16 v[144:147], v[196:199], v[20:23], v[144:147]\n\t"
            "v_mfma_f32_16x16x32_bf16 v[148:151], v[196:199], v[44:47], v[148:151]\n\t"
            "v_mfma_f32_16x16x32_bf16 v[148:151], v[192:195], v[40:43], v[148:151]\n\t"
            "v_mfma_f32_16x16x32_bf16 v[168:171], v[192:195], v[64:67], v[168:171]\n\t"
            "v_mfma_f32_16x16x32_bf16 v[168:171], v[196:199], v[68:71], v[168:171]\n\t"
            "v_mfma_f32_16x16x32_bf16 v[172:175], v[196:199], v[92:95], v[172:175]\n\t"
            "v_mfma_f32_16x16x32_bf16 v[172:175], v[192:195], v[88:91], v[172:175]\n\t"
            "s_nop 1\n\t"
            "v_mul_f32 v144, v236, v144\n\t"
            "v_mfma_f32_16x16x32_bf16 v[152:155], v[192:195], v[112:115], v[152:155]\n\t"
            "v_mul_f32 v145, v237, v145\n\t"
            "v_mul_f32 v146, v248, v146\n\t"
            "v_mfma_f32_16x16x32_bf16 v[152:155], v[196:199], v[116:119], v[152:155]\n\t"
            "v_mul_f32 v148, v236, v148\n\t"
            "v_mul_f32 v149, v237, v149\n\t"
            "v_mfma_f32_16x16x32_bf16 v[156:159], v[196:199], v[140:143], v[156:159]\n\t"
            "ds_read_b128 v[196:199], v233 offset:12416\n\t"
            "v_mul_f32 v168, v236, v168\n\t"
            "v_mfma_f32_16x16x32_bf16 v[156:159], v[192:195], v[136:139], v[156:159]\n\t"
            "ds_read_b128 v[192:195], v232 offset:12416\n\t"
            "v_mul_f32 v169, v237, v169\n\t"
            "s_waitcnt lgkmcnt(2)\n\t"
            "v_mfma_f32_16x16x32_bf16 v[160:163], v[224:227], v[0:3], 0\n\t"
            "v_mul_f32 v172, v236, v172\n\t"
            "v_mul_f32 v173, v237, v173\n\t"
            "v_mfma_f32_16x16x32_bf16 v[160:163], v[228:231], v[4:7], v[160:163]\n\t"
            "v_mul_f32 v152, v236, v152\n\t"
            "v_mul_f32 v153, v237, v153\n\t"
            "v_mfma_f32_16x16x32_bf16 v[164:167], v[228:231], v[28:31], 0\n\t"
            "v_mul_f32 v156, v236, v156\n\t"
            "v_mul_f32 v157, v237, v157\n\t"
            "v_mfma_f32_16x16x32_bf16 v[164:167], v[224:227], v[24:27], v[164:167]\n\t"
            "ds_read_b64 v[236:237], v247 offset:128\n\t"
            "v_mul_f32 v147, v249, v147\n\t"
            "v_mfma_f32_16x16x32_bf16 v[176:179], v[224:227], v[48:51], 0\n\t"
            "v_mul_f32 v150, v248, v150\n\t"
            "v_mul_f32 v151, v249, v151\n\t"
            "v_mfma_f32_16x16x32_bf16 v[176:179], v[228:231], v[52:55], v[176:179]\n\t"
            "v_mul_f32 v170, v248, v170\n\t"
            "v_mul_f32 v171, v249, v171\n\t"
            "v_mfma_f32_16x16x32_bf16 v[180:183], v[228:231], v[76:79], 0\n\t"
            "v_mul_f32 v174, v248, v174\n\t"
            "v_mul_f32 v175, v249, v175\n\t"
            "v_mfma_f32_16x16x32_bf16 v[180:183], v[224:227], v[72:75], v[180:183]\n\t"
            "v_mul_f32 v154, v248, v154\n\t"
            "v_mul_f32 v155, v249, v155\n\t"
            "v_mfma_f32_16x16x32_bf16 v[184:187], v[224:227], v[96:99], 0\n\t"
            "v_mul_f32 v158, v248, v158\n\t"
            "v_mul_f32 v159, v249, v159\n\t"
            "v_mfma_f32_16x16x32_bf16 v[184:187], v[228:231], v[100:103], v[184:187]\n\t"
            "ds_read_b64 v[248:249], v247 offset:136\n\t"
            "v_cvt_pk_bf16_f32 v202, v144, v145\n\t"
            "v_mfma_f32_16x16x32_bf16 v[188:191], v[228:231], v[124:127], 0\n\t"
            "ds_read_b128 v[228:231], v233 offset:12544\n\t"
            "v_cvt_pk_bf16_f32 v203, v146, v147\n\t"
            "v_mfma_f32_16x16x32_bf16 v[188:191], v[224:227], v[120:123], v[188:191]\n\t"
            "ds_read_b128 v[224:227], v232 offset:12544\n\t"
            "v_cvt_pk_bf16_f32 v206, v148, v149\n\t"
            "s_waitcnt lgkmcnt(4)\n\t"
            "v_mfma_f32_16x16x32_bf16 v[160:163], v[192:195], v[8:11], v[160:163]\n\t"
            "s_waitcnt vmcnt(0)\n\t"
            "ds_read_b64 v[144:145], v253\n\t"
            "ds_read_b64 v[146:147], v253 offset:96\n\t"
            "v_mfma_f32_16x16x32_bf16 v[160:163], v[196:199], v[12:15], v[160:163]\n\t"
            "ds_read_b64 v[148:149], v253 offset:1744\n\t"
            "v_cvt_pk_bf16_f32 v207, v150, v151\n\t"
            "v_mfma_f32_16x16x32_bf16 v[164:167], v[196:199], v[36:39], v[164:167]\n\t"
            "ds_read_b64 v[150:151], v253 offset:1840\n\t"
            "v_cvt_pk_bf16_f32 v210, v168, v169\n\t"
            "v_mfma_f32_16x16x32_bf16 v[164:167], v[192:195], v[32:35], v[164:167]\n\t"
            "v_cvt_pk_bf16_f32 v211, v170, v171\n\t"
            "v_cvt_pk_bf16_f32 v214, v172, v173\n\t"
            "v_mfma_f32_16x16x32_bf16 v[176:179], v[192:195], v[56:59], v[176:179]\n\t"
            "ds_read_b128 v[168:171], v254\n\t"
            "v_cvt_pk_bf16_f32 v215, v174, v175\n\t"
            "v_mfma_f32_16x16x32_bf16 v[176:179], v[196:199], v[60:63], v[176:179]\n\t"
            "ds_read_b128 v[172:175], v254 offset:64\n\t"
            "v_cvt_pk_bf16_f32 v218, v152, v153\n\t"
            "v_mfma_f32_16x16x32_bf16 v[180:183], v[196:199], v[84:87], v[180:183]\n\t"
            "ds_read_b64 v[152:153], v253 offset:8\n\t"
            "v_cvt_pk_bf16_f32 v219, v154, v155\n\t"
            "v_mfma_f32_16x16x32_bf16 v[180:183], v[192:195], v[80:83], v[180:183]\n\t"
            "ds_read_b64 v[154:155], v253 offset:104\n\t"
            "v_cvt_pk_bf16_f32 v222, v156, v157\n\t"
            "v_mfma_f32_16x16x32_bf16 v[184:187], v[192:195], v[104:107], v[184:187]\n\t"
            "ds_read_b64 v[156:157], v253 offset:1752\n\t"
            "v_cvt_pk_bf16_f32 v223, v158, v159\n\t"
            "v_mfma_f32_16x16x32_bf16 v[184:187], v[196:199], v[108:111], v[184:187]\n\t"
            "ds_read_b64 v[158:159], v253 offset:1848\n\t"
            "ds_write_b128 v243, v[200:203]\n\t"
            "v_mfma_f32_16x16x32_bf16 v[188:191], v[196:199], v[132:135], v[188:191]\n\t"
            "v_mfma_f32_16x16x32_bf16 v[188:191], v[192:195], v[128:131], v[188:191]\n\t"
            "s_waitcnt lgkmcnt(14)\n\t"
            "ds_write_b128 v243, v[204:207] offset:1536\n\t"
            "s_waitcnt lgkmcnt(12)\n\t"
            "v_mfma_f32_16x16x32_bf16 v[160:163], v[224:227], v[16:19], v[160:163]\n\t"
            "v_mfma_f32_16x16x32_bf16 v[160:163], v[228:231], v[20:23], v[160:163]\n\t"
            "ds_write_b128 v243, v[208:211] offset:3072\n\t"
            "v_mfma_f32_16x16x32_bf16 v[164:167], v[228:231], v[44:47], v[164:167]\n\t"
            "v_mfma_f32_16x16x32_bf16 v[164:167], v[224:227], v[40:43], v[164:167]\n\t"
            "ds_write_b128 v243, v[212:215] offset:4608\n\t"
            "v_mfma_f32_16x16x32_bf16 v[176:179], v[224:227], v[64:67], v[176:179]\n\t"
            "v_mfma_f32_16x16x32_bf16 v[176:179], v[228:231], v[68:71], v[176:179]\n\t"
            "ds_write_b128 v243, v[216:219] offset:6144\n\t"
            "v_mfma_f32_16x16x32_bf16 v[180:183], v[228:231], v[92:95], v[180:183]\n\t"
            "v_mul_f32 v160, v236, v160\n\t"
            "v_mul_f32 v161, v237, v161\n\t"
            "v_mfma_f32_16x16x32_bf16 v[180:183], v[224:227], v[88:91], v[180:183]\n\t"
            "v_mul_f32 v164, v236, v164\n\t"
            "v_mul_f32 v165, v237, v165\n\t"
            "v_mfma_f32_16x16x32_bf16 v[184:187], v[224:227], v[112:115], v[184:187]\n\t"
            "v_mul_f32 v176, v236, v176\n\t"
            "v_mul_f32 v177, v237, v177\n\t"
            "v_mfma_f32_16x16x32_bf16 v[184:187], v[228:231], v[116:119], v[184:187]\n\t"
            "s_waitcnt lgkmcnt(14)\n\t"
            "ds_write_b128 v243, v[220:223] offset:7680\n\t"
            "v_mfma_f32_16x16x32_bf16 v[188:191], v[228:231], v[140:143], v[188:191]\n\t"
            "v_mul_f32 v180, v236, v180\n\t"
            "v_mul_f32 v181, v237, v181\n\t"
            "v_mfma_f32_16x16x32_bf16 v[188:191], v[224:227], v[136:139], v[188:191]\n\t"
            "v_mul_f32 v162, v248, v162\n\t"
            "v_mul_f32 v163, v249, v163\n\t"
            "v_cvt_pk_bf16_f32 v160, v160, v161\n\t"
            "v_mul_f32 v184, v236, v184\n\t"
            "v_mul_f32 v185, v237, v185\n\t"
            "v_mul_f32 v166, v248, v166\n\t"
            "v_mul_f32 v167, v249, v167\n\t"
            "v_cvt_pk_bf16_f32 v164, v164, v165\n\t"
            "v_mul_f32 v188, v236, v188\n\t"
            "v_mul_f32 v189, v237, v189\n\t"
            "v_mul_f32 v178, v248, v178\n\t"
            "v_mul_f32 v179, v249, v179\n\t"
            "v_cvt_pk_bf16_f32 v176, v176, v177\n\t"
            "v_mul_f32 v182, v248, v182\n\t"
            "v_mul_f32 v183, v249, v183\n\t"
            "v_cvt_pk_bf16_f32 v180, v180, v181\n\t"
            "v_mul_f32 v186, v248, v186\n\t"
            "v_mul_f32 v187, v249, v187\n\t"
            "v_cvt_pk_bf16_f32 v184, v184, v185\n\t"
            "v_mul_f32 v190, v248, v190\n\t"
            "v_mul_f32 v191, v249, v191\n\t"
            "v_cvt_pk_bf16_f32 v188, v188, v189\n\t"
            "v_cvt_pk_bf16_f32 v161, v162, v163\n\t"
            "v_cvt_pk_bf16_f32 v165, v166, v167\n\t"
            "v_cvt_pk_bf16_f32 v177, v178, v179\n\t"
            "v_cvt_pk_bf16_f32 v181, v182, v183\n\t"
            "v_cvt_pk_bf16_f32 v185, v186, v187\n\t"
            "v_cvt_pk_bf16_f32 v189, v190, v191\n\t"
            "s_waitcnt lgkmcnt(14)\n\t"
            "ds_read_b64 v[162:163], v253 offset:112\n\t"
            "s_waitcnt lgkmcnt(14)\n\t"
            "ds_read_b64 v[166:167], v253 offset:1856\n\t"
            "s_waitcnt lgkmcnt(14)\n\t"
            "ds_write_b64 v244, v[160:161]\n\t"
            "s_waitcnt lgkmcnt(14)\n\t"
            "ds_read_b64 v[160:161], v253 offset:16\n\t"
            "s_waitcnt lgkmcnt(14)\n\t"
            "ds_write_b64 v244, v[164:165] offset:1536\n\t"
            "s_waitcnt lgkmcnt(14)\n\t"
            "ds_read_b64 v[164:165], v253 offset:1760\n\t"
            "s_waitcnt lgkmcnt(14)\n\t"
            "ds_write_b64 v244, v[176:177] offset:3072\n\t"
            "s_waitcnt lgkmcnt(14)\n\t"
            "ds_write_b64 v244, v[180:181] offset:4608\n\t"
            "s_waitcnt lgkmcnt(14)\n\t"
            "ds_write_b64 v244, v[184:185] offset:6144\n\t"
            "s_waitcnt lgkmcnt(14)\n\t"
            "ds_write_b64 v244, v[188:189] offset:7680\n\t"
            "s_waitcnt lgkmcnt(0)\n\t"
            "s_barrier\n\t"
            "s_cmp_lt_u32 s46, 0x7000000\n\t"
            "s_cbranch_scc1 .Lwc_loop%=\n\t"
            "s_branch .Lwc_end%=\n\t"
            ".Lwc_spin%=:\n\t"
            "s_sleep 1\n\t"
            "ds_read_b32 v235, v255 offset:7856\n\t"
            "s_waitcnt lgkmcnt(0)\n\t"
            "v_readfirstlane_b32 s77, v235\n\t"
            "s_cmp_ge_u32 s77, s70\n\t"
            "s_cbranch_scc0 .Lwc_spin%=\n\t"
            "s_branch .Lwc_go%=\n\t"
            ".Lwc_end%=:\n\t"
            "s_waitcnt lgkmcnt(0)\n\t"
            "s_nop 7\n\t"
            "s_barrier\n\t"
            : "+{v[0:3]}"(W[0]), "+{v[4:7]}"(W[1]), "+{v[8:11]}"(W[2]), "+{v[12:15]}"(W[3]), "+{v[16:19]}"(W[4]), "+{v[20:23]}"(W[5]), "+{v[24:27]}"(W[6]), "+{v[28:31]}"(W[7]), "+{v[32:35]}"(W[8]), "+{v[36:39]}"(W[9]), "+{v[40:43]}"(W[10]), "+{v[44:47]}"(W[11]), "+{v[48:51]}"(W[12]), "+{v[52:55]}"(W[13]), "+{v[56:59]}"(W[14]), "+{v[60:63]}"(W[15]), "+{v[64:67]}"(W[16]), "+{v[68:71]}"(W[17]), "+{v[72:75]}"(W[18]), "+{v[76:79]}"(W[19]), "+{v[80:83]}"(W[20]), "+{v[84:87]}"(W[21]), "+{v[88:91]}"(W[22]), "+{v[92:95]}"(W[23]), "+{v[96:99]}"(W[24]), "+{v[100:103]}"(W[25]), "+{v[104:107]}"(W[26]), "+{v[108:111]}"(W[27]), "+{v[112:115]}"(W[28]), "+{v[116:119]}"(W[29]), "+{v[120:123]}"(W[30]), "+{v[124:127]}"(W[31]), "+{v[128:131]}"(W[32]), "+{v[132:135]}"(W[33]), "+{v[136:139]}"(W[34]), "+{v[140:143]}"(W[35]), "+{v[200:203]}"(xl[0]), "+{v[204:207]}"(xl[1]), "+{v[208:211]}"(xl[2]), "+{v[212:215]}"(xl[3]), "+{v[216:219]}"(xl[4]), "+{v[220:223]}"(xl[5]), "+{v[224:227]}"(xl[6]), "+{v[228:231]}"(xl[7]), "+{v[232:235]}"(xl[8]), B_SIO
            : "{v242}"(pE_), "{v243}"(aVAw_), "{v244}"(aVBw_), "{v245}"(pST_), "{v246}"(aRSw_), "{v247}"(aRSr_), "{v250}"(pXw_), "{v251}"(pSTw_), "{v252}"(aCA_), "{v253}"(aB_), "{v254}"(aA_), "{v255}"(idx_), B_SIN
            : "v144", "v145", "v146", "v147", "v148", "v149", "v150", "v151", "v152", "v153", "v154", "v155", "v156", "v157", "v158", "v159", "v160", "v161", "v162", "v163", "v164", "v165", "v166", "v167", "v168", "v169", "v170", "v171", "v172", "v173", "v174", "v175", "v176", "v177", "v178", "v179", "v180", "v181", "v182", "v183", "v184", "v185", "v186", "v187", "v188", "v189", "v190", "v191", "v192", "v193", "v194", "v195", "v196", "v197", "v198", "v199", "v236", "v237", "v248", "v249", B_SCLOB);
        }
    } else {
        B_PROLOGUE(false)
        unsigned t45, t56;
        {
            LANE_SETUP
            const unsigned oE_ = ldsb + fE + (O_OFF - X_OFF), oO_ = oE_ ^ 64;
            unsigned l16_ = lane * 16;
            unsigned pr[3];
#pragma unroll
            for (int k = 0; k < 3; ++k) {
                const int c8 = 12 * (w - 2) + 4 * k + g;
                const int ws = c8 / 6, r6 = c8 - 6 * ws;
                pr[k] = l15 * 384 + (r6 >> 1) * 128 + (((2 * ws + (r6 & 1)) ^ sw) << 4);
            }
            const unsigned pR0_ = pr[0], pR1_ = pr[1], pR2_ = pr[2];
            B_SCALARS
            asm volatile(
            "s_waitcnt lgkmcnt(0)\n\t"
            "s_nop 7\n\t"
            "s_setprio 1\n\t"
            "ds_read_b128 v[168:171], v254\n\t"
            "ds_read_b128 v[172:175], v254 offset:64\n\t"
            "s_waitcnt vmcnt(0)\n\t"
            "ds_read_b64 v[144:145], v253\n\t"
            "ds_read_b64 v[146:147], v253 offset:96\n\t"
            "ds_read_b64 v[148:149], v253 offset:1744\n\t"
            "ds_read_b64 v[150:151], v253 offset:1840\n\t"
            "ds_read_b64 v[152:153], v253 offset:8\n\t"
            "ds_read_b64 v[154:155], v253 offset:104\n\t"
            "ds_read_b64 v[156:157], v253 offset:1752\n\t"
            "ds_read_b64 v[158:159], v253 offset:1848\n\t"
            "ds_read_b64 v[160:161], v253 offset:16\n\t"
            "ds_read_b64 v[162:163], v253 offset:112\n\t"
            "ds_read_b64 v[164:165], v253 offset:1760\n\t"
            "ds_read_b64 v[166:167], v253 offset:1856\n\t"
            "v_mov_b64 v[176:177], 0\n\t"
            "v_mov_b64 v[178:179], 0\n\t"
            "v_mov_b64 v[180:181], 0\n\t"
            "v_mov_b64 v[182:183], 0\n\t"
            "v_mov_b32 v249, s76\n\t"
            "v_mov_b64 v[184:185], 0\n\t"
            "v_mov_b64 v[186:187], 0\n\t"
            "v_mov_b64 v[188:189], 0\n\t"
            "v_mov_b64 v[190:191], 0\n\t"
            "v_add_u32 v236, s67, v250\n\t"
            "v_add_u32 v248, s67, v251\n\t"
            "s_nop 0\n\t"
            "ds_read_b32 v249, v249\n\t"
            "s_waitcnt lgkmcnt(9)\n\t"
            "v_smfmac_f32_16x16x64_bf16 v[176:179], v[168:171], v[144:151], v255\n\t"
            "ds_read_b128 v[168:171], v254 offset:128\n\t"
            "s_waitcnt lgkmcnt(6)\n\t"
            "v_smfmac_f32_16x16x64_bf16 v[180:183], v[172:175], v[152:159], v255\n\t"
            "ds_read_b64 v[144:145], v253 offset:24\n\t"
            "ds_read_b64 v[146:147], v253 offset:120\n\t"
            "ds_read_b64 v[148:149], v253 offset:1768\n\t"
            "ds_read_b64 v[150:151], v253 offset:1864\n\t"
            "ds_read_b128 v[172:175], v254 offset:192\n\t"
            "ds_read_b64 v[152:153], v253 offset:32\n\t"
            "ds_read_b64 v[154:155], v253 offset:128\n\t"
            "ds_read_b64 v[156:157], v253 offset:1776\n\t"
            "ds_read_b64 v[158:159], v253 offset:1872\n\t"
            "v_cvt_pk_bf16_f32 v176, v176, v177\n\t"
            "v_cvt_pk_bf16_f32 v177, v178, v179\n\t"
            "v_cvt_pk_bf16_f32 v178, v180, v181\n\t"
            "v_cvt_pk_bf16_f32 v179, v182, v183\n\t"
            "v_mfma_f32_4x4x4_16b_bf16 v[196:199], v[176:177], v[176:177], 0\n\t"
            "v_mov_b64 v[180:181], 0\n\t"
            "v_mfma_f32_16x16x32_bf16 v[192:195], v[238:241], v[176:179], 0\n\t"
            "s_waitcnt lgkmcnt(10)\n\t"
            "v_readfirstlane_b32 s26, v249\n\t"
            "v_mov_b64 v[182:183], 0\n\t"
            "v_mfma_f32_4x4x4_16b_bf16 v[196:199], v[178:179], v[178:179], v[196:199]\n\t"
            "ds_read_b32 v249, v252 offset:1024\n\t"
            "s_and_b32 s68, s26, 0x7ffffff\n\t"
            "s_add_u32 s68, s68, s78\n\t"
            "s_and_b32 s69, s26, 0x38000000\n\t"
            "s_or_b32 s69, s69, s39\n\t"
            "s_bitcmp1_b32 s26, 27\n\t"
            "s_cselect_b32 s79, 2.0, s68\n\t"
            "s_waitcnt lgkmcnt(10)\n\t"
            "v_smfmac_f32_16x16x64_bf16 v[184:187], v[168:171], v[160:167], v255\n\t"
            "s_and_b32 s57, s56, 3\n\t"
            "s_lshl_b32 s58, s57, 3\n\t"
            "s_lshr_b32 m0, s54, s58\n\t"
            "ds_read_b128 v[168:171], v254 offset:256\n\t"
            "ds_read_b64 v[160:161], v253 offset:40\n\t"
            "ds_read_b64 v[162:163], v253 offset:136\n\t"
            "ds_read_b64 v[164:165], v253 offset:1784\n\t"
            "s_waitcnt lgkmcnt(9)\n\t"
            "v_smfmac_f32_16x16x64_bf16 v[188:191], v[172:175], v[144:151], v255\n\t"
            "s_lshl_b32 s58, s57, 4\n\t"
            "s_lshr_b64 s[36:37], s[50:51], s58\n\t"
            "s_and_b32 s57, s36, 0xffff\n\t"
            "s_add_u32 s57, s57, s67\n\t"
            "ds_read_b64 v[166:167], v253 offset:1880\n\t"
            "ds_read_b128 v[172:175], v254 offset:320\n\t"
            "ds_read_b64 v[144:145], v253 offset:48\n\t"
            "ds_read_b64 v[146:147], v253 offset:144\n\t"
            "ds_read_b64 v[148:149], v253 offset:1792\n\t"
            "ds_read_b64 v[150:151], v253 offset:1888\n\t"
            "s_lshr_b64 s[36:37], s[52:53], s58\n\t"
            "s_and_b32 s58, s36, 0xffff\n\t"
            "s_add_u32 s58, s58, s67\n\t"
            "s_and_b32 s59, s50, 0xffff\n\t"
            "s_add_u32 s59, s59, s67\n\t"
            "s_and_b32 s38, s52, 0xffff\n\t"
            "s_add_u32 s38, s38, s67\n\t"
            "v_writelane_b32 v236, s59, m0\n\t"
            "v_writelane_b32 v236, s57, 63\n\t"
            "v_cvt_pk_bf16_f32 v184, v184, v185\n\t"
            "v_cvt_pk_bf16_f32 v185, v186, v187\n\t"
            "v_xor_b32 v237, 16, v236\n\t"
            "v_cvt_pk_bf16_f32 v186, v188, v189\n\t"
            "v_mfma_f32_4x4x4_16b_bf16 v[196:199], v[184:185], v[184:185], v[196:199]\n\t"
            "s_waitcnt lgkmcnt(14)\n\t"
            "ds_write_b128 v236, v[176:179]\n\t"
            "v_mov_b64 v[176:177], 0\n\t"
            "v_mov_b64 v[178:179], 0\n\t"
            "v_cvt_pk_bf16_f32 v187, v190, v191\n\t"
            "v_mov_b64 v[188:189], 0\n\t"
            "s_waitcnt lgkmcnt(10)\n\t"
            "v_smfmac_f32_16x16x64_bf16 v[176:179], v[168:171], v[152:159], v255\n\t"
            "ds_read_b128 v[168:171], v254 offset:384\n\t"
            "s_waitcnt lgkmcnt(6)\n\t"
            "v_smfmac_f32_16x16x64_bf16 v[180:183], v[172:175], v[160:167], v255\n\t"
            "ds_read_b64 v[152:153], v253 offset:56\n\t"
            "v_mfma_f32_16x16x32_bf16 v[192:195], v[238:241], v[184:187], v[192:195]\n\t"
            "ds_read_b64 v[154:155], v253 offset:152\n\t"
            "ds_read_b64 v[156:157], v253 offset:1800\n\t"
            "v_mfma_f32_4x4x4_16b_bf16 v[196:199], v[186:187], v[186:187], v[196:199]\n\t"
            "ds_read_b64 v[158:159], v253 offset:1896\n\t"
            "ds_read_b128 v[172:175], v254 offset:448\n\t"
            "ds_read_b64 v[160:161], v253 offset:64\n\t"
            "ds_read_b64 v[162:163], v253 offset:160\n\t"
            "ds_read_b64 v[164:165], v253 offset:1808\n\t"
            "ds_read_b64 v[166:167], v253 offset:1904\n\t"
            "s_waitcnt lgkmcnt(14)\n\t"
            "ds_write_b128 v237, v[184:187]\n\t"
            "v_mov_b64 v[184:185], 0\n\t"
            "v_mov_b64 v[186:187], 0\n\t"
            "v_mov_b64 v[190:191], 0\n\t"
            "v_cvt_pk_bf16_f32 v176, v176, v177\n\t"
            "v_cvt_pk_bf16_f32 v177, v178, v179\n\t"
            "v_cvt_pk_bf16_f32 v178, v180, v181\n\t"
            "v_cvt_pk_bf16_f32 v179, v182, v183\n\t"
            "v_mfma_f32_4x4x4_16b_bf16 v[196:199], v[176:177], v[176:177], v[196:199]\n\t"
            "s_waitcnt lgkmcnt(14)\n\t"
            "ds_write_b128 v236, v[176:179] offset:128\n\t"
            "s_waitcnt lgkmcnt(11)\n\t"
            "v_smfmac_f32_16x16x64_bf16 v[184:187], v[168:171], v[144:151], v255\n\t"
            "ds_read_b128 v[168:171], v254 offset:512\n\t"
            "v_mfma_f32_16x16x32_bf16 v[192:195], v[238:241], v[176:179], v[192:195]\n\t"
            "ds_read_b64 v[144:145], v253 offset:72\n\t"
            "ds_read_b64 v[146:147], v253 offset:168\n\t"
            "s_waitcnt lgkmcnt(9)\n\t"
            "v_smfmac_f32_16x16x64_bf16 v[188:191], v[172:175], v[152:159], v255\n\t"
            "ds_read_b64 v[148:149], v253 offset:1816\n\t"
            "v_mfma_f32_4x4x4_16b_bf16 v[196:199], v[178:179], v[178:179], v[196:199]\n\t"
            "ds_read_b64 v[150:151], v253 offset:1912\n\t"
            "ds_read_b128 v[172:175], v254 offset:576\n\t"
            "ds_read_b64 v[152:153], v253 offset:80\n\t"
            "ds_read_b64 v[154:155], v253 offset:176\n\t"
            "ds_read_b64 v[156:157], v253 offset:1824\n\t"
            "s_waitcnt lgkmcnt(14)\n\t"
            "ds_read_b64 v[158:159], v253 offset:1920\n\t"
            "v_mov_b64 v[176:177], 0\n\t"
            "v_mov_b64 v[178:179], 0\n\t"
            "v_mov_b64 v[180:181], 0\n\t"
            "v_mov_b64 v[182:183], 0\n\t"
            "v_cvt_pk_bf16_f32 v184, v184, v185\n\t"
            "v_cvt_pk_bf16_f32 v185, v186, v187\n\t"
            "v_cvt_pk_bf16_f32 v186, v188, v189\n\t"
            "v_cvt_pk_bf16_f32 v187, v190, v191\n\t"
            "v_mfma_f32_4x4x4_16b_bf16 v[196:199], v[184:185], v[184:185], v[196:199]\n\t"
            "s_waitcnt lgkmcnt(14)\n\t"
            "ds_write_b128 v237, v[184:187] offset:128\n\t"
            "v_mfma_f32_16x16x32_bf16 v[192:195], v[238:241], v[184:187], v[192:195]\n\t"
            "v_mov_b64 v[184:185], 0\n\t"
            "v_mov_b64 v[188:189], 0\n\t"
            "s_waitcnt lgkmcnt(10)\n\t"
            "v_smfmac_f32_16x16x64_bf16 v[176:179], v[168:171], v[160:167], v255\n\t"
            "ds_read_b128 v[168:171], v254 offset:640\n\t"
            "v_mfma_f32_4x4x4_16b_bf16 v[196:199], v[186:187], v[186:187], v[196:199]\n\t"
            "ds_read_b64 v[160:161], v253 offset:88\n\t"
            "ds_read_b64 v[162:163], v253 offset:184\n\t"
            "ds_read_b64 v[164:165], v253 offset:1832\n\t"
            "s_waitcnt lgkmcnt(9)\n\t"
            "v_smfmac_f32_16x16x64_bf16 v[180:183], v[172:175], v[144:151], v255\n\t"
            "ds_read_b64 v[166:167], v253 offset:1928\n\t"
            "ds_read_b128 v[172:175], v254 offset:704\n\t"
            "v_mov_b64 v[186:187], 0\n\t"
            "v_mov_b64 v[190:191], 0\n\t"
            "v_writelane_b32 v248, s38, m0\n\t"
            "v_and_b32 v249, s69, v249\n\t"
            "v_cvt_pk_bf16_f32 v176, v176, v177\n\t"
            "v_cvt_pk_bf16_f32 v177, v178, v179\n\t"
            "v_cvt_pk_bf16_f32 v178, v180, v181\n\t"
            "v_cvt_pk_bf16_f32 v179, v182, v183\n\t"
            "v_mfma_f32_4x4x4_16b_bf16 v[196:199], v[176:177], v[176:177], v[196:199]\n\t"
            "ds_write_b128 v236, v[176:179] offset:256\n\t"
            "v_mfma_f32_16x16x32_bf16 v[192:195], v[238:241], v[176:179], v[192:195]\n\t"
            "v_writelane_b32 v248, s58, 63\n\t"
            "v_add_u32 v236, s48, v244\n\t"
            "v_mfma_f32_4x4x4_16b_bf16 v[196:199], v[178:179], v[178:179], v[196:199]\n\t"
            "ds_read_b128 v[200:203], v236\n\t"
            "ds_read_b128 v[212:215], v236 offset:6144\n\t"
            "s_waitcnt lgkmcnt(8)\n\t"
            "v_smfmac_f32_16x16x64_bf16 v[184:187], v[168:171], v[152:159], v255\n\t"
            "ds_read_b128 v[224:227], v236 offset:12288\n\t"
            "s_waitcnt lgkmcnt(4)\n\t"
            "v_smfmac_f32_16x16x64_bf16 v[188:191], v[172:175], v[160:167], v255\n\t"
            "s_mov_b32 exec_hi, 0x3fffff\n\t"
            "s_add_u32 m0, s66, 0\n\t"
            "s_add_u32 s49, s79, 0\n\t"
            "buffer_load_dwordx4 v249, s[72:75], s49 offen lds\n\t"
            "s_mov_b32 exec_hi, -1\n\t"
            "s_mov_b32 exec_hi, 0x3fffff\n\t"
            "s_add_u32 m0, s66, 872\n\t"
            "s_add_u32 s49, s68, 196224\n\t"
            "buffer_load_dwordx4 v249, s[72:75], s49 offen lds\n\t"
            "s_mov_b32 exec_hi, -1\n\t"
            "s_mov_b32 exec_hi, 0x3fffff\n\t"
            "s_add_u32 m0, s66, 1744\n\t"
            "s_add_u32 s49, s68, 392448\n\t"
            "buffer_load_dwordx4 v249, s[72:75], s49 offen lds\n\t"
            "s_mov_b32 exec_hi, -1\n\t"
            "s_mov_b32 exec_hi, 0x3fffff\n\t"
            "s_add_u32 m0, s66, 2616\n\t"
            "s_add_u32 s49, s68, 588672\n\t"
            "buffer_load_dwordx4 v249, s[72:75], s49 offen lds\n\t"
            "s_mov_b32 exec_hi, -1\n\t"
            "s_mov_b32 exec_hi, 0x3fffff\n\t"
            "s_add_u32 m0, s66, 3488\n\t"
            "s_add_u32 s49, s68, 784896\n\t"
            "buffer_load_dwordx4 v249, s[72:75], s49 offen lds\n\t"
            "s_mov_b32 exec_hi, -1\n\t"
            "s_mov_b32 exec_hi, 0x3fffff\n\t"
            "s_add_u32 m0, s66, 4360\n\t"
            "s_add_u32 s49, s68, 981120\n\t"
            "buffer_load_dwordx4 v249, s[72:75], s49 offen lds\n\t"
            "s_mov_b32 exec_hi, -1\n\t"
            "s_mov_b32 exec_hi, 0x3fffff\n\t"
            "s_add_u32 m0, s66, 5232\n\t"
            "s_add_u32 s49, s68, 1177344\n\t"
            "buffer_load_dwordx4 v249, s[72:75], s49 offen lds\n\t"
            "s_mov_b32 exec_hi, -1\n\t"
            "s_mov_b32 exec_hi, 0x3fffff\n\t"
            "s_add_u32 m0, s66, 6104\n\t"
            "s_add_u32 s49, s68, 1373568\n\t"
            "buffer_load_dwordx4 v249, s[72:75], s49 offen lds\n\t"
            "s_mov_b32 exec_hi, -1\n\t"
            "s_mov_b32 exec_hi, 0x3fffff\n\t"
            "s_add_u32 m0, s66, 6976\n\t"
            "s_add_u32 s49, s68, 1569792\n\t"
            "buffer_load_dwordx4 v249, s[72:75], s49 offen lds\n\t"
            "s_mov_b32 exec_hi, -1\n\t"
            "s_add_u32 s49, s33, 0\n\t"
            "buffer_load_dwordx4 v[0:3], v247, s[28:31], s49 offen\n\t"
            "s_add_u32 s49, s33, 1024\n\t"
            "buffer_load_dwordx4 v[4:7], v247, s[28:31], s49 offen\n\t"
            "s_add_u32 s49, s33, 2048\n\t"
            "buffer_load_dwordx4 v[8:11], v247, s[28:31], s49 offen\n\t"
            "s_add_u32 s49, s33, 3072\n\t"
            "buffer_load_dwordx4 v[12:15], v247, s[28:31], s49 offen\n\t"
            "s_add_u32 s49, s33, 4096\n\t"
            "buffer_load_dwordx4 v[16:19], v247, s[28:31], s49 offen\n\t"
            "s_add_u32 s49, s33, 5120\n\t"
            "buffer_load_dwordx4 v[20:23], v247, s[28:31], s49 offen\n\t"
            "s_add_u32 s49, s33, 6144\n\t"
            "buffer_load_dwordx4 v[24:27], v247, s[28:31], s49 offen\n\t"
            "s_add_u32 s49, s33, 7168\n\t"
            "buffer_load_dwordx4 v[28:31], v247, s[28:31], s49 offen\n\t"
            "s_add_u32 s49, s33, 8192\n\t"
            "buffer_load_dwordx4 v[32:35], v247, s[28:31], s49 offen\n\t"
            "s_add_u32 s49, s33, 9216\n\t"
            "buffer_load_dwordx4 v[36:39], v247, s[28:31], s49 offen\n\t"
            "s_add_u32 s49, s33, 10240\n\t"
            "buffer_load_dwordx4 v[40:43], v247, s[28:31], s49 offen\n\t"
            "s_add_u32 s49, s33, 11264\n\t"
            "buffer_load_dwordx4 v[44:47], v247, s[28:31], s49 offen\n\t"
            "s_add_u32 s49, s33, 12288\n\t"
            "buffer_load_dwordx4 v[48:51], v247, s[28:31], s49 offen\n\t"
            "s_add_u32 s49, s33, 13312\n\t"
            "buffer_load_dwordx4 v[52:55], v247, s[28:31], s49 offen\n\t"
            "s_add_u32 s49, s33, 14336\n\t"
            "buffer_load_dwordx4 v[56:59], v247, s[28:31], s49 offen\n\t"
            "s_add_u32 s49, s33, 15360\n\t"
            "buffer_load_dwordx4 v[60:63], v247, s[28:31], s49 offen\n\t"
            "s_add_u32 s49, s33, 16384\n\t"
            "buffer_load_dwordx4 v[64:67], v247, s[28:31], s49 offen\n\t"
            "s_add_u32 s49, s33, 17408\n\t"
            "buffer_load_dwordx4 v[68:71], v247, s[28:31], s49 offen\n\t"
            "s_add_u32 s49, s33, 18432\n\t"
            "buffer_load_dwordx4 v[72:75], v247, s[28:31], s49 offen\n\t"
            "s_add_u32 s49, s33, 19456\n\t"
            "buffer_load_dwordx4 v[76:79], v247, s[28:31], s49 offen\n\t"
            "s_add_u32 s49, s33, 20480\n\t"
            "buffer_load_dwordx4 v[80:83], v247, s[28:31], s49 offen\n\t"
            "s_add_u32 s49, s33, 21504\n\t"
            "buffer_load_dwordx4 v[84:87], v247, s[28:31], s49 offen\n\t"
            "s_add_u32 s49, s33, 22528\n\t"
            "buffer_load_dwordx4 v[88:91], v247, s[28:31], s49 offen\n\t"
            "s_add_u32 s49, s33, 23552\n\t"
            "buffer_load_dwordx4 v[92:95], v247, s[28:31], s49 offen\n\t"
            "s_add_u32 s49, s33, 24576\n\t"
            "buffer_load_dwordx4 v[96:99], v247, s[28:31], s49 offen\n\t"
            "s_add_u32 s49, s33, 25600\n\t"
            "buffer_load_dwordx4 v[100:103], v247, s[28:31], s49 offen\n\t"
            "s_add_u32 s49, s33, 26624\n\t"
            "buffer_load_dwordx4 v[104:107], v247, s[28:31], s49 offen\n\t"
            "s_add_u32 s49, s33, 27648\n\t"
            "buffer_load_dwordx4 v[108:111], v247, s[28:31], s49 offen\n\t"
            "s_add_u32 s49, s33, 28672\n\t"
            "buffer_load_dwordx4 v[112:115], v247, s[28:31], s49 offen\n\t"
            "s_add_u32 s49, s33, 29696\n\t"
            "buffer_load_dwordx4 v[116:119], v247, s[28:31], s49 offen\n\t"
            "s_add_u32 s49, s33, 30720\n\t"
            "buffer_load_dwordx4 v[120:123], v247, s[28:31], s49 offen\n\t"
            "v_cvt_pk_bf16_f32 v184, v184, v185\n\t"
            "v_cvt_pk_bf16_f32 v185, v186, v187\n\t"
            "v_cvt_pk_bf16_f32 v186, v188, v189\n\t"
            "v_cvt_pk_bf16_f32 v187, v190, v191\n\t"
            "v_mfma_f32_4x4x4_16b_bf16 v[196:199], v[184:185], v[184:185], v[196:199]\n\t"
            "s_add_u32 s49, s33, 31744\n\t"
            "buffer_load_dwordx4 v[124:127], v247, s[28:31], s49 offen\n\t"
            "v_mfma_f32_4x4x4_16b_bf16 v[196:199], v[186:187], v[186:187], v[196:199]\n\t"
            "s_add_u32 s49, s33, 32768\n\t"
            "buffer_load_dwordx4 v[128:131], v247, s[28:31], s49 offen\n\t"
            "v_mfma_f32_16x16x32_bf16 v[192:195], v[238:241], v[184:187], v[192:195]\n\t"
            "ds_write_b128 v237, v[184:187] offset:256\n\t"
            "v_add_u32 v237, s48, v245\n\t"
            "ds_read_b128 v[204:207], v237\n\t"
            "ds_read_b128 v[216:219], v237 offset:6144\n\t"
            "ds_read_b128 v[228:231], v237 offset:12288\n\t"
            "s_add_u32 s49, s33, 33792\n\t"
            "buffer_load_dwordx4 v[132:135], v247, s[28:31], s49 offen\n\t"
            "s_add_u32 s49, s33, 34816\n\t"
            "buffer_load_dwordx4 v[136:139], v247, s[28:31], s49 offen\n\t"
            "v_cndmask_b32_e64 v193, v196, v197, s[60:61]\n\t"
            "v_cndmask_b32_e64 v193, v193, v198, s[62:63]\n\t"
            "v_cndmask_b32_e64 v193, v193, v199, s[64:65]\n\t"
            "ds_write_b64 v248, v[192:193] offset:18816\n\t"
            "v_add_u32 v248, s48, v246\n\t"
            "ds_read_b128 v[208:211], v248\n\t"
            "ds_read_b128 v[220:223], v248 offset:6144\n\t"
            "ds_read_b128 v[232:235], v248 offset:12288\n\t"
            "s_add_u32 s49, s33, 35840\n\t"
            "buffer_load_dwordx4 v[140:143], v247, s[28:31], s49 offen\n\t"
            "s_add_u32 s45, s46, s27\n\t"
            "s_mov_b32 s46, s47\n\t"
            "s_mov_b32 s47, s68\n\t"
            "s_xor_b32 s48, s48, s71\n\t"
            "s_xor_b32 s67, s67, s71\n\t"
            "s_add_u32 s76, s76, 4\n\t"
            "s_add_u32 s70, s70, 4\n\t"
            "s_lshl_b32 s56, s56, 2\n\t"
            "s_lshr_b32 s49, s26, 30\n\t"
            "s_or_b32 s56, s56, s49\n\t"
            "s_waitcnt lgkmcnt(0)\n\t"
            "s_barrier\n\t"
            "s_waitcnt vmcnt(0)\n\t"
            "s_nop 7\n\t"
            ".Lwb_loop%=:\n\t"
            "s_setprio 1\n\t"
            "ds_read_b128 v[168:171], v254\n\t"
            "ds_read_b128 v[172:175], v254 offset:64\n\t"
            "s_waitcnt vmcnt(9)\n\t"
            "ds_read_b64 v[144:145], v253\n\t"
            "ds_read_b64 v[146:147], v253 offset:96\n\t"
            "ds_read_b64 v[148:149], v253 offset:1744\n\t"
            "ds_read_b64 v[150:151], v253 offset:1840\n\t"
            "ds_read_b64 v[152:153], v253 offset:8\n\t"
            "ds_read_b64 v[154:155], v253 offset:104\n\t"
            "ds_read_b64 v[156:157], v253 offset:1752\n\t"
            "ds_read_b64 v[158:159], v253 offset:1848\n\t"
            "ds_read_b64 v[160:161], v253 offset:16\n\t"
            "ds_read_b64 v[162:163], v253 offset:112\n\t"
            "ds_read_b64 v[164:165], v253 offset:1760\n\t"
            "ds_read_b64 v[166:167], v253 offset:1856\n\t"
            "v_mov_b64 v[176:177], 0\n\t"
            "v_mov_b64 v[178:179], 0\n\t"
            "v_mov_b64 v[180:181], 0\n\t"
            "v_mov_b64 v[182:183], 0\n\t"
            "v_mov_b32 v249, s76\n\t"
            "v_mov_b64 v[184:185], 0\n\t"
            "v_mov_b64 v[186:187], 0\n\t"
            "v_mov_b64 v[188:189], 0\n\t"
            "v_mov_b64 v[190:191], 0\n\t"
            "v_add_u32 v236, s67, v250\n\t"
            "v_add_u32 v248, s67, v251\n\t"
            "ds_read_b32 v249, v249\n\t"
            "s_setprio 3\n\t"
            "s_waitcnt lgkmcnt(9)\n\t"
            "v_smfmac_f32_16x16x64_bf16 v[176:179], v[168:171], v[144:151], v255\n\t"
            "ds_read_b128 v[168:171], v254 offset:128\n\t"
            "s_waitcnt lgkmcnt(6)\n\t"
            "v_smfmac_f32_16x16x64_bf16 v[180:183], v[172:175], v[152:159], v255\n\t"
            "s_setprio 1\n\t"
            "ds_read_b64 v[144:145], v253 offset:24\n\t"
            "ds_read_b64 v[146:147], v253 offset:120\n\t"
            "ds_read_b64 v[148:149], v253 offset:1768\n\t"
            "ds_read_b64 v[150:151], v253 offset:1864\n\t"
            "ds_read_b128 v[172:175], v254 offset:192\n\t"
            "ds_read_b64 v[152:153], v253 offset:32\n\t"
            "ds_read_b64 v[154:155], v253 offset:128\n\t"
            "ds_read_b64 v[156:157], v253 offset:1776\n\t"
            "ds_read_b64 v[158:159], v253 offset:1872\n\t"
            "v_cvt_pk_bf16_f32 v176, v176, v177\n\t"
            "v_cvt_pk_bf16_f32 v177, v178, v179\n\t"
            "v_cvt_pk_bf16_f32 v178, v180, v181\n\t"
            "v_cvt_pk_bf16_f32 v179, v182, v183\n\t"
            "s_setprio 3\n\t"
            "v_mfma_f32_4x4x4_16b_bf16 v[196:199], v[176:177], v[176:177], 0\n\t"
            "v_mov_b64 v[180:181], 0\n\t"
            "v_mfma_f32_16x16x32_bf16 v[192:195], v[238:241], v[176:179], 0\n\t"
            "s_waitcnt lgkmcnt(10)\n\t"
            "v_readfirstlane_b32 s26, v249\n\t"
            "v_mov_b64 v[182:183], 0\n\t"
            "v_mfma_f32_4x4x4_16b_bf16 v[196:199], v[178:179], v[178:179], v[196:199]\n\t"
            "ds_read_b32 v249, v252 offset:1024\n\t"
            "s_and_b32 s68, s26, 0x7ffffff\n\t"
            "s_add_u32 s68, s68, s78\n\t"
            "s_and_b32 s69, s26, 0x38000000\n\t"
            "s_or_b32 s69, s69, s39\n\t"
            "s_bitcmp1_b32 s26, 27\n\t"
            "s_cselect_b32 s79, 2.0, s68\n\t"
            "s_waitcnt lgkmcnt(10)\n\t"
            "v_smfmac_f32_16x16x64_bf16 v[184:187], v[168:171], v[160:167], v255\n\t"
            "s_and_b32 s57, s56, 3\n\t"
            "s_lshl_b32 s58, s57, 3\n\t"
            "s_lshr_b32 m0, s54, s58\n\t"
            "ds_read_b128 v[168:171], v254 offset:256\n\t"
            "ds_read_b64 v[160:161], v253 offset:40\n\t"
            "ds_read_b64 v[162:163], v253 offset:136\n\t"
            "ds_read_b64 v[164:165], v253 offset:1784\n\t"
            "s_waitcnt lgkmcnt(9)\n\t"
            "v_smfmac_f32_16x16x64_bf16 v[188:191], v[172:175], v[144:151], v255\n\t"
            "s_setprio 1\n\t"
            "s_lshl_b32 s58, s57, 4\n\t"
            "s_lshr_b64 s[36:37], s[50:51], s58\n\t"
            "s_and_b32 s57, s36, 0xffff\n\t"
            "s_add_u32 s57, s57, s67\n\t"
            "ds_read_b64 v[166:167], v253 offset:1880\n\t"
            "ds_read_b128 v[172:175], v254 offset:320\n\t"
            "ds_read_b64 v[144:145], v253 offset:48\n\t"
            "ds_read_b64 v[146:147], v253 offset:144\n\t"
            "ds_read_b64 v[148:149], v253 offset:1792\n\t"
            "ds_read_b64 v[150:151], v253 offset:1888\n\t"
            "s_lshr_b64 s[36:37], s[52:53], s58\n\t"
            "s_and_b32 s58, s36, 0xffff\n\t"
            "s_add_u32 s58, s58, s67\n\t"
            "s_and_b32 s59, s50, 0xffff\n\t"
            "s_add_u32 s59, s59, s67\n\t"
            "s_and_b32 s38, s52, 0xffff\n\t"
            "s_add_u32 s38, s38, s67\n\t"
            "v_writelane_b32 v236, s59, m0\n\t"
            "v_writelane_b32 v236, s57, 63\n\t"
            "v_cvt_pk_bf16_f32 v184, v184, v185\n\t"
            "v_cvt_pk_bf16_f32 v185, v186, v187\n\t"
            "v_xor_b32 v237, 16, v236\n\t"
            "v_cvt_pk_bf16_f32 v186, v188, v189\n\t"
            "s_setprio 3\n\t"
            "v_mfma_f32_4x4x4_16b_bf16 v[196:199], v[184:185], v[184:185], v[196:199]\n\t"
            "s_setprio 1\n\t"
            "s_waitcnt lgkmcnt(14)\n\t"
            "ds_write_b128 v236, v[176:179]\n\t"
            "v_mov_b64 v[176:177], 0\n\t"
            "v_mov_b64 v[178:179], 0\n\t"
            "v_cvt_pk_bf16_f32 v187, v190, v191\n\t"
            "v_mov_b64 v[188:189], 0\n\t"
            "s_setprio 3\n\t"
            "s_waitcnt lgkmcnt(10)\n\t"
            "v_smfmac_f32_16x16x64_bf16 v[176:179], v[168:171], v[152:159], v255\n\t"
            "ds_read_b128 v[168:171], v254 offset:384\n\t"
            "s_waitcnt lgkmcnt(6)\n\t"
            "v_smfmac_f32_16x16x64_bf16 v[180:183], v[172:175], v[160:167], v255\n\t"
            "ds_read_b64 v[152:153], v253 offset:56\n\t"
            "v_mfma_f32_16x16x32_bf16 v[192:195], v[238:241], v[184:187], v[192:195]\n\t"
            "ds_read_b64 v[154:155], v253 offset:152\n\t"
            "ds_read_b64 v[156:157], v253 offset:1800\n\t"
            "v_mfma_f32_4x4x4_16b_bf16 v[196:199], v[186:187], v[186:187], v[196:199]\n\t"
            "s_setprio 1\n\t"
            "ds_read_b64 v[158:159], v253 offset:1896\n\t"
            "ds_read_b128 v[172:175], v254 offset:448\n\t"
            "ds_read_b64 v[160:161], v253 offset:64\n\t"
            "ds_read_b64 v[162:163], v253 offset:160\n\t"
            "ds_read_b64 v[164:165], v253 offset:1808\n\t"
            "ds_read_b64 v[166:167], v253 offset:1904\n\t"
            "s_waitcnt lgkmcnt(14)\n\t"
            "ds_write_b128 v237, v[184:187]\n\t"
            "v_mov_b64 v[184:185], 0\n\t"
            "v_mov_b64 v[186:187], 0\n\t"
            "v_mov_b64 v[190:191], 0\n\t"
            "v_cvt_pk_bf16_f32 v176, v176, v177\n\t"
            "v_cvt_pk_bf16_f32 v177, v178, v179\n\t"
            "v_cvt_pk_bf16_f32 v178, v180, v181\n\t"
            "v_cvt_pk_bf16_f32 v179, v182, v183\n\t"
            "s_setprio 3\n\t"
            "v_mfma_f32_4x4x4_16b_bf16 v[196:199], v[176:177], v[176:177], v[196:199]\n\t"
            "s_waitcnt lgkmcnt(14)\n\t"
            "ds_write_b128 v236, v[176:179] offset:128\n\t"
            "s_waitcnt lgkmcnt(11)\n\t"
            "v_smfmac_f32_16x16x64_bf16 v[184:187], v[168:171], v[144:151], v255\n\t"
            "ds_read_b128 v[168:171], v254 offset:512\n\t"
            "v_mfma_f32_16x16x32_bf16 v[192:195], v[238:241], v[176:179], v[192:195]\n\t"
            "ds_read_b64 v[144:145], v253 offset:72\n\t"
            "ds_read_b64 v[146:147], v253 offset:168\n\t"
            "s_waitcnt lgkmcnt(9)\n\t"
            "v_smfmac_f32_16x16x64_bf16 v[188:191], v[172:175], v[152:159], v255\n\t"
            "ds_read_b64 v[148:149], v253 offset:1816\n\t"
            "v_mfma_f32_4x4x4_16b_bf16 v[196:199], v[178:179], v[178:179], v[196:199]\n\t"
            "s_setprio 1\n\t"
            "ds_read_b64 v[150:151], v253 offset:1912\n\t"
            "ds_read_b128 v[172:175], v254 offset:576\n\t"
            "ds_read_b64 v[152:153], v253 offset:80\n\t"
            "ds_read_b64 v[154:155], v253 offset:176\n\t"
            "ds_read_b64 v[156:157], v253 offset:1824\n\t"
            "s_waitcnt lgkmcnt(14)\n\t"
            "ds_read_b64 v[158:159], v253 offset:1920\n\t"
            "v_mov_b64 v[176:177], 0\n\t"
            "v_mov_b64 v[178:179], 0\n\t"
            "v_mov_b64 v[180:181], 0\n\t"
            "v_mov_b64 v[182:183], 0\n\t"
            "v_cvt_pk_bf16_f32 v184, v184, v185\n\t"
            "v_cvt_pk_bf16_f32 v185, v186, v187\n\t"
            "v_cvt_pk_bf16_f32 v186, v188, v189\n\t"
            "v_cvt_pk_bf16_f32 v187, v190, v191\n\t"
            "s_setprio 3\n\t"
            "v_mfma_f32_4x4x4_16b_bf16 v[196:199], v[184:185], v[184:185], v[196:199]\n\t"
            "s_waitcnt lgkmcnt(14)\n\t"
            "ds_write_b128 v237, v[184:187] offset:128\n\t"
            "v_mfma_f32_16x16x32_bf16 v[192:195], v[238:241], v[184:187], v[192:195]\n\t"
            "v_mov_b64 v[184:185], 0\n\t"
            "v_mov_b64 v[188:189], 0\n\t"
            "s_waitcnt lgkmcnt(10)\n\t"
            "v_smfmac_f32_16x16x64_bf16 v[176:179], v[168:171], v[160:167], v255\n\t"
            "ds_read_b128 v[168:171], v254 offset:640\n\t"
            "v_mfma_f32_4x4x4_16b_bf16 v[196:199], v[186:187], v[186:187], v[196:199]\n\t"
            "ds_read_b64 v[160:161], v253 offset:88\n\t"
            "ds_read_b64 v[162:163], v253 offset:184\n\t"
            "ds_read_b64 v[164:165], v253 offset:1832\n\t"
            "s_waitcnt lgkmcnt(9)\n\t"
            "v_smfmac_f32_16x16x64_bf16 v[180:183], v[172:175], v[144:151], v255\n\t"
            "s_setprio 1\n\t"
            "ds_read_b64 v[166:167], v253 offset:1928\n\t"
            "ds_read_b128 v[172:175], v254 offset:704\n\t"
            "v_mov_b64 v[186:187], 0\n\t"
            "v_mov_b64 v[190:191], 0\n\t"
            "v_writelane_b32 v248, s38, m0\n\t"
            "v_and_b32 v249, s69, v249\n\t"
            "v_cvt_pk_bf16_f32 v176, v176, v177\n\t"
            "v_cvt_pk_bf16_f32 v177, v178, v179\n\t"
            "v_cvt_pk_bf16_f32 v178, v180, v181\n\t"
            "v_cvt_pk_bf16_f32 v179, v182, v183\n\t"
            "s_setprio 3\n\t"
            "v_mfma_f32_4x4x4_16b_bf16 v[196:199], v[176:177], v[176:177], v[196:199]\n\t"
            "v_writelane_b32 v248, s58, 63\n\t"
            "v_mfma_f32_16x16x32_bf16 v[192:195], v[238:241], v[176:179], v[192:195]\n\t"
            "ds_write_b128 v236, v[176:179] offset:256\n\t"
            "v_mfma_f32_4x4x4_16b_bf16 v[196:199], v[178:179], v[178:179], v[196:199]\n\t"
            "v_lshlrev_b32 v176, 16, v208\n\t"
            "v_and_b32 v177, 0xffff0000, v208\n\t"
            "v_lshlrev_b32 v178, 16, v209\n\t"
            "s_waitcnt lgkmcnt(6)\n\t"
            "v_smfmac_f32_16x16x64_bf16 v[184:187], v[168:171], v[152:159], v255\n\t"
            "s_setprio 1\n\t"
            "v_lshlrev_b32 v152, 16, v200\n\t"
            "v_and_b32 v153, 0xffff0000, v200\n\t"
            "v_lshlrev_b32 v154, 16, v201\n\t"
            "v_and_b32 v155, 0xffff0000, v201\n\t"
            "v_lshlrev_b32 v156, 16, v202\n\t"
            "v_and_b32 v157, 0xffff0000, v202\n\t"
            "s_setprio 3\n\t"
            "s_waitcnt lgkmcnt(1)\n\t"
            "v_smfmac_f32_16x16x64_bf16 v[188:191], v[172:175], v[160:167], v255\n\t"
            "s_setprio 1\n\t"
            "s_mov_b32 exec_hi, 0x3fffff\n\t"
            "s_add_u32 m0, s66, 0\n\t"
            "s_add_u32 s49, s79, 0\n\t"
            "buffer_load_dwordx4 v249, s[72:75], s49 offen lds\n\t"
            "s_mov_b32 exec_hi, -1\n\t"
            "s_mov_b32 exec_hi, 0x3fffff\n\t"
            "s_add_u32 m0, s66, 872\n\t"
            "s_add_u32 s49, s68, 196224\n\t"
            "buffer_load_dwordx4 v249, s[72:75], s49 offen lds\n\t"
            "s_mov_b32 exec_hi, -1\n\t"
            "s_mov_b32 exec_hi, 0x3fffff\n\t"
            "s_add_u32 m0, s66, 1744\n\t"
            "s_add_u32 s49, s68, 392448\n\t"
            "buffer_load_dwordx4 v249, s[72:75], s49 offen lds\n\t"
            "s_mov_b32 exec_hi, -1\n\t"
            "s_mov_b32 exec_hi, 0x3fffff\n\t"
            "s_add_u32 m0, s66, 2616\n\t"
            "s_add_u32 s49, s68, 588672\n\t"
            "buffer_load_dwordx4 v249, s[72:75], s49 offen lds\n\t"
            "s_mov_b32 exec_hi, -1\n\t"
            "s_mov_b32 exec_hi, 0x3fffff\n\t"
            "s_add_u32 m0, s66, 3488\n\t"
            "s_add_u32 s49, s68, 784896\n\t"
            "buffer_load_dwordx4 v249, s[72:75], s49 offen lds\n\t"
            "s_mov_b32 exec_hi, -1\n\t"
            "s_mov_b32 exec_hi, 0x3fffff\n\t"
            "s_add_u32 m0, s66, 4360\n\t"
            "s_add_u32 s49, s68, 981120\n\t"
            "buffer_load_dwordx4 v249, s[72:75], s49 offen lds\n\t"
            "s_mov_b32 exec_hi, -1\n\t"
            "ds_read_b32 v247, v255 offset:7856\n\t"
            "s_mov_b32 exec_hi, 0x3fffff\n\t"
            "s_add_u32 m0, s66, 5232\n\t"
            "s_add_u32 s49, s68, 1177344\n\t"
            "buffer_load_dwordx4 v249, s[72:75], s49 offen lds\n\t"
            "s_mov_b32 exec_hi, -1\n\t"
            "s_mov_b32 exec_hi, 0x3fffff\n\t"
            "s_add_u32 m0, s66, 6104\n\t"
            "s_add_u32 s49, s68, 1373568\n\t"
            "buffer_load_dwordx4 v249, s[72:75], s49 offen lds\n\t"
            "s_mov_b32 exec_hi, -1\n\t"
            "s_mov_b32 exec_hi, 0x3fffff\n\t"
            "s_add_u32 m0, s66, 6976\n\t"
            "s_add_u32 s49, s68, 1569792\n\t"
            "buffer_load_dwordx4 v249, s[72:75], s49 offen lds\n\t"
            "s_mov_b32 exec_hi, -1\n\t"
            "v_cvt_pk_bf16_f32 v184, v184, v185\n\t"
            "v_cvt_pk_bf16_f32 v185, v186, v187\n\t"
            "v_cvt_pk_bf16_f32 v186, v188, v189\n\t"
            "v_cvt_pk_bf16_f32 v187, v190, v191\n\t"
            "s_setprio 3\n\t"
            "v_mfma_f32_4x4x4_16b_bf16 v[196:199], v[184:185], v[184:185], v[196:199]\n\t"
            "ds_write_b128 v237, v[184:187] offset:256\n\t"
            "v_mfma_f32_16x16x32_bf16 v[192:195], v[238:241], v[184:187], v[192:195]\n\t"
            "s_waitcnt lgkmcnt(1)\n\t"
            "v_readfirstlane_b32 s77, v247\n\t"
            "s_cmp_ge_u32 s77, s70\n\t"
            "s_cbranch_scc0 .Lwb_spin%=\n\t"
            ".Lwb_go%=:\n\t"
            "v_mfma_f32_4x4x4_16b_bf16 v[196:199], v[186:187], v[186:187], v[196:199]\n\t"
            "s_setprio 1\n\t"
            "ds_read_b128 v[184:187], v242\n\t"
            "ds_read_b128 v[188:191], v243\n\t"
            "ds_read_b128 v[172:175], v243 offset:128\n\t"
            "v_lshlrev_b32 v158, 16, v203\n\t"
            "v_and_b32 v159, 0xffff0000, v203\n\t"
            "v_lshlrev_b32 v160, 16, v204\n\t"
            "v_and_b32 v161, 0xffff0000, v204\n\t"
            "v_lshlrev_b32 v162, 16, v205\n\t"
            "v_and_b32 v163, 0xffff0000, v205\n\t"
            "v_cndmask_b32_e64 v193, v196, v197, s[60:61]\n\t"
            "v_cndmask_b32_e64 v193, v193, v198, s[62:63]\n\t"
            "v_cndmask_b32_e64 v193, v193, v199, s[64:65]\n\t"
            "v_lshlrev_b32 v164, 16, v206\n\t"
            "v_and_b32 v165, 0xffff0000, v206\n\t"
            "v_lshlrev_b32 v166, 16, v207\n\t"
            "v_and_b32 v167, 0xffff0000, v207\n\t"
            "v_and_b32 v179, 0xffff0000, v209\n\t"
            "v_lshlrev_b32 v180, 16, v210\n\t"
            "v_and_b32 v181, 0xffff0000, v210\n\t"
            "v_lshlrev_b32 v182, 16, v211\n\t"
            "v_and_b32 v183, 0xffff0000, v211\n\t"
            "v_lshlrev_b32 v144, 16, v212\n\t"
            "v_and_b32 v145, 0xffff0000, v212\n\t"
            "v_lshlrev_b32 v146, 16, v213\n\t"
            "v_and_b32 v147, 0xffff0000, v213\n\t"
            "v_lshlrev_b32 v148, 16, v214\n\t"
            "v_and_b32 v149, 0xffff0000, v214\n\t"
            "v_lshlrev_b32 v150, 16, v215\n\t"
            "v_and_b32 v151, 0xffff0000, v215\n\t"
            "v_lshlrev_b32 v168, 16, v216\n\t"
            "v_and_b32 v169, 0xffff0000, v216\n\t"
            "v_lshlrev_b32 v170, 16, v217\n\t"
            "s_setprio 3\n\t"
            "s_waitcnt lgkmcnt(1)\n\t"
            "v_mfma_f32_16x16x32_bf16 v[152:155], v[0:3], v[184:187], v[152:155]\n\t"
            "s_setprio 1\n\t"
            "s_setprio 1\n\t"
            "ds_read_b128 v[196:199], v252\n\t"
            "v_and_b32 v171, 0xffff0000, v217\n\t"
            "v_mfma_f32_16x16x32_bf16 v[152:155], v[4:7], v[188:191], v[152:155]\n\t"
            "s_bfe_u32 s57, s56, 0x20004\n\t"
            "s_lshl_b32 s58, s57, 3\n\t"
            "s_lshr_b32 s58, s55, s58\n\t"
            "s_lshl_b64 s[36:37], s[34:35], s58\n\t"
            "v_mfma_f32_16x16x32_bf16 v[156:159], v[28:31], v[188:191], v[156:159]\n\t"
            "v_add_u32 v236, s48, v244\n\t"
            "v_add_u32 v237, s48, v245\n\t"
            "v_mfma_f32_16x16x32_bf16 v[156:159], v[24:27], v[184:187], v[156:159]\n\t"
            "s_bitcmp1_b32 s57, 1\n\t"
            "s_cselect_b32 s58, 1177344, 0\n\t"
            "s_bitcmp1_b32 s57, 0\n\t"
            "s_cselect_b32 s57, 2304, 0\n\t"
            "s_add_u32 s57, s57, s58\n\t"
            "v_mfma_f32_16x16x32_bf16 v[160:163], v[48:51], v[184:187], v[160:163]\n\t"
            "ds_read_b128 v[200:203], v236\n\t"
            "ds_read_b128 v[204:207], v237\n\t"
            "v_mfma_f32_16x16x32_bf16 v[160:163], v[52:55], v[188:191], v[160:163]\n\t"
            "ds_read_b128 v[212:215], v236 offset:6144\n\t"
            "ds_write_b64 v248, v[192:193] offset:18816\n\t"
            "v_mfma_f32_16x16x32_bf16 v[164:167], v[76:79], v[188:191], v[164:167]\n\t"
            "ds_read_b128 v[192:195], v242 offset:128\n\t"
            "v_add_u32 v248, s48, v246\n\t"
            "v_mfma_f32_16x16x32_bf16 v[164:167], v[72:75], v[184:187], v[164:167]\n\t"
            "ds_read_b128 v[208:211], v248\n\t"
            "s_waitcnt lgkmcnt(6)\n\t"
            "s_mov_b64 exec, s[36:37]\n\t"
            "v_add_u32 v196, s57, v196\n\t"
            "s_mov_b64 exec, -1\n\t"
            "v_mfma_f32_16x16x32_bf16 v[176:179], v[96:99], v[184:187], v[176:179]\n\t"
            "v_mfma_f32_16x16x32_bf16 v[176:179], v[100:103], v[188:191], v[176:179]\n\t"
            "v_mfma_f32_16x16x32_bf16 v[180:183], v[124:127], v[188:191], v[180:183]\n\t"
            "ds_read_b128 v[188:191], v243 offset:256\n\t"
            "v_mfma_f32_16x16x32_bf16 v[180:183], v[120:123], v[184:187], v[180:183]\n\t"
            "ds_read_b128 v[184:187], v242 offset:256\n\t"
            "s_waitcnt lgkmcnt(3)\n\t"
            "v_mfma_f32_16x16x32_bf16 v[152:155], v[8:11], v[192:195], v[152:155]\n\t"
            "v_mfma_f32_16x16x32_bf16 v[152:155], v[12:15], v[172:175], v[152:155]\n\t"
            "v_mfma_f32_16x16x32_bf16 v[156:159], v[36:39], v[172:175], v[156:159]\n\t"
            "v_mfma_f32_16x16x32_bf16 v[156:159], v[32:35], v[192:195], v[156:159]\n\t"
            "v_mfma_f32_16x16x32_bf16 v[160:163], v[56:59], v[192:195], v[160:163]\n\t"
            "v_mfma_f32_16x16x32_bf16 v[160:163], v[60:63], v[172:175], v[160:163]\n\t"
            "v_mfma_f32_16x16x32_bf16 v[164:167], v[84:87], v[172:175], v[164:167]\n\t"
            "v_mfma_f32_16x16x32_bf16 v[164:167], v[80:83], v[192:195], v[164:167]\n\t"
            "v_mfma_f32_16x16x32_bf16 v[176:179], v[104:107], v[192:195], v[176:179]\n\t"
            "v_mfma_f32_16x16x32_bf16 v[176:179], v[108:111], v[172:175], v[176:179]\n\t"
            "v_mfma_f32_16x16x32_bf16 v[180:183], v[132:135], v[172:175], v[180:183]\n\t"
            "ds_read_b128 v[172:175], v243 offset:6144\n\t"
            "v_mfma_f32_16x16x32_bf16 v[180:183], v[128:131], v[192:195], v[180:183]\n\t"
            "ds_read_b128 v[192:195], v242 offset:6144\n\t"
            "s_waitcnt lgkmcnt(2)\n\t"
            "v_mfma_f32_16x16x32_bf16 v[152:155], v[16:19], v[184:187], v[152:155]\n\t"
            "v_mfma_f32_16x16x32_bf16 v[152:155], v[20:23], v[188:191], v[152:155]\n\t"
            "v_mfma_f32_16x16x32_bf16 v[160:163], v[64:67], v[184:187], v[160:163]\n\t"
            "v_mfma_f32_16x16x32_bf16 v[160:163], v[68:71], v[188:191], v[160:163]\n\t"
            "v_mfma_f32_16x16x32_bf16 v[176:179], v[112:115], v[184:187], v[176:179]\n\t"
            "v_mfma_f32_16x16x32_bf16 v[176:179], v[116:119], v[188:191], v[176:179]\n\t"
            "v_mfma_f32_16x16x32_bf16 v[156:159], v[44:47], v[188:191], v[156:159]\n\t"
            "v_mfma_f32_16x16x32_bf16 v[156:159], v[40:43], v[184:187], v[156:159]\n\t"
            "s_nop 1\n\t"
            "v_cvt_pk_bf16_f32 v152, v152, v153\n\t"
            "v_mfma_f32_16x16x32_bf16 v[164:167], v[92:95], v[188:191], v[164:167]\n\t"
            "v_cvt_pk_bf16_f32 v153, v154, v155\n\t"
            "v_mfma_f32_16x16x32_bf16 v[164:167], v[88:91], v[184:187], v[164:167]\n\t"
            "v_cvt_pk_bf16_f32 v160, v160, v161\n\t"
            "v_cvt_pk_bf16_f32 v161, v162, v163\n\t"
            "v_mfma_f32_16x16x32_bf16 v[180:183], v[140:143], v[188:191], v[180:183]\n\t"
            "ds_read_b128 v[188:191], v243 offset:6272\n\t"
            "v_cvt_pk_bf16_f32 v176, v176, v177\n\t"
            "v_mfma_f32_16x16x32_bf16 v[180:183], v[136:139], v[184:187], v[180:183]\n\t"
            "ds_read_b128 v[184:187], v242 offset:6272\n\t"
            "v_cvt_pk_bf16_f32 v177, v178, v179\n\t"
            "s_waitcnt lgkmcnt(2)\n\t"
            "v_mfma_f32_16x16x32_bf16 v[144:147], v[0:3], v[192:195], v[144:147]\n\t"
            "v_cvt_pk_bf16_f32 v154, v156, v157\n\t"
            "v_cvt_pk_bf16_f32 v155, v158, v159\n\t"
            "v_mfma_f32_16x16x32_bf16 v[144:147], v[4:7], v[172:175], v[144:147]\n\t"
            "v_cvt_pk_bf16_f32 v162, v164, v165\n\t"
            "v_cvt_pk_bf16_f32 v163, v166, v167\n\t"
            "v_mfma_f32_16x16x32_bf16 v[148:151], v[28:31], v[172:175], v[148:151]\n\t"
            "v_cvt_pk_bf16_f32 v178, v180, v181\n\t"
            "v_cvt_pk_bf16_f32 v179, v182, v183\n\t"
            "v_mfma_f32_16x16x32_bf16 v[148:151], v[24:27], v[192:195], v[148:151]\n\t"
            "v_lshlrev_b32 v156, 16, v218\n\t"
            "v_and_b32 v157, 0xffff0000, v218\n\t"
            "v_mfma_f32_16x16x32_bf16 v[168:171], v[48:51], v[192:195], v[168:171]\n\t"
            "v_lshlrev_b32 v158, 16, v219\n\t"
            "v_and_b32 v159, 0xffff0000, v219\n\t"
            "v_mfma_f32_16x16x32_bf16 v[168:171], v[52:55], v[172:175], v[168:171]\n\t"
            "ds_read_b128 v[216:219], v237 offset:6144\n\t"
            "v_lshlrev_b32 v164, 16, v220\n\t"
            "v_mfma_f32_16x16x32_bf16 v[156:159], v[76:79], v[172:175], v[156:159]\n\t"
            "v_and_b32 v165, 0xffff0000, v220\n\t"
            "v_lshlrev_b32 v166, 16, v221\n\t"
            "v_mfma_f32_16x16x32_bf16 v[156:159], v[72:75], v[192:195], v[156:159]\n\t"
            "v_and_b32 v167, 0xffff0000, v221\n\t"
            "v_lshlrev_b32 v180, 16, v222\n\t"
            "s_waitcnt lgkmcnt(1)\n\t"
            "v_mfma_f32_16x16x32_bf16 v[144:147], v[8:11], v[184:187], v[144:147]\n\t"
            "v_and_b32 v181, 0xffff0000, v222\n\t"
            "v_lshlrev_b32 v182, 16, v223\n\t"
            "v_mfma_f32_16x16x32_bf16 v[144:147], v[12:15], v[188:191], v[144:147]\n\t"
            "v_and_b32 v183, 0xffff0000, v223\n\t"
            "v_mfma_f32_16x16x32_bf16 v[164:167], v[96:99], v[192:195], v[164:167]\n\t"
            "ds_read_b128 v[220:223], v248 offset:6144\n\t"
            "v_mfma_f32_16x16x32_bf16 v[164:167], v[100:103], v[172:175], v[164:167]\n\t"
            "buffer_store_dwordx4 v[152:155], v196, s[40:43], s45 offen\n\t"
            "v_mfma_f32_16x16x32_bf16 v[180:183], v[124:127], v[172:175], v[180:183]\n\t"
            "ds_read_b128 v[172:175], v243 offset:6400\n\t"
            "v_lshlrev_b32 v152, 16, v224\n\t"
            "v_mfma_f32_16x16x32_bf16 v[180:183], v[120:123], v[192:195], v[180:183]\n\t"
            "ds_read_b128 v[192:195], v242 offset:6400\n\t"
            "buffer_store_dwordx4 v[160:163], v196, s[40:43], s45 offen offset:64\n\t"
            "v_mfma_f32_16x16x32_bf16 v[148:151], v[36:39], v[188:191], v[148:151]\n\t"
            "v_and_b32 v153, 0xffff0000, v224\n\t"
            "v_lshlrev_b32 v154, 16, v225\n\t"
            "v_mfma_f32_16x16x32_bf16 v[148:151], v[32:35], v[184:187], v[148:151]\n\t"
            "buffer_store_dwordx4 v[176:179], v196, s[40:43], s45 offen offset:128\n\t"
            "v_mfma_f32_16x16x32_bf16 v[168:171], v[56:59], v[184:187], v[168:171]\n\t"
            "v_and_b32 v155, 0xffff0000, v225\n\t"
            "v_lshlrev_b32 v160, 16, v226\n\t"
            "v_mfma_f32_16x16x32_bf16 v[168:171], v[60:63], v[188:191], v[168:171]\n\t"
            "v_and_b32 v161, 0xffff0000, v226\n\t"
            "v_lshlrev_b32 v162, 16, v227\n\t"
            "v_mfma_f32_16x16x32_bf16 v[156:159], v[84:87], v[188:191], v[156:159]\n\t"
            "v_and_b32 v163, 0xffff0000, v227\n\t"
            "v_lshlrev_b32 v176, 16, v228\n\t"
            "v_mfma_f32_16x16x32_bf16 v[156:159], v[80:83], v[184:187], v[156:159]\n\t"
            "ds_read_b128 v[224:227], v236 offset:12288\n\t"
            "v_and_b32 v177, 0xffff0000, v228\n\t"
            "v_mfma_f32_16x16x32_bf16 v[164:167], v[104:107], v[184:187], v[164:167]\n\t"
            "v_lshlrev_b32 v178, 16, v229\n\t"
            "v_and_b32 v179, 0xffff0000, v229\n\t"
            "v_mfma_f32_16x16x32_bf16 v[164:167], v[108:111], v[188:191], v[164:167]\n\t"
            "v_mfma_f32_16x16x32_bf16 v[180:183], v[132:135], v[188:191], v[180:183]\n\t"
            "ds_read_b128 v[188:191], v243 offset:12288\n\t"
            "v_mfma_f32_16x16x32_bf16 v[180:183], v[128:131], v[184:187], v[180:183]\n\t"
            "ds_read_b128 v[184:187], v242 offset:12288\n\t"
            "s_waitcnt lgkmcnt(3)\n\t"
            "v_mfma_f32_16x16x32_bf16 v[144:147], v[16:19], v[192:195], v[144:147]\n\t"
            "v_mfma_f32_16x16x32_bf16 v[144:147], v[20:23], v[172:175], v[144:147]\n\t"
            "v_mfma_f32_16x16x32_bf16 v[168:171], v[64:67], v[192:195], v[168:171]\n\t"
            "v_mfma_f32_16x16x32_bf16 v[168:171], v[68:71], v[172:175], v[168:171]\n\t"
            "v_mfma_f32_16x16x32_bf16 v[164:167], v[112:115], v[192:195], v[164:167]\n\t"
            "v_mfma_f32_16x16x32_bf16 v[164:167], v[116:119], v[172:175], v[164:167]\n\t"
            "v_mfma_f32_16x16x32_bf16 v[148:151], v[44:47], v[172:175], v[148:151]\n\t"
            "v_mfma_f32_16x16x32_bf16 v[148:151], v[40:43], v[192:195], v[148:151]\n\t"
            "s_nop 1\n\t"
            "v_cvt_pk_bf16_f32 v144, v144, v145\n\t"
            "v_mfma_f32_16x16x32_bf16 v[156:159], v[92:95], v[172:175], v[156:159]\n\t"
            "v_cvt_pk_bf16_f32 v145, v146, v147\n\t"
            "v_mfma_f32_16x16x32_bf16 v[156:159], v[88:91], v[192:195], v[156:159]\n\t"
            "v_cvt_pk_bf16_f32 v168, v168, v169\n\t"
            "v_cvt_pk_bf16_f32 v169, v170, v171\n\t"
            "v_mfma_f32_16x16x32_bf16 v[180:183], v[140:143], v[172:175], v[180:183]\n\t"
            "ds_read_b128 v[172:175], v243 offset:12416\n\t"
            "v_cvt_pk_bf16_f32 v164, v164, v165\n\t"
            "v_mfma_f32_16x16x32_bf16 v[180:183], v[136:139], v[192:195], v[180:183]\n\t"
            "ds_read_b128 v[192:195], v242 offset:12416\n\t"
            "v_cvt_pk_bf16_f32 v165, v166, v167\n\t"
            "s_waitcnt lgkmcnt(2)\n\t"
            "v_mfma_f32_16x16x32_bf16 v[152:155], v[0:3], v[184:187], v[152:155]\n\t"
            "v_cvt_pk_bf16_f32 v146, v148, v149\n\t"
            "v_cvt_pk_bf16_f32 v147, v150, v151\n\t"
            "v_mfma_f32_16x16x32_bf16 v[152:155], v[4:7], v[188:191], v[152:155]\n\t"
            "v_cvt_pk_bf16_f32 v170, v156, v157\n\t"
            "v_cvt_pk_bf16_f32 v171, v158, v159\n\t"
            "v_mfma_f32_16x16x32_bf16 v[160:163], v[28:31], v[188:191], v[160:163]\n\t"
            "v_cvt_pk_bf16_f32 v166, v180, v181\n\t"
            "v_cvt_pk_bf16_f32 v167, v182, v183\n\t"
            "v_mfma_f32_16x16x32_bf16 v[160:163], v[24:27], v[184:187], v[160:163]\n\t"
            "v_lshlrev_b32 v148, 16, v230\n\t"
            "v_and_b32 v149, 0xffff0000, v230\n\t"
            "v_mfma_f32_16x16x32_bf16 v[176:179], v[48:51], v[184:187], v[176:179]\n\t"
            "v_lshlrev_b32 v150, 16, v231\n\t"
            "v_and_b32 v151, 0xffff0000, v231\n\t"
            "v_mfma_f32_16x16x32_bf16 v[176:179], v[52:55], v[188:191], v[176:179]\n\t"
            "ds_read_b128 v[228:231], v237 offset:12288\n\t"
            "v_lshlrev_b32 v156, 16, v232\n\t"
            "v_mfma_f32_16x16x32_bf16 v[148:151], v[76:79], v[188:191], v[148:151]\n\t"
            "v_and_b32 v157, 0xffff0000, v232\n\t"
            "v_lshlrev_b32 v158, 16, v233\n\t"
            "v_mfma_f32_16x16x32_bf16 v[148:151], v[72:75], v[184:187], v[148:151]\n\t"
            "v_and_b32 v159, 0xffff0000, v233\n\t"
            "v_lshlrev_b32 v180, 16, v234\n\t"
            "s_waitcnt lgkmcnt(1)\n\t"
            "v_mfma_f32_16x16x32_bf16 v[152:155], v[8:11], v[192:195], v[152:155]\n\t"
            "v_and_b32 v181, 0xffff0000, v234\n\t"
            "v_lshlrev_b32 v182, 16, v235\n\t"
            "v_mfma_f32_16x16x32_bf16 v[152:155], v[12:15], v[172:175], v[152:155]\n\t"
            "v_and_b32 v183, 0xffff0000, v235\n\t"
            "v_mfma_f32_16x16x32_bf16 v[156:159], v[96:99], v[184:187], v[156:159]\n\t"
            "ds_read_b128 v[232:235], v248 offset:12288\n\t"
            "v_mfma_f32_16x16x32_bf16 v[156:159], v[100:103], v[188:191], v[156:159]\n\t"
            "buffer_store_dwordx4 v[144:147], v197, s[40:43], s45 offen\n\t"
            "v_mfma_f32_16x16x32_bf16 v[180:183], v[124:127], v[188:191], v[180:183]\n\t"
            "ds_read_b128 v[188:191], v243 offset:12544\n\t"
            "v_mfma_f32_16x16x32_bf16 v[180:183], v[120:123], v[184:187], v[180:183]\n\t"
            "ds_read_b128 v[184:187], v242 offset:12544\n\t"
            "buffer_store_dwordx4 v[168:171], v197, s[40:43], s45 offen offset:64\n\t"
            "v_mfma_f32_16x16x32_bf16 v[160:163], v[36:39], v[172:175], v[160:163]\n\t"
            "v_mfma_f32_16x16x32_bf16 v[160:163], v[32:35], v[192:195], v[160:163]\n\t"
            "buffer_store_dwordx4 v[164:167], v197, s[40:43], s45 offen offset:128\n\t"
            "v_mfma_f32_16x16x32_bf16 v[176:179], v[56:59], v[192:195], v[176:179]\n\t"
            "v_mfma_f32_16x16x32_bf16 v[176:179], v[60:63], v[172:175], v[176:179]\n\t"
            "v_mfma_f32_16x16x32_bf16 v[148:151], v[84:87], v[172:175], v[148:151]\n\t"
            "v_mfma_f32_16x16x32_bf16 v[148:151], v[80:83], v[192:195], v[148:151]\n\t"
            "s_waitcnt lgkmcnt(0)\n\t"
            "s_barrier\n\t"
            "v_mfma_f32_16x16x32_bf16 v[152:155], v[16:19], v[184:187], v[152:155]\n\t"
            "v_mfma_f32_16x16x32_bf16 v[152:155], v[20:23], v[188:191], v[152:155]\n\t"
            "v_mfma_f32_16x16x32_bf16 v[160:163], v[44:47], v[188:191], v[160:163]\n\t"
            "v_mfma_f32_16x16x32_bf16 v[160:163], v[40:43], v[184:187], v[160:163]\n\t"
            "v_mfma_f32_16x16x32_bf16 v[156:159], v[104:107], v[192:195], v[156:159]\n\t"
            "v_mfma_f32_16x16x32_bf16 v[156:159], v[108:111], v[172:175], v[156:159]\n\t"
            "v_mfma_f32_16x16x32_bf16 v[180:183], v[132:135], v[172:175], v[180:183]\n\t"
            "v_mfma_f32_16x16x32_bf16 v[180:183], v[128:131], v[192:195], v[180:183]\n\t"
            "s_nop 1\n\t"
            "v_cvt_pk_bf16_f32 v152, v152, v153\n\t"
            "v_mfma_f32_16x16x32_bf16 v[176:179], v[64:67], v[184:187], v[176:179]\n\t"
            "v_cvt_pk_bf16_f32 v153, v154, v155\n\t"
            "v_mfma_f32_16x16x32_bf16 v[176:179], v[68:71], v[188:191], v[176:179]\n\t"
            "v_cvt_pk_bf16_f32 v154, v160, v161\n\t"
            "v_cvt_pk_bf16_f32 v155, v162, v163\n\t"
            "v_mfma_f32_16x16x32_bf16 v[148:151], v[92:95], v[188:191], v[148:151]\n\t"
            "v_mfma_f32_16x16x32_bf16 v[148:151], v[88:91], v[184:187], v[148:151]\n\t"
            "buffer_store_dwordx4 v[152:155], v198, s[40:43], s45 offen\n\t"
            "v_mfma_f32_16x16x32_bf16 v[156:159], v[112:115], v[184:187], v[156:159]\n\t"
            "s_nop 1\n\t"
            "v_cvt_pk_bf16_f32 v176, v176, v177\n\t"
            "v_mfma_f32_16x16x32_bf16 v[156:159], v[116:119], v[188:191], v[156:159]\n\t"
            "v_cvt_pk_bf16_f32 v177, v178, v179\n\t"
            "v_mfma_f32_16x16x32_bf16 v[180:183], v[140:143], v[188:191], v[180:183]\n\t"
            "v_cvt_pk_bf16_f32 v178, v148, v149\n\t"
            "v_mfma_f32_16x16x32_bf16 v[180:183], v[136:139], v[184:187], v[180:183]\n\t"
            "v_cvt_pk_bf16_f32 v179, v150, v151\n\t"
            "buffer_store_dwordx4 v[176:179], v198, s[40:43], s45 offen offset:64\n\t"
            "s_nop 1\n\t"
            "v_cvt_pk_bf16_f32 v156, v156, v157\n\t"
            "v_cvt_pk_bf16_f32 v157, v158, v159\n\t"
            "s_nop 1\n\t"
            "v_cvt_pk_bf16_f32 v158, v180, v181\n\t"
            "v_cvt_pk_bf16_f32 v159, v182, v183\n\t"
            "buffer_store_dwordx4 v[156:159], v198, s[40:43], s45 offen offset:128\n\t"
            "s_add_u32 s45, s46, s27\n\t"
            "s_mov_b32 s46, s47\n\t"
            "s_mov_b32 s47, s68\n\t"
            "s_xor_b32 s48, s48, s71\n\t"
            "s_xor_b32 s67, s67, s71\n\t"
            "s_add_u32 s76, s76, 4\n\t"
            "s_add_u32 s70, s70, 4\n\t"
            "s_lshl_b32 s56, s56, 2\n\t"
            "s_lshr_b32 s49, s26, 30\n\t"
            "s_or_b32 s56, s56, s49\n\t"
            "s_cmp_lt_u32 s46, 0x7000000\n\t"
            "s_cbranch_scc1 .Lwb_loop%=\n\t"
            "s_branch .Lwb_end%=\n\t"
            ".Lwb_spin%=:\n\t"
            "s_sleep 1\n\t"
            "ds_read_b32 v247, v255 offset:7856\n\t"
            "s_waitcnt lgkmcnt(0)\n\t"
            "v_readfirstlane_b32 s77, v247\n\t"
            "s_cmp_ge_u32 s77, s70\n\t"
            "s_cbranch_scc0 .Lwb_spin%=\n\t"
            "s_branch .Lwb_go%=\n\t"
            ".Lwb_end%=:\n\t"
            "s_waitcnt lgkmcnt(0)\n\t"
            "s_nop 7\n\t"
            : "={v[0:3]}"(W[0]), "={v[4:7]}"(W[1]), "={v[8:11]}"(W[2]), "={v[12:15]}"(W[3]), "={v[16:19]}"(W[4]), "={v[20:23]}"(W[5]), "={v[24:27]}"(W[6]), "={v[28:31]}"(W[7]), "={v[32:35]}"(W[8]), "={v[36:39]}"(W[9]), "={v[40:43]}"(W[10]), "={v[44:47]}"(W[11]), "={v[48:51]}"(W[12]), "={v[52:55]}"(W[13]), "={v[56:59]}"(W[14]), "={v[60:63]}"(W[15]), "={v[64:67]}"(W[16]), "={v[68:71]}"(W[17]), "={v[72:75]}"(W[18]), "={v[76:79]}"(W[19]), "={v[80:83]}"(W[20]), "={v[84:87]}"(W[21]), "={v[88:91]}"(W[22]), "={v[92:95]}"(W[23]), "={v[96:99]}"(W[24]), "={v[100:103]}"(W[25]), "={v[104:107]}"(W[26]), "={v[108:111]}"(W[27]), "={v[112:115]}"(W[28]), "={v[116:119]}"(W[29]), "={v[120:123]}"(W[30]), "={v[124:127]}"(W[31]), "={v[128:131]}"(W[32]), "={v[132:135]}"(W[33]), "={v[136:139]}"(W[34]), "={v[140:143]}"(W[35]), "+{v[200:203]}"(xl[0]), "+{v[204:207]}"(xl[1]), "+{v[208:211]}"(xl[2]), "+{v[212:215]}"(xl[3]), "+{v[216:219]}"(xl[4]), "+{v[220:223]}"(xl[5]), "+{v[224:227]}"(xl[6]), "+{v[228:231]}"(xl[7]), "+{v[232:235]}"(xl[8]), B_SIO, "+{v247}"(l16_)
            : "{v242}"(oE_), "{v243}"(oO_), "{v244}"(pR0_), "{v245}"(pR1_), "{v246}"(pR2_), "{v250}"(pXw_), "{v251}"(pSTw_), "{v252}"(aCA_), "{v253}"(aB_), "{v254}"(aA_), "{v255}"(idx_), B_SIN
            : "v144", "v145", "v146", "v147", "v148", "v149", "v150", "v151", "v152", "v153", "v154", "v155", "v156", "v157", "v158", "v159", "v160", "v161", "v162", "v163", "v164", "v165", "v166", "v167", "v168", "v169", "v170", "v171", "v172", "v173", "v174", "v175", "v176", "v177", "v178", "v179", "v180", "v181", "v182", "v183", "v184", "v185", "v186", "v187", "v188", "v189", "v190", "v191", "v192", "v193", "v194", "v195", "v196", "v197", "v198", "v199", "v236", "v237", "v248", "v249", B_SCLOB);
            t45 = s45; t56 = s56;
        }
        {
            int ln_;
            asm volatile("v_mbcnt_lo_u32_b32 %0, -1, 0\n\tv_mbcnt_hi_u32_b32 %0, -1, %0" : "=v"(ln_));
            const int l15_ = ln_ & 15;
            const int g_ = ln_ >> 4;
#pragma unroll
            for (int lt = 0; lt < 2; ++lt) {
                int i = 16 * lt + l15_;
                const unsigned en = *(const unsigned*)(smem + WL_OFF + i * 4);
                const bool ok = (en & 0x7ffffffu) != 0x7ffffffu;
                i = ok ? i : 0;
                const int lE = OL_OFF + i * 384 + ((g_ ^ ((i >> 1) & 7)) << 4);
                const unsigned o = ok ? (en & 0x7ffffffu) + LPOS(en >> 31) * ROWBYTES + LPOS(en >> 30) * (CH * 2) + 192 * (w - 2) + 16 * g_ : OOB;
                u32x4 rl[3];
#pragma unroll
                for (int k = 0; k < 3; ++k) rl[k] = __builtin_bit_cast(u32x4, __builtin_amdgcn_raw_buffer_load_b128(orsrc, o + 64 * k, 0, 0));
                if (lt == 1) {
                    const unsigned oE_ = ldsb + O_OFF + l15_ * 384 + ((g_ ^ (l15_ >> 1)) << 4), oO_ = oE_ ^ 64, aCA_ = ldsb + CT_OFF + ln_ * 16;
                    const unsigned long long ob = (unsigned long long)out;
                    const u32x4 orr = {(unsigned)ob, (unsigned)(ob >> 32) & 0xffffu, (unsigned)(IMG * IMG * CH * 2), 0x00020000u};
                    asm volatile(
            "s_waitcnt lgkmcnt(0)\n\t"
            "s_barrier\n\t"
            "s_nop 7\n\t"
            "ds_read_b128 v[184:187], v242\n\t"
            "ds_read_b128 v[188:191], v243\n\t"
            "ds_read_b128 v[192:195], v242 offset:128\n\t"
            "ds_read_b128 v[172:175], v243 offset:128\n\t"
            "ds_read_b128 v[196:199], v252\n\t"
            "v_lshlrev_b32 v152, 16, v200\n\t"
            "v_and_b32 v153, 0xffff0000, v200\n\t"
            "v_lshlrev_b32 v154, 16, v201\n\t"
            "v_and_b32 v155, 0xffff0000, v201\n\t"
            "v_lshlrev_b32 v156, 16, v202\n\t"
            "v_and_b32 v157, 0xffff0000, v202\n\t"
            "v_lshlrev_b32 v158, 16, v203\n\t"
            "v_and_b32 v159, 0xffff0000, v203\n\t"
            "v_lshlrev_b32 v160, 16, v204\n\t"
            "v_and_b32 v161, 0xffff0000, v204\n\t"
            "v_lshlrev_b32 v162, 16, v205\n\t"
            "v_and_b32 v163, 0xffff0000, v205\n\t"
            "v_lshlrev_b32 v164, 16, v206\n\t"
            "v_and_b32 v165, 0xffff0000, v206\n\t"
            "v_lshlrev_b32 v166, 16, v207\n\t"
            "v_and_b32 v167, 0xffff0000, v207\n\t"
            "v_lshlrev_b32 v176, 16, v208\n\t"
            "v_and_b32 v177, 0xffff0000, v208\n\t"
            "v_lshlrev_b32 v178, 16, v209\n\t"
            "v_and_b32 v179, 0xffff0000, v209\n\t"
            "v_lshlrev_b32 v180, 16, v210\n\t"
            "v_and_b32 v181, 0xffff0000, v210\n\t"
            "v_lshlrev_b32 v182, 16, v211\n\t"
            "v_and_b32 v183, 0xffff0000, v211\n\t"
            "v_lshlrev_b32 v144, 16, v212\n\t"
            "v_and_b32 v145, 0xffff0000, v212\n\t"
            "v_lshlrev_b32 v146, 16, v213\n\t"
            "s_waitcnt lgkmcnt(3)\n\t"
            "v_mfma_f32_16x16x32_bf16 v[152:155], v[0:3], v[184:187], v[152:155]\n\t"
            "v_and_b32 v147, 0xffff0000, v213\n\t"
            "v_lshlrev_b32 v148, 16, v214\n\t"
            "v_mfma_f32_16x16x32_bf16 v[152:155], v[4:7], v[188:191], v[152:155]\n\t"
            "s_bfe_u32 s57, s56, 0x20004\n\t"
            "s_lshl_b32 s58, s57, 3\n\t"
            "s_lshr_b32 s58, s55, s58\n\t"
            "s_lshl_b64 s[36:37], s[34:35], s58\n\t"
            "v_mfma_f32_16x16x32_bf16 v[156:159], v[28:31], v[188:191], v[156:159]\n\t"
            "v_and_b32 v149, 0xffff0000, v214\n\t"
            "v_lshlrev_b32 v150, 16, v215\n\t"
            "v_mfma_f32_16x16x32_bf16 v[156:159], v[24:27], v[184:187], v[156:159]\n\t"
            "s_bitcmp1_b32 s57, 1\n\t"
            "s_cselect_b32 s58, 1177344, 0\n\t"
            "s_bitcmp1_b32 s57, 0\n\t"
            "s_cselect_b32 s57, 2304, 0\n\t"
            "s_add_u32 s57, s57, s58\n\t"
            "v_mfma_f32_16x16x32_bf16 v[160:163], v[48:51], v[184:187], v[160:163]\n\t"
            "v_and_b32 v151, 0xffff0000, v215\n\t"
            "v_lshlrev_b32 v168, 16, v216\n\t"
            "v_mfma_f32_16x16x32_bf16 v[160:163], v[52:55], v[188:191], v[160:163]\n\t"
            "v_and_b32 v169, 0xffff0000, v216\n\t"
            "v_lshlrev_b32 v170, 16, v217\n\t"
            "v_mfma_f32_16x16x32_bf16 v[164:167], v[76:79], v[188:191], v[164:167]\n\t"
            "v_and_b32 v171, 0xffff0000, v217\n\t"
            "v_mfma_f32_16x16x32_bf16 v[164:167], v[72:75], v[184:187], v[164:167]\n\t"
            "s_waitcnt lgkmcnt(0)\n\t"
            "s_mov_b64 exec, s[36:37]\n\t"
            "v_add_u32 v196, s57, v196\n\t"
            "s_mov_b64 exec, -1\n\t"
            "v_mfma_f32_16x16x32_bf16 v[176:179], v[96:99], v[184:187], v[176:179]\n\t"
            "v_mfma_f32_16x16x32_bf16 v[176:179], v[100:103], v[188:191], v[176:179]\n\t"
            "v_mfma_f32_16x16x32_bf16 v[180:183], v[124:127], v[188:191], v[180:183]\n\t"
            "ds_read_b128 v[188:191], v243 offset:256\n\t"
            "v_mfma_f32_16x16x32_bf16 v[180:183], v[120:123], v[184:187], v[180:183]\n\t"
            "ds_read_b128 v[184:187], v242 offset:256\n\t"
            "v_mfma_f32_16x16x32_bf16 v[152:155], v[8:11], v[192:195], v[152:155]\n\t"
            "v_mfma_f32_16x16x32_bf16 v[152:155], v[12:15], v[172:175], v[152:155]\n\t"
            "v_mfma_f32_16x16x32_bf16 v[156:159], v[36:39], v[172:175], v[156:159]\n\t"
            "v_mfma_f32_16x16x32_bf16 v[156:159], v[32:35], v[192:195], v[156:159]\n\t"
            "v_mfma_f32_16x16x32_bf16 v[160:163], v[56:59], v[192:195], v[160:163]\n\t"
            "v_mfma_f32_16x16x32_bf16 v[160:163], v[60:63], v[172:175], v[160:163]\n\t"
            "v_mfma_f32_16x16x32_bf16 v[164:167], v[84:87], v[172:175], v[164:167]\n\t"
            "v_mfma_f32_16x16x32_bf16 v[164:167], v[80:83], v[192:195], v[164:167]\n\t"
            "v_mfma_f32_16x16x32_bf16 v[176:179], v[104:107], v[192:195], v[176:179]\n\t"
            "v_mfma_f32_16x16x32_bf16 v[176:179], v[108:111], v[172:175], v[176:179]\n\t"
            "v_mfma_f32_16x16x32_bf16 v[180:183], v[132:135], v[172:175], v[180:183]\n\t"
            "ds_read_b128 v[172:175], v243 offset:6144\n\t"
            "v_mfma_f32_16x16x32_bf16 v[180:183], v[128:131], v[192:195], v[180:183]\n\t"
            "ds_read_b128 v[192:195], v242 offset:6144\n\t"
            "s_waitcnt lgkmcnt(2)\n\t"
            "v_mfma_f32_16x16x32_bf16 v[152:155], v[16:19], v[184:187], v[152:155]\n\t"
            "v_mfma_f32_16x16x32_bf16 v[152:155], v[20:23], v[188:191], v[152:155]\n\t"
            "v_mfma_f32_16x16x32_bf16 v[160:163], v[64:67], v[184:187], v[160:163]\n\t"
            "v_mfma_f32_16x16x32_bf16 v[160:163], v[68:71], v[188:191], v[160:163]\n\t"
            "v_mfma_f32_16x16x32_bf16 v[176:179], v[112:115], v[184:187], v[176:179]\n\t"
            "v_mfma_f32_16x16x32_bf16 v[176:179], v[116:119], v[188:191], v[176:179]\n\t"
            "v_mfma_f32_16x16x32_bf16 v[156:159], v[44:47], v[188:191], v[156:159]\n\t"
            "v_mfma_f32_16x16x32_bf16 v[156:159], v[40:43], v[184:187], v[156:159]\n\t"
            "s_nop 1\n\t"
            "v_cvt_pk_bf16_f32 v152, v152, v153\n\t"
            "v_mfma_f32_16x16x32_bf16 v[164:167], v[92:95], v[188:191], v[164:167]\n\t"
            "v_cvt_pk_bf16_f32 v153, v154, v155\n\t"
            "v_mfma_f32_16x16x32_bf16 v[164:167], v[88:91], v[184:187], v[164:167]\n\t"
            "v_cvt_pk_bf16_f32 v160, v160, v161\n\t"
            "v_cvt_pk_bf16_f32 v161, v162, v163\n\t"
            "v_mfma_f32_16x16x32_bf16 v[180:183], v[140:143], v[188:191], v[180:183]\n\t"
            "ds_read_b128 v[188:191], v243 offset:6272\n\t"
            "v_cvt_pk_bf16_f32 v176, v176, v177\n\t"
            "v_mfma_f32_16x16x32_bf16 v[180:183], v[136:139], v[184:187], v[180:183]\n\t"
            "ds_read_b128 v[184:187], v242 offset:6272\n\t"
            "v_cvt_pk_bf16_f32 v177, v178, v179\n\t"
            "s_waitcnt lgkmcnt(2)\n\t"
            "v_mfma_f32_16x16x32_bf16 v[144:147], v[0:3], v[192:195], v[144:147]\n\t"
            "v_cvt_pk_bf16_f32 v154, v156, v157\n\t"
            "v_cvt_pk_bf16_f32 v155, v158, v159\n\t"
            "v_mfma_f32_16x16x32_bf16 v[144:147], v[4:7], v[172:175], v[144:147]\n\t"
            "v_cvt_pk_bf16_f32 v162, v164, v165\n\t"
            "v_cvt_pk_bf16_f32 v163, v166, v167\n\t"
            "v_mfma_f32_16x16x32_bf16 v[148:151], v[28:31], v[172:175], v[148:151]\n\t"
            "v_cvt_pk_bf16_f32 v178, v180, v181\n\t"
            "v_cvt_pk_bf16_f32 v179, v182, v183\n\t"
            "v_mfma_f32_16x16x32_bf16 v[148:151], v[24:27], v[192:195], v[148:151]\n\t"
            "v_lshlrev_b32 v156, 16, v218\n\t"
            "v_and_b32 v157, 0xffff0000, v218\n\t"
            "v_mfma_f32_16x16x32_bf16 v[168:171], v[48:51], v[192:195], v[168:171]\n\t"
            "v_lshlrev_b32 v158, 16, v219\n\t"
            "v_and_b32 v159, 0xffff0000, v219\n\t"
            "v_mfma_f32_16x16x32_bf16 v[168:171], v[52:55], v[172:175], v[168:171]\n\t"
            "v_lshlrev_b32 v164, 16, v220\n\t"
            "v_and_b32 v165, 0xffff0000, v220\n\t"
            "v_mfma_f32_16x16x32_bf16 v[156:159], v[76:79], v[172:175], v[156:159]\n\t"
            "v_lshlrev_b32 v166, 16, v221\n\t"
            "v_and_b32 v167, 0xffff0000, v221\n\t"
            "v_mfma_f32_16x16x32_bf16 v[156:159], v[72:75], v[192:195], v[156:159]\n\t"
            "v_lshlrev_b32 v180, 16, v222\n\t"
            "v_and_b32 v181, 0xffff0000, v222\n\t"
            "v_mfma_f32_16x16x32_bf16 v[164:167], v[96:99], v[192:195], v[164:167]\n\t"
            "v_lshlrev_b32 v182, 16, v223\n\t"
            "v_and_b32 v183, 0xffff0000, v223\n\t"
            "v_mfma_f32_16x16x32_bf16 v[164:167], v[100:103], v[172:175], v[164:167]\n\t"
            "buffer_store_dwordx4 v[152:155], v196, s[40:43], s45 offen\n\t"
            "v_mfma_f32_16x16x32_bf16 v[180:183], v[124:127], v[172:175], v[180:183]\n\t"
            "ds_read_b128 v[172:175], v243 offset:6400\n\t"
            "v_lshlrev_b32 v152, 16, v224\n\t"
            "v_mfma_f32_16x16x32_bf16 v[180:183], v[120:123], v[192:195], v[180:183]\n\t"
            "ds_read_b128 v[192:195], v242 offset:6400\n\t"
            "buffer_store_dwordx4 v[160:163], v196, s[40:43], s45 offen offset:64\n\t"
            "s_waitcnt lgkmcnt(2)\n\t"
            "v_mfma_f32_16x16x32_bf16 v[144:147], v[8:11], v[184:187], v[144:147]\n\t"
            "v_and_b32 v153, 0xffff0000, v224\n\t"
            "v_lshlrev_b32 v154, 16, v225\n\t"
            "v_mfma_f32_16x16x32_bf16 v[144:147], v[12:15], v[188:191], v[144:147]\n\t"
            "buffer_store_dwordx4 v[176:179], v196, s[40:43], s45 offen offset:128\n\t"
            "v_mfma_f32_16x16x32_bf16 v[148:151], v[36:39], v[188:191], v[148:151]\n\t"
            "v_and_b32 v155, 0xffff0000, v225\n\t"
            "v_lshlrev_b32 v160, 16, v226\n\t"
            "v_mfma_f32_16x16x32_bf16 v[148:151], v[32:35], v[184:187], v[148:151]\n\t"
            "v_and_b32 v161, 0xffff0000, v226\n\t"
            "v_lshlrev_b32 v162, 16, v227\n\t"
            "v_mfma_f32_16x16x32_bf16 v[168:171], v[56:59], v[184:187], v[168:171]\n\t"
            "v_and_b32 v163, 0xffff0000, v227\n\t"
            "v_lshlrev_b32 v176, 16, v228\n\t"
            "v_mfma_f32_16x16x32_bf16 v[168:171], v[60:63], v[188:191], v[168:171]\n\t"
            "v_and_b32 v177, 0xffff0000, v228\n\t"
            "v_lshlrev_b32 v178, 16, v229\n\t"
            "v_mfma_f32_16x16x32_bf16 v[156:159], v[84:87], v[188:191], v[156:159]\n\t"
            "v_and_b32 v179, 0xffff0000, v229\n\t"
            "v_mfma_f32_16x16x32_bf16 v[156:159], v[80:83], v[184:187], v[156:159]\n\t"
            "v_mfma_f32_16x16x32_bf16 v[164:167], v[104:107], v[184:187], v[164:167]\n\t"
            "v_mfma_f32_16x16x32_bf16 v[164:167], v[108:111], v[188:191], v[164:167]\n\t"
            "v_mfma_f32_16x16x32_bf16 v[180:183], v[132:135], v[188:191], v[180:183]\n\t"
            "ds_read_b128 v[188:191], v243 offset:12288\n\t"
            "v_mfma_f32_16x16x32_bf16 v[180:183], v[128:131], v[184:187], v[180:183]\n\t"
            "ds_read_b128 v[184:187], v242 offset:12288\n\t"
            "s_waitcnt lgkmcnt(2)\n\t"
            "v_mfma_f32_16x16x32_bf16 v[144:147], v[16:19], v[192:195], v[144:147]\n\t"
            "v_mfma_f32_16x16x32_bf16 v[144:147], v[20:23], v[172:175], v[144:147]\n\t"
            "v_mfma_f32_16x16x32_bf16 v[168:171], v[64:67], v[192:195], v[168:171]\n\t"
            "v_mfma_f32_16x16x32_bf16 v[168:171], v[68:71], v[172:175], v[168:171]\n\t"
            "v_mfma_f32_16x16x32_bf16 v[164:167], v[112:115], v[192:195], v[164:167]\n\t"
            "v_mfma_f32_16x16x32_bf16 v[164:167], v[116:119], v[172:175], v[164:167]\n\t"
            "v_mfma_f32_16x16x32_bf16 v[148:151], v[44:47], v[172:175], v[148:151]\n\t"
            "v_mfma_f32_16x16x32_bf16 v[148:151], v[40:43], v[192:195], v[148:151]\n\t"
            "s_nop 1\n\t"
            "v_cvt_pk_bf16_f32 v144, v144, v145\n\t"
            "v_mfma_f32_16x16x32_bf16 v[156:159], v[92:95], v[172:175], v[156:159]\n\t"
            "v_cvt_pk_bf16_f32 v145, v146, v147\n\t"
            "v_mfma_f32_16x16x32_bf16 v[156:159], v[88:91], v[192:195], v[156:159]\n\t"
            "v_cvt_pk_bf16_f32 v168, v168, v169\n\t"
            "v_cvt_pk_bf16_f32 v169, v170, v171\n\t"
            "v_mfma_f32_16x16x32_bf16 v[180:183], v[140:143], v[172:175], v[180:183]\n\t"
            "ds_read_b128 v[172:175], v243 offset:12416\n\t"
            "v_cvt_pk_bf16_f32 v164, v164, v165\n\t"
            "v_mfma_f32_16x16x32_bf16 v[180:183], v[136:139], v[192:195], v[180:183]\n\t"
            "ds_read_b128 v[192:195], v242 offset:12416\n\t"
            "v_cvt_pk_bf16_f32 v165, v166, v167\n\t"
            "s_waitcnt lgkmcnt(2)\n\t"
            "v_mfma_f32_16x16x32_bf16 v[152:155], v[0:3], v[184:187], v[152:155]\n\t"
            "v_cvt_pk_bf16_f32 v146, v148, v149\n\t"
            "v_cvt_pk_bf16_f32 v147, v150, v151\n\t"
            "v_mfma_f32_16x16x32_bf16 v[152:155], v[4:7], v[188:191], v[152:155]\n\t"
            "v_cvt_pk_bf16_f32 v170, v156, v157\n\t"
            "v_cvt_pk_bf16_f32 v171, v158, v159\n\t"
            "v_mfma_f32_16x16x32_bf16 v[160:163], v[28:31], v[188:191], v[160:163]\n\t"
            "v_cvt_pk_bf16_f32 v166, v180, v181\n\t"
            "v_cvt_pk_bf16_f32 v167, v182, v183\n\t"
            "v_mfma_f32_16x16x32_bf16 v[160:163], v[24:27], v[184:187], v[160:163]\n\t"
            "v_lshlrev_b32 v148, 16, v230\n\t"
            "v_and_b32 v149, 0xffff0000, v230\n\t"
            "v_mfma_f32_16x16x32_bf16 v[176:179], v[48:51], v[184:187], v[176:179]\n\t"
            "v_lshlrev_b32 v150, 16, v231\n\t"
            "v_and_b32 v151, 0xffff0000, v231\n\t"
            "v_mfma_f32_16x16x32_bf16 v[176:179], v[52:55], v[188:191], v[176:179]\n\t"
            "v_lshlrev_b32 v156, 16, v232\n\t"
            "v_and_b32 v157, 0xffff0000, v232\n\t"
            "v_mfma_f32_16x16x32_bf16 v[148:151], v[76:79], v[188:191], v[148:151]\n\t"
            "v_lshlrev_b32 v158, 16, v233\n\t"
            "v_and_b32 v159, 0xffff0000, v233\n\t"
            "v_mfma_f32_16x16x32_bf16 v[148:151], v[72:75], v[184:187], v[148:151]\n\t"
            "v_lshlrev_b32 v180, 16, v234\n\t"
            "v_and_b32 v181, 0xffff0000, v234\n\t"
            "v_mfma_f32_16x16x32_bf16 v[156:159], v[96:99], v[184:187], v[156:159]\n\t"
            "v_lshlrev_b32 v182, 16, v235\n\t"
            "v_and_b32 v183, 0xffff0000, v235\n\t"
            "v_mfma_f32_16x16x32_bf16 v[156:159], v[100:103], v[188:191], v[156:159]\n\t"
            "buffer_store_dwordx4 v[144:147], v197, s[40:43], s45 offen\n\t"
            "v_mfma_f32_16x16x32_bf16 v[180:183], v[124:127], v[188:191], v[180:183]\n\t"
            "ds_read_b128 v[188:191], v243 offset:12544\n\t"
            "v_mfma_f32_16x16x32_bf16 v[180:183], v[120:123], v[184:187], v[180:183]\n\t"
            "ds_read_b128 v[184:187], v242 offset:12544\n\t"
            "buffer_store_dwordx4 v[168:171], v197, s[40:43], s45 offen offset:64\n\t"
            "s_waitcnt lgkmcnt(2)\n\t"
            "v_mfma_f32_16x16x32_bf16 v[152:155], v[8:11], v[192:195], v[152:155]\n\t"
            "v_mfma_f32_16x16x32_bf16 v[152:155], v[12:15], v[172:175], v[152:155]\n\t"
            "buffer_store_dwordx4 v[164:167], v197, s[40:43], s45 offen offset:128\n\t"
            "v_mfma_f32_16x16x32_bf16 v[160:163], v[36:39], v[172:175], v[160:163]\n\t"
            "v_mfma_f32_16x16x32_bf16 v[160:163], v[32:35], v[192:195], v[160:163]\n\t"
            "v_mfma_f32_16x16x32_bf16 v[176:179], v[56:59], v[192:195], v[176:179]\n\t"
            "v_mfma_f32_16x16x32_bf16 v[176:179], v[60:63], v[172:175], v[176:179]\n\t"
            "v_mfma_f32_16x16x32_bf16 v[148:151], v[84:87], v[172:175], v[148:151]\n\t"
            "v_mfma_f32_16x16x32_bf16 v[148:151], v[80:83], v[192:195], v[148:151]\n\t"
            "s_waitcnt lgkmcnt(0)\n\t"
            "v_mfma_f32_16x16x32_bf16 v[152:155], v[16:19], v[184:187], v[152:155]\n\t"
            "v_mfma_f32_16x16x32_bf16 v[152:155], v[20:23], v[188:191], v[152:155]\n\t"
            "v_mfma_f32_16x16x32_bf16 v[160:163], v[44:47], v[188:191], v[160:163]\n\t"
            "v_mfma_f32_16x16x32_bf16 v[160:163], v[40:43], v[184:187], v[160:163]\n\t"
            "v_mfma_f32_16x16x32_bf16 v[156:159], v[104:107], v[192:195], v[156:159]\n\t"
            "v_mfma_f32_16x16x32_bf16 v[156:159], v[108:111], v[172:175], v[156:159]\n\t"
            "v_mfma_f32_16x16x32_bf16 v[180:183], v[132:135], v[172:175], v[180:183]\n\t"
            "v_mfma_f32_16x16x32_bf16 v[180:183], v[128:131], v[192:195], v[180:183]\n\t"
            "s_nop 1\n\t"
            "v_cvt_pk_bf16_f32 v152, v152, v153\n\t"
            "v_mfma_f32_16x16x32_bf16 v[176:179], v[64:67], v[184:187], v[176:179]\n\t"
            "v_cvt_pk_bf16_f32 v153, v154, v155\n\t"
            "v_mfma_f32_16x16x32_bf16 v[176:179], v[68:71], v[188:191], v[176:179]\n\t"
            "v_cvt_pk_bf16_f32 v154, v160, v161\n\t"
            "v_cvt_pk_bf16_f32 v155, v162, v163\n\t"
            "v_mfma_f32_16x16x32_bf16 v[148:151], v[92:95], v[188:191], v[148:151]\n\t"
            "v_mfma_f32_16x16x32_bf16 v[148:151], v[88:91], v[184:187], v[148:151]\n\t"
            "buffer_store_dwordx4 v[152:155], v198, s[40:43], s45 offen\n\t"
            "v_mfma_f32_16x16x32_bf16 v[156:159], v[112:115], v[184:187], v[156:159]\n\t"
            "s_nop 1\n\t"
            "v_cvt_pk_bf16_f32 v176, v176, v177\n\t"
            "v_mfma_f32_16x16x32_bf16 v[156:159], v[116:119], v[188:191], v[156:159]\n\t"
            "v_cvt_pk_bf16_f32 v177, v178, v179\n\t"
            "v_mfma_f32_16x16x32_bf16 v[180:183], v[140:143], v[188:191], v[180:183]\n\t"
            "v_cvt_pk_bf16_f32 v178, v148, v149\n\t"
            "v_mfma_f32_16x16x32_bf16 v[180:183], v[136:139], v[184:187], v[180:183]\n\t"
            "v_cvt_pk_bf16_f32 v179, v150, v151\n\t"
            "buffer_store_dwordx4 v[176:179], v198, s[40:43], s45 offen offset:64\n\t"
            "s_nop 1\n\t"
            "v_cvt_pk_bf16_f32 v156, v156, v157\n\t"
            "v_cvt_pk_bf16_f32 v157, v158, v159\n\t"
            "s_nop 1\n\t"
            "v_cvt_pk_bf16_f32 v158, v180, v181\n\t"
            "v_cvt_pk_bf16_f32 v159, v182, v183\n\t"
            "buffer_store_dwordx4 v[156:159], v198, s[40:43], s45 offen offset:128\n\t"
            "s_nop 7\n\t"
                    :: "{v[0:3]}"(W[0]), "{v[4:7]}"(W[1]), "{v[8:11]}"(W[2]), "{v[12:15]}"(W[3]), "{v[16:19]}"(W[4]), "{v[20:23]}"(W[5]), "{v[24:27]}"(W[6]), "{v[28:31]}"(W[7]), "{v[32:35]}"(W[8]), "{v[36:39]}"(W[9]), "{v[40:43]}"(W[10]), "{v[44:47]}"(W[11]), "{v[48:51]}"(W[12]), "{v[52:55]}"(W[13]), "{v[56:59]}"(W[14]), "{v[60:63]}"(W[15]), "{v[64:67]}"(W[16]), "{v[68:71]}"(W[17]), "{v[72:75]}"(W[18]), "{v[76:79]}"(W[19]), "{v[80:83]}"(W[20]), "{v[84:87]}"(W[21]), "{v[88:91]}"(W[22]), "{v[92:95]}"(W[23]), "{v[96:99]}"(W[24]), "{v[100:103]}"(W[25]), "{v[104:107]}"(W[26]), "{v[108:111]}"(W[27]), "{v[112:115]}"(W[28]), "{v[116:119]}"(W[29]), "{v[120:123]}"(W[30]), "{v[124:127]}"(W[31]), "{v[128:131]}"(W[32]), "{v[132:135]}"(W[33]), "{v[136:139]}"(W[34]), "{v[140:143]}"(W[35]), "{v[200:203]}"(xl[0]), "{v[204:207]}"(xl[1]), "{v[208:211]}"(xl[2]), "{v[212:215]}"(xl[3]), "{v[216:219]}"(xl[4]), "{v[220:223]}"(xl[5]), "{v[224:227]}"(xl[6]), "{v[228:231]}"(xl[7]), "{v[232:235]}"(xl[8]), "{v242}"(oE_), "{v243}"(oO_), "{v252}"(aCA_),
                       "{s[40:43]}"(orr), "{s45}"(t45), "{s56}"(t56), "{s55}"(CROW & 0x0F0F0F0Fu), "{s[34:35]}"(0x0001000100010001ull)
                    : "v144", "v145", "v146", "v147", "v148", "v149", "v150", "v151", "v152", "v153", "v154", "v155", "v156", "v157", "v158", "v159", "v160", "v161", "v162", "v163", "v164", "v165", "v166", "v167", "v168", "v169", "v170", "v171", "v172", "v173", "v174", "v175", "v176", "v177", "v178", "v179", "v180", "v181", "v182", "v183", "v184", "v185", "v186", "v187", "v188", "v189", "v190", "v191", "v192", "v193", "v194", "v195", "v196", "v197", "v198", "v199", "s36", "s37", "s57", "s58", "scc", "memory");
                }
                f32x4 acc[6];
#pragma unroll
                for (int ks = 0; ks < 6; ++ks) {
                    const bf16x8 of = *(const bf16x8*)(smem + ((ks & 1) ? (lE ^ 64) : lE) + (ks >> 1) * 128);
#pragma unroll
                    for (int j = 0; j < 6; ++j) acc[j] = MFMA32(W[j * 6 + ks], of, ks == 0 ? ZERO4 : acc[j]);
                }
#pragma unroll
                for (int k = 0; k < 3; ++k) {
                    const f32x4 a = acc[2 * k], b = acc[2 * k + 1];
                    const u32x4 ov = {pk2(a[0] + bflo(rl[k][0]), a[1] + bfhi(rl[k][0])), pk2(a[2] + bflo(rl[k][1]), a[3] + bfhi(rl[k][1])),
                                      pk2(b[0] + bflo(rl[k][2]), b[1] + bfhi(rl[k][2])), pk2(b[2] + bflo(rl[k][3]), b[3] + bfhi(rl[k][3]))};
                    __builtin_amdgcn_raw_buffer_store_b128(ov, orsrc, o + 64 * k, 0, 0);
                }
                SB();
            }
        }
    }
}

extern "C" void kernel_launch(void* const* d_in, const int* in_sizes, int n_in,
                              void* d_out, int out_size, void* d_ws, size_t ws_size,
                              hipStream_t stream) {
    const unsigned short* x = (const unsigned short*)d_in[0];
    const unsigned short* dw = (const unsigned short*)d_in[1];
    const unsigned short* wqkv = (const unsigned short*)d_in[2];
    const unsigned short* wo = (const unsigned short*)d_in[3];
    unsigned short* out = (unsigned short*)d_out;
    u32x4* wf = (u32x4*)d_ws;
    prep_weights<<<65, 384, 0, stream>>>(wqkv, wo, dw, wf);
    win_attn_kernel<<<GRID, 512, 0, stream>>>(x, wf, out);
}
```

```cpp
#include <hip/hip_runtime.h>
#include <hip/hip_bf16.h>
#include <stdint.h>

typedef short bf16x8 __attribute__((ext_vector_type(8)));
typedef float f32x4 __attribute__((ext_vector_type(4)));
typedef float f32x2 __attribute__((ext_vector_type(2)));
typedef unsigned u32x2 __attribute__((ext_vector_type(2)));
typedef unsigned u32x4 __attribute__((ext_vector_type(4)));

constexpr int IMG = 511;
constexpr int CH = 192;
constexpr int NWX = 73;
constexpr int NWIN = NWX * NWX;
constexpr int GRID = 256;
constexpr int ROWBYTES = IMG * CH * 2;
constexpr int XBIAS = (IMG + 1) * CH * 2;
constexpr unsigned OOB = 0x40000000u;

constexpr int FLAG_OFF = 7856;
constexpr int X_OFF = 32768;
constexpr int X1_OFF = 53632;
constexpr int O_OFF = 74496;
constexpr int V_OFF = 92928;
constexpr int KQL_OFF = 111360;
constexpr int OL_OFF = 128256;
constexpr int AT_OFF = 137472;
constexpr int CT_OFF = AT_OFF + 19584;
constexpr int RS_OFF = CT_OFF + 4096;
constexpr int WL_OFF = RS_OFF + 2304 + 64;
constexpr int LDS_BYTES = WL_OFF + 128;
#define RS_BASE (RS_OFF + (wave < 4 ? wave * 384 : 768 + wave * 192))
constexpr int STL_OFF = O_OFF + 12288;
constexpr int DW_OFF = O_OFF + 8448;
constexpr int PF_OFF = CT_OFF + 2048;
constexpr int PSLEEP = 4;
#define LB_OFF (w < 3 ? V_OFF + w * 13952 : X1_OFF)
#define LPOS(wc) (((wc) & 1) ? 0 : 6)
constexpr unsigned CROW = 0x060a0230u;
constexpr unsigned CLANE = 0x00132C3Fu;

constexpr unsigned long long TOKMAP[6] = {0x20001c190f2a2202ull, 0x052517121f061a13ull, 0x181e08151d0b260cull, 0x1b2b2903102d0901ull, 0x04162e0e2824142cull, 0x110a072127232f0dull};
constexpr unsigned long long ROWMAP[8] = {0x08321b240f1c0006ull, 0x23310e2a09170d14ull, 0x210c2f030a273418ull, 0x1d2b11221f043739ull, 0x3234363a103e192dull, 0x291a20022c3f1605ull, 0x303a3c2628122e15ull, 0x39253d1e01070b13ull};

constexpr int WF_BYTES = 8 * 36 * 1024;
constexpr int FLG_OFF = WF_BYTES;
constexpr int ARR_OFF = FLG_OFF + 512;
constexpr int EPO_OFF = ARR_OFF + 8192;
constexpr int WS_BYTES = EPO_OFF + 128;
constexpr int SC1 = 16;

#define MFMA32(a, b, c) __builtin_amdgcn_mfma_f32_16x16x32_bf16((a), (b), (c), 0, 0, 0)
#define BC8(v) __builtin_bit_cast(bf16x8, (v))
#define BAR() asm volatile("s_waitcnt lgkmcnt(0)\n\ts_barrier" ::: "memory")
#define SB() __builtin_amdgcn_sched_barrier(0)
#define OPQ(x) asm volatile("" : "+v"(x))
#define ZERO4 ((f32x4){0.f, 0.f, 0.f, 0.f})

typedef __bf16 nbf16x2 __attribute__((ext_vector_type(2)));
__device__ __forceinline__ unsigned pk2(float a, float b) {
    f32x2 v = {a, b};
    nbf16x2 r = __builtin_convertvector(v, nbf16x2);
    return __builtin_bit_cast(unsigned, r);
}
__device__ __forceinline__ float bflo(unsigned u) { return __uint_as_float(u << 16); }
__device__ __forceinline__ float bfhi(unsigned u) { return __uint_as_float(u & 0xffff0000u); }

__device__ __forceinline__ float bf2f(unsigned short v) { return __uint_as_float((unsigned)v << 16); }
__device__ __forceinline__ unsigned byte_of(unsigned long long a, unsigned long long b, int i) {
    return (unsigned)(((i & 8) ? b : a) >> (8 * (i & 7))) & 0xFFu;
}
#define M24(a, b) ((unsigned)__mul24((int)(a), (int)(b)))
__device__ __forceinline__ void seq_to_win(int seq, int& wy, int& wx) {
    const bool last = seq >= 5110;
    const int s = last ? 35 : M24(seq, 28729) >> 22, r = seq - M24(146, s);
    const int kx = last ? M24(r, 171) >> 10 : r >> 2, pos = r - M24(last ? 6 : 4, kx);
    const bool low = last && pos >= (kx == 36 ? 2 : 4);
    wy = low ? 72 : 2 * s + (pos & 1);
    wx = 2 * kx + (low ? (kx == 36 ? 0 : pos - 4) : pos >> 1);
}
__device__ __forceinline__ int win_to_seq(int wy, int wx) {
    const int kx = wx >> 1;
    return M24(146, wy == 72 ? 35 : wy >> 1) + M24(wy >= 70 ? 6 : 4, kx) + (wy == 72 ? (kx == 36 ? 2 : 4 + (wx & 1)) : 2 * (wx & 1) + (wy & 1));
}
__device__ __forceinline__ int block_of(int wy, int wx) { return wy >= 70 ? 1295 + (wx & ~1) + (wy == 72) : M24(37, wy >> 1) + (wx >> 1); }
__device__ __forceinline__ void block_pos(int b, int& ky, int& kx) {
    const int s = M24(b, 1772) >> 16, l = b - 1295;
    kx = l >= 0 ? l >> 1 : b - M24(37, s);
    ky = l >= 0 ? 70 + 2 * (l & 1) : 2 * s;
}
__device__ __forceinline__ unsigned win_org(int wy, int wx) { return M24(wy, 7 * ROWBYTES) + M24(wx, 7 * CH * 2); }
__device__ __forceinline__ unsigned wl_entry(int wy, int wx) {
    return win_org(wy, wx) | (wy == 0 ? 0x08000000u : 0u) | (wx == 0 ? 0x10000000u : 0u) |
           (wx == NWX - 1 ? 0x20000000u : 0u) | ((unsigned)(wx & 1) << 30) | ((unsigned)(wy & 1) << 31);
}
__device__ __forceinline__ unsigned dma_off(int lane) {
    const int col = (lane * 43) >> 8;
    return (unsigned)(col * (CH * 2 - 96) + 16 * lane) | (col == 0 ? 0x10000000u : 0u) | (col == 8 ? 0x20000000u : 0u);
}

#if defined(__HIP_DEVICE_COMPILE__)
#define NOPK __attribute__((target("no-packed-fp32-ops")))
#else
#define NOPK
#endif
__global__ __launch_bounds__(512, 2) NOPK void win_attn_kernel(const unsigned short* __restrict__ x,
                                                          const unsigned short* __restrict__ dw, const unsigned short* __restrict__ wqkv,
                                                          const unsigned short* __restrict__ wo, u32x4* wf,
                                                          unsigned short* __restrict__ out) {
    __shared__ __attribute__((aligned(16))) char smem[LDS_BYTES];

    asm volatile("wk_code_begin:");
    const int tid = threadIdx.x;
    const int wave = __builtin_amdgcn_readfirstlane(tid >> 6);
    const int w = wave & 3;
    const unsigned ldsb = (unsigned)(size_t)smem;
    {
        unsigned long long pc;
        unsigned len, rel;
        asm volatile("s_getpc_b64 %0\n.Lwk_here%=:\n\ts_mov_b32 %1, wk_code_end-wk_code_begin\n\ts_mov_b32 %2, .Lwk_here%=-wk_code_begin" : "=s"(pc), "=s"(len), "=s"(rel));
        pc -= rel;
        const u32x4 cr = {(unsigned)pc, (unsigned)(pc >> 32) & 0xffffu, len, 0x00020000u};
        asm volatile("s_mov_b32 m0, %0\n\ts_nop 0\n\tbuffer_load_dwordx4 %1, %2, %3 offen lds"
                     :: "s"(ldsb + X1_OFF + 14336 + w * 1024), "v"((tid & 63) * 16), "s"(cr), "s"((wave * 32 + (blockIdx.x >> 3)) * 1024) : "m0", "memory");
    }
    asm volatile("" :: "s"(x), "s"(dw), "s"(wqkv), "s"(wo), "s"(wf));
    const auto wrsrc = __builtin_amdgcn_make_buffer_rsrc((void*)wf, 0, WS_BYTES, 0x00020000);
    unsigned tag_lo = 0, tag_hi = 0;
#define TAG_READY                                                                                  \
    {                                                                                              \
        unsigned long long tag_;                                                                   \
        asm volatile("s_load_dwordx2 %0, %1, %2\n\ts_waitcnt lgkmcnt(0)" : "=s"(tag_) : "s"(wf), "n"(EPO_OFF) : "memory"); \
        tag_lo = (unsigned)tag_ + 1u; tag_hi = (unsigned)(tag_ >> 32);                             \
    }
    if (blockIdx.x < 36) {
        const int lane = tid & 63, n = lane & 15, g = lane >> 4;
        const bool isO = wave >= 6;
        const int fr = isO ? 216 + 2 * blockIdx.x + (wave - 6) : 6 * blockIdx.x + wave;
        const int tile = fr / 6, ks = fr - 6 * tile, tw = tile / 6, mdt = tile - 6 * tw;
        const int col = isO ? 96 * (tw - 6) + 32 * (mdt >> 1) + 8 * (n >> 2) + 4 * (mdt & 1) + (n & 3)
                            : (tw < 4 ? (mdt < 3 ? 192 : -48) + 48 * tw : 96 * tw) + 16 * mdt + n;
        const int rb = isO ? 384 : 1152;
        const auto rr = __builtin_amdgcn_make_buffer_rsrc((void*)(isO ? wo : wqkv), 0, 192 * rb, 0x00020000);
        const int vo = (48 * (g >> 1) + 8 * (g & 1)) * rb + 2 * col;
        float v[8];
#pragma unroll
        for (int j = 0; j < 8; ++j) v[j] = bf2f(__builtin_amdgcn_raw_buffer_load_b16(rr, vo, (96 * (ks & 1) + 16 * (ks >> 1) + j) * rb, 0));
        TAG_READY
        float* red = (float*)smem;
        red[n * 36 + wave * 4 + g] = ((v[0] + v[1]) + (v[2] + v[3])) + ((v[4] + v[5]) + (v[6] + v[7]));
        BAR();
        float mean = 0.f;
#pragma unroll
        for (int i = 0; i < 6; ++i) {
            const f32x4 p = *(const f32x4*)(red + n * 36 + 4 * i);
            mean += (p[0] + p[1]) + (p[2] + p[3]);
        }
        mean = isO ? 0.f : mean * (1.f / 192.f);
        u32x4 o;
#pragma unroll
        for (int j = 0; j < 4; ++j) o[j] = pk2(v[2 * j] - mean, v[2 * j + 1] - mean);
        __builtin_amdgcn_raw_buffer_store_b128(o, wrsrc, lane * 16, fr * 1024, SC1);
        asm volatile("s_waitcnt vmcnt(0)" ::: "memory");
        BAR();
        if (tid == 0) __builtin_amdgcn_raw_buffer_store_b64((u32x2){tag_lo, tag_hi}, wrsrc, FLG_OFF + blockIdx.x * 8, 0, SC1);
    }
    const int xi = blockIdx.x >> 3, nshort = (blockIdx.x & 7) == 7 ? 5 : 6;
    const int start = 666 * (blockIdx.x & 7) + 21 * xi - (xi < nshort ? xi : nshort);
    const int cnt = xi < nshort ? 20 : 21;

    const auto xrsrc = __builtin_amdgcn_make_buffer_rsrc((void*)((const char*)x - XBIAS), 0, IMG * IMG * CH * 2 + XBIAS, 0x00020000);
    bf16x8 W[36];
#define POLL_ISSUE                                                                                  \
    asm volatile("s_mov_b32 m0, %0\n\ts_mov_b64 exec, 0x3ffff\n\tbuffer_load_dwordx4 %1, %2, %3 offen sc1 lds\n\ts_mov_b64 exec, -1" \
                 :: "s"(ldsb + PF_OFF + w * 512), "v"(lane * 16), "s"(wrsrc), "s"(FLG_OFF) : "m0", "memory");
#define POLL_W                                                                                      \
    {                                                                                               \
        const int fi_ = lane < 36 ? lane : 0;                                                       \
        for (int it_ = 0; it_ < 100000; ++it_) {                                                    \
            u32x2 f_;                                                                               \
            if (it_ < 48) asm volatile("ds_read_b64 %0, %1\n\ts_waitcnt lgkmcnt(0)" : "=v"(f_) : "v"(ldsb + PF_OFF + w * 512 + fi_ * 8) : "memory"); \
            else f_ = __builtin_amdgcn_raw_buffer_load_b64(wrsrc, FLG_OFF + fi_ * 8, 0, SC1);       \
            if (__builtin_amdgcn_ballot_w64(f_[0] != tag_lo || f_[1] != tag_hi) == 0ull) break;     \
            if (it_ < 47) POLL_ISSUE                                                                \
            __builtin_amdgcn_s_sleep(PSLEEP);                                                       \
        }                                                                                           \
        __builtin_amdgcn_fence(__ATOMIC_ACQUIRE, "wavefront");                                      \
        SB();                                                                                       \
    }
#define LANE_SETUP                                                                                 \
    int lane;                                                                                      \
    asm volatile("v_mbcnt_lo_u32_b32 %0, -1, 0\n\tv_mbcnt_hi_u32_b32 %0, -1, %0" : "=v"(lane));    \
    __builtin_assume(lane >= 0 && lane < 64);                                                      \
    const int g = lane >> 4, l15 = lane & 15, sw = l15 >> 1;                                       \
    int fE = X_OFF + l15 * 384 + ((g ^ sw) << 4);                                                  \
    OPQ(fE);

#define DMA_SLICE(en)                                                                              \
        {                                                                                          \
            const unsigned vo_ = dma_off(lane) & (0x401fffffu | ((en) & 0x38000000u));             \
            const unsigned so_ = ((en) & 0x7ffffffu) + 96 * w;                                     \
            _Pragma("unroll") for (int r = 0; r < 9; ++r) {                                        \
                unsigned m0_ = ldsb + w * 8192 + r * 872;                                          \
                unsigned sr_ = (r == 0 && ((en) & 0x08000000u)) ? OOB : so_ + r * ROWBYTES;        \
                asm volatile("s_mov_b32 m0, %0\n\ts_mov_b32 exec_hi, 0x3fffff\n\tbuffer_load_dwordx4 %1, %2, %3 offen nt lds\n\ts_mov_b32 exec_hi, -1" \
                             :: "s"(m0_), "v"(vo_), "s"(xrsrc), "s"(sr_) : "m0", "memory");        \
            }                                                                                      \
        }
#define XPAT(r) (((r) < 48 ? (r) : 48) * 384 + (((2 * w) ^ (((r) >> 1) & 7)) << 4))
#define SPAT(r) ((w >> 1) * 1024 + (w & 1) * 8 + (r) * 16)
#define CONV_CONSTS(var) \
            const unsigned long long rlo = g == 0 ? ROWMAP[0] : (g == 1 ? ROWMAP[2] : (g == 2 ? ROWMAP[4] : ROWMAP[6])); \
            const unsigned long long rhi = g == 0 ? ROWMAP[1] : (g == 1 ? ROWMAP[3] : (g == 2 ? ROWMAP[5] : ROWMAP[7])); \
            int xrow = byte_of(rlo, rhi, l15); \
            if (lane == (int)((CLANE >> (8 * (var))) & 0xFF)) xrow = 48; \
            if (lane == 63) xrow = (CROW >> (8 * (var))) & 0xFF; \
            const unsigned pXw_ = XPAT(xrow); \
            const unsigned pSTw_ = (w >> 1) * 1024 + (w & 1) * 8 + xrow * 16; \
            const int oy = (0x5320 >> (4 * (l15 >> 2))) & 15, ox = (0x5320 >> (4 * (l15 & 3))) & 15; \
            const unsigned aB_ = ldsb + w * 8192 + (oy + (g >> 1)) * 872 + (ox + 2 * (g & 1)) * 96; \
            const int dyi = g - (l15 >> 3); \
            const unsigned aA_ = ldsb + AT_OFF + (l15 & 3) * 16 + \
                                 ((dyi < 0 || dyi > 2) ? 18816 : (dyi == 0 ? 0 : (dyi == 1 ? 6208 : 12544)) + ((l15 >> 2) & 1) * 3072 + w * 768); \
            const unsigned idx_ = (l15 & 3) * 0x5555u; \
            const unsigned se = (l15 == 4 * g) ? 0x3F803F80u : 0u; \
            const u32x4 sel4 = {se, se, se, se};

    unsigned en0, en1;
    {
        LANE_SETUP
        const int px = (lane * 43) >> 8;

        {
            const auto dr = __builtin_amdgcn_make_buffer_rsrc((void*)dw, 0, 9 * 384, 0x00020000);
            unsigned m0_ = ldsb + DW_OFF + w * 864, vo_ = px * 288 + lane * 16;
            asm volatile("s_mov_b32 m0, %0\n\ts_mov_b32 exec_hi, 0x3fffff\n\tbuffer_load_dwordx4 %1, %2, %3 offen lds\n\ts_mov_b32 exec_hi, -1"
                         :: "s"(m0_), "v"(vo_), "s"(dr), "s"(96 * w) : "m0", "memory");
        }
        int wy, wx;
        seq_to_win(start + (lane < cnt ? lane : cnt - 1), wy, wx);
        const unsigned en = wl_entry(wy, wx);
        const int bk = block_of(wy, wx);
        en0 = __builtin_amdgcn_readlane(en, 0);
        en1 = __builtin_amdgcn_readlane(en, 1);
        const int key0 = __builtin_amdgcn_readlane(bk, 0), nblk = __builtin_amdgcn_readlane(bk, 63) - key0 + 1;
        int ky, kx;
        block_pos(key0 + (l15 < nblk ? l15 : 0), ky, kx);
        if (wave < 4) {
            DMA_SLICE(en0)
        } else {
            const unsigned org = l15 < nblk ? (win_org(ky, 2 * kx) + (5 * ROWBYTES + 5 * CH * 2 + XBIAS)) | (kx == 36 ? OOB : 0u) : OOB + 0x8000000u;
            const unsigned lo = (px & 3) * (CH * 2) + (lane - 6 * px) * 16, keep = (px & 2) ? ~0u : ~OOB;
#pragma unroll
            for (int p = 0; p < 4; ++p) {
                if (p < 3 || nblk > 6) {
                    const unsigned oa = __builtin_amdgcn_readlane(org, 2 * p), ob = __builtin_amdgcn_readlane(org, 2 * p + 1);
                    const unsigned vo_ = ((lane < 24 ? oa : ob) & keep) + lo;
#pragma unroll
                    for (int r = 0; r < 4; ++r) {
                        unsigned m0_ = ldsb + LB_OFF + (4 * p + r) * 872;
                        asm volatile("s_mov_b32 m0, %0\n\ts_mov_b32 exec_hi, 0xffff\n\tbuffer_load_dwordx4 %1, %2, %3 offen nt lds\n\ts_mov_b32 exec_hi, -1"
                                     :: "s"(m0_), "v"(vo_), "s"(xrsrc), "s"(96 * w + r * ROWBYTES) : "m0", "memory");
                    }
                }
            }
        }
        SB();
        if (wave == 2) {
            u32x4 so;
#pragma unroll
            for (int e = 0; e < 3; ++e) {
                const int tk = byte_of(TOKMAP[2 * e], TOKMAP[2 * e + 1], l15);
                const int ty = (tk * 37) >> 8, tx = tk - 7 * ty;
                so[e] = (unsigned)(ty * ROWBYTES + tx * (CH * 2)) + 16 * g;
            }
            so[3] = 0u;
            *(u32x4*)(smem + CT_OFF + lane * 16) = so;
            *(unsigned*)(smem + CT_OFF + 1024 + lane * 16) = dma_off(lane);
        }
        if (wave == 0 && lane < 32) *(unsigned*)(smem + WL_OFF + lane * 4) = lane < cnt ? en : 0x7ffffffu;
        if (wave == 1 && lane == 0) *(unsigned*)(smem + FLAG_OFF) = 0u;
        if (lane < 48) *(u32x4*)(smem + AT_OFF + 18816 + lane * 16) = (u32x4){0u, 0u, 0u, 0u};
        CONV_CONSTS(en0 >> 30)
        const int ly = ky + (g >> 1), lx = 2 * kx + (g & 1), li = win_to_seq(ly, lx) - start;
        const bool ok = ly < NWX && lx < NWX && li >= 0 && li < cnt && l15 < nblk;
        const int row = ok ? li : 47, b = l15 < nblk ? l15 : 0;
        const unsigned cB = wave < 4 ? aB_ : ldsb + LB_OFF + (4 * (b >> 1) + (g >> 1)) * 872 + (b & 1) * 384 + (g & 1) * 192;
        const unsigned cX = wave < 4 ? pXw_ : XPAT(row), cS = wave < 4 ? pSTw_ : SPAT(row) + (unsigned)(STL_OFF - O_OFF - 18816);
        if (wave < 4) {
            TAG_READY
            *(u32x2*)(smem + PF_OFF + w * 512 + lane * 8) = (u32x2){~tag_lo, 0u};
        }
        asm volatile("s_waitcnt vmcnt(9)" ::: "memory");
        if (lane < 48) {
            unsigned short dv[9];
#pragma unroll
            for (int i = 0; i < 9; ++i) dv[i] = *(const unsigned short*)(smem + DW_OFF + w * 864 + i * 96 + lane * 2);
#pragma unroll
            for (int dy = 0; dy < 3; ++dy) {
                const unsigned a = dv[3 * dy], b2 = dv[3 * dy + 1] | (dy == 1 ? 0x3F800000u : 0u), c = dv[3 * dy + 2];
                char* p = smem + AT_OFF + (dy == 0 ? 0 : (dy == 1 ? 6208 : 12544)) + w * 768 + lane * 16;
                *(u32x4*)p = (u32x4){a, b2, c, 0u};
                *(u32x4*)(p + 3072) = (u32x4){0u, a, b2, c};
            }
        }
        asm volatile("s_waitcnt vmcnt(0) lgkmcnt(0)" ::: "memory");
        if (wave < 4) POLL_ISSUE
        asm volatile(
            "s_nop 7\n\t"
            "ds_read_b128 v[168:171], v254\n\t"
            "ds_read_b64 v[144:145], v253\n\t"
            "ds_read_b64 v[146:147], v253 offset:96\n\t"
            "ds_read_b64 v[148:149], v253 offset:1744\n\t"
            "ds_read_b64 v[150:151], v253 offset:1840\n\t"
            "ds_read_b128 v[172:175], v254 offset:64\n\t"
            "ds_read_b64 v[152:153], v253 offset:8\n\t"
            "ds_read_b64 v[154:155], v253 offset:104\n\t"
            "ds_read_b64 v[156:157], v253 offset:1752\n\t"
            "ds_read_b64 v[158:159], v253 offset:1848\n\t"
            "ds_read_b64 v[160:161], v253 offset:16\n\t"
            "ds_read_b64 v[162:163], v253 offset:112\n\t"
            "ds_read_b64 v[164:165], v253 offset:1760\n\t"
            "ds_read_b64 v[166:167], v253 offset:1856\n\t"
            "v_mov_b64 v[176:177], 0\n\t"
            "v_mov_b64 v[178:179], 0\n\t"
            "v_mov_b64 v[180:181], 0\n\t"
            "v_mov_b64 v[182:183], 0\n\t"
            "v_mov_b64 v[184:185], 0\n\t"
            "v_mov_b64 v[186:187], 0\n\t"
            "v_mov_b64 v[188:189], 0\n\t"
            "v_mov_b64 v[190:191], 0\n\t"
            "v_add_u32 v236, s67, v250\n\t"
            "v_xor_b32 v237, 16, v236\n\t"
            "v_add_u32 v248, s67, v251\n\t"
            "s_waitcnt lgkmcnt(9)\n\t"
            "v_smfmac_f32_16x16x64_bf16 v[176:179], v[168:171], v[144:151], v255\n\t"
            "ds_read_b128 v[168:171], v254 offset:128\n\t"
            "ds_read_b64 v[144:145], v253 offset:24\n\t"
            "s_waitcnt lgkmcnt(6)\n\t"
            "v_smfmac_f32_16x16x64_bf16 v[180:183], v[172:175], v[152:159], v255\n\t"
            "ds_read_b64 v[146:147], v253 offset:120\n\t"
            "ds_read_b64 v[148:149], v253 offset:1768\n\t"
            "ds_read_b64 v[150:151], v253 offset:1864\n\t"
            "ds_read_b128 v[172:175], v254 offset:192\n\t"
            "ds_read_b64 v[152:153], v253 offset:32\n\t"
            "ds_read_b64 v[154:155], v253 offset:128\n\t"
            "ds_read_b64 v[156:157], v253 offset:1776\n\t"
            "ds_read_b64 v[158:159], v253 offset:1872\n\t"
            "v_cvt_pk_bf16_f32 v176, v176, v177\n\t"
            "v_cvt_pk_bf16_f32 v177, v178, v179\n\t"
            "v_cvt_pk_bf16_f32 v178, v180, v181\n\t"
            "v_cvt_pk_bf16_f32 v179, v182, v183\n\t"
            "v_mfma_f32_4x4x4_16b_bf16 v[196:199], v[176:177], v[176:177], 0\n\t"
            "ds_write_b128 v236, v[176:179]\n\t"
            "v_mfma_f32_16x16x32_bf16 v[192:195], v[238:241], v[176:179], 0\n\t"
            "v_mov_b64 v[176:177], 0\n\t"
            "v_mov_b64 v[180:181], 0\n\t"
            "v_mfma_f32_4x4x4_16b_bf16 v[196:199], v[178:179], v[178:179], v[196:199]\n\t"
            "v_mov_b64 v[178:179], 0\n\t"
            "v_mov_b64 v[182:183], 0\n\t"
            "s_waitcnt lgkmcnt(10)\n\t"
            "v_smfmac_f32_16x16x64_bf16 v[184:187], v[168:171], v[160:167], v255\n\t"
            "ds_read_b128 v[168:171], v254 offset:256\n\t"
            "ds_read_b64 v[160:161], v253 offset:40\n\t"
            "ds_read_b64 v[162:163], v253 offset:136\n\t"
            "ds_read_b64 v[164:165], v253 offset:1784\n\t"
            "ds_read_b64 v[166:167], v253 offset:1880\n\t"
            "s_waitcnt lgkmcnt(10)\n\t"
            "v_smfmac_f32_16x16x64_bf16 v[188:191], v[172:175], v[144:151], v255\n\t"
            "ds_read_b128 v[172:175], v254 offset:320\n\t"
            "ds_read_b64 v[144:145], v253 offset:48\n\t"
            "ds_read_b64 v[146:147], v253 offset:144\n\t"
            "ds_read_b64 v[148:149], v253 offset:1792\n\t"
            "ds_read_b64 v[150:151], v253 offset:1888\n\t"
            "v_cvt_pk_bf16_f32 v184, v184, v185\n\t"
            "v_cvt_pk_bf16_f32 v185, v186, v187\n\t"
            "s_nop 0\n\t"
            "v_cvt_pk_bf16_f32 v186, v188, v189\n\t"
            "v_cvt_pk_bf16_f32 v187, v190, v191\n\t"
            "v_mfma_f32_4x4x4_16b_bf16 v[196:199], v[184:185], v[184:185], v[196:199]\n\t"
            "s_waitcnt lgkmcnt(14)\n\t"
            "ds_write_b128 v237, v[184:187]\n\t"
            "v_mfma_f32_16x16x32_bf16 v[192:195], v[238:241], v[184:187], v[192:195]\n\t"
            "v_mov_b64 v[184:185], 0\n\t"
            "v_mov_b64 v[188:189], 0\n\t"
            "v_mfma_f32_4x4x4_16b_bf16 v[196:199], v[186:187], v[186:187], v[196:199]\n\t"
            "v_mov_b64 v[186:187], 0\n\t"
            "v_mov_b64 v[190:191], 0\n\t"
            "s_waitcnt lgkmcnt(10)\n\t"
            "v_smfmac_f32_16x16x64_bf16 v[176:179], v[168:171], v[152:159], v255\n\t"
            "ds_read_b128 v[168:171], v254 offset:384\n\t"
            "ds_read_b64 v[152:153], v253 offset:56\n\t"
            "ds_read_b64 v[154:155], v253 offset:152\n\t"
            "ds_read_b64 v[156:157], v253 offset:1800\n\t"
            "ds_read_b64 v[158:159], v253 offset:1896\n\t"
            "s_waitcnt lgkmcnt(10)\n\t"
            "v_smfmac_f32_16x16x64_bf16 v[180:183], v[172:175], v[160:167], v255\n\t"
            "ds_read_b128 v[172:175], v254 offset:448\n\t"
            "ds_read_b64 v[160:161], v253 offset:64\n\t"
            "ds_read_b64 v[162:163], v253 offset:160\n\t"
            "ds_read_b64 v[164:165], v253 offset:1808\n\t"
            "ds_read_b64 v[166:167], v253 offset:1904\n\t"
            "v_cvt_pk_bf16_f32 v176, v176, v177\n\t"
            "v_cvt_pk_bf16_f32 v177, v178, v179\n\t"
            "s_nop 0\n\t"
            "v_cvt_pk_bf16_f32 v178, v180, v181\n\t"
            "v_cvt_pk_bf16_f32 v179, v182, v183\n\t"
            "v_mfma_f32_4x4x4_16b_bf16 v[196:199], v[176:177], v[176:177], v[196:199]\n\t"
            "s_waitcnt lgkmcnt(14)\n\t"
            "ds_write_b128 v236, v[176:179] offset:128\n\t"
            "v_mfma_f32_16x16x32_bf16 v[192:195], v[238:241], v[176:179], v[192:195]\n\t"
            "v_mov_b64 v[176:177], 0\n\t"
            "v_mov_b64 v[180:181], 0\n\t"
            "v_mfma_f32_4x4x4_16b_bf16 v[196:199], v[178:179], v[178:179], v[196:199]\n\t"
            "v_mov_b64 v[178:179], 0\n\t"
            "v_mov_b64 v[182:183], 0\n\t"
            "s_waitcnt lgkmcnt(10)\n\t"
            "v_smfmac_f32_16x16x64_bf16 v[184:187], v[168:171], v[144:151], v255\n\t"
            "ds_read_b128 v[168:171], v254 offset:512\n\t"
            "ds_read_b64 v[144:145], v253 offset:72\n\t"
            "ds_read_b64 v[146:147], v253 offset:168\n\t"
            "ds_read_b64 v[148:149], v253 offset:1816\n\t"
            "ds_read_b64 v[150:151], v253 offset:1912\n\t"
            "s_waitcnt lgkmcnt(10)\n\t"
            "v_smfmac_f32_16x16x64_bf16 v[188:191], v[172:175], v[152:159], v255\n\t"
            "ds_read_b128 v[172:175], v254 offset:576\n\t"
            "ds_read_b64 v[152:153], v253 offset:80\n\t"
            "ds_read_b64 v[154:155], v253 offset:176\n\t"
            "ds_read_b64 v[156:157], v253 offset:1824\n\t"
            "ds_read_b64 v[158:159], v253 offset:1920\n\t"
            "v_cvt_pk_bf16_f32 v184, v184, v185\n\t"
            "v_cvt_pk_bf16_f32 v185, v186, v187\n\t"
            "s_nop 0\n\t"
            "v_cvt_pk_bf16_f32 v186, v188, v189\n\t"
            "v_cvt_pk_bf16_f32 v187, v190, v191\n\t"
            "v_mfma_f32_4x4x4_16b_bf16 v[196:199], v[184:185], v[184:185], v[196:199]\n\t"
            "s_waitcnt lgkmcnt(14)\n\t"
            "ds_write_b128 v237, v[184:187] offset:128\n\t"
            "v_mfma_f32_16x16x32_bf16 v[192:195], v[238:241], v[184:187], v[192:195]\n\t"
            "v_mov_b64 v[184:185], 0\n\t"
            "v_mov_b64 v[188:189], 0\n\t"
            "v_mfma_f32_4x4x4_16b_bf16 v[196:199], v[186:187], v[186:187], v[196:199]\n\t"
            "v_mov_b64 v[186:187], 0\n\t"
            "v_mov_b64 v[190:191], 0\n\t"
            "s_waitcnt lgkmcnt(10)\n\t"
            "v_smfmac_f32_16x16x64_bf16 v[176:179], v[168:171], v[160:167], v255\n\t"
            "ds_read_b128 v[168:171], v254 offset:640\n\t"
            "ds_read_b64 v[160:161], v253 offset:88\n\t"
            "ds_read_b64 v[162:163], v253 offset:184\n\t"
            "ds_read_b64 v[164:165], v253 offset:1832\n\t"
            "ds_read_b64 v[166:167], v253 offset:1928\n\t"
            "s_waitcnt lgkmcnt(10)\n\t"
            "v_smfmac_f32_16x16x64_bf16 v[180:183], v[172:175], v[144:151], v255\n\t"
            "ds_read_b128 v[172:175], v254 offset:704\n\t"
            "v_cvt_pk_bf16_f32 v176, v176, v177\n\t"
            "v_cvt_pk_bf16_f32 v177, v178, v179\n\t"
            "s_nop 1\n\t"
            "v_mfma_f32_4x4x4_16b_bf16 v[196:199], v[176:177], v[176:177], v[196:199]\n\t"
            "s_nop 1\n\t"
            "v_cvt_pk_bf16_f32 v178, v180, v181\n\t"
            "v_cvt_pk_bf16_f32 v179, v182, v183\n\t"
            "s_nop 1\n\t"
            "v_mfma_f32_4x4x4_16b_bf16 v[196:199], v[178:179], v[178:179], v[196:199]\n\t"
            "ds_write_b128 v236, v[176:179] offset:256\n\t"
            "s_waitcnt lgkmcnt(6)\n\t"
            "v_smfmac_f32_16x16x64_bf16 v[184:187], v[168:171], v[152:159], v255\n\t"
            "v_mfma_f32_16x16x32_bf16 v[192:195], v[238:241], v[176:179], v[192:195]\n\t"
            "s_waitcnt lgkmcnt(1)\n\t"
            "v_smfmac_f32_16x16x64_bf16 v[188:191], v[172:175], v[160:167], v255\n\t"
            "s_nop 4\n\t"
            "v_cvt_pk_bf16_f32 v184, v184, v185\n\t"
            "v_cvt_pk_bf16_f32 v185, v186, v187\n\t"
            "s_nop 1\n\t"
            "v_mfma_f32_4x4x4_16b_bf16 v[196:199], v[184:185], v[184:185], v[196:199]\n\t"
            "v_cvt_pk_bf16_f32 v186, v188, v189\n\t"
            "v_cvt_pk_bf16_f32 v187, v190, v191\n\t"
            "s_nop 1\n\t"
            "v_mfma_f32_4x4x4_16b_bf16 v[196:199], v[186:187], v[186:187], v[196:199]\n\t"
            "ds_write_b128 v237, v[184:187] offset:256\n\t"
            "v_mfma_f32_16x16x32_bf16 v[192:195], v[238:241], v[184:187], v[192:195]\n\t"
            "s_nop 7\n\t"
            "v_cndmask_b32_e64 v193, v196, v197, s[60:61]\n\t"
            "v_cndmask_b32_e64 v193, v193, v198, s[62:63]\n\t"
            "v_cndmask_b32_e64 v193, v193, v199, s[64:65]\n\t"
            "ds_write_b64 v248, v[192:193] offset:18816\n\t"
            "s_waitcnt lgkmcnt(0)\n\t"
            "s_nop 7\n\t"
            :
            : "{v250}"(cX), "{v251}"(cS), "{v253}"(cB), "{v254}"(aA_), "{v255}"(idx_), "{v[238:241]}"(sel4), "{s67}"(ldsb + (wave < 4 ? X_OFF : O_OFF)),
              "{s[60:61]}"(0x2222222222222222ull), "{s[62:63]}"(0x4444444444444444ull), "{s[64:65]}"(0x8888888888888888ull)
            : "v144", "v145", "v146", "v147", "v148", "v149", "v150", "v151", "v152", "v153", "v154", "v155", "v156", "v157", "v158", "v159", "v160", "v161", "v162", "v163", "v164", "v165", "v166", "v167", "v168", "v169", "v170", "v171", "v172", "v173", "v174", "v175", "v176", "v177", "v178", "v179", "v180", "v181", "v182", "v183", "v184", "v185", "v186", "v187", "v188", "v189", "v190", "v191", "v192", "v193", "v194", "v195", "v196", "v197", "v198", "v199", "v236", "v237", "v248", "v249", "memory");
        if (wave < 4) {
            if (lane == 0) __builtin_amdgcn_raw_buffer_store_b64((u32x2){tag_lo, tag_hi}, wrsrc, ARR_OFF + (blockIdx.x * 4 + w) * 8, 0, SC1);
            POLL_W
        }
    }
    const unsigned long long mrow = 0x0001000100010001ull * ((1u << (cnt - 16)) - 1u);
#define KARGS                                                                                      \
    unsigned long long xb, wb, ob;                                                                 \
    asm volatile("s_load_dwordx2 %0, %3, 0x0\n\ts_load_dwordx2 %1, %3, 0x20\n\ts_load_dwordx2 %2, %3, 0x28\n\ts_waitcnt lgkmcnt(0)" \
                 : "=&s"(xb), "=&s"(wb), "=&s"(ob) : "s"(__builtin_amdgcn_kernarg_segment_ptr()) : "memory"); \
    xb -= XBIAS;                                                                                   \
    const u32x4 wr = {(unsigned)wb, (unsigned)(wb >> 32) & 0xffffu, (unsigned)WS_BYTES, 0x00020000u}; \
    const u32x4 orr = {(unsigned)ob, (unsigned)(ob >> 32) & 0xffffu, (unsigned)(IMG * IMG * CH * 2), 0x00020000u}; \
    const u32x4 xr = {(unsigned)xb, (unsigned)(xb >> 32) & 0xffffu, (unsigned)(IMG * IMG * CH * 2 + XBIAS), 0x00020000u};
    const int tk_ = (xi + 4) / 6;
    const unsigned touch = wave * 36864 + ((xi + 4 - 6 * tk_) * 6 + tk_) * 1024;
#define B_SCALARS \
            CONV_CONSTS(0) \
            const unsigned aCA_ = ldsb + CT_OFF + lane * 16; \
            KARGS \
            unsigned s45 = OOB, s46 = (en0 & 0x7ffffffu) + 96 * w, s47 = (en1 & 0x7ffffffu) + 96 * w; \
            unsigned s48 = ldsb + X_OFF, s67 = ldsb + X1_OFF, s76 = ldsb + WL_OFF + 8, s70 = 0; \
            unsigned s56 = __builtin_amdgcn_readfirstlane((en0 >> 30) * 4 + (en1 >> 30)); \
            unsigned s69 = 0x401fffffu | (en1 & 0x38000000u), s79 = (en1 & 0x08000000u) ? OOB : s47; \
            unsigned long long xp = 0, sp = 0; \
            _Pragma("unroll") for (int v = 0; v < 4; ++v) { \
                const unsigned r = (CROW >> (8 * v)) & 0xFF; \
                xp |= (unsigned long long)XPAT(r) << (16 * v); \
                sp |= (unsigned long long)SPAT(r) << (16 * v); \
            }
#define B_SIO "+{s45}"(s45), "+{s46}"(s46), "+{s47}"(s47), "+{s48}"(s48), "+{s67}"(s67), "+{s76}"(s76), "+{s56}"(s56), "+{s70}"(s70), "+{s69}"(s69), "+{s79}"(s79)
#define B_SIN "{v[238:241]}"(sel4), "{s[40:43]}"(orr), "{s[72:75]}"(xr), "{s39}"(0x401fffffu), "{s[28:31]}"(wr), "{s33}"(wave * 36864), "{s78}"(96 * w), \
              "{s27}"(96 * w - 384), "{s[50:51]}"(xp), "{s[52:53]}"(sp), "{s54}"(CLANE), "{s55}"(CROW & 0x0F0F0F0Fu), "{s[34:35]}"(0x0001000100010001ull), \
              "{s[60:61]}"(0x2222222222222222ull), "{s[62:63]}"(0x4444444444444444ull), "{s[64:65]}"(0x8888888888888888ull), \
              "{s66}"(ldsb + w * 8192), "{s71}"((ldsb + X_OFF) ^ (ldsb + X1_OFF))
#define B_SCLOB "s26", "s77", "s68", "s36", "s37", "s38", "s57", "s58", "s59", "s49", "vcc", "scc", "m0", "memory"

    if (wave < 4) {
        LANE_SETUP
        const int dW = X_OFF + l15 * 384 + (((2 * w + (g >> 1)) ^ sw) << 4) + 8 * (g & 1);
        const int vB = V_OFF + w * 4608 + 1024 + lane * 8, rsR = RS_BASE + l15 * 4;
        {
            const unsigned rsb = ldsb + RS_BASE;
            const unsigned pE_ = fE - X_OFF, aO_ = ldsb + dW + (O_OFF - X_OFF), sel_ = (l15 & 3) ? 0u : 1u, aVB_ = ldsb + vB;
            const unsigned pST_ = lane * 16, aRSw_ = rsb + lane * 4, aPLw_ = rsb + lane * 2, aRSr_ = ldsb + rsR;
            const unsigned g16_ = (l15 & 3) ? ldsb + AT_OFF + 18816 : g * 16, g8_ = (l15 & 3) ? ldsb + AT_OFF + 18816 : g * 8;
            const unsigned loVL_ = ((2 * w + (l15 >> 3)) << 4) + (l15 & 7) * 2, loOL_ = g * 128;
            const unsigned on = (l15 & 3) ? 0u : 0x3F803F80u;
            const u32x4 ones4 = {on, on, on, on};
            unsigned s44 = cnt, s48 = ldsb + X_OFF, s70 = 0;
            unsigned s72 = ldsb + KQL_OFF + w * 192, s73 = ldsb + OL_OFF, s74 = ldsb + KQL_OFF + 784, s77 = ldsb + OL_OFF, s76 = ldsb + KQL_OFF + 768 + w * 4;
            KARGS
            asm volatile(
            "s_nop 4\n\t"
            "buffer_load_dwordx4 v[140:143], v248, s[84:87], s88 offen sc1\n\t"
            "s_add_u32 s49, s46, 0\n\t"
            "buffer_load_dwordx4 v[0:3], v248, s[84:87], s49 offen sc1\n\t"
            "s_add_u32 s49, s46, 6144\n\t"
            "buffer_load_dwordx4 v[24:27], v248, s[84:87], s49 offen sc1\n\t"
            "s_add_u32 s49, s46, 12288\n\t"
            "buffer_load_dwordx4 v[48:51], v248, s[84:87], s49 offen sc1\n\t"
            "s_add_u32 s49, s46, 18432\n\t"
            "buffer_load_dwordx4 v[72:75], v248, s[84:87], s49 offen sc1\n\t"
            "s_add_u32 s49, s46, 24576\n\t"
            "buffer_load_dwordx4 v[96:99], v248, s[84:87], s49 offen sc1\n\t"
            "s_add_u32 s49, s46, 30720\n\t"
            "buffer_load_dwordx4 v[120:123], v248, s[84:87], s49 offen sc1\n\t"
            "s_add_u32 s49, s46, 1024\n\t"
            "buffer_load_dwordx4 v[4:7], v248, s[84:87], s49 offen sc1\n\t"
            "s_add_u32 s49, s46, 7168\n\t"
            "buffer_load_dwordx4 v[28:31], v248, s[84:87], s49 offen sc1\n\t"
            "s_add_u32 s49, s46, 13312\n\t"
            "buffer_load_dwordx4 v[52:55], v248, s[84:87], s49 offen sc1\n\t"
            "s_add_u32 s49, s46, 19456\n\t"
            "buffer_load_dwordx4 v[76:79], v248, s[84:87], s49 offen sc1\n\t"
            "s_add_u32 s49, s46, 25600\n\t"
            "buffer_load_dwordx4 v[100:103], v248, s[84:87], s49 offen sc1\n\t"
            "s_add_u32 s49, s46, 31744\n\t"
            "buffer_load_dwordx4 v[124:127], v248, s[84:87], s49 offen sc1\n\t"
            "s_add_u32 s49, s46, 2048\n\t"
            "buffer_load_dwordx4 v[8:11], v248, s[84:87], s49 offen sc1\n\t"
            "s_add_u32 s49, s46, 8192\n\t"
            "buffer_load_dwordx4 v[32:35], v248, s[84:87], s49 offen sc1\n\t"
            "s_add_u32 s49, s46, 14336\n\t"
            "buffer_load_dwordx4 v[56:59], v248, s[84:87], s49 offen sc1\n\t"
            "s_add_u32 s49, s46, 20480\n\t"
            "buffer_load_dwordx4 v[80:83], v248, s[84:87], s49 offen sc1\n\t"
            "s_add_u32 s49, s46, 26624\n\t"
            "buffer_load_dwordx4 v[104:107], v248, s[84:87], s49 offen sc1\n\t"
            "s_add_u32 s49, s46, 32768\n\t"
            "buffer_load_dwordx4 v[128:131], v248, s[84:87], s49 offen sc1\n\t"
            "s_add_u32 s49, s46, 3072\n\t"
            "buffer_load_dwordx4 v[12:15], v248, s[84:87], s49 offen sc1\n\t"
            "s_add_u32 s49, s46, 9216\n\t"
            "buffer_load_dwordx4 v[36:39], v248, s[84:87], s49 offen sc1\n\t"
            "s_add_u32 s49, s46, 15360\n\t"
            "buffer_load_dwordx4 v[60:63], v248, s[84:87], s49 offen sc1\n\t"
            "s_add_u32 s49, s46, 21504\n\t"
            "buffer_load_dwordx4 v[84:87], v248, s[84:87], s49 offen sc1\n\t"
            "s_add_u32 s49, s46, 27648\n\t"
            "buffer_load_dwordx4 v[108:111], v248, s[84:87], s49 offen sc1\n\t"
            "s_add_u32 s49, s46, 33792\n\t"
            "buffer_load_dwordx4 v[132:135], v248, s[84:87], s49 offen sc1\n\t"
            "s_add_u32 s49, s46, 4096\n\t"
            "buffer_load_dwordx4 v[16:19], v248, s[84:87], s49 offen sc1\n\t"
            "s_add_u32 s49, s46, 10240\n\t"
            "buffer_load_dwordx4 v[40:43], v248, s[84:87], s49 offen sc1\n\t"
            "s_add_u32 s49, s46, 16384\n\t"
            "buffer_load_dwordx4 v[64:67], v248, s[84:87], s49 offen sc1\n\t"
            "s_add_u32 s49, s46, 22528\n\t"
            "buffer_load_dwordx4 v[88:91], v248, s[84:87], s49 offen sc1\n\t"
            "s_add_u32 s49, s46, 28672\n\t"
            "buffer_load_dwordx4 v[112:115], v248, s[84:87], s49 offen sc1\n\t"
            "s_add_u32 s49, s46, 34816\n\t"
            "buffer_load_dwordx4 v[136:139], v248, s[84:87], s49 offen sc1\n\t"
            "s_add_u32 s49, s46, 5120\n\t"
            "buffer_load_dwordx4 v[20:23], v248, s[84:87], s49 offen sc1\n\t"
            "s_add_u32 s49, s46, 11264\n\t"
            "buffer_load_dwordx4 v[44:47], v248, s[84:87], s49 offen sc1\n\t"
            "s_add_u32 s49, s46, 17408\n\t"
            "buffer_load_dwordx4 v[68:71], v248, s[84:87], s49 offen sc1\n\t"
            "s_add_u32 s49, s46, 23552\n\t"
            "buffer_load_dwordx4 v[92:95], v248, s[84:87], s49 offen sc1\n\t"
            "s_add_u32 s49, s46, 29696\n\t"
            "buffer_load_dwordx4 v[116:119], v248, s[84:87], s49 offen sc1\n\t"
            "s_add_u32 s49, s46, 35840\n\t"
            "buffer_load_dwordx4 v[140:143], v248, s[84:87], s49 offen sc1\n\t"
            "s_waitcnt lgkmcnt(0)\n\t"
            "s_barrier\n\t"
            "s_setprio 2\n\t"
            "v_add_u32 v235, s71, v248\n\t"
            "v_add_u32 v232, s71, v244\n\t"
            "v_xor_b32 v233, 64, v232\n\t"
            "v_subrev_u32 v239, s75, v251\n\t"
            "v_mul_u32_u24 v239, 200, v239\n\t"
            "v_lshrrev_b32 v234, 3, v255\n\t"
            "v_add3_u32 v234, v234, v239, s72\n\t"
            "ds_read_b128 v[228:231], v235 offset:12288\n\t"
            "ds_read_b128 v[144:147], v232\n\t"
            "ds_read_b128 v[148:151], v233\n\t"
            "ds_read_b128 v[204:207], v232 offset:6144\n\t"
            "ds_read_b128 v[208:211], v233 offset:6144\n\t"
            "ds_read_b128 v[152:155], v232 offset:128\n\t"
            "ds_read_b128 v[156:159], v233 offset:128\n\t"
            "ds_read_b128 v[212:215], v232 offset:6272\n\t"
            "ds_read_b128 v[224:227], v233 offset:6272\n\t"
            "ds_read_b128 v[160:163], v232 offset:256\n\t"
            "ds_read_b128 v[164:167], v233 offset:256\n\t"
            "s_waitcnt vmcnt(24)\n\t"
            "s_waitcnt lgkmcnt(10)\n\t"
            "v_add_f32 v236, v228, v230\n\t"
            "v_add_f32 v237, v229, v231\n\t"
            "s_waitcnt lgkmcnt(8)\n\t"
            "v_mfma_f32_16x16x32_bf16 v[168:171], v[0:3], v[144:147], 0\n\t"
            "ds_read_b128 v[228:231], v235 offset:13312\n\t"
            "v_lshrrev_b32 v235, 4, v255\n\t"
            "v_mfma_f32_16x16x32_bf16 v[168:171], v[4:7], v[148:151], v[168:171]\n\t"
            "v_add3_u32 v235, v235, v239, s72\n\t"
            "v_mfma_f32_16x16x32_bf16 v[176:179], v[48:51], v[144:147], 0\n\t"
            "v_mfma_f32_16x16x32_bf16 v[176:179], v[52:55], v[148:151], v[176:179]\n\t"
            "v_mfma_f32_16x16x32_bf16 v[172:175], v[24:27], v[144:147], 0\n\t"
            "v_mfma_f32_16x16x32_bf16 v[172:175], v[28:31], v[148:151], v[172:175]\n\t"
            "v_mfma_f32_16x16x32_bf16 v[180:183], v[72:75], v[144:147], 0\n\t"
            "v_mfma_f32_16x16x32_bf16 v[180:183], v[76:79], v[148:151], v[180:183]\n\t"
            "v_mfma_f32_16x16x32_bf16 v[184:187], v[96:99], v[144:147], 0\n\t"
            "s_waitcnt lgkmcnt(0)\n\t"
            "v_add_f32 v228, v228, v230\n\t"
            "v_add_f32 v236, v236, v228\n\t"
            "v_mfma_f32_16x16x32_bf16 v[184:187], v[100:103], v[148:151], v[184:187]\n\t"
            "v_add_f32 v229, v229, v231\n\t"
            "v_mul_f32 v236, 0x3baaaaab, v236\n\t"
            "v_mfma_f32_16x16x32_bf16 v[188:191], v[120:123], v[144:147], 0\n\t"
            "v_add_f32 v237, v237, v229\n\t"
            "v_mul_f32_e64 v236, v236, -v236\n\t"
            "v_mfma_f32_16x16x32_bf16 v[188:191], v[124:127], v[148:151], v[188:191]\n\t"
            "v_fmac_f32 v236, 0x3baaaaab, v237\n\t"
            "v_max_f32 v236, 0, v236\n\t"
            "v_mfma_f32_16x16x32_bf16 v[192:195], v[0:3], v[204:207], 0\n\t"
            "v_add_f32 v236, 0x3727c5ac, v236\n\t"
            "v_mfma_f32_16x16x32_bf16 v[192:195], v[4:7], v[208:211], v[192:195]\n\t"
            "v_rsq_f32 v236, v236\n\t"
            "v_mfma_f32_16x16x32_bf16 v[196:199], v[24:27], v[204:207], 0\n\t"
            "v_mfma_f32_16x16x32_bf16 v[196:199], v[28:31], v[208:211], v[196:199]\n\t"
            "ds_write_b32 v249, v236\n\t"
            "v_mfma_f32_16x16x32_bf16 v[200:203], v[48:51], v[204:207], 0\n\t"
            "ds_read_b32 v236, v251\n\t"
            "ds_read_b32 v237, v251 offset:64\n\t"
            "v_mfma_f32_16x16x32_bf16 v[200:203], v[52:55], v[208:211], v[200:203]\n\t"
            "v_mfma_f32_16x16x32_bf16 v[216:219], v[72:75], v[204:207], 0\n\t"
            "v_mfma_f32_16x16x32_bf16 v[216:219], v[76:79], v[208:211], v[216:219]\n\t"
            "v_mfma_f32_16x16x32_bf16 v[220:223], v[96:99], v[204:207], 0\n\t"
            "v_mfma_f32_16x16x32_bf16 v[220:223], v[100:103], v[208:211], v[220:223]\n\t"
            "v_mfma_f32_16x16x32_bf16 v[228:231], v[120:123], v[204:207], 0\n\t"
            "ds_read_b128 v[204:207], v232 offset:6400\n\t"
            "v_add_u32 v232, s48, v244\n\t"
            "v_mfma_f32_16x16x32_bf16 v[228:231], v[124:127], v[208:211], v[228:231]\n\t"
            "ds_read_b128 v[208:211], v233 offset:6400\n\t"
            "s_waitcnt vmcnt(12)\n\t"
            "v_mfma_f32_16x16x32_bf16 v[168:171], v[8:11], v[152:155], v[168:171]\n\t"
            "v_xor_b32 v233, 64, v232\n\t"
            "v_mfma_f32_16x16x32_bf16 v[168:171], v[12:15], v[156:159], v[168:171]\n\t"
            "v_mfma_f32_16x16x32_bf16 v[176:179], v[56:59], v[152:155], v[176:179]\n\t"
            "v_mfma_f32_16x16x32_bf16 v[176:179], v[60:63], v[156:159], v[176:179]\n\t"
            "v_mfma_f32_16x16x32_bf16 v[172:175], v[32:35], v[152:155], v[172:175]\n\t"
            "v_mfma_f32_16x16x32_bf16 v[172:175], v[36:39], v[156:159], v[172:175]\n\t"
            "v_mfma_f32_16x16x32_bf16 v[180:183], v[80:83], v[152:155], v[180:183]\n\t"
            "v_mfma_f32_16x16x32_bf16 v[180:183], v[84:87], v[156:159], v[180:183]\n\t"
            "v_mfma_f32_16x16x32_bf16 v[184:187], v[104:107], v[152:155], v[184:187]\n\t"
            "v_mfma_f32_16x16x32_bf16 v[184:187], v[108:111], v[156:159], v[184:187]\n\t"
            "v_mfma_f32_16x16x32_bf16 v[188:191], v[128:131], v[152:155], v[188:191]\n\t"
            "v_mfma_f32_16x16x32_bf16 v[188:191], v[132:135], v[156:159], v[188:191]\n\t"
            "v_mfma_f32_16x16x32_bf16 v[192:195], v[8:11], v[212:215], v[192:195]\n\t"
            "v_mfma_f32_16x16x32_bf16 v[192:195], v[12:15], v[224:227], v[192:195]\n\t"
            "v_mfma_f32_16x16x32_bf16 v[196:199], v[32:35], v[212:215], v[196:199]\n\t"
            "v_mfma_f32_16x16x32_bf16 v[196:199], v[36:39], v[224:227], v[196:199]\n\t"
            "v_mfma_f32_16x16x32_bf16 v[200:203], v[56:59], v[212:215], v[200:203]\n\t"
            "v_mfma_f32_16x16x32_bf16 v[200:203], v[60:63], v[224:227], v[200:203]\n\t"
            "v_mfma_f32_16x16x32_bf16 v[216:219], v[80:83], v[212:215], v[216:219]\n\t"
            "v_mfma_f32_16x16x32_bf16 v[216:219], v[84:87], v[224:227], v[216:219]\n\t"
            "v_mfma_f32_16x16x32_bf16 v[220:223], v[104:107], v[212:215], v[220:223]\n\t"
            "v_mfma_f32_16x16x32_bf16 v[220:223], v[108:111], v[224:227], v[220:223]\n\t"
            "v_mfma_f32_16x16x32_bf16 v[228:231], v[128:131], v[212:215], v[228:231]\n\t"
            "ds_read_b128 v[212:215], v232 offset:256\n\t"
            "v_mfma_f32_16x16x32_bf16 v[228:231], v[132:135], v[224:227], v[228:231]\n\t"
            "ds_read_b128 v[224:227], v233 offset:256\n\t"
            "s_waitcnt vmcnt(0)\n\t"
            "v_mfma_f32_16x16x32_bf16 v[168:171], v[16:19], v[160:163], v[168:171]\n\t"
            "v_mfma_f32_16x16x32_bf16 v[168:171], v[20:23], v[164:167], v[168:171]\n\t"
            "v_mfma_f32_16x16x32_bf16 v[176:179], v[64:67], v[160:163], v[176:179]\n\t"
            "v_mfma_f32_16x16x32_bf16 v[176:179], v[68:71], v[164:167], v[176:179]\n\t"
            "v_mfma_f32_16x16x32_bf16 v[180:183], v[88:91], v[160:163], v[180:183]\n\t"
            "v_mfma_f32_16x16x32_bf16 v[180:183], v[92:95], v[164:167], v[180:183]\n\t"
            "v_mfma_f32_16x16x32_bf16 v[172:175], v[40:43], v[160:163], v[172:175]\n\t"
            "v_mfma_f32_16x16x32_bf16 v[172:175], v[44:47], v[164:167], v[172:175]\n\t"
            "s_waitcnt lgkmcnt(5)\n\t"
            "s_nop 0\n\t"
            "v_mul_f32 v168, v236, v168\n\t"
            "v_mfma_f32_16x16x32_bf16 v[184:187], v[112:115], v[160:163], v[184:187]\n\t"
            "v_mul_f32 v169, v236, v169\n\t"
            "v_mul_f32 v170, v236, v170\n\t"
            "v_mfma_f32_16x16x32_bf16 v[184:187], v[116:119], v[164:167], v[184:187]\n\t"
            "v_mul_f32 v176, v236, v176\n\t"
            "v_mul_f32 v177, v236, v177\n\t"
            "v_mfma_f32_16x16x32_bf16 v[188:191], v[136:139], v[160:163], v[188:191]\n\t"
            "v_mul_f32 v171, v236, v171\n\t"
            "v_mul_f32 v178, v236, v178\n\t"
            "v_mfma_f32_16x16x32_bf16 v[188:191], v[140:143], v[164:167], v[188:191]\n\t"
            "v_mul_f32 v179, v236, v179\n\t"
            "v_cvt_pk_bf16_f32 v168, v168, v169\n\t"
            "s_waitcnt lgkmcnt(2)\n\t"
            "v_mfma_f32_16x16x32_bf16 v[192:195], v[16:19], v[204:207], v[192:195]\n\t"
            "v_cvt_pk_bf16_f32 v176, v176, v177\n\t"
            "v_cvt_pk_bf16_f32 v180, v180, v181\n\t"
            "v_mfma_f32_16x16x32_bf16 v[192:195], v[20:23], v[208:211], v[192:195]\n\t"
            "v_mul_f32 v172, v236, v172\n\t"
            "v_mul_f32 v173, v236, v173\n\t"
            "v_mfma_f32_16x16x32_bf16 v[196:199], v[40:43], v[204:207], v[196:199]\n\t"
            "v_mul_f32 v174, v236, v174\n\t"
            "v_mul_f32 v175, v236, v175\n\t"
            "v_mfma_f32_16x16x32_bf16 v[196:199], v[44:47], v[208:211], v[196:199]\n\t"
            "v_cvt_pk_bf16_f32 v169, v170, v171\n\t"
            "v_cvt_pk_bf16_f32 v177, v178, v179\n\t"
            "v_mfma_f32_16x16x32_bf16 v[200:203], v[64:67], v[204:207], v[200:203]\n\t"
            "v_cvt_pk_bf16_f32 v181, v182, v183\n\t"
            "v_cvt_pk_bf16_f32 v170, v172, v173\n\t"
            "v_mfma_f32_16x16x32_bf16 v[200:203], v[68:71], v[208:211], v[200:203]\n\t"
            "v_cvt_pk_bf16_f32 v171, v174, v175\n\t"
            "v_cvt_pk_bf16_f32 v182, v184, v185\n\t"
            "v_mfma_f32_16x16x32_bf16 v[216:219], v[88:91], v[204:207], v[216:219]\n\t"
            "v_cvt_pk_bf16_f32 v183, v186, v187\n\t"
            "v_cvt_pk_bf16_f32 v178, v188, v189\n\t"
            "v_mfma_f32_16x16x32_bf16 v[216:219], v[92:95], v[208:211], v[216:219]\n\t"
            "v_cvt_pk_bf16_f32 v179, v190, v191\n\t"
            "v_mul_f32 v236, 0x3e553b94, v236\n\t"
            "v_mfma_f32_16x16x32_bf16 v[220:223], v[112:115], v[204:207], v[220:223]\n\t"
            "v_add_u32 v184, s76, v239\n\t"
            "v_add_u32 v185, s74, v239\n\t"
            "v_mfma_f32_16x16x32_bf16 v[220:223], v[116:119], v[208:211], v[220:223]\n\t"
            "s_mov_b64 exec, -1\n\t"
            "ds_write_b128 v234, v[168:171]\n\t"
            "ds_write_b128 v234, v[180:183] offset:64\n\t"
            "ds_write_b64 v235, v[176:177] offset:128\n\t"
            "ds_write_b64 v235, v[178:179] offset:160\n\t"
            "s_mov_b64 exec, -1\n\t"
            "v_mfma_f32_16x16x16_bf16 v[172:175], v[176:177], v[178:179], 0\n\t"
            "v_mul_f32 v192, v237, v192\n\t"
            "v_mul_f32 v193, v237, v193\n\t"
            "v_mul_f32 v200, v237, v200\n\t"
            "v_mfma_f32_16x16x32_bf16 v[228:231], v[136:139], v[204:207], v[228:231]\n\t"
            "ds_read_b128 v[204:207], v232\n\t"
            "v_mul_f32 v201, v237, v201\n\t"
            "v_mfma_f32_16x16x32_bf16 v[228:231], v[140:143], v[208:211], v[228:231]\n\t"
            "ds_read_b128 v[208:211], v233\n\t"
            "v_mul_f32 v194, v237, v194\n\t"
            "v_mfma_f32_16x16x32_bf16 v[172:175], v[168:171], v[180:183], v[172:175]\n\t"
            "v_mul_f32 v195, v237, v195\n\t"
            "v_mul_f32 v202, v237, v202\n\t"
            "v_mul_f32 v203, v237, v203\n\t"
            "v_cvt_pk_bf16_f32 v192, v192, v193\n\t"
            "v_cvt_pk_bf16_f32 v200, v200, v201\n\t"
            "v_cvt_pk_bf16_f32 v216, v216, v217\n\t"
            "v_mul_f32 v196, v237, v196\n\t"
            "v_mul_f32 v197, v237, v197\n\t"
            "v_mul_f32 v198, v237, v198\n\t"
            "v_mul_f32 v199, v237, v199\n\t"
            "v_cvt_pk_bf16_f32 v193, v194, v195\n\t"
            "v_cvt_pk_bf16_f32 v201, v202, v203\n\t"
            "v_cvt_pk_bf16_f32 v217, v218, v219\n\t"
            "v_cvt_pk_bf16_f32 v194, v196, v197\n\t"
            "v_cvt_pk_bf16_f32 v195, v198, v199\n\t"
            "v_cvt_pk_bf16_f32 v218, v220, v221\n\t"
            "v_cvt_pk_bf16_f32 v219, v222, v223\n\t"
            "v_cvt_pk_bf16_f32 v202, v228, v229\n\t"
            "v_cvt_pk_bf16_f32 v203, v230, v231\n\t"
            "v_mul_f32 v237, 0x3e553b94, v237\n\t"
            "v_add_u32 v220, s76, v239\n\t"
            "v_mfma_f32_16x16x16_bf16 v[196:199], v[200:201], v[202:203], 0\n\t"
            "s_and_b32 exec_lo, s56, 0x11111111\n\t"
            "s_and_b32 exec_hi, s57, 0x11111111\n\t"
            "ds_write_b32 v184, v172 offset:0\n\t"
            "s_and_b32 exec_lo, s56, 0x22222222\n\t"
            "s_and_b32 exec_hi, s57, 0x22222222\n\t"
            "ds_write_b32 v184, v173 offset:0\n\t"
            "s_and_b32 exec_lo, s56, 0x44444444\n\t"
            "s_and_b32 exec_hi, s57, 0x44444444\n\t"
            "ds_write_b32 v184, v174 offset:0\n\t"
            "s_and_b32 exec_lo, s56, 0x88888888\n\t"
            "s_and_b32 exec_hi, s57, 0x88888888\n\t"
            "ds_write_b32 v184, v175 offset:0\n\t"
            "s_mov_b64 exec, s[56:57]\n\t"
            "ds_write_b32 v185, v236 offset:0\n\t"
            "s_mov_b64 exec, -1\n\t"
            "s_waitcnt lgkmcnt(5)\n\t"
            "v_mfma_f32_16x16x32_bf16 v[144:147], v[0:3], v[204:207], 0\n\t"
            "v_add_u32 v221, s74, v239\n\t"
            "v_mfma_f32_16x16x32_bf16 v[144:147], v[4:7], v[208:211], v[144:147]\n\t"
            "s_mov_b64 exec, s[54:55]\n\t"
            "ds_write_b128 v234, v[192:195] offset:12800\n\t"
            "ds_write_b128 v234, v[216:219] offset:12864\n\t"
            "ds_write_b64 v235, v[200:201] offset:12928\n\t"
            "ds_write_b64 v235, v[202:203] offset:12960\n\t"
            "s_mov_b64 exec, -1\n\t"
            "v_mfma_f32_16x16x32_bf16 v[148:151], v[28:31], v[208:211], 0\n\t"
            "v_add_u32 v235, s48, v248\n\t"
            "v_mfma_f32_16x16x32_bf16 v[148:151], v[24:27], v[204:207], v[148:151]\n\t"
            "ds_read_b128 v[228:231], v235 offset:18816\n\t"
            "v_mfma_f32_16x16x32_bf16 v[152:155], v[48:51], v[204:207], 0\n\t"
            "v_mfma_f32_16x16x32_bf16 v[152:155], v[52:55], v[208:211], v[152:155]\n\t"
            "v_mfma_f32_16x16x32_bf16 v[156:159], v[76:79], v[208:211], 0\n\t"
            "v_mfma_f32_16x16x32_bf16 v[156:159], v[72:75], v[204:207], v[156:159]\n\t"
            "v_mfma_f32_16x16x32_bf16 v[160:163], v[96:99], v[204:207], 0\n\t"
            "v_mfma_f32_16x16x32_bf16 v[160:163], v[100:103], v[208:211], v[160:163]\n\t"
            "v_mfma_f32_16x16x32_bf16 v[164:167], v[124:127], v[208:211], 0\n\t"
            "ds_read_b128 v[208:211], v233 offset:128\n\t"
            "v_mfma_f32_16x16x32_bf16 v[164:167], v[120:123], v[204:207], v[164:167]\n\t"
            "ds_read_b128 v[204:207], v232 offset:128\n\t"
            "s_waitcnt lgkmcnt(2)\n\t"
            "v_add_f32 v236, v228, v230\n\t"
            "v_mfma_f32_16x16x32_bf16 v[196:199], v[192:195], v[216:219], v[196:199]\n\t"
            "s_nop 7\n\t"
            "s_and_b32 exec_lo, s78, 0x11111111\n\t"
            "s_and_b32 exec_hi, s79, 0x11111111\n\t"
            "ds_write_b32 v220, v196 offset:12800\n\t"
            "s_and_b32 exec_lo, s78, 0x22222222\n\t"
            "s_and_b32 exec_hi, s79, 0x22222222\n\t"
            "ds_write_b32 v220, v197 offset:12800\n\t"
            "s_and_b32 exec_lo, s78, 0x44444444\n\t"
            "s_and_b32 exec_hi, s79, 0x44444444\n\t"
            "ds_write_b32 v220, v198 offset:12800\n\t"
            "s_and_b32 exec_lo, s78, 0x88888888\n\t"
            "s_and_b32 exec_hi, s79, 0x88888888\n\t"
            "ds_write_b32 v220, v199 offset:12800\n\t"
            "s_mov_b64 exec, s[78:79]\n\t"
            "ds_write_b32 v221, v237 offset:12800\n\t"
            "s_mov_b64 exec, -1\n\t"
            "s_waitcnt lgkmcnt(5)\n\t"
            "v_mfma_f32_16x16x32_bf16 v[144:147], v[8:11], v[204:207], v[144:147]\n\t"
            "v_add_f32 v237, v229, v231\n\t"
            "v_mfma_f32_16x16x32_bf16 v[144:147], v[12:15], v[208:211], v[144:147]\n\t"
            "ds_read_b128 v[228:231], v235 offset:19840\n\t"
            "v_mfma_f32_16x16x32_bf16 v[148:151], v[36:39], v[208:211], v[148:151]\n\t"
            "v_mfma_f32_16x16x32_bf16 v[148:151], v[32:35], v[204:207], v[148:151]\n\t"
            "v_mfma_f32_16x16x32_bf16 v[152:155], v[56:59], v[204:207], v[152:155]\n\t"
            "v_mfma_f32_16x16x32_bf16 v[152:155], v[60:63], v[208:211], v[152:155]\n\t"
            "v_mfma_f32_16x16x32_bf16 v[156:159], v[84:87], v[208:211], v[156:159]\n\t"
            "v_mfma_f32_16x16x32_bf16 v[156:159], v[80:83], v[204:207], v[156:159]\n\t"
            "v_mfma_f32_16x16x32_bf16 v[160:163], v[104:107], v[204:207], v[160:163]\n\t"
            "v_mfma_f32_16x16x32_bf16 v[160:163], v[108:111], v[208:211], v[160:163]\n\t"
            "s_waitcnt lgkmcnt(0)\n\t"
            "v_add_f32 v228, v228, v230\n\t"
            "v_add_f32 v236, v236, v228\n\t"
            "v_mfma_f32_16x16x32_bf16 v[164:167], v[132:135], v[208:211], v[164:167]\n\t"
            "ds_read_b128 v[208:211], v233 offset:6144\n\t"
            "v_add_f32 v229, v229, v231\n\t"
            "v_mfma_f32_16x16x32_bf16 v[164:167], v[128:131], v[204:207], v[164:167]\n\t"
            "ds_read_b128 v[204:207], v232 offset:6144\n\t"
            "v_mul_f32 v236, 0x3baaaaab, v236\n\t"
            "v_mfma_f32_16x16x32_bf16 v[144:147], v[16:19], v[212:215], v[144:147]\n\t"
            "v_add_f32 v237, v237, v229\n\t"
            "v_mul_f32_e64 v236, v236, -v236\n\t"
            "v_mfma_f32_16x16x32_bf16 v[144:147], v[20:23], v[224:227], v[144:147]\n\t"
            "v_fmac_f32 v236, 0x3baaaaab, v237\n\t"
            "v_max_f32 v236, 0, v236\n\t"
            "v_mfma_f32_16x16x32_bf16 v[148:151], v[44:47], v[224:227], v[148:151]\n\t"
            "v_add_f32 v236, 0x3727c5ac, v236\n\t"
            "v_mfma_f32_16x16x32_bf16 v[148:151], v[40:43], v[212:215], v[148:151]\n\t"
            "v_rsq_f32 v236, v236\n\t"
            "v_mfma_f32_16x16x32_bf16 v[152:155], v[64:67], v[212:215], v[152:155]\n\t"
            "v_mfma_f32_16x16x32_bf16 v[152:155], v[68:71], v[224:227], v[152:155]\n\t"
            "ds_write_b32 v249, v236\n\t"
            "v_mfma_f32_16x16x32_bf16 v[156:159], v[92:95], v[224:227], v[156:159]\n\t"
            "ds_read_b32 v236, v251\n\t"
            "ds_read_b32 v237, v251 offset:64\n\t"
            "v_mfma_f32_16x16x32_bf16 v[156:159], v[88:91], v[212:215], v[156:159]\n\t"
            "ds_read_b32 v238, v251 offset:128\n\t"
            "v_mfma_f32_16x16x32_bf16 v[160:163], v[112:115], v[212:215], v[160:163]\n\t"
            "v_mfma_f32_16x16x32_bf16 v[160:163], v[116:119], v[224:227], v[160:163]\n\t"
            "v_mfma_f32_16x16x32_bf16 v[164:167], v[140:143], v[224:227], v[164:167]\n\t"
            "ds_read_b128 v[224:227], v233 offset:6272\n\t"
            "v_mfma_f32_16x16x32_bf16 v[164:167], v[136:139], v[212:215], v[164:167]\n\t"
            "ds_read_b128 v[212:215], v232 offset:6272\n\t"
            "s_waitcnt lgkmcnt(6)\n\t"
            "v_mfma_f32_16x16x32_bf16 v[168:171], v[0:3], v[204:207], 0\n\t"
            "v_cvt_pk_bf16_f32 v156, v156, v157\n\t"
            "v_cvt_pk_bf16_f32 v157, v158, v159\n\t"
            "v_mfma_f32_16x16x32_bf16 v[168:171], v[4:7], v[208:211], v[168:171]\n\t"
            "v_cvt_pk_bf16_f32 v158, v160, v161\n\t"
            "v_cvt_pk_bf16_f32 v159, v162, v163\n\t"
            "v_mfma_f32_16x16x32_bf16 v[172:175], v[28:31], v[208:211], 0\n\t"
            "s_waitcnt lgkmcnt(4)\n\t"
            "v_mul_f32 v144, v236, v144\n\t"
            "v_mul_f32 v145, v236, v145\n\t"
            "v_mfma_f32_16x16x32_bf16 v[172:175], v[24:27], v[204:207], v[172:175]\n\t"
            "v_mul_f32 v152, v236, v152\n\t"
            "v_mul_f32 v153, v236, v153\n\t"
            "v_mfma_f32_16x16x32_bf16 v[176:179], v[48:51], v[204:207], 0\n\t"
            "v_mul_f32 v146, v236, v146\n\t"
            "v_mul_f32 v147, v236, v147\n\t"
            "v_mfma_f32_16x16x32_bf16 v[176:179], v[52:55], v[208:211], v[176:179]\n\t"
            "v_mul_f32 v154, v236, v154\n\t"
            "v_mul_f32 v155, v236, v155\n\t"
            "v_mfma_f32_16x16x32_bf16 v[180:183], v[76:79], v[208:211], 0\n\t"
            "v_cvt_pk_bf16_f32 v144, v144, v145\n\t"
            "v_cvt_pk_bf16_f32 v152, v152, v153\n\t"
            "v_mfma_f32_16x16x32_bf16 v[180:183], v[72:75], v[204:207], v[180:183]\n\t"
            "v_mul_f32 v148, v236, v148\n\t"
            "v_mul_f32 v149, v236, v149\n\t"
            "v_mfma_f32_16x16x32_bf16 v[184:187], v[96:99], v[204:207], 0\n\t"
            "v_mul_f32 v150, v236, v150\n\t"
            "v_mul_f32 v151, v236, v151\n\t"
            "v_mfma_f32_16x16x32_bf16 v[184:187], v[100:103], v[208:211], v[184:187]\n\t"
            "v_cvt_pk_bf16_f32 v145, v146, v147\n\t"
            "v_cvt_pk_bf16_f32 v153, v154, v155\n\t"
            "v_mfma_f32_16x16x32_bf16 v[188:191], v[124:127], v[208:211], 0\n\t"
            "ds_read_b128 v[208:211], v233 offset:6400\n\t"
            "v_cvt_pk_bf16_f32 v146, v148, v149\n\t"
            "v_mfma_f32_16x16x32_bf16 v[188:191], v[120:123], v[204:207], v[188:191]\n\t"
            "ds_read_b128 v[204:207], v232 offset:6400\n\t"
            "v_cvt_pk_bf16_f32 v147, v150, v151\n\t"
            "s_waitcnt lgkmcnt(2)\n\t"
            "v_mfma_f32_16x16x32_bf16 v[168:171], v[8:11], v[212:215], v[168:171]\n\t"
            "v_cvt_pk_bf16_f32 v154, v164, v165\n\t"
            "v_cvt_pk_bf16_f32 v155, v166, v167\n\t"
            "v_mfma_f32_16x16x32_bf16 v[168:171], v[12:15], v[224:227], v[168:171]\n\t"
            "v_mul_f32 v236, 0x3e553b94, v236\n\t"
            "v_mfma_f32_16x16x32_bf16 v[172:175], v[36:39], v[224:227], v[172:175]\n\t"
            "v_mfma_f32_16x16x32_bf16 v[172:175], v[32:35], v[212:215], v[172:175]\n\t"
            "v_mfma_f32_16x16x32_bf16 v[176:179], v[56:59], v[212:215], v[176:179]\n\t"
            "v_mfma_f32_16x16x32_bf16 v[176:179], v[60:63], v[224:227], v[176:179]\n\t"
            "v_mfma_f32_16x16x32_bf16 v[180:183], v[84:87], v[224:227], v[180:183]\n\t"
            "v_mfma_f32_16x16x32_bf16 v[180:183], v[80:83], v[212:215], v[180:183]\n\t"
            "v_mfma_f32_16x16x32_bf16 v[184:187], v[104:107], v[212:215], v[184:187]\n\t"
            "v_mfma_f32_16x16x32_bf16 v[184:187], v[108:111], v[224:227], v[184:187]\n\t"
            "v_mfma_f32_16x16x32_bf16 v[188:191], v[132:135], v[224:227], v[188:191]\n\t"
            "ds_read_b128 v[224:227], v233 offset:12288\n\t"
            "v_mfma_f32_16x16x32_bf16 v[188:191], v[128:131], v[212:215], v[188:191]\n\t"
            "ds_read_b128 v[212:215], v232 offset:12288\n\t"
            "s_waitcnt lgkmcnt(2)\n\t"
            "v_mfma_f32_16x16x32_bf16 v[168:171], v[16:19], v[204:207], v[168:171]\n\t"
            "v_mfma_f32_16x16x32_bf16 v[168:171], v[20:23], v[208:211], v[168:171]\n\t"
            "v_mfma_f32_16x16x32_bf16 v[172:175], v[44:47], v[208:211], v[172:175]\n\t"
            "v_mfma_f32_16x16x32_bf16 v[172:175], v[40:43], v[204:207], v[172:175]\n\t"
            "v_mfma_f32_16x16x32_bf16 v[176:179], v[64:67], v[204:207], v[176:179]\n\t"
            "v_mfma_f32_16x16x32_bf16 v[176:179], v[68:71], v[208:211], v[176:179]\n\t"
            "v_mfma_f32_16x16x32_bf16 v[180:183], v[92:95], v[208:211], v[180:183]\n\t"
            "v_mfma_f32_16x16x32_bf16 v[180:183], v[88:91], v[204:207], v[180:183]\n\t"
            "v_mfma_f32_16x16x32_bf16 v[184:187], v[112:115], v[204:207], v[184:187]\n\t"
            "v_mfma_f32_16x16x32_bf16 v[184:187], v[116:119], v[208:211], v[184:187]\n\t"
            "v_mfma_f32_16x16x32_bf16 v[188:191], v[140:143], v[208:211], v[188:191]\n\t"
            "ds_read_b128 v[208:211], v233 offset:12416\n\t"
            "v_mfma_f32_16x16x32_bf16 v[188:191], v[136:139], v[204:207], v[188:191]\n\t"
            "ds_read_b128 v[204:207], v232 offset:12416\n\t"
            "s_waitcnt lgkmcnt(0)\n\t"
            "s_nop 7\n\t"
            ".Lwa_loop%=:\n\t"
            "s_waitcnt lgkmcnt(2)\n\t"
            "v_mfma_f32_16x16x32_bf16 v[192:195], v[0:3], v[212:215], 0\n\t"
            "s_bfe_u32 s49, s70, 0x30001\n\t"
            "s_lshl_b32 s49, s49, 4\n\t"
            "s_or_b32 s73, s77, s49\n\t"
            "v_mfma_f32_16x16x32_bf16 v[192:195], v[4:7], v[224:227], v[192:195]\n\t"
            "v_mad_u32_u24 v220, v246, s72, v253\n\t"
            "v_mad_u32_u24 v216, v246, s72, v252\n\t"
            "v_mfma_f32_16x16x32_bf16 v[148:151], v[28:31], v[224:227], 0\n\t"
            "ds_read_b64 v[222:223], v220 offset:160\n\t"
            "ds_read_b64 v[220:221], v220 offset:128\n\t"
            "v_mfma_f32_16x16x32_bf16 v[148:151], v[24:27], v[212:215], v[148:151]\n\t"
            "v_mul_f32 v176, v237, v176\n\t"
            "v_mul_f32 v177, v237, v177\n\t"
            "v_mfma_f32_16x16x32_bf16 v[196:199], v[48:51], v[212:215], 0\n\t"
            "v_mul_f32 v178, v237, v178\n\t"
            "v_mul_f32 v179, v237, v179\n\t"
            "v_mfma_f32_16x16x32_bf16 v[196:199], v[52:55], v[224:227], v[196:199]\n\t"
            "v_cvt_pk_bf16_f32 v176, v176, v177\n\t"
            "v_mul_f32 v168, v237, v168\n\t"
            "v_mfma_f32_16x16x32_bf16 v[200:203], v[76:79], v[224:227], 0\n\t"
            "v_mul_f32 v169, v237, v169\n\t"
            "v_cvt_pk_bf16_f32 v177, v178, v179\n\t"
            "v_mfma_f32_16x16x32_bf16 v[200:203], v[72:75], v[212:215], v[200:203]\n\t"
            "v_mul_f32 v170, v237, v170\n\t"
            "v_mul_f32 v171, v237, v171\n\t"
            "v_mfma_f32_16x16x32_bf16 v[160:163], v[96:99], v[212:215], 0\n\t"
            "v_cvt_pk_bf16_f32 v168, v168, v169\n\t"
            "v_cvt_pk_bf16_f32 v180, v180, v181\n\t"
            "v_mfma_f32_16x16x32_bf16 v[160:163], v[100:103], v[224:227], v[160:163]\n\t"
            "v_cvt_pk_bf16_f32 v178, v188, v189\n\t"
            "v_cvt_pk_bf16_f32 v179, v190, v191\n\t"
            "v_mfma_f32_16x16x32_bf16 v[164:167], v[124:127], v[224:227], 0\n\t"
            "ds_read_b128 v[224:227], v233 offset:12544\n\t"
            "v_mul_f32 v172, v237, v172\n\t"
            "v_mfma_f32_16x16x32_bf16 v[164:167], v[120:123], v[212:215], v[164:167]\n\t"
            "ds_read_b128 v[212:215], v232 offset:12544\n\t"
            "v_mul_f32 v173, v237, v173\n\t"
            "s_waitcnt lgkmcnt(4)\n\t"
            "v_mfma_f32_16x16x32_bf16 v[192:195], v[8:11], v[204:207], v[192:195]\n\t"
            "v_mul_f32 v174, v237, v174\n\t"
            "v_mul_f32 v175, v237, v175\n\t"
            "v_mfma_f32_16x16x32_bf16 v[192:195], v[12:15], v[208:211], v[192:195]\n\t"
            "v_cvt_pk_bf16_f32 v169, v170, v171\n\t"
            "v_cvt_pk_bf16_f32 v181, v182, v183\n\t"
            "v_mfma_f32_16x16x32_bf16 v[196:199], v[56:59], v[204:207], v[196:199]\n\t"
            "v_cvt_pk_bf16_f32 v170, v172, v173\n\t"
            "v_cvt_pk_bf16_f32 v171, v174, v175\n\t"
            "v_mfma_f32_16x16x32_bf16 v[196:199], v[60:63], v[208:211], v[196:199]\n\t"
            "v_cvt_pk_bf16_f32 v182, v184, v185\n\t"
            "v_cvt_pk_bf16_f32 v183, v186, v187\n\t"
            "v_mfma_f32_16x16x32_bf16 v[160:163], v[104:107], v[204:207], v[160:163]\n\t"
            "v_mov_b32 v230, s76\n\t"
            "v_mov_b32 v239, s74\n\t"
            "v_mfma_f32_16x16x32_bf16 v[160:163], v[108:111], v[208:211], v[160:163]\n\t"
            "s_add_u32 s70, s70, 1\n\t"
            "s_add_u32 s72, s72, 800\n\t"
            "s_add_u32 s74, s74, 800\n\t"
            "s_add_u32 s76, s76, 800\n\t"
            "v_mfma_f32_16x16x32_bf16 v[148:151], v[36:39], v[208:211], v[148:151]\n\t"
            "s_add_u32 s77, s77, 384\n\t"
            "s_xor_b32 s48, s48, s47\n\t"
            "s_sub_u32 s44, s44, 1\n\t"
            "v_mfma_f32_16x16x32_bf16 v[148:151], v[32:35], v[204:207], v[148:151]\n\t"
            "ds_read_b32 v230, v230\n\t"
            "ds_read_b32 v239, v239\n\t"
            "v_mfma_f32_16x16x32_bf16 v[200:203], v[84:87], v[208:211], v[200:203]\n\t"
            "v_mul_f32 v237, 0x3e553b94, v237\n\t"
            "v_mfma_f32_16x16x32_bf16 v[200:203], v[80:83], v[204:207], v[200:203]\n\t"
            "v_mfma_f32_16x16x32_bf16 v[164:167], v[132:135], v[208:211], v[164:167]\n\t"
            "v_mfma_f32_16x16x32_bf16 v[164:167], v[128:131], v[204:207], v[164:167]\n\t"
            "s_waitcnt lgkmcnt(2)\n\t"
            "v_mfma_f32_16x16x32_bf16 v[192:195], v[16:19], v[212:215], v[192:195]\n\t"
            "v_mfma_f32_16x16x32_bf16 v[192:195], v[20:23], v[224:227], v[192:195]\n\t"
            "v_mfma_f32_16x16x32_bf16 v[196:199], v[64:67], v[212:215], v[196:199]\n\t"
            "v_mfma_f32_16x16x32_bf16 v[196:199], v[68:71], v[224:227], v[196:199]\n\t"
            "v_mfma_f32_16x16x32_bf16 v[160:163], v[112:115], v[212:215], v[160:163]\n\t"
            "v_mfma_f32_16x16x32_bf16 v[160:163], v[116:119], v[224:227], v[160:163]\n\t"
            "v_mfma_f32_16x16x32_bf16 v[148:151], v[44:47], v[224:227], v[148:151]\n\t"
            "v_mfma_f32_16x16x32_bf16 v[148:151], v[40:43], v[212:215], v[148:151]\n\t"
            "s_nop 1\n\t"
            "v_mul_f32 v192, v238, v192\n\t"
            "v_mfma_f32_16x16x32_bf16 v[200:203], v[92:95], v[224:227], v[200:203]\n\t"
            "v_mul_f32 v193, v238, v193\n\t"
            "v_mul_f32 v194, v238, v194\n\t"
            "v_mfma_f32_16x16x32_bf16 v[200:203], v[88:91], v[212:215], v[200:203]\n\t"
            "v_mul_f32 v195, v238, v195\n\t"
            "v_cvt_pk_bf16_f32 v192, v192, v193\n\t"
            "v_mfma_f32_16x16x32_bf16 v[164:167], v[140:143], v[224:227], v[164:167]\n\t"
            "ds_read_b128 v[224:227], v216 offset:64\n\t"
            "ds_read_b128 v[216:219], v216\n\t"
            "v_mfma_f32_16x16x32_bf16 v[164:167], v[136:139], v[212:215], v[164:167]\n\t"
            "v_mul_f32 v148, v238, v148\n\t"
            "v_mul_f32 v149, v238, v149\n\t"
            "v_mfma_f32_16x16x16_bf16 v[172:175], v[220:221], v[154:155], 0\n\t"
            "v_mul_f32 v150, v238, v150\n\t"
            "v_mul_f32 v151, v238, v151\n\t"
            "v_cvt_pk_bf16_f32 v193, v194, v195\n\t"
            "v_mfma_f32_16x16x16_bf16 v[184:187], v[152:153], v[178:179], 0\n\t"
            "v_mul_f32 v196, v238, v196\n\t"
            "v_mul_f32 v197, v238, v197\n\t"
            "v_cvt_pk_bf16_f32 v194, v148, v149\n\t"
            "v_mfma_f32_16x16x16_bf16 v[208:211], v[220:221], v[178:179], 0\n\t"
            "v_cvt_pk_bf16_f32 v195, v150, v151\n\t"
            "v_cvt_pk_bf16_f32 v200, v200, v201\n\t"
            "v_mul_f32 v198, v238, v198\n\t"
            "v_mfma_f32_16x16x16_bf16 v[148:151], v[152:153], v[154:155], 0\n\t"
            "v_mul_f32 v199, v238, v199\n\t"
            "v_cvt_pk_bf16_f32 v196, v196, v197\n\t"
            "v_cvt_pk_bf16_f32 v201, v202, v203\n\t"
            "v_mfma_f32_16x16x16_bf16 v[204:207], v[176:177], v[178:179], 0\n\t"
            "v_cvt_pk_bf16_f32 v197, v198, v199\n\t"
            "v_cvt_pk_bf16_f32 v202, v160, v161\n\t"
            "v_cvt_pk_bf16_f32 v203, v162, v163\n\t"
            "v_mfma_f32_16x16x16_bf16 v[160:163], v[176:177], v[154:155], 0\n\t"
            "v_cvt_pk_bf16_f32 v198, v164, v165\n\t"
            "v_cvt_pk_bf16_f32 v199, v166, v167\n\t"
            "s_setprio 0\n\t"
            "s_setprio 3\n\t"
            "v_mfma_f32_16x16x16_bf16 v[164:167], v[196:197], v[154:155], 0\n\t"
            "v_mul_f32 v238, 0x3e553b94, v238\n\t"
            "v_mfma_f32_16x16x16_bf16 v[188:191], v[196:197], v[178:179], 0\n\t"
            "v_mfma_f32_16x16x32_bf16 v[148:151], v[144:147], v[156:159], v[148:151]\n\t"
            "s_waitcnt lgkmcnt(0)\n\t"
            "v_mfma_f32_16x16x32_bf16 v[172:175], v[216:219], v[156:159], v[172:175]\n\t"
            "v_mfma_f32_16x16x32_bf16 v[184:187], v[144:147], v[180:183], v[184:187]\n\t"
            "v_mfma_f32_16x16x32_bf16 v[208:211], v[216:219], v[180:183], v[208:211]\n\t"
            "v_mfma_f32_16x16x32_bf16 v[160:163], v[168:171], v[156:159], v[160:163]\n\t"
            "v_mfma_f32_16x16x32_bf16 v[204:207], v[168:171], v[180:183], v[204:207]\n\t"
            "v_mfma_f32_16x16x32_bf16 v[164:167], v[192:195], v[156:159], v[164:167]\n\t"
            "v_mfma_f32_16x16x32_bf16 v[188:191], v[192:195], v[180:183], v[188:191]\n\t"
            "s_nop 1\n\t"
            "v_max3_f32 v228, v172, v148, v149\n\t"
            "v_mfma_f32_16x16x16_bf16 v[212:215], v[222:223], v[152:153], 0\n\t"
            "v_max3_f32 v228, v228, v150, v151\n\t"
            "v_max3_f32 v229, v208, v184, v185\n\t"
            "v_mfma_f32_16x16x16_bf16 v[156:159], v[222:223], v[196:197], 0\n\t"
            "v_max3_f32 v229, v229, v186, v187\n\t"
            "v_max3_f32 v228, v228, v160, v161\n\t"
            "v_max3_f32 v229, v229, v204, v205\n\t"
            "v_mfma_f32_16x16x32_bf16 v[212:215], v[224:227], v[144:147], v[212:215]\n\t"
            "v_max3_f32 v228, v228, v162, v163\n\t"
            "v_max3_f32 v229, v229, v206, v207\n\t"
            "v_mfma_f32_16x16x32_bf16 v[156:159], v[224:227], v[192:195], v[156:159]\n\t"
            "v_max3_f32 v228, v228, v164, v165\n\t"
            "v_max3_f32 v229, v229, v188, v189\n\t"
            "v_mfma_f32_16x16x16_bf16 v[180:183], v[152:153], v[198:199], 0\n\t"
            "s_setprio 0\n\t"
            "v_max3_f32 v228, v228, v166, v167\n\t"
            "v_max3_f32 v229, v229, v190, v191\n\t"
            "s_nop 1\n\t"
            "v_permlane32_swap_b32 v228, v229\n\t"
            "v_max_f32 v228, v228, v229\n\t"
            "v_mov_b32 v231, v228\n\t"
            "s_setprio 3\n\t"
            "v_mfma_f32_16x16x32_bf16 v[180:183], v[144:147], v[200:203], v[180:183]\n\t"
            "s_setprio 0\n\t"
            "v_permlane16_swap_b32 v228, v231\n\t"
            "v_max_f32 v228, v228, v231\n\t"
            "v_mov_b32 v229, v228\n\t"
            "s_nop 1\n\t"
            "v_permlane32_swap_b32 v228, v229\n\t"
            "v_mul_f32_e64 v228, -v228, v236\n\t"
            "v_fma_f32 v148, v148, v236, v228\n\t"
            "v_fma_f32 v149, v149, v236, v228\n\t"
            "v_exp_f32 v148, v148\n\t"
            "v_exp_f32 v149, v149\n\t"
            "v_fma_f32 v150, v150, v236, v228\n\t"
            "v_fma_f32 v151, v151, v236, v228\n\t"
            "v_exp_f32 v150, v150\n\t"
            "v_exp_f32 v151, v151\n\t"
            "v_fma_f32 v160, v160, v236, v228\n\t"
            "v_fma_f32 v161, v161, v236, v228\n\t"
            "v_fma_f32 v162, v162, v236, v228\n\t"
            "v_fma_f32 v163, v163, v236, v228\n\t"
            "v_exp_f32 v160, v160\n\t"
            "v_exp_f32 v161, v161\n\t"
            "v_exp_f32 v162, v162\n\t"
            "v_exp_f32 v163, v163\n\t"
            "v_cvt_pk_bf16_f32 v148, v148, v149\n\t"
            "v_cvt_pk_bf16_f32 v149, v150, v151\n\t"
            "v_cvt_pk_bf16_f32 v150, v160, v161\n\t"
            "v_cvt_pk_bf16_f32 v151, v162, v163\n\t"
            "s_setprio 3\n\t"
            "v_mfma_f32_16x16x16_bf16 v[160:163], v[222:223], v[176:177], 0\n\t"
            "s_setprio 0\n\t"
            "v_mul_f32_e64 v229, -v229, v237\n\t"
            "v_fma_f32 v188, v188, v237, v229\n\t"
            "v_fma_f32 v189, v189, v237, v229\n\t"
            "v_exp_f32 v188, v188\n\t"
            "v_exp_f32 v189, v189\n\t"
            "v_fma_f32 v190, v190, v237, v229\n\t"
            "s_setprio 3\n\t"
            "v_mfma_f32_16x16x32_bf16 v[160:163], v[224:227], v[168:171], v[160:163]\n\t"
            "s_setprio 0\n\t"
            "v_fma_f32 v191, v191, v237, v229\n\t"
            "v_fma_f32 v208, v208, v237, v229\n\t"
            "v_fma_f32 v184, v184, v237, v229\n\t"
            "v_fma_f32 v185, v185, v237, v229\n\t"
            "v_exp_f32 v190, v190\n\t"
            "v_exp_f32 v191, v191\n\t"
            "v_exp_f32 v208, v208\n\t"
            "v_fma_f32 v164, v164, v236, v228\n\t"
            "v_max3_f32 v234, v212, v160, v156\n\t"
            "v_max_f32 v234, v234, v230\n\t"
            "v_fma_f32 v165, v165, v236, v228\n\t"
            "v_exp_f32 v184, v184\n\t"
            "v_max_f32_dpp v234, v234, v234 row_ror:8 row_mask:0xf bank_mask:0xf\n\t"
            "v_exp_f32 v185, v185\n\t"
            "v_fma_f32 v186, v186, v237, v229\n\t"
            "v_max_f32_dpp v234, v234, v234 row_ror:4 row_mask:0xf bank_mask:0xf\n\t"
            "v_fma_f32 v187, v187, v237, v229\n\t"
            "v_exp_f32 v164, v164\n\t"
            "v_exp_f32 v165, v165\n\t"
            "v_fma_f32 v166, v166, v236, v228\n\t"
            "v_fma_f32 v167, v167, v236, v228\n\t"
            "v_fma_f32 v172, v172, v236, v228\n\t"
            "v_exp_f32 v186, v186\n\t"
            "v_exp_f32 v187, v187\n\t"
            "v_fma_f32 v204, v204, v237, v229\n\t"
            "v_fma_f32 v205, v205, v237, v229\n\t"
            "v_fma_f32 v206, v206, v237, v229\n\t"
            "v_fma_f32 v207, v207, v237, v229\n\t"
            "v_cvt_pk_bf16_f32 v188, v188, v189\n\t"
            "v_exp_f32 v166, v166\n\t"
            "v_exp_f32 v167, v167\n\t"
            "v_exp_f32 v172, v172\n\t"
            "v_exp_f32 v204, v204\n\t"
            "v_exp_f32 v205, v205\n\t"
            "v_exp_f32 v206, v206\n\t"
            "v_exp_f32 v207, v207\n\t"
            "v_cvt_pk_bf16_f32 v189, v190, v191\n\t"
            "v_cvt_pk_bf16_f32 v208, v208, 0\n\t"
            "v_max_f32_dpp v234, v234, v234 row_ror:2 row_mask:0xf bank_mask:0xf\n\t"
            "v_cvt_pk_bf16_f32 v184, v184, v185\n\t"
            "v_cndmask_b32_e64 v190, 0, v208, s[58:59]\n\t"
            "s_setprio 3\n\t"
            "v_mfma_f32_16x16x16_bf16 v[208:211], v[220:221], v[198:199], 0\n\t"
            "s_setprio 0\n\t"
            "v_cvt_pk_bf16_f32 v164, v164, v165\n\t"
            "v_cndmask_b32_e64 v235, v212, v160, s[60:61]\n\t"
            "v_cvt_pk_bf16_f32 v185, v186, v187\n\t"
            "v_cvt_pk_bf16_f32 v165, v166, v167\n\t"
            "v_cvt_pk_bf16_f32 v172, v172, 0\n\t"
            "v_cndmask_b32_e64 v235, v235, v156, s[62:63]\n\t"
            "v_cvt_pk_bf16_f32 v186, v204, v205\n\t"
            "s_setprio 3\n\t"
            "v_mfma_f32_16x16x32_bf16 v[208:211], v[216:219], v[200:203], v[208:211]\n\t"
            "v_cvt_pk_bf16_f32 v187, v206, v207\n\t"
            "v_cndmask_b32_e64 v166, 0, v172, s[58:59]\n\t"
            "v_mfma_f32_16x16x16_bf16 v[204:207], v[176:177], v[198:199], 0\n\t"
            "v_max_f32_dpp v234, v234, v234 row_ror:1 row_mask:0xf bank_mask:0xf\n\t"
            "v_cndmask_b32_e64 v235, v235, v230, s[64:65]\n\t"
            "v_sub_f32 v235, v235, v234\n\t"
            "v_mfma_f32_16x16x16_bf16 v[172:175], v[196:197], v[198:199], 0\n\t"
            "v_mul_f32 v235, v239, v235\n\t"
            "v_exp_f32 v235, v235\n\t"
            "v_mfma_f32_16x16x32_bf16 v[204:207], v[168:171], v[200:203], v[204:207]\n\t"
            "v_cvt_pk_bf16_f32 v235, v235, 0\n\t"
            "v_cndmask_b32_e64 v235, 0, v235, s[66:67]\n\t"
            "v_max3_f32 v230, v208, v180, v181\n\t"
            "v_mfma_f32_16x16x32_bf16 v[172:175], v[192:195], v[200:203], v[172:175]\n\t"
            "s_setprio 0\n\t"
            "ds_write_b16 v250, v235 offset:256\n\t"
            "v_max3_f32 v230, v230, v182, v183\n\t"
            "v_mov_b32 v167, 0\n\t"
            "v_mov_b32 v191, 0\n\t"
            "v_max3_f32 v230, v230, v204, v205\n\t"
            "v_max3_f32 v230, v230, v206, v207\n\t"
            "s_nop 0\n\t"
            "v_max3_f32 v230, v230, v172, v173\n\t"
            "v_max3_f32 v230, v230, v174, v175\n\t"
            "v_mov_b32 v231, v230\n\t"
            "s_nop 1\n\t"
            "v_permlane32_swap_b32 v230, v231\n\t"
            "v_max_f32 v230, v230, v231\n\t"
            "v_mov_b32 v231, v230\n\t"
            "s_nop 1\n\t"
            "v_permlane16_swap_b32 v230, v231\n\t"
            "v_max_f32 v230, v230, v231\n\t"
            "v_mul_f32_e64 v230, -v230, v238\n\t"
            "v_fma_f32 v180, v180, v238, v230\n\t"
            "v_fma_f32 v181, v181, v238, v230\n\t"
            "v_fma_f32 v172, v172, v238, v230\n\t"
            "v_fma_f32 v173, v173, v238, v230\n\t"
            "v_exp_f32 v180, v180\n\t"
            "v_exp_f32 v181, v181\n\t"
            "v_fma_f32 v182, v182, v238, v230\n\t"
            "v_fma_f32 v183, v183, v238, v230\n\t"
            "v_exp_f32 v172, v172\n\t"
            "v_exp_f32 v173, v173\n\t"
            "v_fma_f32 v174, v174, v238, v230\n\t"
            "v_fma_f32 v175, v175, v238, v230\n\t"
            "v_fma_f32 v208, v208, v238, v230\n\t"
            "v_exp_f32 v182, v182\n\t"
            "v_exp_f32 v183, v183\n\t"
            "v_fma_f32 v204, v204, v238, v230\n\t"
            "v_fma_f32 v205, v205, v238, v230\n\t"
            "v_fma_f32 v206, v206, v238, v230\n\t"
            "v_fma_f32 v207, v207, v238, v230\n\t"
            "v_exp_f32 v174, v174\n\t"
            "v_exp_f32 v175, v175\n\t"
            "v_exp_f32 v208, v208\n\t"
            "v_exp_f32 v204, v204\n\t"
            "v_exp_f32 v205, v205\n\t"
            "v_exp_f32 v206, v206\n\t"
            "v_exp_f32 v207, v207\n\t"
            "v_cvt_pk_bf16_f32 v180, v180, v181\n\t"
            "v_cvt_pk_bf16_f32 v172, v172, v173\n\t"
            "v_cvt_pk_bf16_f32 v181, v182, v183\n\t"
            "v_cvt_pk_bf16_f32 v173, v174, v175\n\t"
            "v_cvt_pk_bf16_f32 v208, v208, 0\n\t"
            "v_cvt_pk_bf16_f32 v182, v204, v205\n\t"
            "v_cvt_pk_bf16_f32 v183, v206, v207\n\t"
            "v_cndmask_b32_e64 v174, 0, v208, s[58:59]\n\t"
            "v_mov_b32 v175, 0\n\t"
            "s_barrier\n\t"
            "s_setprio 2\n\t"
            "v_mfma_f32_16x16x32_bf16 v[200:203], v[240:243], v[148:151], 0\n\t"
            "ds_read_b64 v[176:177], v247\n\t"
            "ds_read_b64 v[196:197], v247 offset:1536\n\t"
            "v_mfma_f32_16x16x32_bf16 v[200:203], v[240:243], v[164:167], v[200:203]\n\t"
            "ds_read_b64 v[220:221], v247 offset:3072\n\t"
            "v_xor_b32 v234, s73, v254\n\t"
            "v_add_u32 v235, s45, v248\n\t"
            "v_mov_b32 v179, 0\n\t"
            "v_mov_b32 v199, 0\n\t"
            "v_add_u32 v232, s48, v244\n\t"
            "v_xor_b32 v233, 64, v232\n\t"
            "v_mov_b32 v223, 0\n\t"
            "v_rcp_f32 v201, v200\n\t"
            "ds_read_b128 v[168:171], v235\n\t"
            "ds_read_u16 v178, v234\n\t"
            "ds_read_b128 v[192:195], v235 offset:1536\n\t"
            "ds_read_u16 v198, v234 offset:128\n\t"
            "ds_read_b128 v[216:219], v235 offset:3072\n\t"
            "ds_read_u16 v222, v234 offset:256\n\t"
            "ds_read_b128 v[204:207], v232\n\t"
            "ds_read_b128 v[208:211], v233\n\t"
            "v_add_u32 v235, s48, v248\n\t"
            "v_mad_u32_u24 v234, v246, s75, v253\n\t"
            "ds_read_b128 v[228:231], v235 offset:18816\n\t"
            "s_waitcnt lgkmcnt(7)\n\t"
            "v_mfma_f32_16x16x32_bf16 v[224:227], v[168:171], v[148:151], 0\n\t"
            "v_mfma_f32_16x16x32_bf16 v[224:227], v[176:179], v[164:167], v[224:227]\n\t"
            "s_waitcnt lgkmcnt(5)\n\t"
            "v_mfma_f32_16x16x32_bf16 v[212:215], v[192:195], v[148:151], 0\n\t"
            "s_waitcnt lgkmcnt(0)\n\t"
            "v_add_f32 v236, v228, v230\n\t"
            "v_mfma_f32_16x16x32_bf16 v[212:215], v[196:199], v[164:167], v[212:215]\n\t"
            "v_add_f32 v237, v229, v231\n\t"
            "v_mfma_f32_16x16x32_bf16 v[148:151], v[216:219], v[148:151], 0\n\t"
            "ds_read_b128 v[228:231], v235 offset:19840\n\t"
            "v_mfma_f32_16x16x32_bf16 v[148:151], v[220:223], v[164:167], v[148:151]\n\t"
            "v_mul_f32 v224, v201, v224\n\t"
            "v_mul_f32 v225, v201, v225\n\t"
            "v_mfma_f32_16x16x32_bf16 v[144:147], v[0:3], v[204:207], 0\n\t"
            "s_nop 0\n\t"
            "v_mul_f32 v212, v201, v212\n\t"
            "v_mul_f32 v213, v201, v213\n\t"
            "v_mfma_f32_16x16x32_bf16 v[144:147], v[4:7], v[208:211], v[144:147]\n\t"
            "v_mul_f32 v226, v201, v226\n\t"
            "v_mul_f32 v227, v201, v227\n\t"
            "v_mfma_f32_16x16x32_bf16 v[152:155], v[48:51], v[204:207], 0\n\t"
            "v_cvt_pk_bf16_f32 v224, v224, v225\n\t"
            "v_mul_f32 v214, v201, v214\n\t"
            "v_mfma_f32_16x16x32_bf16 v[152:155], v[52:55], v[208:211], v[152:155]\n\t"
            "v_mul_f32 v215, v201, v215\n\t"
            "v_cvt_pk_bf16_f32 v212, v212, v213\n\t"
            "v_mfma_f32_16x16x32_bf16 v[156:159], v[76:79], v[208:211], 0\n\t"
            "v_cvt_pk_bf16_f32 v225, v226, v227\n\t"
            "v_cvt_pk_bf16_f32 v213, v214, v215\n\t"
            "v_mfma_f32_16x16x32_bf16 v[156:159], v[72:75], v[204:207], v[156:159]\n\t"
            "ds_write_b64 v245, v[224:225]\n\t"
            "v_mfma_f32_16x16x32_bf16 v[224:227], v[168:171], v[184:187], 0\n\t"
            "v_mul_f32 v148, v201, v148\n\t"
            "v_mul_f32 v149, v201, v149\n\t"
            "v_mfma_f32_16x16x32_bf16 v[224:227], v[176:179], v[188:191], v[224:227]\n\t"
            "ds_write_b64 v245, v[212:213] offset:128\n\t"
            "v_mfma_f32_16x16x32_bf16 v[212:215], v[192:195], v[184:187], 0\n\t"
            "v_mul_f32 v150, v201, v150\n\t"
            "v_mul_f32 v151, v201, v151\n\t"
            "v_mfma_f32_16x16x32_bf16 v[212:215], v[196:199], v[188:191], v[212:215]\n\t"
            "v_cvt_pk_bf16_f32 v148, v148, v149\n\t"
            "v_cvt_pk_bf16_f32 v149, v150, v151\n\t"
            "v_mfma_f32_16x16x32_bf16 v[200:203], v[240:243], v[184:187], 0\n\t"
            "s_waitcnt lgkmcnt(2)\n\t"
            "v_add_f32 v228, v228, v230\n\t"
            "v_add_f32 v236, v236, v228\n\t"
            "v_mfma_f32_16x16x32_bf16 v[200:203], v[240:243], v[188:191], v[200:203]\n\t"
            "ds_write_b64 v245, v[148:149] offset:256\n\t"
            "v_mfma_f32_16x16x32_bf16 v[184:187], v[216:219], v[184:187], 0\n\t"
            "v_add_f32 v229, v229, v231\n\t"
            "v_mul_f32 v236, 0x3baaaaab, v236\n\t"
            "v_mfma_f32_16x16x32_bf16 v[184:187], v[220:223], v[188:191], v[184:187]\n\t"
            "v_add_f32 v237, v237, v229\n\t"
            "v_mul_f32_e64 v236, v236, -v236\n\t"
            "v_mfma_f32_16x16x32_bf16 v[148:151], v[28:31], v[208:211], 0\n\t"
            "v_rcp_f32 v201, v200\n\t"
            "v_mfma_f32_16x16x32_bf16 v[148:151], v[24:27], v[204:207], v[148:151]\n\t"
            "v_mul_f32 v224, v201, v224\n\t"
            "v_mul_f32 v225, v201, v225\n\t"
            "v_mfma_f32_16x16x32_bf16 v[160:163], v[96:99], v[204:207], 0\n\t"
            "v_mul_f32 v226, v201, v226\n\t"
            "v_mul_f32 v227, v201, v227\n\t"
            "v_mfma_f32_16x16x32_bf16 v[160:163], v[100:103], v[208:211], v[160:163]\n\t"
            "v_mul_f32 v212, v201, v212\n\t"
            "v_mul_f32 v213, v201, v213\n\t"
            "v_mfma_f32_16x16x32_bf16 v[164:167], v[124:127], v[208:211], 0\n\t"
            "ds_read_b128 v[208:211], v233 offset:128\n\t"
            "v_mul_f32 v214, v201, v214\n\t"
            "v_mfma_f32_16x16x32_bf16 v[164:167], v[120:123], v[204:207], v[164:167]\n\t"
            "ds_read_b128 v[204:207], v232 offset:128\n\t"
            "v_mul_f32 v215, v201, v215\n\t"
            "v_mfma_f32_16x16x32_bf16 v[188:191], v[192:195], v[180:183], 0\n\t"
            "v_mul_f32 v184, v201, v184\n\t"
            "v_mul_f32 v185, v201, v185\n\t"
            "v_mfma_f32_16x16x32_bf16 v[188:191], v[196:199], v[172:175], v[188:191]\n\t"
            "v_mul_f32 v186, v201, v186\n\t"
            "v_mul_f32 v187, v201, v187\n\t"
            "v_mfma_f32_16x16x32_bf16 v[200:203], v[240:243], v[180:183], 0\n\t"
            "v_cvt_pk_bf16_f32 v224, v224, v225\n\t"
            "v_cvt_pk_bf16_f32 v225, v226, v227\n\t"
            "v_mfma_f32_16x16x32_bf16 v[200:203], v[240:243], v[172:175], v[200:203]\n\t"
            "ds_write_b64 v245, v[224:225] offset:6144\n\t"
            "v_mfma_f32_16x16x32_bf16 v[224:227], v[168:171], v[180:183], 0\n\t"
            "v_cvt_pk_bf16_f32 v184, v184, v185\n\t"
            "v_cvt_pk_bf16_f32 v185, v186, v187\n\t"
            "v_mfma_f32_16x16x32_bf16 v[224:227], v[176:179], v[172:175], v[224:227]\n\t"
            "ds_read_b64 v[186:187], v234 offset:288\n\t"
            "ds_write_b64 v245, v[184:185] offset:6400\n\t"
            "v_mfma_f32_16x16x32_bf16 v[180:183], v[216:219], v[180:183], 0\n\t"
            "ds_read_b64 v[184:185], v234 offset:256\n\t"
            "v_cvt_pk_bf16_f32 v212, v212, v213\n\t"
            "v_mfma_f32_16x16x32_bf16 v[180:183], v[220:223], v[172:175], v[180:183]\n\t"
            "v_rcp_f32 v201, v200\n\t"
            "s_waitcnt lgkmcnt(4)\n\t"
            "v_mfma_f32_16x16x32_bf16 v[144:147], v[8:11], v[204:207], v[144:147]\n\t"
            "v_mul_f32 v188, v201, v188\n\t"
            "v_mul_f32 v189, v201, v189\n\t"
            "v_mfma_f32_16x16x32_bf16 v[144:147], v[12:15], v[208:211], v[144:147]\n\t"
            "v_mul_f32 v190, v201, v190\n\t"
            "v_mul_f32 v191, v201, v191\n\t"
            "v_mfma_f32_16x16x32_bf16 v[148:151], v[36:39], v[208:211], v[148:151]\n\t"
            "v_cvt_pk_bf16_f32 v188, v188, v189\n\t"
            "v_cvt_pk_bf16_f32 v189, v190, v191\n\t"
            "v_mfma_f32_16x16x32_bf16 v[148:151], v[32:35], v[204:207], v[148:151]\n\t"
            "ds_read_b64 v[190:191], v234 offset:352\n\t"
            "ds_write_b64 v245, v[188:189] offset:12416\n\t"
            "v_mfma_f32_16x16x32_bf16 v[152:155], v[56:59], v[204:207], v[152:155]\n\t"
            "ds_read_b64 v[188:189], v234 offset:320\n\t"
            "v_mul_f32 v224, v201, v224\n\t"
            "v_mfma_f32_16x16x32_bf16 v[152:155], v[60:63], v[208:211], v[152:155]\n\t"
            "v_mul_f32 v225, v201, v225\n\t"
            "v_mul_f32 v226, v201, v226\n\t"
            "v_mfma_f32_16x16x32_bf16 v[156:159], v[84:87], v[208:211], v[156:159]\n\t"
            "v_mul_f32 v227, v201, v227\n\t"
            "v_cvt_pk_bf16_f32 v224, v224, v225\n\t"
            "v_mfma_f32_16x16x32_bf16 v[156:159], v[80:83], v[204:207], v[156:159]\n\t"
            "v_cvt_pk_bf16_f32 v213, v214, v215\n\t"
            "v_cvt_pk_bf16_f32 v225, v226, v227\n\t"
            "v_mfma_f32_16x16x32_bf16 v[160:163], v[104:107], v[204:207], v[160:163]\n\t"
            "v_mul_f32 v180, v201, v180\n\t"
            "v_mul_f32 v181, v201, v181\n\t"
            "v_mfma_f32_16x16x32_bf16 v[160:163], v[108:111], v[208:211], v[160:163]\n\t"
            "ds_write_b64 v245, v[212:213] offset:6272\n\t"
            "v_mfma_f32_16x16x32_bf16 v[164:167], v[132:135], v[208:211], v[164:167]\n\t"
            "ds_read_b128 v[212:215], v232 offset:256\n\t"
            "ds_read_b128 v[208:211], v233 offset:6144\n\t"
            "v_mfma_f32_16x16x32_bf16 v[164:167], v[128:131], v[204:207], v[164:167]\n\t"
            "ds_read_b128 v[204:207], v232 offset:6144\n\t"
            "ds_write_b64 v245, v[224:225] offset:12288\n\t"
            "s_waitcnt lgkmcnt(5)\n\t"
            "v_mfma_f32_16x16x32_bf16 v[168:171], v[184:187], v[168:171], 0\n\t"
            "ds_read_b128 v[224:227], v233 offset:256\n\t"
            "v_mul_f32 v182, v201, v182\n\t"
            "v_mfma_f32_16x16x32_bf16 v[168:171], v[188:191], v[176:179], v[168:171]\n\t"
            "v_mul_f32 v183, v201, v183\n\t"
            "v_cvt_pk_bf16_f32 v180, v180, v181\n\t"
            "v_mfma_f32_16x16x32_bf16 v[200:203], v[240:243], v[184:187], 0\n\t"
            "v_cvt_pk_bf16_f32 v181, v182, v183\n\t"
            "v_fmac_f32 v236, 0x3baaaaab, v237\n\t"
            "v_mfma_f32_16x16x32_bf16 v[200:203], v[240:243], v[188:191], v[200:203]\n\t"
            "ds_write_b64 v245, v[180:181] offset:12544\n\t"
            "v_mfma_f32_16x16x32_bf16 v[192:195], v[184:187], v[192:195], 0\n\t"
            "v_max_f32 v236, 0, v236\n\t"
            "v_add_f32 v236, 0x3727c5ac, v236\n\t"
            "v_mfma_f32_16x16x32_bf16 v[192:195], v[188:191], v[196:199], v[192:195]\n\t"
            "v_rsq_f32 v236, v236\n\t"
            "v_mfma_f32_16x16x32_bf16 v[216:219], v[184:187], v[216:219], 0\n\t"
            "s_nop 0\n\t"
            "v_rcp_f32 v201, v200\n\t"
            "v_mfma_f32_16x16x32_bf16 v[216:219], v[188:191], v[220:223], v[216:219]\n\t"
            "ds_write_b32 v249, v236\n\t"
            "s_waitcnt lgkmcnt(2)\n\t"
            "v_mfma_f32_16x16x32_bf16 v[144:147], v[16:19], v[212:215], v[144:147]\n\t"
            "ds_read_b32 v236, v251\n\t"
            "ds_read_b32 v237, v251 offset:64\n\t"
            "v_mfma_f32_16x16x32_bf16 v[144:147], v[20:23], v[224:227], v[144:147]\n\t"
            "ds_read_b32 v238, v251 offset:128\n\t"
            "v_cndmask_b32_e64 v203, v168, v192, s[60:61]\n\t"
            "v_mfma_f32_16x16x32_bf16 v[148:151], v[44:47], v[224:227], v[148:151]\n\t"
            "v_cndmask_b32_e64 v203, v203, v216, s[62:63]\n\t"
            "v_xad_u32 v202, s73, v254, v255\n\t"
            "v_mfma_f32_16x16x32_bf16 v[148:151], v[40:43], v[212:215], v[148:151]\n\t"
            "v_mul_f32_dpp v203, v201, v203 quad_perm:[0,0,0,0] row_mask:0xf bank_mask:0xf\n\t"
            "v_cvt_pk_bf16_f32 v203, v203, 0\n\t"
            "v_mfma_f32_16x16x32_bf16 v[152:155], v[64:67], v[212:215], v[152:155]\n\t"
            "v_mfma_f32_16x16x32_bf16 v[152:155], v[68:71], v[224:227], v[152:155]\n\t"
            "s_mov_b64 exec, s[68:69]\n\t"
            "ds_write_b16 v202, v203\n\t"
            "s_mov_b64 exec, 1\n\t"
            "ds_add_u32 v253, v246 offset:7856\n\t"
            "s_mov_b64 exec, -1\n\t"
            "v_mfma_f32_16x16x32_bf16 v[156:159], v[92:95], v[224:227], v[156:159]\n\t"
            "s_waitcnt lgkmcnt(4)\n\t"
            "v_mul_f32 v144, v236, v144\n\t"
            "v_mfma_f32_16x16x32_bf16 v[156:159], v[88:91], v[212:215], v[156:159]\n\t"
            "s_cmp_eq_u32 s44, 0\n\t"
            "s_cbranch_scc1 .Lwa_done%=\n\t"
            "v_mul_f32 v145, v236, v145\n\t"
            "v_mul_f32 v152, v236, v152\n\t"
            "v_mfma_f32_16x16x32_bf16 v[160:163], v[112:115], v[212:215], v[160:163]\n\t"
            "v_mul_f32 v153, v236, v153\n\t"
            "v_mul_f32 v146, v236, v146\n\t"
            "v_mfma_f32_16x16x32_bf16 v[160:163], v[116:119], v[224:227], v[160:163]\n\t"
            "v_mul_f32 v147, v236, v147\n\t"
            "v_mul_f32 v154, v236, v154\n\t"
            "v_mfma_f32_16x16x32_bf16 v[164:167], v[140:143], v[224:227], v[164:167]\n\t"
            "ds_read_b128 v[224:227], v233 offset:6272\n\t"
            "v_mul_f32 v155, v236, v155\n\t"
            "v_mfma_f32_16x16x32_bf16 v[164:167], v[136:139], v[212:215], v[164:167]\n\t"
            "ds_read_b128 v[212:215], v232 offset:6272\n\t"
            "v_cvt_pk_bf16_f32 v144, v144, v145\n\t"
            "v_mfma_f32_16x16x32_bf16 v[168:171], v[0:3], v[204:207], 0\n\t"
            "v_cvt_pk_bf16_f32 v152, v152, v153\n\t"
            "v_cvt_pk_bf16_f32 v156, v156, v157\n\t"
            "v_mfma_f32_16x16x32_bf16 v[168:171], v[4:7], v[208:211], v[168:171]\n\t"
            "v_mul_f32 v148, v236, v148\n\t"
            "v_mul_f32 v149, v236, v149\n\t"
            "v_mfma_f32_16x16x32_bf16 v[172:175], v[28:31], v[208:211], 0\n\t"
            "v_mul_f32 v150, v236, v150\n\t"
            "v_mul_f32 v151, v236, v151\n\t"
            "v_mfma_f32_16x16x32_bf16 v[172:175], v[24:27], v[204:207], v[172:175]\n\t"
            "v_cvt_pk_bf16_f32 v145, v146, v147\n\t"
            "v_cvt_pk_bf16_f32 v153, v154, v155\n\t"
            "v_mfma_f32_16x16x32_bf16 v[176:179], v[48:51], v[204:207], 0\n\t"
            "v_cvt_pk_bf16_f32 v157, v158, v159\n\t"
            "v_cvt_pk_bf16_f32 v146, v148, v149\n\t"
            "v_mfma_f32_16x16x32_bf16 v[176:179], v[52:55], v[208:211], v[176:179]\n\t"
            "v_cvt_pk_bf16_f32 v147, v150, v151\n\t"
            "v_cvt_pk_bf16_f32 v158, v160, v161\n\t"
            "v_mfma_f32_16x16x32_bf16 v[180:183], v[76:79], v[208:211], 0\n\t"
            "v_cvt_pk_bf16_f32 v159, v162, v163\n\t"
            "v_cvt_pk_bf16_f32 v154, v164, v165\n\t"
            "v_mfma_f32_16x16x32_bf16 v[180:183], v[72:75], v[204:207], v[180:183]\n\t"
            "v_cvt_pk_bf16_f32 v155, v166, v167\n\t"
            "v_mul_f32 v236, 0x3e553b94, v236\n\t"
            "v_mfma_f32_16x16x32_bf16 v[184:187], v[96:99], v[204:207], 0\n\t"
            "v_mfma_f32_16x16x32_bf16 v[184:187], v[100:103], v[208:211], v[184:187]\n\t"
            "v_mfma_f32_16x16x32_bf16 v[188:191], v[124:127], v[208:211], 0\n\t"
            "ds_read_b128 v[208:211], v233 offset:6400\n\t"
            "v_mfma_f32_16x16x32_bf16 v[188:191], v[120:123], v[204:207], v[188:191]\n\t"
            "ds_read_b128 v[204:207], v232 offset:6400\n\t"
            "s_waitcnt lgkmcnt(2)\n\t"
            "v_mfma_f32_16x16x32_bf16 v[168:171], v[8:11], v[212:215], v[168:171]\n\t"
            "v_mfma_f32_16x16x32_bf16 v[168:171], v[12:15], v[224:227], v[168:171]\n\t"
            "v_mfma_f32_16x16x32_bf16 v[172:175], v[36:39], v[224:227], v[172:175]\n\t"
            "v_mfma_f32_16x16x32_bf16 v[172:175], v[32:35], v[212:215], v[172:175]\n\t"
            "v_mfma_f32_16x16x32_bf16 v[176:179], v[56:59], v[212:215], v[176:179]\n\t"
            "v_mfma_f32_16x16x32_bf16 v[176:179], v[60:63], v[224:227], v[176:179]\n\t"
            "v_mfma_f32_16x16x32_bf16 v[180:183], v[84:87], v[224:227], v[180:183]\n\t"
            "v_mfma_f32_16x16x32_bf16 v[180:183], v[80:83], v[212:215], v[180:183]\n\t"
            "v_mfma_f32_16x16x32_bf16 v[184:187], v[104:107], v[212:215], v[184:187]\n\t"
            "v_mfma_f32_16x16x32_bf16 v[184:187], v[108:111], v[224:227], v[184:187]\n\t"
            "v_mfma_f32_16x16x32_bf16 v[188:191], v[132:135], v[224:227], v[188:191]\n\t"
            "ds_read_b128 v[224:227], v233 offset:12288\n\t"
            "v_mfma_f32_16x16x32_bf16 v[188:191], v[128:131], v[212:215], v[188:191]\n\t"
            "ds_read_b128 v[212:215], v232 offset:12288\n\t"
            "s_waitcnt lgkmcnt(2)\n\t"
            "v_mfma_f32_16x16x32_bf16 v[168:171], v[16:19], v[204:207], v[168:171]\n\t"
            "v_mfma_f32_16x16x32_bf16 v[168:171], v[20:23], v[208:211], v[168:171]\n\t"
            "v_mfma_f32_16x16x32_bf16 v[172:175], v[44:47], v[208:211], v[172:175]\n\t"
            "v_mfma_f32_16x16x32_bf16 v[172:175], v[40:43], v[204:207], v[172:175]\n\t"
            "v_mfma_f32_16x16x32_bf16 v[176:179], v[64:67], v[204:207], v[176:179]\n\t"
            "v_mfma_f32_16x16x32_bf16 v[176:179], v[68:71], v[208:211], v[176:179]\n\t"
            "v_mfma_f32_16x16x32_bf16 v[180:183], v[92:95], v[208:211], v[180:183]\n\t"
            "v_mfma_f32_16x16x32_bf16 v[180:183], v[88:91], v[204:207], v[180:183]\n\t"
            "v_mfma_f32_16x16x32_bf16 v[184:187], v[112:115], v[204:207], v[184:187]\n\t"
            "v_mfma_f32_16x16x32_bf16 v[184:187], v[116:119], v[208:211], v[184:187]\n\t"
            "v_mfma_f32_16x16x32_bf16 v[188:191], v[140:143], v[208:211], v[188:191]\n\t"
            "ds_read_b128 v[208:211], v233 offset:12416\n\t"
            "v_mfma_f32_16x16x32_bf16 v[188:191], v[136:139], v[204:207], v[188:191]\n\t"
            "ds_read_b128 v[204:207], v232 offset:12416\n\t"
            "s_branch .Lwa_loop%=\n\t"
            ".Lwa_done%=:\n\t"
            "s_waitcnt lgkmcnt(0)\n\t"
            "s_nop 7\n\t"
            "s_barrier\n\t"
            : "+{s44}"(s44), "+{s48}"(s48), "+{s70}"(s70), "+{s72}"(s72), "+{s73}"(s73), "+{s74}"(s74), "+{s76}"(s76), "+{s77}"(s77)
            : "{v244}"(pE_), "{v245}"(aO_), "{v246}"(sel_), "{v247}"(aVB_), "{v248}"(pST_), "{v249}"(aRSw_), "{v250}"(aPLw_), "{v251}"(aRSr_), "{v252}"(g16_), "{v253}"(g8_), "{v254}"(loVL_), "{v255}"(loOL_), "{v[240:243]}"(ones4), "{s47}"((ldsb + X_OFF) ^ (ldsb + X1_OFF)),
              "{s[58:59]}"(0xFFFFull), "{s[60:61]}"(0xFFFF0000ull), "{s[62:63]}"(0xFFFF00000000ull), "{s[64:65]}"(0xFFFF000000000000ull),
              "{s[66:67]}"(0x1FFFFFFFFFFFFull), "{s75}"(rsb), "{s45}"(ldsb + V_OFF + w * 4608), "{s[68:69]}"(0xFFFFFFFFFFFFull),
              "{s[84:87]}"(wr), "{s46}"(wave * 36864), "{s88}"(touch), "{s71}"(ldsb + O_OFF), "{s[54:55]}"(mrow), "{s[56:57]}"(0xF0000F0000F0000Full),
              "{s[78:79]}"(mrow & 0xF0000F0000F0000Full)
            : "v144", "v145", "v146", "v147", "v148", "v149", "v150", "v151", "v152", "v153", "v154", "v155", "v156", "v157", "v158", "v159", "v160", "v161", "v162", "v163", "v164", "v165", "v166", "v167", "v168", "v169", "v170", "v171", "v172", "v173", "v174", "v175", "v176", "v177", "v178", "v179", "v180", "v181", "v182", "v183", "v184", "v185", "v186", "v187", "v188", "v189", "v190", "v191", "v192", "v193", "v194", "v195", "v196", "v197", "v198", "v199", "v200", "v201", "v202", "v203", "v204", "v205", "v206", "v207", "v208", "v209", "v210", "v211", "v212", "v213", "v214", "v215", "v216", "v217", "v218", "v219", "v220", "v221", "v222", "v223", "v224", "v225", "v226", "v227", "v228", "v229", "v230", "v231", "v232", "v233", "v234", "v235", "v236", "v237", "v238", "v239", "v0", "v1", "v2", "v3", "v4", "v5", "v6", "v7", "v8", "v9", "v10", "v11", "v12", "v13", "v14", "v15", "v16", "v17", "v18", "v19", "v20", "v21", "v22", "v23", "v24", "v25", "v26", "v27", "v28", "v29", "v30", "v31", "v32", "v33", "v34", "v35", "v36", "v37", "v38", "v39", "v40", "v41", "v42", "v43", "v44", "v45", "v46", "v47", "v48", "v49", "v50", "v51", "v52", "v53", "v54", "v55", "v56", "v57", "v58", "v59", "v60", "v61", "v62", "v63", "v64", "v65", "v66", "v67", "v68", "v69", "v70", "v71", "v72", "v73", "v74", "v75", "v76", "v77", "v78", "v79", "v80", "v81", "v82", "v83", "v84", "v85", "v86", "v87", "v88", "v89", "v90", "v91", "v92", "v93", "v94", "v95", "v96", "v97", "v98", "v99", "v100", "v101", "v102", "v103", "v104", "v105", "v106", "v107", "v108", "v109", "v110", "v111", "v112", "v113", "v114", "v115", "v116", "v117", "v118", "v119", "v120", "v121", "v122", "v123", "v124", "v125", "v126", "v127", "v128", "v129", "v130", "v131", "v132", "v133", "v134", "v135", "v136", "v137", "v138", "v139", "v140", "v141", "v142", "v143",  "m0", "s49", "vcc", "scc", "memory");

        }
        if (wave == 0 && blockIdx.x == 0) {
            int ln_;
            asm volatile("v_mbcnt_lo_u32_b32 %0, -1, 0\n\tv_mbcnt_hi_u32_b32 %0, -1, %0" : "=v"(ln_));
            KARGS
            unsigned long long tg;
            asm volatile("s_load_dwordx2 %0, %1, %2\n\ts_waitcnt lgkmcnt(0)" : "=s"(tg) : "s"(wb), "n"(EPO_OFF) : "memory");
            const unsigned lo = (unsigned)tg + 1u, hi = (unsigned)(tg >> 32);
            const auto wq = __builtin_amdgcn_make_buffer_rsrc((void*)wb, 0, WS_BYTES, 0x00020000);
            for (int it_ = 0; it_ < 100000; ++it_) {
                bool bad = false;
#pragma unroll
                for (int k = 0; k < 8; ++k) {
                    const u32x4 a_ = __builtin_amdgcn_raw_buffer_load_b128(wq, ARR_OFF + ln_ * 16, k * 1024, SC1);
                    bad |= a_[0] != lo || a_[1] != hi || a_[2] != lo || a_[3] != hi;
                }
                if (__builtin_amdgcn_ballot_w64(bad) == 0ull) break;
                __builtin_amdgcn_s_sleep(2);
            }
            if (ln_ == 0) __builtin_amdgcn_raw_buffer_store_b64((u32x2){lo, hi}, wq, EPO_OFF, 0, SC1);
        }
        asm volatile("wk_code_end:");
        __builtin_amdgcn_endpgm();
    } else if (wave < 6) {
        LANE_SETUP
        {
            const unsigned rsb = ldsb + RS_BASE;
            const unsigned pE_ = fE - X_OFF;
            const unsigned aVAw_ = ldsb + V_OFF + w * 9216 + lane * 16, aVBw_ = ldsb + V_OFF + w * 9216 + 1024 + lane * 8;
            const unsigned pST_ = lane * 16, aRSw_ = (lane < 48 ? rsb : ldsb + RS_OFF + 2304 - 192) + lane * 4, aRSr_ = rsb + (lane & 48);
            B_SCALARS
            unsigned q0 = ldsb + O_OFF + pE_, q1 = rsb + l15 * 4, q2 = ldsb + OL_OFF + l15 * 384 + (((4 * w + (g >> 1)) ^ sw) << 4) + 8 * (g & 1);
            asm volatile(
            "s_waitcnt lgkmcnt(0)\n\t"
            "s_nop 7\n\t"
            "s_setprio 3\n\t"
            "s_barrier\n\t"
            "buffer_load_dwordx4 v[140:143], v245, s[28:31], s82 offen sc1\n\t"
            "s_add_u32 s49, s33, 0\n\t"
            "buffer_load_dwordx4 v[0:3], v245, s[28:31], s49 offen sc1\n\t"
            "s_add_u32 s49, s33, 6144\n\t"
            "buffer_load_dwordx4 v[24:27], v245, s[28:31], s49 offen sc1\n\t"
            "s_add_u32 s49, s33, 12288\n\t"
            "buffer_load_dwordx4 v[48:51], v245, s[28:31], s49 offen sc1\n\t"
            "s_add_u32 s49, s33, 18432\n\t"
            "buffer_load_dwordx4 v[72:75], v245, s[28:31], s49 offen sc1\n\t"
            "s_add_u32 s49, s33, 24576\n\t"
            "buffer_load_dwordx4 v[96:99], v245, s[28:31], s49 offen sc1\n\t"
            "s_add_u32 s49, s33, 30720\n\t"
            "buffer_load_dwordx4 v[120:123], v245, s[28:31], s49 offen sc1\n\t"
            "s_add_u32 s49, s33, 1024\n\t"
            "buffer_load_dwordx4 v[4:7], v245, s[28:31], s49 offen sc1\n\t"
            "s_add_u32 s49, s33, 7168\n\t"
            "buffer_load_dwordx4 v[28:31], v245, s[28:31], s49 offen sc1\n\t"
            "s_add_u32 s49, s33, 13312\n\t"
            "buffer_load_dwordx4 v[52:55], v245, s[28:31], s49 offen sc1\n\t"
            "s_add_u32 s49, s33, 19456\n\t"
            "buffer_load_dwordx4 v[76:79], v245, s[28:31], s49 offen sc1\n\t"
            "s_add_u32 s49, s33, 25600\n\t"
            "buffer_load_dwordx4 v[100:103], v245, s[28:31], s49 offen sc1\n\t"
            "s_add_u32 s49, s33, 31744\n\t"
            "buffer_load_dwordx4 v[124:127], v245, s[28:31], s49 offen sc1\n\t"
            "s_add_u32 s49, s33, 2048\n\t"
            "buffer_load_dwordx4 v[8:11], v245, s[28:31], s49 offen sc1\n\t"
            "s_add_u32 s49, s33, 8192\n\t"
            "buffer_load_dwordx4 v[32:35], v245, s[28:31], s49 offen sc1\n\t"
            "s_add_u32 s49, s33, 14336\n\t"
            "buffer_load_dwordx4 v[56:59], v245, s[28:31], s49 offen sc1\n\t"
            "s_add_u32 s49, s33, 20480\n\t"
            "buffer_load_dwordx4 v[80:83], v245, s[28:31], s49 offen sc1\n\t"
            "s_add_u32 s49, s33, 26624\n\t"
            "buffer_load_dwordx4 v[104:107], v245, s[28:31], s49 offen sc1\n\t"
            "s_add_u32 s49, s33, 32768\n\t"
            "buffer_load_dwordx4 v[128:131], v245, s[28:31], s49 offen sc1\n\t"
            "s_add_u32 s49, s33, 3072\n\t"
            "buffer_load_dwordx4 v[12:15], v245, s[28:31], s49 offen sc1\n\t"
            "s_add_u32 s49, s33, 9216\n\t"
            "buffer_load_dwordx4 v[36:39], v245, s[28:31], s49 offen sc1\n\t"
            "s_add_u32 s49, s33, 15360\n\t"
            "buffer_load_dwordx4 v[60:63], v245, s[28:31], s49 offen sc1\n\t"
            "s_add_u32 s49, s33, 21504\n\t"
            "buffer_load_dwordx4 v[84:87], v245, s[28:31], s49 offen sc1\n\t"
            "s_add_u32 s49, s33, 27648\n\t"
            "buffer_load_dwordx4 v[108:111], v245, s[28:31], s49 offen sc1\n\t"
            "s_add_u32 s49, s33, 33792\n\t"
            "buffer_load_dwordx4 v[132:135], v245, s[28:31], s49 offen sc1\n\t"
            "s_add_u32 s49, s33, 4096\n\t"
            "buffer_load_dwordx4 v[16:19], v245, s[28:31], s49 offen sc1\n\t"
            "s_add_u32 s49, s33, 10240\n\t"
            "buffer_load_dwordx4 v[40:43], v245, s[28:31], s49 offen sc1\n\t"
            "s_add_u32 s49, s33, 16384\n\t"
            "buffer_load_dwordx4 v[64:67], v245, s[28:31], s49 offen sc1\n\t"
            "s_add_u32 s49, s33, 22528\n\t"
            "buffer_load_dwordx4 v[88:91], v245, s[28:31], s49 offen sc1\n\t"
            "s_add_u32 s49, s33, 28672\n\t"
            "buffer_load_dwordx4 v[112:115], v245, s[28:31], s49 offen sc1\n\t"
            "s_add_u32 s49, s33, 34816\n\t"
            "buffer_load_dwordx4 v[136:139], v245, s[28:31], s49 offen sc1\n\t"
            "s_add_u32 s49, s33, 5120\n\t"
            "buffer_load_dwordx4 v[20:23], v245, s[28:31], s49 offen sc1\n\t"
            "s_add_u32 s49, s33, 11264\n\t"
            "buffer_load_dwordx4 v[44:47], v245, s[28:31], s49 offen sc1\n\t"
            "s_add_u32 s49, s33, 17408\n\t"
            "buffer_load_dwordx4 v[68:71], v245, s[28:31], s49 offen sc1\n\t"
            "s_add_u32 s49, s33, 23552\n\t"
            "buffer_load_dwordx4 v[92:95], v245, s[28:31], s49 offen sc1\n\t"
            "s_add_u32 s49, s33, 29696\n\t"
            "buffer_load_dwordx4 v[116:119], v245, s[28:31], s49 offen sc1\n\t"
            "s_add_u32 s49, s33, 35840\n\t"
            "buffer_load_dwordx4 v[140:143], v245, s[28:31], s49 offen sc1\n\t"
            "ds_read_b32 v249, v252 offset:1024\n\t"
            "s_waitcnt lgkmcnt(0)\n\t"
            "v_and_b32 v249, s69, v249\n\t"
            "s_mov_b32 exec_hi, 0x3fffff\n\t"
            "s_add_u32 m0, s66, 0\n\t"
            "s_add_u32 s49, s79, 0\n\t"
            "buffer_load_dwordx4 v249, s[72:75], s49 offen nt lds\n\t"
            "s_mov_b32 exec_hi, -1\n\t"
            "s_mov_b32 exec_hi, 0x3fffff\n\t"
            "s_add_u32 m0, s66, 872\n\t"
            "s_add_u32 s49, s47, 196224\n\t"
            "buffer_load_dwordx4 v249, s[72:75], s49 offen nt lds\n\t"
            "s_mov_b32 exec_hi, -1\n\t"
            "s_mov_b32 exec_hi, 0x3fffff\n\t"
            "s_add_u32 m0, s66, 1744\n\t"
            "s_add_u32 s49, s47, 392448\n\t"
            "buffer_load_dwordx4 v249, s[72:75], s49 offen nt lds\n\t"
            "s_mov_b32 exec_hi, -1\n\t"
            "s_mov_b32 exec_hi, 0x3fffff\n\t"
            "s_add_u32 m0, s66, 2616\n\t"
            "s_add_u32 s49, s47, 588672\n\t"
            "buffer_load_dwordx4 v249, s[72:75], s49 offen nt lds\n\t"
            "s_mov_b32 exec_hi, -1\n\t"
            "s_mov_b32 exec_hi, 0x3fffff\n\t"
            "s_add_u32 m0, s66, 3488\n\t"
            "s_add_u32 s49, s47, 784896\n\t"
            "buffer_load_dwordx4 v249, s[72:75], s49 offen nt lds\n\t"
            "s_mov_b32 exec_hi, -1\n\t"
            "s_mov_b32 exec_hi, 0x3fffff\n\t"
            "s_add_u32 m0, s66, 4360\n\t"
            "s_add_u32 s49, s47, 981120\n\t"
            "buffer_load_dwordx4 v249, s[72:75], s49 offen nt lds\n\t"
            "s_mov_b32 exec_hi, -1\n\t"
            "s_mov_b32 exec_hi, 0x3fffff\n\t"
            "s_add_u32 m0, s66, 5232\n\t"
            "s_add_u32 s49, s47, 1177344\n\t"
            "buffer_load_dwordx4 v249, s[72:75], s49 offen nt lds\n\t"
            "s_mov_b32 exec_hi, -1\n\t"
            "s_mov_b32 exec_hi, 0x3fffff\n\t"
            "s_add_u32 m0, s66, 6104\n\t"
            "s_add_u32 s49, s47, 1373568\n\t"
            "buffer_load_dwordx4 v249, s[72:75], s49 offen nt lds\n\t"
            "s_mov_b32 exec_hi, -1\n\t"
            "s_mov_b32 exec_hi, 0x3fffff\n\t"
            "s_add_u32 m0, s66, 6976\n\t"
            "s_add_u32 s49, s47, 1569792\n\t"
            "buffer_load_dwordx4 v249, s[72:75], s49 offen nt lds\n\t"
            "s_mov_b32 exec_hi, -1\n\t"
            "s_setprio 3\n\t"
            "ds_read_b128 v[200:203], v232\n\t"
            "ds_read_b128 v[208:211], v232 offset:128\n\t"
            "ds_read_b128 v[216:219], v232 offset:256\n\t"
            "ds_read_b128 v[192:195], v232 offset:6272\n\t"
            "v_add_u32 v228, s44, v245\n\t"
            "v_xor_b32 v235, 64, v232\n\t"
            "s_setprio 3\n\t"
            "s_waitcnt vmcnt(9)\n\t"
            "s_nop 0\n\t"
            "ds_read_b128 v[224:227], v228 offset:12288\n\t"
            "ds_read_b128 v[228:231], v228 offset:13312\n\t"
            "ds_read_b128 v[204:207], v235\n\t"
            "ds_read_b128 v[212:215], v235 offset:128\n\t"
            "ds_read_b128 v[220:223], v235 offset:256\n\t"
            "ds_read_b128 v[196:199], v235 offset:6272\n\t"
            "v_xor_b32 v248, 32, v234\n\t"
            "s_waitcnt lgkmcnt(5)\n\t"
            "v_add_f32 v224, v224, v226\n\t"
            "s_waitcnt lgkmcnt(4)\n\t"
            "v_add_f32 v228, v228, v230\n\t"
            "s_waitcnt lgkmcnt(3)\n\t"
            "v_mfma_f32_16x16x32_bf16 v[144:147], v[0:3], v[200:203], 0\n\t"
            "v_add_f32 v224, v224, v228\n\t"
            "v_add_f32 v225, v225, v227\n\t"
            "v_mfma_f32_16x16x32_bf16 v[144:147], v[4:7], v[204:207], v[144:147]\n\t"
            "v_add_f32 v229, v229, v231\n\t"
            "v_mul_f32 v224, 0x3baaaaab, v224\n\t"
            "v_mfma_f32_16x16x32_bf16 v[148:151], v[24:27], v[200:203], 0\n\t"
            "v_add_f32 v225, v225, v229\n\t"
            "v_mul_f32_e64 v224, v224, -v224\n\t"
            "v_mfma_f32_16x16x32_bf16 v[148:151], v[28:31], v[204:207], v[148:151]\n\t"
            "ds_read_b128 v[228:231], v235 offset:6144\n\t"
            "v_fmac_f32 v224, 0x3baaaaab, v225\n\t"
            "v_mfma_f32_16x16x32_bf16 v[168:171], v[48:51], v[200:203], 0\n\t"
            "v_max_f32 v224, 0, v224\n\t"
            "v_add_f32 v224, 0x3727c5ac, v224\n\t"
            "v_mfma_f32_16x16x32_bf16 v[168:171], v[52:55], v[204:207], v[168:171]\n\t"
            "v_rsq_f32 v224, v224\n\t"
            "v_mfma_f32_16x16x32_bf16 v[172:175], v[72:75], v[200:203], 0\n\t"
            "v_mfma_f32_16x16x32_bf16 v[172:175], v[76:79], v[204:207], v[172:175]\n\t"
            "ds_write_b32 v246, v224\n\t"
            "v_mfma_f32_16x16x32_bf16 v[152:155], v[96:99], v[200:203], 0\n\t"
            "ds_read_b128 v[224:227], v232 offset:6144\n\t"
            "ds_read_b32 v236, v233\n\t"
            "v_mfma_f32_16x16x32_bf16 v[152:155], v[100:103], v[204:207], v[152:155]\n\t"
            "ds_read_b32 v237, v233 offset:64\n\t"
            "v_mfma_f32_16x16x32_bf16 v[156:159], v[120:123], v[200:203], 0\n\t"
            "v_mfma_f32_16x16x32_bf16 v[156:159], v[124:127], v[204:207], v[156:159]\n\t"
            "s_waitcnt lgkmcnt(7)\n\t"
            "v_mfma_f32_16x16x32_bf16 v[144:147], v[8:11], v[208:211], v[144:147]\n\t"
            "v_mfma_f32_16x16x32_bf16 v[144:147], v[12:15], v[212:215], v[144:147]\n\t"
            "v_mfma_f32_16x16x32_bf16 v[148:151], v[32:35], v[208:211], v[148:151]\n\t"
            "v_mfma_f32_16x16x32_bf16 v[148:151], v[36:39], v[212:215], v[148:151]\n\t"
            "v_mfma_f32_16x16x32_bf16 v[168:171], v[56:59], v[208:211], v[168:171]\n\t"
            "v_mfma_f32_16x16x32_bf16 v[168:171], v[60:63], v[212:215], v[168:171]\n\t"
            "s_waitcnt lgkmcnt(2)\n\t"
            "v_mfma_f32_16x16x32_bf16 v[160:163], v[0:3], v[224:227], 0\n\t"
            "v_mfma_f32_16x16x32_bf16 v[160:163], v[4:7], v[228:231], v[160:163]\n\t"
            "v_mfma_f32_16x16x32_bf16 v[164:167], v[24:27], v[224:227], 0\n\t"
            "v_mfma_f32_16x16x32_bf16 v[164:167], v[28:31], v[228:231], v[164:167]\n\t"
            "v_mfma_f32_16x16x32_bf16 v[176:179], v[48:51], v[224:227], 0\n\t"
            "v_mfma_f32_16x16x32_bf16 v[176:179], v[52:55], v[228:231], v[176:179]\n\t"
            "v_mfma_f32_16x16x32_bf16 v[180:183], v[72:75], v[224:227], 0\n\t"
            "v_mfma_f32_16x16x32_bf16 v[180:183], v[76:79], v[228:231], v[180:183]\n\t"
            "v_mfma_f32_16x16x32_bf16 v[184:187], v[96:99], v[224:227], 0\n\t"
            "v_mfma_f32_16x16x32_bf16 v[184:187], v[100:103], v[228:231], v[184:187]\n\t"
            "v_mfma_f32_16x16x32_bf16 v[188:191], v[120:123], v[224:227], 0\n\t"
            "ds_read_b128 v[224:227], v232 offset:6400\n\t"
            "v_add_u32 v232, s48, v242\n\t"
            "v_mfma_f32_16x16x32_bf16 v[188:191], v[124:127], v[228:231], v[188:191]\n\t"
            "ds_read_b128 v[228:231], v235 offset:6400\n\t"
            "v_xor_b32 v233, 64, v232\n\t"
            "v_mfma_f32_16x16x32_bf16 v[172:175], v[80:83], v[208:211], v[172:175]\n\t"
            "v_mfma_f32_16x16x32_bf16 v[172:175], v[84:87], v[212:215], v[172:175]\n\t"
            "v_mfma_f32_16x16x32_bf16 v[152:155], v[104:107], v[208:211], v[152:155]\n\t"
            "v_mfma_f32_16x16x32_bf16 v[152:155], v[108:111], v[212:215], v[152:155]\n\t"
            "v_mfma_f32_16x16x32_bf16 v[156:159], v[128:131], v[208:211], v[156:159]\n\t"
            "v_mfma_f32_16x16x32_bf16 v[156:159], v[132:135], v[212:215], v[156:159]\n\t"
            "v_mfma_f32_16x16x32_bf16 v[144:147], v[16:19], v[216:219], v[144:147]\n\t"
            "v_mfma_f32_16x16x32_bf16 v[144:147], v[20:23], v[220:223], v[144:147]\n\t"
            "v_mfma_f32_16x16x32_bf16 v[148:151], v[40:43], v[216:219], v[148:151]\n\t"
            "v_mfma_f32_16x16x32_bf16 v[148:151], v[44:47], v[220:223], v[148:151]\n\t"
            "v_mfma_f32_16x16x32_bf16 v[168:171], v[64:67], v[216:219], v[168:171]\n\t"
            "v_mfma_f32_16x16x32_bf16 v[168:171], v[68:71], v[220:223], v[168:171]\n\t"
            "v_mfma_f32_16x16x32_bf16 v[172:175], v[88:91], v[216:219], v[172:175]\n\t"
            "v_mfma_f32_16x16x32_bf16 v[172:175], v[92:95], v[220:223], v[172:175]\n\t"
            "s_waitcnt lgkmcnt(3)\n\t"
            "s_nop 0\n\t"
            "v_mul_f32 v144, v236, v144\n\t"
            "v_mfma_f32_16x16x32_bf16 v[152:155], v[112:115], v[216:219], v[152:155]\n\t"
            "v_mul_f32 v145, v236, v145\n\t"
            "v_mul_f32 v146, v236, v146\n\t"
            "v_mfma_f32_16x16x32_bf16 v[152:155], v[116:119], v[220:223], v[152:155]\n\t"
            "v_mul_f32 v148, v236, v148\n\t"
            "v_mul_f32 v149, v236, v149\n\t"
            "v_mfma_f32_16x16x32_bf16 v[156:159], v[136:139], v[216:219], v[156:159]\n\t"
            "v_mul_f32 v168, v236, v168\n\t"
            "v_mul_f32 v169, v236, v169\n\t"
            "v_mfma_f32_16x16x32_bf16 v[156:159], v[140:143], v[220:223], v[156:159]\n\t"
            "v_mul_f32 v172, v236, v172\n\t"
            "v_mul_f32 v173, v236, v173\n\t"
            "v_mfma_f32_16x16x32_bf16 v[160:163], v[8:11], v[192:195], v[160:163]\n\t"
            "v_mul_f32 v152, v236, v152\n\t"
            "v_mul_f32 v153, v236, v153\n\t"
            "v_mfma_f32_16x16x32_bf16 v[160:163], v[12:15], v[196:199], v[160:163]\n\t"
            "v_mul_f32 v147, v236, v147\n\t"
            "v_cvt_pk_bf16_f32 v144, v144, v145\n\t"
            "v_mfma_f32_16x16x32_bf16 v[164:167], v[32:35], v[192:195], v[164:167]\n\t"
            "v_mul_f32 v156, v236, v156\n\t"
            "v_mul_f32 v157, v236, v157\n\t"
            "v_mfma_f32_16x16x32_bf16 v[164:167], v[36:39], v[196:199], v[164:167]\n\t"
            "v_mul_f32 v150, v236, v150\n\t"
            "v_mul_f32 v151, v236, v151\n\t"
            "v_mfma_f32_16x16x32_bf16 v[176:179], v[56:59], v[192:195], v[176:179]\n\t"
            "v_cvt_pk_bf16_f32 v148, v148, v149\n\t"
            "v_mul_f32 v170, v236, v170\n\t"
            "v_mfma_f32_16x16x32_bf16 v[176:179], v[60:63], v[196:199], v[176:179]\n\t"
            "v_mul_f32 v171, v236, v171\n\t"
            "v_cvt_pk_bf16_f32 v168, v168, v169\n\t"
            "v_mfma_f32_16x16x32_bf16 v[180:183], v[80:83], v[192:195], v[180:183]\n\t"
            "v_mul_f32 v174, v236, v174\n\t"
            "v_mul_f32 v175, v236, v175\n\t"
            "v_mfma_f32_16x16x32_bf16 v[180:183], v[84:87], v[196:199], v[180:183]\n\t"
            "v_cvt_pk_bf16_f32 v172, v172, v173\n\t"
            "v_mul_f32 v154, v236, v154\n\t"
            "v_mfma_f32_16x16x32_bf16 v[184:187], v[104:107], v[192:195], v[184:187]\n\t"
            "v_mul_f32 v155, v236, v155\n\t"
            "v_cvt_pk_bf16_f32 v152, v152, v153\n\t"
            "v_mfma_f32_16x16x32_bf16 v[184:187], v[108:111], v[196:199], v[184:187]\n\t"
            "v_mul_f32 v158, v236, v158\n\t"
            "v_mul_f32 v159, v236, v159\n\t"
            "v_mfma_f32_16x16x32_bf16 v[188:191], v[128:131], v[192:195], v[188:191]\n\t"
            "ds_read_b128 v[192:195], v232 offset:128\n\t"
            "v_cvt_pk_bf16_f32 v156, v156, v157\n\t"
            "v_mfma_f32_16x16x32_bf16 v[188:191], v[132:135], v[196:199], v[188:191]\n\t"
            "ds_read_b128 v[196:199], v233 offset:128\n\t"
            "v_cvt_pk_bf16_f32 v145, v146, v147\n\t"
            "s_waitcnt lgkmcnt(2)\n\t"
            "v_mfma_f32_16x16x32_bf16 v[160:163], v[16:19], v[224:227], v[160:163]\n\t"
            "v_cvt_pk_bf16_f32 v149, v150, v151\n\t"
            "v_cvt_pk_bf16_f32 v169, v170, v171\n\t"
            "v_mfma_f32_16x16x32_bf16 v[160:163], v[20:23], v[228:231], v[160:163]\n\t"
            "v_cvt_pk_bf16_f32 v173, v174, v175\n\t"
            "v_cvt_pk_bf16_f32 v153, v154, v155\n\t"
            "v_mfma_f32_16x16x32_bf16 v[164:167], v[40:43], v[224:227], v[164:167]\n\t"
            "v_cvt_pk_bf16_f32 v157, v158, v159\n\t"
            "v_mfma_f32_16x16x32_bf16 v[164:167], v[44:47], v[228:231], v[164:167]\n\t"
            "s_mov_b64 exec, -1\n\t"
            "ds_write_b64 v234, v[144:145]\n\t"
            "ds_write_b64 v234, v[148:149] offset:128\n\t"
            "ds_write_b64 v234, v[168:169] offset:256\n\t"
            "ds_write_b64 v248, v[172:173]\n\t"
            "ds_write_b64 v248, v[152:153] offset:128\n\t"
            "ds_write_b64 v248, v[156:157] offset:256\n\t"
            "s_mov_b64 exec, -1\n\t"
            "v_mfma_f32_16x16x32_bf16 v[176:179], v[64:67], v[224:227], v[176:179]\n\t"
            "v_mul_f32 v160, v237, v160\n\t"
            "v_mul_f32 v161, v237, v161\n\t"
            "v_mfma_f32_16x16x32_bf16 v[176:179], v[68:71], v[228:231], v[176:179]\n\t"
            "v_mul_f32 v164, v237, v164\n\t"
            "v_mul_f32 v165, v237, v165\n\t"
            "v_mfma_f32_16x16x32_bf16 v[180:183], v[88:91], v[224:227], v[180:183]\n\t"
            "v_mul_f32 v162, v237, v162\n\t"
            "v_mul_f32 v163, v237, v163\n\t"
            "v_mfma_f32_16x16x32_bf16 v[180:183], v[92:95], v[228:231], v[180:183]\n\t"
            "v_cvt_pk_bf16_f32 v160, v160, v161\n\t"
            "v_mul_f32 v166, v237, v166\n\t"
            "v_mfma_f32_16x16x32_bf16 v[184:187], v[112:115], v[224:227], v[184:187]\n\t"
            "v_mul_f32 v176, v237, v176\n\t"
            "v_mul_f32 v177, v237, v177\n\t"
            "v_mfma_f32_16x16x32_bf16 v[184:187], v[116:119], v[228:231], v[184:187]\n\t"
            "v_mul_f32 v167, v237, v167\n\t"
            "v_cvt_pk_bf16_f32 v164, v164, v165\n\t"
            "v_mfma_f32_16x16x32_bf16 v[188:191], v[136:139], v[224:227], v[188:191]\n\t"
            "ds_read_b128 v[224:227], v232\n\t"
            "v_mul_f32 v180, v237, v180\n\t"
            "v_mfma_f32_16x16x32_bf16 v[188:191], v[140:143], v[228:231], v[188:191]\n\t"
            "ds_read_b128 v[228:231], v233\n\t"
            "v_mul_f32 v181, v237, v181\n\t"
            "v_mul_f32 v184, v237, v184\n\t"
            "v_mul_f32 v185, v237, v185\n\t"
            "v_mul_f32 v178, v237, v178\n\t"
            "v_mul_f32 v179, v237, v179\n\t"
            "v_cvt_pk_bf16_f32 v176, v176, v177\n\t"
            "v_mul_f32 v182, v237, v182\n\t"
            "v_mul_f32 v188, v237, v188\n\t"
            "v_mul_f32 v189, v237, v189\n\t"
            "v_mul_f32 v183, v237, v183\n\t"
            "v_cvt_pk_bf16_f32 v180, v180, v181\n\t"
            "v_mul_f32 v186, v237, v186\n\t"
            "v_mul_f32 v187, v237, v187\n\t"
            "v_cvt_pk_bf16_f32 v184, v184, v185\n\t"
            "v_mul_f32 v190, v237, v190\n\t"
            "v_mul_f32 v191, v237, v191\n\t"
            "v_cvt_pk_bf16_f32 v188, v188, v189\n\t"
            "v_cvt_pk_bf16_f32 v161, v162, v163\n\t"
            "v_cvt_pk_bf16_f32 v165, v166, v167\n\t"
            "v_cvt_pk_bf16_f32 v177, v178, v179\n\t"
            "v_cvt_pk_bf16_f32 v181, v182, v183\n\t"
            "v_cvt_pk_bf16_f32 v185, v186, v187\n\t"
            "v_cvt_pk_bf16_f32 v189, v190, v191\n\t"
            "s_waitcnt lgkmcnt(9)\n\t"
            "s_mov_b64 exec, s[80:81]\n\t"
            "ds_write_b64 v234, v[160:161] offset:6144\n\t"
            "ds_write_b64 v234, v[164:165] offset:6272\n\t"
            "ds_write_b64 v234, v[176:177] offset:6400\n\t"
            "ds_write_b64 v248, v[180:181] offset:6144\n\t"
            "ds_write_b64 v248, v[184:185] offset:6272\n\t"
            "ds_write_b64 v248, v[188:189] offset:6400\n\t"
            "s_mov_b64 exec, -1\n\t"
            "s_waitcnt lgkmcnt(6)\n\t"
            "v_mfma_f32_16x16x32_bf16 v[208:211], v[224:227], v[48:51], 0\n\t"
            "v_add_u32 v234, s48, v245\n\t"
            "v_mfma_f32_16x16x32_bf16 v[208:211], v[228:231], v[52:55], v[208:211]\n\t"
            "ds_read_b128 v[200:203], v234 offset:18816\n\t"
            "ds_read_b128 v[204:207], v234 offset:19840\n\t"
            "v_mfma_f32_16x16x32_bf16 v[212:215], v[228:231], v[76:79], 0\n\t"
            "v_mfma_f32_16x16x32_bf16 v[212:215], v[224:227], v[72:75], v[212:215]\n\t"
            "v_mfma_f32_16x16x32_bf16 v[216:219], v[224:227], v[96:99], 0\n\t"
            "v_mfma_f32_16x16x32_bf16 v[216:219], v[228:231], v[100:103], v[216:219]\n\t"
            "v_mfma_f32_16x16x32_bf16 v[220:223], v[228:231], v[124:127], 0\n\t"
            "v_mfma_f32_16x16x32_bf16 v[220:223], v[224:227], v[120:123], v[220:223]\n\t"
            "v_mfma_f32_16x16x32_bf16 v[208:211], v[192:195], v[56:59], v[208:211]\n\t"
            "v_mfma_f32_16x16x32_bf16 v[208:211], v[196:199], v[60:63], v[208:211]\n\t"
            "s_waitcnt lgkmcnt(1)\n\t"
            "v_add_f32 v200, v200, v202\n\t"
            "s_waitcnt lgkmcnt(0)\n\t"
            "v_add_f32 v204, v204, v206\n\t"
            "v_mfma_f32_16x16x32_bf16 v[212:215], v[196:199], v[84:87], v[212:215]\n\t"
            "v_add_f32 v200, v200, v204\n\t"
            "v_add_f32 v201, v201, v203\n\t"
            "v_mfma_f32_16x16x32_bf16 v[212:215], v[192:195], v[80:83], v[212:215]\n\t"
            "v_add_f32 v205, v205, v207\n\t"
            "v_mul_f32 v200, 0x3baaaaab, v200\n\t"
            "v_mfma_f32_16x16x32_bf16 v[216:219], v[192:195], v[104:107], v[216:219]\n\t"
            "v_add_f32 v201, v201, v205\n\t"
            "v_mul_f32_e64 v200, v200, -v200\n\t"
            "v_mfma_f32_16x16x32_bf16 v[216:219], v[196:199], v[108:111], v[216:219]\n\t"
            "v_fmac_f32 v200, 0x3baaaaab, v201\n\t"
            "v_max_f32 v200, 0, v200\n\t"
            "v_mfma_f32_16x16x32_bf16 v[204:207], v[228:231], v[28:31], 0\n\t"
            "v_add_f32 v200, 0x3727c5ac, v200\n\t"
            "v_mfma_f32_16x16x32_bf16 v[204:207], v[224:227], v[24:27], v[204:207]\n\t"
            "v_rsq_f32 v200, v200\n\t"
            "v_mfma_f32_16x16x32_bf16 v[204:207], v[196:199], v[36:39], v[204:207]\n\t"
            "v_mfma_f32_16x16x32_bf16 v[204:207], v[192:195], v[32:35], v[204:207]\n\t"
            "ds_write_b32 v246, v200\n\t"
            "v_mfma_f32_16x16x32_bf16 v[200:203], v[224:227], v[0:3], 0\n\t"
            "ds_read_b128 v[224:227], v232 offset:256\n\t"
            "ds_read_b64 v[236:237], v247\n\t"
            "v_mfma_f32_16x16x32_bf16 v[200:203], v[228:231], v[4:7], v[200:203]\n\t"
            "ds_read_b128 v[228:231], v233 offset:256\n\t"
            "ds_read_b64 v[248:249], v247 offset:8\n\t"
            "v_mfma_f32_16x16x32_bf16 v[200:203], v[192:195], v[8:11], v[200:203]\n\t"
            "v_mfma_f32_16x16x32_bf16 v[200:203], v[196:199], v[12:15], v[200:203]\n\t"
            "v_mfma_f32_16x16x32_bf16 v[220:223], v[196:199], v[132:135], v[220:223]\n\t"
            "ds_read_b128 v[196:199], v233 offset:6144\n\t"
            "v_mfma_f32_16x16x32_bf16 v[220:223], v[192:195], v[128:131], v[220:223]\n\t"
            "ds_read_b128 v[192:195], v232 offset:6144\n\t"
            "s_waitcnt lgkmcnt(3)\n\t"
            "v_mfma_f32_16x16x32_bf16 v[200:203], v[224:227], v[16:19], v[200:203]\n\t"
            "v_mfma_f32_16x16x32_bf16 v[200:203], v[228:231], v[20:23], v[200:203]\n\t"
            "v_mfma_f32_16x16x32_bf16 v[204:207], v[228:231], v[44:47], v[204:207]\n\t"
            "v_mfma_f32_16x16x32_bf16 v[204:207], v[224:227], v[40:43], v[204:207]\n\t"
            "v_mfma_f32_16x16x32_bf16 v[208:211], v[224:227], v[64:67], v[208:211]\n\t"
            "v_mfma_f32_16x16x32_bf16 v[208:211], v[228:231], v[68:71], v[208:211]\n\t"
            "v_mfma_f32_16x16x32_bf16 v[212:215], v[228:231], v[92:95], v[212:215]\n\t"
            "v_mfma_f32_16x16x32_bf16 v[212:215], v[224:227], v[88:91], v[212:215]\n\t"
            "s_nop 1\n\t"
            "v_mul_f32 v200, v236, v200\n\t"
            "v_mfma_f32_16x16x32_bf16 v[216:219], v[224:227], v[112:115], v[216:219]\n\t"
            "v_mul_f32 v201, v237, v201\n\t"
            "s_waitcnt lgkmcnt(2)\n\t"
            "v_mul_f32 v202, v248, v202\n\t"
            "v_mfma_f32_16x16x32_bf16 v[216:219], v[228:231], v[116:119], v[216:219]\n\t"
            "v_mul_f32 v204, v236, v204\n\t"
            "v_mul_f32 v205, v237, v205\n\t"
            "v_mfma_f32_16x16x32_bf16 v[220:223], v[228:231], v[140:143], v[220:223]\n\t"
            "ds_read_b128 v[228:231], v233 offset:6272\n\t"
            "v_mul_f32 v208, v236, v208\n\t"
            "v_mfma_f32_16x16x32_bf16 v[220:223], v[224:227], v[136:139], v[220:223]\n\t"
            "ds_read_b128 v[224:227], v232 offset:6272\n\t"
            "v_mul_f32 v209, v237, v209\n\t"
            "s_waitcnt lgkmcnt(2)\n\t"
            "v_mfma_f32_16x16x32_bf16 v[144:147], v[192:195], v[0:3], 0\n\t"
            "v_mul_f32 v212, v236, v212\n\t"
            "v_mul_f32 v213, v237, v213\n\t"
            "v_mfma_f32_16x16x32_bf16 v[144:147], v[196:199], v[4:7], v[144:147]\n\t"
            "v_mul_f32 v216, v236, v216\n\t"
            "v_mul_f32 v217, v237, v217\n\t"
            "v_mfma_f32_16x16x32_bf16 v[148:151], v[196:199], v[28:31], 0\n\t"
            "v_mul_f32 v220, v236, v220\n\t"
            "v_mul_f32 v221, v237, v221\n\t"
            "v_mfma_f32_16x16x32_bf16 v[148:151], v[192:195], v[24:27], v[148:151]\n\t"
            "ds_read_b64 v[236:237], v247 offset:64\n\t"
            "v_mul_f32 v203, v249, v203\n\t"
            "v_mfma_f32_16x16x32_bf16 v[168:171], v[192:195], v[48:51], 0\n\t"
            "v_mul_f32 v206, v248, v206\n\t"
            "v_mul_f32 v207, v249, v207\n\t"
            "v_mfma_f32_16x16x32_bf16 v[168:171], v[196:199], v[52:55], v[168:171]\n\t"
            "v_mul_f32 v210, v248, v210\n\t"
            "v_mul_f32 v211, v249, v211\n\t"
            "v_mfma_f32_16x16x32_bf16 v[172:175], v[196:199], v[76:79], 0\n\t"
            "v_mul_f32 v214, v248, v214\n\t"
            "v_mul_f32 v215, v249, v215\n\t"
            "v_mfma_f32_16x16x32_bf16 v[172:175], v[192:195], v[72:75], v[172:175]\n\t"
            "v_mul_f32 v218, v248, v218\n\t"
            "v_mul_f32 v219, v249, v219\n\t"
            "v_mfma_f32_16x16x32_bf16 v[152:155], v[192:195], v[96:99], 0\n\t"
            "v_mul_f32 v222, v248, v222\n\t"
            "v_mul_f32 v223, v249, v223\n\t"
            "v_mfma_f32_16x16x32_bf16 v[152:155], v[196:199], v[100:103], v[152:155]\n\t"
            "ds_read_b64 v[248:249], v247 offset:72\n\t"
            "v_cvt_pk_bf16_f32 v200, v200, v201\n\t"
            "v_mfma_f32_16x16x32_bf16 v[156:159], v[196:199], v[124:127], 0\n\t"
            "ds_read_b128 v[196:199], v233 offset:6400\n\t"
            "v_cvt_pk_bf16_f32 v204, v204, v205\n\t"
            "v_mfma_f32_16x16x32_bf16 v[156:159], v[192:195], v[120:123], v[156:159]\n\t"
            "ds_read_b128 v[192:195], v232 offset:6400\n\t"
            "v_cvt_pk_bf16_f32 v208, v208, v209\n\t"
            "s_waitcnt lgkmcnt(4)\n\t"
            "v_mfma_f32_16x16x32_bf16 v[144:147], v[224:227], v[8:11], v[144:147]\n\t"
            "v_cvt_pk_bf16_f32 v201, v202, v203\n\t"
            "v_cvt_pk_bf16_f32 v205, v206, v207\n\t"
            "v_mfma_f32_16x16x32_bf16 v[144:147], v[228:231], v[12:15], v[144:147]\n\t"
            "v_cvt_pk_bf16_f32 v209, v210, v211\n\t"
            "v_cvt_pk_bf16_f32 v212, v212, v213\n\t"
            "v_mfma_f32_16x16x32_bf16 v[148:151], v[228:231], v[36:39], v[148:151]\n\t"
            "v_cvt_pk_bf16_f32 v216, v216, v217\n\t"
            "v_cvt_pk_bf16_f32 v220, v220, v221\n\t"
            "v_mfma_f32_16x16x32_bf16 v[148:151], v[224:227], v[32:35], v[148:151]\n\t"
            "v_cvt_pk_bf16_f32 v213, v214, v215\n\t"
            "v_cvt_pk_bf16_f32 v217, v218, v219\n\t"
            "v_mfma_f32_16x16x32_bf16 v[168:171], v[224:227], v[56:59], v[168:171]\n\t"
            "v_cvt_pk_bf16_f32 v221, v222, v223\n\t"
            "v_mfma_f32_16x16x32_bf16 v[168:171], v[228:231], v[60:63], v[168:171]\n\t"
            "v_mfma_f32_16x16x32_bf16 v[172:175], v[228:231], v[84:87], v[172:175]\n\t"
            "v_mfma_f32_16x16x32_bf16 v[172:175], v[224:227], v[80:83], v[172:175]\n\t"
            "v_mfma_f32_16x16x32_bf16 v[152:155], v[224:227], v[104:107], v[152:155]\n\t"
            "v_mfma_f32_16x16x32_bf16 v[152:155], v[228:231], v[108:111], v[152:155]\n\t"
            "v_mfma_f32_16x16x32_bf16 v[156:159], v[228:231], v[132:135], v[156:159]\n\t"
            "ds_read_b128 v[228:231], v233 offset:12288\n\t"
            "v_mfma_f32_16x16x32_bf16 v[156:159], v[224:227], v[128:131], v[156:159]\n\t"
            "ds_read_b128 v[224:227], v232 offset:12288\n\t"
            "s_waitcnt lgkmcnt(2)\n\t"
            "v_mfma_f32_16x16x32_bf16 v[144:147], v[192:195], v[16:19], v[144:147]\n\t"
            "v_mfma_f32_16x16x32_bf16 v[144:147], v[196:199], v[20:23], v[144:147]\n\t"
            "v_mfma_f32_16x16x32_bf16 v[148:151], v[196:199], v[44:47], v[148:151]\n\t"
            "v_mfma_f32_16x16x32_bf16 v[148:151], v[192:195], v[40:43], v[148:151]\n\t"
            "v_mfma_f32_16x16x32_bf16 v[168:171], v[192:195], v[64:67], v[168:171]\n\t"
            "v_mfma_f32_16x16x32_bf16 v[168:171], v[196:199], v[68:71], v[168:171]\n\t"
            "v_mfma_f32_16x16x32_bf16 v[172:175], v[196:199], v[92:95], v[172:175]\n\t"
            "v_mfma_f32_16x16x32_bf16 v[172:175], v[192:195], v[88:91], v[172:175]\n\t"
            "s_nop 1\n\t"
            "v_mul_f32 v144, v236, v144\n\t"
            "v_mfma_f32_16x16x32_bf16 v[152:155], v[192:195], v[112:115], v[152:155]\n\t"
            "v_mul_f32 v145, v237, v145\n\t"
            "v_mul_f32 v146, v248, v146\n\t"
            "v_mfma_f32_16x16x32_bf16 v[152:155], v[196:199], v[116:119], v[152:155]\n\t"
            "v_mul_f32 v148, v236, v148\n\t"
            "v_mul_f32 v149, v237, v149\n\t"
            "v_mfma_f32_16x16x32_bf16 v[156:159], v[196:199], v[140:143], v[156:159]\n\t"
            "ds_read_b128 v[196:199], v233 offset:12416\n\t"
            "v_mul_f32 v168, v236, v168\n\t"
            "v_mfma_f32_16x16x32_bf16 v[156:159], v[192:195], v[136:139], v[156:159]\n\t"
            "ds_read_b128 v[192:195], v232 offset:12416\n\t"
            "v_mul_f32 v169, v237, v169\n\t"
            "s_waitcnt lgkmcnt(2)\n\t"
            "v_mfma_f32_16x16x32_bf16 v[160:163], v[224:227], v[0:3], 0\n\t"
            "v_mul_f32 v172, v236, v172\n\t"
            "v_mul_f32 v173, v237, v173\n\t"
            "v_mfma_f32_16x16x32_bf16 v[160:163], v[228:231], v[4:7], v[160:163]\n\t"
            "v_mul_f32 v152, v236, v152\n\t"
            "v_mul_f32 v153, v237, v153\n\t"
            "v_mfma_f32_16x16x32_bf16 v[164:167], v[228:231], v[28:31], 0\n\t"
            "v_mul_f32 v156, v236, v156\n\t"
            "v_mul_f32 v157, v237, v157\n\t"
            "v_mfma_f32_16x16x32_bf16 v[164:167], v[224:227], v[24:27], v[164:167]\n\t"
            "ds_read_b64 v[236:237], v247 offset:128\n\t"
            "v_mul_f32 v147, v249, v147\n\t"
            "v_mfma_f32_16x16x32_bf16 v[176:179], v[224:227], v[48:51], 0\n\t"
            "v_mul_f32 v150, v248, v150\n\t"
            "v_mul_f32 v151, v249, v151\n\t"
            "v_mfma_f32_16x16x32_bf16 v[176:179], v[228:231], v[52:55], v[176:179]\n\t"
            "v_mul_f32 v170, v248, v170\n\t"
            "v_mul_f32 v171, v249, v171\n\t"
            "v_mfma_f32_16x16x32_bf16 v[180:183], v[228:231], v[76:79], 0\n\t"
            "v_mul_f32 v174, v248, v174\n\t"
            "v_mul_f32 v175, v249, v175\n\t"
            "v_mfma_f32_16x16x32_bf16 v[180:183], v[224:227], v[72:75], v[180:183]\n\t"
            "v_mul_f32 v154, v248, v154\n\t"
            "v_mul_f32 v155, v249, v155\n\t"
            "v_mfma_f32_16x16x32_bf16 v[184:187], v[224:227], v[96:99], 0\n\t"
            "v_mul_f32 v158, v248, v158\n\t"
            "v_mul_f32 v159, v249, v159\n\t"
            "v_mfma_f32_16x16x32_bf16 v[184:187], v[228:231], v[100:103], v[184:187]\n\t"
            "ds_read_b64 v[248:249], v247 offset:136\n\t"
            "v_cvt_pk_bf16_f32 v202, v144, v145\n\t"
            "v_mfma_f32_16x16x32_bf16 v[188:191], v[228:231], v[124:127], 0\n\t"
            "ds_read_b128 v[228:231], v233 offset:12544\n\t"
            "v_cvt_pk_bf16_f32 v203, v146, v147\n\t"
            "v_mfma_f32_16x16x32_bf16 v[188:191], v[224:227], v[120:123], v[188:191]\n\t"
            "ds_read_b128 v[224:227], v232 offset:12544\n\t"
            "v_cvt_pk_bf16_f32 v206, v148, v149\n\t"
            "s_waitcnt lgkmcnt(4)\n\t"
            "v_mfma_f32_16x16x32_bf16 v[176:179], v[192:195], v[56:59], v[176:179]\n\t"
            "s_waitcnt vmcnt(0)\n\t"
            "ds_read_b64 v[144:145], v253\n\t"
            "ds_read_b64 v[146:147], v253 offset:96\n\t"
            "v_mfma_f32_16x16x32_bf16 v[176:179], v[196:199], v[60:63], v[176:179]\n\t"
            "ds_read_b64 v[148:149], v253 offset:1744\n\t"
            "v_cvt_pk_bf16_f32 v207, v150, v151\n\t"
            "v_mfma_f32_16x16x32_bf16 v[180:183], v[196:199], v[84:87], v[180:183]\n\t"
            "ds_read_b64 v[150:151], v253 offset:1840\n\t"
            "v_cvt_pk_bf16_f32 v210, v168, v169\n\t"
            "v_mfma_f32_16x16x32_bf16 v[180:183], v[192:195], v[80:83], v[180:183]\n\t"
            "v_cvt_pk_bf16_f32 v211, v170, v171\n\t"
            "v_cvt_pk_bf16_f32 v214, v172, v173\n\t"
            "v_mfma_f32_16x16x32_bf16 v[160:163], v[192:195], v[8:11], v[160:163]\n\t"
            "ds_read_b128 v[168:171], v254\n\t"
            "v_cvt_pk_bf16_f32 v215, v174, v175\n\t"
            "v_mfma_f32_16x16x32_bf16 v[160:163], v[196:199], v[12:15], v[160:163]\n\t"
            "ds_read_b128 v[172:175], v254 offset:64\n\t"
            "v_cvt_pk_bf16_f32 v218, v152, v153\n\t"
            "v_mfma_f32_16x16x32_bf16 v[164:167], v[196:199], v[36:39], v[164:167]\n\t"
            "ds_read_b64 v[152:153], v253 offset:8\n\t"
            "v_cvt_pk_bf16_f32 v219, v154, v155\n\t"
            "v_mfma_f32_16x16x32_bf16 v[164:167], v[192:195], v[32:35], v[164:167]\n\t"
            "ds_read_b64 v[154:155], v253 offset:104\n\t"
            "v_cvt_pk_bf16_f32 v222, v156, v157\n\t"
            "s_waitcnt lgkmcnt(8)\n\t"
            "v_mfma_f32_16x16x32_bf16 v[176:179], v[224:227], v[64:67], v[176:179]\n\t"
            "ds_read_b64 v[156:157], v253 offset:1752\n\t"
            "v_cvt_pk_bf16_f32 v223, v158, v159\n\t"
            "v_mfma_f32_16x16x32_bf16 v[176:179], v[228:231], v[68:71], v[176:179]\n\t"
            "ds_read_b64 v[158:159], v253 offset:1848\n\t"
            "ds_write_b128 v243, v[200:203]\n\t"
            "v_mfma_f32_16x16x32_bf16 v[184:187], v[192:195], v[104:107], v[184:187]\n\t"
            "v_mfma_f32_16x16x32_bf16 v[184:187], v[196:199], v[108:111], v[184:187]\n\t"
            "ds_write_b128 v243, v[204:207] offset:1536\n\t"
            "v_mfma_f32_16x16x32_bf16 v[188:191], v[196:199], v[132:135], v[188:191]\n\t"
            "s_nop 1\n\t"
            "v_mul_f32 v176, v236, v176\n\t"
            "v_mfma_f32_16x16x32_bf16 v[188:191], v[192:195], v[128:131], v[188:191]\n\t"
            "v_mul_f32 v177, v237, v177\n\t"
            "v_mul_f32 v178, v248, v178\n\t"
            "v_mfma_f32_16x16x32_bf16 v[180:183], v[228:231], v[92:95], v[180:183]\n\t"
            "v_mul_f32 v179, v249, v179\n\t"
            "v_cvt_pk_bf16_f32 v176, v176, v177\n\t"
            "v_mfma_f32_16x16x32_bf16 v[180:183], v[224:227], v[88:91], v[180:183]\n\t"
            "v_cvt_pk_bf16_f32 v177, v178, v179\n\t"
            "v_mov_b64 v[178:179], 0\n\t"
            "v_mfma_f32_16x16x32_bf16 v[160:163], v[224:227], v[16:19], v[160:163]\n\t"
            "v_mfma_f32_16x16x32_bf16 v[160:163], v[228:231], v[20:23], v[160:163]\n\t"
            "ds_write_b64 v244, v[176:177] offset:3072\n\t"
            "v_mfma_f32_16x16x32_bf16 v[164:167], v[228:231], v[44:47], v[164:167]\n\t"
            "v_mov_b64 v[176:177], 0\n\t"
            "v_mfma_f32_16x16x32_bf16 v[164:167], v[224:227], v[40:43], v[164:167]\n\t"
            "v_mul_f32 v180, v236, v180\n\t"
            "v_mul_f32 v181, v237, v181\n\t"
            "v_mfma_f32_16x16x32_bf16 v[184:187], v[224:227], v[112:115], v[184:187]\n\t"
            "s_nop 0\n\t"
            "v_mul_f32 v160, v236, v160\n\t"
            "v_mul_f32 v161, v237, v161\n\t"
            "v_mfma_f32_16x16x32_bf16 v[184:187], v[228:231], v[116:119], v[184:187]\n\t"
            "v_mul_f32 v182, v248, v182\n\t"
            "v_mul_f32 v183, v249, v183\n\t"
            "v_mfma_f32_16x16x32_bf16 v[188:191], v[228:231], v[140:143], v[188:191]\n\t"
            "v_mul_f32 v164, v236, v164\n\t"
            "v_mul_f32 v165, v237, v165\n\t"
            "v_mfma_f32_16x16x32_bf16 v[188:191], v[224:227], v[136:139], v[188:191]\n\t"
            "v_cvt_pk_bf16_f32 v180, v180, v181\n\t"
            "v_mul_f32 v162, v248, v162\n\t"
            "s_waitcnt lgkmcnt(8)\n\t"
            "v_smfmac_f32_16x16x64_bf16 v[176:179], v[168:171], v[144:151], v255\n\t"
            "ds_read_b128 v[168:171], v254 offset:128\n\t"
            "ds_read_b64 v[144:145], v253 offset:24\n\t"
            "ds_read_b64 v[146:147], v253 offset:120\n\t"
            "ds_read_b64 v[148:149], v253 offset:1768\n\t"
            "ds_read_b64 v[150:151], v253 offset:1864\n\t"
            "v_mul_f32 v163, v249, v163\n\t"
            "v_cvt_pk_bf16_f32 v160, v160, v161\n\t"
            "v_mul_f32 v166, v248, v166\n\t"
            "v_mul_f32 v167, v249, v167\n\t"
            "v_cvt_pk_bf16_f32 v164, v164, v165\n\t"
            "v_cvt_pk_bf16_f32 v181, v182, v183\n\t"
            "v_cvt_pk_bf16_f32 v161, v162, v163\n\t"
            "v_cvt_pk_bf16_f32 v165, v166, v167\n\t"
            "v_mov_b64 v[182:183], 0\n\t"
            "v_mul_f32 v187, v249, v187\n\t"
            "v_mul_f32 v191, v249, v191\n\t"
            "v_mov_b32 v249, s76\n\t"
            "v_mul_f32 v184, v236, v184\n\t"
            "v_mul_f32 v185, v237, v185\n\t"
            "v_mul_f32 v186, v248, v186\n\t"
            "v_cvt_pk_bf16_f32 v184, v184, v185\n\t"
            "v_cvt_pk_bf16_f32 v185, v186, v187\n\t"
            "v_mul_f32 v188, v236, v188\n\t"
            "v_mul_f32 v189, v237, v189\n\t"
            "v_mul_f32 v190, v248, v190\n\t"
            "v_cvt_pk_bf16_f32 v188, v188, v189\n\t"
            "v_mov_b64 v[186:187], 0\n\t"
            "v_cvt_pk_bf16_f32 v189, v190, v191\n\t"
            "v_mov_b64 v[190:191], 0\n\t"
            "v_cvt_pk_bf16_f32 v176, v176, v177\n\t"
            "v_add_u32 v236, s67, v250\n\t"
            "v_cvt_pk_bf16_f32 v177, v178, v179\n\t"
            "v_add_u32 v248, s67, v251\n\t"
            "s_nop 0\n\t"
            "v_mfma_f32_4x4x4_16b_bf16 v[196:199], v[176:177], v[176:177], 0\n\t"
            "ds_read_b64 v[162:163], v253 offset:112\n\t"
            "ds_read_b64 v[166:167], v253 offset:1856\n\t"
            "s_waitcnt lgkmcnt(14)\n\t"
            "ds_read_b32 v249, v249\n\t"
            "s_waitcnt lgkmcnt(14)\n\t"
            "ds_write_b64 v244, v[180:181] offset:4608\n\t"
            "s_waitcnt lgkmcnt(14)\n\t"
            "ds_write_b64 v244, v[160:161]\n\t"
            "s_waitcnt lgkmcnt(14)\n\t"
            "ds_read_b64 v[160:161], v253 offset:16\n\t"
            "s_waitcnt lgkmcnt(14)\n\t"
            "ds_write_b64 v244, v[164:165] offset:1536\n\t"
            "s_waitcnt lgkmcnt(14)\n\t"
            "ds_read_b64 v[164:165], v253 offset:1760\n\t"
            "v_mov_b64 v[180:181], 0\n\t"
            "s_nop 1\n\t"
            "v_smfmac_f32_16x16x64_bf16 v[180:183], v[172:175], v[152:159], v255\n\t"
            "s_waitcnt lgkmcnt(14)\n\t"
            "ds_read_b128 v[172:175], v254 offset:192\n\t"
            "s_waitcnt lgkmcnt(14)\n\t"
            "ds_read_b64 v[152:153], v253 offset:32\n\t"
            "s_waitcnt lgkmcnt(14)\n\t"
            "ds_read_b64 v[154:155], v253 offset:128\n\t"
            "s_waitcnt lgkmcnt(14)\n\t"
            "ds_read_b64 v[156:157], v253 offset:1776\n\t"
            "s_waitcnt lgkmcnt(14)\n\t"
            "ds_read_b64 v[158:159], v253 offset:1872\n\t"
            "s_waitcnt lgkmcnt(14)\n\t"
            "ds_write_b64 v244, v[184:185] offset:6144\n\t"
            "v_mov_b64 v[184:185], 0\n\t"
            "v_cvt_pk_bf16_f32 v178, v180, v181\n\t"
            "v_cvt_pk_bf16_f32 v179, v182, v183\n\t"
            "v_mov_b64 v[180:181], 0\n\t"
            "s_waitcnt lgkmcnt(11)\n\t"
            "v_readfirstlane_b32 s26, v249\n\t"
            "v_mfma_f32_16x16x32_bf16 v[192:195], v[238:241], v[176:179], 0\n\t"
            "ds_read_b32 v249, v252 offset:1024\n\t"
            "ds_write_b64 v244, v[188:189] offset:7680\n\t"
            "v_mfma_f32_4x4x4_16b_bf16 v[196:199], v[178:179], v[178:179], v[196:199]\n\t"
            "v_mov_b64 v[188:189], 0\n\t"
            "s_and_b32 s68, s26, 0x7ffffff\n\t"
            "s_add_u32 s68, s68, s78\n\t"
            "s_and_b32 s69, s26, 0x38000000\n\t"
            "s_or_b32 s69, s69, s39\n\t"
            "s_bitcmp1_b32 s26, 27\n\t"
            "s_cselect_b32 s79, 2.0, s68\n\t"
            "s_waitcnt lgkmcnt(8)\n\t"
            "v_smfmac_f32_16x16x64_bf16 v[184:187], v[168:171], v[160:167], v255\n\t"
            "s_and_b32 s57, s56, 3\n\t"
            "s_lshl_b32 s58, s57, 3\n\t"
            "s_lshr_b32 m0, s54, s58\n\t"
            "s_waitcnt lgkmcnt(7)\n\t"
            "v_smfmac_f32_16x16x64_bf16 v[188:191], v[172:175], v[144:151], v255\n\t"
            "s_lshl_b32 s58, s57, 4\n\t"
            "s_lshr_b64 s[36:37], s[50:51], s58\n\t"
            "s_and_b32 s57, s36, 0xffff\n\t"
            "s_add_u32 s57, s57, s67\n\t"
            "ds_read_b128 v[168:171], v254 offset:256\n\t"
            "ds_read_b64 v[160:161], v253 offset:40\n\t"
            "ds_read_b64 v[162:163], v253 offset:136\n\t"
            "ds_read_b64 v[164:165], v253 offset:1784\n\t"
            "ds_read_b64 v[166:167], v253 offset:1880\n\t"
            "ds_read_b128 v[172:175], v254 offset:320\n\t"
            "ds_read_b64 v[144:145], v253 offset:48\n\t"
            "ds_read_b64 v[146:147], v253 offset:144\n\t"
            "s_waitcnt lgkmcnt(14)\n\t"
            "ds_read_b64 v[148:149], v253 offset:1792\n\t"
            "s_waitcnt lgkmcnt(14)\n\t"
            "ds_read_b64 v[150:151], v253 offset:1888\n\t"
            "s_lshr_b64 s[36:37], s[52:53], s58\n\t"
            "s_and_b32 s58, s36, 0xffff\n\t"
            "s_add_u32 s58, s58, s67\n\t"
            "s_and_b32 s59, s50, 0xffff\n\t"
            "s_add_u32 s59, s59, s67\n\t"
            "s_and_b32 s38, s52, 0xffff\n\t"
            "s_add_u32 s38, s38, s67\n\t"
            "v_writelane_b32 v236, s59, m0\n\t"
            "v_writelane_b32 v236, s57, 63\n\t"
            "v_mov_b64 v[182:183], 0\n\t"
            "v_cvt_pk_bf16_f32 v184, v184, v185\n\t"
            "v_cvt_pk_bf16_f32 v185, v186, v187\n\t"
            "v_xor_b32 v237, 16, v236\n\t"
            "v_cvt_pk_bf16_f32 v186, v188, v189\n\t"
            "v_mfma_f32_4x4x4_16b_bf16 v[196:199], v[184:185], v[184:185], v[196:199]\n\t"
            "s_waitcnt lgkmcnt(14)\n\t"
            "ds_write_b128 v236, v[176:179]\n\t"
            "v_mov_b64 v[176:177], 0\n\t"
            "v_mov_b64 v[178:179], 0\n\t"
            "v_cvt_pk_bf16_f32 v187, v190, v191\n\t"
            "v_mov_b64 v[188:189], 0\n\t"
            "s_waitcnt lgkmcnt(10)\n\t"
            "v_smfmac_f32_16x16x64_bf16 v[176:179], v[168:171], v[152:159], v255\n\t"
            "ds_read_b128 v[168:171], v254 offset:384\n\t"
            "s_waitcnt lgkmcnt(6)\n\t"
            "v_smfmac_f32_16x16x64_bf16 v[180:183], v[172:175], v[160:167], v255\n\t"
            "ds_read_b64 v[152:153], v253 offset:56\n\t"
            "v_mfma_f32_16x16x32_bf16 v[192:195], v[238:241], v[184:187], v[192:195]\n\t"
            "ds_read_b64 v[154:155], v253 offset:152\n\t"
            "ds_read_b64 v[156:157], v253 offset:1800\n\t"
            "v_mfma_f32_4x4x4_16b_bf16 v[196:199], v[186:187], v[186:187], v[196:199]\n\t"
            "ds_read_b64 v[158:159], v253 offset:1896\n\t"
            "ds_read_b128 v[172:175], v254 offset:448\n\t"
            "ds_read_b64 v[160:161], v253 offset:64\n\t"
            "ds_read_b64 v[162:163], v253 offset:160\n\t"
            "ds_read_b64 v[164:165], v253 offset:1808\n\t"
            "ds_read_b64 v[166:167], v253 offset:1904\n\t"
            "s_waitcnt lgkmcnt(14)\n\t"
            "ds_write_b128 v237, v[184:187]\n\t"
            "v_mov_b64 v[184:185], 0\n\t"
            "v_mov_b64 v[186:187], 0\n\t"
            "v_mov_b64 v[190:191], 0\n\t"
            "v_cvt_pk_bf16_f32 v176, v176, v177\n\t"
            "v_cvt_pk_bf16_f32 v177, v178, v179\n\t"
            "v_cvt_pk_bf16_f32 v178, v180, v181\n\t"
            "v_cvt_pk_bf16_f32 v179, v182, v183\n\t"
            "v_mfma_f32_4x4x4_16b_bf16 v[196:199], v[176:177], v[176:177], v[196:199]\n\t"
            "s_waitcnt lgkmcnt(14)\n\t"
            "ds_write_b128 v236, v[176:179] offset:128\n\t"
            "s_waitcnt lgkmcnt(11)\n\t"
            "v_smfmac_f32_16x16x64_bf16 v[184:187], v[168:171], v[144:151], v255\n\t"
            "ds_read_b128 v[168:171], v254 offset:512\n\t"
            "v_mfma_f32_16x16x32_bf16 v[192:195], v[238:241], v[176:179], v[192:195]\n\t"
            "ds_read_b64 v[144:145], v253 offset:72\n\t"
            "ds_read_b64 v[146:147], v253 offset:168\n\t"
            "s_waitcnt lgkmcnt(9)\n\t"
            "v_smfmac_f32_16x16x64_bf16 v[188:191], v[172:175], v[152:159], v255\n\t"
            "ds_read_b64 v[148:149], v253 offset:1816\n\t"
            "v_mfma_f32_4x4x4_16b_bf16 v[196:199], v[178:179], v[178:179], v[196:199]\n\t"
            "ds_read_b64 v[150:151], v253 offset:1912\n\t"
            "ds_read_b128 v[172:175], v254 offset:576\n\t"
            "ds_read_b64 v[152:153], v253 offset:80\n\t"
            "ds_read_b64 v[154:155], v253 offset:176\n\t"
            "ds_read_b64 v[156:157], v253 offset:1824\n\t"
            "s_waitcnt lgkmcnt(14)\n\t"
            "ds_read_b64 v[158:159], v253 offset:1920\n\t"
            "v_mov_b64 v[176:177], 0\n\t"
            "v_mov_b64 v[178:179], 0\n\t"
            "v_mov_b64 v[180:181], 0\n\t"
            "v_mov_b64 v[182:183], 0\n\t"
            "v_cvt_pk_bf16_f32 v184, v184, v185\n\t"
            "v_cvt_pk_bf16_f32 v185, v186, v187\n\t"
            "v_cvt_pk_bf16_f32 v186, v188, v189\n\t"
            "v_cvt_pk_bf16_f32 v187, v190, v191\n\t"
            "v_mfma_f32_4x4x4_16b_bf16 v[196:199], v[184:185], v[184:185], v[196:199]\n\t"
            "s_waitcnt lgkmcnt(14)\n\t"
            "ds_write_b128 v237, v[184:187] offset:128\n\t"
            "v_mfma_f32_16x16x32_bf16 v[192:195], v[238:241], v[184:187], v[192:195]\n\t"
            "v_mov_b64 v[184:185], 0\n\t"
            "v_mov_b64 v[188:189], 0\n\t"
            "s_waitcnt lgkmcnt(10)\n\t"
            "v_smfmac_f32_16x16x64_bf16 v[176:179], v[168:171], v[160:167], v255\n\t"
            "ds_read_b128 v[168:171], v254 offset:640\n\t"
            "v_mfma_f32_4x4x4_16b_bf16 v[196:199], v[186:187], v[186:187], v[196:199]\n\t"
            "ds_read_b64 v[160:161], v253 offset:88\n\t"
            "ds_read_b64 v[162:163], v253 offset:184\n\t"
            "ds_read_b64 v[164:165], v253 offset:1832\n\t"
            "s_waitcnt lgkmcnt(9)\n\t"
            "v_smfmac_f32_16x16x64_bf16 v[180:183], v[172:175], v[144:151], v255\n\t"
            "ds_read_b64 v[166:167], v253 offset:1928\n\t"
            "ds_read_b128 v[172:175], v254 offset:704\n\t"
            "v_mov_b64 v[186:187], 0\n\t"
            "v_cvt_pk_bf16_f32 v176, v176, v177\n\t"
            "v_mov_b64 v[190:191], 0\n\t"
            "v_cvt_pk_bf16_f32 v177, v178, v179\n\t"
            "v_writelane_b32 v248, s38, m0\n\t"
            "v_writelane_b32 v248, s58, 63\n\t"
            "v_mfma_f32_4x4x4_16b_bf16 v[196:199], v[176:177], v[176:177], v[196:199]\n\t"
            "v_cvt_pk_bf16_f32 v178, v180, v181\n\t"
            "v_cvt_pk_bf16_f32 v179, v182, v183\n\t"
            "v_and_b32 v249, s69, v249\n\t"
            "s_nop 0\n\t"
            "v_mfma_f32_4x4x4_16b_bf16 v[196:199], v[178:179], v[178:179], v[196:199]\n\t"
            "ds_write_b128 v243, v[208:211] offset:3072\n\t"
            "v_mfma_f32_16x16x32_bf16 v[192:195], v[238:241], v[176:179], v[192:195]\n\t"
            "ds_write_b128 v243, v[212:215] offset:4608\n\t"
            "s_waitcnt lgkmcnt(7)\n\t"
            "v_smfmac_f32_16x16x64_bf16 v[184:187], v[168:171], v[152:159], v255\n\t"
            "ds_write_b128 v243, v[216:219] offset:6144\n\t"
            "s_waitcnt lgkmcnt(3)\n\t"
            "v_smfmac_f32_16x16x64_bf16 v[188:191], v[172:175], v[160:167], v255\n\t"
            "ds_write_b128 v243, v[220:223] offset:7680\n\t"
            "ds_write_b128 v236, v[176:179] offset:256\n\t"
            "s_mov_b32 exec_hi, 0x3fffff\n\t"
            "s_add_u32 m0, s66, 0\n\t"
            "s_add_u32 s49, s79, 0\n\t"
            "buffer_load_dwordx4 v249, s[72:75], s49 offen nt lds\n\t"
            "s_mov_b32 exec_hi, -1\n\t"
            "v_cvt_pk_bf16_f32 v184, v184, v185\n\t"
            "v_cvt_pk_bf16_f32 v185, v186, v187\n\t"
            "v_cvt_pk_bf16_f32 v186, v188, v189\n\t"
            "v_cvt_pk_bf16_f32 v187, v190, v191\n\t"
            "v_mfma_f32_4x4x4_16b_bf16 v[196:199], v[184:185], v[184:185], v[196:199]\n\t"
            "ds_write_b128 v237, v[184:187] offset:256\n\t"
            "v_mfma_f32_16x16x32_bf16 v[192:195], v[238:241], v[184:187], v[192:195]\n\t"
            "s_mov_b32 exec_hi, 0x3fffff\n\t"
            "s_add_u32 m0, s66, 872\n\t"
            "s_add_u32 s49, s68, 196224\n\t"
            "buffer_load_dwordx4 v249, s[72:75], s49 offen nt lds\n\t"
            "s_mov_b32 exec_hi, -1\n\t"
            "v_mfma_f32_4x4x4_16b_bf16 v[196:199], v[186:187], v[186:187], v[196:199]\n\t"
            "s_mov_b32 exec_hi, 0x3fffff\n\t"
            "s_add_u32 m0, s66, 1744\n\t"
            "s_add_u32 s49, s68, 392448\n\t"
            "buffer_load_dwordx4 v249, s[72:75], s49 offen nt lds\n\t"
            "s_mov_b32 exec_hi, -1\n\t"
            "s_mov_b32 exec_hi, 0x3fffff\n\t"
            "s_add_u32 m0, s66, 2616\n\t"
            "s_add_u32 s49, s68, 588672\n\t"
            "buffer_load_dwordx4 v249, s[72:75], s49 offen nt lds\n\t"
            "s_mov_b32 exec_hi, -1\n\t"
            "v_cndmask_b32_e64 v193, v196, v197, s[60:61]\n\t"
            "v_cndmask_b32_e64 v193, v193, v198, s[62:63]\n\t"
            "v_cndmask_b32_e64 v193, v193, v199, s[64:65]\n\t"
            "ds_write_b64 v248, v[192:193] offset:18816\n\t"
            "s_waitcnt lgkmcnt(0)\n\t"
            "s_barrier\n\t"
            "s_mov_b32 exec_hi, 0x3fffff\n\t"
            "s_add_u32 m0, s66, 3488\n\t"
            "s_add_u32 s49, s68, 784896\n\t"
            "buffer_load_dwordx4 v249, s[72:75], s49 offen nt lds\n\t"
            "s_mov_b32 exec_hi, -1\n\t"
            "s_mov_b32 exec_hi, 0x3fffff\n\t"
            "s_add_u32 m0, s66, 4360\n\t"
            "s_add_u32 s49, s68, 981120\n\t"
            "buffer_load_dwordx4 v249, s[72:75], s49 offen nt lds\n\t"
            "s_mov_b32 exec_hi, -1\n\t"
            "s_mov_b32 exec_hi, 0x3fffff\n\t"
            "s_add_u32 m0, s66, 5232\n\t"
            "s_add_u32 s49, s68, 1177344\n\t"
            "buffer_load_dwordx4 v249, s[72:75], s49 offen nt lds\n\t"
            "s_mov_b32 exec_hi, -1\n\t"
            "s_mov_b32 exec_hi, 0x3fffff\n\t"
            "s_add_u32 m0, s66, 6104\n\t"
            "s_add_u32 s49, s68, 1373568\n\t"
            "buffer_load_dwordx4 v249, s[72:75], s49 offen nt lds\n\t"
            "s_mov_b32 exec_hi, -1\n\t"
            "s_mov_b32 exec_hi, 0x3fffff\n\t"
            "s_add_u32 m0, s66, 6976\n\t"
            "s_add_u32 s49, s68, 1569792\n\t"
            "buffer_load_dwordx4 v249, s[72:75], s49 offen nt lds\n\t"
            "s_mov_b32 exec_hi, -1\n\t"
            "s_add_u32 s45, s46, s27\n\t"
            "s_mov_b32 s46, s47\n\t"
            "s_mov_b32 s47, s68\n\t"
            "s_xor_b32 s48, s48, s71\n\t"
            "s_xor_b32 s67, s67, s71\n\t"
            "s_add_u32 s76, s76, 4\n\t"
            "s_add_u32 s70, s70, 4\n\t"
            "s_lshl_b32 s56, s56, 2\n\t"
            "s_lshr_b32 s49, s26, 30\n\t"
            "s_or_b32 s56, s56, s49\n\t"
            "ds_read_b128 v[168:171], v254\n\t"
            "ds_read_b128 v[172:175], v254 offset:64\n\t"
            "s_waitcnt vmcnt(0)\n\t"
            "ds_read_b64 v[144:145], v253\n\t"
            "ds_read_b64 v[146:147], v253 offset:96\n\t"
            "ds_read_b64 v[148:149], v253 offset:1744\n\t"
            "ds_read_b64 v[150:151], v253 offset:1840\n\t"
            "ds_read_b64 v[152:153], v253 offset:8\n\t"
            "ds_read_b64 v[154:155], v253 offset:104\n\t"
            "ds_read_b64 v[156:157], v253 offset:1752\n\t"
            "ds_read_b64 v[158:159], v253 offset:1848\n\t"
            "ds_read_b64 v[160:161], v253 offset:16\n\t"
            "ds_read_b64 v[162:163], v253 offset:112\n\t"
            "ds_read_b64 v[164:165], v253 offset:1760\n\t"
            "ds_read_b64 v[166:167], v253 offset:1856\n\t"
            "s_waitcnt lgkmcnt(0)\n\t"
            "s_nop 7\n\t"
            ".Lwc_loop%=:\n\t"
            "s_setprio 1\n\t"
            "v_mov_b64 v[176:177], 0\n\t"
            "v_mov_b64 v[178:179], 0\n\t"
            "v_mov_b64 v[180:181], 0\n\t"
            "v_mov_b64 v[182:183], 0\n\t"
            "s_setprio 3\n\t"
            "v_smfmac_f32_16x16x64_bf16 v[176:179], v[168:171], v[144:151], v255\n\t"
            "ds_read_b128 v[168:171], v254 offset:128\n\t"
            "v_smfmac_f32_16x16x64_bf16 v[180:183], v[172:175], v[152:159], v255\n\t"
            "s_setprio 1\n\t"
            "ds_read_b64 v[144:145], v253 offset:24\n\t"
            "ds_read_b64 v[146:147], v253 offset:120\n\t"
            "ds_read_b64 v[148:149], v253 offset:1768\n\t"
            "ds_read_b64 v[150:151], v253 offset:1864\n\t"
            "ds_read_b128 v[172:175], v254 offset:192\n\t"
            "ds_read_b64 v[152:153], v253 offset:32\n\t"
            "ds_read_b64 v[154:155], v253 offset:128\n\t"
            "ds_read_b64 v[156:157], v253 offset:1776\n\t"
            "ds_read_b64 v[158:159], v253 offset:1872\n\t"
            "v_mov_b32 v249, s76\n\t"
            "v_mov_b64 v[184:185], 0\n\t"
            "v_mov_b64 v[186:187], 0\n\t"
            "v_mov_b64 v[188:189], 0\n\t"
            "v_mov_b64 v[190:191], 0\n\t"
            "v_cvt_pk_bf16_f32 v176, v176, v177\n\t"
            "v_add_u32 v236, s67, v250\n\t"
            "v_cvt_pk_bf16_f32 v177, v178, v179\n\t"
            "v_cvt_pk_bf16_f32 v178, v180, v181\n\t"
            "v_cvt_pk_bf16_f32 v179, v182, v183\n\t"
            "s_setprio 3\n\t"
            "v_mfma_f32_4x4x4_16b_bf16 v[196:199], v[176:177], v[176:177], 0\n\t"
            "ds_read_b32 v249, v249\n\t"
            "v_mfma_f32_16x16x32_bf16 v[192:195], v[238:241], v[176:179], 0\n\t"
            "v_mov_b64 v[180:181], 0\n\t"
            "v_mov_b64 v[182:183], 0\n\t"
            "v_mfma_f32_4x4x4_16b_bf16 v[196:199], v[178:179], v[178:179], v[196:199]\n\t"
            "v_add_u32 v234, s48, v245\n\t"
            "s_waitcnt lgkmcnt(10)\n\t"
            "v_smfmac_f32_16x16x64_bf16 v[184:187], v[168:171], v[160:167], v255\n\t"
            "ds_read_b128 v[168:171], v254 offset:256\n\t"
            "ds_read_b64 v[160:161], v253 offset:40\n\t"
            "ds_read_b64 v[162:163], v253 offset:136\n\t"
            "ds_read_b64 v[164:165], v253 offset:1784\n\t"
            "ds_read_b64 v[166:167], v253 offset:1880\n\t"
            "s_waitcnt lgkmcnt(10)\n\t"
            "v_smfmac_f32_16x16x64_bf16 v[188:191], v[172:175], v[144:151], v255\n\t"
            "s_setprio 1\n\t"
            "ds_read_b128 v[172:175], v254 offset:320\n\t"
            "ds_read_b64 v[144:145], v253 offset:48\n\t"
            "ds_read_b64 v[146:147], v253 offset:144\n\t"
            "ds_read_b64 v[148:149], v253 offset:1792\n\t"
            "ds_read_b64 v[150:151], v253 offset:1888\n\t"
            "s_waitcnt lgkmcnt(14)\n\t"
            "ds_read_b128 v[200:203], v234 offset:18816\n\t"
            "s_waitcnt lgkmcnt(14)\n\t"
            "ds_read_b128 v[204:207], v234 offset:19840\n\t"
            "v_cvt_pk_bf16_f32 v184, v184, v185\n\t"
            "v_cvt_pk_bf16_f32 v185, v186, v187\n\t"
            "v_cvt_pk_bf16_f32 v186, v188, v189\n\t"
            "v_cvt_pk_bf16_f32 v187, v190, v191\n\t"
            "s_setprio 3\n\t"
            "v_mfma_f32_4x4x4_16b_bf16 v[196:199], v[184:185], v[184:185], v[196:199]\n\t"
            "v_mov_b64 v[188:189], 0\n\t"
            "v_mfma_f32_16x16x32_bf16 v[192:195], v[238:241], v[184:187], v[192:195]\n\t"
            "s_waitcnt lgkmcnt(12)\n\t"
            "v_readfirstlane_b32 s26, v249\n\t"
            "v_mov_b64 v[190:191], 0\n\t"
            "v_mfma_f32_4x4x4_16b_bf16 v[196:199], v[186:187], v[186:187], v[196:199]\n\t"
            "ds_read_b32 v249, v252 offset:1024\n\t"
            "s_and_b32 s68, s26, 0x7ffffff\n\t"
            "s_add_u32 s68, s68, s78\n\t"
            "s_and_b32 s69, s26, 0x38000000\n\t"
            "s_or_b32 s69, s69, s39\n\t"
            "s_bitcmp1_b32 s26, 27\n\t"
            "s_cselect_b32 s79, 2.0, s68\n\t"
            "s_and_b32 s57, s56, 3\n\t"
            "s_lshl_b32 s58, s57, 3\n\t"
            "s_lshr_b32 m0, s54, s58\n\t"
            "s_lshl_b32 s58, s57, 4\n\t"
            "s_lshr_b64 s[36:37], s[50:51], s58\n\t"
            "s_and_b32 s57, s36, 0xffff\n\t"
            "s_add_u32 s57, s57, s67\n\t"
            "s_waitcnt lgkmcnt(7)\n\t"
            "v_smfmac_f32_16x16x64_bf16 v[180:183], v[172:175], v[160:167], v255\n\t"
            "s_setprio 1\n\t"
            "s_lshr_b64 s[36:37], s[52:53], s58\n\t"
            "s_and_b32 s58, s36, 0xffff\n\t"
            "s_add_u32 s58, s58, s67\n\t"
            "ds_read_b128 v[172:175], v254 offset:448\n\t"
            "ds_read_b64 v[160:161], v253 offset:64\n\t"
            "ds_read_b64 v[162:163], v253 offset:160\n\t"
            "ds_read_b64 v[164:165], v253 offset:1808\n\t"
            "ds_read_b64 v[166:167], v253 offset:1904\n\t"
            "s_and_b32 s59, s50, 0xffff\n\t"
            "s_add_u32 s59, s59, s67\n\t"
            "s_and_b32 s38, s52, 0xffff\n\t"
            "s_add_u32 s38, s38, s67\n\t"
            "v_writelane_b32 v236, s59, m0\n\t"
            "v_writelane_b32 v236, s57, 63\n\t"
            "v_xor_b32 v237, 16, v236\n\t"
            "v_add_u32 v248, s67, v251\n\t"
            "v_writelane_b32 v248, s38, m0\n\t"
            "s_waitcnt lgkmcnt(5)\n\t"
            "v_and_b32 v249, s69, v249\n\t"
            "v_add_u32 v232, s48, v242\n\t"
            "v_xor_b32 v233, 64, v232\n\t"
            "v_add_f32 v200, v200, v202\n\t"
            "v_add_f32 v204, v204, v206\n\t"
            "v_add_f32 v200, v200, v204\n\t"
            "v_add_f32 v201, v201, v203\n\t"
            "v_add_f32 v205, v205, v207\n\t"
            "v_mul_f32 v200, 0x3baaaaab, v200\n\t"
            "v_add_f32 v201, v201, v205\n\t"
            "v_mul_f32_e64 v200, v200, -v200\n\t"
            "v_fmac_f32 v200, 0x3baaaaab, v201\n\t"
            "v_max_f32 v200, 0, v200\n\t"
            "v_writelane_b32 v248, s58, 63\n\t"
            "v_add_f32 v200, 0x3727c5ac, v200\n\t"
            "v_rsq_f32 v200, v200\n\t"
            "ds_read_b128 v[224:227], v232\n\t"
            "ds_read_b128 v[228:231], v233\n\t"
            "ds_write_b128 v236, v[176:179]\n\t"
            "v_mov_b64 v[176:177], 0\n\t"
            "v_mov_b64 v[178:179], 0\n\t"
            "s_setprio 3\n\t"
            "s_nop 0\n\t"
            "v_smfmac_f32_16x16x64_bf16 v[176:179], v[168:171], v[152:159], v255\n\t"
            "s_setprio 1\n\t"
            "ds_read_b128 v[168:171], v254 offset:384\n\t"
            "ds_read_b64 v[152:153], v253 offset:56\n\t"
            "ds_read_b64 v[154:155], v253 offset:152\n\t"
            "ds_read_b64 v[156:157], v253 offset:1800\n\t"
            "ds_read_b64 v[158:159], v253 offset:1896\n\t"
            "ds_write_b128 v237, v[184:187]\n\t"
            "v_mov_b64 v[184:185], 0\n\t"
            "v_mov_b64 v[186:187], 0\n\t"
            "v_cvt_pk_bf16_f32 v176, v176, v177\n\t"
            "v_cvt_pk_bf16_f32 v177, v178, v179\n\t"
            "v_cvt_pk_bf16_f32 v178, v180, v181\n\t"
            "v_cvt_pk_bf16_f32 v179, v182, v183\n\t"
            "s_setprio 3\n\t"
            "v_mfma_f32_4x4x4_16b_bf16 v[196:199], v[176:177], v[176:177], v[196:199]\n\t"
            "ds_write_b128 v236, v[176:179] offset:128\n\t"
            "v_mfma_f32_16x16x32_bf16 v[192:195], v[238:241], v[176:179], v[192:195]\n\t"
            "v_mov_b64 v[176:177], 0\n\t"
            "v_mov_b64 v[180:181], 0\n\t"
            "v_mfma_f32_4x4x4_16b_bf16 v[196:199], v[178:179], v[178:179], v[196:199]\n\t"
            "v_mov_b64 v[178:179], 0\n\t"
            "s_waitcnt lgkmcnt(8)\n\t"
            "v_mfma_f32_16x16x32_bf16 v[204:207], v[228:231], v[28:31], 0\n\t"
            "v_mov_b64 v[182:183], 0\n\t"
            "v_mfma_f32_16x16x32_bf16 v[204:207], v[224:227], v[24:27], v[204:207]\n\t"
            "ds_write_b32 v246, v200\n\t"
            "s_waitcnt lgkmcnt(7)\n\t"
            "v_smfmac_f32_16x16x64_bf16 v[184:187], v[168:171], v[144:151], v255\n\t"
            "ds_read_b128 v[168:171], v254 offset:512\n\t"
            "s_waitcnt lgkmcnt(4)\n\t"
            "v_smfmac_f32_16x16x64_bf16 v[188:191], v[172:175], v[152:159], v255\n\t"
            "ds_read_b64 v[144:145], v253 offset:72\n\t"
            "v_mfma_f32_16x16x32_bf16 v[200:203], v[224:227], v[0:3], 0\n\t"
            "ds_read_b64 v[146:147], v253 offset:168\n\t"
            "ds_read_b64 v[148:149], v253 offset:1816\n\t"
            "v_mfma_f32_16x16x32_bf16 v[200:203], v[228:231], v[4:7], v[200:203]\n\t"
            "ds_read_b64 v[150:151], v253 offset:1912\n\t"
            "ds_read_b128 v[172:175], v254 offset:576\n\t"
            "v_mfma_f32_16x16x32_bf16 v[208:211], v[224:227], v[48:51], 0\n\t"
            "ds_read_b64 v[152:153], v253 offset:80\n\t"
            "ds_read_b64 v[154:155], v253 offset:176\n\t"
            "v_mfma_f32_16x16x32_bf16 v[208:211], v[228:231], v[52:55], v[208:211]\n\t"
            "ds_read_b64 v[156:157], v253 offset:1824\n\t"
            "ds_read_b64 v[158:159], v253 offset:1920\n\t"
            "v_mfma_f32_16x16x32_bf16 v[212:215], v[228:231], v[76:79], 0\n\t"
            "v_cvt_pk_bf16_f32 v184, v184, v185\n\t"
            "v_cvt_pk_bf16_f32 v185, v186, v187\n\t"
            "v_mfma_f32_16x16x32_bf16 v[212:215], v[224:227], v[72:75], v[212:215]\n\t"
            "v_cvt_pk_bf16_f32 v186, v188, v189\n\t"
            "v_cvt_pk_bf16_f32 v187, v190, v191\n\t"
            "v_mfma_f32_4x4x4_16b_bf16 v[196:199], v[184:185], v[184:185], v[196:199]\n\t"
            "ds_write_b128 v237, v[184:187] offset:128\n\t"
            "s_waitcnt lgkmcnt(10)\n\t"
            "v_smfmac_f32_16x16x64_bf16 v[176:179], v[168:171], v[160:167], v255\n\t"
            "ds_read_b128 v[168:171], v254 offset:640\n\t"
            "v_mfma_f32_16x16x32_bf16 v[192:195], v[238:241], v[184:187], v[192:195]\n\t"
            "ds_read_b64 v[160:161], v253 offset:88\n\t"
            "ds_read_b64 v[162:163], v253 offset:184\n\t"
            "s_waitcnt lgkmcnt(8)\n\t"
            "v_smfmac_f32_16x16x64_bf16 v[180:183], v[172:175], v[144:151], v255\n\t"
            "ds_read_b64 v[164:165], v253 offset:1832\n\t"
            "v_mfma_f32_4x4x4_16b_bf16 v[196:199], v[186:187], v[186:187], v[196:199]\n\t"
            "ds_read_b64 v[166:167], v253 offset:1928\n\t"
            "v_mfma_f32_16x16x32_bf16 v[216:219], v[224:227], v[96:99], 0\n\t"
            "ds_read_b128 v[172:175], v254 offset:704\n\t"
            "v_mov_b64 v[184:185], 0\n\t"
            "v_mfma_f32_16x16x32_bf16 v[216:219], v[228:231], v[100:103], v[216:219]\n\t"
            "v_mov_b64 v[186:187], 0\n\t"
            "v_mov_b64 v[188:189], 0\n\t"
            "v_mfma_f32_16x16x32_bf16 v[220:223], v[228:231], v[124:127], 0\n\t"
            "ds_read_b128 v[228:231], v233 offset:256\n\t"
            "v_mov_b64 v[190:191], 0\n\t"
            "v_mfma_f32_16x16x32_bf16 v[220:223], v[224:227], v[120:123], v[220:223]\n\t"
            "s_setprio 1\n\t"
            "ds_read_b128 v[224:227], v232 offset:256\n\t"
            "v_cvt_pk_bf16_f32 v176, v176, v177\n\t"
            "v_cvt_pk_bf16_f32 v177, v178, v179\n\t"
            "v_cvt_pk_bf16_f32 v178, v180, v181\n\t"
            "v_cvt_pk_bf16_f32 v179, v182, v183\n\t"
            "s_setprio 3\n\t"
            "v_mfma_f32_4x4x4_16b_bf16 v[196:199], v[176:177], v[176:177], v[196:199]\n\t"
            "ds_write_b128 v236, v[176:179] offset:256\n\t"
            "s_waitcnt lgkmcnt(8)\n\t"
            "v_smfmac_f32_16x16x64_bf16 v[184:187], v[168:171], v[152:159], v255\n\t"
            "v_mfma_f32_4x4x4_16b_bf16 v[196:199], v[178:179], v[178:179], v[196:199]\n\t"
            "v_mfma_f32_16x16x32_bf16 v[192:195], v[238:241], v[176:179], v[192:195]\n\t"
            "s_waitcnt lgkmcnt(3)\n\t"
            "v_smfmac_f32_16x16x64_bf16 v[188:191], v[172:175], v[160:167], v255\n\t"
            "s_setprio 1\n\t"
            "s_mov_b32 exec_hi, 0x3fffff\n\t"
            "s_add_u32 m0, s66, 0\n\t"
            "s_add_u32 s49, s79, 0\n\t"
            "buffer_load_dwordx4 v249, s[72:75], s49 offen lds\n\t"
            "s_mov_b32 exec_hi, -1\n\t"
            "s_mov_b32 exec_hi, 0x3fffff\n\t"
            "s_add_u32 m0, s66, 872\n\t"
            "s_add_u32 s49, s68, 196224\n\t"
            "buffer_load_dwordx4 v249, s[72:75], s49 offen lds\n\t"
            "s_mov_b32 exec_hi, -1\n\t"
            "v_cvt_pk_bf16_f32 v184, v184, v185\n\t"
            "v_cvt_pk_bf16_f32 v185, v186, v187\n\t"
            "v_cvt_pk_bf16_f32 v186, v188, v189\n\t"
            "v_cvt_pk_bf16_f32 v187, v190, v191\n\t"
            "s_setprio 3\n\t"
            "v_mfma_f32_4x4x4_16b_bf16 v[196:199], v[184:185], v[184:185], v[196:199]\n\t"
            "s_mov_b32 exec_hi, 0x3fffff\n\t"
            "s_add_u32 m0, s66, 1744\n\t"
            "s_add_u32 s49, s68, 392448\n\t"
            "buffer_load_dwordx4 v249, s[72:75], s49 offen lds\n\t"
            "s_mov_b32 exec_hi, -1\n\t"
            "v_mfma_f32_4x4x4_16b_bf16 v[196:199], v[186:187], v[186:187], v[196:199]\n\t"
            "s_mov_b32 exec_hi, 0x3fffff\n\t"
            "s_add_u32 m0, s66, 2616\n\t"
            "s_add_u32 s49, s68, 588672\n\t"
            "buffer_load_dwordx4 v249, s[72:75], s49 offen lds\n\t"
            "s_mov_b32 exec_hi, -1\n\t"
            "v_mfma_f32_16x16x32_bf16 v[192:195], v[238:241], v[184:187], v[192:195]\n\t"
            "s_setprio 1\n\t"
            "s_mov_b32 exec_hi, 0x3fffff\n\t"
            "s_add_u32 m0, s66, 3488\n\t"
            "s_add_u32 s49, s68, 784896\n\t"
            "buffer_load_dwordx4 v249, s[72:75], s49 offen lds\n\t"
            "s_mov_b32 exec_hi, -1\n\t"
            "s_mov_b32 exec_hi, 0x3fffff\n\t"
            "s_add_u32 m0, s66, 4360\n\t"
            "s_add_u32 s49, s68, 981120\n\t"
            "buffer_load_dwordx4 v249, s[72:75], s49 offen lds\n\t"
            "s_mov_b32 exec_hi, -1\n\t"
            "v_cndmask_b32_e64 v193, v196, v197, s[60:61]\n\t"
            "v_cndmask_b32_e64 v193, v193, v198, s[62:63]\n\t"
            "v_cndmask_b32_e64 v193, v193, v199, s[64:65]\n\t"
            "s_setprio 1\n\t"
            "ds_read_b128 v[196:199], v233 offset:128\n\t"
            "ds_write_b64 v248, v[192:193] offset:18816\n\t"
            "ds_read_b128 v[192:195], v232 offset:128\n\t"
            "s_mov_b32 exec_hi, 0x3fffff\n\t"
            "s_add_u32 m0, s66, 5232\n\t"
            "s_add_u32 s49, s68, 1177344\n\t"
            "buffer_load_dwordx4 v249, s[72:75], s49 offen lds\n\t"
            "s_mov_b32 exec_hi, -1\n\t"
            "s_mov_b32 exec_hi, 0x3fffff\n\t"
            "s_add_u32 m0, s66, 6104\n\t"
            "s_add_u32 s49, s68, 1373568\n\t"
            "buffer_load_dwordx4 v249, s[72:75], s49 offen lds\n\t"
            "s_mov_b32 exec_hi, -1\n\t"
            "s_mov_b32 exec_hi, 0x3fffff\n\t"
            "s_add_u32 m0, s66, 6976\n\t"
            "s_add_u32 s49, s68, 1569792\n\t"
            "buffer_load_dwordx4 v249, s[72:75], s49 offen lds\n\t"
            "s_mov_b32 exec_hi, -1\n\t"
            "s_waitcnt lgkmcnt(0)\n\t"
            "v_mfma_f32_16x16x32_bf16 v[200:203], v[192:195], v[8:11], v[200:203]\n\t"
            "ds_read_b64 v[248:249], v247 offset:8\n\t"
            "v_mfma_f32_16x16x32_bf16 v[200:203], v[196:199], v[12:15], v[200:203]\n\t"
            "ds_write_b128 v237, v[184:187] offset:256\n\t"
            "v_mfma_f32_16x16x32_bf16 v[204:207], v[196:199], v[36:39], v[204:207]\n\t"
            "ds_read_b64 v[236:237], v247\n\t"
            "v_mfma_f32_16x16x32_bf16 v[204:207], v[192:195], v[32:35], v[204:207]\n\t"
            "ds_read_b32 v235, v255 offset:7856\n\t"
            "v_mfma_f32_16x16x32_bf16 v[208:211], v[192:195], v[56:59], v[208:211]\n\t"
            "v_mfma_f32_16x16x32_bf16 v[208:211], v[196:199], v[60:63], v[208:211]\n\t"
            "v_mfma_f32_16x16x32_bf16 v[212:215], v[196:199], v[84:87], v[212:215]\n\t"
            "v_mfma_f32_16x16x32_bf16 v[212:215], v[192:195], v[80:83], v[212:215]\n\t"
            "v_mfma_f32_16x16x32_bf16 v[216:219], v[192:195], v[104:107], v[216:219]\n\t"
            "v_mfma_f32_16x16x32_bf16 v[216:219], v[196:199], v[108:111], v[216:219]\n\t"
            "v_mfma_f32_16x16x32_bf16 v[220:223], v[196:199], v[132:135], v[220:223]\n\t"
            "ds_read_b128 v[196:199], v233 offset:6144\n\t"
            "v_mfma_f32_16x16x32_bf16 v[220:223], v[192:195], v[128:131], v[220:223]\n\t"
            "ds_read_b128 v[192:195], v232 offset:6144\n\t"
            "s_waitcnt lgkmcnt(2)\n\t"
            "v_readfirstlane_b32 s77, v235\n\t"
            "s_cmp_ge_u32 s77, s70\n\t"
            "s_cbranch_scc0 .Lwc_spin%=\n\t"
            ".Lwc_go%=:\n\t"
            "v_mfma_f32_16x16x32_bf16 v[200:203], v[224:227], v[16:19], v[200:203]\n\t"
            "s_add_u32 s45, s46, s27\n\t"
            "s_mov_b32 s46, s47\n\t"
            "s_mov_b32 s47, s68\n\t"
            "v_mfma_f32_16x16x32_bf16 v[200:203], v[228:231], v[20:23], v[200:203]\n\t"
            "s_xor_b32 s48, s48, s71\n\t"
            "s_xor_b32 s67, s67, s71\n\t"
            "s_add_u32 s76, s76, 4\n\t"
            "s_add_u32 s70, s70, 4\n\t"
            "v_mfma_f32_16x16x32_bf16 v[204:207], v[228:231], v[44:47], v[204:207]\n\t"
            "s_lshl_b32 s56, s56, 2\n\t"
            "s_lshr_b32 s49, s26, 30\n\t"
            "s_or_b32 s56, s56, s49\n\t"
            "v_mfma_f32_16x16x32_bf16 v[204:207], v[224:227], v[40:43], v[204:207]\n\t"
            "v_mul_f32 v200, v236, v200\n\t"
            "v_mul_f32 v201, v237, v201\n\t"
            "v_mfma_f32_16x16x32_bf16 v[208:211], v[224:227], v[64:67], v[208:211]\n\t"
            "v_mul_f32 v202, v248, v202\n\t"
            "v_mul_f32 v203, v249, v203\n\t"
            "v_mfma_f32_16x16x32_bf16 v[208:211], v[228:231], v[68:71], v[208:211]\n\t"
            "v_cvt_pk_bf16_f32 v200, v200, v201\n\t"
            "v_cvt_pk_bf16_f32 v201, v202, v203\n\t"
            "v_mfma_f32_16x16x32_bf16 v[212:215], v[228:231], v[92:95], v[212:215]\n\t"
            "v_mul_f32 v204, v236, v204\n\t"
            "v_mul_f32 v205, v237, v205\n\t"
            "v_mfma_f32_16x16x32_bf16 v[212:215], v[224:227], v[88:91], v[212:215]\n\t"
            "v_mul_f32 v206, v248, v206\n\t"
            "v_mul_f32 v207, v249, v207\n\t"
            "v_mfma_f32_16x16x32_bf16 v[216:219], v[224:227], v[112:115], v[216:219]\n\t"
            "v_mul_f32 v208, v236, v208\n\t"
            "v_mul_f32 v209, v237, v209\n\t"
            "v_mfma_f32_16x16x32_bf16 v[216:219], v[228:231], v[116:119], v[216:219]\n\t"
            "v_mul_f32 v210, v248, v210\n\t"
            "v_mul_f32 v211, v249, v211\n\t"
            "v_mfma_f32_16x16x32_bf16 v[220:223], v[228:231], v[140:143], v[220:223]\n\t"
            "ds_read_b128 v[228:231], v233 offset:6272\n\t"
            "v_mul_f32 v212, v236, v212\n\t"
            "v_mfma_f32_16x16x32_bf16 v[220:223], v[224:227], v[136:139], v[220:223]\n\t"
            "ds_read_b128 v[224:227], v232 offset:6272\n\t"
            "v_mul_f32 v213, v237, v213\n\t"
            "s_waitcnt lgkmcnt(2)\n\t"
            "v_mfma_f32_16x16x32_bf16 v[144:147], v[192:195], v[0:3], 0\n\t"
            "v_mul_f32 v216, v236, v216\n\t"
            "v_mul_f32 v217, v237, v217\n\t"
            "v_mfma_f32_16x16x32_bf16 v[144:147], v[196:199], v[4:7], v[144:147]\n\t"
            "v_mul_f32 v214, v248, v214\n\t"
            "v_mul_f32 v215, v249, v215\n\t"
            "v_mfma_f32_16x16x32_bf16 v[148:151], v[196:199], v[28:31], 0\n\t"
            "v_mul_f32 v220, v236, v220\n\t"
            "v_mul_f32 v221, v237, v221\n\t"
            "v_mfma_f32_16x16x32_bf16 v[148:151], v[192:195], v[24:27], v[148:151]\n\t"
            "ds_read_b64 v[236:237], v247 offset:64\n\t"
            "v_mul_f32 v218, v248, v218\n\t"
            "v_mfma_f32_16x16x32_bf16 v[168:171], v[192:195], v[48:51], 0\n\t"
            "v_mul_f32 v219, v249, v219\n\t"
            "v_mul_f32 v222, v248, v222\n\t"
            "v_mfma_f32_16x16x32_bf16 v[168:171], v[196:199], v[52:55], v[168:171]\n\t"
            "v_mul_f32 v223, v249, v223\n\t"
            "v_cvt_pk_bf16_f32 v204, v204, v205\n\t"
            "v_mfma_f32_16x16x32_bf16 v[172:175], v[196:199], v[76:79], 0\n\t"
            "ds_read_b64 v[248:249], v247 offset:72\n\t"
            "v_cvt_pk_bf16_f32 v208, v208, v209\n\t"
            "v_mfma_f32_16x16x32_bf16 v[172:175], v[192:195], v[72:75], v[172:175]\n\t"
            "v_cvt_pk_bf16_f32 v212, v212, v213\n\t"
            "v_cvt_pk_bf16_f32 v216, v216, v217\n\t"
            "v_mfma_f32_16x16x32_bf16 v[152:155], v[192:195], v[96:99], 0\n\t"
            "v_cvt_pk_bf16_f32 v220, v220, v221\n\t"
            "v_cvt_pk_bf16_f32 v205, v206, v207\n\t"
            "v_mfma_f32_16x16x32_bf16 v[152:155], v[196:199], v[100:103], v[152:155]\n\t"
            "v_cvt_pk_bf16_f32 v209, v210, v211\n\t"
            "v_cvt_pk_bf16_f32 v213, v214, v215\n\t"
            "v_mfma_f32_16x16x32_bf16 v[156:159], v[196:199], v[124:127], 0\n\t"
            "ds_read_b128 v[196:199], v233 offset:6400\n\t"
            "v_cvt_pk_bf16_f32 v217, v218, v219\n\t"
            "v_mfma_f32_16x16x32_bf16 v[156:159], v[192:195], v[120:123], v[156:159]\n\t"
            "ds_read_b128 v[192:195], v232 offset:6400\n\t"
            "v_cvt_pk_bf16_f32 v221, v222, v223\n\t"
            "s_waitcnt lgkmcnt(4)\n\t"
            "v_mfma_f32_16x16x32_bf16 v[144:147], v[224:227], v[8:11], v[144:147]\n\t"
            "v_mfma_f32_16x16x32_bf16 v[144:147], v[228:231], v[12:15], v[144:147]\n\t"
            "v_mfma_f32_16x16x32_bf16 v[148:151], v[228:231], v[36:39], v[148:151]\n\t"
            "v_mfma_f32_16x16x32_bf16 v[148:151], v[224:227], v[32:35], v[148:151]\n\t"
            "v_mfma_f32_16x16x32_bf16 v[168:171], v[224:227], v[56:59], v[168:171]\n\t"
            "v_mfma_f32_16x16x32_bf16 v[168:171], v[228:231], v[60:63], v[168:171]\n\t"
            "v_mfma_f32_16x16x32_bf16 v[172:175], v[228:231], v[84:87], v[172:175]\n\t"
            "v_mfma_f32_16x16x32_bf16 v[172:175], v[224:227], v[80:83], v[172:175]\n\t"
            "v_mfma_f32_16x16x32_bf16 v[152:155], v[224:227], v[104:107], v[152:155]\n\t"
            "v_mfma_f32_16x16x32_bf16 v[152:155], v[228:231], v[108:111], v[152:155]\n\t"
            "v_mfma_f32_16x16x32_bf16 v[156:159], v[228:231], v[132:135], v[156:159]\n\t"
            "ds_read_b128 v[228:231], v233 offset:12288\n\t"
            "v_mfma_f32_16x16x32_bf16 v[156:159], v[224:227], v[128:131], v[156:159]\n\t"
            "ds_read_b128 v[224:227], v232 offset:12288\n\t"
            "s_waitcnt lgkmcnt(2)\n\t"
            "v_mfma_f32_16x16x32_bf16 v[144:147], v[192:195], v[16:19], v[144:147]\n\t"
            "v_mfma_f32_16x16x32_bf16 v[144:147], v[196:199], v[20:23], v[144:147]\n\t"
            "v_mfma_f32_16x16x32_bf16 v[148:151], v[196:199], v[44:47], v[148:151]\n\t"
            "v_mfma_f32_16x16x32_bf16 v[148:151], v[192:195], v[40:43], v[148:151]\n\t"
            "v_mfma_f32_16x16x32_bf16 v[168:171], v[192:195], v[64:67], v[168:171]\n\t"
            "v_mfma_f32_16x16x32_bf16 v[168:171], v[196:199], v[68:71], v[168:171]\n\t"
            "v_mfma_f32_16x16x32_bf16 v[172:175], v[196:199], v[92:95], v[172:175]\n\t"
            "v_mfma_f32_16x16x32_bf16 v[172:175], v[192:195], v[88:91], v[172:175]\n\t"
            "s_nop 1\n\t"
            "v_mul_f32 v144, v236, v144\n\t"
            "v_mfma_f32_16x16x32_bf16 v[152:155], v[192:195], v[112:115], v[152:155]\n\t"
            "v_mul_f32 v145, v237, v145\n\t"
            "v_mul_f32 v146, v248, v146\n\t"
            "v_mfma_f32_16x16x32_bf16 v[152:155], v[196:199], v[116:119], v[152:155]\n\t"
            "v_mul_f32 v148, v236, v148\n\t"
            "v_mul_f32 v149, v237, v149\n\t"
            "v_mfma_f32_16x16x32_bf16 v[156:159], v[196:199], v[140:143], v[156:159]\n\t"
            "ds_read_b128 v[196:199], v233 offset:12416\n\t"
            "v_mul_f32 v168, v236, v168\n\t"
            "v_mfma_f32_16x16x32_bf16 v[156:159], v[192:195], v[136:139], v[156:159]\n\t"
            "ds_read_b128 v[192:195], v232 offset:12416\n\t"
            "v_mul_f32 v169, v237, v169\n\t"
            "s_waitcnt lgkmcnt(2)\n\t"
            "v_mfma_f32_16x16x32_bf16 v[160:163], v[224:227], v[0:3], 0\n\t"
            "v_mul_f32 v172, v236, v172\n\t"
            "v_mul_f32 v173, v237, v173\n\t"
            "v_mfma_f32_16x16x32_bf16 v[160:163], v[228:231], v[4:7], v[160:163]\n\t"
            "v_mul_f32 v152, v236, v152\n\t"
            "v_mul_f32 v153, v237, v153\n\t"
            "v_mfma_f32_16x16x32_bf16 v[164:167], v[228:231], v[28:31], 0\n\t"
            "v_mul_f32 v156, v236, v156\n\t"
            "v_mul_f32 v157, v237, v157\n\t"
            "v_mfma_f32_16x16x32_bf16 v[164:167], v[224:227], v[24:27], v[164:167]\n\t"
            "ds_read_b64 v[236:237], v247 offset:128\n\t"
            "v_mul_f32 v147, v249, v147\n\t"
            "v_mfma_f32_16x16x32_bf16 v[176:179], v[224:227], v[48:51], 0\n\t"
            "v_mul_f32 v150, v248, v150\n\t"
            "v_mul_f32 v151, v249, v151\n\t"
            "v_mfma_f32_16x16x32_bf16 v[176:179], v[228:231], v[52:55], v[176:179]\n\t"
            "v_mul_f32 v170, v248, v170\n\t"
            "v_mul_f32 v171, v249, v171\n\t"
            "v_mfma_f32_16x16x32_bf16 v[180:183], v[228:231], v[76:79], 0\n\t"
            "v_mul_f32 v174, v248, v174\n\t"
            "v_mul_f32 v175, v249, v175\n\t"
            "v_mfma_f32_16x16x32_bf16 v[180:183], v[224:227], v[72:75], v[180:183]\n\t"
            "v_mul_f32 v154, v248, v154\n\t"
            "v_mul_f32 v155, v249, v155\n\t"
            "v_mfma_f32_16x16x32_bf16 v[184:187], v[224:227], v[96:99], 0\n\t"
            "v_mul_f32 v158, v248, v158\n\t"
            "v_mul_f32 v159, v249, v159\n\t"
            "v_mfma_f32_16x16x32_bf16 v[184:187], v[228:231], v[100:103], v[184:187]\n\t"
            "ds_read_b64 v[248:249], v247 offset:136\n\t"
            "v_cvt_pk_bf16_f32 v202, v144, v145\n\t"
            "v_mfma_f32_16x16x32_bf16 v[188:191], v[228:231], v[124:127], 0\n\t"
            "ds_read_b128 v[228:231], v233 offset:12544\n\t"
            "v_cvt_pk_bf16_f32 v203, v146, v147\n\t"
            "v_mfma_f32_16x16x32_bf16 v[188:191], v[224:227], v[120:123], v[188:191]\n\t"
            "ds_read_b128 v[224:227], v232 offset:12544\n\t"
            "v_cvt_pk_bf16_f32 v206, v148, v149\n\t"
            "s_waitcnt lgkmcnt(4)\n\t"
            "v_mfma_f32_16x16x32_bf16 v[160:163], v[192:195], v[8:11], v[160:163]\n\t"
            "s_waitcnt vmcnt(0)\n\t"
            "ds_read_b64 v[144:145], v253\n\t"
            "ds_read_b64 v[146:147], v253 offset:96\n\t"
            "v_mfma_f32_16x16x32_bf16 v[160:163], v[196:199], v[12:15], v[160:163]\n\t"
            "ds_read_b64 v[148:149], v253 offset:1744\n\t"
            "v_cvt_pk_bf16_f32 v207, v150, v151\n\t"
            "v_mfma_f32_16x16x32_bf16 v[164:167], v[196:199], v[36:39], v[164:167]\n\t"
            "ds_read_b64 v[150:151], v253 offset:1840\n\t"
            "v_cvt_pk_bf16_f32 v210, v168, v169\n\t"
            "v_mfma_f32_16x16x32_bf16 v[164:167], v[192:195], v[32:35], v[164:167]\n\t"
            "v_cvt_pk_bf16_f32 v211, v170, v171\n\t"
            "v_cvt_pk_bf16_f32 v214, v172, v173\n\t"
            "v_mfma_f32_16x16x32_bf16 v[176:179], v[192:195], v[56:59], v[176:179]\n\t"
            "ds_read_b128 v[168:171], v254\n\t"
            "v_cvt_pk_bf16_f32 v215, v174, v175\n\t"
            "v_mfma_f32_16x16x32_bf16 v[176:179], v[196:199], v[60:63], v[176:179]\n\t"
            "ds_read_b128 v[172:175], v254 offset:64\n\t"
            "v_cvt_pk_bf16_f32 v218, v152, v153\n\t"
            "v_mfma_f32_16x16x32_bf16 v[180:183], v[196:199], v[84:87], v[180:183]\n\t"
            "ds_read_b64 v[152:153], v253 offset:8\n\t"
            "v_cvt_pk_bf16_f32 v219, v154, v155\n\t"
            "v_mfma_f32_16x16x32_bf16 v[180:183], v[192:195], v[80:83], v[180:183]\n\t"
            "ds_read_b64 v[154:155], v253 offset:104\n\t"
            "v_cvt_pk_bf16_f32 v222, v156, v157\n\t"
            "v_mfma_f32_16x16x32_bf16 v[184:187], v[192:195], v[104:107], v[184:187]\n\t"
            "ds_read_b64 v[156:157], v253 offset:1752\n\t"
            "v_cvt_pk_bf16_f32 v223, v158, v159\n\t"
            "v_mfma_f32_16x16x32_bf16 v[184:187], v[196:199], v[108:111], v[184:187]\n\t"
            "ds_read_b64 v[158:159], v253 offset:1848\n\t"
            "ds_write_b128 v243, v[200:203]\n\t"
            "v_mfma_f32_16x16x32_bf16 v[188:191], v[196:199], v[132:135], v[188:191]\n\t"
            "v_mfma_f32_16x16x32_bf16 v[188:191], v[192:195], v[128:131], v[188:191]\n\t"
            "s_waitcnt lgkmcnt(14)\n\t"
            "ds_write_b128 v243, v[204:207] offset:1536\n\t"
            "s_waitcnt lgkmcnt(12)\n\t"
            "v_mfma_f32_16x16x32_bf16 v[160:163], v[224:227], v[16:19], v[160:163]\n\t"
            "v_mfma_f32_16x16x32_bf16 v[160:163], v[228:231], v[20:23], v[160:163]\n\t"
            "ds_write_b128 v243, v[208:211] offset:3072\n\t"
            "v_mfma_f32_16x16x32_bf16 v[164:167], v[228:231], v[44:47], v[164:167]\n\t"
            "v_mfma_f32_16x16x32_bf16 v[164:167], v[224:227], v[40:43], v[164:167]\n\t"
            "ds_write_b128 v243, v[212:215] offset:4608\n\t"
            "v_mfma_f32_16x16x32_bf16 v[176:179], v[224:227], v[64:67], v[176:179]\n\t"
            "v_mfma_f32_16x16x32_bf16 v[176:179], v[228:231], v[68:71], v[176:179]\n\t"
            "ds_write_b128 v243, v[216:219] offset:6144\n\t"
            "v_mfma_f32_16x16x32_bf16 v[180:183], v[228:231], v[92:95], v[180:183]\n\t"
            "v_mul_f32 v160, v236, v160\n\t"
            "v_mul_f32 v161, v237, v161\n\t"
            "v_mfma_f32_16x16x32_bf16 v[180:183], v[224:227], v[88:91], v[180:183]\n\t"
            "v_mul_f32 v164, v236, v164\n\t"
            "v_mul_f32 v165, v237, v165\n\t"
            "v_mfma_f32_16x16x32_bf16 v[184:187], v[224:227], v[112:115], v[184:187]\n\t"
            "v_mul_f32 v176, v236, v176\n\t"
            "v_mul_f32 v177, v237, v177\n\t"
            "v_mfma_f32_16x16x32_bf16 v[184:187], v[228:231], v[116:119], v[184:187]\n\t"
            "s_waitcnt lgkmcnt(14)\n\t"
            "ds_write_b128 v243, v[220:223] offset:7680\n\t"
            "v_mfma_f32_16x16x32_bf16 v[188:191], v[228:231], v[140:143], v[188:191]\n\t"
            "v_mul_f32 v180, v236, v180\n\t"
            "v_mul_f32 v181, v237, v181\n\t"
            "v_mfma_f32_16x16x32_bf16 v[188:191], v[224:227], v[136:139], v[188:191]\n\t"
            "v_mul_f32 v162, v248, v162\n\t"
            "v_mul_f32 v163, v249, v163\n\t"
            "v_cvt_pk_bf16_f32 v160, v160, v161\n\t"
            "v_mul_f32 v184, v236, v184\n\t"
            "v_mul_f32 v185, v237, v185\n\t"
            "v_mul_f32 v166, v248, v166\n\t"
            "v_mul_f32 v167, v249, v167\n\t"
            "v_cvt_pk_bf16_f32 v164, v164, v165\n\t"
            "v_mul_f32 v188, v236, v188\n\t"
            "v_mul_f32 v189, v237, v189\n\t"
            "v_mul_f32 v178, v248, v178\n\t"
            "v_mul_f32 v179, v249, v179\n\t"
            "v_cvt_pk_bf16_f32 v176, v176, v177\n\t"
            "v_mul_f32 v182, v248, v182\n\t"
            "v_mul_f32 v183, v249, v183\n\t"
            "v_cvt_pk_bf16_f32 v180, v180, v181\n\t"
            "v_mul_f32 v186, v248, v186\n\t"
            "v_mul_f32 v187, v249, v187\n\t"
            "v_cvt_pk_bf16_f32 v184, v184, v185\n\t"
            "v_mul_f32 v190, v248, v190\n\t"
            "v_mul_f32 v191, v249, v191\n\t"
            "v_cvt_pk_bf16_f32 v188, v188, v189\n\t"
            "v_cvt_pk_bf16_f32 v161, v162, v163\n\t"
            "v_cvt_pk_bf16_f32 v165, v166, v167\n\t"
            "v_cvt_pk_bf16_f32 v177, v178, v179\n\t"
            "v_cvt_pk_bf16_f32 v181, v182, v183\n\t"
            "v_cvt_pk_bf16_f32 v185, v186, v187\n\t"
            "v_cvt_pk_bf16_f32 v189, v190, v191\n\t"
            "s_waitcnt lgkmcnt(14)\n\t"
            "ds_read_b64 v[162:163], v253 offset:112\n\t"
            "s_waitcnt lgkmcnt(14)\n\t"
            "ds_read_b64 v[166:167], v253 offset:1856\n\t"
            "s_waitcnt lgkmcnt(14)\n\t"
            "ds_write_b64 v244, v[160:161]\n\t"
            "s_waitcnt lgkmcnt(14)\n\t"
            "ds_read_b64 v[160:161], v253 offset:16\n\t"
            "s_waitcnt lgkmcnt(14)\n\t"
            "ds_write_b64 v244, v[164:165] offset:1536\n\t"
            "s_waitcnt lgkmcnt(14)\n\t"
            "ds_read_b64 v[164:165], v253 offset:1760\n\t"
            "s_waitcnt lgkmcnt(14)\n\t"
            "ds_write_b64 v244, v[176:177] offset:3072\n\t"
            "s_waitcnt lgkmcnt(14)\n\t"
            "ds_write_b64 v244, v[180:181] offset:4608\n\t"
            "s_waitcnt lgkmcnt(14)\n\t"
            "ds_write_b64 v244, v[184:185] offset:6144\n\t"
            "s_waitcnt lgkmcnt(14)\n\t"
            "ds_write_b64 v244, v[188:189] offset:7680\n\t"
            "s_waitcnt lgkmcnt(0)\n\t"
            "s_barrier\n\t"
            "s_cmp_lt_u32 s46, 0x7000000\n\t"
            "s_cbranch_scc1 .Lwc_loop%=\n\t"
            "s_branch .Lwc_end%=\n\t"
            ".Lwc_spin%=:\n\t"
            "s_sleep 1\n\t"
            "ds_read_b32 v235, v255 offset:7856\n\t"
            "s_waitcnt lgkmcnt(0)\n\t"
            "v_readfirstlane_b32 s77, v235\n\t"
            "s_cmp_ge_u32 s77, s70\n\t"
            "s_cbranch_scc0 .Lwc_spin%=\n\t"
            "s_branch .Lwc_go%=\n\t"
            ".Lwc_end%=:\n\t"
            "s_waitcnt lgkmcnt(0)\n\t"
            "s_nop 7\n\t"
            "s_barrier\n\t"
            : "+{v232}"(q0), "+{v233}"(q1), "+{v234}"(q2), B_SIO
            : "{v242}"(pE_), "{v243}"(aVAw_), "{v244}"(aVBw_), "{v245}"(pST_), "{v246}"(aRSw_), "{v247}"(aRSr_), "{v250}"(pXw_), "{v251}"(pSTw_), "{v252}"(aCA_), "{v253}"(aB_), "{v254}"(aA_), "{v255}"(idx_), B_SIN, "{s44}"(ldsb + O_OFF), "{s[80:81]}"(mrow), "{s82}"(touch)
            : "v144", "v145", "v146", "v147", "v148", "v149", "v150", "v151", "v152", "v153", "v154", "v155", "v156", "v157", "v158", "v159", "v160", "v161", "v162", "v163", "v164", "v165", "v166", "v167", "v168", "v169", "v170", "v171", "v172", "v173", "v174", "v175", "v176", "v177", "v178", "v179", "v180", "v181", "v182", "v183", "v184", "v185", "v186", "v187", "v188", "v189", "v190", "v191", "v192", "v193", "v194", "v195", "v196", "v197", "v198", "v199", "v236", "v237", "v248", "v249", "v0", "v1", "v2", "v3", "v4", "v5", "v6", "v7", "v8", "v9", "v10", "v11", "v12", "v13", "v14", "v15", "v16", "v17", "v18", "v19", "v20", "v21", "v22", "v23", "v24", "v25", "v26", "v27", "v28", "v29", "v30", "v31", "v32", "v33", "v34", "v35", "v36", "v37", "v38", "v39", "v40", "v41", "v42", "v43", "v44", "v45", "v46", "v47", "v48", "v49", "v50", "v51", "v52", "v53", "v54", "v55", "v56", "v57", "v58", "v59", "v60", "v61", "v62", "v63", "v64", "v65", "v66", "v67", "v68", "v69", "v70", "v71", "v72", "v73", "v74", "v75", "v76", "v77", "v78", "v79", "v80", "v81", "v82", "v83", "v84", "v85", "v86", "v87", "v88", "v89", "v90", "v91", "v92", "v93", "v94", "v95", "v96", "v97", "v98", "v99", "v100", "v101", "v102", "v103", "v104", "v105", "v106", "v107", "v108", "v109", "v110", "v111", "v112", "v113", "v114", "v115", "v116", "v117", "v118", "v119", "v120", "v121", "v122", "v123", "v124", "v125", "v126", "v127", "v128", "v129", "v130", "v131", "v132", "v133", "v134", "v135", "v136", "v137", "v138", "v139", "v140", "v141", "v142", "v143", "v235", "v200", "v201", "v202", "v203", "v204", "v205", "v206", "v207", "v208", "v209", "v210", "v211", "v212", "v213", "v214", "v215", "v216", "v217", "v218", "v219", "v220", "v221", "v222", "v223", "v224", "v225", "v226", "v227", "v228", "v229", "v230", "v231", B_SCLOB);
        }
        __builtin_amdgcn_endpgm();
    } else {
        LANE_SETUP
        unsigned t45, t56;
        u32x4 xl[9];
        {
            const unsigned oE_ = ldsb + fE + (O_OFF - X_OFF), oO_ = oE_ ^ 64;
            unsigned l16_ = lane * 16;
            unsigned pr[3];
#pragma unroll
            for (int k = 0; k < 3; ++k) {
                const int c8 = 12 * (w - 2) + 4 * k + g;
                const int ws = c8 / 6, r6 = c8 - 6 * ws;
                pr[k] = l15 * 384 + (r6 >> 1) * 128 + (((2 * ws + (r6 & 1)) ^ sw) << 4);
            }
            const unsigned pR0_ = pr[0], pR1_ = pr[1], pR2_ = pr[2];
#pragma unroll
            for (int lt = 0; lt < 2; ++lt) {
                int wy, wx;
                seq_to_win(start + 16 * lt + l15, wy, wx);
                xl[8][lt] = xl[8][2 + lt] = 16 * lt + l15 < cnt ? win_org(wy, wx) + LPOS(wy) * ROWBYTES + LPOS(wx) * (CH * 2) + 192 * (w - 2) + 16 * g : OOB;
            }
            B_SCALARS
            asm volatile(
            "s_waitcnt lgkmcnt(0)\n\t"
            "s_nop 7\n\t"
            "s_barrier\n\t"
            "s_sleep 16\n\t"
            "ds_read_b32 v249, v252 offset:1024\n\t"
            "s_waitcnt lgkmcnt(0)\n\t"
            "v_and_b32 v249, s69, v249\n\t"
            "s_mov_b32 exec_hi, 0x3fffff\n\t"
            "s_add_u32 m0, s66, 0\n\t"
            "s_add_u32 s49, s79, 0\n\t"
            "buffer_load_dwordx4 v249, s[72:75], s49 offen nt lds\n\t"
            "s_mov_b32 exec_hi, -1\n\t"
            "s_mov_b32 exec_hi, 0x3fffff\n\t"
            "s_add_u32 m0, s66, 872\n\t"
            "s_add_u32 s49, s47, 196224\n\t"
            "buffer_load_dwordx4 v249, s[72:75], s49 offen nt lds\n\t"
            "s_mov_b32 exec_hi, -1\n\t"
            "s_mov_b32 exec_hi, 0x3fffff\n\t"
            "s_add_u32 m0, s66, 1744\n\t"
            "s_add_u32 s49, s47, 392448\n\t"
            "buffer_load_dwordx4 v249, s[72:75], s49 offen nt lds\n\t"
            "s_mov_b32 exec_hi, -1\n\t"
            "s_mov_b32 exec_hi, 0x3fffff\n\t"
            "s_add_u32 m0, s66, 2616\n\t"
            "s_add_u32 s49, s47, 588672\n\t"
            "buffer_load_dwordx4 v249, s[72:75], s49 offen nt lds\n\t"
            "s_mov_b32 exec_hi, -1\n\t"
            "s_mov_b32 exec_hi, 0x3fffff\n\t"
            "s_add_u32 m0, s66, 3488\n\t"
            "s_add_u32 s49, s47, 784896\n\t"
            "buffer_load_dwordx4 v249, s[72:75], s49 offen nt lds\n\t"
            "s_mov_b32 exec_hi, -1\n\t"
            "s_mov_b32 exec_hi, 0x3fffff\n\t"
            "s_add_u32 m0, s66, 4360\n\t"
            "s_add_u32 s49, s47, 981120\n\t"
            "buffer_load_dwordx4 v249, s[72:75], s49 offen nt lds\n\t"
            "s_mov_b32 exec_hi, -1\n\t"
            "s_mov_b32 exec_hi, 0x3fffff\n\t"
            "s_add_u32 m0, s66, 5232\n\t"
            "s_add_u32 s49, s47, 1177344\n\t"
            "buffer_load_dwordx4 v249, s[72:75], s49 offen nt lds\n\t"
            "s_mov_b32 exec_hi, -1\n\t"
            "s_mov_b32 exec_hi, 0x3fffff\n\t"
            "s_add_u32 m0, s66, 6104\n\t"
            "s_add_u32 s49, s47, 1373568\n\t"
            "buffer_load_dwordx4 v249, s[72:75], s49 offen nt lds\n\t"
            "s_mov_b32 exec_hi, -1\n\t"
            "s_mov_b32 exec_hi, 0x3fffff\n\t"
            "s_add_u32 m0, s66, 6976\n\t"
            "s_add_u32 s49, s47, 1569792\n\t"
            "buffer_load_dwordx4 v249, s[72:75], s49 offen nt lds\n\t"
            "s_mov_b32 exec_hi, -1\n\t"
            "s_setprio 1\n\t"
            "ds_read_b128 v[168:171], v254\n\t"
            "ds_read_b128 v[172:175], v254 offset:64\n\t"
            "s_waitcnt vmcnt(0)\n\t"
            "ds_read_b64 v[144:145], v253\n\t"
            "ds_read_b64 v[146:147], v253 offset:96\n\t"
            "ds_read_b64 v[148:149], v253 offset:1744\n\t"
            "ds_read_b64 v[150:151], v253 offset:1840\n\t"
            "ds_read_b64 v[152:153], v253 offset:8\n\t"
            "ds_read_b64 v[154:155], v253 offset:104\n\t"
            "ds_read_b64 v[156:157], v253 offset:1752\n\t"
            "ds_read_b64 v[158:159], v253 offset:1848\n\t"
            "ds_read_b64 v[160:161], v253 offset:16\n\t"
            "ds_read_b64 v[162:163], v253 offset:112\n\t"
            "ds_read_b64 v[164:165], v253 offset:1760\n\t"
            "ds_read_b64 v[166:167], v253 offset:1856\n\t"
            "v_mov_b64 v[176:177], 0\n\t"
            "v_mov_b64 v[178:179], 0\n\t"
            "v_mov_b64 v[180:181], 0\n\t"
            "v_mov_b64 v[182:183], 0\n\t"
            "v_mov_b32 v249, s76\n\t"
            "v_mov_b64 v[184:185], 0\n\t"
            "v_mov_b64 v[186:187], 0\n\t"
            "v_mov_b64 v[188:189], 0\n\t"
            "v_mov_b64 v[190:191], 0\n\t"
            "v_add_u32 v236, s67, v250\n\t"
            "v_add_u32 v248, s67, v251\n\t"
            "s_nop 0\n\t"
            "ds_read_b32 v249, v249\n\t"
            "s_waitcnt lgkmcnt(9)\n\t"
            "v_smfmac_f32_16x16x64_bf16 v[176:179], v[168:171], v[144:151], v255\n\t"
            "ds_read_b128 v[168:171], v254 offset:128\n\t"
            "s_waitcnt lgkmcnt(6)\n\t"
            "v_smfmac_f32_16x16x64_bf16 v[180:183], v[172:175], v[152:159], v255\n\t"
            "ds_read_b64 v[144:145], v253 offset:24\n\t"
            "ds_read_b64 v[146:147], v253 offset:120\n\t"
            "ds_read_b64 v[148:149], v253 offset:1768\n\t"
            "ds_read_b64 v[150:151], v253 offset:1864\n\t"
            "ds_read_b128 v[172:175], v254 offset:192\n\t"
            "ds_read_b64 v[152:153], v253 offset:32\n\t"
            "ds_read_b64 v[154:155], v253 offset:128\n\t"
            "ds_read_b64 v[156:157], v253 offset:1776\n\t"
            "ds_read_b64 v[158:159], v253 offset:1872\n\t"
            "v_cvt_pk_bf16_f32 v176, v176, v177\n\t"
            "v_cvt_pk_bf16_f32 v177, v178, v179\n\t"
            "v_cvt_pk_bf16_f32 v178, v180, v181\n\t"
            "v_cvt_pk_bf16_f32 v179, v182, v183\n\t"
            "v_mfma_f32_4x4x4_16b_bf16 v[196:199], v[176:177], v[176:177], 0\n\t"
            "v_mov_b64 v[180:181], 0\n\t"
            "v_mfma_f32_16x16x32_bf16 v[192:195], v[238:241], v[176:179], 0\n\t"
            "s_waitcnt lgkmcnt(10)\n\t"
            "v_readfirstlane_b32 s26, v249\n\t"
            "v_mov_b64 v[182:183], 0\n\t"
            "v_mfma_f32_4x4x4_16b_bf16 v[196:199], v[178:179], v[178:179], v[196:199]\n\t"
            "ds_read_b32 v249, v252 offset:1024\n\t"
            "s_and_b32 s68, s26, 0x7ffffff\n\t"
            "s_add_u32 s68, s68, s78\n\t"
            "s_and_b32 s69, s26, 0x38000000\n\t"
            "s_or_b32 s69, s69, s39\n\t"
            "s_bitcmp1_b32 s26, 27\n\t"
            "s_cselect_b32 s79, 2.0, s68\n\t"
            "s_waitcnt lgkmcnt(10)\n\t"
            "v_smfmac_f32_16x16x64_bf16 v[184:187], v[168:171], v[160:167], v255\n\t"
            "s_and_b32 s57, s56, 3\n\t"
            "s_lshl_b32 s58, s57, 3\n\t"
            "s_lshr_b32 m0, s54, s58\n\t"
            "ds_read_b128 v[168:171], v254 offset:256\n\t"
            "ds_read_b64 v[160:161], v253 offset:40\n\t"
            "ds_read_b64 v[162:163], v253 offset:136\n\t"
            "ds_read_b64 v[164:165], v253 offset:1784\n\t"
            "s_waitcnt lgkmcnt(9)\n\t"
            "v_smfmac_f32_16x16x64_bf16 v[188:191], v[172:175], v[144:151], v255\n\t"
            "s_lshl_b32 s58, s57, 4\n\t"
            "s_lshr_b64 s[36:37], s[50:51], s58\n\t"
            "s_and_b32 s57, s36, 0xffff\n\t"
            "s_add_u32 s57, s57, s67\n\t"
            "ds_read_b64 v[166:167], v253 offset:1880\n\t"
            "ds_read_b128 v[172:175], v254 offset:320\n\t"
            "ds_read_b64 v[144:145], v253 offset:48\n\t"
            "ds_read_b64 v[146:147], v253 offset:144\n\t"
            "ds_read_b64 v[148:149], v253 offset:1792\n\t"
            "ds_read_b64 v[150:151], v253 offset:1888\n\t"
            "s_lshr_b64 s[36:37], s[52:53], s58\n\t"
            "s_and_b32 s58, s36, 0xffff\n\t"
            "s_add_u32 s58, s58, s67\n\t"
            "s_and_b32 s59, s50, 0xffff\n\t"
            "s_add_u32 s59, s59, s67\n\t"
            "s_and_b32 s38, s52, 0xffff\n\t"
            "s_add_u32 s38, s38, s67\n\t"
            "v_writelane_b32 v236, s59, m0\n\t"
            "v_writelane_b32 v236, s57, 63\n\t"
            "v_cvt_pk_bf16_f32 v184, v184, v185\n\t"
            "v_cvt_pk_bf16_f32 v185, v186, v187\n\t"
            "v_xor_b32 v237, 16, v236\n\t"
            "v_cvt_pk_bf16_f32 v186, v188, v189\n\t"
            "v_mfma_f32_4x4x4_16b_bf16 v[196:199], v[184:185], v[184:185], v[196:199]\n\t"
            "s_waitcnt lgkmcnt(14)\n\t"
            "ds_write_b128 v236, v[176:179]\n\t"
            "v_mov_b64 v[176:177], 0\n\t"
            "v_mov_b64 v[178:179], 0\n\t"
            "v_cvt_pk_bf16_f32 v187, v190, v191\n\t"
            "v_mov_b64 v[188:189], 0\n\t"
            "s_waitcnt lgkmcnt(10)\n\t"
            "v_smfmac_f32_16x16x64_bf16 v[176:179], v[168:171], v[152:159], v255\n\t"
            "ds_read_b128 v[168:171], v254 offset:384\n\t"
            "s_waitcnt lgkmcnt(6)\n\t"
            "v_smfmac_f32_16x16x64_bf16 v[180:183], v[172:175], v[160:167], v255\n\t"
            "ds_read_b64 v[152:153], v253 offset:56\n\t"
            "v_mfma_f32_16x16x32_bf16 v[192:195], v[238:241], v[184:187], v[192:195]\n\t"
            "ds_read_b64 v[154:155], v253 offset:152\n\t"
            "ds_read_b64 v[156:157], v253 offset:1800\n\t"
            "v_mfma_f32_4x4x4_16b_bf16 v[196:199], v[186:187], v[186:187], v[196:199]\n\t"
            "ds_read_b64 v[158:159], v253 offset:1896\n\t"
            "ds_read_b128 v[172:175], v254 offset:448\n\t"
            "ds_read_b64 v[160:161], v253 offset:64\n\t"
            "ds_read_b64 v[162:163], v253 offset:160\n\t"
            "ds_read_b64 v[164:165], v253 offset:1808\n\t"
            "ds_read_b64 v[166:167], v253 offset:1904\n\t"
            "s_waitcnt lgkmcnt(14)\n\t"
            "ds_write_b128 v237, v[184:187]\n\t"
            "v_mov_b64 v[184:185], 0\n\t"
            "v_mov_b64 v[186:187], 0\n\t"
            "v_mov_b64 v[190:191], 0\n\t"
            "v_cvt_pk_bf16_f32 v176, v176, v177\n\t"
            "v_cvt_pk_bf16_f32 v177, v178, v179\n\t"
            "v_cvt_pk_bf16_f32 v178, v180, v181\n\t"
            "v_cvt_pk_bf16_f32 v179, v182, v183\n\t"
            "v_mfma_f32_4x4x4_16b_bf16 v[196:199], v[176:177], v[176:177], v[196:199]\n\t"
            "s_waitcnt lgkmcnt(14)\n\t"
            "ds_write_b128 v236, v[176:179] offset:128\n\t"
            "s_waitcnt lgkmcnt(11)\n\t"
            "v_smfmac_f32_16x16x64_bf16 v[184:187], v[168:171], v[144:151], v255\n\t"
            "ds_read_b128 v[168:171], v254 offset:512\n\t"
            "v_mfma_f32_16x16x32_bf16 v[192:195], v[238:241], v[176:179], v[192:195]\n\t"
            "ds_read_b64 v[144:145], v253 offset:72\n\t"
            "ds_read_b64 v[146:147], v253 offset:168\n\t"
            "s_waitcnt lgkmcnt(9)\n\t"
            "v_smfmac_f32_16x16x64_bf16 v[188:191], v[172:175], v[152:159], v255\n\t"
            "ds_read_b64 v[148:149], v253 offset:1816\n\t"
            "v_mfma_f32_4x4x4_16b_bf16 v[196:199], v[178:179], v[178:179], v[196:199]\n\t"
            "ds_read_b64 v[150:151], v253 offset:1912\n\t"
            "ds_read_b128 v[172:175], v254 offset:576\n\t"
            "ds_read_b64 v[152:153], v253 offset:80\n\t"
            "ds_read_b64 v[154:155], v253 offset:176\n\t"
            "ds_read_b64 v[156:157], v253 offset:1824\n\t"
            "s_waitcnt lgkmcnt(14)\n\t"
            "ds_read_b64 v[158:159], v253 offset:1920\n\t"
            "v_mov_b64 v[176:177], 0\n\t"
            "v_mov_b64 v[178:179], 0\n\t"
            "v_mov_b64 v[180:181], 0\n\t"
            "v_mov_b64 v[182:183], 0\n\t"
            "v_cvt_pk_bf16_f32 v184, v184, v185\n\t"
            "v_cvt_pk_bf16_f32 v185, v186, v187\n\t"
            "v_cvt_pk_bf16_f32 v186, v188, v189\n\t"
            "v_cvt_pk_bf16_f32 v187, v190, v191\n\t"
            "v_mfma_f32_4x4x4_16b_bf16 v[196:199], v[184:185], v[184:185], v[196:199]\n\t"
            "s_waitcnt lgkmcnt(14)\n\t"
            "ds_write_b128 v237, v[184:187] offset:128\n\t"
            "v_mfma_f32_16x16x32_bf16 v[192:195], v[238:241], v[184:187], v[192:195]\n\t"
            "v_mov_b64 v[184:185], 0\n\t"
            "v_mov_b64 v[188:189], 0\n\t"
            "s_waitcnt lgkmcnt(10)\n\t"
            "v_smfmac_f32_16x16x64_bf16 v[176:179], v[168:171], v[160:167], v255\n\t"
            "ds_read_b128 v[168:171], v254 offset:640\n\t"
            "v_mfma_f32_4x4x4_16b_bf16 v[196:199], v[186:187], v[186:187], v[196:199]\n\t"
            "ds_read_b64 v[160:161], v253 offset:88\n\t"
            "ds_read_b64 v[162:163], v253 offset:184\n\t"
            "ds_read_b64 v[164:165], v253 offset:1832\n\t"
            "s_waitcnt lgkmcnt(9)\n\t"
            "v_smfmac_f32_16x16x64_bf16 v[180:183], v[172:175], v[144:151], v255\n\t"
            "ds_read_b64 v[166:167], v253 offset:1928\n\t"
            "ds_read_b128 v[172:175], v254 offset:704\n\t"
            "v_mov_b64 v[186:187], 0\n\t"
            "v_mov_b64 v[190:191], 0\n\t"
            "v_writelane_b32 v248, s38, m0\n\t"
            "v_and_b32 v249, s69, v249\n\t"
            "v_cvt_pk_bf16_f32 v176, v176, v177\n\t"
            "v_cvt_pk_bf16_f32 v177, v178, v179\n\t"
            "v_cvt_pk_bf16_f32 v178, v180, v181\n\t"
            "v_cvt_pk_bf16_f32 v179, v182, v183\n\t"
            "v_mfma_f32_4x4x4_16b_bf16 v[196:199], v[176:177], v[176:177], v[196:199]\n\t"
            "ds_write_b128 v236, v[176:179] offset:256\n\t"
            "v_mfma_f32_16x16x32_bf16 v[192:195], v[238:241], v[176:179], v[192:195]\n\t"
            "v_add_u32 v236, s44, v244\n\t"
            "v_writelane_b32 v248, s58, 63\n\t"
            "v_mfma_f32_4x4x4_16b_bf16 v[196:199], v[178:179], v[178:179], v[196:199]\n\t"
            "ds_read_b128 v[200:203], v236\n\t"
            "ds_read_b128 v[212:215], v236 offset:6144\n\t"
            "s_waitcnt lgkmcnt(8)\n\t"
            "v_smfmac_f32_16x16x64_bf16 v[184:187], v[168:171], v[152:159], v255\n\t"
            "v_add_u32 v236, s48, v244\n\t"
            "ds_read_b128 v[224:227], v236 offset:12288\n\t"
            "s_waitcnt lgkmcnt(4)\n\t"
            "v_smfmac_f32_16x16x64_bf16 v[188:191], v[172:175], v[160:167], v255\n\t"
            "s_mov_b32 exec_hi, 0x3fffff\n\t"
            "s_add_u32 m0, s66, 0\n\t"
            "s_add_u32 s49, s79, 0\n\t"
            "buffer_load_dwordx4 v249, s[72:75], s49 offen nt lds\n\t"
            "s_mov_b32 exec_hi, -1\n\t"
            "s_mov_b32 exec_hi, 0x3fffff\n\t"
            "s_add_u32 m0, s66, 872\n\t"
            "s_add_u32 s49, s68, 196224\n\t"
            "buffer_load_dwordx4 v249, s[72:75], s49 offen nt lds\n\t"
            "s_mov_b32 exec_hi, -1\n\t"
            "s_mov_b32 exec_hi, 0x3fffff\n\t"
            "s_add_u32 m0, s66, 1744\n\t"
            "s_add_u32 s49, s68, 392448\n\t"
            "buffer_load_dwordx4 v249, s[72:75], s49 offen nt lds\n\t"
            "s_mov_b32 exec_hi, -1\n\t"
            "s_mov_b32 exec_hi, 0x3fffff\n\t"
            "s_add_u32 m0, s66, 2616\n\t"
            "s_add_u32 s49, s68, 588672\n\t"
            "buffer_load_dwordx4 v249, s[72:75], s49 offen nt lds\n\t"
            "s_mov_b32 exec_hi, -1\n\t"
            "s_mov_b32 exec_hi, 0x3fffff\n\t"
            "s_add_u32 m0, s66, 3488\n\t"
            "s_add_u32 s49, s68, 784896\n\t"
            "buffer_load_dwordx4 v249, s[72:75], s49 offen nt lds\n\t"
            "s_mov_b32 exec_hi, -1\n\t"
            "s_mov_b32 exec_hi, 0x3fffff\n\t"
            "s_add_u32 m0, s66, 4360\n\t"
            "s_add_u32 s49, s68, 981120\n\t"
            "buffer_load_dwordx4 v249, s[72:75], s49 offen nt lds\n\t"
            "s_mov_b32 exec_hi, -1\n\t"
            "s_mov_b32 exec_hi, 0x3fffff\n\t"
            "s_add_u32 m0, s66, 5232\n\t"
            "s_add_u32 s49, s68, 1177344\n\t"
            "buffer_load_dwordx4 v249, s[72:75], s49 offen nt lds\n\t"
            "s_mov_b32 exec_hi, -1\n\t"
            "s_mov_b32 exec_hi, 0x3fffff\n\t"
            "s_add_u32 m0, s66, 6104\n\t"
            "s_add_u32 s49, s68, 1373568\n\t"
            "buffer_load_dwordx4 v249, s[72:75], s49 offen nt lds\n\t"
            "s_mov_b32 exec_hi, -1\n\t"
            "s_mov_b32 exec_hi, 0x3fffff\n\t"
            "s_add_u32 m0, s66, 6976\n\t"
            "s_add_u32 s49, s68, 1569792\n\t"
            "buffer_load_dwordx4 v249, s[72:75], s49 offen nt lds\n\t"
            "s_mov_b32 exec_hi, -1\n\t"
            "s_add_u32 s49, s33, 0\n\t"
            "buffer_load_dwordx4 v[0:3], v247, s[28:31], s49 offen sc1\n\t"
            "s_add_u32 s49, s33, 1024\n\t"
            "buffer_load_dwordx4 v[4:7], v247, s[28:31], s49 offen sc1\n\t"
            "s_add_u32 s49, s33, 2048\n\t"
            "buffer_load_dwordx4 v[8:11], v247, s[28:31], s49 offen sc1\n\t"
            "s_add_u32 s49, s33, 3072\n\t"
            "buffer_load_dwordx4 v[12:15], v247, s[28:31], s49 offen sc1\n\t"
            "s_add_u32 s49, s33, 4096\n\t"
            "buffer_load_dwordx4 v[16:19], v247, s[28:31], s49 offen sc1\n\t"
            "s_add_u32 s49, s33, 5120\n\t"
            "buffer_load_dwordx4 v[20:23], v247, s[28:31], s49 offen sc1\n\t"
            "s_add_u32 s49, s33, 6144\n\t"
            "buffer_load_dwordx4 v[24:27], v247, s[28:31], s49 offen sc1\n\t"
            "s_add_u32 s49, s33, 7168\n\t"
            "buffer_load_dwordx4 v[28:31], v247, s[28:31], s49 offen sc1\n\t"
            "s_add_u32 s49, s33, 8192\n\t"
            "buffer_load_dwordx4 v[32:35], v247, s[28:31], s49 offen sc1\n\t"
            "s_add_u32 s49, s33, 9216\n\t"
            "buffer_load_dwordx4 v[36:39], v247, s[28:31], s49 offen sc1\n\t"
            "s_add_u32 s49, s33, 10240\n\t"
            "buffer_load_dwordx4 v[40:43], v247, s[28:31], s49 offen sc1\n\t"
            "s_add_u32 s49, s33, 11264\n\t"
            "buffer_load_dwordx4 v[44:47], v247, s[28:31], s49 offen sc1\n\t"
            "s_add_u32 s49, s33, 12288\n\t"
            "buffer_load_dwordx4 v[48:51], v247, s[28:31], s49 offen sc1\n\t"
            "s_add_u32 s49, s33, 13312\n\t"
            "buffer_load_dwordx4 v[52:55], v247, s[28:31], s49 offen sc1\n\t"
            "s_add_u32 s49, s33, 14336\n\t"
            "buffer_load_dwordx4 v[56:59], v247, s[28:31], s49 offen sc1\n\t"
            "s_add_u32 s49, s33, 15360\n\t"
            "buffer_load_dwordx4 v[60:63], v247, s[28:31], s49 offen sc1\n\t"
            "s_add_u32 s49, s33, 16384\n\t"
            "buffer_load_dwordx4 v[64:67], v247, s[28:31], s49 offen sc1\n\t"
            "s_add_u32 s49, s33, 17408\n\t"
            "buffer_load_dwordx4 v[68:71], v247, s[28:31], s49 offen sc1\n\t"
            "s_add_u32 s49, s33, 18432\n\t"
            "buffer_load_dwordx4 v[72:75], v247, s[28:31], s49 offen sc1\n\t"
            "s_add_u32 s49, s33, 19456\n\t"
            "buffer_load_dwordx4 v[76:79], v247, s[28:31], s49 offen sc1\n\t"
            "s_add_u32 s49, s33, 20480\n\t"
            "buffer_load_dwordx4 v[80:83], v247, s[28:31], s49 offen sc1\n\t"
            "s_add_u32 s49, s33, 21504\n\t"
            "buffer_load_dwordx4 v[84:87], v247, s[28:31], s49 offen sc1\n\t"
            "s_add_u32 s49, s33, 22528\n\t"
            "buffer_load_dwordx4 v[88:91], v247, s[28:31], s49 offen sc1\n\t"
            "s_add_u32 s49, s33, 23552\n\t"
            "buffer_load_dwordx4 v[92:95], v247, s[28:31], s49 offen sc1\n\t"
            "s_add_u32 s49, s33, 24576\n\t"
            "buffer_load_dwordx4 v[96:99], v247, s[28:31], s49 offen sc1\n\t"
            "s_add_u32 s49, s33, 25600\n\t"
            "buffer_load_dwordx4 v[100:103], v247, s[28:31], s49 offen sc1\n\t"
            "s_add_u32 s49, s33, 26624\n\t"
            "buffer_load_dwordx4 v[104:107], v247, s[28:31], s49 offen sc1\n\t"
            "s_add_u32 s49, s33, 27648\n\t"
            "buffer_load_dwordx4 v[108:111], v247, s[28:31], s49 offen sc1\n\t"
            "s_add_u32 s49, s33, 28672\n\t"
            "buffer_load_dwordx4 v[112:115], v247, s[28:31], s49 offen sc1\n\t"
            "s_add_u32 s49, s33, 29696\n\t"
            "buffer_load_dwordx4 v[116:119], v247, s[28:31], s49 offen sc1\n\t"
            "s_add_u32 s49, s33, 30720\n\t"
            "buffer_load_dwordx4 v[120:123], v247, s[28:31], s49 offen sc1\n\t"
            "s_add_u32 s49, s33, 31744\n\t"
            "buffer_load_dwordx4 v[124:127], v247, s[28:31], s49 offen sc1\n\t"
            "v_cvt_pk_bf16_f32 v184, v184, v185\n\t"
            "v_cvt_pk_bf16_f32 v185, v186, v187\n\t"
            "v_cvt_pk_bf16_f32 v186, v188, v189\n\t"
            "v_cvt_pk_bf16_f32 v187, v190, v191\n\t"
            "v_mfma_f32_4x4x4_16b_bf16 v[196:199], v[184:185], v[184:185], v[196:199]\n\t"
            "s_add_u32 s49, s33, 32768\n\t"
            "buffer_load_dwordx4 v[128:131], v247, s[28:31], s49 offen sc1\n\t"
            "v_mfma_f32_4x4x4_16b_bf16 v[196:199], v[186:187], v[186:187], v[196:199]\n\t"
            "ds_write_b128 v237, v[184:187] offset:256\n\t"
            "v_mfma_f32_16x16x32_bf16 v[192:195], v[238:241], v[184:187], v[192:195]\n\t"
            "s_add_u32 s49, s33, 33792\n\t"
            "buffer_load_dwordx4 v[132:135], v247, s[28:31], s49 offen sc1\n\t"
            "v_add_u32 v237, s44, v245\n\t"
            "ds_read_b128 v[204:207], v237\n\t"
            "ds_read_b128 v[216:219], v237 offset:6144\n\t"
            "s_add_u32 s49, s33, 34816\n\t"
            "buffer_load_dwordx4 v[136:139], v247, s[28:31], s49 offen sc1\n\t"
            "s_add_u32 s49, s33, 35840\n\t"
            "buffer_load_dwordx4 v[140:143], v247, s[28:31], s49 offen sc1\n\t"
            "v_cndmask_b32_e64 v193, v196, v197, s[60:61]\n\t"
            "v_cndmask_b32_e64 v193, v193, v198, s[62:63]\n\t"
            "v_cndmask_b32_e64 v193, v193, v199, s[64:65]\n\t"
            "v_add_u32 v237, s48, v245\n\t"
            "s_add_u32 s45, s46, s27\n\t"
            "s_mov_b32 s46, s47\n\t"
            "s_mov_b32 s47, s68\n\t"
            "ds_read_b128 v[228:231], v237 offset:12288\n\t"
            "ds_write_b64 v248, v[192:193] offset:18816\n\t"
            "v_add_u32 v248, s44, v246\n\t"
            "ds_read_b128 v[208:211], v248\n\t"
            "ds_read_b128 v[220:223], v248 offset:6144\n\t"
            "s_waitcnt lgkmcnt(9)\n\t"
            "buffer_store_dwordx4 v[200:203], v232, s[40:43], 0 offen\n\t"
            "s_waitcnt lgkmcnt(5)\n\t"
            "buffer_store_dwordx4 v[204:207], v232, s[40:43], 0 offen offset:64\n\t"
            "v_add_u32 v248, s48, v246\n\t"
            "s_xor_b32 s48, s48, s71\n\t"
            "s_xor_b32 s67, s67, s71\n\t"
            "s_add_u32 s76, s76, 4\n\t"
            "s_add_u32 s70, s70, 4\n\t"
            "ds_read_b128 v[200:203], v236\n\t"
            "ds_read_b128 v[204:207], v237\n\t"
            "s_lshl_b32 s56, s56, 2\n\t"
            "s_lshr_b32 s49, s26, 30\n\t"
            "s_or_b32 s56, s56, s49\n\t"
            "s_waitcnt lgkmcnt(3)\n\t"
            "buffer_store_dwordx4 v[208:211], v232, s[40:43], 0 offen offset:128\n\t"
            "buffer_store_dwordx4 v[212:215], v233, s[40:43], 0 offen\n\t"
            "buffer_store_dwordx4 v[216:219], v233, s[40:43], 0 offen offset:64\n\t"
            "ds_read_b128 v[208:211], v248\n\t"
            "ds_read_b128 v[212:215], v236 offset:6144\n\t"
            "ds_read_b128 v[216:219], v237 offset:6144\n\t"
            "s_waitcnt lgkmcnt(5)\n\t"
            "buffer_store_dwordx4 v[220:223], v233, s[40:43], 0 offen offset:128\n\t"
            "ds_read_b128 v[232:235], v248 offset:12288\n\t"
            "s_nop 0\n\t"
            "ds_read_b128 v[220:223], v248 offset:6144\n\t"
            "s_waitcnt lgkmcnt(0)\n\t"
            "s_barrier\n\t"
            "s_waitcnt vmcnt(0)\n\t"
            "s_nop 7\n\t"
            ".Lwb_loop%=:\n\t"
            "s_setprio 1\n\t"
            "ds_read_b128 v[168:171], v254\n\t"
            "ds_read_b128 v[172:175], v254 offset:64\n\t"
            "s_waitcnt vmcnt(9)\n\t"
            "ds_read_b64 v[144:145], v253\n\t"
            "ds_read_b64 v[146:147], v253 offset:96\n\t"
            "ds_read_b64 v[148:149], v253 offset:1744\n\t"
            "ds_read_b64 v[150:151], v253 offset:1840\n\t"
            "ds_read_b64 v[152:153], v253 offset:8\n\t"
            "ds_read_b64 v[154:155], v253 offset:104\n\t"
            "ds_read_b64 v[156:157], v253 offset:1752\n\t"
            "ds_read_b64 v[158:159], v253 offset:1848\n\t"
            "ds_read_b64 v[160:161], v253 offset:16\n\t"
            "ds_read_b64 v[162:163], v253 offset:112\n\t"
            "ds_read_b64 v[164:165], v253 offset:1760\n\t"
            "ds_read_b64 v[166:167], v253 offset:1856\n\t"
            "v_mov_b64 v[176:177], 0\n\t"
            "v_mov_b64 v[178:179], 0\n\t"
            "v_mov_b64 v[180:181], 0\n\t"
            "v_mov_b64 v[182:183], 0\n\t"
            "v_mov_b32 v249, s76\n\t"
            "v_mov_b64 v[184:185], 0\n\t"
            "v_mov_b64 v[186:187], 0\n\t"
            "v_mov_b64 v[188:189], 0\n\t"
            "v_mov_b64 v[190:191], 0\n\t"
            "v_add_u32 v236, s67, v250\n\t"
            "v_add_u32 v248, s67, v251\n\t"
            "ds_read_b32 v249, v249\n\t"
            "s_setprio 3\n\t"
            "s_waitcnt lgkmcnt(9)\n\t"
            "v_smfmac_f32_16x16x64_bf16 v[176:179], v[168:171], v[144:151], v255\n\t"
            "ds_read_b128 v[168:171], v254 offset:128\n\t"
            "s_waitcnt lgkmcnt(6)\n\t"
            "v_smfmac_f32_16x16x64_bf16 v[180:183], v[172:175], v[152:159], v255\n\t"
            "s_setprio 1\n\t"
            "ds_read_b64 v[144:145], v253 offset:24\n\t"
            "ds_read_b64 v[146:147], v253 offset:120\n\t"
            "ds_read_b64 v[148:149], v253 offset:1768\n\t"
            "ds_read_b64 v[150:151], v253 offset:1864\n\t"
            "ds_read_b128 v[172:175], v254 offset:192\n\t"
            "ds_read_b64 v[152:153], v253 offset:32\n\t"
            "ds_read_b64 v[154:155], v253 offset:128\n\t"
            "ds_read_b64 v[156:157], v253 offset:1776\n\t"
            "ds_read_b64 v[158:159], v253 offset:1872\n\t"
            "v_cvt_pk_bf16_f32 v176, v176, v177\n\t"
            "v_cvt_pk_bf16_f32 v177, v178, v179\n\t"
            "v_cvt_pk_bf16_f32 v178, v180, v181\n\t"
            "v_cvt_pk_bf16_f32 v179, v182, v183\n\t"
            "s_setprio 3\n\t"
            "v_mfma_f32_4x4x4_16b_bf16 v[196:199], v[176:177], v[176:177], 0\n\t"
            "v_mov_b64 v[180:181], 0\n\t"
            "v_mfma_f32_16x16x32_bf16 v[192:195], v[238:241], v[176:179], 0\n\t"
            "s_waitcnt lgkmcnt(10)\n\t"
            "v_readfirstlane_b32 s26, v249\n\t"
            "v_mov_b64 v[182:183], 0\n\t"
            "v_mfma_f32_4x4x4_16b_bf16 v[196:199], v[178:179], v[178:179], v[196:199]\n\t"
            "ds_read_b32 v249, v252 offset:1024\n\t"
            "s_and_b32 s68, s26, 0x7ffffff\n\t"
            "s_add_u32 s68, s68, s78\n\t"
            "s_and_b32 s69, s26, 0x38000000\n\t"
            "s_or_b32 s69, s69, s39\n\t"
            "s_bitcmp1_b32 s26, 27\n\t"
            "s_cselect_b32 s79, 2.0, s68\n\t"
            "s_waitcnt lgkmcnt(10)\n\t"
            "v_smfmac_f32_16x16x64_bf16 v[184:187], v[168:171], v[160:167], v255\n\t"
            "s_and_b32 s57, s56, 3\n\t"
            "s_lshl_b32 s58, s57, 3\n\t"
            "s_lshr_b32 m0, s54, s58\n\t"
            "ds_read_b128 v[168:171], v254 offset:256\n\t"
            "ds_read_b64 v[160:161], v253 offset:40\n\t"
            "ds_read_b64 v[162:163], v253 offset:136\n\t"
            "ds_read_b64 v[164:165], v253 offset:1784\n\t"
            "s_waitcnt lgkmcnt(9)\n\t"
            "v_smfmac_f32_16x16x64_bf16 v[188:191], v[172:175], v[144:151], v255\n\t"
            "s_setprio 1\n\t"
            "s_lshl_b32 s58, s57, 4\n\t"
            "s_lshr_b64 s[36:37], s[50:51], s58\n\t"
            "s_and_b32 s57, s36, 0xffff\n\t"
            "s_add_u32 s57, s57, s67\n\t"
            "ds_read_b64 v[166:167], v253 offset:1880\n\t"
            "ds_read_b128 v[172:175], v254 offset:320\n\t"
            "ds_read_b64 v[144:145], v253 offset:48\n\t"
            "ds_read_b64 v[146:147], v253 offset:144\n\t"
            "ds_read_b64 v[148:149], v253 offset:1792\n\t"
            "ds_read_b64 v[150:151], v253 offset:1888\n\t"
            "s_lshr_b64 s[36:37], s[52:53], s58\n\t"
            "s_and_b32 s58, s36, 0xffff\n\t"
            "s_add_u32 s58, s58, s67\n\t"
            "s_and_b32 s59, s50, 0xffff\n\t"
            "s_add_u32 s59, s59, s67\n\t"
            "s_and_b32 s38, s52, 0xffff\n\t"
            "s_add_u32 s38, s38, s67\n\t"
            "v_writelane_b32 v236, s59, m0\n\t"
            "v_writelane_b32 v236, s57, 63\n\t"
            "v_cvt_pk_bf16_f32 v184, v184, v185\n\t"
            "v_cvt_pk_bf16_f32 v185, v186, v187\n\t"
            "v_xor_b32 v237, 16, v236\n\t"
            "v_cvt_pk_bf16_f32 v186, v188, v189\n\t"
            "s_setprio 3\n\t"
            "v_mfma_f32_4x4x4_16b_bf16 v[196:199], v[184:185], v[184:185], v[196:199]\n\t"
            "s_setprio 1\n\t"
            "s_waitcnt lgkmcnt(14)\n\t"
            "ds_write_b128 v236, v[176:179]\n\t"
            "v_mov_b64 v[176:177], 0\n\t"
            "v_mov_b64 v[178:179], 0\n\t"
            "v_cvt_pk_bf16_f32 v187, v190, v191\n\t"
            "v_mov_b64 v[188:189], 0\n\t"
            "s_setprio 3\n\t"
            "s_waitcnt lgkmcnt(10)\n\t"
            "v_smfmac_f32_16x16x64_bf16 v[176:179], v[168:171], v[152:159], v255\n\t"
            "ds_read_b128 v[168:171], v254 offset:384\n\t"
            "s_waitcnt lgkmcnt(6)\n\t"
            "v_smfmac_f32_16x16x64_bf16 v[180:183], v[172:175], v[160:167], v255\n\t"
            "ds_read_b64 v[152:153], v253 offset:56\n\t"
            "v_mfma_f32_16x16x32_bf16 v[192:195], v[238:241], v[184:187], v[192:195]\n\t"
            "ds_read_b64 v[154:155], v253 offset:152\n\t"
            "ds_read_b64 v[156:157], v253 offset:1800\n\t"
            "v_mfma_f32_4x4x4_16b_bf16 v[196:199], v[186:187], v[186:187], v[196:199]\n\t"
            "s_setprio 1\n\t"
            "ds_read_b64 v[158:159], v253 offset:1896\n\t"
            "ds_read_b128 v[172:175], v254 offset:448\n\t"
            "ds_read_b64 v[160:161], v253 offset:64\n\t"
            "ds_read_b64 v[162:163], v253 offset:160\n\t"
            "ds_read_b64 v[164:165], v253 offset:1808\n\t"
            "ds_read_b64 v[166:167], v253 offset:1904\n\t"
            "s_waitcnt lgkmcnt(14)\n\t"
            "ds_write_b128 v237, v[184:187]\n\t"
            "v_mov_b64 v[184:185], 0\n\t"
            "v_mov_b64 v[186:187], 0\n\t"
            "v_mov_b64 v[190:191], 0\n\t"
            "v_cvt_pk_bf16_f32 v176, v176, v177\n\t"
            "v_cvt_pk_bf16_f32 v177, v178, v179\n\t"
            "v_cvt_pk_bf16_f32 v178, v180, v181\n\t"
            "v_cvt_pk_bf16_f32 v179, v182, v183\n\t"
            "s_setprio 3\n\t"
            "v_mfma_f32_4x4x4_16b_bf16 v[196:199], v[176:177], v[176:177], v[196:199]\n\t"
            "s_waitcnt lgkmcnt(14)\n\t"
            "ds_write_b128 v236, v[176:179] offset:128\n\t"
            "s_waitcnt lgkmcnt(11)\n\t"
            "v_smfmac_f32_16x16x64_bf16 v[184:187], v[168:171], v[144:151], v255\n\t"
            "ds_read_b128 v[168:171], v254 offset:512\n\t"
            "v_mfma_f32_16x16x32_bf16 v[192:195], v[238:241], v[176:179], v[192:195]\n\t"
            "ds_read_b64 v[144:145], v253 offset:72\n\t"
            "ds_read_b64 v[146:147], v253 offset:168\n\t"
            "s_waitcnt lgkmcnt(9)\n\t"
            "v_smfmac_f32_16x16x64_bf16 v[188:191], v[172:175], v[152:159], v255\n\t"
            "ds_read_b64 v[148:149], v253 offset:1816\n\t"
            "v_mfma_f32_4x4x4_16b_bf16 v[196:199], v[178:179], v[178:179], v[196:199]\n\t"
            "s_setprio 1\n\t"
            "ds_read_b64 v[150:151], v253 offset:1912\n\t"
            "ds_read_b128 v[172:175], v254 offset:576\n\t"
            "ds_read_b64 v[152:153], v253 offset:80\n\t"
            "ds_read_b64 v[154:155], v253 offset:176\n\t"
            "ds_read_b64 v[156:157], v253 offset:1824\n\t"
            "s_waitcnt lgkmcnt(14)\n\t"
            "ds_read_b64 v[158:159], v253 offset:1920\n\t"
            "v_mov_b64 v[176:177], 0\n\t"
            "v_mov_b64 v[178:179], 0\n\t"
            "v_mov_b64 v[180:181], 0\n\t"
            "v_mov_b64 v[182:183], 0\n\t"
            "v_cvt_pk_bf16_f32 v184, v184, v185\n\t"
            "v_cvt_pk_bf16_f32 v185, v186, v187\n\t"
            "v_cvt_pk_bf16_f32 v186, v188, v189\n\t"
            "v_cvt_pk_bf16_f32 v187, v190, v191\n\t"
            "s_setprio 3\n\t"
            "v_mfma_f32_4x4x4_16b_bf16 v[196:199], v[184:185], v[184:185], v[196:199]\n\t"
            "s_waitcnt lgkmcnt(14)\n\t"
            "ds_write_b128 v237, v[184:187] offset:128\n\t"
            "v_mfma_f32_16x16x32_bf16 v[192:195], v[238:241], v[184:187], v[192:195]\n\t"
            "v_mov_b64 v[184:185], 0\n\t"
            "v_mov_b64 v[188:189], 0\n\t"
            "s_waitcnt lgkmcnt(10)\n\t"
            "v_smfmac_f32_16x16x64_bf16 v[176:179], v[168:171], v[160:167], v255\n\t"
            "ds_read_b128 v[168:171], v254 offset:640\n\t"
            "v_mfma_f32_4x4x4_16b_bf16 v[196:199], v[186:187], v[186:187], v[196:199]\n\t"
            "ds_read_b64 v[160:161], v253 offset:88\n\t"
            "ds_read_b64 v[162:163], v253 offset:184\n\t"
            "ds_read_b64 v[164:165], v253 offset:1832\n\t"
            "s_waitcnt lgkmcnt(9)\n\t"
            "v_smfmac_f32_16x16x64_bf16 v[180:183], v[172:175], v[144:151], v255\n\t"
            "s_setprio 1\n\t"
            "ds_read_b64 v[166:167], v253 offset:1928\n\t"
            "ds_read_b128 v[172:175], v254 offset:704\n\t"
            "v_mov_b64 v[186:187], 0\n\t"
            "v_mov_b64 v[190:191], 0\n\t"
            "v_writelane_b32 v248, s38, m0\n\t"
            "v_and_b32 v249, s69, v249\n\t"
            "v_cvt_pk_bf16_f32 v176, v176, v177\n\t"
            "v_cvt_pk_bf16_f32 v177, v178, v179\n\t"
            "v_cvt_pk_bf16_f32 v178, v180, v181\n\t"
            "v_cvt_pk_bf16_f32 v179, v182, v183\n\t"
            "s_setprio 3\n\t"
            "v_mfma_f32_4x4x4_16b_bf16 v[196:199], v[176:177], v[176:177], v[196:199]\n\t"
            "v_writelane_b32 v248, s58, 63\n\t"
            "v_mfma_f32_16x16x32_bf16 v[192:195], v[238:241], v[176:179], v[192:195]\n\t"
            "ds_write_b128 v236, v[176:179] offset:256\n\t"
            "v_mfma_f32_4x4x4_16b_bf16 v[196:199], v[178:179], v[178:179], v[196:199]\n\t"
            "v_lshlrev_b32 v176, 16, v208\n\t"
            "v_and_b32 v177, 0xffff0000, v208\n\t"
            "v_lshlrev_b32 v178, 16, v209\n\t"
            "s_waitcnt lgkmcnt(6)\n\t"
            "v_smfmac_f32_16x16x64_bf16 v[184:187], v[168:171], v[152:159], v255\n\t"
            "s_setprio 1\n\t"
            "v_lshlrev_b32 v152, 16, v200\n\t"
            "v_and_b32 v153, 0xffff0000, v200\n\t"
            "v_lshlrev_b32 v154, 16, v201\n\t"
            "v_and_b32 v155, 0xffff0000, v201\n\t"
            "v_lshlrev_b32 v156, 16, v202\n\t"
            "v_and_b32 v157, 0xffff0000, v202\n\t"
            "s_setprio 3\n\t"
            "s_waitcnt lgkmcnt(1)\n\t"
            "v_smfmac_f32_16x16x64_bf16 v[188:191], v[172:175], v[160:167], v255\n\t"
            "s_setprio 1\n\t"
            "s_mov_b32 exec_hi, 0x3fffff\n\t"
            "s_add_u32 m0, s66, 0\n\t"
            "s_add_u32 s49, s79, 0\n\t"
            "buffer_load_dwordx4 v249, s[72:75], s49 offen lds\n\t"
            "s_mov_b32 exec_hi, -1\n\t"
            "s_mov_b32 exec_hi, 0x3fffff\n\t"
            "s_add_u32 m0, s66, 872\n\t"
            "s_add_u32 s49, s68, 196224\n\t"
            "buffer_load_dwordx4 v249, s[72:75], s49 offen lds\n\t"
            "s_mov_b32 exec_hi, -1\n\t"
            "s_mov_b32 exec_hi, 0x3fffff\n\t"
            "s_add_u32 m0, s66, 1744\n\t"
            "s_add_u32 s49, s68, 392448\n\t"
            "buffer_load_dwordx4 v249, s[72:75], s49 offen lds\n\t"
            "s_mov_b32 exec_hi, -1\n\t"
            "s_mov_b32 exec_hi, 0x3fffff\n\t"
            "s_add_u32 m0, s66, 2616\n\t"
            "s_add_u32 s49, s68, 588672\n\t"
            "buffer_load_dwordx4 v249, s[72:75], s49 offen lds\n\t"
            "s_mov_b32 exec_hi, -1\n\t"
            "s_mov_b32 exec_hi, 0x3fffff\n\t"
            "s_add_u32 m0, s66, 3488\n\t"
            "s_add_u32 s49, s68, 784896\n\t"
            "buffer_load_dwordx4 v249, s[72:75], s49 offen lds\n\t"
            "s_mov_b32 exec_hi, -1\n\t"
            "s_mov_b32 exec_hi, 0x3fffff\n\t"
            "s_add_u32 m0, s66, 4360\n\t"
            "s_add_u32 s49, s68, 981120\n\t"
            "buffer_load_dwordx4 v249, s[72:75], s49 offen lds\n\t"
            "s_mov_b32 exec_hi, -1\n\t"
            "ds_read_b32 v247, v255 offset:7856\n\t"
            "s_mov_b32 exec_hi, 0x3fffff\n\t"
            "s_add_u32 m0, s66, 5232\n\t"
            "s_add_u32 s49, s68, 1177344\n\t"
            "buffer_load_dwordx4 v249, s[72:75], s49 offen lds\n\t"
            "s_mov_b32 exec_hi, -1\n\t"
            "s_mov_b32 exec_hi, 0x3fffff\n\t"
            "s_add_u32 m0, s66, 6104\n\t"
            "s_add_u32 s49, s68, 1373568\n\t"
            "buffer_load_dwordx4 v249, s[72:75], s49 offen lds\n\t"
            "s_mov_b32 exec_hi, -1\n\t"
            "s_mov_b32 exec_hi, 0x3fffff\n\t"
            "s_add_u32 m0, s66, 6976\n\t"
            "s_add_u32 s49, s68, 1569792\n\t"
            "buffer_load_dwordx4 v249, s[72:75], s49 offen lds\n\t"
            "s_mov_b32 exec_hi, -1\n\t"
            "v_cvt_pk_bf16_f32 v184, v184, v185\n\t"
            "v_cvt_pk_bf16_f32 v185, v186, v187\n\t"
            "v_cvt_pk_bf16_f32 v186, v188, v189\n\t"
            "v_cvt_pk_bf16_f32 v187, v190, v191\n\t"
            "s_setprio 3\n\t"
            "v_mfma_f32_4x4x4_16b_bf16 v[196:199], v[184:185], v[184:185], v[196:199]\n\t"
            "ds_write_b128 v237, v[184:187] offset:256\n\t"
            "v_mfma_f32_16x16x32_bf16 v[192:195], v[238:241], v[184:187], v[192:195]\n\t"
            "s_waitcnt lgkmcnt(1)\n\t"
            "v_readfirstlane_b32 s77, v247\n\t"
            "s_cmp_ge_u32 s77, s70\n\t"
            "s_cbranch_scc0 .Lwb_spin%=\n\t"
            ".Lwb_go%=:\n\t"
            "v_mfma_f32_4x4x4_16b_bf16 v[196:199], v[186:187], v[186:187], v[196:199]\n\t"
            "s_setprio 1\n\t"
            "ds_read_b128 v[184:187], v242\n\t"
            "ds_read_b128 v[188:191], v243\n\t"
            "ds_read_b128 v[172:175], v243 offset:128\n\t"
            "v_lshlrev_b32 v158, 16, v203\n\t"
            "v_and_b32 v159, 0xffff0000, v203\n\t"
            "v_lshlrev_b32 v160, 16, v204\n\t"
            "v_and_b32 v161, 0xffff0000, v204\n\t"
            "v_lshlrev_b32 v162, 16, v205\n\t"
            "v_and_b32 v163, 0xffff0000, v205\n\t"
            "v_cndmask_b32_e64 v193, v196, v197, s[60:61]\n\t"
            "v_cndmask_b32_e64 v193, v193, v198, s[62:63]\n\t"
            "v_cndmask_b32_e64 v193, v193, v199, s[64:65]\n\t"
            "v_lshlrev_b32 v164, 16, v206\n\t"
            "v_and_b32 v165, 0xffff0000, v206\n\t"
            "v_lshlrev_b32 v166, 16, v207\n\t"
            "v_and_b32 v167, 0xffff0000, v207\n\t"
            "v_and_b32 v179, 0xffff0000, v209\n\t"
            "v_lshlrev_b32 v180, 16, v210\n\t"
            "v_and_b32 v181, 0xffff0000, v210\n\t"
            "v_lshlrev_b32 v182, 16, v211\n\t"
            "v_and_b32 v183, 0xffff0000, v211\n\t"
            "v_lshlrev_b32 v144, 16, v212\n\t"
            "v_and_b32 v145, 0xffff0000, v212\n\t"
            "v_lshlrev_b32 v146, 16, v213\n\t"
            "v_and_b32 v147, 0xffff0000, v213\n\t"
            "v_lshlrev_b32 v148, 16, v214\n\t"
            "v_and_b32 v149, 0xffff0000, v214\n\t"
            "v_lshlrev_b32 v150, 16, v215\n\t"
            "v_and_b32 v151, 0xffff0000, v215\n\t"
            "v_lshlrev_b32 v168, 16, v216\n\t"
            "v_and_b32 v169, 0xffff0000, v216\n\t"
            "v_lshlrev_b32 v170, 16, v217\n\t"
            "s_setprio 3\n\t"
            "s_waitcnt lgkmcnt(1)\n\t"
            "v_mfma_f32_16x16x32_bf16 v[152:155], v[0:3], v[184:187], v[152:155]\n\t"
            "s_setprio 1\n\t"
            "s_setprio 1\n\t"
            "ds_read_b128 v[196:199], v252\n\t"
            "v_and_b32 v171, 0xffff0000, v217\n\t"
            "v_mfma_f32_16x16x32_bf16 v[152:155], v[4:7], v[188:191], v[152:155]\n\t"
            "s_bfe_u32 s57, s56, 0x20004\n\t"
            "s_lshl_b32 s58, s57, 3\n\t"
            "s_lshr_b32 s58, s55, s58\n\t"
            "s_lshl_b64 s[36:37], s[34:35], s58\n\t"
            "v_mfma_f32_16x16x32_bf16 v[156:159], v[28:31], v[188:191], v[156:159]\n\t"
            "v_add_u32 v236, s48, v244\n\t"
            "v_add_u32 v237, s48, v245\n\t"
            "v_mfma_f32_16x16x32_bf16 v[156:159], v[24:27], v[184:187], v[156:159]\n\t"
            "s_bitcmp1_b32 s57, 1\n\t"
            "s_cselect_b32 s58, 1177344, 0\n\t"
            "s_bitcmp1_b32 s57, 0\n\t"
            "s_cselect_b32 s57, 2304, 0\n\t"
            "s_add_u32 s57, s57, s58\n\t"
            "v_mfma_f32_16x16x32_bf16 v[160:163], v[48:51], v[184:187], v[160:163]\n\t"
            "ds_read_b128 v[200:203], v236\n\t"
            "ds_read_b128 v[204:207], v237\n\t"
            "v_mfma_f32_16x16x32_bf16 v[160:163], v[52:55], v[188:191], v[160:163]\n\t"
            "ds_read_b128 v[212:215], v236 offset:6144\n\t"
            "ds_write_b64 v248, v[192:193] offset:18816\n\t"
            "v_mfma_f32_16x16x32_bf16 v[164:167], v[76:79], v[188:191], v[164:167]\n\t"
            "ds_read_b128 v[192:195], v242 offset:128\n\t"
            "v_add_u32 v248, s48, v246\n\t"
            "v_mfma_f32_16x16x32_bf16 v[164:167], v[72:75], v[184:187], v[164:167]\n\t"
            "ds_read_b128 v[208:211], v248\n\t"
            "s_waitcnt lgkmcnt(6)\n\t"
            "s_mov_b64 exec, s[36:37]\n\t"
            "v_add_u32 v196, s57, v196\n\t"
            "s_mov_b64 exec, -1\n\t"
            "v_mfma_f32_16x16x32_bf16 v[176:179], v[96:99], v[184:187], v[176:179]\n\t"
            "v_mfma_f32_16x16x32_bf16 v[176:179], v[100:103], v[188:191], v[176:179]\n\t"
            "v_mfma_f32_16x16x32_bf16 v[180:183], v[124:127], v[188:191], v[180:183]\n\t"
            "ds_read_b128 v[188:191], v243 offset:256\n\t"
            "v_mfma_f32_16x16x32_bf16 v[180:183], v[120:123], v[184:187], v[180:183]\n\t"
            "ds_read_b128 v[184:187], v242 offset:256\n\t"
            "s_waitcnt lgkmcnt(3)\n\t"
            "v_mfma_f32_16x16x32_bf16 v[152:155], v[8:11], v[192:195], v[152:155]\n\t"
            "v_mfma_f32_16x16x32_bf16 v[152:155], v[12:15], v[172:175], v[152:155]\n\t"
            "v_mfma_f32_16x16x32_bf16 v[156:159], v[36:39], v[172:175], v[156:159]\n\t"
            "v_mfma_f32_16x16x32_bf16 v[156:159], v[32:35], v[192:195], v[156:159]\n\t"
            "v_mfma_f32_16x16x32_bf16 v[160:163], v[56:59], v[192:195], v[160:163]\n\t"
            "v_mfma_f32_16x16x32_bf16 v[160:163], v[60:63], v[172:175], v[160:163]\n\t"
            "v_mfma_f32_16x16x32_bf16 v[164:167], v[84:87], v[172:175], v[164:167]\n\t"
            "v_mfma_f32_16x16x32_bf16 v[164:167], v[80:83], v[192:195], v[164:167]\n\t"
            "v_mfma_f32_16x16x32_bf16 v[176:179], v[104:107], v[192:195], v[176:179]\n\t"
            "v_mfma_f32_16x16x32_bf16 v[176:179], v[108:111], v[172:175], v[176:179]\n\t"
            "v_mfma_f32_16x16x32_bf16 v[180:183], v[132:135], v[172:175], v[180:183]\n\t"
            "ds_read_b128 v[172:175], v243 offset:6144\n\t"
            "v_mfma_f32_16x16x32_bf16 v[180:183], v[128:131], v[192:195], v[180:183]\n\t"
            "ds_read_b128 v[192:195], v242 offset:6144\n\t"
            "s_waitcnt lgkmcnt(2)\n\t"
            "v_mfma_f32_16x16x32_bf16 v[152:155], v[16:19], v[184:187], v[152:155]\n\t"
            "v_mfma_f32_16x16x32_bf16 v[152:155], v[20:23], v[188:191], v[152:155]\n\t"
            "v_mfma_f32_16x16x32_bf16 v[160:163], v[64:67], v[184:187], v[160:163]\n\t"
            "v_mfma_f32_16x16x32_bf16 v[160:163], v[68:71], v[188:191], v[160:163]\n\t"
            "v_mfma_f32_16x16x32_bf16 v[176:179], v[112:115], v[184:187], v[176:179]\n\t"
            "v_mfma_f32_16x16x32_bf16 v[176:179], v[116:119], v[188:191], v[176:179]\n\t"
            "v_mfma_f32_16x16x32_bf16 v[156:159], v[44:47], v[188:191], v[156:159]\n\t"
            "v_mfma_f32_16x16x32_bf16 v[156:159], v[40:43], v[184:187], v[156:159]\n\t"
            "s_nop 1\n\t"
            "v_cvt_pk_bf16_f32 v152, v152, v153\n\t"
            "v_mfma_f32_16x16x32_bf16 v[164:167], v[92:95], v[188:191], v[164:167]\n\t"
            "v_cvt_pk_bf16_f32 v153, v154, v155\n\t"
            "v_mfma_f32_16x16x32_bf16 v[164:167], v[88:91], v[184:187], v[164:167]\n\t"
            "v_cvt_pk_bf16_f32 v160, v160, v161\n\t"
            "v_cvt_pk_bf16_f32 v161, v162, v163\n\t"
            "v_mfma_f32_16x16x32_bf16 v[180:183], v[140:143], v[188:191], v[180:183]\n\t"
            "ds_read_b128 v[188:191], v243 offset:6272\n\t"
            "v_cvt_pk_bf16_f32 v176, v176, v177\n\t"
            "v_mfma_f32_16x16x32_bf16 v[180:183], v[136:139], v[184:187], v[180:183]\n\t"
            "ds_read_b128 v[184:187], v242 offset:6272\n\t"
            "v_cvt_pk_bf16_f32 v177, v178, v179\n\t"
            "s_waitcnt lgkmcnt(2)\n\t"
            "v_mfma_f32_16x16x32_bf16 v[144:147], v[0:3], v[192:195], v[144:147]\n\t"
            "v_cvt_pk_bf16_f32 v154, v156, v157\n\t"
            "v_cvt_pk_bf16_f32 v155, v158, v159\n\t"
            "v_mfma_f32_16x16x32_bf16 v[144:147], v[4:7], v[172:175], v[144:147]\n\t"
            "v_cvt_pk_bf16_f32 v162, v164, v165\n\t"
            "v_cvt_pk_bf16_f32 v163, v166, v167\n\t"
            "v_mfma_f32_16x16x32_bf16 v[148:151], v[28:31], v[172:175], v[148:151]\n\t"
            "v_cvt_pk_bf16_f32 v178, v180, v181\n\t"
            "v_cvt_pk_bf16_f32 v179, v182, v183\n\t"
            "v_mfma_f32_16x16x32_bf16 v[148:151], v[24:27], v[192:195], v[148:151]\n\t"
            "v_lshlrev_b32 v156, 16, v218\n\t"
            "v_and_b32 v157, 0xffff0000, v218\n\t"
            "v_mfma_f32_16x16x32_bf16 v[168:171], v[48:51], v[192:195], v[168:171]\n\t"
            "v_lshlrev_b32 v158, 16, v219\n\t"
            "v_and_b32 v159, 0xffff0000, v219\n\t"
            "v_mfma_f32_16x16x32_bf16 v[168:171], v[52:55], v[172:175], v[168:171]\n\t"
            "ds_read_b128 v[216:219], v237 offset:6144\n\t"
            "v_lshlrev_b32 v164, 16, v220\n\t"
            "v_mfma_f32_16x16x32_bf16 v[156:159], v[76:79], v[172:175], v[156:159]\n\t"
            "v_and_b32 v165, 0xffff0000, v220\n\t"
            "v_lshlrev_b32 v166, 16, v221\n\t"
            "v_mfma_f32_16x16x32_bf16 v[156:159], v[72:75], v[192:195], v[156:159]\n\t"
            "v_and_b32 v167, 0xffff0000, v221\n\t"
            "v_lshlrev_b32 v180, 16, v222\n\t"
            "s_waitcnt lgkmcnt(1)\n\t"
            "v_mfma_f32_16x16x32_bf16 v[144:147], v[8:11], v[184:187], v[144:147]\n\t"
            "v_and_b32 v181, 0xffff0000, v222\n\t"
            "v_lshlrev_b32 v182, 16, v223\n\t"
            "v_mfma_f32_16x16x32_bf16 v[144:147], v[12:15], v[188:191], v[144:147]\n\t"
            "v_and_b32 v183, 0xffff0000, v223\n\t"
            "v_mfma_f32_16x16x32_bf16 v[164:167], v[96:99], v[192:195], v[164:167]\n\t"
            "ds_read_b128 v[220:223], v248 offset:6144\n\t"
            "v_mfma_f32_16x16x32_bf16 v[164:167], v[100:103], v[172:175], v[164:167]\n\t"
            "buffer_store_dwordx4 v[152:155], v196, s[40:43], s45 offen\n\t"
            "v_mfma_f32_16x16x32_bf16 v[180:183], v[124:127], v[172:175], v[180:183]\n\t"
            "ds_read_b128 v[172:175], v243 offset:6400\n\t"
            "v_lshlrev_b32 v152, 16, v224\n\t"
            "v_mfma_f32_16x16x32_bf16 v[180:183], v[120:123], v[192:195], v[180:183]\n\t"
            "ds_read_b128 v[192:195], v242 offset:6400\n\t"
            "buffer_store_dwordx4 v[160:163], v196, s[40:43], s45 offen offset:64\n\t"
            "v_mfma_f32_16x16x32_bf16 v[148:151], v[36:39], v[188:191], v[148:151]\n\t"
            "v_and_b32 v153, 0xffff0000, v224\n\t"
            "v_lshlrev_b32 v154, 16, v225\n\t"
            "v_mfma_f32_16x16x32_bf16 v[148:151], v[32:35], v[184:187], v[148:151]\n\t"
            "buffer_store_dwordx4 v[176:179], v196, s[40:43], s45 offen offset:128\n\t"
            "v_mfma_f32_16x16x32_bf16 v[168:171], v[56:59], v[184:187], v[168:171]\n\t"
            "v_and_b32 v155, 0xffff0000, v225\n\t"
            "v_lshlrev_b32 v160, 16, v226\n\t"
            "v_mfma_f32_16x16x32_bf16 v[168:171], v[60:63], v[188:191], v[168:171]\n\t"
            "v_and_b32 v161, 0xffff0000, v226\n\t"
            "v_lshlrev_b32 v162, 16, v227\n\t"
            "v_mfma_f32_16x16x32_bf16 v[156:159], v[84:87], v[188:191], v[156:159]\n\t"
            "v_and_b32 v163, 0xffff0000, v227\n\t"
            "v_lshlrev_b32 v176, 16, v228\n\t"
            "v_mfma_f32_16x16x32_bf16 v[156:159], v[80:83], v[184:187], v[156:159]\n\t"
            "ds_read_b128 v[224:227], v236 offset:12288\n\t"
            "v_and_b32 v177, 0xffff0000, v228\n\t"
            "v_mfma_f32_16x16x32_bf16 v[164:167], v[104:107], v[184:187], v[164:167]\n\t"
            "v_lshlrev_b32 v178, 16, v229\n\t"
            "v_and_b32 v179, 0xffff0000, v229\n\t"
            "v_mfma_f32_16x16x32_bf16 v[164:167], v[108:111], v[188:191], v[164:167]\n\t"
            "v_mfma_f32_16x16x32_bf16 v[180:183], v[132:135], v[188:191], v[180:183]\n\t"
            "ds_read_b128 v[188:191], v243 offset:12288\n\t"
            "v_mfma_f32_16x16x32_bf16 v[180:183], v[128:131], v[184:187], v[180:183]\n\t"
            "ds_read_b128 v[184:187], v242 offset:12288\n\t"
            "s_waitcnt lgkmcnt(3)\n\t"
            "v_mfma_f32_16x16x32_bf16 v[144:147], v[16:19], v[192:195], v[144:147]\n\t"
            "v_mfma_f32_16x16x32_bf16 v[144:147], v[20:23], v[172:175], v[144:147]\n\t"
            "v_mfma_f32_16x16x32_bf16 v[168:171], v[64:67], v[192:195], v[168:171]\n\t"
            "v_mfma_f32_16x16x32_bf16 v[168:171], v[68:71], v[172:175], v[168:171]\n\t"
            "v_mfma_f32_16x16x32_bf16 v[164:167], v[112:115], v[192:195], v[164:167]\n\t"
            "v_mfma_f32_16x16x32_bf16 v[164:167], v[116:119], v[172:175], v[164:167]\n\t"
            "v_mfma_f32_16x16x32_bf16 v[148:151], v[44:47], v[172:175], v[148:151]\n\t"
            "v_mfma_f32_16x16x32_bf16 v[148:151], v[40:43], v[192:195], v[148:151]\n\t"
            "s_nop 1\n\t"
            "v_cvt_pk_bf16_f32 v144, v144, v145\n\t"
            "v_mfma_f32_16x16x32_bf16 v[156:159], v[92:95], v[172:175], v[156:159]\n\t"
            "v_cvt_pk_bf16_f32 v145, v146, v147\n\t"
            "v_mfma_f32_16x16x32_bf16 v[156:159], v[88:91], v[192:195], v[156:159]\n\t"
            "v_cvt_pk_bf16_f32 v168, v168, v169\n\t"
            "v_cvt_pk_bf16_f32 v169, v170, v171\n\t"
            "v_mfma_f32_16x16x32_bf16 v[180:183], v[140:143], v[172:175], v[180:183]\n\t"
            "ds_read_b128 v[172:175], v243 offset:12416\n\t"
            "v_cvt_pk_bf16_f32 v164, v164, v165\n\t"
            "v_mfma_f32_16x16x32_bf16 v[180:183], v[136:139], v[192:195], v[180:183]\n\t"
            "ds_read_b128 v[192:195], v242 offset:12416\n\t"
            "v_cvt_pk_bf16_f32 v165, v166, v167\n\t"
            "s_waitcnt lgkmcnt(2)\n\t"
            "v_mfma_f32_16x16x32_bf16 v[152:155], v[0:3], v[184:187], v[152:155]\n\t"
            "v_cvt_pk_bf16_f32 v146, v148, v149\n\t"
            "v_cvt_pk_bf16_f32 v147, v150, v151\n\t"
            "v_mfma_f32_16x16x32_bf16 v[152:155], v[4:7], v[188:191], v[152:155]\n\t"
            "v_cvt_pk_bf16_f32 v170, v156, v157\n\t"
            "v_cvt_pk_bf16_f32 v171, v158, v159\n\t"
            "v_mfma_f32_16x16x32_bf16 v[160:163], v[28:31], v[188:191], v[160:163]\n\t"
            "v_cvt_pk_bf16_f32 v166, v180, v181\n\t"
            "v_cvt_pk_bf16_f32 v167, v182, v183\n\t"
            "v_mfma_f32_16x16x32_bf16 v[160:163], v[24:27], v[184:187], v[160:163]\n\t"
            "v_lshlrev_b32 v148, 16, v230\n\t"
            "v_and_b32 v149, 0xffff0000, v230\n\t"
            "v_mfma_f32_16x16x32_bf16 v[176:179], v[48:51], v[184:187], v[176:179]\n\t"
            "v_lshlrev_b32 v150, 16, v231\n\t"
            "v_and_b32 v151, 0xffff0000, v231\n\t"
            "v_mfma_f32_16x16x32_bf16 v[176:179], v[52:55], v[188:191], v[176:179]\n\t"
            "ds_read_b128 v[228:231], v237 offset:12288\n\t"
            "v_lshlrev_b32 v156, 16, v232\n\t"
            "v_mfma_f32_16x16x32_bf16 v[148:151], v[76:79], v[188:191], v[148:151]\n\t"
            "v_and_b32 v157, 0xffff0000, v232\n\t"
            "v_lshlrev_b32 v158, 16, v233\n\t"
            "v_mfma_f32_16x16x32_bf16 v[148:151], v[72:75], v[184:187], v[148:151]\n\t"
            "v_and_b32 v159, 0xffff0000, v233\n\t"
            "v_lshlrev_b32 v180, 16, v234\n\t"
            "s_waitcnt lgkmcnt(1)\n\t"
            "v_mfma_f32_16x16x32_bf16 v[152:155], v[8:11], v[192:195], v[152:155]\n\t"
            "v_and_b32 v181, 0xffff0000, v234\n\t"
            "v_lshlrev_b32 v182, 16, v235\n\t"
            "v_mfma_f32_16x16x32_bf16 v[152:155], v[12:15], v[172:175], v[152:155]\n\t"
            "v_and_b32 v183, 0xffff0000, v235\n\t"
            "v_mfma_f32_16x16x32_bf16 v[156:159], v[96:99], v[184:187], v[156:159]\n\t"
            "ds_read_b128 v[232:235], v248 offset:12288\n\t"
            "v_mfma_f32_16x16x32_bf16 v[156:159], v[100:103], v[188:191], v[156:159]\n\t"
            "buffer_store_dwordx4 v[144:147], v197, s[40:43], s45 offen\n\t"
            "v_mfma_f32_16x16x32_bf16 v[180:183], v[124:127], v[188:191], v[180:183]\n\t"
            "ds_read_b128 v[188:191], v243 offset:12544\n\t"
            "v_mfma_f32_16x16x32_bf16 v[180:183], v[120:123], v[184:187], v[180:183]\n\t"
            "ds_read_b128 v[184:187], v242 offset:12544\n\t"
            "buffer_store_dwordx4 v[168:171], v197, s[40:43], s45 offen offset:64\n\t"
            "v_mfma_f32_16x16x32_bf16 v[160:163], v[36:39], v[172:175], v[160:163]\n\t"
            "v_mfma_f32_16x16x32_bf16 v[160:163], v[32:35], v[192:195], v[160:163]\n\t"
            "buffer_store_dwordx4 v[164:167], v197, s[40:43], s45 offen offset:128\n\t"
            "v_mfma_f32_16x16x32_bf16 v[176:179], v[56:59], v[192:195], v[176:179]\n\t"
            "v_mfma_f32_16x16x32_bf16 v[176:179], v[60:63], v[172:175], v[176:179]\n\t"
            "v_mfma_f32_16x16x32_bf16 v[148:151], v[84:87], v[172:175], v[148:151]\n\t"
            "v_mfma_f32_16x16x32_bf16 v[148:151], v[80:83], v[192:195], v[148:151]\n\t"
            "s_waitcnt lgkmcnt(0)\n\t"
            "s_barrier\n\t"
            "v_mfma_f32_16x16x32_bf16 v[152:155], v[16:19], v[184:187], v[152:155]\n\t"
            "v_mfma_f32_16x16x32_bf16 v[152:155], v[20:23], v[188:191], v[152:155]\n\t"
            "v_mfma_f32_16x16x32_bf16 v[160:163], v[44:47], v[188:191], v[160:163]\n\t"
            "v_mfma_f32_16x16x32_bf16 v[160:163], v[40:43], v[184:187], v[160:163]\n\t"
            "v_mfma_f32_16x16x32_bf16 v[156:159], v[104:107], v[192:195], v[156:159]\n\t"
            "v_mfma_f32_16x16x32_bf16 v[156:159], v[108:111], v[172:175], v[156:159]\n\t"
            "v_mfma_f32_16x16x32_bf16 v[180:183], v[132:135], v[172:175], v[180:183]\n\t"
            "v_mfma_f32_16x16x32_bf16 v[180:183], v[128:131], v[192:195], v[180:183]\n\t"
            "s_nop 1\n\t"
            "v_cvt_pk_bf16_f32 v152, v152, v153\n\t"
            "v_mfma_f32_16x16x32_bf16 v[176:179], v[64:67], v[184:187], v[176:179]\n\t"
            "v_cvt_pk_bf16_f32 v153, v154, v155\n\t"
            "v_mfma_f32_16x16x32_bf16 v[176:179], v[68:71], v[188:191], v[176:179]\n\t"
            "v_cvt_pk_bf16_f32 v154, v160, v161\n\t"
            "v_cvt_pk_bf16_f32 v155, v162, v163\n\t"
            "v_mfma_f32_16x16x32_bf16 v[148:151], v[92:95], v[188:191], v[148:151]\n\t"
            "v_mfma_f32_16x16x32_bf16 v[148:151], v[88:91], v[184:187], v[148:151]\n\t"
            "buffer_store_dwordx4 v[152:155], v198, s[40:43], s45 offen\n\t"
            "v_mfma_f32_16x16x32_bf16 v[156:159], v[112:115], v[184:187], v[156:159]\n\t"
            "s_nop 1\n\t"
            "v_cvt_pk_bf16_f32 v176, v176, v177\n\t"
            "v_mfma_f32_16x16x32_bf16 v[156:159], v[116:119], v[188:191], v[156:159]\n\t"
            "v_cvt_pk_bf16_f32 v177, v178, v179\n\t"
            "v_mfma_f32_16x16x32_bf16 v[180:183], v[140:143], v[188:191], v[180:183]\n\t"
            "v_cvt_pk_bf16_f32 v178, v148, v149\n\t"
            "v_mfma_f32_16x16x32_bf16 v[180:183], v[136:139], v[184:187], v[180:183]\n\t"
            "v_cvt_pk_bf16_f32 v179, v150, v151\n\t"
            "buffer_store_dwordx4 v[176:179], v198, s[40:43], s45 offen offset:64\n\t"
            "s_nop 1\n\t"
            "v_cvt_pk_bf16_f32 v156, v156, v157\n\t"
            "v_cvt_pk_bf16_f32 v157, v158, v159\n\t"
            "s_nop 1\n\t"
            "v_cvt_pk_bf16_f32 v158, v180, v181\n\t"
            "v_cvt_pk_bf16_f32 v159, v182, v183\n\t"
            "buffer_store_dwordx4 v[156:159], v198, s[40:43], s45 offen offset:128\n\t"
            "s_add_u32 s45, s46, s27\n\t"
            "s_mov_b32 s46, s47\n\t"
            "s_mov_b32 s47, s68\n\t"
            "s_xor_b32 s48, s48, s71\n\t"
            "s_xor_b32 s67, s67, s71\n\t"
            "s_add_u32 s76, s76, 4\n\t"
            "s_add_u32 s70, s70, 4\n\t"
            "s_lshl_b32 s56, s56, 2\n\t"
            "s_lshr_b32 s49, s26, 30\n\t"
            "s_or_b32 s56, s56, s49\n\t"
            "s_cmp_lt_u32 s46, 0x7000000\n\t"
            "s_cbranch_scc1 .Lwb_loop%=\n\t"
            "s_branch .Lwb_end%=\n\t"
            ".Lwb_spin%=:\n\t"
            "s_sleep 1\n\t"
            "ds_read_b32 v247, v255 offset:7856\n\t"
            "s_waitcnt lgkmcnt(0)\n\t"
            "v_readfirstlane_b32 s77, v247\n\t"
            "s_cmp_ge_u32 s77, s70\n\t"
            "s_cbranch_scc0 .Lwb_spin%=\n\t"
            "s_branch .Lwb_go%=\n\t"
            ".Lwb_end%=:\n\t"
            "s_waitcnt lgkmcnt(0)\n\t"
            "s_nop 7\n\t"
            : "={v[0:3]}"(W[0]), "={v[4:7]}"(W[1]), "={v[8:11]}"(W[2]), "={v[12:15]}"(W[3]), "={v[16:19]}"(W[4]), "={v[20:23]}"(W[5]), "={v[24:27]}"(W[6]), "={v[28:31]}"(W[7]), "={v[32:35]}"(W[8]), "={v[36:39]}"(W[9]), "={v[40:43]}"(W[10]), "={v[44:47]}"(W[11]), "={v[48:51]}"(W[12]), "={v[52:55]}"(W[13]), "={v[56:59]}"(W[14]), "={v[60:63]}"(W[15]), "={v[64:67]}"(W[16]), "={v[68:71]}"(W[17]), "={v[72:75]}"(W[18]), "={v[76:79]}"(W[19]), "={v[80:83]}"(W[20]), "={v[84:87]}"(W[21]), "={v[88:91]}"(W[22]), "={v[92:95]}"(W[23]), "={v[96:99]}"(W[24]), "={v[100:103]}"(W[25]), "={v[104:107]}"(W[26]), "={v[108:111]}"(W[27]), "={v[112:115]}"(W[28]), "={v[116:119]}"(W[29]), "={v[120:123]}"(W[30]), "={v[124:127]}"(W[31]), "={v[128:131]}"(W[32]), "={v[132:135]}"(W[33]), "={v[136:139]}"(W[34]), "={v[140:143]}"(W[35]), "={v[200:203]}"(xl[0]), "={v[204:207]}"(xl[1]), "={v[208:211]}"(xl[2]), "={v[212:215]}"(xl[3]), "={v[216:219]}"(xl[4]), "={v[220:223]}"(xl[5]), "={v[224:227]}"(xl[6]), "={v[228:231]}"(xl[7]), "+{v[232:235]}"(xl[8]), B_SIO, "+{v247}"(l16_)
            : "{v242}"(oE_), "{v243}"(oO_), "{v244}"(pR0_), "{v245}"(pR1_), "{v246}"(pR2_), "{v250}"(pXw_), "{v251}"(pSTw_), "{v252}"(aCA_), "{v253}"(aB_), "{v254}"(aA_), "{v255}"(idx_), B_SIN, "{s44}"(ldsb + O_OFF)
            : "v144", "v145", "v146", "v147", "v148", "v149", "v150", "v151", "v152", "v153", "v154", "v155", "v156", "v157", "v158", "v159", "v160", "v161", "v162", "v163", "v164", "v165", "v166", "v167", "v168", "v169", "v170", "v171", "v172", "v173", "v174", "v175", "v176", "v177", "v178", "v179", "v180", "v181", "v182", "v183", "v184", "v185", "v186", "v187", "v188", "v189", "v190", "v191", "v192", "v193", "v194", "v195", "v196", "v197", "v198", "v199", "v236", "v237", "v248", "v249", B_SCLOB);
            t45 = s45; t56 = s56;
        }
        {
            int ln_;
            asm volatile("v_mbcnt_lo_u32_b32 %0, -1, 0\n\tv_mbcnt_hi_u32_b32 %0, -1, %0" : "=v"(ln_));
            const int l15_ = ln_ & 15;
            const int g_ = ln_ >> 4;
            KARGS
            const auto orsrc = __builtin_amdgcn_make_buffer_rsrc((void*)ob, 0, IMG * IMG * CH * 2, 0x00020000);
#pragma unroll
            for (int lt = 0; lt < 2; ++lt) {
                int i = 16 * lt + l15_;
                const unsigned en = *(const unsigned*)(smem + WL_OFF + i * 4);
                const bool ok = (en & 0x7ffffffu) != 0x7ffffffu;
                i = ok ? i : 0;
                const int lE = OL_OFF + i * 384 + ((g_ ^ ((i >> 1) & 7)) << 4);
                const unsigned o = ok ? (en & 0x7ffffffu) + LPOS(en >> 31) * ROWBYTES + LPOS(en >> 30) * (CH * 2) + 192 * (w - 2) + 16 * g_ : OOB;
                u32x4 rl[3];
#pragma unroll
                for (int k = 0; k < 3; ++k) rl[k] = __builtin_bit_cast(u32x4, __builtin_amdgcn_raw_buffer_load_b128(orsrc, o + 64 * k, 0, 0));
                if (lt == 1) {
                    const unsigned oE_ = ldsb + O_OFF + l15_ * 384 + ((g_ ^ (l15_ >> 1)) << 4), oO_ = oE_ ^ 64, aCA_ = ldsb + CT_OFF + ln_ * 16;
                    asm volatile(
            "s_waitcnt lgkmcnt(0)\n\t"
            "s_barrier\n\t"
            "s_nop 7\n\t"
            "ds_read_b128 v[184:187], v242\n\t"
            "ds_read_b128 v[188:191], v243\n\t"
            "ds_read_b128 v[192:195], v242 offset:128\n\t"
            "ds_read_b128 v[172:175], v243 offset:128\n\t"
            "ds_read_b128 v[196:199], v252\n\t"
            "v_lshlrev_b32 v152, 16, v200\n\t"
            "v_and_b32 v153, 0xffff0000, v200\n\t"
            "v_lshlrev_b32 v154, 16, v201\n\t"
            "v_and_b32 v155, 0xffff0000, v201\n\t"
            "v_lshlrev_b32 v156, 16, v202\n\t"
            "v_and_b32 v157, 0xffff0000, v202\n\t"
            "v_lshlrev_b32 v158, 16, v203\n\t"
            "v_and_b32 v159, 0xffff0000, v203\n\t"
            "v_lshlrev_b32 v160, 16, v204\n\t"
            "v_and_b32 v161, 0xffff0000, v204\n\t"
            "v_lshlrev_b32 v162, 16, v205\n\t"
            "v_and_b32 v163, 0xffff0000, v205\n\t"
            "v_lshlrev_b32 v164, 16, v206\n\t"
            "v_and_b32 v165, 0xffff0000, v206\n\t"
            "v_lshlrev_b32 v166, 16, v207\n\t"
            "v_and_b32 v167, 0xffff0000, v207\n\t"
            "v_lshlrev_b32 v176, 16, v208\n\t"
            "v_and_b32 v177, 0xffff0000, v208\n\t"
            "v_lshlrev_b32 v178, 16, v209\n\t"
            "v_and_b32 v179, 0xffff0000, v209\n\t"
            "v_lshlrev_b32 v180, 16, v210\n\t"
            "v_and_b32 v181, 0xffff0000, v210\n\t"
            "v_lshlrev_b32 v182, 16, v211\n\t"
            "v_and_b32 v183, 0xffff0000, v211\n\t"
            "v_lshlrev_b32 v144, 16, v212\n\t"
            "v_and_b32 v145, 0xffff0000, v212\n\t"
            "v_lshlrev_b32 v146, 16, v213\n\t"
            "s_waitcnt lgkmcnt(3)\n\t"
            "v_mfma_f32_16x16x32_bf16 v[152:155], v[0:3], v[184:187], v[152:155]\n\t"
            "v_and_b32 v147, 0xffff0000, v213\n\t"
            "v_lshlrev_b32 v148, 16, v214\n\t"
            "v_mfma_f32_16x16x32_bf16 v[152:155], v[4:7], v[188:191], v[152:155]\n\t"
            "s_bfe_u32 s57, s56, 0x20004\n\t"
            "s_lshl_b32 s58, s57, 3\n\t"
            "s_lshr_b32 s58, s55, s58\n\t"
            "s_lshl_b64 s[36:37], s[34:35], s58\n\t"
            "v_mfma_f32_16x16x32_bf16 v[156:159], v[28:31], v[188:191], v[156:159]\n\t"
            "v_and_b32 v149, 0xffff0000, v214\n\t"
            "v_lshlrev_b32 v150, 16, v215\n\t"
            "v_mfma_f32_16x16x32_bf16 v[156:159], v[24:27], v[184:187], v[156:159]\n\t"
            "s_bitcmp1_b32 s57, 1\n\t"
            "s_cselect_b32 s58, 1177344, 0\n\t"
            "s_bitcmp1_b32 s57, 0\n\t"
            "s_cselect_b32 s57, 2304, 0\n\t"
            "s_add_u32 s57, s57, s58\n\t"
            "v_mfma_f32_16x16x32_bf16 v[160:163], v[48:51], v[184:187], v[160:163]\n\t"
            "v_and_b32 v151, 0xffff0000, v215\n\t"
            "v_lshlrev_b32 v168, 16, v216\n\t"
            "v_mfma_f32_16x16x32_bf16 v[160:163], v[52:55], v[188:191], v[160:163]\n\t"
            "v_and_b32 v169, 0xffff0000, v216\n\t"
            "v_lshlrev_b32 v170, 16, v217\n\t"
            "v_mfma_f32_16x16x32_bf16 v[164:167], v[76:79], v[188:191], v[164:167]\n\t"
            "v_and_b32 v171, 0xffff0000, v217\n\t"
            "v_mfma_f32_16x16x32_bf16 v[164:167], v[72:75], v[184:187], v[164:167]\n\t"
            "s_waitcnt lgkmcnt(0)\n\t"
            "s_mov_b64 exec, s[36:37]\n\t"
            "v_add_u32 v196, s57, v196\n\t"
            "s_mov_b64 exec, -1\n\t"
            "v_mfma_f32_16x16x32_bf16 v[176:179], v[96:99], v[184:187], v[176:179]\n\t"
            "v_mfma_f32_16x16x32_bf16 v[176:179], v[100:103], v[188:191], v[176:179]\n\t"
            "v_mfma_f32_16x16x32_bf16 v[180:183], v[124:127], v[188:191], v[180:183]\n\t"
            "ds_read_b128 v[188:191], v243 offset:256\n\t"
            "v_mfma_f32_16x16x32_bf16 v[180:183], v[120:123], v[184:187], v[180:183]\n\t"
            "ds_read_b128 v[184:187], v242 offset:256\n\t"
            "v_mfma_f32_16x16x32_bf16 v[152:155], v[8:11], v[192:195], v[152:155]\n\t"
            "v_mfma_f32_16x16x32_bf16 v[152:155], v[12:15], v[172:175], v[152:155]\n\t"
            "v_mfma_f32_16x16x32_bf16 v[156:159], v[36:39], v[172:175], v[156:159]\n\t"
            "v_mfma_f32_16x16x32_bf16 v[156:159], v[32:35], v[192:195], v[156:159]\n\t"
            "v_mfma_f32_16x16x32_bf16 v[160:163], v[56:59], v[192:195], v[160:163]\n\t"
            "v_mfma_f32_16x16x32_bf16 v[160:163], v[60:63], v[172:175], v[160:163]\n\t"
            "v_mfma_f32_16x16x32_bf16 v[164:167], v[84:87], v[172:175], v[164:167]\n\t"
            "v_mfma_f32_16x16x32_bf16 v[164:167], v[80:83], v[192:195], v[164:167]\n\t"
            "v_mfma_f32_16x16x32_bf16 v[176:179], v[104:107], v[192:195], v[176:179]\n\t"
            "v_mfma_f32_16x16x32_bf16 v[176:179], v[108:111], v[172:175], v[176:179]\n\t"
            "v_mfma_f32_16x16x32_bf16 v[180:183], v[132:135], v[172:175], v[180:183]\n\t"
            "ds_read_b128 v[172:175], v243 offset:6144\n\t"
            "v_mfma_f32_16x16x32_bf16 v[180:183], v[128:131], v[192:195], v[180:183]\n\t"
            "ds_read_b128 v[192:195], v242 offset:6144\n\t"
            "s_waitcnt lgkmcnt(2)\n\t"
            "v_mfma_f32_16x16x32_bf16 v[152:155], v[16:19], v[184:187], v[152:155]\n\t"
            "v_mfma_f32_16x16x32_bf16 v[152:155], v[20:23], v[188:191], v[152:155]\n\t"
            "v_mfma_f32_16x16x32_bf16 v[160:163], v[64:67], v[184:187], v[160:163]\n\t"
            "v_mfma_f32_16x16x32_bf16 v[160:163], v[68:71], v[188:191], v[160:163]\n\t"
            "v_mfma_f32_16x16x32_bf16 v[176:179], v[112:115], v[184:187], v[176:179]\n\t"
            "v_mfma_f32_16x16x32_bf16 v[176:179], v[116:119], v[188:191], v[176:179]\n\t"
            "v_mfma_f32_16x16x32_bf16 v[156:159], v[44:47], v[188:191], v[156:159]\n\t"
            "v_mfma_f32_16x16x32_bf16 v[156:159], v[40:43], v[184:187], v[156:159]\n\t"
            "s_nop 1\n\t"
            "v_cvt_pk_bf16_f32 v152, v152, v153\n\t"
            "v_mfma_f32_16x16x32_bf16 v[164:167], v[92:95], v[188:191], v[164:167]\n\t"
            "v_cvt_pk_bf16_f32 v153, v154, v155\n\t"
            "v_mfma_f32_16x16x32_bf16 v[164:167], v[88:91], v[184:187], v[164:167]\n\t"
            "v_cvt_pk_bf16_f32 v160, v160, v161\n\t"
            "v_cvt_pk_bf16_f32 v161, v162, v163\n\t"
            "v_mfma_f32_16x16x32_bf16 v[180:183], v[140:143], v[188:191], v[180:183]\n\t"
            "ds_read_b128 v[188:191], v243 offset:6272\n\t"
            "v_cvt_pk_bf16_f32 v176, v176, v177\n\t"
            "v_mfma_f32_16x16x32_bf16 v[180:183], v[136:139], v[184:187], v[180:183]\n\t"
            "ds_read_b128 v[184:187], v242 offset:6272\n\t"
            "v_cvt_pk_bf16_f32 v177, v178, v179\n\t"
            "s_waitcnt lgkmcnt(2)\n\t"
            "v_mfma_f32_16x16x32_bf16 v[144:147], v[0:3], v[192:195], v[144:147]\n\t"
            "v_cvt_pk_bf16_f32 v154, v156, v157\n\t"
            "v_cvt_pk_bf16_f32 v155, v158, v159\n\t"
            "v_mfma_f32_16x16x32_bf16 v[144:147], v[4:7], v[172:175], v[144:147]\n\t"
            "v_cvt_pk_bf16_f32 v162, v164, v165\n\t"
            "v_cvt_pk_bf16_f32 v163, v166, v167\n\t"
            "v_mfma_f32_16x16x32_bf16 v[148:151], v[28:31], v[172:175], v[148:151]\n\t"
            "v_cvt_pk_bf16_f32 v178, v180, v181\n\t"
            "v_cvt_pk_bf16_f32 v179, v182, v183\n\t"
            "v_mfma_f32_16x16x32_bf16 v[148:151], v[24:27], v[192:195], v[148:151]\n\t"
            "v_lshlrev_b32 v156, 16, v218\n\t"
            "v_and_b32 v157, 0xffff0000, v218\n\t"
            "v_mfma_f32_16x16x32_bf16 v[168:171], v[48:51], v[192:195], v[168:171]\n\t"
            "v_lshlrev_b32 v158, 16, v219\n\t"
            "v_and_b32 v159, 0xffff0000, v219\n\t"
            "v_mfma_f32_16x16x32_bf16 v[168:171], v[52:55], v[172:175], v[168:171]\n\t"
            "v_lshlrev_b32 v164, 16, v220\n\t"
            "v_and_b32 v165, 0xffff0000, v220\n\t"
            "v_mfma_f32_16x16x32_bf16 v[156:159], v[76:79], v[172:175], v[156:159]\n\t"
            "v_lshlrev_b32 v166, 16, v221\n\t"
            "v_and_b32 v167, 0xffff0000, v221\n\t"
            "v_mfma_f32_16x16x32_bf16 v[156:159], v[72:75], v[192:195], v[156:159]\n\t"
            "v_lshlrev_b32 v180, 16, v222\n\t"
            "v_and_b32 v181, 0xffff0000, v222\n\t"
            "v_mfma_f32_16x16x32_bf16 v[164:167], v[96:99], v[192:195], v[164:167]\n\t"
            "v_lshlrev_b32 v182, 16, v223\n\t"
            "v_and_b32 v183, 0xffff0000, v223\n\t"
            "v_mfma_f32_16x16x32_bf16 v[164:167], v[100:103], v[172:175], v[164:167]\n\t"
            "buffer_store_dwordx4 v[152:155], v196, s[40:43], s45 offen\n\t"
            "v_mfma_f32_16x16x32_bf16 v[180:183], v[124:127], v[172:175], v[180:183]\n\t"
            "ds_read_b128 v[172:175], v243 offset:6400\n\t"
            "v_lshlrev_b32 v152, 16, v224\n\t"
            "v_mfma_f32_16x16x32_bf16 v[180:183], v[120:123], v[192:195], v[180:183]\n\t"
            "ds_read_b128 v[192:195], v242 offset:6400\n\t"
            "buffer_store_dwordx4 v[160:163], v196, s[40:43], s45 offen offset:64\n\t"
            "s_waitcnt lgkmcnt(2)\n\t"
            "v_mfma_f32_16x16x32_bf16 v[144:147], v[8:11], v[184:187], v[144:147]\n\t"
            "v_and_b32 v153, 0xffff0000, v224\n\t"
            "v_lshlrev_b32 v154, 16, v225\n\t"
            "v_mfma_f32_16x16x32_bf16 v[144:147], v[12:15], v[188:191], v[144:147]\n\t"
            "buffer_store_dwordx4 v[176:179], v196, s[40:43], s45 offen offset:128\n\t"
            "v_mfma_f32_16x16x32_bf16 v[148:151], v[36:39], v[188:191], v[148:151]\n\t"
            "v_and_b32 v155, 0xffff0000, v225\n\t"
            "v_lshlrev_b32 v160, 16, v226\n\t"
            "v_mfma_f32_16x16x32_bf16 v[148:151], v[32:35], v[184:187], v[148:151]\n\t"
            "v_and_b32 v161, 0xffff0000, v226\n\t"
            "v_lshlrev_b32 v162, 16, v227\n\t"
            "v_mfma_f32_16x16x32_bf16 v[168:171], v[56:59], v[184:187], v[168:171]\n\t"
            "v_and_b32 v163, 0xffff0000, v227\n\t"
            "v_lshlrev_b32 v176, 16, v228\n\t"
            "v_mfma_f32_16x16x32_bf16 v[168:171], v[60:63], v[188:191], v[168:171]\n\t"
            "v_and_b32 v177, 0xffff0000, v228\n\t"
            "v_lshlrev_b32 v178, 16, v229\n\t"
            "v_mfma_f32_16x16x32_bf16 v[156:159], v[84:87], v[188:191], v[156:159]\n\t"
            "v_and_b32 v179, 0xffff0000, v229\n\t"
            "v_mfma_f32_16x16x32_bf16 v[156:159], v[80:83], v[184:187], v[156:159]\n\t"
            "v_mfma_f32_16x16x32_bf16 v[164:167], v[104:107], v[184:187], v[164:167]\n\t"
            "v_mfma_f32_16x16x32_bf16 v[164:167], v[108:111], v[188:191], v[164:167]\n\t"
            "v_mfma_f32_16x16x32_bf16 v[180:183], v[132:135], v[188:191], v[180:183]\n\t"
            "ds_read_b128 v[188:191], v243 offset:12288\n\t"
            "v_mfma_f32_16x16x32_bf16 v[180:183], v[128:131], v[184:187], v[180:183]\n\t"
            "ds_read_b128 v[184:187], v242 offset:12288\n\t"
            "s_waitcnt lgkmcnt(2)\n\t"
            "v_mfma_f32_16x16x32_bf16 v[144:147], v[16:19], v[192:195], v[144:147]\n\t"
            "v_mfma_f32_16x16x32_bf16 v[144:147], v[20:23], v[172:175], v[144:147]\n\t"
            "v_mfma_f32_16x16x32_bf16 v[168:171], v[64:67], v[192:195], v[168:171]\n\t"
            "v_mfma_f32_16x16x32_bf16 v[168:171], v[68:71], v[172:175], v[168:171]\n\t"
            "v_mfma_f32_16x16x32_bf16 v[164:167], v[112:115], v[192:195], v[164:167]\n\t"
            "v_mfma_f32_16x16x32_bf16 v[164:167], v[116:119], v[172:175], v[164:167]\n\t"
            "v_mfma_f32_16x16x32_bf16 v[148:151], v[44:47], v[172:175], v[148:151]\n\t"
            "v_mfma_f32_16x16x32_bf16 v[148:151], v[40:43], v[192:195], v[148:151]\n\t"
            "s_nop 1\n\t"
            "v_cvt_pk_bf16_f32 v144, v144, v145\n\t"
            "v_mfma_f32_16x16x32_bf16 v[156:159], v[92:95], v[172:175], v[156:159]\n\t"
            "v_cvt_pk_bf16_f32 v145, v146, v147\n\t"
            "v_mfma_f32_16x16x32_bf16 v[156:159], v[88:91], v[192:195], v[156:159]\n\t"
            "v_cvt_pk_bf16_f32 v168, v168, v169\n\t"
            "v_cvt_pk_bf16_f32 v169, v170, v171\n\t"
            "v_mfma_f32_16x16x32_bf16 v[180:183], v[140:143], v[172:175], v[180:183]\n\t"
            "ds_read_b128 v[172:175], v243 offset:12416\n\t"
            "v_cvt_pk_bf16_f32 v164, v164, v165\n\t"
            "v_mfma_f32_16x16x32_bf16 v[180:183], v[136:139], v[192:195], v[180:183]\n\t"
            "ds_read_b128 v[192:195], v242 offset:12416\n\t"
            "v_cvt_pk_bf16_f32 v165, v166, v167\n\t"
            "s_waitcnt lgkmcnt(2)\n\t"
            "v_mfma_f32_16x16x32_bf16 v[152:155], v[0:3], v[184:187], v[152:155]\n\t"
            "v_cvt_pk_bf16_f32 v146, v148, v149\n\t"
            "v_cvt_pk_bf16_f32 v147, v150, v151\n\t"
            "v_mfma_f32_16x16x32_bf16 v[152:155], v[4:7], v[188:191], v[152:155]\n\t"
            "v_cvt_pk_bf16_f32 v170, v156, v157\n\t"
            "v_cvt_pk_bf16_f32 v171, v158, v159\n\t"
            "v_mfma_f32_16x16x32_bf16 v[160:163], v[28:31], v[188:191], v[160:163]\n\t"
            "v_cvt_pk_bf16_f32 v166, v180, v181\n\t"
            "v_cvt_pk_bf16_f32 v167, v182, v183\n\t"
            "v_mfma_f32_16x16x32_bf16 v[160:163], v[24:27], v[184:187], v[160:163]\n\t"
            "v_lshlrev_b32 v148, 16, v230\n\t"
            "v_and_b32 v149, 0xffff0000, v230\n\t"
            "v_mfma_f32_16x16x32_bf16 v[176:179], v[48:51], v[184:187], v[176:179]\n\t"
            "v_lshlrev_b32 v150, 16, v231\n\t"
            "v_and_b32 v151, 0xffff0000, v231\n\t"
            "v_mfma_f32_16x16x32_bf16 v[176:179], v[52:55], v[188:191], v[176:179]\n\t"
            "v_lshlrev_b32 v156, 16, v232\n\t"
            "v_and_b32 v157, 0xffff0000, v232\n\t"
            "v_mfma_f32_16x16x32_bf16 v[148:151], v[76:79], v[188:191], v[148:151]\n\t"
            "v_lshlrev_b32 v158, 16, v233\n\t"
            "v_and_b32 v159, 0xffff0000, v233\n\t"
            "v_mfma_f32_16x16x32_bf16 v[148:151], v[72:75], v[184:187], v[148:151]\n\t"
            "v_lshlrev_b32 v180, 16, v234\n\t"
            "v_and_b32 v181, 0xffff0000, v234\n\t"
            "v_mfma_f32_16x16x32_bf16 v[156:159], v[96:99], v[184:187], v[156:159]\n\t"
            "v_lshlrev_b32 v182, 16, v235\n\t"
            "v_and_b32 v183, 0xffff0000, v235\n\t"
            "v_mfma_f32_16x16x32_bf16 v[156:159], v[100:103], v[188:191], v[156:159]\n\t"
            "buffer_store_dwordx4 v[144:147], v197, s[40:43], s45 offen\n\t"
            "v_mfma_f32_16x16x32_bf16 v[180:183], v[124:127], v[188:191], v[180:183]\n\t"
            "ds_read_b128 v[188:191], v243 offset:12544\n\t"
            "v_mfma_f32_16x16x32_bf16 v[180:183], v[120:123], v[184:187], v[180:183]\n\t"
            "ds_read_b128 v[184:187], v242 offset:12544\n\t"
            "buffer_store_dwordx4 v[168:171], v197, s[40:43], s45 offen offset:64\n\t"
            "s_waitcnt lgkmcnt(2)\n\t"
            "v_mfma_f32_16x16x32_bf16 v[152:155], v[8:11], v[192:195], v[152:155]\n\t"
            "v_mfma_f32_16x16x32_bf16 v[152:155], v[12:15], v[172:175], v[152:155]\n\t"
            "buffer_store_dwordx4 v[164:167], v197, s[40:43], s45 offen offset:128\n\t"
            "v_mfma_f32_16x16x32_bf16 v[160:163], v[36:39], v[172:175], v[160:163]\n\t"
            "v_mfma_f32_16x16x32_bf16 v[160:163], v[32:35], v[192:195], v[160:163]\n\t"
            "v_mfma_f32_16x16x32_bf16 v[176:179], v[56:59], v[192:195], v[176:179]\n\t"
            "v_mfma_f32_16x16x32_bf16 v[176:179], v[60:63], v[172:175], v[176:179]\n\t"
            "v_mfma_f32_16x16x32_bf16 v[148:151], v[84:87], v[172:175], v[148:151]\n\t"
            "v_mfma_f32_16x16x32_bf16 v[148:151], v[80:83], v[192:195], v[148:151]\n\t"
            "s_waitcnt lgkmcnt(0)\n\t"
            "v_mfma_f32_16x16x32_bf16 v[152:155], v[16:19], v[184:187], v[152:155]\n\t"
            "v_mfma_f32_16x16x32_bf16 v[152:155], v[20:23], v[188:191], v[152:155]\n\t"
            "v_mfma_f32_16x16x32_bf16 v[160:163], v[44:47], v[188:191], v[160:163]\n\t"
            "v_mfma_f32_16x16x32_bf16 v[160:163], v[40:43], v[184:187], v[160:163]\n\t"
            "v_mfma_f32_16x16x32_bf16 v[156:159], v[104:107], v[192:195], v[156:159]\n\t"
            "v_mfma_f32_16x16x32_bf16 v[156:159], v[108:111], v[172:175], v[156:159]\n\t"
            "v_mfma_f32_16x16x32_bf16 v[180:183], v[132:135], v[172:175], v[180:183]\n\t"
            "v_mfma_f32_16x16x32_bf16 v[180:183], v[128:131], v[192:195], v[180:183]\n\t"
            "s_nop 1\n\t"
            "v_cvt_pk_bf16_f32 v152, v152, v153\n\t"
            "v_mfma_f32_16x16x32_bf16 v[176:179], v[64:67], v[184:187], v[176:179]\n\t"
            "v_cvt_pk_bf16_f32 v153, v154, v155\n\t"
            "v_mfma_f32_16x16x32_bf16 v[176:179], v[68:71], v[188:191], v[176:179]\n\t"
            "v_cvt_pk_bf16_f32 v154, v160, v161\n\t"
            "v_cvt_pk_bf16_f32 v155, v162, v163\n\t"
            "v_mfma_f32_16x16x32_bf16 v[148:151], v[92:95], v[188:191], v[148:151]\n\t"
            "v_mfma_f32_16x16x32_bf16 v[148:151], v[88:91], v[184:187], v[148:151]\n\t"
            "buffer_store_dwordx4 v[152:155], v198, s[40:43], s45 offen\n\t"
            "v_mfma_f32_16x16x32_bf16 v[156:159], v[112:115], v[184:187], v[156:159]\n\t"
            "s_nop 1\n\t"
            "v_cvt_pk_bf16_f32 v176, v176, v177\n\t"
            "v_mfma_f32_16x16x32_bf16 v[156:159], v[116:119], v[188:191], v[156:159]\n\t"
            "v_cvt_pk_bf16_f32 v177, v178, v179\n\t"
            "v_mfma_f32_16x16x32_bf16 v[180:183], v[140:143], v[188:191], v[180:183]\n\t"
            "v_cvt_pk_bf16_f32 v178, v148, v149\n\t"
            "v_mfma_f32_16x16x32_bf16 v[180:183], v[136:139], v[184:187], v[180:183]\n\t"
            "v_cvt_pk_bf16_f32 v179, v150, v151\n\t"
            "buffer_store_dwordx4 v[176:179], v198, s[40:43], s45 offen offset:64\n\t"
            "s_nop 1\n\t"
            "v_cvt_pk_bf16_f32 v156, v156, v157\n\t"
            "v_cvt_pk_bf16_f32 v157, v158, v159\n\t"
            "s_nop 1\n\t"
            "v_cvt_pk_bf16_f32 v158, v180, v181\n\t"
            "v_cvt_pk_bf16_f32 v159, v182, v183\n\t"
            "buffer_store_dwordx4 v[156:159], v198, s[40:43], s45 offen offset:128\n\t"
            "s_nop 7\n\t"
                    :: "{v[0:3]}"(W[0]), "{v[4:7]}"(W[1]), "{v[8:11]}"(W[2]), "{v[12:15]}"(W[3]), "{v[16:19]}"(W[4]), "{v[20:23]}"(W[5]), "{v[24:27]}"(W[6]), "{v[28:31]}"(W[7]), "{v[32:35]}"(W[8]), "{v[36:39]}"(W[9]), "{v[40:43]}"(W[10]), "{v[44:47]}"(W[11]), "{v[48:51]}"(W[12]), "{v[52:55]}"(W[13]), "{v[56:59]}"(W[14]), "{v[60:63]}"(W[15]), "{v[64:67]}"(W[16]), "{v[68:71]}"(W[17]), "{v[72:75]}"(W[18]), "{v[76:79]}"(W[19]), "{v[80:83]}"(W[20]), "{v[84:87]}"(W[21]), "{v[88:91]}"(W[22]), "{v[92:95]}"(W[23]), "{v[96:99]}"(W[24]), "{v[100:103]}"(W[25]), "{v[104:107]}"(W[26]), "{v[108:111]}"(W[27]), "{v[112:115]}"(W[28]), "{v[116:119]}"(W[29]), "{v[120:123]}"(W[30]), "{v[124:127]}"(W[31]), "{v[128:131]}"(W[32]), "{v[132:135]}"(W[33]), "{v[136:139]}"(W[34]), "{v[140:143]}"(W[35]), "{v[200:203]}"(xl[0]), "{v[204:207]}"(xl[1]), "{v[208:211]}"(xl[2]), "{v[212:215]}"(xl[3]), "{v[216:219]}"(xl[4]), "{v[220:223]}"(xl[5]), "{v[224:227]}"(xl[6]), "{v[228:231]}"(xl[7]), "{v[232:235]}"(xl[8]), "{v242}"(oE_), "{v243}"(oO_), "{v252}"(aCA_),
                       "{s[40:43]}"(orr), "{s45}"(t45), "{s56}"(t56), "{s55}"(CROW & 0x0F0F0F0Fu), "{s[34:35]}"(0x0001000100010001ull)
                    : "v144", "v145", "v146", "v147", "v148", "v149", "v150", "v151", "v152", "v153", "v154", "v155", "v156", "v157", "v158", "v159", "v160", "v161", "v162", "v163", "v164", "v165", "v166", "v167", "v168", "v169", "v170", "v171", "v172", "v173", "v174", "v175", "v176", "v177", "v178", "v179", "v180", "v181", "v182", "v183", "v184", "v185", "v186", "v187", "v188", "v189", "v190", "v191", "v192", "v193", "v194", "v195", "v196", "v197", "v198", "v199", "s36", "s37", "s57", "s58", "scc", "memory");
                }
                f32x4 acc[6];
#pragma unroll
                for (int ks = 0; ks < 6; ++ks) {
                    const bf16x8 of = *(const bf16x8*)(smem + ((ks & 1) ? (lE ^ 64) : lE) + (ks >> 1) * 128);
#pragma unroll
                    for (int j = 0; j < 6; ++j) acc[j] = MFMA32(W[j * 6 + ks], of, ks == 0 ? ZERO4 : acc[j]);
                }
#pragma unroll
                for (int k = 0; k < 3; ++k) {
                    const f32x4 a = acc[2 * k], b = acc[2 * k + 1];
                    const u32x4 ov = {pk2(a[0] + bflo(rl[k][0]), a[1] + bfhi(rl[k][0])), pk2(a[2] + bflo(rl[k][1]), a[3] + bfhi(rl[k][1])),
                                      pk2(b[0] + bflo(rl[k][2]), b[1] + bfhi(rl[k][2])), pk2(b[2] + bflo(rl[k][3]), b[3] + bfhi(rl[k][3]))};
                    __builtin_amdgcn_raw_buffer_store_b128(ov, orsrc, o + 64 * k, 0, 0);
                }
                SB();
            }
        }
        __builtin_amdgcn_endpgm();
    }
}

extern "C" void kernel_launch(void* const* d_in, const int* in_sizes, int n_in,
                              void* d_out, int out_size, void* d_ws, size_t ws_size,
                              hipStream_t stream) {
    const unsigned short* x = (const unsigned short*)d_in[0];
    const unsigned short* dw = (const unsigned short*)d_in[1];
    const unsigned short* wqkv = (const unsigned short*)d_in[2];
    const unsigned short* wo = (const unsigned short*)d_in[3];
    unsigned short* out = (unsigned short*)d_out;
    u32x4* wf = (u32x4*)d_ws;
    win_attn_kernel<<<GRID, 512, 0, stream>>>(x, dw, wqkv, wo, wf, out);
}
```

```cpp
#include <hip/hip_runtime.h>
#include <hip/hip_bf16.h>
#include <stdint.h>

typedef short bf16x8 __attribute__((ext_vector_type(8)));
typedef float f32x4 __attribute__((ext_vector_type(4)));
typedef float f32x2 __attribute__((ext_vector_type(2)));
typedef unsigned u32x2 __attribute__((ext_vector_type(2)));
typedef unsigned u32x4 __attribute__((ext_vector_type(4)));

constexpr int IMG = 511;
constexpr int CH = 192;
constexpr int NWX = 73;
constexpr int NWIN = NWX * NWX;
constexpr int GRID = 256;
constexpr int ROWBYTES = IMG * CH * 2;
constexpr int XBIAS = (IMG + 1) * CH * 2;
constexpr unsigned OOB = 0x40000000u;

constexpr int FLAG_OFF = 7856;
constexpr int X_OFF = 32768;
constexpr int X1_OFF = 53632;
constexpr int O_OFF = 74496;
constexpr int V_OFF = 92928;
constexpr int KQL_OFF = 111360;
constexpr int OL_OFF = 128256;
constexpr int AT_OFF = 137472;
constexpr int CT_OFF = AT_OFF + 19584;
constexpr int RS_OFF = CT_OFF + 4096;
constexpr int WL_OFF = RS_OFF + 2304 + 64;
constexpr int LDS_BYTES = WL_OFF + 128;
#define RS_BASE (RS_OFF + (wave < 4 ? wave * 384 : 768 + wave * 192))
constexpr int STL_OFF = O_OFF + 12288;
constexpr int DW_OFF = O_OFF + 8448;
#define LX_OFF(i) ((i) < 2 ? CT_OFF + 1024 + (i) * 384 : KQL_OFF + ((i) - 2) * 800)
constexpr int PF_OFF = CT_OFF + 2048;
constexpr int PSLEEP = 4;
#define LB_OFF (w < 3 ? V_OFF + w * 13952 : X1_OFF)
#define LPOS(wc) (((wc) & 1) ? 0 : 6)
constexpr unsigned CROW = 0x060a0230u;
constexpr unsigned CLANE = 0x00132C3Fu;

constexpr unsigned long long TOKMAP[6] = {0x20001c190f2a2202ull, 0x052517121f061a13ull, 0x181e08151d0b260cull, 0x1b2b2903102d0901ull, 0x04162e0e2824142cull, 0x110a072127232f0dull};
constexpr unsigned long long ROWMAP[8] = {0x08321b240f1c0006ull, 0x23310e2a09170d14ull, 0x210c2f030a273418ull, 0x1d2b11221f043739ull, 0x3234363a103e192dull, 0x291a20022c3f1605ull, 0x303a3c2628122e15ull, 0x39253d1e01070b13ull};

constexpr int WF_BYTES = 8 * 36 * 1024;
constexpr int FLG_OFF = WF_BYTES;
constexpr int ARR_OFF = FLG_OFF + 512;
constexpr int EPO_OFF = ARR_OFF + 8192;
constexpr int WS_BYTES = EPO_OFF + 128;
constexpr int SC1 = 16;

#define MFMA32(a, b, c) __builtin_amdgcn_mfma_f32_16x16x32_bf16((a), (b), (c), 0, 0, 0)
#define BC8(v) __builtin_bit_cast(bf16x8, (v))
#define BAR() asm volatile("s_waitcnt lgkmcnt(0)\n\ts_barrier" ::: "memory")
#define SB() __builtin_amdgcn_sched_barrier(0)
#define OPQ(x) asm volatile("" : "+v"(x))
#define ZERO4 ((f32x4){0.f, 0.f, 0.f, 0.f})

typedef __bf16 nbf16x2 __attribute__((ext_vector_type(2)));
__device__ __forceinline__ unsigned pk2(float a, float b) {
    f32x2 v = {a, b};
    nbf16x2 r = __builtin_convertvector(v, nbf16x2);
    return __builtin_bit_cast(unsigned, r);
}
__device__ __forceinline__ float bflo(unsigned u) { return __uint_as_float(u << 16); }
__device__ __forceinline__ float bfhi(unsigned u) { return __uint_as_float(u & 0xffff0000u); }

__device__ __forceinline__ float bf2f(unsigned short v) { return __uint_as_float((unsigned)v << 16); }
__device__ __forceinline__ unsigned byte_of(unsigned long long a, unsigned long long b, int i) {
    return (unsigned)(((i & 8) ? b : a) >> (8 * (i & 7))) & 0xFFu;
}
#define M24(a, b) ((unsigned)__mul24((int)(a), (int)(b)))
__device__ __forceinline__ void seq_to_win(int seq, int& wy, int& wx) {
    const bool last = seq >= 5110;
    const int s = last ? 35 : M24(seq, 28729) >> 22, r = seq - M24(146, s);
    const int kx = last ? M24(r, 171) >> 10 : r >> 2, pos = r - M24(last ? 6 : 4, kx);
    const bool low = last && pos >= (kx == 36 ? 2 : 4);
    wy = low ? 72 : 2 * s + (pos & 1);
    wx = 2 * kx + (low ? (kx == 36 ? 0 : pos - 4) : pos >> 1);
}
__device__ __forceinline__ int win_to_seq(int wy, int wx) {
    const int kx = wx >> 1;
    return M24(146, wy == 72 ? 35 : wy >> 1) + M24(wy >= 70 ? 6 : 4, kx) + (wy == 72 ? (kx == 36 ? 2 : 4 + (wx & 1)) : 2 * (wx & 1) + (wy & 1));
}
__device__ __forceinline__ int block_of(int wy, int wx) { return wy >= 70 ? 1295 + (wx & ~1) + (wy == 72) : M24(37, wy >> 1) + (wx >> 1); }
__device__ __forceinline__ void block_pos(int b, int& ky, int& kx) {
    const int s = M24(b, 1772) >> 16, l = b - 1295;
    kx = l >= 0 ? l >> 1 : b - M24(37, s);
    ky = l >= 0 ? 70 + 2 * (l & 1) : 2 * s;
}
__device__ __forceinline__ unsigned win_org(int wy, int wx) { return M24(wy, 7 * ROWBYTES) + M24(wx, 7 * CH * 2); }
__device__ __forceinline__ unsigned wl_entry(int wy, int wx) {
    return win_org(wy, wx) | (wy == 0 ? 0x08000000u : 0u) | (wx == 0 ? 0x10000000u : 0u) |
           (wx == NWX - 1 ? 0x20000000u : 0u) | ((unsigned)(wx & 1) << 30) | ((unsigned)(wy & 1) << 31);
}
__device__ __forceinline__ unsigned dma_off(int lane) {
    const int col = (lane * 43) >> 8;
    return (unsigned)(col * (CH * 2 - 96) + 16 * lane) | (col == 0 ? 0x10000000u : 0u) | (col == 8 ? 0x20000000u : 0u);
}

#if defined(__HIP_DEVICE_COMPILE__)
#define NOPK __attribute__((target("no-packed-fp32-ops")))
#else
#define NOPK
#endif
__global__ __launch_bounds__(512, 2) NOPK void win_attn_kernel(const unsigned short* __restrict__ x,
                                                          const unsigned short* __restrict__ dw, const unsigned short* __restrict__ wqkv,
                                                          const unsigned short* __restrict__ wo, u32x4* wf,
                                                          unsigned short* __restrict__ out) {
    __shared__ __attribute__((aligned(16))) char smem[LDS_BYTES];

    asm volatile("wk_code_begin:");
    const int tid = threadIdx.x;
    const int wave = __builtin_amdgcn_readfirstlane(tid >> 6);
    const int w = wave & 3;
    ((unsigned*)((char*)wf + (1 << 23)))[(blockIdx.x * 8 + wave) * 8 + 4] = (unsigned)__builtin_amdgcn_s_memrealtime();
    const unsigned ldsb = (unsigned)(size_t)smem;
    {
        unsigned long long pc;
        unsigned len, rel;
        asm volatile("s_getpc_b64 %0\n.Lwk_here%=:\n\ts_mov_b32 %1, wk_code_end-wk_code_begin\n\ts_mov_b32 %2, .Lwk_here%=-wk_code_begin" : "=s"(pc), "=s"(len), "=s"(rel));
        pc -= rel;
        const u32x4 cr = {(unsigned)pc, (unsigned)(pc >> 32) & 0xffffu, len, 0x00020000u};
        asm volatile("s_mov_b32 m0, %0\n\ts_nop 0\n\tbuffer_load_dwordx4 %1, %2, %3 offen lds"
                     :: "s"(ldsb + X1_OFF + 14336 + w * 1024), "v"((tid & 63) * 16), "s"(cr), "s"((wave * 32 + (blockIdx.x >> 3)) * 1024) : "m0", "memory");
    }
    asm volatile("" :: "s"(x), "s"(dw), "s"(wqkv), "s"(wo), "s"(wf));
    const auto wrsrc = __builtin_amdgcn_make_buffer_rsrc((void*)wf, 0, WS_BYTES, 0x00020000);
    unsigned tag_lo = 0, tag_hi = 0;
#define TAG_READY                                                                                  \
    {                                                                                              \
        unsigned long long tag_;                                                                   \
        asm volatile("s_load_dwordx2 %0, %1, %2\n\ts_waitcnt lgkmcnt(0)" : "=s"(tag_) : "s"(wf), "n"(EPO_OFF) : "memory"); \
        tag_lo = (unsigned)tag_ + 1u; tag_hi = (unsigned)(tag_ >> 32);                             \
    }
    if (blockIdx.x < 36) {
        const int lane = tid & 63, n = lane & 15, g = lane >> 4;
        const bool isO = wave >= 6;
        const int fr = isO ? 216 + 2 * blockIdx.x + (wave - 6) : 6 * blockIdx.x + wave;
        const int tile = fr / 6, ks = fr - 6 * tile, tw = tile / 6, mdt = tile - 6 * tw;
        const int col = isO ? 96 * (tw - 6) + 32 * (mdt >> 1) + 8 * (n >> 2) + 4 * (mdt & 1) + (n & 3)
                            : (tw < 4 ? (mdt < 3 ? 192 : -48) + 48 * tw : 96 * tw) + 16 * mdt + n;
        const int rb = isO ? 384 : 1152;
        const auto rr = __builtin_amdgcn_make_buffer_rsrc((void*)(isO ? wo : wqkv), 0, 192 * rb, 0x00020000);
        const int vo = (48 * (g >> 1) + 8 * (g & 1)) * rb + 2 * col;
        float v[8];
#pragma unroll
        for (int j = 0; j < 8; ++j) v[j] = bf2f(__builtin_amdgcn_raw_buffer_load_b16(rr, vo, (96 * (ks & 1) + 16 * (ks >> 1) + j) * rb, 0));
        TAG_READY
        float* red = (float*)smem;
        red[n * 36 + wave * 4 + g] = ((v[0] + v[1]) + (v[2] + v[3])) + ((v[4] + v[5]) + (v[6] + v[7]));
        BAR();
        float mean = 0.f;
#pragma unroll
        for (int i = 0; i < 6; ++i) {
            const f32x4 p = *(const f32x4*)(red + n * 36 + 4 * i);
            mean += (p[0] + p[1]) + (p[2] + p[3]);
        }
        mean = isO ? 0.f : mean * (1.f / 192.f);
        u32x4 o;
#pragma unroll
        for (int j = 0; j < 4; ++j) o[j] = pk2(v[2 * j] - mean, v[2 * j + 1] - mean);
        __builtin_amdgcn_raw_buffer_store_b128(o, wrsrc, lane * 16, fr * 1024, SC1);
        asm volatile("s_waitcnt vmcnt(0)" ::: "memory");
        BAR();
        if (tid == 0) __builtin_amdgcn_raw_buffer_store_b64((u32x2){tag_lo, tag_hi}, wrsrc, FLG_OFF + blockIdx.x * 8, 0, SC1);
    }
    const int xi = blockIdx.x >> 3, nshort = (blockIdx.x & 7) == 7 ? 5 : 6;
    const int start = 666 * (blockIdx.x & 7) + 21 * xi - (xi < nshort ? xi : nshort);
    const int cnt = xi < nshort ? 20 : 21;

    const auto xrsrc = __builtin_amdgcn_make_buffer_rsrc((void*)((const char*)x - XBIAS), 0, IMG * IMG * CH * 2 + XBIAS, 0x00020000);
    bf16x8 W[36];
#define POLL_ISSUE                                                                                  \
    asm volatile("s_mov_b32 m0, %0\n\ts_mov_b64 exec, 0x3ffff\n\tbuffer_load_dwordx4 %1, %2, %3 offen sc1 lds\n\ts_mov_b64 exec, -1" \
                 :: "s"(ldsb + PF_OFF + w * 512), "v"(lane * 16), "s"(wrsrc), "s"(FLG_OFF) : "m0", "memory");
#define POLL_W                                                                                      \
    {                                                                                               \
        const int fi_ = lane < 36 ? lane : 0;                                                       \
        for (int it_ = 0; it_ < 100000; ++it_) {                                                    \
            u32x2 f_;                                                                               \
            if (it_ < 48) asm volatile("ds_read_b64 %0, %1\n\ts_waitcnt lgkmcnt(0)" : "=v"(f_) : "v"(ldsb + PF_OFF + w * 512 + fi_ * 8) : "memory"); \
            else f_ = __builtin_amdgcn_raw_buffer_load_b64(wrsrc, FLG_OFF + fi_ * 8, 0, SC1);       \
            if (__builtin_amdgcn_ballot_w64(f_[0] != tag_lo || f_[1] != tag_hi) == 0ull) break;     \
            if (it_ < 47) POLL_ISSUE                                                                \
            __builtin_amdgcn_s_sleep(PSLEEP);                                                       \
        }                                                                                           \
        __builtin_amdgcn_fence(__ATOMIC_ACQUIRE, "wavefront");                                      \
        SB();                                                                                       \
    }
#define LANE_SETUP                                                                                 \
    int lane;                                                                                      \
    asm volatile("v_mbcnt_lo_u32_b32 %0, -1, 0\n\tv_mbcnt_hi_u32_b32 %0, -1, %0" : "=v"(lane));    \
    __builtin_assume(lane >= 0 && lane < 64);                                                      \
    const int g = lane >> 4, l15 = lane & 15, sw = l15 >> 1;                                       \
    int fE = X_OFF + l15 * 384 + ((g ^ sw) << 4);                                                  \
    OPQ(fE);

#define DMA_SLICE(en)                                                                              \
        {                                                                                          \
            const unsigned vo_ = dma_off(lane) & (0x401fffffu | ((en) & 0x38000000u));             \
            const unsigned so_ = ((en) & 0x7ffffffu) + 96 * w;                                     \
            _Pragma("unroll") for (int r = 0; r < 9; ++r) {                                        \
                unsigned m0_ = ldsb + w * 8192 + r * 872;                                          \
                unsigned sr_ = (r == 0 && ((en) & 0x08000000u)) ? OOB : so_ + r * ROWBYTES;        \
                asm volatile("s_mov_b32 m0, %0\n\ts_mov_b32 exec_hi, 0x3fffff\n\tbuffer_load_dwordx4 %1, %2, %3 offen lds\n\ts_mov_b32 exec_hi, -1" \
                             :: "s"(m0_), "v"(vo_), "s"(xrsrc), "s"(sr_) : "m0", "memory");        \
            }                                                                                      \
        }
#define XPAT(r) (((r) < 48 ? (r) : 48) * 384 + (((2 * w) ^ (((r) >> 1) & 7)) << 4))
#define SPAT(r) ((w >> 1) * 1024 + (w & 1) * 8 + (r) * 16)
#define CONV_CONSTS(var) \
            const unsigned long long rlo = g == 0 ? ROWMAP[0] : (g == 1 ? ROWMAP[2] : (g == 2 ? ROWMAP[4] : ROWMAP[6])); \
            const unsigned long long rhi = g == 0 ? ROWMAP[1] : (g == 1 ? ROWMAP[3] : (g == 2 ? ROWMAP[5] : ROWMAP[7])); \
            int xrow = byte_of(rlo, rhi, l15); \
            if (lane == (int)((CLANE >> (8 * (var))) & 0xFF)) xrow = 48; \
            if (lane == 63) xrow = (CROW >> (8 * (var))) & 0xFF; \
            const unsigned pXw_ = XPAT(xrow); \
            const unsigned pSTw_ = (w >> 1) * 1024 + (w & 1) * 8 + xrow * 16; \
            const int oy = (0x5320 >> (4 * (l15 >> 2))) & 15, ox = (0x5320 >> (4 * (l15 & 3))) & 15; \
            const unsigned aB_ = ldsb + w * 8192 + (oy + (g >> 1)) * 872 + (ox + 2 * (g & 1)) * 96; \
            const int dyi = g - (l15 >> 3); \
            const unsigned aA_ = ldsb + AT_OFF + (l15 & 3) * 16 + \
                                 ((dyi < 0 || dyi > 2) ? 18816 : (dyi == 0 ? 0 : (dyi == 1 ? 6208 : 12544)) + ((l15 >> 2) & 1) * 3072 + w * 768); \
            const unsigned idx_ = (l15 & 3) * 0x5555u; \
            const unsigned se = (l15 == 4 * g) ? 0x3F803F80u : 0u; \
            const u32x4 sel4 = {se, se, se, se};

    unsigned en0, en1;
    {
        LANE_SETUP
        const int px = (lane * 43) >> 8;

        {
            const auto dr = __builtin_amdgcn_make_buffer_rsrc((void*)dw, 0, 9 * 384, 0x00020000);
            unsigned m0_ = ldsb + DW_OFF + w * 864, vo_ = px * 288 + lane * 16;
            asm volatile("s_mov_b32 m0, %0\n\ts_mov_b32 exec_hi, 0x3fffff\n\tbuffer_load_dwordx4 %1, %2, %3 offen lds\n\ts_mov_b32 exec_hi, -1"
                         :: "s"(m0_), "v"(vo_), "s"(dr), "s"(96 * w) : "m0", "memory");
        }
        int wy, wx;
        seq_to_win(start + (lane < cnt ? lane : cnt - 1), wy, wx);
        const unsigned en = wl_entry(wy, wx);
        const int bk = block_of(wy, wx);
        en0 = __builtin_amdgcn_readlane(en, 0);
        en1 = __builtin_amdgcn_readlane(en, 1);
        const int key0 = __builtin_amdgcn_readlane(bk, 0), nblk = __builtin_amdgcn_readlane(bk, 63) - key0 + 1;
        int ky, kx;
        block_pos(key0 + (l15 < nblk ? l15 : 0), ky, kx);
        if (wave < 4) {
            DMA_SLICE(en0)
        } else {
            const unsigned org = l15 < nblk ? (win_org(ky, 2 * kx) + (5 * ROWBYTES + 5 * CH * 2 + XBIAS)) | (kx == 36 ? OOB : 0u) : OOB + 0x8000000u;
            const unsigned lo = (px & 3) * (CH * 2) + (lane - 6 * px) * 16, keep = (px & 2) ? ~0u : ~OOB;
#pragma unroll
            for (int p = 0; p < 4; ++p) {
                if (p < 3 || nblk > 6) {
                    const unsigned oa = __builtin_amdgcn_readlane(org, 2 * p), ob = __builtin_amdgcn_readlane(org, 2 * p + 1);
                    const unsigned vo_ = ((lane < 24 ? oa : ob) & keep) + lo;
#pragma unroll
                    for (int r = 0; r < 4; ++r) {
                        unsigned m0_ = ldsb + LB_OFF + (4 * p + r) * 872;
                        asm volatile("s_mov_b32 m0, %0\n\ts_mov_b32 exec_hi, 0xffff\n\tbuffer_load_dwordx4 %1, %2, %3 offen lds\n\ts_mov_b32 exec_hi, -1"
                                     :: "s"(m0_), "v"(vo_), "s"(xrsrc), "s"(96 * w + r * ROWBYTES) : "m0", "memory");
                    }
                }
            }
        }
        SB();
        if (wave == 2) {
            u32x4 so;
#pragma unroll
            for (int e = 0; e < 3; ++e) {
                const int tk = byte_of(TOKMAP[2 * e], TOKMAP[2 * e + 1], l15);
                const int ty = (tk * 37) >> 8, tx = tk - 7 * ty;
                so[e] = (unsigned)(ty * ROWBYTES + tx * (CH * 2)) + 16 * g;
            }
            so[3] = dma_off(lane);
            *(u32x4*)(smem + CT_OFF + lane * 16) = so;
        }
        if (wave == 0 && lane < 32) *(unsigned*)(smem + WL_OFF + lane * 4) = lane < cnt ? en : 0x7ffffffu;
        if (wave == 1 && lane == 0) *(u32x2*)(smem + FLAG_OFF) = (u32x2){0u, 0u};
        if (lane < 48) *(u32x4*)(smem + AT_OFF + 18816 + lane * 16) = (u32x4){0u, 0u, 0u, 0u};
        CONV_CONSTS(en0 >> 30)
        const int ly = ky + (g >> 1), lx = 2 * kx + (g & 1), li = win_to_seq(ly, lx) - start;
        const bool ok = ly < NWX && lx < NWX && li >= 0 && li < cnt && l15 < nblk;
        const int row = ok ? li : 47, b = l15 < nblk ? l15 : 0;
        const unsigned cB = wave < 4 ? aB_ : ldsb + LB_OFF + (4 * (b >> 1) + (g >> 1)) * 872 + (b & 1) * 384 + (g & 1) * 192;
        const unsigned cX = wave < 4 ? (lane == (int)((CLANE >> (8 * (en0 >> 30))) & 0xFF) ? LX_OFF(0) - X_OFF + 32 * w : pXw_) : XPAT(row), cS = wave < 4 ? pSTw_ : SPAT(row) + (unsigned)(STL_OFF - O_OFF - 18816);
        if (wave < 4) {
            TAG_READY
            *(u32x2*)(smem + PF_OFF + w * 512 + lane * 8) = (u32x2){~tag_lo, 0u};
        }
        asm volatile("s_waitcnt vmcnt(9)" ::: "memory");
        if (lane < 48) {
            unsigned short dv[9];
#pragma unroll
            for (int i = 0; i < 9; ++i) dv[i] = *(const unsigned short*)(smem + DW_OFF + w * 864 + i * 96 + lane * 2);
#pragma unroll
            for (int dy = 0; dy < 3; ++dy) {
                const unsigned a = dv[3 * dy], b2 = dv[3 * dy + 1] | (dy == 1 ? 0x3F800000u : 0u), c = dv[3 * dy + 2];
                char* p = smem + AT_OFF + (dy == 0 ? 0 : (dy == 1 ? 6208 : 12544)) + w * 768 + lane * 16;
                *(u32x4*)p = (u32x4){a, b2, c, 0u};
                *(u32x4*)(p + 3072) = (u32x4){0u, a, b2, c};
            }
        }
        asm volatile("s_waitcnt vmcnt(0) lgkmcnt(0)" ::: "memory");
        if (wave < 4) POLL_ISSUE
        asm volatile(
            "s_nop 7\n\t"
            "ds_read_b128 v[168:171], v254\n\t"
            "ds_read_b64 v[144:145], v253\n\t"
            "ds_read_b64 v[146:147], v253 offset:96\n\t"
            "ds_read_b64 v[148:149], v253 offset:1744\n\t"
            "ds_read_b64 v[150:151], v253 offset:1840\n\t"
            "ds_read_b128 v[172:175], v254 offset:64\n\t"
            "ds_read_b64 v[152:153], v253 offset:8\n\t"
            "ds_read_b64 v[154:155], v253 offset:104\n\t"
            "ds_read_b64 v[156:157], v253 offset:1752\n\t"
            "ds_read_b64 v[158:159], v253 offset:1848\n\t"
            "ds_read_b64 v[160:161], v253 offset:16\n\t"
            "ds_read_b64 v[162:163], v253 offset:112\n\t"
            "ds_read_b64 v[164:165], v253 offset:1760\n\t"
            "ds_read_b64 v[166:167], v253 offset:1856\n\t"
            "v_mov_b64 v[176:177], 0\n\t"
            "v_mov_b64 v[178:179], 0\n\t"
            "v_mov_b64 v[180:181], 0\n\t"
            "v_mov_b64 v[182:183], 0\n\t"
            "v_mov_b64 v[184:185], 0\n\t"
            "v_mov_b64 v[186:187], 0\n\t"
            "v_mov_b64 v[188:189], 0\n\t"
            "v_mov_b64 v[190:191], 0\n\t"
            "v_add_u32 v236, s67, v250\n\t"
            "v_xor_b32 v237, 16, v236\n\t"
            "v_add_u32 v248, s67, v251\n\t"
            "s_waitcnt lgkmcnt(9)\n\t"
            "v_smfmac_f32_16x16x64_bf16 v[176:179], v[168:171], v[144:151], v255\n\t"
            "ds_read_b128 v[168:171], v254 offset:128\n\t"
            "ds_read_b64 v[144:145], v253 offset:24\n\t"
            "s_waitcnt lgkmcnt(6)\n\t"
            "v_smfmac_f32_16x16x64_bf16 v[180:183], v[172:175], v[152:159], v255\n\t"
            "ds_read_b64 v[146:147], v253 offset:120\n\t"
            "ds_read_b64 v[148:149], v253 offset:1768\n\t"
            "ds_read_b64 v[150:151], v253 offset:1864\n\t"
            "ds_read_b128 v[172:175], v254 offset:192\n\t"
            "ds_read_b64 v[152:153], v253 offset:32\n\t"
            "ds_read_b64 v[154:155], v253 offset:128\n\t"
            "ds_read_b64 v[156:157], v253 offset:1776\n\t"
            "ds_read_b64 v[158:159], v253 offset:1872\n\t"
            "v_cvt_pk_bf16_f32 v176, v176, v177\n\t"
            "v_cvt_pk_bf16_f32 v177, v178, v179\n\t"
            "v_cvt_pk_bf16_f32 v178, v180, v181\n\t"
            "v_cvt_pk_bf16_f32 v179, v182, v183\n\t"
            "v_mfma_f32_4x4x4_16b_bf16 v[196:199], v[176:177], v[176:177], 0\n\t"
            "ds_write_b128 v236, v[176:179]\n\t"
            "v_mfma_f32_16x16x32_bf16 v[192:195], v[238:241], v[176:179], 0\n\t"
            "v_mov_b64 v[176:177], 0\n\t"
            "v_mov_b64 v[180:181], 0\n\t"
            "v_mfma_f32_4x4x4_16b_bf16 v[196:199], v[178:179], v[178:179], v[196:199]\n\t"
            "v_mov_b64 v[178:179], 0\n\t"
            "v_mov_b64 v[182:183], 0\n\t"
            "s_waitcnt lgkmcnt(10)\n\t"
            "v_smfmac_f32_16x16x64_bf16 v[184:187], v[168:171], v[160:167], v255\n\t"
            "ds_read_b128 v[168:171], v254 offset:256\n\t"
            "ds_read_b64 v[160:161], v253 offset:40\n\t"
            "ds_read_b64 v[162:163], v253 offset:136\n\t"
            "ds_read_b64 v[164:165], v253 offset:1784\n\t"
            "ds_read_b64 v[166:167], v253 offset:1880\n\t"
            "s_waitcnt lgkmcnt(10)\n\t"
            "v_smfmac_f32_16x16x64_bf16 v[188:191], v[172:175], v[144:151], v255\n\t"
            "ds_read_b128 v[172:175], v254 offset:320\n\t"
            "ds_read_b64 v[144:145], v253 offset:48\n\t"
            "ds_read_b64 v[146:147], v253 offset:144\n\t"
            "ds_read_b64 v[148:149], v253 offset:1792\n\t"
            "ds_read_b64 v[150:151], v253 offset:1888\n\t"
            "v_cvt_pk_bf16_f32 v184, v184, v185\n\t"
            "v_cvt_pk_bf16_f32 v185, v186, v187\n\t"
            "s_nop 0\n\t"
            "v_cvt_pk_bf16_f32 v186, v188, v189\n\t"
            "v_cvt_pk_bf16_f32 v187, v190, v191\n\t"
            "v_mfma_f32_4x4x4_16b_bf16 v[196:199], v[184:185], v[184:185], v[196:199]\n\t"
            "s_waitcnt lgkmcnt(14)\n\t"
            "ds_write_b128 v237, v[184:187]\n\t"
            "v_mfma_f32_16x16x32_bf16 v[192:195], v[238:241], v[184:187], v[192:195]\n\t"
            "v_mov_b64 v[184:185], 0\n\t"
            "v_mov_b64 v[188:189], 0\n\t"
            "v_mfma_f32_4x4x4_16b_bf16 v[196:199], v[186:187], v[186:187], v[196:199]\n\t"
            "v_mov_b64 v[186:187], 0\n\t"
            "v_mov_b64 v[190:191], 0\n\t"
            "s_waitcnt lgkmcnt(10)\n\t"
            "v_smfmac_f32_16x16x64_bf16 v[176:179], v[168:171], v[152:159], v255\n\t"
            "ds_read_b128 v[168:171], v254 offset:384\n\t"
            "ds_read_b64 v[152:153], v253 offset:56\n\t"
            "ds_read_b64 v[154:155], v253 offset:152\n\t"
            "ds_read_b64 v[156:157], v253 offset:1800\n\t"
            "ds_read_b64 v[158:159], v253 offset:1896\n\t"
            "s_waitcnt lgkmcnt(10)\n\t"
            "v_smfmac_f32_16x16x64_bf16 v[180:183], v[172:175], v[160:167], v255\n\t"
            "ds_read_b128 v[172:175], v254 offset:448\n\t"
            "ds_read_b64 v[160:161], v253 offset:64\n\t"
            "ds_read_b64 v[162:163], v253 offset:160\n\t"
            "ds_read_b64 v[164:165], v253 offset:1808\n\t"
            "ds_read_b64 v[166:167], v253 offset:1904\n\t"
            "v_cvt_pk_bf16_f32 v176, v176, v177\n\t"
            "v_cvt_pk_bf16_f32 v177, v178, v179\n\t"
            "s_nop 0\n\t"
            "v_cvt_pk_bf16_f32 v178, v180, v181\n\t"
            "v_cvt_pk_bf16_f32 v179, v182, v183\n\t"
            "v_mfma_f32_4x4x4_16b_bf16 v[196:199], v[176:177], v[176:177], v[196:199]\n\t"
            "s_waitcnt lgkmcnt(14)\n\t"
            "ds_write_b128 v236, v[176:179] offset:128\n\t"
            "v_mfma_f32_16x16x32_bf16 v[192:195], v[238:241], v[176:179], v[192:195]\n\t"
            "v_mov_b64 v[176:177], 0\n\t"
            "v_mov_b64 v[180:181], 0\n\t"
            "v_mfma_f32_4x4x4_16b_bf16 v[196:199], v[178:179], v[178:179], v[196:199]\n\t"
            "v_mov_b64 v[178:179], 0\n\t"
            "v_mov_b64 v[182:183], 0\n\t"
            "s_waitcnt lgkmcnt(10)\n\t"
            "v_smfmac_f32_16x16x64_bf16 v[184:187], v[168:171], v[144:151], v255\n\t"
            "ds_read_b128 v[168:171], v254 offset:512\n\t"
            "ds_read_b64 v[144:145], v253 offset:72\n\t"
            "ds_read_b64 v[146:147], v253 offset:168\n\t"
            "ds_read_b64 v[148:149], v253 offset:1816\n\t"
            "ds_read_b64 v[150:151], v253 offset:1912\n\t"
            "s_waitcnt lgkmcnt(10)\n\t"
            "v_smfmac_f32_16x16x64_bf16 v[188:191], v[172:175], v[152:159], v255\n\t"
            "ds_read_b128 v[172:175], v254 offset:576\n\t"
            "ds_read_b64 v[152:153], v253 offset:80\n\t"
            "ds_read_b64 v[154:155], v253 offset:176\n\t"
            "ds_read_b64 v[156:157], v253 offset:1824\n\t"
            "ds_read_b64 v[158:159], v253 offset:1920\n\t"
            "v_cvt_pk_bf16_f32 v184, v184, v185\n\t"
            "v_cvt_pk_bf16_f32 v185, v186, v187\n\t"
            "s_nop 0\n\t"
            "v_cvt_pk_bf16_f32 v186, v188, v189\n\t"
            "v_cvt_pk_bf16_f32 v187, v190, v191\n\t"
            "v_mfma_f32_4x4x4_16b_bf16 v[196:199], v[184:185], v[184:185], v[196:199]\n\t"
            "s_waitcnt lgkmcnt(14)\n\t"
            "ds_write_b128 v237, v[184:187] offset:128\n\t"
            "v_mfma_f32_16x16x32_bf16 v[192:195], v[238:241], v[184:187], v[192:195]\n\t"
            "v_mov_b64 v[184:185], 0\n\t"
            "v_mov_b64 v[188:189], 0\n\t"
            "v_mfma_f32_4x4x4_16b_bf16 v[196:199], v[186:187], v[186:187], v[196:199]\n\t"
            "v_mov_b64 v[186:187], 0\n\t"
            "v_mov_b64 v[190:191], 0\n\t"
            "s_waitcnt lgkmcnt(10)\n\t"
            "v_smfmac_f32_16x16x64_bf16 v[176:179], v[168:171], v[160:167], v255\n\t"
            "ds_read_b128 v[168:171], v254 offset:640\n\t"
            "ds_read_b64 v[160:161], v253 offset:88\n\t"
            "ds_read_b64 v[162:163], v253 offset:184\n\t"
            "ds_read_b64 v[164:165], v253 offset:1832\n\t"
            "ds_read_b64 v[166:167], v253 offset:1928\n\t"
            "s_waitcnt lgkmcnt(10)\n\t"
            "v_smfmac_f32_16x16x64_bf16 v[180:183], v[172:175], v[144:151], v255\n\t"
            "ds_read_b128 v[172:175], v254 offset:704\n\t"
            "v_cvt_pk_bf16_f32 v176, v176, v177\n\t"
            "v_cvt_pk_bf16_f32 v177, v178, v179\n\t"
            "s_nop 1\n\t"
            "v_mfma_f32_4x4x4_16b_bf16 v[196:199], v[176:177], v[176:177], v[196:199]\n\t"
            "s_nop 1\n\t"
            "v_cvt_pk_bf16_f32 v178, v180, v181\n\t"
            "v_cvt_pk_bf16_f32 v179, v182, v183\n\t"
            "s_nop 1\n\t"
            "v_mfma_f32_4x4x4_16b_bf16 v[196:199], v[178:179], v[178:179], v[196:199]\n\t"
            "ds_write_b128 v236, v[176:179] offset:256\n\t"
            "v_mfma_f32_16x16x32_bf16 v[192:195], v[238:241], v[176:179], v[192:195]\n\t"
            "s_waitcnt lgkmcnt(6)\n\t"
            "v_smfmac_f32_16x16x64_bf16 v[184:187], v[168:171], v[152:159], v255\n\t"
            "s_waitcnt lgkmcnt(1)\n\t"
            "v_smfmac_f32_16x16x64_bf16 v[188:191], v[172:175], v[160:167], v255\n\t"
            "s_nop 5\n\t"
            "v_cvt_pk_bf16_f32 v184, v184, v185\n\t"
            "v_cvt_pk_bf16_f32 v185, v186, v187\n\t"
            "s_nop 1\n\t"
            "v_mfma_f32_4x4x4_16b_bf16 v[196:199], v[184:185], v[184:185], v[196:199]\n\t"
            "v_cvt_pk_bf16_f32 v186, v188, v189\n\t"
            "v_cvt_pk_bf16_f32 v187, v190, v191\n\t"
            "s_nop 1\n\t"
            "v_mfma_f32_4x4x4_16b_bf16 v[196:199], v[186:187], v[186:187], v[196:199]\n\t"
            "v_mfma_f32_16x16x32_bf16 v[192:195], v[238:241], v[184:187], v[192:195]\n\t"
            "ds_write_b128 v237, v[184:187] offset:256\n\t"
            "s_nop 6\n\t"
            "v_cndmask_b32_e64 v193, v196, v197, s[60:61]\n\t"
            "v_cndmask_b32_e64 v193, v193, v198, s[62:63]\n\t"
            "v_cndmask_b32_e64 v193, v193, v199, s[64:65]\n\t"
            "ds_write_b64 v248, v[192:193] offset:18816\n\t"
            "s_waitcnt lgkmcnt(0)\n\t"
            "s_nop 7\n\t"
            :
            : "{v250}"(cX), "{v251}"(cS), "{v253}"(cB), "{v254}"(aA_), "{v255}"(idx_), "{v[238:241]}"(sel4), "{s67}"(ldsb + (wave < 4 ? X_OFF : O_OFF)),
              "{s[60:61]}"(0x2222222222222222ull), "{s[62:63]}"(0x4444444444444444ull), "{s[64:65]}"(0x8888888888888888ull)
            : "v144", "v145", "v146", "v147", "v148", "v149", "v150", "v151", "v152", "v153", "v154", "v155", "v156", "v157", "v158", "v159", "v160", "v161", "v162", "v163", "v164", "v165", "v166", "v167", "v168", "v169", "v170", "v171", "v172", "v173", "v174", "v175", "v176", "v177", "v178", "v179", "v180", "v181", "v182", "v183", "v184", "v185", "v186", "v187", "v188", "v189", "v190", "v191", "v192", "v193", "v194", "v195", "v196", "v197", "v198", "v199", "v236", "v237", "v248", "v249", "memory");
        if (wave < 4) {
            if (lane == 0) __builtin_amdgcn_raw_buffer_store_b64((u32x2){tag_lo, tag_hi}, wrsrc, ARR_OFF + (blockIdx.x * 4 + w) * 8, 0, SC1);
            POLL_W
        }
    }
    const unsigned long long mrow = 0x0001000100010001ull * ((1u << (cnt - 16)) - 1u);
#define KARGS                                                                                      \
    unsigned long long xb, wb, ob;                                                                 \
    asm volatile("s_load_dwordx2 %0, %3, 0x0\n\ts_load_dwordx2 %1, %3, 0x20\n\ts_load_dwordx2 %2, %3, 0x28\n\ts_waitcnt lgkmcnt(0)" \
                 : "=&s"(xb), "=&s"(wb), "=&s"(ob) : "s"(__builtin_amdgcn_kernarg_segment_ptr()) : "memory"); \
    xb -= XBIAS;                                                                                   \
    const u32x4 wr = {(unsigned)wb, (unsigned)(wb >> 32) & 0xffffu, (unsigned)WS_BYTES, 0x00020000u}; \
    const u32x4 orr = {(unsigned)ob, (unsigned)(ob >> 32) & 0xffffu, (unsigned)(IMG * IMG * CH * 2), 0x00020000u}; \
    const u32x4 xr = {(unsigned)xb, (unsigned)(xb >> 32) & 0xffffu, (unsigned)(IMG * IMG * CH * 2 + XBIAS), 0x00020000u};
    const int tk_ = (xi + 4) / 6;
    const unsigned touch = wave * 36864 + ((xi + 4 - 6 * tk_) * 6 + tk_) * 1024;
#define B_SCALARS \
            CONV_CONSTS(0) \
            const unsigned aCA_ = ldsb + CT_OFF + lane * 16; \
            KARGS \
            unsigned s45 = OOB, s46 = (en0 & 0x7ffffffu) + 96 * w, s47 = (en1 & 0x7ffffffu) + 96 * w; \
            unsigned s48 = ldsb + X_OFF, s67 = ldsb + X1_OFF, s76 = ldsb + WL_OFF + 8, s70 = 0; \
            unsigned s56 = __builtin_amdgcn_readfirstlane((en0 >> 30) * 4 + (en1 >> 30)); \
            unsigned s69 = 0x401fffffu | (en1 & 0x38000000u), s79 = (en1 & 0x08000000u) ? OOB : s47, s59 = ldsb + LX_OFF(1) + 32 * w; \
            unsigned long long xp = 0, sp = 0; \
            _Pragma("unroll") for (int v = 0; v < 4; ++v) { \
                const unsigned r = (CROW >> (8 * v)) & 0xFF; \
                xp |= (unsigned long long)XPAT(r) << (16 * v); \
                sp |= (unsigned long long)SPAT(r) << (16 * v); \
            }
#define B_SIO "+{s45}"(s45), "+{s46}"(s46), "+{s47}"(s47), "+{s48}"(s48), "+{s67}"(s67), "+{s76}"(s76), "+{s56}"(s56), "+{s70}"(s70), "+{s69}"(s69), "+{s79}"(s79), "+{s59}"(s59)
#define B_SIN "{v[238:241]}"(sel4), "{s[40:43]}"(orr), "{s[72:75]}"(xr), "{s39}"(0x401fffffu), "{s[28:31]}"(wr), "{s33}"(wave * 36864), "{s78}"(96 * w), \
              "{s27}"(96 * w - 384), "{s[50:51]}"(xp), "{s[52:53]}"(sp), "{s54}"(CLANE), "{s55}"(CROW & 0x0F0F0F0Fu), "{s[34:35]}"(0x0001000100010001ull), \
              "{s[60:61]}"(0x2222222222222222ull), "{s[62:63]}"(0x4444444444444444ull), "{s[64:65]}"(0x8888888888888888ull), \
              "{s66}"(ldsb + w * 8192), "{s71}"((ldsb + X_OFF) ^ (ldsb + X1_OFF))
#define B_SCLOB "s83", "s26", "s77", "s68", "s36", "s37", "s38", "s57", "s58", "s49", "vcc", "scc", "m0", "memory"

    if (wave < 4) {
        LANE_SETUP
        const int dW = X_OFF + l15 * 384 + (((2 * w + (g >> 1)) ^ sw) << 4) + 8 * (g & 1);
        const int vB = V_OFF + w * 4608 + 1024 + lane * 8, rsR = RS_BASE + l15 * 4;
        {
            const unsigned rsb = ldsb + RS_BASE;
            const unsigned pE_ = fE - X_OFF, aO_ = ldsb + dW + (O_OFF - X_OFF), sel_ = (l15 & 3) ? 0u : 1u, aVB_ = ldsb + vB;
            const unsigned pST_ = lane * 16, aRSw_ = rsb + lane * 4, aPLw_ = rsb + lane * 2, aRSr_ = ldsb + rsR;
            const unsigned g16_ = (l15 & 3) ? ldsb + AT_OFF + 18816 : g * 16, g8_ = (l15 & 3) ? ldsb + AT_OFF + 18816 : g * 8;
            const unsigned loVL_ = ((2 * w + (l15 >> 3)) << 4) + (l15 & 7) * 2, loOL_ = g * 128;
            const unsigned on = (l15 & 3) ? 0u : 0x3F803F80u;
            const u32x4 ones4 = {on, on, on, on};
            unsigned s44 = cnt, s48 = ldsb + X_OFF, s70 = 0;
            unsigned s72 = ldsb + KQL_OFF + w * 192, s73 = ldsb + OL_OFF, s74 = ldsb + KQL_OFF + 784, s77 = ldsb + OL_OFF, s76 = ldsb + KQL_OFF + 768 + w * 4;
            KARGS
            unsigned tt_;
            asm volatile(
            "s_nop 4\n\t"
            "buffer_load_dwordx4 v[140:143], v248, s[84:87], s88 offen sc1\n\t"
            "s_add_u32 s49, s46, 0\n\t"
            "buffer_load_dwordx4 v[0:3], v248, s[84:87], s49 offen sc1\n\t"
            "s_add_u32 s49, s46, 6144\n\t"
            "buffer_load_dwordx4 v[24:27], v248, s[84:87], s49 offen sc1\n\t"
            "s_add_u32 s49, s46, 12288\n\t"
            "buffer_load_dwordx4 v[48:51], v248, s[84:87], s49 offen sc1\n\t"
            "s_add_u32 s49, s46, 18432\n\t"
            "buffer_load_dwordx4 v[72:75], v248, s[84:87], s49 offen sc1\n\t"
            "s_add_u32 s49, s46, 24576\n\t"
            "buffer_load_dwordx4 v[96:99], v248, s[84:87], s49 offen sc1\n\t"
            "s_add_u32 s49, s46, 30720\n\t"
            "buffer_load_dwordx4 v[120:123], v248, s[84:87], s49 offen sc1\n\t"
            "s_add_u32 s49, s46, 1024\n\t"
            "buffer_load_dwordx4 v[4:7], v248, s[84:87], s49 offen sc1\n\t"
            "s_add_u32 s49, s46, 7168\n\t"
            "buffer_load_dwordx4 v[28:31], v248, s[84:87], s49 offen sc1\n\t"
            "s_add_u32 s49, s46, 13312\n\t"
            "buffer_load_dwordx4 v[52:55], v248, s[84:87], s49 offen sc1\n\t"
            "s_add_u32 s49, s46, 19456\n\t"
            "buffer_load_dwordx4 v[76:79], v248, s[84:87], s49 offen sc1\n\t"
            "s_add_u32 s49, s46, 25600\n\t"
            "buffer_load_dwordx4 v[100:103], v248, s[84:87], s49 offen sc1\n\t"
            "s_add_u32 s49, s46, 31744\n\t"
            "buffer_load_dwordx4 v[124:127], v248, s[84:87], s49 offen sc1\n\t"
            "s_add_u32 s49, s46, 2048\n\t"
            "buffer_load_dwordx4 v[8:11], v248, s[84:87], s49 offen sc1\n\t"
            "s_add_u32 s49, s46, 8192\n\t"
            "buffer_load_dwordx4 v[32:35], v248, s[84:87], s49 offen sc1\n\t"
            "s_add_u32 s49, s46, 14336\n\t"
            "buffer_load_dwordx4 v[56:59], v248, s[84:87], s49 offen sc1\n\t"
            "s_add_u32 s49, s46, 20480\n\t"
            "buffer_load_dwordx4 v[80:83], v248, s[84:87], s49 offen sc1\n\t"
            "s_add_u32 s49, s46, 26624\n\t"
            "buffer_load_dwordx4 v[104:107], v248, s[84:87], s49 offen sc1\n\t"
            "s_add_u32 s49, s46, 32768\n\t"
            "buffer_load_dwordx4 v[128:131], v248, s[84:87], s49 offen sc1\n\t"
            "s_add_u32 s49, s46, 3072\n\t"
            "buffer_load_dwordx4 v[12:15], v248, s[84:87], s49 offen sc1\n\t"
            "s_add_u32 s49, s46, 9216\n\t"
            "buffer_load_dwordx4 v[36:39], v248, s[84:87], s49 offen sc1\n\t"
            "s_add_u32 s49, s46, 15360\n\t"
            "buffer_load_dwordx4 v[60:63], v248, s[84:87], s49 offen sc1\n\t"
            "s_add_u32 s49, s46, 21504\n\t"
            "buffer_load_dwordx4 v[84:87], v248, s[84:87], s49 offen sc1\n\t"
            "s_add_u32 s49, s46, 27648\n\t"
            "buffer_load_dwordx4 v[108:111], v248, s[84:87], s49 offen sc1\n\t"
            "s_add_u32 s49, s46, 33792\n\t"
            "buffer_load_dwordx4 v[132:135], v248, s[84:87], s49 offen sc1\n\t"
            "s_add_u32 s49, s46, 4096\n\t"
            "buffer_load_dwordx4 v[16:19], v248, s[84:87], s49 offen sc1\n\t"
            "s_add_u32 s49, s46, 10240\n\t"
            "buffer_load_dwordx4 v[40:43], v248, s[84:87], s49 offen sc1\n\t"
            "s_add_u32 s49, s46, 16384\n\t"
            "buffer_load_dwordx4 v[64:67], v248, s[84:87], s49 offen sc1\n\t"
            "s_add_u32 s49, s46, 22528\n\t"
            "buffer_load_dwordx4 v[88:91], v248, s[84:87], s49 offen sc1\n\t"
            "s_add_u32 s49, s46, 28672\n\t"
            "buffer_load_dwordx4 v[112:115], v248, s[84:87], s49 offen sc1\n\t"
            "s_add_u32 s49, s46, 34816\n\t"
            "buffer_load_dwordx4 v[136:139], v248, s[84:87], s49 offen sc1\n\t"
            "s_add_u32 s49, s46, 5120\n\t"
            "buffer_load_dwordx4 v[20:23], v248, s[84:87], s49 offen sc1\n\t"
            "s_add_u32 s49, s46, 11264\n\t"
            "buffer_load_dwordx4 v[44:47], v248, s[84:87], s49 offen sc1\n\t"
            "s_add_u32 s49, s46, 17408\n\t"
            "buffer_load_dwordx4 v[68:71], v248, s[84:87], s49 offen sc1\n\t"
            "s_add_u32 s49, s46, 23552\n\t"
            "buffer_load_dwordx4 v[92:95], v248, s[84:87], s49 offen sc1\n\t"
            "s_add_u32 s49, s46, 29696\n\t"
            "buffer_load_dwordx4 v[116:119], v248, s[84:87], s49 offen sc1\n\t"
            "s_add_u32 s49, s46, 35840\n\t"
            "buffer_load_dwordx4 v[140:143], v248, s[84:87], s49 offen sc1\n\t"
            "s_waitcnt lgkmcnt(0)\n\t"
            "s_barrier\n\t"
            "s_setprio 2\n\t"
            "v_add_u32 v235, s71, v248\n\t"
            "v_add_u32 v232, s71, v244\n\t"
            "v_xor_b32 v233, 64, v232\n\t"
            "v_subrev_u32 v239, s75, v251\n\t"
            "v_mul_u32_u24 v239, 200, v239\n\t"
            "v_lshrrev_b32 v234, 3, v255\n\t"
            "v_add3_u32 v234, v234, v239, s72\n\t"
            "ds_read_b128 v[228:231], v235 offset:12288\n\t"
            "ds_read_b128 v[144:147], v232\n\t"
            "ds_read_b128 v[148:151], v233\n\t"
            "ds_read_b128 v[204:207], v232 offset:6144\n\t"
            "ds_read_b128 v[208:211], v233 offset:6144\n\t"
            "ds_read_b128 v[152:155], v232 offset:128\n\t"
            "ds_read_b128 v[156:159], v233 offset:128\n\t"
            "ds_read_b128 v[212:215], v232 offset:6272\n\t"
            "ds_read_b128 v[224:227], v233 offset:6272\n\t"
            "ds_read_b128 v[160:163], v232 offset:256\n\t"
            "ds_read_b128 v[164:167], v233 offset:256\n\t"
            "s_waitcnt vmcnt(24)\n\t"
            "s_waitcnt lgkmcnt(10)\n\t"
            "v_add_f32 v236, v228, v230\n\t"
            "v_add_f32 v237, v229, v231\n\t"
            "s_waitcnt lgkmcnt(8)\n\t"
            "v_mfma_f32_16x16x32_bf16 v[168:171], v[0:3], v[144:147], 0\n\t"
            "ds_read_b128 v[228:231], v235 offset:13312\n\t"
            "v_lshrrev_b32 v235, 4, v255\n\t"
            "v_mfma_f32_16x16x32_bf16 v[168:171], v[4:7], v[148:151], v[168:171]\n\t"
            "v_add3_u32 v235, v235, v239, s72\n\t"
            "v_mfma_f32_16x16x32_bf16 v[176:179], v[48:51], v[144:147], 0\n\t"
            "v_mfma_f32_16x16x32_bf16 v[176:179], v[52:55], v[148:151], v[176:179]\n\t"
            "v_mfma_f32_16x16x32_bf16 v[172:175], v[24:27], v[144:147], 0\n\t"
            "v_mfma_f32_16x16x32_bf16 v[172:175], v[28:31], v[148:151], v[172:175]\n\t"
            "v_mfma_f32_16x16x32_bf16 v[180:183], v[72:75], v[144:147], 0\n\t"
            "v_mfma_f32_16x16x32_bf16 v[180:183], v[76:79], v[148:151], v[180:183]\n\t"
            "v_mfma_f32_16x16x32_bf16 v[184:187], v[96:99], v[144:147], 0\n\t"
            "v_mfma_f32_16x16x32_bf16 v[184:187], v[100:103], v[148:151], v[184:187]\n\t"
            "v_mfma_f32_16x16x32_bf16 v[188:191], v[120:123], v[144:147], 0\n\t"
            "s_waitcnt lgkmcnt(0)\n\t"
            "v_add_f32 v228, v228, v230\n\t"
            "v_add_f32 v236, v236, v228\n\t"
            "v_mfma_f32_16x16x32_bf16 v[188:191], v[124:127], v[148:151], v[188:191]\n\t"
            "v_add_f32 v229, v229, v231\n\t"
            "v_mul_f32 v236, 0x3baaaaab, v236\n\t"
            "v_mfma_f32_16x16x32_bf16 v[192:195], v[0:3], v[204:207], 0\n\t"
            "v_add_f32 v237, v237, v229\n\t"
            "v_mul_f32_e64 v236, v236, -v236\n\t"
            "v_mfma_f32_16x16x32_bf16 v[192:195], v[4:7], v[208:211], v[192:195]\n\t"
            "v_fmac_f32 v236, 0x3baaaaab, v237\n\t"
            "v_max_f32 v236, 0, v236\n\t"
            "v_mfma_f32_16x16x32_bf16 v[196:199], v[24:27], v[204:207], 0\n\t"
            "v_add_f32 v236, 0x3727c5ac, v236\n\t"
            "v_rsq_f32 v236, v236\n\t"
            "v_mfma_f32_16x16x32_bf16 v[196:199], v[28:31], v[208:211], v[196:199]\n\t"
            "ds_write_b32 v249, v236\n\t"
            "v_mfma_f32_16x16x32_bf16 v[200:203], v[48:51], v[204:207], 0\n\t"
            "ds_read_b32 v236, v251\n\t"
            "ds_read_b32 v237, v251 offset:64\n\t"
            "v_mfma_f32_16x16x32_bf16 v[200:203], v[52:55], v[208:211], v[200:203]\n\t"
            "v_mfma_f32_16x16x32_bf16 v[216:219], v[72:75], v[204:207], 0\n\t"
            "v_mfma_f32_16x16x32_bf16 v[216:219], v[76:79], v[208:211], v[216:219]\n\t"
            "v_mfma_f32_16x16x32_bf16 v[220:223], v[96:99], v[204:207], 0\n\t"
            "v_mfma_f32_16x16x32_bf16 v[220:223], v[100:103], v[208:211], v[220:223]\n\t"
            "v_mfma_f32_16x16x32_bf16 v[228:231], v[120:123], v[204:207], 0\n\t"
            "ds_read_b128 v[204:207], v232 offset:6400\n\t"
            "v_add_u32 v232, s48, v244\n\t"
            "v_mfma_f32_16x16x32_bf16 v[228:231], v[124:127], v[208:211], v[228:231]\n\t"
            "ds_read_b128 v[208:211], v233 offset:6400\n\t"
            "s_waitcnt vmcnt(12)\n\t"
            "v_mfma_f32_16x16x32_bf16 v[168:171], v[8:11], v[152:155], v[168:171]\n\t"
            "v_xor_b32 v233, 64, v232\n\t"
            "v_mfma_f32_16x16x32_bf16 v[168:171], v[12:15], v[156:159], v[168:171]\n\t"
            "v_mfma_f32_16x16x32_bf16 v[176:179], v[56:59], v[152:155], v[176:179]\n\t"
            "v_mfma_f32_16x16x32_bf16 v[176:179], v[60:63], v[156:159], v[176:179]\n\t"
            "v_mfma_f32_16x16x32_bf16 v[172:175], v[32:35], v[152:155], v[172:175]\n\t"
            "v_mfma_f32_16x16x32_bf16 v[172:175], v[36:39], v[156:159], v[172:175]\n\t"
            "v_mfma_f32_16x16x32_bf16 v[180:183], v[80:83], v[152:155], v[180:183]\n\t"
            "v_mfma_f32_16x16x32_bf16 v[180:183], v[84:87], v[156:159], v[180:183]\n\t"
            "v_mfma_f32_16x16x32_bf16 v[184:187], v[104:107], v[152:155], v[184:187]\n\t"
            "v_mfma_f32_16x16x32_bf16 v[184:187], v[108:111], v[156:159], v[184:187]\n\t"
            "v_mfma_f32_16x16x32_bf16 v[188:191], v[128:131], v[152:155], v[188:191]\n\t"
            "v_mfma_f32_16x16x32_bf16 v[188:191], v[132:135], v[156:159], v[188:191]\n\t"
            "v_mfma_f32_16x16x32_bf16 v[192:195], v[8:11], v[212:215], v[192:195]\n\t"
            "v_mfma_f32_16x16x32_bf16 v[192:195], v[12:15], v[224:227], v[192:195]\n\t"
            "v_mfma_f32_16x16x32_bf16 v[196:199], v[32:35], v[212:215], v[196:199]\n\t"
            "v_mfma_f32_16x16x32_bf16 v[196:199], v[36:39], v[224:227], v[196:199]\n\t"
            "v_mfma_f32_16x16x32_bf16 v[200:203], v[56:59], v[212:215], v[200:203]\n\t"
            "v_mfma_f32_16x16x32_bf16 v[200:203], v[60:63], v[224:227], v[200:203]\n\t"
            "v_mfma_f32_16x16x32_bf16 v[216:219], v[80:83], v[212:215], v[216:219]\n\t"
            "v_mfma_f32_16x16x32_bf16 v[216:219], v[84:87], v[224:227], v[216:219]\n\t"
            "v_mfma_f32_16x16x32_bf16 v[220:223], v[104:107], v[212:215], v[220:223]\n\t"
            "v_mfma_f32_16x16x32_bf16 v[220:223], v[108:111], v[224:227], v[220:223]\n\t"
            "v_mfma_f32_16x16x32_bf16 v[228:231], v[128:131], v[212:215], v[228:231]\n\t"
            "ds_read_b128 v[212:215], v232 offset:256\n\t"
            "v_mfma_f32_16x16x32_bf16 v[228:231], v[132:135], v[224:227], v[228:231]\n\t"
            "ds_read_b128 v[224:227], v233 offset:256\n\t"
            "s_waitcnt vmcnt(0)\n\t"
            "v_mfma_f32_16x16x32_bf16 v[168:171], v[16:19], v[160:163], v[168:171]\n\t"
            "v_mfma_f32_16x16x32_bf16 v[168:171], v[20:23], v[164:167], v[168:171]\n\t"
            "v_mfma_f32_16x16x32_bf16 v[176:179], v[64:67], v[160:163], v[176:179]\n\t"
            "v_mfma_f32_16x16x32_bf16 v[176:179], v[68:71], v[164:167], v[176:179]\n\t"
            "v_mfma_f32_16x16x32_bf16 v[180:183], v[88:91], v[160:163], v[180:183]\n\t"
            "v_mfma_f32_16x16x32_bf16 v[180:183], v[92:95], v[164:167], v[180:183]\n\t"
            "v_mfma_f32_16x16x32_bf16 v[172:175], v[40:43], v[160:163], v[172:175]\n\t"
            "v_mfma_f32_16x16x32_bf16 v[172:175], v[44:47], v[164:167], v[172:175]\n\t"
            "s_waitcnt lgkmcnt(5)\n\t"
            "s_nop 0\n\t"
            "v_mul_f32 v168, v236, v168\n\t"
            "v_mfma_f32_16x16x32_bf16 v[184:187], v[112:115], v[160:163], v[184:187]\n\t"
            "v_mul_f32 v169, v236, v169\n\t"
            "v_mul_f32 v170, v236, v170\n\t"
            "v_mfma_f32_16x16x32_bf16 v[184:187], v[116:119], v[164:167], v[184:187]\n\t"
            "v_mul_f32 v176, v236, v176\n\t"
            "v_mul_f32 v177, v236, v177\n\t"
            "v_mfma_f32_16x16x32_bf16 v[188:191], v[136:139], v[160:163], v[188:191]\n\t"
            "v_mul_f32 v171, v236, v171\n\t"
            "v_mul_f32 v178, v236, v178\n\t"
            "v_mfma_f32_16x16x32_bf16 v[188:191], v[140:143], v[164:167], v[188:191]\n\t"
            "v_mul_f32 v179, v236, v179\n\t"
            "v_cvt_pk_bf16_f32 v168, v168, v169\n\t"
            "s_waitcnt lgkmcnt(2)\n\t"
            "v_mfma_f32_16x16x32_bf16 v[192:195], v[16:19], v[204:207], v[192:195]\n\t"
            "v_cvt_pk_bf16_f32 v176, v176, v177\n\t"
            "v_cvt_pk_bf16_f32 v180, v180, v181\n\t"
            "v_mfma_f32_16x16x32_bf16 v[192:195], v[20:23], v[208:211], v[192:195]\n\t"
            "v_mul_f32 v172, v236, v172\n\t"
            "v_mul_f32 v173, v236, v173\n\t"
            "v_mfma_f32_16x16x32_bf16 v[196:199], v[40:43], v[204:207], v[196:199]\n\t"
            "v_mul_f32 v174, v236, v174\n\t"
            "v_mul_f32 v175, v236, v175\n\t"
            "v_mfma_f32_16x16x32_bf16 v[196:199], v[44:47], v[208:211], v[196:199]\n\t"
            "v_cvt_pk_bf16_f32 v169, v170, v171\n\t"
            "v_cvt_pk_bf16_f32 v177, v178, v179\n\t"
            "v_mfma_f32_16x16x32_bf16 v[200:203], v[64:67], v[204:207], v[200:203]\n\t"
            "v_cvt_pk_bf16_f32 v181, v182, v183\n\t"
            "v_cvt_pk_bf16_f32 v170, v172, v173\n\t"
            "v_mfma_f32_16x16x32_bf16 v[200:203], v[68:71], v[208:211], v[200:203]\n\t"
            "v_cvt_pk_bf16_f32 v171, v174, v175\n\t"
            "v_cvt_pk_bf16_f32 v182, v184, v185\n\t"
            "v_mfma_f32_16x16x32_bf16 v[216:219], v[88:91], v[204:207], v[216:219]\n\t"
            "v_cvt_pk_bf16_f32 v183, v186, v187\n\t"
            "v_cvt_pk_bf16_f32 v178, v188, v189\n\t"
            "v_mfma_f32_16x16x32_bf16 v[216:219], v[92:95], v[208:211], v[216:219]\n\t"
            "v_cvt_pk_bf16_f32 v179, v190, v191\n\t"
            "v_mul_f32 v236, 0x3e553b94, v236\n\t"
            "v_mfma_f32_16x16x32_bf16 v[220:223], v[112:115], v[204:207], v[220:223]\n\t"
            "v_add_u32 v184, s76, v239\n\t"
            "v_add_u32 v185, s74, v239\n\t"
            "v_mfma_f32_16x16x32_bf16 v[220:223], v[116:119], v[208:211], v[220:223]\n\t"
            "s_mov_b64 exec, -1\n\t"
            "ds_write_b128 v234, v[168:171]\n\t"
            "ds_write_b128 v234, v[180:183] offset:64\n\t"
            "ds_write_b64 v235, v[176:177] offset:128\n\t"
            "ds_write_b64 v235, v[178:179] offset:160\n\t"
            "s_mov_b64 exec, -1\n\t"
            "v_mfma_f32_16x16x16_bf16 v[172:175], v[176:177], v[178:179], 0\n\t"
            "v_mul_f32 v192, v237, v192\n\t"
            "v_mul_f32 v193, v237, v193\n\t"
            "v_mul_f32 v200, v237, v200\n\t"
            "v_mfma_f32_16x16x32_bf16 v[228:231], v[136:139], v[204:207], v[228:231]\n\t"
            "ds_read_b128 v[204:207], v232\n\t"
            "v_mul_f32 v201, v237, v201\n\t"
            "v_mfma_f32_16x16x32_bf16 v[228:231], v[140:143], v[208:211], v[228:231]\n\t"
            "ds_read_b128 v[208:211], v233\n\t"
            "v_mul_f32 v194, v237, v194\n\t"
            "v_mfma_f32_16x16x32_bf16 v[172:175], v[168:171], v[180:183], v[172:175]\n\t"
            "v_mul_f32 v195, v237, v195\n\t"
            "v_mul_f32 v202, v237, v202\n\t"
            "v_mul_f32 v203, v237, v203\n\t"
            "v_cvt_pk_bf16_f32 v192, v192, v193\n\t"
            "v_cvt_pk_bf16_f32 v200, v200, v201\n\t"
            "v_cvt_pk_bf16_f32 v216, v216, v217\n\t"
            "v_mul_f32 v196, v237, v196\n\t"
            "v_mul_f32 v197, v237, v197\n\t"
            "v_mul_f32 v198, v237, v198\n\t"
            "v_mul_f32 v199, v237, v199\n\t"
            "v_cvt_pk_bf16_f32 v193, v194, v195\n\t"
            "v_cvt_pk_bf16_f32 v201, v202, v203\n\t"
            "v_cvt_pk_bf16_f32 v217, v218, v219\n\t"
            "v_cvt_pk_bf16_f32 v194, v196, v197\n\t"
            "v_cvt_pk_bf16_f32 v195, v198, v199\n\t"
            "v_cvt_pk_bf16_f32 v218, v220, v221\n\t"
            "v_cvt_pk_bf16_f32 v219, v222, v223\n\t"
            "v_cvt_pk_bf16_f32 v202, v228, v229\n\t"
            "v_cvt_pk_bf16_f32 v203, v230, v231\n\t"
            "v_mul_f32 v237, 0x3e553b94, v237\n\t"
            "v_add_u32 v220, s76, v239\n\t"
            "v_mfma_f32_16x16x16_bf16 v[196:199], v[200:201], v[202:203], 0\n\t"
            "s_and_b32 exec_lo, s56, 0x11111111\n\t"
            "s_and_b32 exec_hi, s57, 0x11111111\n\t"
            "ds_write_b32 v184, v172 offset:0\n\t"
            "s_and_b32 exec_lo, s56, 0x22222222\n\t"
            "s_and_b32 exec_hi, s57, 0x22222222\n\t"
            "ds_write_b32 v184, v173 offset:0\n\t"
            "s_and_b32 exec_lo, s56, 0x44444444\n\t"
            "s_and_b32 exec_hi, s57, 0x44444444\n\t"
            "ds_write_b32 v184, v174 offset:0\n\t"
            "s_and_b32 exec_lo, s56, 0x88888888\n\t"
            "s_and_b32 exec_hi, s57, 0x88888888\n\t"
            "ds_write_b32 v184, v175 offset:0\n\t"
            "s_mov_b64 exec, s[56:57]\n\t"
            "ds_write_b32 v185, v236 offset:0\n\t"
            "s_mov_b64 exec, -1\n\t"
            "s_waitcnt lgkmcnt(5)\n\t"
            "v_mfma_f32_16x16x32_bf16 v[144:147], v[0:3], v[204:207], 0\n\t"
            "v_add_u32 v221, s74, v239\n\t"
            "v_mfma_f32_16x16x32_bf16 v[144:147], v[4:7], v[208:211], v[144:147]\n\t"
            "s_mov_b64 exec, s[54:55]\n\t"
            "ds_write_b128 v234, v[192:195] offset:12800\n\t"
            "ds_write_b128 v234, v[216:219] offset:12864\n\t"
            "ds_write_b64 v235, v[200:201] offset:12928\n\t"
            "ds_write_b64 v235, v[202:203] offset:12960\n\t"
            "s_mov_b64 exec, -1\n\t"
            "v_mfma_f32_16x16x32_bf16 v[148:151], v[28:31], v[208:211], 0\n\t"
            "v_add_u32 v235, s48, v248\n\t"
            "v_mfma_f32_16x16x32_bf16 v[148:151], v[24:27], v[204:207], v[148:151]\n\t"
            "ds_read_b128 v[228:231], v235 offset:18816\n\t"
            "v_mfma_f32_16x16x32_bf16 v[152:155], v[48:51], v[204:207], 0\n\t"
            "v_mfma_f32_16x16x32_bf16 v[152:155], v[52:55], v[208:211], v[152:155]\n\t"
            "v_mfma_f32_16x16x32_bf16 v[156:159], v[76:79], v[208:211], 0\n\t"
            "v_mfma_f32_16x16x32_bf16 v[156:159], v[72:75], v[204:207], v[156:159]\n\t"
            "v_mfma_f32_16x16x32_bf16 v[160:163], v[96:99], v[204:207], 0\n\t"
            "v_mfma_f32_16x16x32_bf16 v[160:163], v[100:103], v[208:211], v[160:163]\n\t"
            "v_mfma_f32_16x16x32_bf16 v[164:167], v[124:127], v[208:211], 0\n\t"
            "ds_read_b128 v[208:211], v233 offset:128\n\t"
            "v_mfma_f32_16x16x32_bf16 v[164:167], v[120:123], v[204:207], v[164:167]\n\t"
            "ds_read_b128 v[204:207], v232 offset:128\n\t"
            "v_mfma_f32_16x16x32_bf16 v[196:199], v[192:195], v[216:219], v[196:199]\n\t"
            "s_waitcnt lgkmcnt(2)\n\t"
            "v_add_f32 v236, v228, v230\n\t"
            "s_nop 5\n\t"
            "s_and_b32 exec_lo, s78, 0x11111111\n\t"
            "s_and_b32 exec_hi, s79, 0x11111111\n\t"
            "ds_write_b32 v220, v196 offset:12800\n\t"
            "s_and_b32 exec_lo, s78, 0x22222222\n\t"
            "s_and_b32 exec_hi, s79, 0x22222222\n\t"
            "ds_write_b32 v220, v197 offset:12800\n\t"
            "s_and_b32 exec_lo, s78, 0x44444444\n\t"
            "s_and_b32 exec_hi, s79, 0x44444444\n\t"
            "ds_write_b32 v220, v198 offset:12800\n\t"
            "s_and_b32 exec_lo, s78, 0x88888888\n\t"
            "s_and_b32 exec_hi, s79, 0x88888888\n\t"
            "ds_write_b32 v220, v199 offset:12800\n\t"
            "s_mov_b64 exec, s[78:79]\n\t"
            "ds_write_b32 v221, v237 offset:12800\n\t"
            "s_mov_b64 exec, -1\n\t"
            "s_waitcnt lgkmcnt(5)\n\t"
            "v_mfma_f32_16x16x32_bf16 v[144:147], v[8:11], v[204:207], v[144:147]\n\t"
            "v_add_f32 v237, v229, v231\n\t"
            "v_mfma_f32_16x16x32_bf16 v[144:147], v[12:15], v[208:211], v[144:147]\n\t"
            "ds_read_b128 v[228:231], v235 offset:19840\n\t"
            "v_mfma_f32_16x16x32_bf16 v[148:151], v[36:39], v[208:211], v[148:151]\n\t"
            "v_mfma_f32_16x16x32_bf16 v[148:151], v[32:35], v[204:207], v[148:151]\n\t"
            "v_mfma_f32_16x16x32_bf16 v[152:155], v[56:59], v[204:207], v[152:155]\n\t"
            "v_mfma_f32_16x16x32_bf16 v[152:155], v[60:63], v[208:211], v[152:155]\n\t"
            "v_mfma_f32_16x16x32_bf16 v[156:159], v[84:87], v[208:211], v[156:159]\n\t"
            "v_mfma_f32_16x16x32_bf16 v[156:159], v[80:83], v[204:207], v[156:159]\n\t"
            "v_mfma_f32_16x16x32_bf16 v[160:163], v[104:107], v[204:207], v[160:163]\n\t"
            "v_mfma_f32_16x16x32_bf16 v[160:163], v[108:111], v[208:211], v[160:163]\n\t"
            "v_mfma_f32_16x16x32_bf16 v[164:167], v[132:135], v[208:211], v[164:167]\n\t"
            "ds_read_b128 v[208:211], v233 offset:6144\n\t"
            "v_mfma_f32_16x16x32_bf16 v[164:167], v[128:131], v[204:207], v[164:167]\n\t"
            "ds_read_b128 v[204:207], v232 offset:6144\n\t"
            "s_waitcnt lgkmcnt(2)\n\t"
            "v_add_f32 v228, v228, v230\n\t"
            "v_mfma_f32_16x16x32_bf16 v[144:147], v[16:19], v[212:215], v[144:147]\n\t"
            "v_add_f32 v236, v236, v228\n\t"
            "v_add_f32 v229, v229, v231\n\t"
            "v_mfma_f32_16x16x32_bf16 v[144:147], v[20:23], v[224:227], v[144:147]\n\t"
            "v_mul_f32 v236, 0x3baaaaab, v236\n\t"
            "v_add_f32 v237, v237, v229\n\t"
            "v_mfma_f32_16x16x32_bf16 v[148:151], v[44:47], v[224:227], v[148:151]\n\t"
            "v_mul_f32_e64 v236, v236, -v236\n\t"
            "v_fmac_f32 v236, 0x3baaaaab, v237\n\t"
            "v_mfma_f32_16x16x32_bf16 v[148:151], v[40:43], v[212:215], v[148:151]\n\t"
            "v_max_f32 v236, 0, v236\n\t"
            "v_add_f32 v236, 0x3727c5ac, v236\n\t"
            "v_mfma_f32_16x16x32_bf16 v[152:155], v[64:67], v[212:215], v[152:155]\n\t"
            "v_rsq_f32 v236, v236\n\t"
            "v_mfma_f32_16x16x32_bf16 v[152:155], v[68:71], v[224:227], v[152:155]\n\t"
            "ds_write_b32 v249, v236\n\t"
            "v_mfma_f32_16x16x32_bf16 v[156:159], v[92:95], v[224:227], v[156:159]\n\t"
            "ds_read_b32 v236, v251\n\t"
            "ds_read_b32 v237, v251 offset:64\n\t"
            "v_mfma_f32_16x16x32_bf16 v[156:159], v[88:91], v[212:215], v[156:159]\n\t"
            "ds_read_b32 v238, v251 offset:128\n\t"
            "v_mfma_f32_16x16x32_bf16 v[160:163], v[112:115], v[212:215], v[160:163]\n\t"
            "v_mfma_f32_16x16x32_bf16 v[160:163], v[116:119], v[224:227], v[160:163]\n\t"
            "v_mfma_f32_16x16x32_bf16 v[164:167], v[140:143], v[224:227], v[164:167]\n\t"
            "ds_read_b128 v[224:227], v233 offset:6272\n\t"
            "v_mfma_f32_16x16x32_bf16 v[164:167], v[136:139], v[212:215], v[164:167]\n\t"
            "ds_read_b128 v[212:215], v232 offset:6272\n\t"
            "s_waitcnt lgkmcnt(6)\n\t"
            "v_mfma_f32_16x16x32_bf16 v[168:171], v[0:3], v[204:207], 0\n\t"
            "v_cvt_pk_bf16_f32 v156, v156, v157\n\t"
            "v_cvt_pk_bf16_f32 v157, v158, v159\n\t"
            "v_mfma_f32_16x16x32_bf16 v[168:171], v[4:7], v[208:211], v[168:171]\n\t"
            "v_cvt_pk_bf16_f32 v158, v160, v161\n\t"
            "v_cvt_pk_bf16_f32 v159, v162, v163\n\t"
            "v_mfma_f32_16x16x32_bf16 v[172:175], v[28:31], v[208:211], 0\n\t"
            "v_mfma_f32_16x16x32_bf16 v[172:175], v[24:27], v[204:207], v[172:175]\n\t"
            "v_mfma_f32_16x16x32_bf16 v[176:179], v[48:51], v[204:207], 0\n\t"
            "s_waitcnt lgkmcnt(4)\n\t"
            "v_mul_f32 v144, v236, v144\n\t"
            "v_mul_f32 v145, v236, v145\n\t"
            "v_mfma_f32_16x16x32_bf16 v[176:179], v[52:55], v[208:211], v[176:179]\n\t"
            "v_mul_f32 v152, v236, v152\n\t"
            "v_mul_f32 v153, v236, v153\n\t"
            "v_mfma_f32_16x16x32_bf16 v[180:183], v[76:79], v[208:211], 0\n\t"
            "v_mul_f32 v146, v236, v146\n\t"
            "v_mul_f32 v147, v236, v147\n\t"
            "v_mfma_f32_16x16x32_bf16 v[180:183], v[72:75], v[204:207], v[180:183]\n\t"
            "v_mul_f32 v154, v236, v154\n\t"
            "v_mul_f32 v155, v236, v155\n\t"
            "v_mfma_f32_16x16x32_bf16 v[184:187], v[96:99], v[204:207], 0\n\t"
            "v_cvt_pk_bf16_f32 v144, v144, v145\n\t"
            "v_cvt_pk_bf16_f32 v152, v152, v153\n\t"
            "v_mfma_f32_16x16x32_bf16 v[184:187], v[100:103], v[208:211], v[184:187]\n\t"
            "v_mul_f32 v148, v236, v148\n\t"
            "v_mul_f32 v149, v236, v149\n\t"
            "v_mfma_f32_16x16x32_bf16 v[188:191], v[124:127], v[208:211], 0\n\t"
            "ds_read_b128 v[208:211], v233 offset:6400\n\t"
            "v_mul_f32 v150, v236, v150\n\t"
            "v_mfma_f32_16x16x32_bf16 v[188:191], v[120:123], v[204:207], v[188:191]\n\t"
            "ds_read_b128 v[204:207], v232 offset:6400\n\t"
            "v_mul_f32 v151, v236, v151\n\t"
            "s_waitcnt lgkmcnt(2)\n\t"
            "v_mfma_f32_16x16x32_bf16 v[168:171], v[8:11], v[212:215], v[168:171]\n\t"
            "v_cvt_pk_bf16_f32 v145, v146, v147\n\t"
            "v_cvt_pk_bf16_f32 v153, v154, v155\n\t"
            "v_mfma_f32_16x16x32_bf16 v[168:171], v[12:15], v[224:227], v[168:171]\n\t"
            "v_cvt_pk_bf16_f32 v146, v148, v149\n\t"
            "v_cvt_pk_bf16_f32 v147, v150, v151\n\t"
            "v_mfma_f32_16x16x32_bf16 v[172:175], v[36:39], v[224:227], v[172:175]\n\t"
            "v_cvt_pk_bf16_f32 v154, v164, v165\n\t"
            "v_cvt_pk_bf16_f32 v155, v166, v167\n\t"
            "v_mfma_f32_16x16x32_bf16 v[172:175], v[32:35], v[212:215], v[172:175]\n\t"
            "v_mul_f32 v236, 0x3e553b94, v236\n\t"
            "v_mfma_f32_16x16x32_bf16 v[176:179], v[56:59], v[212:215], v[176:179]\n\t"
            "v_mfma_f32_16x16x32_bf16 v[176:179], v[60:63], v[224:227], v[176:179]\n\t"
            "v_mfma_f32_16x16x32_bf16 v[180:183], v[84:87], v[224:227], v[180:183]\n\t"
            "v_mfma_f32_16x16x32_bf16 v[180:183], v[80:83], v[212:215], v[180:183]\n\t"
            "v_mfma_f32_16x16x32_bf16 v[184:187], v[104:107], v[212:215], v[184:187]\n\t"
            "v_mfma_f32_16x16x32_bf16 v[184:187], v[108:111], v[224:227], v[184:187]\n\t"
            "v_mfma_f32_16x16x32_bf16 v[188:191], v[132:135], v[224:227], v[188:191]\n\t"
            "ds_read_b128 v[224:227], v233 offset:12288\n\t"
            "v_mfma_f32_16x16x32_bf16 v[188:191], v[128:131], v[212:215], v[188:191]\n\t"
            "ds_read_b128 v[212:215], v232 offset:12288\n\t"
            "s_waitcnt lgkmcnt(2)\n\t"
            "v_mfma_f32_16x16x32_bf16 v[168:171], v[16:19], v[204:207], v[168:171]\n\t"
            "v_mfma_f32_16x16x32_bf16 v[168:171], v[20:23], v[208:211], v[168:171]\n\t"
            "v_mfma_f32_16x16x32_bf16 v[172:175], v[44:47], v[208:211], v[172:175]\n\t"
            "v_mfma_f32_16x16x32_bf16 v[172:175], v[40:43], v[204:207], v[172:175]\n\t"
            "v_mfma_f32_16x16x32_bf16 v[176:179], v[64:67], v[204:207], v[176:179]\n\t"
            "v_mfma_f32_16x16x32_bf16 v[176:179], v[68:71], v[208:211], v[176:179]\n\t"
            "v_mfma_f32_16x16x32_bf16 v[180:183], v[92:95], v[208:211], v[180:183]\n\t"
            "v_mfma_f32_16x16x32_bf16 v[180:183], v[88:91], v[204:207], v[180:183]\n\t"
            "v_mfma_f32_16x16x32_bf16 v[184:187], v[112:115], v[204:207], v[184:187]\n\t"
            "v_mfma_f32_16x16x32_bf16 v[184:187], v[116:119], v[208:211], v[184:187]\n\t"
            "v_mfma_f32_16x16x32_bf16 v[188:191], v[140:143], v[208:211], v[188:191]\n\t"
            "ds_read_b128 v[208:211], v233 offset:12416\n\t"
            "v_mfma_f32_16x16x32_bf16 v[188:191], v[136:139], v[204:207], v[188:191]\n\t"
            "ds_read_b128 v[204:207], v232 offset:12416\n\t"
            "s_waitcnt lgkmcnt(0)\n\t"
            "s_waitcnt lgkmcnt(0)\n\t"
            "s_memtime s[52:53]\n\t"
            "s_memrealtime s[50:51]\n\t"
            "s_waitcnt lgkmcnt(0)\n\t"
            "s_and_b32 s52, s52, 0xffff\n\t"
            "s_lshl_b32 s50, s50, 16\n\t"
            "s_or_b32 s52, s52, s50\n\t"
            "s_mov_b32 m0, s70\n\t"
            "s_nop 0\n\t"
            "s_movreld_b32 s20, s52\n\t"
            "s_nop 7\n\t"
            ".p2align 6\n\t"
            "s_nop 0\n\t"
            "s_nop 0\n\t"
            "s_nop 0\n\t"
            ".Lwa_loop%=:\n\t"
            "s_waitcnt lgkmcnt(2)\n\t"
            "v_mfma_f32_16x16x32_bf16 v[192:195], v[0:3], v[212:215], 0\n\t"
            "s_bfe_u32 s49, s70, 0x30001\n\t"
            "s_lshl_b32 s49, s49, 4\n\t"
            "s_or_b32 s73, s77, s49\n\t"
            "v_mfma_f32_16x16x32_bf16 v[192:195], v[4:7], v[224:227], v[192:195]\n\t"
            "v_mad_u32_u24 v220, v246, s72, v253\n\t"
            "v_mad_u32_u24 v216, v246, s72, v252\n\t"
            "v_mfma_f32_16x16x32_bf16 v[148:151], v[28:31], v[224:227], 0\n\t"
            "ds_read_b64 v[222:223], v220 offset:160\n\t"
            "ds_read_b64 v[220:221], v220 offset:128\n\t"
            "v_mfma_f32_16x16x32_bf16 v[148:151], v[24:27], v[212:215], v[148:151]\n\t"
            "v_mul_f32 v176, v237, v176\n\t"
            "v_mul_f32 v177, v237, v177\n\t"
            "v_mfma_f32_16x16x32_bf16 v[196:199], v[48:51], v[212:215], 0\n\t"
            "v_mul_f32 v178, v237, v178\n\t"
            "v_mul_f32 v179, v237, v179\n\t"
            "v_mfma_f32_16x16x32_bf16 v[196:199], v[52:55], v[224:227], v[196:199]\n\t"
            "v_cvt_pk_bf16_f32 v176, v176, v177\n\t"
            "v_mul_f32 v168, v237, v168\n\t"
            "v_mfma_f32_16x16x32_bf16 v[200:203], v[76:79], v[224:227], 0\n\t"
            "v_mul_f32 v169, v237, v169\n\t"
            "v_cvt_pk_bf16_f32 v177, v178, v179\n\t"
            "v_mfma_f32_16x16x32_bf16 v[200:203], v[72:75], v[212:215], v[200:203]\n\t"
            "v_mul_f32 v170, v237, v170\n\t"
            "v_mul_f32 v171, v237, v171\n\t"
            "v_mfma_f32_16x16x32_bf16 v[160:163], v[96:99], v[212:215], 0\n\t"
            "v_cvt_pk_bf16_f32 v168, v168, v169\n\t"
            "v_cvt_pk_bf16_f32 v180, v180, v181\n\t"
            "v_mfma_f32_16x16x32_bf16 v[160:163], v[100:103], v[224:227], v[160:163]\n\t"
            "v_cvt_pk_bf16_f32 v178, v188, v189\n\t"
            "v_cvt_pk_bf16_f32 v179, v190, v191\n\t"
            "v_mfma_f32_16x16x32_bf16 v[164:167], v[124:127], v[224:227], 0\n\t"
            "ds_read_b128 v[224:227], v233 offset:12544\n\t"
            "v_mul_f32 v172, v237, v172\n\t"
            "v_mfma_f32_16x16x32_bf16 v[164:167], v[120:123], v[212:215], v[164:167]\n\t"
            "ds_read_b128 v[212:215], v232 offset:12544\n\t"
            "v_mul_f32 v173, v237, v173\n\t"
            "s_waitcnt lgkmcnt(4)\n\t"
            "v_mfma_f32_16x16x32_bf16 v[192:195], v[8:11], v[204:207], v[192:195]\n\t"
            "v_mul_f32 v174, v237, v174\n\t"
            "v_mul_f32 v175, v237, v175\n\t"
            "v_mfma_f32_16x16x32_bf16 v[192:195], v[12:15], v[208:211], v[192:195]\n\t"
            "v_cvt_pk_bf16_f32 v169, v170, v171\n\t"
            "v_cvt_pk_bf16_f32 v181, v182, v183\n\t"
            "v_mfma_f32_16x16x32_bf16 v[196:199], v[56:59], v[204:207], v[196:199]\n\t"
            "v_cvt_pk_bf16_f32 v170, v172, v173\n\t"
            "v_cvt_pk_bf16_f32 v171, v174, v175\n\t"
            "v_mfma_f32_16x16x32_bf16 v[196:199], v[60:63], v[208:211], v[196:199]\n\t"
            "v_cvt_pk_bf16_f32 v182, v184, v185\n\t"
            "v_cvt_pk_bf16_f32 v183, v186, v187\n\t"
            "v_mfma_f32_16x16x32_bf16 v[160:163], v[104:107], v[204:207], v[160:163]\n\t"
            "v_mov_b32 v230, s76\n\t"
            "v_mov_b32 v239, s74\n\t"
            "v_mfma_f32_16x16x32_bf16 v[160:163], v[108:111], v[208:211], v[160:163]\n\t"
            "s_add_u32 s70, s70, 1\n\t"
            "s_add_u32 s72, s72, 800\n\t"
            "s_add_u32 s74, s74, 800\n\t"
            "s_add_u32 s76, s76, 800\n\t"
            "v_mfma_f32_16x16x32_bf16 v[148:151], v[36:39], v[208:211], v[148:151]\n\t"
            "s_add_u32 s77, s77, 384\n\t"
            "s_xor_b32 s48, s48, s47\n\t"
            "s_sub_u32 s44, s44, 1\n\t"
            "v_mfma_f32_16x16x32_bf16 v[148:151], v[32:35], v[204:207], v[148:151]\n\t"
            "ds_read_b32 v230, v230\n\t"
            "ds_read_b32 v239, v239\n\t"
            "v_mfma_f32_16x16x32_bf16 v[200:203], v[84:87], v[208:211], v[200:203]\n\t"
            "v_mul_f32 v237, 0x3e553b94, v237\n\t"
            "v_mfma_f32_16x16x32_bf16 v[200:203], v[80:83], v[204:207], v[200:203]\n\t"
            "v_mfma_f32_16x16x32_bf16 v[164:167], v[132:135], v[208:211], v[164:167]\n\t"
            "v_mfma_f32_16x16x32_bf16 v[164:167], v[128:131], v[204:207], v[164:167]\n\t"
            "s_waitcnt lgkmcnt(2)\n\t"
            "v_mfma_f32_16x16x32_bf16 v[192:195], v[16:19], v[212:215], v[192:195]\n\t"
            "v_mfma_f32_16x16x32_bf16 v[192:195], v[20:23], v[224:227], v[192:195]\n\t"
            "v_mfma_f32_16x16x32_bf16 v[196:199], v[64:67], v[212:215], v[196:199]\n\t"
            "v_mfma_f32_16x16x32_bf16 v[196:199], v[68:71], v[224:227], v[196:199]\n\t"
            "v_mfma_f32_16x16x32_bf16 v[160:163], v[112:115], v[212:215], v[160:163]\n\t"
            "v_mfma_f32_16x16x32_bf16 v[160:163], v[116:119], v[224:227], v[160:163]\n\t"
            "v_mfma_f32_16x16x32_bf16 v[148:151], v[44:47], v[224:227], v[148:151]\n\t"
            "v_mfma_f32_16x16x32_bf16 v[148:151], v[40:43], v[212:215], v[148:151]\n\t"
            "s_nop 1\n\t"
            "v_mul_f32 v192, v238, v192\n\t"
            "v_mfma_f32_16x16x32_bf16 v[200:203], v[92:95], v[224:227], v[200:203]\n\t"
            "v_mul_f32 v193, v238, v193\n\t"
            "v_mul_f32 v194, v238, v194\n\t"
            "v_mfma_f32_16x16x32_bf16 v[200:203], v[88:91], v[212:215], v[200:203]\n\t"
            "v_mul_f32 v195, v238, v195\n\t"
            "v_cvt_pk_bf16_f32 v192, v192, v193\n\t"
            "v_mfma_f32_16x16x32_bf16 v[164:167], v[140:143], v[224:227], v[164:167]\n\t"
            "ds_read_b128 v[224:227], v216 offset:64\n\t"
            "ds_read_b128 v[216:219], v216\n\t"
            "v_mfma_f32_16x16x32_bf16 v[164:167], v[136:139], v[212:215], v[164:167]\n\t"
            "v_mul_f32 v148, v238, v148\n\t"
            "v_mul_f32 v149, v238, v149\n\t"
            "v_mfma_f32_16x16x16_bf16 v[172:175], v[220:221], v[154:155], 0\n\t"
            "v_mul_f32 v150, v238, v150\n\t"
            "v_mul_f32 v151, v238, v151\n\t"
            "v_cvt_pk_bf16_f32 v193, v194, v195\n\t"
            "v_mfma_f32_16x16x16_bf16 v[184:187], v[152:153], v[178:179], 0\n\t"
            "v_mul_f32 v196, v238, v196\n\t"
            "v_mul_f32 v197, v238, v197\n\t"
            "v_cvt_pk_bf16_f32 v194, v148, v149\n\t"
            "v_mfma_f32_16x16x16_bf16 v[208:211], v[220:221], v[178:179], 0\n\t"
            "v_cvt_pk_bf16_f32 v195, v150, v151\n\t"
            "v_cvt_pk_bf16_f32 v200, v200, v201\n\t"
            "v_mul_f32 v198, v238, v198\n\t"
            "v_mfma_f32_16x16x16_bf16 v[148:151], v[152:153], v[154:155], 0\n\t"
            "v_mul_f32 v199, v238, v199\n\t"
            "v_cvt_pk_bf16_f32 v196, v196, v197\n\t"
            "v_cvt_pk_bf16_f32 v201, v202, v203\n\t"
            "v_mfma_f32_16x16x16_bf16 v[204:207], v[176:177], v[178:179], 0\n\t"
            "v_cvt_pk_bf16_f32 v197, v198, v199\n\t"
            "v_cvt_pk_bf16_f32 v202, v160, v161\n\t"
            "v_cvt_pk_bf16_f32 v203, v162, v163\n\t"
            "v_mfma_f32_16x16x16_bf16 v[160:163], v[176:177], v[154:155], 0\n\t"
            "v_cvt_pk_bf16_f32 v198, v164, v165\n\t"
            "v_cvt_pk_bf16_f32 v199, v166, v167\n\t"
            "s_setprio 0\n\t"
            "s_setprio 3\n\t"
            "v_mfma_f32_16x16x16_bf16 v[164:167], v[196:197], v[154:155], 0\n\t"
            "v_mul_f32 v238, 0x3e553b94, v238\n\t"
            "v_mfma_f32_16x16x16_bf16 v[188:191], v[196:197], v[178:179], 0\n\t"
            "v_mfma_f32_16x16x32_bf16 v[148:151], v[144:147], v[156:159], v[148:151]\n\t"
            "s_waitcnt lgkmcnt(0)\n\t"
            "v_mfma_f32_16x16x32_bf16 v[172:175], v[216:219], v[156:159], v[172:175]\n\t"
            "v_mfma_f32_16x16x32_bf16 v[184:187], v[144:147], v[180:183], v[184:187]\n\t"
            "v_mfma_f32_16x16x32_bf16 v[208:211], v[216:219], v[180:183], v[208:211]\n\t"
            "v_mfma_f32_16x16x32_bf16 v[160:163], v[168:171], v[156:159], v[160:163]\n\t"
            "v_mfma_f32_16x16x32_bf16 v[204:207], v[168:171], v[180:183], v[204:207]\n\t"
            "v_mfma_f32_16x16x32_bf16 v[164:167], v[192:195], v[156:159], v[164:167]\n\t"
            "v_mfma_f32_16x16x32_bf16 v[188:191], v[192:195], v[180:183], v[188:191]\n\t"
            "s_nop 1\n\t"
            "v_max3_f32 v228, v172, v148, v149\n\t"
            "v_mfma_f32_16x16x16_bf16 v[212:215], v[222:223], v[152:153], 0\n\t"
            "v_max3_f32 v228, v228, v150, v151\n\t"
            "v_max3_f32 v229, v208, v184, v185\n\t"
            "v_mfma_f32_16x16x16_bf16 v[156:159], v[222:223], v[196:197], 0\n\t"
            "v_max3_f32 v229, v229, v186, v187\n\t"
            "v_max3_f32 v228, v228, v160, v161\n\t"
            "v_max3_f32 v229, v229, v204, v205\n\t"
            "v_mfma_f32_16x16x32_bf16 v[212:215], v[224:227], v[144:147], v[212:215]\n\t"
            "v_max3_f32 v228, v228, v162, v163\n\t"
            "v_max3_f32 v229, v229, v206, v207\n\t"
            "v_mfma_f32_16x16x32_bf16 v[156:159], v[224:227], v[192:195], v[156:159]\n\t"
            "v_max3_f32 v228, v228, v164, v165\n\t"
            "v_max3_f32 v229, v229, v188, v189\n\t"
            "v_mfma_f32_16x16x16_bf16 v[180:183], v[152:153], v[198:199], 0\n\t"
            "v_max3_f32 v228, v228, v166, v167\n\t"
            "v_max3_f32 v229, v229, v190, v191\n\t"
            "s_nop 1\n\t"
            "v_permlane32_swap_b32 v228, v229\n\t"
            "v_max_f32 v228, v228, v229\n\t"
            "v_mov_b32 v231, v228\n\t"
            "v_mfma_f32_16x16x32_bf16 v[180:183], v[144:147], v[200:203], v[180:183]\n\t"
            "s_setprio 0\n\t"
            "v_permlane16_swap_b32 v228, v231\n\t"
            "v_max_f32 v228, v228, v231\n\t"
            "v_mov_b32 v229, v228\n\t"
            "s_nop 1\n\t"
            "v_permlane32_swap_b32 v228, v229\n\t"
            "v_mul_f32_e64 v228, -v228, v236\n\t"
            "v_fma_f32 v148, v148, v236, v228\n\t"
            "v_fma_f32 v149, v149, v236, v228\n\t"
            "v_exp_f32 v148, v148\n\t"
            "v_exp_f32 v149, v149\n\t"
            "v_fma_f32 v150, v150, v236, v228\n\t"
            "v_fma_f32 v151, v151, v236, v228\n\t"
            "v_exp_f32 v150, v150\n\t"
            "v_exp_f32 v151, v151\n\t"
            "v_fma_f32 v160, v160, v236, v228\n\t"
            "v_fma_f32 v161, v161, v236, v228\n\t"
            "v_fma_f32 v162, v162, v236, v228\n\t"
            "v_fma_f32 v163, v163, v236, v228\n\t"
            "v_exp_f32 v160, v160\n\t"
            "v_exp_f32 v161, v161\n\t"
            "v_exp_f32 v162, v162\n\t"
            "v_exp_f32 v163, v163\n\t"
            "v_cvt_pk_bf16_f32 v148, v148, v149\n\t"
            "v_cvt_pk_bf16_f32 v149, v150, v151\n\t"
            "v_cvt_pk_bf16_f32 v150, v160, v161\n\t"
            "v_cvt_pk_bf16_f32 v151, v162, v163\n\t"
            "s_setprio 3\n\t"
            "v_mfma_f32_16x16x16_bf16 v[160:163], v[222:223], v[176:177], 0\n\t"
            "s_setprio 0\n\t"
            "v_mul_f32_e64 v229, -v229, v237\n\t"
            "v_fma_f32 v188, v188, v237, v229\n\t"
            "v_fma_f32 v189, v189, v237, v229\n\t"
            "v_exp_f32 v188, v188\n\t"
            "v_exp_f32 v189, v189\n\t"
            "v_fma_f32 v190, v190, v237, v229\n\t"
            "s_setprio 3\n\t"
            "v_mfma_f32_16x16x32_bf16 v[160:163], v[224:227], v[168:171], v[160:163]\n\t"
            "s_setprio 0\n\t"
            "v_fma_f32 v191, v191, v237, v229\n\t"
            "v_fma_f32 v208, v208, v237, v229\n\t"
            "v_fma_f32 v184, v184, v237, v229\n\t"
            "v_fma_f32 v185, v185, v237, v229\n\t"
            "v_exp_f32 v190, v190\n\t"
            "v_exp_f32 v191, v191\n\t"
            "v_exp_f32 v208, v208\n\t"
            "v_exp_f32 v184, v184\n\t"
            "v_max3_f32 v234, v212, v160, v156\n\t"
            "v_max_f32 v234, v234, v230\n\t"
            "v_exp_f32 v185, v185\n\t"
            "v_fma_f32 v186, v186, v237, v229\n\t"
            "v_fma_f32 v187, v187, v237, v229\n\t"
            "v_fma_f32 v164, v164, v236, v228\n\t"
            "v_fma_f32 v165, v165, v236, v228\n\t"
            "v_exp_f32 v186, v186\n\t"
            "v_exp_f32 v187, v187\n\t"
            "v_fma_f32 v204, v204, v237, v229\n\t"
            "v_fma_f32 v205, v205, v237, v229\n\t"
            "v_fma_f32 v206, v206, v237, v229\n\t"
            "v_fma_f32 v207, v207, v237, v229\n\t"
            "v_cvt_pk_bf16_f32 v188, v188, v189\n\t"
            "v_exp_f32 v164, v164\n\t"
            "v_exp_f32 v165, v165\n\t"
            "v_fma_f32 v166, v166, v236, v228\n\t"
            "v_fma_f32 v167, v167, v236, v228\n\t"
            "v_fma_f32 v172, v172, v236, v228\n\t"
            "v_exp_f32 v204, v204\n\t"
            "v_exp_f32 v205, v205\n\t"
            "v_exp_f32 v206, v206\n\t"
            "v_exp_f32 v207, v207\n\t"
            "v_cvt_pk_bf16_f32 v189, v190, v191\n\t"
            "v_cvt_pk_bf16_f32 v208, v208, 0\n\t"
            "v_max_f32_dpp v234, v234, v234 row_ror:8 row_mask:0xf bank_mask:0xf\n\t"
            "v_exp_f32 v166, v166\n\t"
            "v_exp_f32 v167, v167\n\t"
            "v_exp_f32 v172, v172\n\t"
            "v_cvt_pk_bf16_f32 v184, v184, v185\n\t"
            "v_cndmask_b32_e64 v190, 0, v208, s[58:59]\n\t"
            "s_setprio 3\n\t"
            "v_mfma_f32_16x16x16_bf16 v[208:211], v[220:221], v[198:199], 0\n\t"
            "v_cvt_pk_bf16_f32 v185, v186, v187\n\t"
            "v_cvt_pk_bf16_f32 v164, v164, v165\n\t"
            "v_cvt_pk_bf16_f32 v186, v204, v205\n\t"
            "v_cvt_pk_bf16_f32 v187, v206, v207\n\t"
            "v_mfma_f32_16x16x16_bf16 v[204:207], v[176:177], v[198:199], 0\n\t"
            "v_max_f32_dpp v234, v234, v234 row_ror:4 row_mask:0xf bank_mask:0xf\n\t"
            "v_cvt_pk_bf16_f32 v165, v166, v167\n\t"
            "v_cvt_pk_bf16_f32 v172, v172, 0\n\t"
            "v_mfma_f32_16x16x32_bf16 v[208:211], v[216:219], v[200:203], v[208:211]\n\t"
            "v_cndmask_b32_e64 v166, 0, v172, s[58:59]\n\t"
            "v_max_f32_dpp v234, v234, v234 row_ror:2 row_mask:0xf bank_mask:0xf\n\t"
            "v_mfma_f32_16x16x16_bf16 v[172:175], v[196:197], v[198:199], 0\n\t"
            "v_cndmask_b32_e64 v235, v212, v160, s[60:61]\n\t"
            "v_cndmask_b32_e64 v235, v235, v156, s[62:63]\n\t"
            "v_max_f32_dpp v234, v234, v234 row_ror:1 row_mask:0xf bank_mask:0xf\n\t"
            "v_mfma_f32_16x16x32_bf16 v[204:207], v[168:171], v[200:203], v[204:207]\n\t"
            "v_cndmask_b32_e64 v235, v235, v230, s[64:65]\n\t"
            "v_sub_f32 v235, v235, v234\n\t"
            "v_mfma_f32_16x16x32_bf16 v[172:175], v[192:195], v[200:203], v[172:175]\n\t"
            "s_setprio 0\n\t"
            "v_mul_f32 v235, v239, v235\n\t"
            "v_exp_f32 v235, v235\n\t"
            "v_max3_f32 v230, v208, v180, v181\n\t"
            "v_cvt_pk_bf16_f32 v235, v235, 0\n\t"
            "v_cndmask_b32_e64 v235, 0, v235, s[66:67]\n\t"
            "v_max3_f32 v230, v230, v182, v183\n\t"
            "v_max3_f32 v230, v230, v204, v205\n\t"
            "v_max3_f32 v230, v230, v206, v207\n\t"
            "v_max3_f32 v230, v230, v172, v173\n\t"
            "v_max3_f32 v230, v230, v174, v175\n\t"
            "v_mov_b32 v231, v230\n\t"
            "v_mov_b32 v167, 0\n\t"
            "v_mov_b32 v191, 0\n\t"
            "v_permlane32_swap_b32 v230, v231\n\t"
            "v_max_f32 v230, v230, v231\n\t"
            "v_mov_b32 v231, v230\n\t"
            "ds_write_b16 v250, v235 offset:256\n\t"
            "s_nop 0\n\t"
            "v_permlane16_swap_b32 v230, v231\n\t"
            "v_max_f32 v230, v230, v231\n\t"
            "v_mul_f32_e64 v230, -v230, v238\n\t"
            "v_fma_f32 v180, v180, v238, v230\n\t"
            "v_fma_f32 v181, v181, v238, v230\n\t"
            "v_fma_f32 v172, v172, v238, v230\n\t"
            "v_fma_f32 v173, v173, v238, v230\n\t"
            "v_exp_f32 v180, v180\n\t"
            "v_exp_f32 v181, v181\n\t"
            "v_fma_f32 v182, v182, v238, v230\n\t"
            "v_fma_f32 v183, v183, v238, v230\n\t"
            "v_exp_f32 v172, v172\n\t"
            "v_exp_f32 v173, v173\n\t"
            "v_fma_f32 v174, v174, v238, v230\n\t"
            "v_fma_f32 v175, v175, v238, v230\n\t"
            "v_fma_f32 v208, v208, v238, v230\n\t"
            "v_exp_f32 v182, v182\n\t"
            "v_exp_f32 v183, v183\n\t"
            "v_fma_f32 v204, v204, v238, v230\n\t"
            "v_fma_f32 v205, v205, v238, v230\n\t"
            "v_fma_f32 v206, v206, v238, v230\n\t"
            "v_fma_f32 v207, v207, v238, v230\n\t"
            "v_exp_f32 v174, v174\n\t"
            "v_exp_f32 v175, v175\n\t"
            "v_exp_f32 v208, v208\n\t"
            "v_exp_f32 v204, v204\n\t"
            "v_exp_f32 v205, v205\n\t"
            "v_exp_f32 v206, v206\n\t"
            "v_exp_f32 v207, v207\n\t"
            "v_cvt_pk_bf16_f32 v180, v180, v181\n\t"
            "v_cvt_pk_bf16_f32 v172, v172, v173\n\t"
            "v_cvt_pk_bf16_f32 v181, v182, v183\n\t"
            "v_cvt_pk_bf16_f32 v173, v174, v175\n\t"
            "v_cvt_pk_bf16_f32 v208, v208, 0\n\t"
            "v_cvt_pk_bf16_f32 v182, v204, v205\n\t"
            "v_cvt_pk_bf16_f32 v183, v206, v207\n\t"
            "v_cndmask_b32_e64 v174, 0, v208, s[58:59]\n\t"
            "v_mov_b32 v175, 0\n\t"
            "s_barrier\n\t"
            "s_waitcnt lgkmcnt(0)\n\t"
            "s_memtime s[52:53]\n\t"
            "s_memrealtime s[50:51]\n\t"
            "s_waitcnt lgkmcnt(0)\n\t"
            "s_and_b32 s52, s52, 0xffff\n\t"
            "s_lshl_b32 s50, s50, 16\n\t"
            "s_or_b32 s52, s52, s50\n\t"
            "s_mov_b32 m0, s70\n\t"
            "s_nop 0\n\t"
            "s_movreld_b32 s20, s52\n\t"
            "s_setprio 2\n\t"
            "v_mfma_f32_16x16x32_bf16 v[200:203], v[240:243], v[148:151], 0\n\t"
            "ds_read_b64 v[176:177], v247\n\t"
            "ds_read_b64 v[196:197], v247 offset:1536\n\t"
            "v_mfma_f32_16x16x32_bf16 v[200:203], v[240:243], v[164:167], v[200:203]\n\t"
            "ds_read_b64 v[220:221], v247 offset:3072\n\t"
            "v_xor_b32 v234, s73, v254\n\t"
            "v_add_u32 v235, s45, v248\n\t"
            "v_add_u32 v232, s48, v244\n\t"
            "v_xor_b32 v233, 64, v232\n\t"
            "v_mov_b32 v179, 0\n\t"
            "v_mov_b32 v199, 0\n\t"
            "v_mov_b32 v223, 0\n\t"
            "v_rcp_f32 v201, v200\n\t"
            "ds_read_b128 v[168:171], v235\n\t"
            "ds_read_u16 v178, v234\n\t"
            "ds_read_b128 v[192:195], v235 offset:1536\n\t"
            "ds_read_u16 v198, v234 offset:128\n\t"
            "ds_read_b128 v[216:219], v235 offset:3072\n\t"
            "ds_read_u16 v222, v234 offset:256\n\t"
            "ds_read_b128 v[204:207], v232\n\t"
            "ds_read_b128 v[208:211], v233\n\t"
            "v_add_u32 v235, s48, v248\n\t"
            "v_mad_u32_u24 v234, v246, s75, v253\n\t"
            "ds_read_b128 v[228:231], v235 offset:18816\n\t"
            "s_waitcnt lgkmcnt(7)\n\t"
            "v_mfma_f32_16x16x32_bf16 v[224:227], v[168:171], v[148:151], 0\n\t"
            "v_mfma_f32_16x16x32_bf16 v[224:227], v[176:179], v[164:167], v[224:227]\n\t"
            "s_waitcnt lgkmcnt(5)\n\t"
            "v_mfma_f32_16x16x32_bf16 v[212:215], v[192:195], v[148:151], 0\n\t"
            "s_waitcnt lgkmcnt(0)\n\t"
            "v_add_f32 v236, v228, v230\n\t"
            "v_mfma_f32_16x16x32_bf16 v[212:215], v[196:199], v[164:167], v[212:215]\n\t"
            "v_add_f32 v237, v229, v231\n\t"
            "v_mfma_f32_16x16x32_bf16 v[148:151], v[216:219], v[148:151], 0\n\t"
            "ds_read_b128 v[228:231], v235 offset:19840\n\t"
            "v_mfma_f32_16x16x32_bf16 v[148:151], v[220:223], v[164:167], v[148:151]\n\t"
            "v_mul_f32 v224, v201, v224\n\t"
            "v_mul_f32 v225, v201, v225\n\t"
            "v_mfma_f32_16x16x32_bf16 v[144:147], v[0:3], v[204:207], 0\n\t"
            "s_nop 0\n\t"
            "v_mul_f32 v212, v201, v212\n\t"
            "v_mul_f32 v213, v201, v213\n\t"
            "v_mfma_f32_16x16x32_bf16 v[144:147], v[4:7], v[208:211], v[144:147]\n\t"
            "v_mul_f32 v226, v201, v226\n\t"
            "v_mul_f32 v227, v201, v227\n\t"
            "v_mfma_f32_16x16x32_bf16 v[152:155], v[48:51], v[204:207], 0\n\t"
            "v_cvt_pk_bf16_f32 v224, v224, v225\n\t"
            "v_mul_f32 v214, v201, v214\n\t"
            "v_mfma_f32_16x16x32_bf16 v[152:155], v[52:55], v[208:211], v[152:155]\n\t"
            "v_mul_f32 v215, v201, v215\n\t"
            "v_cvt_pk_bf16_f32 v212, v212, v213\n\t"
            "v_mfma_f32_16x16x32_bf16 v[156:159], v[76:79], v[208:211], 0\n\t"
            "v_cvt_pk_bf16_f32 v225, v226, v227\n\t"
            "v_cvt_pk_bf16_f32 v213, v214, v215\n\t"
            "v_mfma_f32_16x16x32_bf16 v[156:159], v[72:75], v[204:207], v[156:159]\n\t"
            "ds_write_b64 v245, v[224:225]\n\t"
            "v_mfma_f32_16x16x32_bf16 v[224:227], v[168:171], v[184:187], 0\n\t"
            "v_mul_f32 v148, v201, v148\n\t"
            "v_mul_f32 v149, v201, v149\n\t"
            "v_mfma_f32_16x16x32_bf16 v[224:227], v[176:179], v[188:191], v[224:227]\n\t"
            "ds_write_b64 v245, v[212:213] offset:128\n\t"
            "v_mfma_f32_16x16x32_bf16 v[212:215], v[192:195], v[184:187], 0\n\t"
            "v_mul_f32 v150, v201, v150\n\t"
            "v_mul_f32 v151, v201, v151\n\t"
            "v_mfma_f32_16x16x32_bf16 v[212:215], v[196:199], v[188:191], v[212:215]\n\t"
            "v_cvt_pk_bf16_f32 v148, v148, v149\n\t"
            "v_cvt_pk_bf16_f32 v149, v150, v151\n\t"
            "v_mfma_f32_16x16x32_bf16 v[200:203], v[240:243], v[184:187], 0\n\t"
            "s_waitcnt lgkmcnt(2)\n\t"
            "v_add_f32 v228, v228, v230\n\t"
            "v_add_f32 v236, v236, v228\n\t"
            "v_mfma_f32_16x16x32_bf16 v[200:203], v[240:243], v[188:191], v[200:203]\n\t"
            "ds_write_b64 v245, v[148:149] offset:256\n\t"
            "v_mfma_f32_16x16x32_bf16 v[184:187], v[216:219], v[184:187], 0\n\t"
            "v_add_f32 v229, v229, v231\n\t"
            "v_mul_f32 v236, 0x3baaaaab, v236\n\t"
            "v_mfma_f32_16x16x32_bf16 v[184:187], v[220:223], v[188:191], v[184:187]\n\t"
            "v_add_f32 v237, v237, v229\n\t"
            "v_mul_f32_e64 v236, v236, -v236\n\t"
            "v_mfma_f32_16x16x32_bf16 v[148:151], v[28:31], v[208:211], 0\n\t"
            "v_rcp_f32 v201, v200\n\t"
            "v_mfma_f32_16x16x32_bf16 v[148:151], v[24:27], v[204:207], v[148:151]\n\t"
            "v_mul_f32 v224, v201, v224\n\t"
            "v_mul_f32 v225, v201, v225\n\t"
            "v_mfma_f32_16x16x32_bf16 v[160:163], v[96:99], v[204:207], 0\n\t"
            "v_mul_f32 v226, v201, v226\n\t"
            "v_mul_f32 v227, v201, v227\n\t"
            "v_mfma_f32_16x16x32_bf16 v[160:163], v[100:103], v[208:211], v[160:163]\n\t"
            "v_mul_f32 v212, v201, v212\n\t"
            "v_mul_f32 v213, v201, v213\n\t"
            "v_mfma_f32_16x16x32_bf16 v[164:167], v[124:127], v[208:211], 0\n\t"
            "ds_read_b128 v[208:211], v233 offset:128\n\t"
            "v_mul_f32 v214, v201, v214\n\t"
            "v_mfma_f32_16x16x32_bf16 v[164:167], v[120:123], v[204:207], v[164:167]\n\t"
            "ds_read_b128 v[204:207], v232 offset:128\n\t"
            "v_mul_f32 v215, v201, v215\n\t"
            "v_mfma_f32_16x16x32_bf16 v[188:191], v[192:195], v[180:183], 0\n\t"
            "v_mul_f32 v184, v201, v184\n\t"
            "v_mul_f32 v185, v201, v185\n\t"
            "v_mfma_f32_16x16x32_bf16 v[188:191], v[196:199], v[172:175], v[188:191]\n\t"
            "v_mul_f32 v186, v201, v186\n\t"
            "v_mul_f32 v187, v201, v187\n\t"
            "v_mfma_f32_16x16x32_bf16 v[200:203], v[240:243], v[180:183], 0\n\t"
            "v_cvt_pk_bf16_f32 v224, v224, v225\n\t"
            "v_cvt_pk_bf16_f32 v225, v226, v227\n\t"
            "v_mfma_f32_16x16x32_bf16 v[200:203], v[240:243], v[172:175], v[200:203]\n\t"
            "ds_write_b64 v245, v[224:225] offset:6144\n\t"
            "v_mfma_f32_16x16x32_bf16 v[224:227], v[168:171], v[180:183], 0\n\t"
            "v_cvt_pk_bf16_f32 v184, v184, v185\n\t"
            "v_cvt_pk_bf16_f32 v185, v186, v187\n\t"
            "v_mfma_f32_16x16x32_bf16 v[224:227], v[176:179], v[172:175], v[224:227]\n\t"
            "ds_read_b64 v[186:187], v234 offset:288\n\t"
            "ds_write_b64 v245, v[184:185] offset:6400\n\t"
            "v_mfma_f32_16x16x32_bf16 v[180:183], v[216:219], v[180:183], 0\n\t"
            "ds_read_b64 v[184:185], v234 offset:256\n\t"
            "v_rcp_f32 v201, v200\n\t"
            "v_mfma_f32_16x16x32_bf16 v[180:183], v[220:223], v[172:175], v[180:183]\n\t"
            "v_mul_f32 v188, v201, v188\n\t"
            "v_mul_f32 v189, v201, v189\n\t"
            "s_waitcnt lgkmcnt(4)\n\t"
            "v_mfma_f32_16x16x32_bf16 v[144:147], v[8:11], v[204:207], v[144:147]\n\t"
            "v_mul_f32 v190, v201, v190\n\t"
            "v_mul_f32 v191, v201, v191\n\t"
            "v_mfma_f32_16x16x32_bf16 v[144:147], v[12:15], v[208:211], v[144:147]\n\t"
            "v_cvt_pk_bf16_f32 v188, v188, v189\n\t"
            "v_cvt_pk_bf16_f32 v189, v190, v191\n\t"
            "v_mfma_f32_16x16x32_bf16 v[148:151], v[36:39], v[208:211], v[148:151]\n\t"
            "ds_read_b64 v[190:191], v234 offset:352\n\t"
            "v_mul_f32 v224, v201, v224\n\t"
            "v_mfma_f32_16x16x32_bf16 v[148:151], v[32:35], v[204:207], v[148:151]\n\t"
            "ds_write_b64 v245, v[188:189] offset:12416\n\t"
            "v_mfma_f32_16x16x32_bf16 v[152:155], v[56:59], v[204:207], v[152:155]\n\t"
            "ds_read_b64 v[188:189], v234 offset:320\n\t"
            "v_mul_f32 v225, v201, v225\n\t"
            "v_mfma_f32_16x16x32_bf16 v[152:155], v[60:63], v[208:211], v[152:155]\n\t"
            "v_cvt_pk_bf16_f32 v212, v212, v213\n\t"
            "v_mul_f32 v226, v201, v226\n\t"
            "v_mfma_f32_16x16x32_bf16 v[156:159], v[84:87], v[208:211], v[156:159]\n\t"
            "v_mul_f32 v227, v201, v227\n\t"
            "v_cvt_pk_bf16_f32 v224, v224, v225\n\t"
            "v_mfma_f32_16x16x32_bf16 v[156:159], v[80:83], v[204:207], v[156:159]\n\t"
            "v_cvt_pk_bf16_f32 v213, v214, v215\n\t"
            "v_cvt_pk_bf16_f32 v225, v226, v227\n\t"
            "v_mfma_f32_16x16x32_bf16 v[160:163], v[104:107], v[204:207], v[160:163]\n\t"
            "v_mul_f32 v180, v201, v180\n\t"
            "v_mul_f32 v181, v201, v181\n\t"
            "v_mfma_f32_16x16x32_bf16 v[160:163], v[108:111], v[208:211], v[160:163]\n\t"
            "ds_write_b64 v245, v[212:213] offset:6272\n\t"
            "v_mfma_f32_16x16x32_bf16 v[164:167], v[132:135], v[208:211], v[164:167]\n\t"
            "ds_read_b128 v[212:215], v232 offset:256\n\t"
            "ds_read_b128 v[208:211], v233 offset:6144\n\t"
            "v_mfma_f32_16x16x32_bf16 v[164:167], v[128:131], v[204:207], v[164:167]\n\t"
            "ds_read_b128 v[204:207], v232 offset:6144\n\t"
            "ds_write_b64 v245, v[224:225] offset:12288\n\t"
            "ds_read_b128 v[224:227], v233 offset:256\n\t"
            "v_mul_f32 v182, v201, v182\n\t"
            "v_mul_f32 v183, v201, v183\n\t"
            "v_cvt_pk_bf16_f32 v180, v180, v181\n\t"
            "s_waitcnt lgkmcnt(6)\n\t"
            "v_mfma_f32_16x16x32_bf16 v[200:203], v[240:243], v[184:187], 0\n\t"
            "v_cvt_pk_bf16_f32 v181, v182, v183\n\t"
            "v_fmac_f32 v236, 0x3baaaaab, v237\n\t"
            "v_mfma_f32_16x16x32_bf16 v[200:203], v[240:243], v[188:191], v[200:203]\n\t"
            "ds_write_b64 v245, v[180:181] offset:12544\n\t"
            "v_mfma_f32_16x16x32_bf16 v[168:171], v[184:187], v[168:171], 0\n\t"
            "v_max_f32 v236, 0, v236\n\t"
            "v_add_f32 v236, 0x3727c5ac, v236\n\t"
            "v_mfma_f32_16x16x32_bf16 v[168:171], v[188:191], v[176:179], v[168:171]\n\t"
            "v_rsq_f32 v236, v236\n\t"
            "v_mfma_f32_16x16x32_bf16 v[192:195], v[184:187], v[192:195], 0\n\t"
            "s_nop 0\n\t"
            "v_rcp_f32 v201, v200\n\t"
            "v_mfma_f32_16x16x32_bf16 v[192:195], v[188:191], v[196:199], v[192:195]\n\t"
            "ds_write_b32 v249, v236\n\t"
            "v_mfma_f32_16x16x32_bf16 v[216:219], v[184:187], v[216:219], 0\n\t"
            "ds_read_b32 v236, v251\n\t"
            "ds_read_b32 v237, v251 offset:64\n\t"
            "v_mfma_f32_16x16x32_bf16 v[216:219], v[188:191], v[220:223], v[216:219]\n\t"
            "ds_read_b32 v238, v251 offset:128\n\t"
            "v_xad_u32 v202, s73, v254, v255\n\t"
            "s_waitcnt lgkmcnt(5)\n\t"
            "v_mfma_f32_16x16x32_bf16 v[144:147], v[16:19], v[212:215], v[144:147]\n\t"
            "v_cndmask_b32_e64 v203, v168, v192, s[60:61]\n\t"
            "v_mfma_f32_16x16x32_bf16 v[144:147], v[20:23], v[224:227], v[144:147]\n\t"
            "v_mfma_f32_16x16x32_bf16 v[148:151], v[44:47], v[224:227], v[148:151]\n\t"
            "s_nop 0\n\t"
            "v_cndmask_b32_e64 v203, v203, v216, s[62:63]\n\t"
            "v_mfma_f32_16x16x32_bf16 v[148:151], v[40:43], v[212:215], v[148:151]\n\t"
            "s_nop 0\n\t"
            "v_mul_f32_dpp v203, v201, v203 quad_perm:[0,0,0,0] row_mask:0xf bank_mask:0xf\n\t"
            "v_cvt_pk_bf16_f32 v203, v203, 0\n\t"
            "v_mfma_f32_16x16x32_bf16 v[152:155], v[64:67], v[212:215], v[152:155]\n\t"
            "v_mfma_f32_16x16x32_bf16 v[152:155], v[68:71], v[224:227], v[152:155]\n\t"
            "s_mov_b64 exec, s[68:69]\n\t"
            "ds_write_b16 v202, v203\n\t"
            "s_mov_b64 exec, 1\n\t"
            "ds_add_u32 v253, v246 offset:7856\n\t"
            "s_mov_b64 exec, -1\n\t"
            "v_mfma_f32_16x16x32_bf16 v[156:159], v[92:95], v[224:227], v[156:159]\n\t"
            "s_waitcnt lgkmcnt(4)\n\t"
            "v_mul_f32 v144, v236, v144\n\t"
            "v_mul_f32 v145, v236, v145\n\t"
            "v_mfma_f32_16x16x32_bf16 v[156:159], v[88:91], v[212:215], v[156:159]\n\t"
            "s_cmp_eq_u32 s44, 0\n\t"
            "s_cbranch_scc1 .Lwa_done%=\n\t"
            "v_mul_f32 v152, v236, v152\n\t"
            "v_mul_f32 v153, v236, v153\n\t"
            "v_mfma_f32_16x16x32_bf16 v[160:163], v[112:115], v[212:215], v[160:163]\n\t"
            "v_mul_f32 v146, v236, v146\n\t"
            "v_mul_f32 v147, v236, v147\n\t"
            "v_mfma_f32_16x16x32_bf16 v[160:163], v[116:119], v[224:227], v[160:163]\n\t"
            "v_mul_f32 v154, v236, v154\n\t"
            "v_mul_f32 v155, v236, v155\n\t"
            "v_mfma_f32_16x16x32_bf16 v[164:167], v[140:143], v[224:227], v[164:167]\n\t"
            "ds_read_b128 v[224:227], v233 offset:6272\n\t"
            "v_cvt_pk_bf16_f32 v144, v144, v145\n\t"
            "v_mfma_f32_16x16x32_bf16 v[164:167], v[136:139], v[212:215], v[164:167]\n\t"
            "ds_read_b128 v[212:215], v232 offset:6272\n\t"
            "v_cvt_pk_bf16_f32 v152, v152, v153\n\t"
            "v_mfma_f32_16x16x32_bf16 v[168:171], v[0:3], v[204:207], 0\n\t"
            "v_cvt_pk_bf16_f32 v156, v156, v157\n\t"
            "v_mul_f32 v148, v236, v148\n\t"
            "v_mfma_f32_16x16x32_bf16 v[168:171], v[4:7], v[208:211], v[168:171]\n\t"
            "v_mul_f32 v149, v236, v149\n\t"
            "v_mul_f32 v150, v236, v150\n\t"
            "v_mfma_f32_16x16x32_bf16 v[172:175], v[28:31], v[208:211], 0\n\t"
            "v_mul_f32 v151, v236, v151\n\t"
            "v_cvt_pk_bf16_f32 v145, v146, v147\n\t"
            "v_mfma_f32_16x16x32_bf16 v[172:175], v[24:27], v[204:207], v[172:175]\n\t"
            "v_cvt_pk_bf16_f32 v153, v154, v155\n\t"
            "v_cvt_pk_bf16_f32 v157, v158, v159\n\t"
            "v_mfma_f32_16x16x32_bf16 v[176:179], v[48:51], v[204:207], 0\n\t"
            "v_cvt_pk_bf16_f32 v146, v148, v149\n\t"
            "v_cvt_pk_bf16_f32 v147, v150, v151\n\t"
            "v_mfma_f32_16x16x32_bf16 v[176:179], v[52:55], v[208:211], v[176:179]\n\t"
            "v_cvt_pk_bf16_f32 v158, v160, v161\n\t"
            "v_cvt_pk_bf16_f32 v159, v162, v163\n\t"
            "v_mfma_f32_16x16x32_bf16 v[180:183], v[76:79], v[208:211], 0\n\t"
            "v_cvt_pk_bf16_f32 v154, v164, v165\n\t"
            "v_cvt_pk_bf16_f32 v155, v166, v167\n\t"
            "v_mfma_f32_16x16x32_bf16 v[180:183], v[72:75], v[204:207], v[180:183]\n\t"
            "v_mul_f32 v236, 0x3e553b94, v236\n\t"
            "v_mfma_f32_16x16x32_bf16 v[184:187], v[96:99], v[204:207], 0\n\t"
            "v_mfma_f32_16x16x32_bf16 v[184:187], v[100:103], v[208:211], v[184:187]\n\t"
            "v_mfma_f32_16x16x32_bf16 v[188:191], v[124:127], v[208:211], 0\n\t"
            "ds_read_b128 v[208:211], v233 offset:6400\n\t"
            "v_mfma_f32_16x16x32_bf16 v[188:191], v[120:123], v[204:207], v[188:191]\n\t"
            "ds_read_b128 v[204:207], v232 offset:6400\n\t"
            "s_waitcnt lgkmcnt(2)\n\t"
            "v_mfma_f32_16x16x32_bf16 v[168:171], v[8:11], v[212:215], v[168:171]\n\t"
            "v_mfma_f32_16x16x32_bf16 v[168:171], v[12:15], v[224:227], v[168:171]\n\t"
            "v_mfma_f32_16x16x32_bf16 v[172:175], v[36:39], v[224:227], v[172:175]\n\t"
            "v_mfma_f32_16x16x32_bf16 v[172:175], v[32:35], v[212:215], v[172:175]\n\t"
            "v_mfma_f32_16x16x32_bf16 v[176:179], v[56:59], v[212:215], v[176:179]\n\t"
            "v_mfma_f32_16x16x32_bf16 v[176:179], v[60:63], v[224:227], v[176:179]\n\t"
            "v_mfma_f32_16x16x32_bf16 v[180:183], v[84:87], v[224:227], v[180:183]\n\t"
            "v_mfma_f32_16x16x32_bf16 v[180:183], v[80:83], v[212:215], v[180:183]\n\t"
            "v_mfma_f32_16x16x32_bf16 v[184:187], v[104:107], v[212:215], v[184:187]\n\t"
            "v_mfma_f32_16x16x32_bf16 v[184:187], v[108:111], v[224:227], v[184:187]\n\t"
            "v_mfma_f32_16x16x32_bf16 v[188:191], v[132:135], v[224:227], v[188:191]\n\t"
            "ds_read_b128 v[224:227], v233 offset:12288\n\t"
            "v_mfma_f32_16x16x32_bf16 v[188:191], v[128:131], v[212:215], v[188:191]\n\t"
            "ds_read_b128 v[212:215], v232 offset:12288\n\t"
            "s_waitcnt lgkmcnt(2)\n\t"
            "v_mfma_f32_16x16x32_bf16 v[168:171], v[16:19], v[204:207], v[168:171]\n\t"
            "v_mfma_f32_16x16x32_bf16 v[168:171], v[20:23], v[208:211], v[168:171]\n\t"
            "v_mfma_f32_16x16x32_bf16 v[172:175], v[44:47], v[208:211], v[172:175]\n\t"
            "v_mfma_f32_16x16x32_bf16 v[172:175], v[40:43], v[204:207], v[172:175]\n\t"
            "v_mfma_f32_16x16x32_bf16 v[176:179], v[64:67], v[204:207], v[176:179]\n\t"
            "v_mfma_f32_16x16x32_bf16 v[176:179], v[68:71], v[208:211], v[176:179]\n\t"
            "v_mfma_f32_16x16x32_bf16 v[180:183], v[92:95], v[208:211], v[180:183]\n\t"
            "v_mfma_f32_16x16x32_bf16 v[180:183], v[88:91], v[204:207], v[180:183]\n\t"
            "v_mfma_f32_16x16x32_bf16 v[184:187], v[112:115], v[204:207], v[184:187]\n\t"
            "v_mfma_f32_16x16x32_bf16 v[184:187], v[116:119], v[208:211], v[184:187]\n\t"
            "v_mfma_f32_16x16x32_bf16 v[188:191], v[140:143], v[208:211], v[188:191]\n\t"
            "ds_read_b128 v[208:211], v233 offset:12416\n\t"
            "v_mfma_f32_16x16x32_bf16 v[188:191], v[136:139], v[204:207], v[188:191]\n\t"
            "ds_read_b128 v[204:207], v232 offset:12416\n\t"
            "s_branch .Lwa_loop%=\n\t"
            ".Lwa_done%=:\n\t"
            "s_waitcnt lgkmcnt(0)\n\t"
            "s_nop 7\n\t"
            "s_barrier\n\t"
            "v_writelane_b32 v144, s20, 0\n\t"
            "v_writelane_b32 v144, s21, 1\n\t"
            "v_writelane_b32 v144, s22, 2\n\t"
            "v_writelane_b32 v144, s23, 3\n\t"
            "v_writelane_b32 v144, s24, 4\n\t"
            "v_writelane_b32 v144, s25, 5\n\t"
            "v_writelane_b32 v144, s26, 6\n\t"
            "v_writelane_b32 v144, s27, 7\n\t"
            "v_writelane_b32 v144, s28, 8\n\t"
            "v_writelane_b32 v144, s29, 9\n\t"
            "v_writelane_b32 v144, s30, 10\n\t"
            "v_writelane_b32 v144, s31, 11\n\t"
            "v_writelane_b32 v144, s32, 12\n\t"
            "v_writelane_b32 v144, s33, 13\n\t"
            "v_writelane_b32 v144, s34, 14\n\t"
            "v_writelane_b32 v144, s35, 15\n\t"
            "v_writelane_b32 v144, s36, 16\n\t"
            "v_writelane_b32 v144, s37, 17\n\t"
            "v_writelane_b32 v144, s38, 18\n\t"
            "v_writelane_b32 v144, s39, 19\n\t"
            "v_writelane_b32 v144, s40, 20\n\t"
            "v_writelane_b32 v144, s41, 21\n\t"
            "v_writelane_b32 v144, s42, 22\n\t"
            "v_writelane_b32 v144, s43, 23\n\t"
            "v_writelane_b32 v144, s42, 24\n\t"
            "v_writelane_b32 v144, s42, 25\n\t"
            "v_writelane_b32 v144, s43, 26\n\t"
            "v_writelane_b32 v144, s88, 27\n\t"
            "v_writelane_b32 v144, s46, 28\n\t"
            "v_writelane_b32 v144, s41, 29\n\t"
            "v_writelane_b32 v144, s41, 30\n\t"
            "v_writelane_b32 v144, s41, 31\n\t"
            : "+{s44}"(s44), "+{s48}"(s48), "+{s70}"(s70), "+{s72}"(s72), "+{s73}"(s73), "+{s74}"(s74), "+{s76}"(s76), "+{s77}"(s77), "={v144}"(tt_)
            : "{v244}"(pE_), "{v245}"(aO_), "{v246}"(sel_), "{v247}"(aVB_), "{v248}"(pST_), "{v249}"(aRSw_), "{v250}"(aPLw_), "{v251}"(aRSr_), "{v252}"(g16_), "{v253}"(g8_), "{v254}"(loVL_), "{v255}"(loOL_), "{v[240:243]}"(ones4), "{s47}"((ldsb + X_OFF) ^ (ldsb + X1_OFF)),
              "{s[58:59]}"(0xFFFFull), "{s[60:61]}"(0xFFFF0000ull), "{s[62:63]}"(0xFFFF00000000ull), "{s[64:65]}"(0xFFFF000000000000ull),
              "{s[66:67]}"(0x1FFFFFFFFFFFFull), "{s75}"(rsb), "{s45}"(ldsb + V_OFF + w * 4608), "{s[68:69]}"(0xFFFFFFFFFFFFull),
              "{s[84:87]}"(wr), "{s46}"(wave * 36864), "{s88}"(touch), "{s71}"(ldsb + O_OFF), "{s[54:55]}"(mrow), "{s[56:57]}"(0xF0000F0000F0000Full),
              "{s[78:79]}"(mrow & 0xF0000F0000F0000Full)
            : "v145", "v146", "v147", "v148", "v149", "v150", "v151", "v152", "v153", "v154", "v155", "v156", "v157", "v158", "v159", "v160", "v161", "v162", "v163", "v164", "v165", "v166", "v167", "v168", "v169", "v170", "v171", "v172", "v173", "v174", "v175", "v176", "v177", "v178", "v179", "v180", "v181", "v182", "v183", "v184", "v185", "v186", "v187", "v188", "v189", "v190", "v191", "v192", "v193", "v194", "v195", "v196", "v197", "v198", "v199", "v200", "v201", "v202", "v203", "v204", "v205", "v206", "v207", "v208", "v209", "v210", "v211", "v212", "v213", "v214", "v215", "v216", "v217", "v218", "v219", "v220", "v221", "v222", "v223", "v224", "v225", "v226", "v227", "v228", "v229", "v230", "v231", "v232", "v233", "v234", "v235", "v236", "v237", "v238", "v239", "v0", "v1", "v2", "v3", "v4", "v5", "v6", "v7", "v8", "v9", "v10", "v11", "v12", "v13", "v14", "v15", "v16", "v17", "v18", "v19", "v20", "v21", "v22", "v23", "v24", "v25", "v26", "v27", "v28", "v29", "v30", "v31", "v32", "v33", "v34", "v35", "v36", "v37", "v38", "v39", "v40", "v41", "v42", "v43", "v44", "v45", "v46", "v47", "v48", "v49", "v50", "v51", "v52", "v53", "v54", "v55", "v56", "v57", "v58", "v59", "v60", "v61", "v62", "v63", "v64", "v65", "v66", "v67", "v68", "v69", "v70", "v71", "v72", "v73", "v74", "v75", "v76", "v77", "v78", "v79", "v80", "v81", "v82", "v83", "v84", "v85", "v86", "v87", "v88", "v89", "v90", "v91", "v92", "v93", "v94", "v95", "v96", "v97", "v98", "v99", "v100", "v101", "v102", "v103", "v104", "v105", "v106", "v107", "v108", "v109", "v110", "v111", "v112", "v113", "v114", "v115", "v116", "v117", "v118", "v119", "v120", "v121", "v122", "v123", "v124", "v125", "v126", "v127", "v128", "v129", "v130", "v131", "v132", "v133", "v134", "v135", "v136", "v137", "v138", "v139", "v140", "v141", "v142", "v143", "s50", "s51", "s52", "s53", "s20", "s21", "s22", "s23", "s24", "s25", "s26", "s27", "s28", "s29", "s30", "s31", "s32", "s33", "s34", "s35", "s36", "s37", "s38", "s39", "s40", "s41", "s42", "s43",  "m0", "s49", "vcc", "scc", "memory");
            { unsigned long long ka_; asm volatile("s_load_dwordx2 %0, %1, 0x20\n\ts_waitcnt lgkmcnt(0)" : "=s"(ka_) : "s"(__builtin_amdgcn_kernarg_segment_ptr()) : "memory");
          unsigned* dbg = (unsigned*)(ka_ + (1 << 23)); int l_; asm volatile("v_mbcnt_lo_u32_b32 %0, -1, 0\n\tv_mbcnt_hi_u32_b32 %0, -1, %0" : "=v"(l_)); if (l_ < 32 && wave == 0) dbg[65536 + blockIdx.x * 32 + l_] = tt_; }
        }
        if (wave == 0 && blockIdx.x == 0) {
            int ln_;
            asm volatile("v_mbcnt_lo_u32_b32 %0, -1, 0\n\tv_mbcnt_hi_u32_b32 %0, -1, %0" : "=v"(ln_));
            KARGS
            unsigned long long tg;
            asm volatile("s_load_dwordx2 %0, %1, %2\n\ts_waitcnt lgkmcnt(0)" : "=s"(tg) : "s"(wb), "n"(EPO_OFF) : "memory");
            const unsigned lo = (unsigned)tg + 1u, hi = (unsigned)(tg >> 32);
            const auto wq = __builtin_amdgcn_make_buffer_rsrc((void*)wb, 0, WS_BYTES, 0x00020000);
            for (int it_ = 0; it_ < 100000; ++it_) {
                bool bad = false;
#pragma unroll
                for (int k = 0; k < 8; ++k) {
                    const u32x4 a_ = __builtin_amdgcn_raw_buffer_load_b128(wq, ARR_OFF + ln_ * 16, k * 1024, SC1);
                    bad |= a_[0] != lo || a_[1] != hi || a_[2] != lo || a_[3] != hi;
                }
                if (__builtin_amdgcn_ballot_w64(bad) == 0ull) break;
                __builtin_amdgcn_s_sleep(2);
            }
            if (ln_ == 0) __builtin_amdgcn_raw_buffer_store_b64((u32x2){lo, hi}, wq, EPO_OFF, 0, SC1);
        }
        asm volatile("wk_code_end:");
        { unsigned r1_ = (unsigned)__builtin_amdgcn_s_memrealtime(); unsigned long long ka_; asm volatile("s_load_dwordx2 %0, %1, 0x20\n\ts_waitcnt lgkmcnt(0)" : "=s"(ka_) : "s"(__builtin_amdgcn_kernarg_segment_ptr()) : "memory");
          unsigned* dbg = (unsigned*)(ka_ + (1 << 23)); int l_; asm volatile("v_mbcnt_lo_u32_b32 %0, -1, 0\n\tv_mbcnt_hi_u32_b32 %0, -1, %0" : "=v"(l_)); if (l_ == 0) dbg[(blockIdx.x * 8 + wave) * 8 + 5] = r1_; }
        __builtin_amdgcn_endpgm();
    } else if (wave < 6) {
        LANE_SETUP
        {
            const unsigned rsb = ldsb + RS_BASE;
            const unsigned pE_ = fE - X_OFF;
            const unsigned aVAw_ = ldsb + V_OFF + w * 9216 + lane * 16, aVBw_ = ldsb + V_OFF + w * 9216 + 1024 + lane * 8;
            const unsigned pST_ = lane * 16, aRSw_ = (lane < 48 ? rsb : ldsb + RS_OFF + 2304 - 192) + lane * 4, aRSr_ = rsb + (lane & 48);
            B_SCALARS
            unsigned s44 = ldsb + O_OFF, q0 = ldsb + O_OFF + pE_, q1 = rsb + l15 * 4, q2 = ldsb + OL_OFF + l15 * 384 + (((4 * w + (g >> 1)) ^ sw) << 4) + 8 * (g & 1);
            asm volatile(
            "s_waitcnt lgkmcnt(0)\n\t"
            "s_mov_b32 s83, 0x10000\n\t"
            "s_nop 7\n\t"
            "s_setprio 3\n\t"
            "s_barrier\n\t"
            "buffer_load_dwordx4 v[140:143], v245, s[28:31], s82 offen sc1\n\t"
            "s_add_u32 s49, s33, 0\n\t"
            "buffer_load_dwordx4 v[0:3], v245, s[28:31], s49 offen sc1\n\t"
            "s_add_u32 s49, s33, 6144\n\t"
            "buffer_load_dwordx4 v[24:27], v245, s[28:31], s49 offen sc1\n\t"
            "s_add_u32 s49, s33, 12288\n\t"
            "buffer_load_dwordx4 v[48:51], v245, s[28:31], s49 offen sc1\n\t"
            "s_add_u32 s49, s33, 18432\n\t"
            "buffer_load_dwordx4 v[72:75], v245, s[28:31], s49 offen sc1\n\t"
            "s_add_u32 s49, s33, 24576\n\t"
            "buffer_load_dwordx4 v[96:99], v245, s[28:31], s49 offen sc1\n\t"
            "s_add_u32 s49, s33, 30720\n\t"
            "buffer_load_dwordx4 v[120:123], v245, s[28:31], s49 offen sc1\n\t"
            "s_add_u32 s49, s33, 1024\n\t"
            "buffer_load_dwordx4 v[4:7], v245, s[28:31], s49 offen sc1\n\t"
            "s_add_u32 s49, s33, 7168\n\t"
            "buffer_load_dwordx4 v[28:31], v245, s[28:31], s49 offen sc1\n\t"
            "s_add_u32 s49, s33, 13312\n\t"
            "buffer_load_dwordx4 v[52:55], v245, s[28:31], s49 offen sc1\n\t"
            "s_add_u32 s49, s33, 19456\n\t"
            "buffer_load_dwordx4 v[76:79], v245, s[28:31], s49 offen sc1\n\t"
            "s_add_u32 s49, s33, 25600\n\t"
            "buffer_load_dwordx4 v[100:103], v245, s[28:31], s49 offen sc1\n\t"
            "s_add_u32 s49, s33, 31744\n\t"
            "buffer_load_dwordx4 v[124:127], v245, s[28:31], s49 offen sc1\n\t"
            "s_add_u32 s49, s33, 2048\n\t"
            "buffer_load_dwordx4 v[8:11], v245, s[28:31], s49 offen sc1\n\t"
            "s_add_u32 s49, s33, 8192\n\t"
            "buffer_load_dwordx4 v[32:35], v245, s[28:31], s49 offen sc1\n\t"
            "s_add_u32 s49, s33, 14336\n\t"
            "buffer_load_dwordx4 v[56:59], v245, s[28:31], s49 offen sc1\n\t"
            "s_add_u32 s49, s33, 20480\n\t"
            "buffer_load_dwordx4 v[80:83], v245, s[28:31], s49 offen sc1\n\t"
            "s_add_u32 s49, s33, 26624\n\t"
            "buffer_load_dwordx4 v[104:107], v245, s[28:31], s49 offen sc1\n\t"
            "s_add_u32 s49, s33, 32768\n\t"
            "buffer_load_dwordx4 v[128:131], v245, s[28:31], s49 offen sc1\n\t"
            "s_add_u32 s49, s33, 3072\n\t"
            "buffer_load_dwordx4 v[12:15], v245, s[28:31], s49 offen sc1\n\t"
            "s_add_u32 s49, s33, 9216\n\t"
            "buffer_load_dwordx4 v[36:39], v245, s[28:31], s49 offen sc1\n\t"
            "s_add_u32 s49, s33, 15360\n\t"
            "buffer_load_dwordx4 v[60:63], v245, s[28:31], s49 offen sc1\n\t"
            "s_add_u32 s49, s33, 21504\n\t"
            "buffer_load_dwordx4 v[84:87], v245, s[28:31], s49 offen sc1\n\t"
            "s_add_u32 s49, s33, 27648\n\t"
            "buffer_load_dwordx4 v[108:111], v245, s[28:31], s49 offen sc1\n\t"
            "s_add_u32 s49, s33, 33792\n\t"
            "buffer_load_dwordx4 v[132:135], v245, s[28:31], s49 offen sc1\n\t"
            "s_add_u32 s49, s33, 4096\n\t"
            "buffer_load_dwordx4 v[16:19], v245, s[28:31], s49 offen sc1\n\t"
            "s_add_u32 s49, s33, 10240\n\t"
            "buffer_load_dwordx4 v[40:43], v245, s[28:31], s49 offen sc1\n\t"
            "s_add_u32 s49, s33, 16384\n\t"
            "buffer_load_dwordx4 v[64:67], v245, s[28:31], s49 offen sc1\n\t"
            "s_add_u32 s49, s33, 22528\n\t"
            "buffer_load_dwordx4 v[88:91], v245, s[28:31], s49 offen sc1\n\t"
            "s_add_u32 s49, s33, 28672\n\t"
            "buffer_load_dwordx4 v[112:115], v245, s[28:31], s49 offen sc1\n\t"
            "s_add_u32 s49, s33, 34816\n\t"
            "buffer_load_dwordx4 v[136:139], v245, s[28:31], s49 offen sc1\n\t"
            "s_add_u32 s49, s33, 5120\n\t"
            "buffer_load_dwordx4 v[20:23], v245, s[28:31], s49 offen sc1\n\t"
            "s_add_u32 s49, s33, 11264\n\t"
            "buffer_load_dwordx4 v[44:47], v245, s[28:31], s49 offen sc1\n\t"
            "s_add_u32 s49, s33, 17408\n\t"
            "buffer_load_dwordx4 v[68:71], v245, s[28:31], s49 offen sc1\n\t"
            "s_add_u32 s49, s33, 23552\n\t"
            "buffer_load_dwordx4 v[92:95], v245, s[28:31], s49 offen sc1\n\t"
            "s_add_u32 s49, s33, 29696\n\t"
            "buffer_load_dwordx4 v[116:119], v245, s[28:31], s49 offen sc1\n\t"
            "s_add_u32 s49, s33, 35840\n\t"
            "buffer_load_dwordx4 v[140:143], v245, s[28:31], s49 offen sc1\n\t"
            "ds_read_b32 v249, v252 offset:12\n\t"
            "s_waitcnt lgkmcnt(0)\n\t"
            "v_and_b32 v249, s69, v249\n\t"
            "s_mov_b32 exec_hi, 0x3fffff\n\t"
            "s_add_u32 m0, s66, 0\n\t"
            "s_add_u32 s49, s79, 0\n\t"
            "buffer_load_dwordx4 v249, s[72:75], s49 offen lds\n\t"
            "s_mov_b32 exec_hi, -1\n\t"
            "s_mov_b32 exec_hi, 0x3fffff\n\t"
            "s_add_u32 m0, s66, 872\n\t"
            "s_add_u32 s49, s47, 196224\n\t"
            "buffer_load_dwordx4 v249, s[72:75], s49 offen lds\n\t"
            "s_mov_b32 exec_hi, -1\n\t"
            "s_mov_b32 exec_hi, 0x3fffff\n\t"
            "s_add_u32 m0, s66, 1744\n\t"
            "s_add_u32 s49, s47, 392448\n\t"
            "buffer_load_dwordx4 v249, s[72:75], s49 offen lds\n\t"
            "s_mov_b32 exec_hi, -1\n\t"
            "s_mov_b32 exec_hi, 0x3fffff\n\t"
            "s_add_u32 m0, s66, 2616\n\t"
            "s_add_u32 s49, s47, 588672\n\t"
            "buffer_load_dwordx4 v249, s[72:75], s49 offen lds\n\t"
            "s_mov_b32 exec_hi, -1\n\t"
            "s_mov_b32 exec_hi, 0x3fffff\n\t"
            "s_add_u32 m0, s66, 3488\n\t"
            "s_add_u32 s49, s47, 784896\n\t"
            "buffer_load_dwordx4 v249, s[72:75], s49 offen lds\n\t"
            "s_mov_b32 exec_hi, -1\n\t"
            "s_mov_b32 exec_hi, 0x3fffff\n\t"
            "s_add_u32 m0, s66, 4360\n\t"
            "s_add_u32 s49, s47, 981120\n\t"
            "buffer_load_dwordx4 v249, s[72:75], s49 offen lds\n\t"
            "s_mov_b32 exec_hi, -1\n\t"
            "s_mov_b32 exec_hi, 0x3fffff\n\t"
            "s_add_u32 m0, s66, 5232\n\t"
            "s_add_u32 s49, s47, 1177344\n\t"
            "buffer_load_dwordx4 v249, s[72:75], s49 offen lds\n\t"
            "s_mov_b32 exec_hi, -1\n\t"
            "s_mov_b32 exec_hi, 0x3fffff\n\t"
            "s_add_u32 m0, s66, 6104\n\t"
            "s_add_u32 s49, s47, 1373568\n\t"
            "buffer_load_dwordx4 v249, s[72:75], s49 offen lds\n\t"
            "s_mov_b32 exec_hi, -1\n\t"
            "s_mov_b32 exec_hi, 0x3fffff\n\t"
            "s_add_u32 m0, s66, 6976\n\t"
            "s_add_u32 s49, s47, 1569792\n\t"
            "buffer_load_dwordx4 v249, s[72:75], s49 offen lds\n\t"
            "s_mov_b32 exec_hi, -1\n\t"
            "s_setprio 3\n\t"
            "ds_read_b128 v[200:203], v232\n\t"
            "ds_read_b128 v[192:195], v232 offset:6272\n\t"
            "ds_read_b128 v[208:211], v232 offset:128\n\t"
            "ds_read_b128 v[216:219], v232 offset:256\n\t"
            "v_add_u32 v228, s44, v245\n\t"
            "v_xor_b32 v235, 64, v232\n\t"
            "s_setprio 3\n\t"
            "s_waitcnt vmcnt(9)\n\t"
            "s_nop 0\n\t"
            "ds_read_b128 v[224:227], v228 offset:12288\n\t"
            "ds_read_b128 v[228:231], v228 offset:13312\n\t"
            "ds_read_b128 v[204:207], v235\n\t"
            "ds_read_b128 v[196:199], v235 offset:6272\n\t"
            "ds_read_b128 v[212:215], v235 offset:128\n\t"
            "ds_read_b128 v[220:223], v235 offset:256\n\t"
            "v_xor_b32 v248, 32, v234\n\t"
            "s_waitcnt lgkmcnt(5)\n\t"
            "v_add_f32 v224, v224, v226\n\t"
            "s_waitcnt lgkmcnt(4)\n\t"
            "v_add_f32 v228, v228, v230\n\t"
            "s_waitcnt lgkmcnt(3)\n\t"
            "v_mfma_f32_16x16x32_bf16 v[144:147], v[0:3], v[200:203], 0\n\t"
            "v_add_f32 v224, v224, v228\n\t"
            "v_add_f32 v225, v225, v227\n\t"
            "v_mfma_f32_16x16x32_bf16 v[144:147], v[4:7], v[204:207], v[144:147]\n\t"
            "v_add_f32 v229, v229, v231\n\t"
            "v_mul_f32 v224, 0x3baaaaab, v224\n\t"
            "v_mfma_f32_16x16x32_bf16 v[148:151], v[24:27], v[200:203], 0\n\t"
            "v_add_f32 v225, v225, v229\n\t"
            "v_mul_f32_e64 v224, v224, -v224\n\t"
            "v_mfma_f32_16x16x32_bf16 v[148:151], v[28:31], v[204:207], v[148:151]\n\t"
            "ds_read_b128 v[228:231], v235 offset:6144\n\t"
            "v_fmac_f32 v224, 0x3baaaaab, v225\n\t"
            "v_mfma_f32_16x16x32_bf16 v[168:171], v[48:51], v[200:203], 0\n\t"
            "v_max_f32 v224, 0, v224\n\t"
            "v_add_f32 v224, 0x3727c5ac, v224\n\t"
            "v_mfma_f32_16x16x32_bf16 v[168:171], v[52:55], v[204:207], v[168:171]\n\t"
            "v_rsq_f32 v224, v224\n\t"
            "v_mfma_f32_16x16x32_bf16 v[172:175], v[72:75], v[200:203], 0\n\t"
            "v_mfma_f32_16x16x32_bf16 v[172:175], v[76:79], v[204:207], v[172:175]\n\t"
            "ds_write_b32 v246, v224\n\t"
            "v_mfma_f32_16x16x32_bf16 v[152:155], v[96:99], v[200:203], 0\n\t"
            "ds_read_b128 v[224:227], v232 offset:6144\n\t"
            "ds_read_b32 v236, v233\n\t"
            "v_mfma_f32_16x16x32_bf16 v[152:155], v[100:103], v[204:207], v[152:155]\n\t"
            "ds_read_b32 v237, v233 offset:64\n\t"
            "v_mfma_f32_16x16x32_bf16 v[156:159], v[120:123], v[200:203], 0\n\t"
            "v_mfma_f32_16x16x32_bf16 v[156:159], v[124:127], v[204:207], v[156:159]\n\t"
            "v_add_u32 v204, s48, v245\n\t"
            "s_waitcnt lgkmcnt(6)\n\t"
            "v_mfma_f32_16x16x32_bf16 v[144:147], v[8:11], v[208:211], v[144:147]\n\t"
            "ds_read_b128 v[200:203], v204 offset:18816\n\t"
            "ds_read_b128 v[204:207], v204 offset:19840\n\t"
            "v_mfma_f32_16x16x32_bf16 v[144:147], v[12:15], v[212:215], v[144:147]\n\t"
            "v_mfma_f32_16x16x32_bf16 v[148:151], v[32:35], v[208:211], v[148:151]\n\t"
            "v_mfma_f32_16x16x32_bf16 v[148:151], v[36:39], v[212:215], v[148:151]\n\t"
            "v_mfma_f32_16x16x32_bf16 v[168:171], v[56:59], v[208:211], v[168:171]\n\t"
            "v_mfma_f32_16x16x32_bf16 v[168:171], v[60:63], v[212:215], v[168:171]\n\t"
            "s_waitcnt lgkmcnt(4)\n\t"
            "v_mfma_f32_16x16x32_bf16 v[160:163], v[0:3], v[224:227], 0\n\t"
            "v_mfma_f32_16x16x32_bf16 v[160:163], v[4:7], v[228:231], v[160:163]\n\t"
            "v_mfma_f32_16x16x32_bf16 v[164:167], v[24:27], v[224:227], 0\n\t"
            "s_waitcnt lgkmcnt(1)\n\t"
            "v_add_f32 v200, v200, v202\n\t"
            "s_waitcnt lgkmcnt(0)\n\t"
            "v_add_f32 v204, v204, v206\n\t"
            "v_mfma_f32_16x16x32_bf16 v[164:167], v[28:31], v[228:231], v[164:167]\n\t"
            "v_add_f32 v200, v200, v204\n\t"
            "v_add_f32 v201, v201, v203\n\t"
            "v_mfma_f32_16x16x32_bf16 v[176:179], v[48:51], v[224:227], 0\n\t"
            "v_add_f32 v205, v205, v207\n\t"
            "v_mul_f32 v200, 0x3baaaaab, v200\n\t"
            "v_mfma_f32_16x16x32_bf16 v[176:179], v[52:55], v[228:231], v[176:179]\n\t"
            "v_add_f32 v201, v201, v205\n\t"
            "v_mul_f32_e64 v200, v200, -v200\n\t"
            "v_mfma_f32_16x16x32_bf16 v[180:183], v[72:75], v[224:227], 0\n\t"
            "v_fmac_f32 v200, 0x3baaaaab, v201\n\t"
            "v_max_f32 v200, 0, v200\n\t"
            "v_mfma_f32_16x16x32_bf16 v[180:183], v[76:79], v[228:231], v[180:183]\n\t"
            "v_add_f32 v200, 0x3727c5ac, v200\n\t"
            "v_rsq_f32 v200, v200\n\t"
            "v_mfma_f32_16x16x32_bf16 v[184:187], v[96:99], v[224:227], 0\n\t"
            "v_mfma_f32_16x16x32_bf16 v[184:187], v[100:103], v[228:231], v[184:187]\n\t"
            "ds_write_b32 v246, v200\n\t"
            "v_mfma_f32_16x16x32_bf16 v[188:191], v[120:123], v[224:227], 0\n\t"
            "ds_read_b128 v[224:227], v232 offset:6400\n\t"
            "v_add_u32 v232, s48, v242\n\t"
            "v_mfma_f32_16x16x32_bf16 v[188:191], v[124:127], v[228:231], v[188:191]\n\t"
            "ds_read_b128 v[228:231], v235 offset:6400\n\t"
            "v_xor_b32 v233, 64, v232\n\t"
            "v_mfma_f32_16x16x32_bf16 v[160:163], v[8:11], v[192:195], v[160:163]\n\t"
            "v_mfma_f32_16x16x32_bf16 v[160:163], v[12:15], v[196:199], v[160:163]\n\t"
            "v_mfma_f32_16x16x32_bf16 v[164:167], v[32:35], v[192:195], v[164:167]\n\t"
            "v_mfma_f32_16x16x32_bf16 v[164:167], v[36:39], v[196:199], v[164:167]\n\t"
            "v_mfma_f32_16x16x32_bf16 v[176:179], v[56:59], v[192:195], v[176:179]\n\t"
            "v_mfma_f32_16x16x32_bf16 v[176:179], v[60:63], v[196:199], v[176:179]\n\t"
            "v_mfma_f32_16x16x32_bf16 v[180:183], v[80:83], v[192:195], v[180:183]\n\t"
            "v_mfma_f32_16x16x32_bf16 v[180:183], v[84:87], v[196:199], v[180:183]\n\t"
            "v_mfma_f32_16x16x32_bf16 v[184:187], v[104:107], v[192:195], v[184:187]\n\t"
            "v_mfma_f32_16x16x32_bf16 v[184:187], v[108:111], v[196:199], v[184:187]\n\t"
            "v_mfma_f32_16x16x32_bf16 v[188:191], v[128:131], v[192:195], v[188:191]\n\t"
            "ds_read_b128 v[192:195], v232 offset:128\n\t"
            "v_mfma_f32_16x16x32_bf16 v[188:191], v[132:135], v[196:199], v[188:191]\n\t"
            "ds_read_b128 v[196:199], v233 offset:128\n\t"
            "s_waitcnt lgkmcnt(2)\n\t"
            "v_mfma_f32_16x16x32_bf16 v[160:163], v[16:19], v[224:227], v[160:163]\n\t"
            "v_mfma_f32_16x16x32_bf16 v[160:163], v[20:23], v[228:231], v[160:163]\n\t"
            "v_mfma_f32_16x16x32_bf16 v[164:167], v[40:43], v[224:227], v[164:167]\n\t"
            "v_mfma_f32_16x16x32_bf16 v[164:167], v[44:47], v[228:231], v[164:167]\n\t"
            "v_mfma_f32_16x16x32_bf16 v[176:179], v[64:67], v[224:227], v[176:179]\n\t"
            "v_mfma_f32_16x16x32_bf16 v[176:179], v[68:71], v[228:231], v[176:179]\n\t"
            "v_mfma_f32_16x16x32_bf16 v[180:183], v[88:91], v[224:227], v[180:183]\n\t"
            "v_mfma_f32_16x16x32_bf16 v[180:183], v[92:95], v[228:231], v[180:183]\n\t"
            "s_nop 1\n\t"
            "v_mul_f32 v160, v237, v160\n\t"
            "v_mfma_f32_16x16x32_bf16 v[184:187], v[112:115], v[224:227], v[184:187]\n\t"
            "v_mul_f32 v161, v237, v161\n\t"
            "v_mul_f32 v162, v237, v162\n\t"
            "v_mfma_f32_16x16x32_bf16 v[184:187], v[116:119], v[228:231], v[184:187]\n\t"
            "v_mul_f32 v164, v237, v164\n\t"
            "v_mul_f32 v165, v237, v165\n\t"
            "v_mfma_f32_16x16x32_bf16 v[188:191], v[136:139], v[224:227], v[188:191]\n\t"
            "ds_read_b128 v[224:227], v232\n\t"
            "v_mul_f32 v176, v237, v176\n\t"
            "v_mfma_f32_16x16x32_bf16 v[188:191], v[140:143], v[228:231], v[188:191]\n\t"
            "ds_read_b128 v[228:231], v233\n\t"
            "v_mul_f32 v177, v237, v177\n\t"
            "v_mfma_f32_16x16x32_bf16 v[172:175], v[80:83], v[208:211], v[172:175]\n\t"
            "v_mul_f32 v180, v237, v180\n\t"
            "v_mul_f32 v181, v237, v181\n\t"
            "v_mfma_f32_16x16x32_bf16 v[172:175], v[84:87], v[212:215], v[172:175]\n\t"
            "v_mul_f32 v184, v237, v184\n\t"
            "v_mul_f32 v185, v237, v185\n\t"
            "v_mfma_f32_16x16x32_bf16 v[152:155], v[104:107], v[208:211], v[152:155]\n\t"
            "v_mul_f32 v188, v237, v188\n\t"
            "v_mul_f32 v189, v237, v189\n\t"
            "v_mfma_f32_16x16x32_bf16 v[152:155], v[108:111], v[212:215], v[152:155]\n\t"
            "v_mul_f32 v163, v237, v163\n\t"
            "v_cvt_pk_bf16_f32 v160, v160, v161\n\t"
            "v_mfma_f32_16x16x32_bf16 v[156:159], v[128:131], v[208:211], v[156:159]\n\t"
            "v_mul_f32 v166, v237, v166\n\t"
            "v_mul_f32 v167, v237, v167\n\t"
            "v_mfma_f32_16x16x32_bf16 v[156:159], v[132:135], v[212:215], v[156:159]\n\t"
            "v_cvt_pk_bf16_f32 v164, v164, v165\n\t"
            "v_mul_f32 v178, v237, v178\n\t"
            "v_mfma_f32_16x16x32_bf16 v[144:147], v[16:19], v[216:219], v[144:147]\n\t"
            "v_mul_f32 v179, v237, v179\n\t"
            "v_cvt_pk_bf16_f32 v176, v176, v177\n\t"
            "v_mfma_f32_16x16x32_bf16 v[144:147], v[20:23], v[220:223], v[144:147]\n\t"
            "v_mul_f32 v182, v237, v182\n\t"
            "v_mul_f32 v183, v237, v183\n\t"
            "v_mfma_f32_16x16x32_bf16 v[148:151], v[40:43], v[216:219], v[148:151]\n\t"
            "v_cvt_pk_bf16_f32 v180, v180, v181\n\t"
            "v_mul_f32 v186, v237, v186\n\t"
            "v_mfma_f32_16x16x32_bf16 v[148:151], v[44:47], v[220:223], v[148:151]\n\t"
            "v_mul_f32 v187, v237, v187\n\t"
            "v_cvt_pk_bf16_f32 v184, v184, v185\n\t"
            "v_mfma_f32_16x16x32_bf16 v[168:171], v[64:67], v[216:219], v[168:171]\n\t"
            "v_mul_f32 v144, v236, v144\n\t"
            "v_mul_f32 v145, v236, v145\n\t"
            "v_mfma_f32_16x16x32_bf16 v[168:171], v[68:71], v[220:223], v[168:171]\n\t"
            "v_mul_f32 v146, v236, v146\n\t"
            "v_mul_f32 v147, v236, v147\n\t"
            "v_mfma_f32_16x16x32_bf16 v[172:175], v[88:91], v[216:219], v[172:175]\n\t"
            "v_mul_f32 v148, v236, v148\n\t"
            "v_mul_f32 v149, v236, v149\n\t"
            "v_mfma_f32_16x16x32_bf16 v[172:175], v[92:95], v[220:223], v[172:175]\n\t"
            "v_cvt_pk_bf16_f32 v144, v144, v145\n\t"
            "v_mul_f32 v150, v236, v150\n\t"
            "v_mfma_f32_16x16x32_bf16 v[152:155], v[112:115], v[216:219], v[152:155]\n\t"
            "v_mul_f32 v168, v236, v168\n\t"
            "v_mul_f32 v169, v236, v169\n\t"
            "v_mfma_f32_16x16x32_bf16 v[152:155], v[116:119], v[220:223], v[152:155]\n\t"
            "v_mul_f32 v151, v236, v151\n\t"
            "v_cvt_pk_bf16_f32 v148, v148, v149\n\t"
            "v_mfma_f32_16x16x32_bf16 v[156:159], v[136:139], v[216:219], v[156:159]\n\t"
            "v_mul_f32 v172, v236, v172\n\t"
            "v_mul_f32 v173, v236, v173\n\t"
            "v_mfma_f32_16x16x32_bf16 v[156:159], v[140:143], v[220:223], v[156:159]\n\t"
            "v_mul_f32 v170, v236, v170\n\t"
            "v_mul_f32 v171, v236, v171\n\t"
            "s_waitcnt lgkmcnt(0)\n\t"
            "v_mfma_f32_16x16x32_bf16 v[200:203], v[224:227], v[0:3], 0\n\t"
            "v_mul_f32 v152, v236, v152\n\t"
            "v_mul_f32 v153, v236, v153\n\t"
            "v_mfma_f32_16x16x32_bf16 v[200:203], v[228:231], v[4:7], v[200:203]\n\t"
            "v_cvt_pk_bf16_f32 v168, v168, v169\n\t"
            "v_mul_f32 v174, v236, v174\n\t"
            "v_mfma_f32_16x16x32_bf16 v[204:207], v[228:231], v[28:31], 0\n\t"
            "v_mul_f32 v156, v236, v156\n\t"
            "v_mul_f32 v157, v236, v157\n\t"
            "v_mfma_f32_16x16x32_bf16 v[204:207], v[224:227], v[24:27], v[204:207]\n\t"
            "v_mul_f32 v175, v236, v175\n\t"
            "v_cvt_pk_bf16_f32 v172, v172, v173\n\t"
            "v_mfma_f32_16x16x32_bf16 v[208:211], v[224:227], v[48:51], 0\n\t"
            "v_mul_f32 v154, v236, v154\n\t"
            "v_mul_f32 v155, v236, v155\n\t"
            "v_mfma_f32_16x16x32_bf16 v[208:211], v[228:231], v[52:55], v[208:211]\n\t"
            "v_cvt_pk_bf16_f32 v152, v152, v153\n\t"
            "v_mul_f32 v158, v236, v158\n\t"
            "v_mfma_f32_16x16x32_bf16 v[212:215], v[228:231], v[76:79], 0\n\t"
            "v_mul_f32 v159, v236, v159\n\t"
            "v_cvt_pk_bf16_f32 v156, v156, v157\n\t"
            "v_mfma_f32_16x16x32_bf16 v[212:215], v[224:227], v[72:75], v[212:215]\n\t"
            "v_cvt_pk_bf16_f32 v145, v146, v147\n\t"
            "v_cvt_pk_bf16_f32 v149, v150, v151\n\t"
            "v_mfma_f32_16x16x32_bf16 v[216:219], v[224:227], v[96:99], 0\n\t"
            "v_cvt_pk_bf16_f32 v169, v170, v171\n\t"
            "v_cvt_pk_bf16_f32 v173, v174, v175\n\t"
            "v_mfma_f32_16x16x32_bf16 v[216:219], v[228:231], v[100:103], v[216:219]\n\t"
            "v_cvt_pk_bf16_f32 v153, v154, v155\n\t"
            "v_cvt_pk_bf16_f32 v157, v158, v159\n\t"
            "v_mfma_f32_16x16x32_bf16 v[220:223], v[228:231], v[124:127], 0\n\t"
            "ds_read_b128 v[228:231], v233 offset:256\n\t"
            "v_mul_f32 v190, v237, v190\n\t"
            "v_mfma_f32_16x16x32_bf16 v[220:223], v[224:227], v[120:123], v[220:223]\n\t"
            "ds_read_b128 v[224:227], v232 offset:256\n\t"
            "s_mov_b64 exec, -1\n\t"
            "ds_write_b64 v234, v[144:145]\n\t"
            "ds_write_b64 v234, v[148:149] offset:128\n\t"
            "ds_write_b64 v234, v[168:169] offset:256\n\t"
            "ds_write_b64 v248, v[172:173]\n\t"
            "ds_write_b64 v248, v[152:153] offset:128\n\t"
            "ds_write_b64 v248, v[156:157] offset:256\n\t"
            "s_mov_b64 exec, -1\n\t"
            "v_mfma_f32_16x16x32_bf16 v[200:203], v[192:195], v[8:11], v[200:203]\n\t"
            "v_mul_f32 v191, v237, v191\n\t"
            "v_cvt_pk_bf16_f32 v188, v188, v189\n\t"
            "v_mfma_f32_16x16x32_bf16 v[200:203], v[196:199], v[12:15], v[200:203]\n\t"
            "ds_read_b64 v[236:237], v247\n\t"
            "v_cvt_pk_bf16_f32 v161, v162, v163\n\t"
            "v_mfma_f32_16x16x32_bf16 v[204:207], v[196:199], v[36:39], v[204:207]\n\t"
            "v_cvt_pk_bf16_f32 v165, v166, v167\n\t"
            "v_cvt_pk_bf16_f32 v177, v178, v179\n\t"
            "v_mfma_f32_16x16x32_bf16 v[204:207], v[192:195], v[32:35], v[204:207]\n\t"
            "v_cvt_pk_bf16_f32 v181, v182, v183\n\t"
            "v_cvt_pk_bf16_f32 v185, v186, v187\n\t"
            "v_mfma_f32_16x16x32_bf16 v[208:211], v[192:195], v[56:59], v[208:211]\n\t"
            "v_cvt_pk_bf16_f32 v189, v190, v191\n\t"
            "v_mfma_f32_16x16x32_bf16 v[208:211], v[196:199], v[60:63], v[208:211]\n\t"
            "s_mov_b64 exec, s[80:81]\n\t"
            "ds_write_b64 v234, v[160:161] offset:6144\n\t"
            "ds_write_b64 v234, v[164:165] offset:6272\n\t"
            "ds_write_b64 v234, v[176:177] offset:6400\n\t"
            "ds_write_b64 v248, v[180:181] offset:6144\n\t"
            "ds_write_b64 v248, v[184:185] offset:6272\n\t"
            "ds_write_b64 v248, v[188:189] offset:6400\n\t"
            "s_mov_b64 exec, -1\n\t"
            "v_mfma_f32_16x16x32_bf16 v[212:215], v[196:199], v[84:87], v[212:215]\n\t"
            "s_waitcnt lgkmcnt(14)\n\t"
            "ds_read_b64 v[248:249], v247 offset:8\n\t"
            "v_mfma_f32_16x16x32_bf16 v[212:215], v[192:195], v[80:83], v[212:215]\n\t"
            "v_mfma_f32_16x16x32_bf16 v[216:219], v[192:195], v[104:107], v[216:219]\n\t"
            "v_mfma_f32_16x16x32_bf16 v[216:219], v[196:199], v[108:111], v[216:219]\n\t"
            "v_mfma_f32_16x16x32_bf16 v[220:223], v[196:199], v[132:135], v[220:223]\n\t"
            "s_waitcnt lgkmcnt(14)\n\t"
            "ds_read_b128 v[196:199], v233 offset:6144\n\t"
            "v_mfma_f32_16x16x32_bf16 v[220:223], v[192:195], v[128:131], v[220:223]\n\t"
            "s_waitcnt lgkmcnt(14)\n\t"
            "ds_read_b128 v[192:195], v232 offset:6144\n\t"
            "v_mfma_f32_16x16x32_bf16 v[200:203], v[224:227], v[16:19], v[200:203]\n\t"
            "v_mfma_f32_16x16x32_bf16 v[200:203], v[228:231], v[20:23], v[200:203]\n\t"
            "v_mfma_f32_16x16x32_bf16 v[204:207], v[228:231], v[44:47], v[204:207]\n\t"
            "v_mfma_f32_16x16x32_bf16 v[204:207], v[224:227], v[40:43], v[204:207]\n\t"
            "v_mfma_f32_16x16x32_bf16 v[208:211], v[224:227], v[64:67], v[208:211]\n\t"
            "v_mfma_f32_16x16x32_bf16 v[208:211], v[228:231], v[68:71], v[208:211]\n\t"
            "v_mfma_f32_16x16x32_bf16 v[212:215], v[228:231], v[92:95], v[212:215]\n\t"
            "v_mfma_f32_16x16x32_bf16 v[212:215], v[224:227], v[88:91], v[212:215]\n\t"
            "s_waitcnt lgkmcnt(9)\n\t"
            "s_nop 0\n\t"
            "v_mul_f32 v200, v236, v200\n\t"
            "v_mfma_f32_16x16x32_bf16 v[216:219], v[224:227], v[112:115], v[216:219]\n\t"
            "v_mul_f32 v201, v237, v201\n\t"
            "s_waitcnt lgkmcnt(2)\n\t"
            "v_mul_f32 v202, v248, v202\n\t"
            "v_mfma_f32_16x16x32_bf16 v[216:219], v[228:231], v[116:119], v[216:219]\n\t"
            "v_mul_f32 v204, v236, v204\n\t"
            "v_mul_f32 v205, v237, v205\n\t"
            "v_mfma_f32_16x16x32_bf16 v[220:223], v[228:231], v[140:143], v[220:223]\n\t"
            "ds_read_b128 v[228:231], v233 offset:6272\n\t"
            "v_mul_f32 v208, v236, v208\n\t"
            "v_mfma_f32_16x16x32_bf16 v[220:223], v[224:227], v[136:139], v[220:223]\n\t"
            "ds_read_b128 v[224:227], v232 offset:6272\n\t"
            "v_mul_f32 v209, v237, v209\n\t"
            "s_waitcnt lgkmcnt(2)\n\t"
            "v_mfma_f32_16x16x32_bf16 v[144:147], v[192:195], v[0:3], 0\n\t"
            "v_mul_f32 v212, v236, v212\n\t"
            "v_mul_f32 v213, v237, v213\n\t"
            "v_mfma_f32_16x16x32_bf16 v[144:147], v[196:199], v[4:7], v[144:147]\n\t"
            "v_mul_f32 v216, v236, v216\n\t"
            "v_mul_f32 v217, v237, v217\n\t"
            "v_mfma_f32_16x16x32_bf16 v[148:151], v[196:199], v[28:31], 0\n\t"
            "v_mul_f32 v220, v236, v220\n\t"
            "v_mul_f32 v221, v237, v221\n\t"
            "v_mfma_f32_16x16x32_bf16 v[148:151], v[192:195], v[24:27], v[148:151]\n\t"
            "ds_read_b64 v[236:237], v247 offset:64\n\t"
            "v_mul_f32 v203, v249, v203\n\t"
            "v_mfma_f32_16x16x32_bf16 v[168:171], v[192:195], v[48:51], 0\n\t"
            "v_mul_f32 v206, v248, v206\n\t"
            "v_mul_f32 v207, v249, v207\n\t"
            "v_mfma_f32_16x16x32_bf16 v[168:171], v[196:199], v[52:55], v[168:171]\n\t"
            "v_mul_f32 v210, v248, v210\n\t"
            "v_mul_f32 v211, v249, v211\n\t"
            "v_mfma_f32_16x16x32_bf16 v[172:175], v[196:199], v[76:79], 0\n\t"
            "v_mul_f32 v214, v248, v214\n\t"
            "v_mul_f32 v215, v249, v215\n\t"
            "v_mfma_f32_16x16x32_bf16 v[172:175], v[192:195], v[72:75], v[172:175]\n\t"
            "v_mul_f32 v218, v248, v218\n\t"
            "v_mul_f32 v219, v249, v219\n\t"
            "v_mfma_f32_16x16x32_bf16 v[152:155], v[192:195], v[96:99], 0\n\t"
            "v_mul_f32 v222, v248, v222\n\t"
            "v_mul_f32 v223, v249, v223\n\t"
            "v_mfma_f32_16x16x32_bf16 v[152:155], v[196:199], v[100:103], v[152:155]\n\t"
            "ds_read_b64 v[248:249], v247 offset:72\n\t"
            "v_cvt_pk_bf16_f32 v200, v200, v201\n\t"
            "v_mfma_f32_16x16x32_bf16 v[156:159], v[196:199], v[124:127], 0\n\t"
            "ds_read_b128 v[196:199], v233 offset:6400\n\t"
            "v_cvt_pk_bf16_f32 v204, v204, v205\n\t"
            "v_mfma_f32_16x16x32_bf16 v[156:159], v[192:195], v[120:123], v[156:159]\n\t"
            "ds_read_b128 v[192:195], v232 offset:6400\n\t"
            "v_cvt_pk_bf16_f32 v208, v208, v209\n\t"
            "s_waitcnt lgkmcnt(4)\n\t"
            "v_mfma_f32_16x16x32_bf16 v[144:147], v[224:227], v[8:11], v[144:147]\n\t"
            "v_cvt_pk_bf16_f32 v201, v202, v203\n\t"
            "v_cvt_pk_bf16_f32 v205, v206, v207\n\t"
            "v_mfma_f32_16x16x32_bf16 v[144:147], v[228:231], v[12:15], v[144:147]\n\t"
            "v_cvt_pk_bf16_f32 v209, v210, v211\n\t"
            "v_cvt_pk_bf16_f32 v212, v212, v213\n\t"
            "v_mfma_f32_16x16x32_bf16 v[148:151], v[228:231], v[36:39], v[148:151]\n\t"
            "v_cvt_pk_bf16_f32 v216, v216, v217\n\t"
            "v_cvt_pk_bf16_f32 v220, v220, v221\n\t"
            "v_mfma_f32_16x16x32_bf16 v[148:151], v[224:227], v[32:35], v[148:151]\n\t"
            "v_cvt_pk_bf16_f32 v213, v214, v215\n\t"
            "v_cvt_pk_bf16_f32 v217, v218, v219\n\t"
            "v_mfma_f32_16x16x32_bf16 v[168:171], v[224:227], v[56:59], v[168:171]\n\t"
            "v_cvt_pk_bf16_f32 v221, v222, v223\n\t"
            "v_mfma_f32_16x16x32_bf16 v[168:171], v[228:231], v[60:63], v[168:171]\n\t"
            "v_mfma_f32_16x16x32_bf16 v[172:175], v[228:231], v[84:87], v[172:175]\n\t"
            "v_mfma_f32_16x16x32_bf16 v[172:175], v[224:227], v[80:83], v[172:175]\n\t"
            "v_mfma_f32_16x16x32_bf16 v[152:155], v[224:227], v[104:107], v[152:155]\n\t"
            "v_mfma_f32_16x16x32_bf16 v[152:155], v[228:231], v[108:111], v[152:155]\n\t"
            "v_mfma_f32_16x16x32_bf16 v[156:159], v[228:231], v[132:135], v[156:159]\n\t"
            "ds_read_b128 v[228:231], v233 offset:12288\n\t"
            "v_mfma_f32_16x16x32_bf16 v[156:159], v[224:227], v[128:131], v[156:159]\n\t"
            "ds_read_b128 v[224:227], v232 offset:12288\n\t"
            "s_waitcnt lgkmcnt(2)\n\t"
            "v_mfma_f32_16x16x32_bf16 v[144:147], v[192:195], v[16:19], v[144:147]\n\t"
            "v_mfma_f32_16x16x32_bf16 v[144:147], v[196:199], v[20:23], v[144:147]\n\t"
            "v_mfma_f32_16x16x32_bf16 v[148:151], v[196:199], v[44:47], v[148:151]\n\t"
            "v_mfma_f32_16x16x32_bf16 v[148:151], v[192:195], v[40:43], v[148:151]\n\t"
            "v_mfma_f32_16x16x32_bf16 v[168:171], v[192:195], v[64:67], v[168:171]\n\t"
            "v_mfma_f32_16x16x32_bf16 v[168:171], v[196:199], v[68:71], v[168:171]\n\t"
            "v_mfma_f32_16x16x32_bf16 v[172:175], v[196:199], v[92:95], v[172:175]\n\t"
            "v_mfma_f32_16x16x32_bf16 v[172:175], v[192:195], v[88:91], v[172:175]\n\t"
            "s_nop 1\n\t"
            "v_mul_f32 v144, v236, v144\n\t"
            "v_mfma_f32_16x16x32_bf16 v[152:155], v[192:195], v[112:115], v[152:155]\n\t"
            "v_mul_f32 v145, v237, v145\n\t"
            "v_mul_f32 v146, v248, v146\n\t"
            "v_mfma_f32_16x16x32_bf16 v[152:155], v[196:199], v[116:119], v[152:155]\n\t"
            "v_mul_f32 v148, v236, v148\n\t"
            "v_mul_f32 v149, v237, v149\n\t"
            "v_mfma_f32_16x16x32_bf16 v[156:159], v[196:199], v[140:143], v[156:159]\n\t"
            "ds_read_b128 v[196:199], v233 offset:12416\n\t"
            "v_mul_f32 v168, v236, v168\n\t"
            "v_mfma_f32_16x16x32_bf16 v[156:159], v[192:195], v[136:139], v[156:159]\n\t"
            "ds_read_b128 v[192:195], v232 offset:12416\n\t"
            "v_mul_f32 v169, v237, v169\n\t"
            "s_waitcnt lgkmcnt(2)\n\t"
            "v_mfma_f32_16x16x32_bf16 v[160:163], v[224:227], v[0:3], 0\n\t"
            "v_mul_f32 v172, v236, v172\n\t"
            "v_mul_f32 v173, v237, v173\n\t"
            "v_mfma_f32_16x16x32_bf16 v[160:163], v[228:231], v[4:7], v[160:163]\n\t"
            "v_mul_f32 v152, v236, v152\n\t"
            "v_mul_f32 v153, v237, v153\n\t"
            "v_mfma_f32_16x16x32_bf16 v[164:167], v[228:231], v[28:31], 0\n\t"
            "v_mul_f32 v156, v236, v156\n\t"
            "v_mul_f32 v157, v237, v157\n\t"
            "v_mfma_f32_16x16x32_bf16 v[164:167], v[224:227], v[24:27], v[164:167]\n\t"
            "ds_read_b64 v[236:237], v247 offset:128\n\t"
            "v_mul_f32 v147, v249, v147\n\t"
            "v_mfma_f32_16x16x32_bf16 v[176:179], v[224:227], v[48:51], 0\n\t"
            "v_mul_f32 v150, v248, v150\n\t"
            "v_mul_f32 v151, v249, v151\n\t"
            "v_mfma_f32_16x16x32_bf16 v[176:179], v[228:231], v[52:55], v[176:179]\n\t"
            "v_mul_f32 v170, v248, v170\n\t"
            "v_mul_f32 v171, v249, v171\n\t"
            "v_mfma_f32_16x16x32_bf16 v[180:183], v[228:231], v[76:79], 0\n\t"
            "v_mul_f32 v174, v248, v174\n\t"
            "v_mul_f32 v175, v249, v175\n\t"
            "v_mfma_f32_16x16x32_bf16 v[180:183], v[224:227], v[72:75], v[180:183]\n\t"
            "v_mul_f32 v154, v248, v154\n\t"
            "v_mul_f32 v155, v249, v155\n\t"
            "v_mfma_f32_16x16x32_bf16 v[184:187], v[224:227], v[96:99], 0\n\t"
            "v_mul_f32 v158, v248, v158\n\t"
            "v_mul_f32 v159, v249, v159\n\t"
            "v_mfma_f32_16x16x32_bf16 v[184:187], v[228:231], v[100:103], v[184:187]\n\t"
            "ds_read_b64 v[248:249], v247 offset:136\n\t"
            "v_cvt_pk_bf16_f32 v202, v144, v145\n\t"
            "v_mfma_f32_16x16x32_bf16 v[188:191], v[228:231], v[124:127], 0\n\t"
            "ds_read_b128 v[228:231], v233 offset:12544\n\t"
            "v_cvt_pk_bf16_f32 v203, v146, v147\n\t"
            "v_mfma_f32_16x16x32_bf16 v[188:191], v[224:227], v[120:123], v[188:191]\n\t"
            "ds_read_b128 v[224:227], v232 offset:12544\n\t"
            "v_cvt_pk_bf16_f32 v206, v148, v149\n\t"
            "s_waitcnt lgkmcnt(4)\n\t"
            "v_mfma_f32_16x16x32_bf16 v[188:191], v[196:199], v[132:135], v[188:191]\n\t"
            "v_cvt_pk_bf16_f32 v207, v150, v151\n\t"
            "v_cvt_pk_bf16_f32 v210, v168, v169\n\t"
            "v_mfma_f32_16x16x32_bf16 v[188:191], v[192:195], v[128:131], v[188:191]\n\t"
            "v_cvt_pk_bf16_f32 v211, v170, v171\n\t"
            "v_cvt_pk_bf16_f32 v214, v172, v173\n\t"
            "v_mfma_f32_16x16x32_bf16 v[176:179], v[192:195], v[56:59], v[176:179]\n\t"
            "ds_read_b128 v[168:171], v254\n\t"
            "v_cvt_pk_bf16_f32 v215, v174, v175\n\t"
            "v_mfma_f32_16x16x32_bf16 v[176:179], v[196:199], v[60:63], v[176:179]\n\t"
            "ds_read_b128 v[172:175], v254 offset:64\n\t"
            "v_cvt_pk_bf16_f32 v218, v152, v153\n\t"
            "v_mfma_f32_16x16x32_bf16 v[180:183], v[196:199], v[84:87], v[180:183]\n\t"
            "v_cvt_pk_bf16_f32 v219, v154, v155\n\t"
            "v_cvt_pk_bf16_f32 v222, v156, v157\n\t"
            "v_mfma_f32_16x16x32_bf16 v[180:183], v[192:195], v[80:83], v[180:183]\n\t"
            "v_cvt_pk_bf16_f32 v223, v158, v159\n\t"
            "v_mfma_f32_16x16x32_bf16 v[160:163], v[192:195], v[8:11], v[160:163]\n\t"
            "v_mfma_f32_16x16x32_bf16 v[160:163], v[196:199], v[12:15], v[160:163]\n\t"
            "ds_write_b128 v243, v[200:203]\n\t"
            "s_waitcnt lgkmcnt(3)\n\t"
            "v_mfma_f32_16x16x32_bf16 v[188:191], v[228:231], v[140:143], v[188:191]\n\t"
            "v_mfma_f32_16x16x32_bf16 v[188:191], v[224:227], v[136:139], v[188:191]\n\t"
            "s_waitcnt vmcnt(0)\n\t"
            "ds_read_b64 v[144:145], v253\n\t"
            "v_mfma_f32_16x16x32_bf16 v[164:167], v[196:199], v[36:39], v[164:167]\n\t"
            "ds_read_b64 v[146:147], v253 offset:96\n\t"
            "ds_read_b64 v[148:149], v253 offset:1744\n\t"
            "v_mfma_f32_16x16x32_bf16 v[164:167], v[192:195], v[32:35], v[164:167]\n\t"
            "ds_read_b64 v[150:151], v253 offset:1840\n\t"
            "ds_read_b64 v[152:153], v253 offset:8\n\t"
            "v_mfma_f32_16x16x32_bf16 v[176:179], v[224:227], v[64:67], v[176:179]\n\t"
            "ds_read_b64 v[154:155], v253 offset:104\n\t"
            "ds_read_b64 v[156:157], v253 offset:1752\n\t"
            "v_mfma_f32_16x16x32_bf16 v[176:179], v[228:231], v[68:71], v[176:179]\n\t"
            "ds_read_b64 v[158:159], v253 offset:1848\n\t"
            "v_mul_f32 v191, v249, v191\n\t"
            "v_mfma_f32_16x16x32_bf16 v[180:183], v[228:231], v[92:95], v[180:183]\n\t"
            "v_mul_f32 v188, v236, v188\n\t"
            "v_mul_f32 v189, v237, v189\n\t"
            "v_mfma_f32_16x16x32_bf16 v[180:183], v[224:227], v[88:91], v[180:183]\n\t"
            "v_mul_f32 v190, v248, v190\n\t"
            "v_cvt_pk_bf16_f32 v188, v188, v189\n\t"
            "v_mfma_f32_16x16x32_bf16 v[160:163], v[224:227], v[16:19], v[160:163]\n\t"
            "v_mul_f32 v176, v236, v176\n\t"
            "v_mul_f32 v177, v237, v177\n\t"
            "v_mfma_f32_16x16x32_bf16 v[160:163], v[228:231], v[20:23], v[160:163]\n\t"
            "v_mul_f32 v178, v248, v178\n\t"
            "v_mul_f32 v179, v249, v179\n\t"
            "v_mfma_f32_16x16x32_bf16 v[164:167], v[228:231], v[44:47], v[164:167]\n\t"
            "v_cvt_pk_bf16_f32 v176, v176, v177\n\t"
            "v_cvt_pk_bf16_f32 v177, v178, v179\n\t"
            "v_mfma_f32_16x16x32_bf16 v[164:167], v[224:227], v[40:43], v[164:167]\n\t"
            "ds_write_b64 v244, v[176:177] offset:3072\n\t"
            "v_mfma_f32_16x16x32_bf16 v[184:187], v[192:195], v[104:107], v[184:187]\n\t"
            "v_mul_f32 v180, v236, v180\n\t"
            "v_mul_f32 v181, v237, v181\n\t"
            "v_mfma_f32_16x16x32_bf16 v[184:187], v[196:199], v[108:111], v[184:187]\n\t"
            "v_mul_f32 v160, v236, v160\n\t"
            "v_mul_f32 v161, v237, v161\n\t"
            "v_mfma_f32_16x16x32_bf16 v[184:187], v[224:227], v[112:115], v[184:187]\n\t"
            "v_mul_f32 v164, v236, v164\n\t"
            "v_mul_f32 v165, v237, v165\n\t"
            "v_mfma_f32_16x16x32_bf16 v[184:187], v[228:231], v[116:119], v[184:187]\n\t"
            "v_mul_f32 v182, v248, v182\n\t"
            "v_mul_f32 v183, v249, v183\n\t"
            "v_cvt_pk_bf16_f32 v180, v180, v181\n\t"
            "v_mov_b64 v[176:177], 0\n\t"
            "v_mov_b64 v[178:179], 0\n\t"
            "v_mul_f32 v162, v248, v162\n\t"
            "v_mul_f32 v163, v249, v163\n\t"
            "s_waitcnt lgkmcnt(5)\n\t"
            "v_smfmac_f32_16x16x64_bf16 v[176:179], v[168:171], v[144:151], v255\n\t"
            "ds_read_b128 v[168:171], v254 offset:128\n\t"
            "ds_read_b64 v[144:145], v253 offset:24\n\t"
            "ds_read_b64 v[146:147], v253 offset:120\n\t"
            "ds_read_b64 v[148:149], v253 offset:1768\n\t"
            "ds_read_b64 v[150:151], v253 offset:1864\n\t"
            "v_cvt_pk_bf16_f32 v160, v160, v161\n\t"
            "v_mul_f32 v166, v248, v166\n\t"
            "v_mul_f32 v167, v249, v167\n\t"
            "v_cvt_pk_bf16_f32 v164, v164, v165\n\t"
            "v_cvt_pk_bf16_f32 v181, v182, v183\n\t"
            "v_cvt_pk_bf16_f32 v161, v162, v163\n\t"
            "v_cvt_pk_bf16_f32 v165, v166, v167\n\t"
            "v_mov_b64 v[182:183], 0\n\t"
            "v_mul_f32 v187, v249, v187\n\t"
            "v_mov_b32 v249, s76\n\t"
            "v_mul_f32 v184, v236, v184\n\t"
            "v_mul_f32 v185, v237, v185\n\t"
            "v_mul_f32 v186, v248, v186\n\t"
            "v_cvt_pk_bf16_f32 v184, v184, v185\n\t"
            "v_cvt_pk_bf16_f32 v185, v186, v187\n\t"
            "v_mov_b64 v[186:187], 0\n\t"
            "v_cvt_pk_bf16_f32 v189, v190, v191\n\t"
            "v_mov_b64 v[190:191], 0\n\t"
            "v_cvt_pk_bf16_f32 v176, v176, v177\n\t"
            "v_add_u32 v236, s67, v250\n\t"
            "v_cvt_pk_bf16_f32 v177, v178, v179\n\t"
            "v_add_u32 v248, s67, v251\n\t"
            "s_nop 0\n\t"
            "v_mfma_f32_4x4x4_16b_bf16 v[196:199], v[176:177], v[176:177], 0\n\t"
            "ds_read_b64 v[162:163], v253 offset:112\n\t"
            "ds_read_b64 v[166:167], v253 offset:1856\n\t"
            "ds_read_b32 v249, v249\n\t"
            "ds_write_b64 v244, v[180:181] offset:4608\n\t"
            "ds_write_b64 v244, v[160:161]\n\t"
            "s_waitcnt lgkmcnt(14)\n\t"
            "ds_read_b64 v[160:161], v253 offset:16\n\t"
            "s_waitcnt lgkmcnt(14)\n\t"
            "ds_write_b64 v244, v[164:165] offset:1536\n\t"
            "s_waitcnt lgkmcnt(14)\n\t"
            "ds_read_b64 v[164:165], v253 offset:1760\n\t"
            "v_mov_b64 v[180:181], 0\n\t"
            "s_waitcnt lgkmcnt(14)\n\t"
            "s_nop 0\n\t"
            "v_smfmac_f32_16x16x64_bf16 v[180:183], v[172:175], v[152:159], v255\n\t"
            "ds_read_b128 v[172:175], v254 offset:192\n\t"
            "s_waitcnt lgkmcnt(14)\n\t"
            "ds_read_b64 v[152:153], v253 offset:32\n\t"
            "s_waitcnt lgkmcnt(14)\n\t"
            "ds_read_b64 v[154:155], v253 offset:128\n\t"
            "s_waitcnt lgkmcnt(14)\n\t"
            "ds_read_b64 v[156:157], v253 offset:1776\n\t"
            "s_waitcnt lgkmcnt(14)\n\t"
            "ds_read_b64 v[158:159], v253 offset:1872\n\t"
            "s_waitcnt lgkmcnt(14)\n\t"
            "ds_write_b64 v244, v[184:185] offset:6144\n\t"
            "v_mov_b64 v[184:185], 0\n\t"
            "v_cvt_pk_bf16_f32 v178, v180, v181\n\t"
            "v_cvt_pk_bf16_f32 v179, v182, v183\n\t"
            "v_mov_b64 v[180:181], 0\n\t"
            "s_waitcnt lgkmcnt(11)\n\t"
            "v_readfirstlane_b32 s26, v249\n\t"
            "v_mfma_f32_16x16x32_bf16 v[192:195], v[238:241], v[176:179], 0\n\t"
            "ds_read_b32 v249, v252 offset:12\n\t"
            "ds_write_b64 v244, v[188:189] offset:7680\n\t"
            "v_mfma_f32_4x4x4_16b_bf16 v[196:199], v[178:179], v[178:179], v[196:199]\n\t"
            "v_mov_b64 v[188:189], 0\n\t"
            "s_and_b32 s68, s26, 0x7ffffff\n\t"
            "s_add_u32 s68, s68, s78\n\t"
            "s_and_b32 s69, s26, 0x38000000\n\t"
            "s_or_b32 s69, s69, s39\n\t"
            "s_bitcmp1_b32 s26, 27\n\t"
            "s_cselect_b32 s79, 2.0, s68\n\t"
            "s_waitcnt lgkmcnt(8)\n\t"
            "v_smfmac_f32_16x16x64_bf16 v[184:187], v[168:171], v[160:167], v255\n\t"
            "s_and_b32 s57, s56, 3\n\t"
            "s_lshl_b32 s58, s57, 3\n\t"
            "s_lshr_b32 m0, s54, s58\n\t"
            "s_waitcnt lgkmcnt(7)\n\t"
            "v_smfmac_f32_16x16x64_bf16 v[188:191], v[172:175], v[144:151], v255\n\t"
            "s_lshl_b32 s58, s57, 4\n\t"
            "s_lshr_b64 s[36:37], s[50:51], s58\n\t"
            "s_and_b32 s57, s36, 0xffff\n\t"
            "s_add_u32 s57, s57, s67\n\t"
            "ds_read_b128 v[168:171], v254 offset:256\n\t"
            "ds_read_b64 v[160:161], v253 offset:40\n\t"
            "ds_read_b64 v[162:163], v253 offset:136\n\t"
            "ds_read_b64 v[164:165], v253 offset:1784\n\t"
            "ds_read_b64 v[166:167], v253 offset:1880\n\t"
            "ds_read_b128 v[172:175], v254 offset:320\n\t"
            "ds_read_b64 v[144:145], v253 offset:48\n\t"
            "ds_read_b64 v[146:147], v253 offset:144\n\t"
            "s_waitcnt lgkmcnt(14)\n\t"
            "ds_read_b64 v[148:149], v253 offset:1792\n\t"
            "s_waitcnt lgkmcnt(14)\n\t"
            "ds_read_b64 v[150:151], v253 offset:1888\n\t"
            "v_writelane_b32 v236, s57, 63\n\t"
            "v_writelane_b32 v236, s59, m0\n\t"
            "v_mov_b64 v[182:183], 0\n\t"
            "v_cvt_pk_bf16_f32 v184, v184, v185\n\t"
            "v_cvt_pk_bf16_f32 v185, v186, v187\n\t"
            "v_xor_b32 v237, 16, v236\n\t"
            "v_cvt_pk_bf16_f32 v186, v188, v189\n\t"
            "v_mfma_f32_4x4x4_16b_bf16 v[196:199], v[184:185], v[184:185], v[196:199]\n\t"
            "s_waitcnt lgkmcnt(14)\n\t"
            "ds_write_b128 v236, v[176:179]\n\t"
            "v_mov_b64 v[176:177], 0\n\t"
            "v_mov_b64 v[178:179], 0\n\t"
            "v_cvt_pk_bf16_f32 v187, v190, v191\n\t"
            "v_mov_b64 v[188:189], 0\n\t"
            "v_mov_b64 v[190:191], 0\n\t"
            "v_mfma_f32_16x16x32_bf16 v[192:195], v[238:241], v[184:187], v[192:195]\n\t"
            "s_lshr_b64 s[36:37], s[52:53], s58\n\t"
            "s_and_b32 s58, s36, 0xffff\n\t"
            "s_add_u32 s58, s58, s67\n\t"
            "s_waitcnt lgkmcnt(10)\n\t"
            "v_smfmac_f32_16x16x64_bf16 v[176:179], v[168:171], v[152:159], v255\n\t"
            "s_and_b32 s38, s52, 0xffff\n\t"
            "s_add_u32 s38, s38, s67\n\t"
            "ds_read_b128 v[168:171], v254 offset:384\n\t"
            "s_waitcnt lgkmcnt(6)\n\t"
            "v_smfmac_f32_16x16x64_bf16 v[180:183], v[172:175], v[160:167], v255\n\t"
            "ds_read_b64 v[152:153], v253 offset:56\n\t"
            "v_mfma_f32_4x4x4_16b_bf16 v[196:199], v[186:187], v[186:187], v[196:199]\n\t"
            "ds_read_b64 v[154:155], v253 offset:152\n\t"
            "ds_read_b64 v[156:157], v253 offset:1800\n\t"
            "ds_read_b64 v[158:159], v253 offset:1896\n\t"
            "ds_read_b128 v[172:175], v254 offset:448\n\t"
            "ds_read_b64 v[160:161], v253 offset:64\n\t"
            "ds_read_b64 v[162:163], v253 offset:160\n\t"
            "ds_read_b64 v[164:165], v253 offset:1808\n\t"
            "ds_read_b64 v[166:167], v253 offset:1904\n\t"
            "s_waitcnt lgkmcnt(14)\n\t"
            "ds_write_b128 v237, v[184:187]\n\t"
            "v_mov_b64 v[184:185], 0\n\t"
            "v_mov_b64 v[186:187], 0\n\t"
            "v_cvt_pk_bf16_f32 v176, v176, v177\n\t"
            "v_cvt_pk_bf16_f32 v177, v178, v179\n\t"
            "v_cvt_pk_bf16_f32 v178, v180, v181\n\t"
            "v_cvt_pk_bf16_f32 v179, v182, v183\n\t"
            "v_mfma_f32_4x4x4_16b_bf16 v[196:199], v[176:177], v[176:177], v[196:199]\n\t"
            "s_waitcnt lgkmcnt(14)\n\t"
            "ds_write_b128 v236, v[176:179] offset:128\n\t"
            "s_waitcnt lgkmcnt(11)\n\t"
            "v_smfmac_f32_16x16x64_bf16 v[184:187], v[168:171], v[144:151], v255\n\t"
            "ds_read_b128 v[168:171], v254 offset:512\n\t"
            "v_mfma_f32_16x16x32_bf16 v[192:195], v[238:241], v[176:179], v[192:195]\n\t"
            "ds_read_b64 v[144:145], v253 offset:72\n\t"
            "ds_read_b64 v[146:147], v253 offset:168\n\t"
            "v_mfma_f32_4x4x4_16b_bf16 v[196:199], v[178:179], v[178:179], v[196:199]\n\t"
            "ds_read_b64 v[148:149], v253 offset:1816\n\t"
            "s_waitcnt lgkmcnt(10)\n\t"
            "v_smfmac_f32_16x16x64_bf16 v[188:191], v[172:175], v[152:159], v255\n\t"
            "ds_read_b64 v[150:151], v253 offset:1912\n\t"
            "ds_read_b128 v[172:175], v254 offset:576\n\t"
            "ds_read_b64 v[152:153], v253 offset:80\n\t"
            "ds_read_b64 v[154:155], v253 offset:176\n\t"
            "ds_read_b64 v[156:157], v253 offset:1824\n\t"
            "s_waitcnt lgkmcnt(14)\n\t"
            "ds_read_b64 v[158:159], v253 offset:1920\n\t"
            "v_mov_b64 v[176:177], 0\n\t"
            "v_mov_b64 v[178:179], 0\n\t"
            "v_mov_b64 v[180:181], 0\n\t"
            "v_mov_b64 v[182:183], 0\n\t"
            "v_cvt_pk_bf16_f32 v184, v184, v185\n\t"
            "v_cvt_pk_bf16_f32 v185, v186, v187\n\t"
            "v_cvt_pk_bf16_f32 v186, v188, v189\n\t"
            "v_cvt_pk_bf16_f32 v187, v190, v191\n\t"
            "v_mfma_f32_4x4x4_16b_bf16 v[196:199], v[184:185], v[184:185], v[196:199]\n\t"
            "s_waitcnt lgkmcnt(14)\n\t"
            "ds_write_b128 v237, v[184:187] offset:128\n\t"
            "v_mfma_f32_16x16x32_bf16 v[192:195], v[238:241], v[184:187], v[192:195]\n\t"
            "v_mov_b64 v[184:185], 0\n\t"
            "v_mov_b64 v[188:189], 0\n\t"
            "s_waitcnt lgkmcnt(10)\n\t"
            "v_smfmac_f32_16x16x64_bf16 v[176:179], v[168:171], v[160:167], v255\n\t"
            "ds_read_b128 v[168:171], v254 offset:640\n\t"
            "v_mfma_f32_4x4x4_16b_bf16 v[196:199], v[186:187], v[186:187], v[196:199]\n\t"
            "ds_read_b64 v[160:161], v253 offset:88\n\t"
            "ds_read_b64 v[162:163], v253 offset:184\n\t"
            "ds_read_b64 v[164:165], v253 offset:1832\n\t"
            "s_waitcnt lgkmcnt(9)\n\t"
            "v_smfmac_f32_16x16x64_bf16 v[180:183], v[172:175], v[144:151], v255\n\t"
            "ds_read_b64 v[166:167], v253 offset:1928\n\t"
            "ds_read_b128 v[172:175], v254 offset:704\n\t"
            "v_mov_b64 v[186:187], 0\n\t"
            "v_cvt_pk_bf16_f32 v176, v176, v177\n\t"
            "v_mov_b64 v[190:191], 0\n\t"
            "v_cvt_pk_bf16_f32 v177, v178, v179\n\t"
            "v_writelane_b32 v248, s58, 63\n\t"
            "v_writelane_b32 v248, s38, m0\n\t"
            "v_mfma_f32_4x4x4_16b_bf16 v[196:199], v[176:177], v[176:177], v[196:199]\n\t"
            "v_cvt_pk_bf16_f32 v178, v180, v181\n\t"
            "v_cvt_pk_bf16_f32 v179, v182, v183\n\t"
            "v_and_b32 v249, s69, v249\n\t"
            "s_nop 0\n\t"
            "v_mfma_f32_4x4x4_16b_bf16 v[196:199], v[178:179], v[178:179], v[196:199]\n\t"
            "ds_write_b128 v236, v[176:179] offset:256\n\t"
            "v_mfma_f32_16x16x32_bf16 v[192:195], v[238:241], v[176:179], v[192:195]\n\t"
            "ds_write_b128 v243, v[204:207] offset:1536\n\t"
            "s_waitcnt lgkmcnt(7)\n\t"
            "v_smfmac_f32_16x16x64_bf16 v[184:187], v[168:171], v[152:159], v255\n\t"
            "ds_write_b128 v243, v[208:211] offset:3072\n\t"
            "s_waitcnt lgkmcnt(3)\n\t"
            "v_smfmac_f32_16x16x64_bf16 v[188:191], v[172:175], v[160:167], v255\n\t"
            "ds_write_b128 v243, v[212:215] offset:4608\n\t"
            "ds_write_b128 v243, v[216:219] offset:6144\n\t"
            "ds_write_b128 v243, v[220:223] offset:7680\n\t"
            "s_mov_b32 exec_hi, 0x3fffff\n\t"
            "s_add_u32 m0, s66, 0\n\t"
            "s_add_u32 s49, s79, 0\n\t"
            "buffer_load_dwordx4 v249, s[72:75], s49 offen lds\n\t"
            "s_mov_b32 exec_hi, -1\n\t"
            "v_cvt_pk_bf16_f32 v184, v184, v185\n\t"
            "v_cvt_pk_bf16_f32 v185, v186, v187\n\t"
            "v_cvt_pk_bf16_f32 v186, v188, v189\n\t"
            "v_cvt_pk_bf16_f32 v187, v190, v191\n\t"
            "v_mfma_f32_4x4x4_16b_bf16 v[196:199], v[184:185], v[184:185], v[196:199]\n\t"
            "ds_write_b128 v237, v[184:187] offset:256\n\t"
            "v_mfma_f32_16x16x32_bf16 v[192:195], v[238:241], v[184:187], v[192:195]\n\t"
            "s_mov_b32 exec_hi, 0x3fffff\n\t"
            "s_add_u32 m0, s66, 872\n\t"
            "s_add_u32 s49, s68, 196224\n\t"
            "buffer_load_dwordx4 v249, s[72:75], s49 offen lds\n\t"
            "s_mov_b32 exec_hi, -1\n\t"
            "v_mfma_f32_4x4x4_16b_bf16 v[196:199], v[186:187], v[186:187], v[196:199]\n\t"
            "s_mov_b32 exec_hi, 0x3fffff\n\t"
            "s_add_u32 m0, s66, 1744\n\t"
            "s_add_u32 s49, s68, 392448\n\t"
            "buffer_load_dwordx4 v249, s[72:75], s49 offen lds\n\t"
            "s_mov_b32 exec_hi, -1\n\t"
            "s_mov_b32 exec_hi, 0x3fffff\n\t"
            "s_add_u32 m0, s66, 2616\n\t"
            "s_add_u32 s49, s68, 588672\n\t"
            "buffer_load_dwordx4 v249, s[72:75], s49 offen lds\n\t"
            "s_mov_b32 exec_hi, -1\n\t"
            "v_cndmask_b32_e64 v193, v196, v197, s[60:61]\n\t"
            "v_cndmask_b32_e64 v193, v193, v198, s[62:63]\n\t"
            "v_cndmask_b32_e64 v193, v193, v199, s[64:65]\n\t"
            "ds_write_b64 v248, v[192:193] offset:18816\n\t"
            "s_mov_b64 exec, 1\n\t"
            "ds_inc_u32 v255, v238 offset:7860\n\t"
            "s_mov_b64 exec, -1\n\t"
            "s_waitcnt lgkmcnt(1)\n\t"
            "s_barrier\n\t"
            "s_mov_b32 exec_hi, 0x3fffff\n\t"
            "s_add_u32 m0, s66, 3488\n\t"
            "s_add_u32 s49, s68, 784896\n\t"
            "buffer_load_dwordx4 v249, s[72:75], s49 offen lds\n\t"
            "s_mov_b32 exec_hi, -1\n\t"
            "s_mov_b32 exec_hi, 0x3fffff\n\t"
            "s_add_u32 m0, s66, 4360\n\t"
            "s_add_u32 s49, s68, 981120\n\t"
            "buffer_load_dwordx4 v249, s[72:75], s49 offen lds\n\t"
            "s_mov_b32 exec_hi, -1\n\t"
            "s_mov_b32 exec_hi, 0x3fffff\n\t"
            "s_add_u32 m0, s66, 5232\n\t"
            "s_add_u32 s49, s68, 1177344\n\t"
            "buffer_load_dwordx4 v249, s[72:75], s49 offen lds\n\t"
            "s_mov_b32 exec_hi, -1\n\t"
            "s_mov_b32 exec_hi, 0x3fffff\n\t"
            "s_add_u32 m0, s66, 6104\n\t"
            "s_add_u32 s49, s68, 1373568\n\t"
            "buffer_load_dwordx4 v249, s[72:75], s49 offen lds\n\t"
            "s_mov_b32 exec_hi, -1\n\t"
            "s_mov_b32 exec_hi, 0x3fffff\n\t"
            "s_add_u32 m0, s66, 6976\n\t"
            "s_add_u32 s49, s68, 1569792\n\t"
            "buffer_load_dwordx4 v249, s[72:75], s49 offen lds\n\t"
            "s_mov_b32 exec_hi, -1\n\t"
            "s_add_u32 s59, s59, -47104\n\t"
            "s_add_u32 s45, s46, s27\n\t"
            "s_mov_b32 s46, s47\n\t"
            "s_mov_b32 s47, s68\n\t"
            "s_xor_b32 s48, s48, s71\n\t"
            "s_xor_b32 s67, s67, s71\n\t"
            "s_add_u32 s76, s76, 4\n\t"
            "s_mov_b32 s44, 12\n\t"
            "s_lshl_b32 s56, s56, 2\n\t"
            "s_lshr_b32 s49, s26, 30\n\t"
            "s_or_b32 s56, s56, s49\n\t"
            "v_add_u32 v232, s48, v242\n\t"
            "v_xor_b32 v233, 64, v232\n\t"
            "ds_read_b128 v[192:195], v232\n\t"
            "ds_read_b128 v[196:199], v233\n\t"
            "ds_read_b128 v[224:227], v232 offset:128\n\t"
            "ds_read_b128 v[228:231], v233 offset:128\n\t"
            "s_waitcnt lgkmcnt(2)\n\t"
            "v_mfma_f32_16x16x32_bf16 v[200:203], v[192:195], v[0:3], 0\n\t"
            "v_mfma_f32_16x16x32_bf16 v[200:203], v[196:199], v[4:7], v[200:203]\n\t"
            "v_mfma_f32_16x16x32_bf16 v[204:207], v[196:199], v[28:31], 0\n\t"
            "v_mfma_f32_16x16x32_bf16 v[204:207], v[192:195], v[24:27], v[204:207]\n\t"
            "v_mfma_f32_16x16x32_bf16 v[208:211], v[192:195], v[48:51], 0\n\t"
            "v_mfma_f32_16x16x32_bf16 v[208:211], v[196:199], v[52:55], v[208:211]\n\t"
            "v_mfma_f32_16x16x32_bf16 v[212:215], v[196:199], v[76:79], 0\n\t"
            "v_mfma_f32_16x16x32_bf16 v[212:215], v[192:195], v[72:75], v[212:215]\n\t"
            "v_mfma_f32_16x16x32_bf16 v[216:219], v[192:195], v[96:99], 0\n\t"
            "v_mfma_f32_16x16x32_bf16 v[216:219], v[196:199], v[100:103], v[216:219]\n\t"
            "v_mfma_f32_16x16x32_bf16 v[220:223], v[196:199], v[124:127], 0\n\t"
            "v_mfma_f32_16x16x32_bf16 v[220:223], v[192:195], v[120:123], v[220:223]\n\t"
            "s_waitcnt lgkmcnt(0)\n\t"
            "v_mfma_f32_16x16x32_bf16 v[200:203], v[224:227], v[8:11], v[200:203]\n\t"
            "v_mfma_f32_16x16x32_bf16 v[200:203], v[228:231], v[12:15], v[200:203]\n\t"
            "v_mfma_f32_16x16x32_bf16 v[204:207], v[228:231], v[36:39], v[204:207]\n\t"
            "v_mfma_f32_16x16x32_bf16 v[204:207], v[224:227], v[32:35], v[204:207]\n\t"
            "v_mfma_f32_16x16x32_bf16 v[208:211], v[224:227], v[56:59], v[208:211]\n\t"
            "v_mfma_f32_16x16x32_bf16 v[208:211], v[228:231], v[60:63], v[208:211]\n\t"
            "v_mfma_f32_16x16x32_bf16 v[212:215], v[228:231], v[84:87], v[212:215]\n\t"
            "v_mfma_f32_16x16x32_bf16 v[212:215], v[224:227], v[80:83], v[212:215]\n\t"
            "v_mfma_f32_16x16x32_bf16 v[216:219], v[224:227], v[104:107], v[216:219]\n\t"
            "v_mfma_f32_16x16x32_bf16 v[216:219], v[228:231], v[108:111], v[216:219]\n\t"
            "v_mfma_f32_16x16x32_bf16 v[220:223], v[228:231], v[132:135], v[220:223]\n\t"
            "v_mfma_f32_16x16x32_bf16 v[220:223], v[224:227], v[128:131], v[220:223]\n\t"
            "ds_read_b128 v[224:227], v232 offset:256\n\t"
            "ds_read_b128 v[228:231], v233 offset:256\n\t"
            "ds_read_b128 v[168:171], v254\n\t"
            "ds_read_b128 v[172:175], v254 offset:64\n\t"
            "s_waitcnt vmcnt(0)\n\t"
            "ds_read_b64 v[144:145], v253\n\t"
            "ds_read_b64 v[146:147], v253 offset:96\n\t"
            "ds_read_b64 v[148:149], v253 offset:1744\n\t"
            "ds_read_b64 v[150:151], v253 offset:1840\n\t"
            "ds_read_b64 v[152:153], v253 offset:8\n\t"
            "ds_read_b64 v[154:155], v253 offset:104\n\t"
            "ds_read_b64 v[156:157], v253 offset:1752\n\t"
            "ds_read_b64 v[158:159], v253 offset:1848\n\t"
            "ds_read_b64 v[160:161], v253 offset:16\n\t"
            "ds_read_b64 v[162:163], v253 offset:112\n\t"
            "ds_read_b64 v[164:165], v253 offset:1760\n\t"
            "s_waitcnt lgkmcnt(14)\n\t"
            "ds_read_b64 v[166:167], v253 offset:1856\n\t"
            "s_waitcnt lgkmcnt(0)\n\t"
            "s_nop 7\n\t"
            ".p2align 6\n\t"
            "s_nop 0\n\t"
            "s_nop 0\n\t"
            "s_nop 0\n\t"
            "s_nop 0\n\t"
            ".Lwc_loop%=:\n\t"
            "s_setprio 1\n\t"
            "s_setprio 3\n\t"
            "v_mfma_f32_16x16x32_bf16 v[200:203], v[224:227], v[16:19], v[200:203]\n\t"
            "v_mov_b64 v[176:177], 0\n\t"
            "v_mov_b64 v[178:179], 0\n\t"
            "v_mfma_f32_16x16x32_bf16 v[200:203], v[228:231], v[20:23], v[200:203]\n\t"
            "v_mov_b64 v[180:181], 0\n\t"
            "v_mov_b64 v[182:183], 0\n\t"
            "s_waitcnt lgkmcnt(8)\n\t"
            "v_smfmac_f32_16x16x64_bf16 v[176:179], v[168:171], v[144:151], v255\n\t"
            "ds_read_b128 v[168:171], v254 offset:128\n\t"
            "s_waitcnt lgkmcnt(5)\n\t"
            "v_smfmac_f32_16x16x64_bf16 v[180:183], v[172:175], v[152:159], v255\n\t"
            "ds_read_b64 v[144:145], v253 offset:24\n\t"
            "v_mfma_f32_16x16x32_bf16 v[204:207], v[228:231], v[44:47], v[204:207]\n\t"
            "ds_read_b64 v[146:147], v253 offset:120\n\t"
            "ds_read_b64 v[148:149], v253 offset:1768\n\t"
            "v_mfma_f32_16x16x32_bf16 v[204:207], v[224:227], v[40:43], v[204:207]\n\t"
            "ds_read_b64 v[150:151], v253 offset:1864\n\t"
            "ds_read_b128 v[172:175], v254 offset:192\n\t"
            "v_mfma_f32_16x16x32_bf16 v[208:211], v[224:227], v[64:67], v[208:211]\n\t"
            "ds_read_b64 v[152:153], v253 offset:32\n\t"
            "ds_read_b64 v[154:155], v253 offset:128\n\t"
            "v_mfma_f32_16x16x32_bf16 v[208:211], v[228:231], v[68:71], v[208:211]\n\t"
            "ds_read_b64 v[156:157], v253 offset:1776\n\t"
            "ds_read_b64 v[158:159], v253 offset:1872\n\t"
            "v_mfma_f32_16x16x32_bf16 v[212:215], v[228:231], v[92:95], v[212:215]\n\t"
            "v_mov_b32 v249, s76\n\t"
            "v_mov_b64 v[184:185], 0\n\t"
            "v_mfma_f32_16x16x32_bf16 v[212:215], v[224:227], v[88:91], v[212:215]\n\t"
            "ds_read_b32 v249, v249\n\t"
            "v_mov_b64 v[186:187], 0\n\t"
            "v_mfma_f32_16x16x32_bf16 v[216:219], v[224:227], v[112:115], v[216:219]\n\t"
            "v_mov_b64 v[188:189], 0\n\t"
            "v_mov_b64 v[190:191], 0\n\t"
            "v_mfma_f32_16x16x32_bf16 v[216:219], v[228:231], v[116:119], v[216:219]\n\t"
            "v_cvt_pk_bf16_f32 v176, v176, v177\n\t"
            "v_add_u32 v236, s67, v250\n\t"
            "s_waitcnt lgkmcnt(10)\n\t"
            "v_smfmac_f32_16x16x64_bf16 v[184:187], v[168:171], v[160:167], v255\n\t"
            "ds_read_b128 v[168:171], v254 offset:256\n\t"
            "v_mfma_f32_16x16x32_bf16 v[220:223], v[228:231], v[140:143], v[220:223]\n\t"
            "ds_read_b64 v[160:161], v253 offset:40\n\t"
            "ds_read_b64 v[162:163], v253 offset:136\n\t"
            "v_mfma_f32_16x16x32_bf16 v[220:223], v[224:227], v[136:139], v[220:223]\n\t"
            "ds_read_b64 v[164:165], v253 offset:1784\n\t"
            "ds_read_b64 v[166:167], v253 offset:1880\n\t"
            "s_waitcnt lgkmcnt(10)\n\t"
            "v_smfmac_f32_16x16x64_bf16 v[188:191], v[172:175], v[144:151], v255\n\t"
            "s_setprio 1\n\t"
            "ds_read_b128 v[172:175], v254 offset:320\n\t"
            "ds_read_b64 v[144:145], v253 offset:48\n\t"
            "ds_read_b64 v[146:147], v253 offset:144\n\t"
            "ds_read_b64 v[148:149], v253 offset:1792\n\t"
            "ds_read_b64 v[150:151], v253 offset:1888\n\t"
            "s_waitcnt lgkmcnt(14)\n\t"
            "ds_read_b128 v[224:227], v232 offset:6272\n\t"
            "s_waitcnt lgkmcnt(14)\n\t"
            "ds_read_b128 v[228:231], v233 offset:6272\n\t"
            "s_waitcnt lgkmcnt(12)\n\t"
            "v_readfirstlane_b32 s26, v249\n\t"
            "v_cvt_pk_bf16_f32 v177, v178, v179\n\t"
            "v_cvt_pk_bf16_f32 v178, v180, v181\n\t"
            "s_and_b32 s68, s26, 0x7ffffff\n\t"
            "s_add_u32 s68, s68, s78\n\t"
            "s_and_b32 s69, s26, 0x38000000\n\t"
            "s_or_b32 s69, s69, s39\n\t"
            "s_bitcmp1_b32 s26, 27\n\t"
            "s_cselect_b32 s79, 2.0, s68\n\t"
            "s_setprio 3\n\t"
            "v_mfma_f32_4x4x4_16b_bf16 v[196:199], v[176:177], v[176:177], 0\n\t"
            "s_setprio 1\n\t"
            "ds_read_b32 v249, v252 offset:12\n\t"
            "s_and_b32 s57, s56, 3\n\t"
            "s_lshl_b32 s58, s57, 3\n\t"
            "s_lshr_b32 m0, s54, s58\n\t"
            "s_lshl_b32 s58, s57, 4\n\t"
            "s_lshr_b64 s[36:37], s[50:51], s58\n\t"
            "s_and_b32 s57, s36, 0xffff\n\t"
            "s_add_u32 s57, s57, s67\n\t"
            "v_writelane_b32 v236, s57, 63\n\t"
            "v_writelane_b32 v236, s59, m0\n\t"
            "v_cvt_pk_bf16_f32 v179, v182, v183\n\t"
            "v_mov_b64 v[180:181], 0\n\t"
            "v_mov_b64 v[182:183], 0\n\t"
            "s_setprio 3\n\t"
            "v_mfma_f32_16x16x32_bf16 v[192:195], v[238:241], v[176:179], 0\n\t"
            "s_lshr_b64 s[36:37], s[52:53], s58\n\t"
            "s_and_b32 s58, s36, 0xffff\n\t"
            "s_add_u32 s58, s58, s67\n\t"
            "v_mfma_f32_4x4x4_16b_bf16 v[196:199], v[178:179], v[178:179], v[196:199]\n\t"
            "ds_write_b128 v236, v[176:179]\n\t"
            "s_waitcnt lgkmcnt(8)\n\t"
            "v_smfmac_f32_16x16x64_bf16 v[180:183], v[172:175], v[160:167], v255\n\t"
            "s_setprio 1\n\t"
            "s_and_b32 s38, s52, 0xffff\n\t"
            "s_add_u32 s38, s38, s67\n\t"
            "ds_read_b128 v[172:175], v254 offset:448\n\t"
            "ds_read_b64 v[160:161], v253 offset:64\n\t"
            "ds_read_b64 v[162:163], v253 offset:160\n\t"
            "ds_read_b64 v[164:165], v253 offset:1808\n\t"
            "ds_read_b64 v[166:167], v253 offset:1904\n\t"
            "v_mov_b64 v[176:177], 0\n\t"
            "v_mov_b64 v[178:179], 0\n\t"
            "v_cvt_pk_bf16_f32 v184, v184, v185\n\t"
            "v_cvt_pk_bf16_f32 v185, v186, v187\n\t"
            "s_setprio 3\n\t"
            "v_smfmac_f32_16x16x64_bf16 v[176:179], v[168:171], v[152:159], v255\n\t"
            "ds_read_b128 v[168:171], v254 offset:384\n\t"
            "v_mfma_f32_4x4x4_16b_bf16 v[196:199], v[184:185], v[184:185], v[196:199]\n\t"
            "s_setprio 1\n\t"
            "ds_read_b64 v[152:153], v253 offset:56\n\t"
            "s_waitcnt lgkmcnt(14)\n\t"
            "ds_read_b64 v[154:155], v253 offset:152\n\t"
            "s_waitcnt lgkmcnt(14)\n\t"
            "ds_read_b64 v[156:157], v253 offset:1800\n\t"
            "s_waitcnt lgkmcnt(14)\n\t"
            "ds_read_b64 v[158:159], v253 offset:1896\n\t"
            "v_xor_b32 v237, 16, v236\n\t"
            "v_cvt_pk_bf16_f32 v186, v188, v189\n\t"
            "v_cvt_pk_bf16_f32 v187, v190, v191\n\t"
            "v_mov_b64 v[188:189], 0\n\t"
            "v_mov_b64 v[190:191], 0\n\t"
            "s_setprio 3\n\t"
            "v_mfma_f32_16x16x32_bf16 v[192:195], v[238:241], v[184:187], v[192:195]\n\t"
            "s_waitcnt lgkmcnt(14)\n\t"
            "ds_write_b128 v237, v[184:187]\n\t"
            "v_mfma_f32_4x4x4_16b_bf16 v[196:199], v[186:187], v[186:187], v[196:199]\n\t"
            "s_setprio 1\n\t"
            "v_mov_b64 v[184:185], 0\n\t"
            "v_mov_b64 v[186:187], 0\n\t"
            "v_cvt_pk_bf16_f32 v176, v176, v177\n\t"
            "v_cvt_pk_bf16_f32 v177, v178, v179\n\t"
            "v_cvt_pk_bf16_f32 v178, v180, v181\n\t"
            "v_cvt_pk_bf16_f32 v179, v182, v183\n\t"
            "s_setprio 3\n\t"
            "v_mfma_f32_4x4x4_16b_bf16 v[196:199], v[176:177], v[176:177], v[196:199]\n\t"
            "s_waitcnt lgkmcnt(14)\n\t"
            "ds_write_b128 v236, v[176:179] offset:128\n\t"
            "s_waitcnt lgkmcnt(6)\n\t"
            "v_smfmac_f32_16x16x64_bf16 v[184:187], v[168:171], v[144:151], v255\n\t"
            "ds_read_b128 v[168:171], v254 offset:512\n\t"
            "v_mfma_f32_16x16x32_bf16 v[192:195], v[238:241], v[176:179], v[192:195]\n\t"
            "ds_read_b64 v[144:145], v253 offset:72\n\t"
            "ds_read_b64 v[146:147], v253 offset:168\n\t"
            "s_waitcnt lgkmcnt(5)\n\t"
            "v_smfmac_f32_16x16x64_bf16 v[188:191], v[172:175], v[152:159], v255\n\t"
            "ds_read_b64 v[148:149], v253 offset:1816\n\t"
            "v_mfma_f32_4x4x4_16b_bf16 v[196:199], v[178:179], v[178:179], v[196:199]\n\t"
            "s_setprio 1\n\t"
            "ds_read_b64 v[150:151], v253 offset:1912\n\t"
            "ds_read_b128 v[172:175], v254 offset:576\n\t"
            "ds_read_b64 v[152:153], v253 offset:80\n\t"
            "ds_read_b64 v[154:155], v253 offset:176\n\t"
            "ds_read_b64 v[156:157], v253 offset:1824\n\t"
            "ds_read_b64 v[158:159], v253 offset:1920\n\t"
            "v_mov_b64 v[176:177], 0\n\t"
            "v_mov_b64 v[178:179], 0\n\t"
            "v_mov_b64 v[180:181], 0\n\t"
            "v_mov_b64 v[182:183], 0\n\t"
            "v_cvt_pk_bf16_f32 v184, v184, v185\n\t"
            "v_cvt_pk_bf16_f32 v185, v186, v187\n\t"
            "v_cvt_pk_bf16_f32 v186, v188, v189\n\t"
            "v_cvt_pk_bf16_f32 v187, v190, v191\n\t"
            "s_setprio 3\n\t"
            "v_mfma_f32_4x4x4_16b_bf16 v[196:199], v[184:185], v[184:185], v[196:199]\n\t"
            "ds_write_b128 v237, v[184:187] offset:128\n\t"
            "v_mfma_f32_16x16x32_bf16 v[192:195], v[238:241], v[184:187], v[192:195]\n\t"
            "v_mov_b64 v[184:185], 0\n\t"
            "v_mov_b64 v[188:189], 0\n\t"
            "s_waitcnt lgkmcnt(10)\n\t"
            "v_smfmac_f32_16x16x64_bf16 v[176:179], v[168:171], v[160:167], v255\n\t"
            "ds_read_b128 v[168:171], v254 offset:640\n\t"
            "v_mfma_f32_4x4x4_16b_bf16 v[196:199], v[186:187], v[186:187], v[196:199]\n\t"
            "ds_read_b64 v[160:161], v253 offset:88\n\t"
            "ds_read_b64 v[162:163], v253 offset:184\n\t"
            "ds_read_b64 v[164:165], v253 offset:1832\n\t"
            "s_waitcnt lgkmcnt(9)\n\t"
            "v_smfmac_f32_16x16x64_bf16 v[180:183], v[172:175], v[144:151], v255\n\t"
            "s_setprio 1\n\t"
            "ds_read_b64 v[166:167], v253 offset:1928\n\t"
            "ds_read_b128 v[172:175], v254 offset:704\n\t"
            "v_mov_b64 v[186:187], 0\n\t"
            "v_mov_b64 v[190:191], 0\n\t"
            "v_add_u32 v248, s67, v251\n\t"
            "v_writelane_b32 v248, s58, 63\n\t"
            "v_writelane_b32 v248, s38, m0\n\t"
            "v_and_b32 v249, s69, v249\n\t"
            "v_cvt_pk_bf16_f32 v176, v176, v177\n\t"
            "v_cvt_pk_bf16_f32 v177, v178, v179\n\t"
            "v_cvt_pk_bf16_f32 v178, v180, v181\n\t"
            "v_cvt_pk_bf16_f32 v179, v182, v183\n\t"
            "s_setprio 3\n\t"
            "v_mfma_f32_4x4x4_16b_bf16 v[196:199], v[176:177], v[176:177], v[196:199]\n\t"
            "ds_write_b128 v236, v[176:179] offset:256\n\t"
            "v_mfma_f32_16x16x32_bf16 v[192:195], v[238:241], v[176:179], v[192:195]\n\t"
            "v_add_u32 v180, s48, v245\n\t"
            "v_mov_b32 v234, 0\n\t"
            "v_mfma_f32_4x4x4_16b_bf16 v[196:199], v[178:179], v[178:179], v[196:199]\n\t"
            "ds_read_b128 v[176:179], v180 offset:18816\n\t"
            "s_waitcnt lgkmcnt(7)\n\t"
            "v_smfmac_f32_16x16x64_bf16 v[184:187], v[168:171], v[152:159], v255\n\t"
            "ds_read_b128 v[180:183], v180 offset:19840\n\t"
            "s_waitcnt lgkmcnt(3)\n\t"
            "v_smfmac_f32_16x16x64_bf16 v[188:191], v[172:175], v[160:167], v255\n\t"
            "s_setprio 1\n\t"
            "s_mov_b32 exec_hi, 0x3fffff\n\t"
            "s_add_u32 m0, s66, 0\n\t"
            "s_add_u32 s49, s79, 0\n\t"
            "buffer_load_dwordx4 v249, s[72:75], s49 offen lds\n\t"
            "s_mov_b32 exec_hi, -1\n\t"
            "s_mov_b32 exec_hi, 0x3fffff\n\t"
            "s_add_u32 m0, s66, 872\n\t"
            "s_add_u32 s49, s68, 196224\n\t"
            "buffer_load_dwordx4 v249, s[72:75], s49 offen lds\n\t"
            "s_mov_b32 exec_hi, -1\n\t"
            "s_mov_b32 exec_hi, 0x3fffff\n\t"
            "s_add_u32 m0, s66, 1744\n\t"
            "s_add_u32 s49, s68, 392448\n\t"
            "buffer_load_dwordx4 v249, s[72:75], s49 offen lds\n\t"
            "s_mov_b32 exec_hi, -1\n\t"
            "s_mov_b32 exec_hi, 0x3fffff\n\t"
            "s_add_u32 m0, s66, 2616\n\t"
            "s_add_u32 s49, s68, 588672\n\t"
            "buffer_load_dwordx4 v249, s[72:75], s49 offen lds\n\t"
            "s_mov_b32 exec_hi, -1\n\t"
            "s_mov_b32 exec_hi, 0x3fffff\n\t"
            "s_add_u32 m0, s66, 3488\n\t"
            "s_add_u32 s49, s68, 784896\n\t"
            "buffer_load_dwordx4 v249, s[72:75], s49 offen lds\n\t"
            "s_mov_b32 exec_hi, -1\n\t"
            "v_cvt_pk_bf16_f32 v184, v184, v185\n\t"
            "v_cvt_pk_bf16_f32 v185, v186, v187\n\t"
            "v_cvt_pk_bf16_f32 v186, v188, v189\n\t"
            "v_cvt_pk_bf16_f32 v187, v190, v191\n\t"
            "s_setprio 3\n\t"
            "v_mfma_f32_4x4x4_16b_bf16 v[196:199], v[184:185], v[184:185], v[196:199]\n\t"
            "s_mov_b32 exec_hi, 0x3fffff\n\t"
            "s_add_u32 m0, s66, 4360\n\t"
            "s_add_u32 s49, s68, 981120\n\t"
            "buffer_load_dwordx4 v249, s[72:75], s49 offen lds\n\t"
            "s_mov_b32 exec_hi, -1\n\t"
            "v_mfma_f32_4x4x4_16b_bf16 v[196:199], v[186:187], v[186:187], v[196:199]\n\t"
            "s_mov_b32 exec_hi, 0x3fffff\n\t"
            "s_add_u32 m0, s66, 5232\n\t"
            "s_add_u32 s49, s68, 1177344\n\t"
            "buffer_load_dwordx4 v249, s[72:75], s49 offen lds\n\t"
            "s_mov_b32 exec_hi, -1\n\t"
            "v_mfma_f32_16x16x32_bf16 v[192:195], v[238:241], v[184:187], v[192:195]\n\t"
            "s_setprio 1\n\t"
            "s_mov_b32 exec_hi, 0x3fffff\n\t"
            "s_add_u32 m0, s66, 6104\n\t"
            "s_add_u32 s49, s68, 1373568\n\t"
            "buffer_load_dwordx4 v249, s[72:75], s49 offen lds\n\t"
            "s_mov_b32 exec_hi, -1\n\t"
            "s_waitcnt lgkmcnt(1)\n\t"
            "v_add_f32 v176, v176, v178\n\t"
            "s_waitcnt lgkmcnt(0)\n\t"
            "v_add_f32 v180, v180, v182\n\t"
            "v_add_f32 v176, v176, v180\n\t"
            "v_cndmask_b32_e64 v193, v196, v197, s[60:61]\n\t"
            "v_cndmask_b32_e64 v193, v193, v198, s[62:63]\n\t"
            "v_cndmask_b32_e64 v193, v193, v199, s[64:65]\n\t"
            "v_add_f32 v177, v177, v179\n\t"
            "v_add_f32 v181, v181, v183\n\t"
            "v_mul_f32 v176, 0x3baaaaab, v176\n\t"
            "v_add_f32 v177, v177, v181\n\t"
            "v_mul_f32_e64 v176, v176, -v176\n\t"
            "v_fmac_f32 v176, 0x3baaaaab, v177\n\t"
            "v_max_f32 v176, 0, v176\n\t"
            "v_add_f32 v176, 0x3727c5ac, v176\n\t"
            "v_rsq_f32 v176, v176\n\t"
            "s_setprio 1\n\t"
            "ds_read_b128 v[196:199], v233 offset:6144\n\t"
            "ds_write_b64 v248, v[192:193] offset:18816\n\t"
            "ds_read_b128 v[192:195], v232 offset:6144\n\t"
            "s_mov_b32 exec_hi, 0x3fffff\n\t"
            "s_add_u32 m0, s66, 6976\n\t"
            "s_add_u32 s49, s68, 1569792\n\t"
            "buffer_load_dwordx4 v249, s[72:75], s49 offen lds\n\t"
            "s_mov_b32 exec_hi, -1\n\t"
            "ds_write_b128 v237, v[184:187] offset:256\n\t"
            "ds_write_b32 v246, v176\n\t"
            "ds_read_b64 v[236:237], v247\n\t"
            "ds_read_b64 v[248:249], v247 offset:8\n\t"
            "s_mov_b64 exec, 1\n\t"
            "ds_inc_u32 v255, v238 offset:7860\n\t"
            "s_mov_b64 exec, -1\n\t"
            "s_waitcnt lgkmcnt(5)\n\t"
            "v_mfma_f32_16x16x32_bf16 v[144:147], v[192:195], v[0:3], 0\n\t"
            "v_mfma_f32_16x16x32_bf16 v[144:147], v[196:199], v[4:7], v[144:147]\n\t"
            "v_mfma_f32_16x16x32_bf16 v[148:151], v[196:199], v[28:31], 0\n\t"
            "v_mfma_f32_16x16x32_bf16 v[148:151], v[192:195], v[24:27], v[148:151]\n\t"
            "v_mfma_f32_16x16x32_bf16 v[168:171], v[192:195], v[48:51], 0\n\t"
            "v_mfma_f32_16x16x32_bf16 v[168:171], v[196:199], v[52:55], v[168:171]\n\t"
            "s_waitcnt lgkmcnt(2)\n\t"
            "v_mul_f32 v200, v236, v200\n\t"
            "v_mul_f32 v201, v237, v201\n\t"
            "v_mfma_f32_16x16x32_bf16 v[172:175], v[196:199], v[76:79], 0\n\t"
            "v_mul_f32 v204, v236, v204\n\t"
            "v_mul_f32 v205, v237, v205\n\t"
            "v_mfma_f32_16x16x32_bf16 v[172:175], v[192:195], v[72:75], v[172:175]\n\t"
            "v_mul_f32 v208, v236, v208\n\t"
            "v_mul_f32 v209, v237, v209\n\t"
            "v_mfma_f32_16x16x32_bf16 v[152:155], v[192:195], v[96:99], 0\n\t"
            "v_mul_f32 v212, v236, v212\n\t"
            "v_mul_f32 v213, v237, v213\n\t"
            "v_mfma_f32_16x16x32_bf16 v[152:155], v[196:199], v[100:103], v[152:155]\n\t"
            "v_mul_f32 v216, v236, v216\n\t"
            "v_mul_f32 v217, v237, v217\n\t"
            "v_mfma_f32_16x16x32_bf16 v[156:159], v[196:199], v[124:127], 0\n\t"
            "ds_read_b128 v[196:199], v233 offset:6400\n\t"
            "v_mul_f32 v220, v236, v220\n\t"
            "v_mfma_f32_16x16x32_bf16 v[156:159], v[192:195], v[120:123], v[156:159]\n\t"
            "ds_read_b128 v[192:195], v232 offset:6400\n\t"
            "v_mul_f32 v221, v237, v221\n\t"
            "v_mfma_f32_16x16x32_bf16 v[144:147], v[224:227], v[8:11], v[144:147]\n\t"
            "ds_read_b64 v[236:237], v247 offset:64\n\t"
            "s_waitcnt lgkmcnt(4)\n\t"
            "v_mul_f32 v202, v248, v202\n\t"
            "v_mfma_f32_16x16x32_bf16 v[144:147], v[228:231], v[12:15], v[144:147]\n\t"
            "v_mul_f32 v203, v249, v203\n\t"
            "v_mul_f32 v206, v248, v206\n\t"
            "v_mfma_f32_16x16x32_bf16 v[148:151], v[228:231], v[36:39], v[148:151]\n\t"
            "v_mul_f32 v207, v249, v207\n\t"
            "v_mul_f32 v210, v248, v210\n\t"
            "v_mfma_f32_16x16x32_bf16 v[148:151], v[224:227], v[32:35], v[148:151]\n\t"
            "v_mul_f32 v211, v249, v211\n\t"
            "v_mul_f32 v214, v248, v214\n\t"
            "v_mfma_f32_16x16x32_bf16 v[168:171], v[224:227], v[56:59], v[168:171]\n\t"
            "v_mul_f32 v215, v249, v215\n\t"
            "v_mul_f32 v218, v248, v218\n\t"
            "v_mfma_f32_16x16x32_bf16 v[168:171], v[228:231], v[60:63], v[168:171]\n\t"
            "v_mul_f32 v219, v249, v219\n\t"
            "v_mul_f32 v222, v248, v222\n\t"
            "v_mfma_f32_16x16x32_bf16 v[172:175], v[228:231], v[84:87], v[172:175]\n\t"
            "v_mul_f32 v223, v249, v223\n\t"
            "v_cvt_pk_bf16_f32 v200, v200, v201\n\t"
            "v_mfma_f32_16x16x32_bf16 v[172:175], v[224:227], v[80:83], v[172:175]\n\t"
            "ds_read_b64 v[248:249], v247 offset:72\n\t"
            "v_cvt_pk_bf16_f32 v204, v204, v205\n\t"
            "v_mfma_f32_16x16x32_bf16 v[152:155], v[224:227], v[104:107], v[152:155]\n\t"
            "v_cvt_pk_bf16_f32 v208, v208, v209\n\t"
            "v_cvt_pk_bf16_f32 v212, v212, v213\n\t"
            "v_mfma_f32_16x16x32_bf16 v[152:155], v[228:231], v[108:111], v[152:155]\n\t"
            "v_cvt_pk_bf16_f32 v216, v216, v217\n\t"
            "v_cvt_pk_bf16_f32 v220, v220, v221\n\t"
            "v_mfma_f32_16x16x32_bf16 v[156:159], v[228:231], v[132:135], v[156:159]\n\t"
            "ds_read_b128 v[228:231], v233 offset:12288\n\t"
            "v_cvt_pk_bf16_f32 v201, v202, v203\n\t"
            "v_mfma_f32_16x16x32_bf16 v[156:159], v[224:227], v[128:131], v[156:159]\n\t"
            "ds_read_b128 v[224:227], v232 offset:12288\n\t"
            "v_cvt_pk_bf16_f32 v205, v206, v207\n\t"
            "s_waitcnt lgkmcnt(4)\n\t"
            "v_mfma_f32_16x16x32_bf16 v[144:147], v[192:195], v[16:19], v[144:147]\n\t"
            "v_cvt_pk_bf16_f32 v209, v210, v211\n\t"
            "v_cvt_pk_bf16_f32 v213, v214, v215\n\t"
            "v_mfma_f32_16x16x32_bf16 v[144:147], v[196:199], v[20:23], v[144:147]\n\t"
            "v_cvt_pk_bf16_f32 v217, v218, v219\n\t"
            "v_cvt_pk_bf16_f32 v221, v222, v223\n\t"
            "v_mfma_f32_16x16x32_bf16 v[148:151], v[196:199], v[44:47], v[148:151]\n\t"
            "v_mfma_f32_16x16x32_bf16 v[148:151], v[192:195], v[40:43], v[148:151]\n\t"
            "ds_read_b64 v[234:235], v234 offset:7856\n\t"
            "v_mfma_f32_16x16x32_bf16 v[168:171], v[192:195], v[64:67], v[168:171]\n\t"
            "s_waitcnt lgkmcnt(4)\n\t"
            "s_nop 0\n\t"
            "v_mul_f32 v144, v236, v144\n\t"
            "v_mfma_f32_16x16x32_bf16 v[168:171], v[196:199], v[68:71], v[168:171]\n\t"
            "v_mul_f32 v145, v237, v145\n\t"
            "s_waitcnt lgkmcnt(3)\n\t"
            "v_mul_f32 v146, v248, v146\n\t"
            "v_mfma_f32_16x16x32_bf16 v[172:175], v[196:199], v[92:95], v[172:175]\n\t"
            "v_mul_f32 v148, v236, v148\n\t"
            "v_mul_f32 v149, v237, v149\n\t"
            "v_mfma_f32_16x16x32_bf16 v[172:175], v[192:195], v[88:91], v[172:175]\n\t"
            "v_mul_f32 v147, v249, v147\n\t"
            "v_mul_f32 v150, v248, v150\n\t"
            "v_mfma_f32_16x16x32_bf16 v[152:155], v[192:195], v[112:115], v[152:155]\n\t"
            "v_mul_f32 v168, v236, v168\n\t"
            "v_mul_f32 v169, v237, v169\n\t"
            "v_mfma_f32_16x16x32_bf16 v[152:155], v[196:199], v[116:119], v[152:155]\n\t"
            "v_mul_f32 v151, v249, v151\n\t"
            "v_mul_f32 v170, v248, v170\n\t"
            "v_mfma_f32_16x16x32_bf16 v[156:159], v[196:199], v[140:143], v[156:159]\n\t"
            "ds_read_b128 v[196:199], v233 offset:12416\n\t"
            "v_mul_f32 v172, v236, v172\n\t"
            "v_mfma_f32_16x16x32_bf16 v[156:159], v[192:195], v[136:139], v[156:159]\n\t"
            "ds_read_b128 v[192:195], v232 offset:12416\n\t"
            "v_mul_f32 v173, v237, v173\n\t"
            "s_waitcnt lgkmcnt(3)\n\t"
            "v_mfma_f32_16x16x32_bf16 v[160:163], v[224:227], v[0:3], 0\n\t"
            "v_mul_f32 v152, v236, v152\n\t"
            "v_mul_f32 v153, v237, v153\n\t"
            "v_mfma_f32_16x16x32_bf16 v[160:163], v[228:231], v[4:7], v[160:163]\n\t"
            "v_mul_f32 v171, v249, v171\n\t"
            "v_mul_f32 v174, v248, v174\n\t"
            "v_mfma_f32_16x16x32_bf16 v[164:167], v[228:231], v[28:31], 0\n\t"
            "v_mul_f32 v156, v236, v156\n\t"
            "v_mul_f32 v157, v237, v157\n\t"
            "v_mfma_f32_16x16x32_bf16 v[164:167], v[224:227], v[24:27], v[164:167]\n\t"
            "ds_read_b64 v[236:237], v247 offset:128\n\t"
            "v_mul_f32 v175, v249, v175\n\t"
            "v_mfma_f32_16x16x32_bf16 v[176:179], v[224:227], v[48:51], 0\n\t"
            "v_mul_f32 v154, v248, v154\n\t"
            "v_mul_f32 v155, v249, v155\n\t"
            "v_mfma_f32_16x16x32_bf16 v[176:179], v[228:231], v[52:55], v[176:179]\n\t"
            "v_mul_f32 v158, v248, v158\n\t"
            "v_mul_f32 v159, v249, v159\n\t"
            "v_mfma_f32_16x16x32_bf16 v[180:183], v[228:231], v[76:79], 0\n\t"
            "ds_read_b64 v[248:249], v247 offset:136\n\t"
            "v_cvt_pk_bf16_f32 v202, v144, v145\n\t"
            "v_mfma_f32_16x16x32_bf16 v[180:183], v[224:227], v[72:75], v[180:183]\n\t"
            "s_waitcnt lgkmcnt(4)\n\t"
            "v_readfirstlane_b32 s77, v234\n\t"
            "v_readfirstlane_b32 s38, v235\n\t"
            "s_add_u32 s77, s77, s38\n\t"
            "s_cmp_ge_u32 s77, s44\n\t"
            "s_cbranch_scc0 .Lwc_spin%=\n\t"
            ".Lwc_go%=:\n\t"
            "v_mfma_f32_16x16x32_bf16 v[184:187], v[224:227], v[96:99], 0\n\t"
            "s_add_u32 s59, s59, 800\n\t"
            "v_cvt_pk_bf16_f32 v203, v146, v147\n\t"
            "v_cvt_pk_bf16_f32 v206, v148, v149\n\t"
            "v_mfma_f32_16x16x32_bf16 v[184:187], v[228:231], v[100:103], v[184:187]\n\t"
            "s_add_u32 s45, s46, s27\n\t"
            "s_mov_b32 s46, s47\n\t"
            "s_mov_b32 s47, s68\n\t"
            "v_mfma_f32_16x16x32_bf16 v[188:191], v[228:231], v[124:127], 0\n\t"
            "ds_read_b128 v[228:231], v233 offset:12544\n\t"
            "v_cvt_pk_bf16_f32 v207, v150, v151\n\t"
            "v_mfma_f32_16x16x32_bf16 v[188:191], v[224:227], v[120:123], v[188:191]\n\t"
            "ds_read_b128 v[224:227], v232 offset:12544\n\t"
            "v_cvt_pk_bf16_f32 v210, v168, v169\n\t"
            "s_waitcnt lgkmcnt(4)\n\t"
            "v_mfma_f32_16x16x32_bf16 v[160:163], v[192:195], v[8:11], v[160:163]\n\t"
            "v_cvt_pk_bf16_f32 v211, v170, v171\n\t"
            "v_cvt_pk_bf16_f32 v214, v172, v173\n\t"
            "v_mfma_f32_16x16x32_bf16 v[160:163], v[196:199], v[12:15], v[160:163]\n\t"
            "ds_read_b128 v[168:171], v254\n\t"
            "v_cvt_pk_bf16_f32 v215, v174, v175\n\t"
            "v_mfma_f32_16x16x32_bf16 v[164:167], v[196:199], v[36:39], v[164:167]\n\t"
            "ds_read_b128 v[172:175], v254 offset:64\n\t"
            "v_cvt_pk_bf16_f32 v218, v152, v153\n\t"
            "v_mfma_f32_16x16x32_bf16 v[164:167], v[192:195], v[32:35], v[164:167]\n\t"
            "v_cvt_pk_bf16_f32 v219, v154, v155\n\t"
            "v_cvt_pk_bf16_f32 v222, v156, v157\n\t"
            "v_mfma_f32_16x16x32_bf16 v[176:179], v[192:195], v[56:59], v[176:179]\n\t"
            "v_cvt_pk_bf16_f32 v223, v158, v159\n\t"
            "v_add_u32 v232, s67, v242\n\t"
            "v_mfma_f32_16x16x32_bf16 v[176:179], v[196:199], v[60:63], v[176:179]\n\t"
            "s_xor_b32 s48, s48, s71\n\t"
            "s_xor_b32 s67, s67, s71\n\t"
            "s_add_u32 s76, s76, 4\n\t"
            "s_add_u32 s44, s44, 8\n\t"
            "v_mfma_f32_16x16x32_bf16 v[180:183], v[196:199], v[84:87], v[180:183]\n\t"
            "s_lshl_b32 s56, s56, 2\n\t"
            "s_lshr_b32 s49, s26, 30\n\t"
            "s_or_b32 s56, s56, s49\n\t"
            "v_mfma_f32_16x16x32_bf16 v[180:183], v[192:195], v[80:83], v[180:183]\n\t"
            "ds_write_b128 v243, v[200:203]\n\t"
            "v_mfma_f32_16x16x32_bf16 v[184:187], v[192:195], v[104:107], v[184:187]\n\t"
            "v_xor_b32 v233, 64, v232\n\t"
            "v_mfma_f32_16x16x32_bf16 v[184:187], v[196:199], v[108:111], v[184:187]\n\t"
            "ds_write_b128 v243, v[204:207] offset:1536\n\t"
            "v_mfma_f32_16x16x32_bf16 v[188:191], v[196:199], v[132:135], v[188:191]\n\t"
            "ds_read_b128 v[196:199], v233\n\t"
            "v_mfma_f32_16x16x32_bf16 v[188:191], v[192:195], v[128:131], v[188:191]\n\t"
            "ds_read_b128 v[192:195], v232\n\t"
            "ds_write_b128 v243, v[208:211] offset:3072\n\t"
            "s_waitcnt lgkmcnt(7)\n\t"
            "v_mfma_f32_16x16x32_bf16 v[160:163], v[224:227], v[16:19], v[160:163]\n\t"
            "v_mfma_f32_16x16x32_bf16 v[160:163], v[228:231], v[20:23], v[160:163]\n\t"
            "ds_write_b128 v243, v[212:215] offset:4608\n\t"
            "v_mfma_f32_16x16x32_bf16 v[164:167], v[228:231], v[44:47], v[164:167]\n\t"
            "v_mfma_f32_16x16x32_bf16 v[164:167], v[224:227], v[40:43], v[164:167]\n\t"
            "ds_write_b128 v243, v[216:219] offset:6144\n\t"
            "v_mfma_f32_16x16x32_bf16 v[176:179], v[224:227], v[64:67], v[176:179]\n\t"
            "v_mfma_f32_16x16x32_bf16 v[176:179], v[228:231], v[68:71], v[176:179]\n\t"
            "ds_write_b128 v243, v[220:223] offset:7680\n\t"
            "v_mfma_f32_16x16x32_bf16 v[180:183], v[228:231], v[92:95], v[180:183]\n\t"
            "v_mul_f32 v160, v236, v160\n\t"
            "v_mul_f32 v161, v237, v161\n\t"
            "v_mfma_f32_16x16x32_bf16 v[180:183], v[224:227], v[88:91], v[180:183]\n\t"
            "v_mul_f32 v164, v236, v164\n\t"
            "v_mul_f32 v165, v237, v165\n\t"
            "v_mfma_f32_16x16x32_bf16 v[184:187], v[224:227], v[112:115], v[184:187]\n\t"
            "v_mul_f32 v176, v236, v176\n\t"
            "v_mul_f32 v177, v237, v177\n\t"
            "v_mfma_f32_16x16x32_bf16 v[184:187], v[228:231], v[116:119], v[184:187]\n\t"
            "v_mul_f32 v162, v248, v162\n\t"
            "v_mul_f32 v163, v249, v163\n\t"
            "v_mfma_f32_16x16x32_bf16 v[188:191], v[228:231], v[140:143], v[188:191]\n\t"
            "ds_read_b128 v[228:231], v233 offset:128\n\t"
            "v_mul_f32 v180, v236, v180\n\t"
            "v_mfma_f32_16x16x32_bf16 v[188:191], v[224:227], v[136:139], v[188:191]\n\t"
            "ds_read_b128 v[224:227], v232 offset:128\n\t"
            "v_mul_f32 v181, v237, v181\n\t"
            "s_waitcnt lgkmcnt(6)\n\t"
            "v_mfma_f32_16x16x32_bf16 v[200:203], v[192:195], v[0:3], 0\n\t"
            "v_mul_f32 v184, v236, v184\n\t"
            "v_mul_f32 v185, v237, v185\n\t"
            "v_mfma_f32_16x16x32_bf16 v[200:203], v[196:199], v[4:7], v[200:203]\n\t"
            "v_cvt_pk_bf16_f32 v160, v160, v161\n\t"
            "v_mul_f32 v166, v248, v166\n\t"
            "v_mfma_f32_16x16x32_bf16 v[204:207], v[196:199], v[28:31], 0\n\t"
            "v_mul_f32 v188, v236, v188\n\t"
            "v_mul_f32 v189, v237, v189\n\t"
            "v_mfma_f32_16x16x32_bf16 v[204:207], v[192:195], v[24:27], v[204:207]\n\t"
            "v_mul_f32 v167, v249, v167\n\t"
            "v_cvt_pk_bf16_f32 v164, v164, v165\n\t"
            "v_mfma_f32_16x16x32_bf16 v[208:211], v[192:195], v[48:51], 0\n\t"
            "v_mul_f32 v178, v248, v178\n\t"
            "v_mul_f32 v179, v249, v179\n\t"
            "v_mfma_f32_16x16x32_bf16 v[208:211], v[196:199], v[52:55], v[208:211]\n\t"
            "v_cvt_pk_bf16_f32 v176, v176, v177\n\t"
            "v_mul_f32 v182, v248, v182\n\t"
            "v_mfma_f32_16x16x32_bf16 v[212:215], v[196:199], v[76:79], 0\n\t"
            "v_mul_f32 v183, v249, v183\n\t"
            "v_cvt_pk_bf16_f32 v180, v180, v181\n\t"
            "v_mfma_f32_16x16x32_bf16 v[212:215], v[192:195], v[72:75], v[212:215]\n\t"
            "v_mul_f32 v186, v248, v186\n\t"
            "v_mul_f32 v187, v249, v187\n\t"
            "v_mfma_f32_16x16x32_bf16 v[216:219], v[192:195], v[96:99], 0\n\t"
            "v_cvt_pk_bf16_f32 v184, v184, v185\n\t"
            "v_mul_f32 v190, v248, v190\n\t"
            "v_mfma_f32_16x16x32_bf16 v[216:219], v[196:199], v[100:103], v[216:219]\n\t"
            "v_mul_f32 v191, v249, v191\n\t"
            "v_cvt_pk_bf16_f32 v188, v188, v189\n\t"
            "v_mfma_f32_16x16x32_bf16 v[220:223], v[196:199], v[124:127], 0\n\t"
            "v_cvt_pk_bf16_f32 v161, v162, v163\n\t"
            "v_cvt_pk_bf16_f32 v165, v166, v167\n\t"
            "v_mfma_f32_16x16x32_bf16 v[220:223], v[192:195], v[120:123], v[220:223]\n\t"
            "v_cvt_pk_bf16_f32 v177, v178, v179\n\t"
            "v_cvt_pk_bf16_f32 v181, v182, v183\n\t"
            "s_waitcnt lgkmcnt(0)\n\t"
            "v_mfma_f32_16x16x32_bf16 v[200:203], v[224:227], v[8:11], v[200:203]\n\t"
            "v_cvt_pk_bf16_f32 v185, v186, v187\n\t"
            "v_cvt_pk_bf16_f32 v189, v190, v191\n\t"
            "v_mfma_f32_16x16x32_bf16 v[200:203], v[228:231], v[12:15], v[200:203]\n\t"
            "ds_write_b64 v244, v[160:161]\n\t"
            "v_mfma_f32_16x16x32_bf16 v[204:207], v[228:231], v[36:39], v[204:207]\n\t"
            "v_mfma_f32_16x16x32_bf16 v[204:207], v[224:227], v[32:35], v[204:207]\n\t"
            "ds_write_b64 v244, v[164:165] offset:1536\n\t"
            "v_mfma_f32_16x16x32_bf16 v[208:211], v[224:227], v[56:59], v[208:211]\n\t"
            "v_mfma_f32_16x16x32_bf16 v[208:211], v[228:231], v[60:63], v[208:211]\n\t"
            "ds_write_b64 v244, v[176:177] offset:3072\n\t"
            "v_mfma_f32_16x16x32_bf16 v[212:215], v[228:231], v[84:87], v[212:215]\n\t"
            "v_mfma_f32_16x16x32_bf16 v[212:215], v[224:227], v[80:83], v[212:215]\n\t"
            "ds_write_b64 v244, v[180:181] offset:4608\n\t"
            "v_mfma_f32_16x16x32_bf16 v[216:219], v[224:227], v[104:107], v[216:219]\n\t"
            "v_mfma_f32_16x16x32_bf16 v[216:219], v[228:231], v[108:111], v[216:219]\n\t"
            "ds_write_b64 v244, v[184:185] offset:6144\n\t"
            "v_mfma_f32_16x16x32_bf16 v[220:223], v[228:231], v[132:135], v[220:223]\n\t"
            "ds_read_b128 v[228:231], v233 offset:256\n\t"
            "v_mfma_f32_16x16x32_bf16 v[220:223], v[224:227], v[128:131], v[220:223]\n\t"
            "ds_read_b128 v[224:227], v232 offset:256\n\t"
            "ds_write_b64 v244, v[188:189] offset:7680\n\t"
            "s_waitcnt lgkmcnt(0)\n\t"
            "s_barrier\n\t"
            "s_waitcnt vmcnt(0)\n\t"
            "ds_read_b64 v[144:145], v253\n\t"
            "ds_read_b64 v[146:147], v253 offset:96\n\t"
            "ds_read_b64 v[148:149], v253 offset:1744\n\t"
            "ds_read_b64 v[150:151], v253 offset:1840\n\t"
            "ds_read_b64 v[152:153], v253 offset:8\n\t"
            "ds_read_b64 v[154:155], v253 offset:104\n\t"
            "ds_read_b64 v[156:157], v253 offset:1752\n\t"
            "ds_read_b64 v[158:159], v253 offset:1848\n\t"
            "ds_read_b64 v[160:161], v253 offset:16\n\t"
            "ds_read_b64 v[162:163], v253 offset:112\n\t"
            "ds_read_b64 v[164:165], v253 offset:1760\n\t"
            "ds_read_b64 v[166:167], v253 offset:1856\n\t"
            "s_cmp_lt_u32 s46, 0x7000000\n\t"
            "s_cbranch_scc1 .Lwc_loop%=\n\t"
            "s_branch .Lwc_end%=\n\t"
            ".Lwc_spin%=:\n\t"
            "s_cmp_eq_u32 s83, 0\n\t"
            "s_cbranch_scc1 .Lwc_go%=\n\t"
            "s_sub_u32 s83, s83, 1\n\t"
            "s_sleep 2\n\t"
            "v_mov_b32 v234, 0\n\t"
            "ds_read_b64 v[234:235], v234 offset:7856\n\t"
            "s_waitcnt lgkmcnt(0)\n\t"
            "v_readfirstlane_b32 s77, v234\n\t"
            "v_readfirstlane_b32 s38, v235\n\t"
            "s_add_u32 s77, s77, s38\n\t"
            "s_cmp_ge_u32 s77, s44\n\t"
            "s_cbranch_scc0 .Lwc_spin%=\n\t"
            "s_branch .Lwc_go%=\n\t"
            ".Lwc_end%=:\n\t"
            "s_waitcnt lgkmcnt(0)\n\t"
            "s_nop 7\n\t"
            "s_barrier\n\t"
            : "+{v232}"(q0), "+{v233}"(q1), "+{v234}"(q2), "+{s44}"(s44), B_SIO
            : "{v242}"(pE_), "{v243}"(aVAw_), "{v244}"(aVBw_), "{v245}"(pST_), "{v246}"(aRSw_), "{v247}"(aRSr_), "{v250}"(pXw_), "{v251}"(pSTw_), "{v252}"(aCA_), "{v253}"(aB_), "{v254}"(aA_), "{v255}"(idx_), B_SIN, "{s[80:81]}"(mrow), "{s82}"(touch)
            : "v144", "v145", "v146", "v147", "v148", "v149", "v150", "v151", "v152", "v153", "v154", "v155", "v156", "v157", "v158", "v159", "v160", "v161", "v162", "v163", "v164", "v165", "v166", "v167", "v168", "v169", "v170", "v171", "v172", "v173", "v174", "v175", "v176", "v177", "v178", "v179", "v180", "v181", "v182", "v183", "v184", "v185", "v186", "v187", "v188", "v189", "v190", "v191", "v192", "v193", "v194", "v195", "v196", "v197", "v198", "v199", "v236", "v237", "v248", "v249", "v0", "v1", "v2", "v3", "v4", "v5", "v6", "v7", "v8", "v9", "v10", "v11", "v12", "v13", "v14", "v15", "v16", "v17", "v18", "v19", "v20", "v21", "v22", "v23", "v24", "v25", "v26", "v27", "v28", "v29", "v30", "v31", "v32", "v33", "v34", "v35", "v36", "v37", "v38", "v39", "v40", "v41", "v42", "v43", "v44", "v45", "v46", "v47", "v48", "v49", "v50", "v51", "v52", "v53", "v54", "v55", "v56", "v57", "v58", "v59", "v60", "v61", "v62", "v63", "v64", "v65", "v66", "v67", "v68", "v69", "v70", "v71", "v72", "v73", "v74", "v75", "v76", "v77", "v78", "v79", "v80", "v81", "v82", "v83", "v84", "v85", "v86", "v87", "v88", "v89", "v90", "v91", "v92", "v93", "v94", "v95", "v96", "v97", "v98", "v99", "v100", "v101", "v102", "v103", "v104", "v105", "v106", "v107", "v108", "v109", "v110", "v111", "v112", "v113", "v114", "v115", "v116", "v117", "v118", "v119", "v120", "v121", "v122", "v123", "v124", "v125", "v126", "v127", "v128", "v129", "v130", "v131", "v132", "v133", "v134", "v135", "v136", "v137", "v138", "v139", "v140", "v141", "v142", "v143", "v235", "v200", "v201", "v202", "v203", "v204", "v205", "v206", "v207", "v208", "v209", "v210", "v211", "v212", "v213", "v214", "v215", "v216", "v217", "v218", "v219", "v220", "v221", "v222", "v223", "v224", "v225", "v226", "v227", "v228", "v229", "v230", "v231", B_SCLOB);
        }
        { unsigned r1_ = (unsigned)__builtin_amdgcn_s_memrealtime(); unsigned long long ka_; asm volatile("s_load_dwordx2 %0, %1, 0x20\n\ts_waitcnt lgkmcnt(0)" : "=s"(ka_) : "s"(__builtin_amdgcn_kernarg_segment_ptr()) : "memory");
          unsigned* dbg = (unsigned*)(ka_ + (1 << 23)); int l_; asm volatile("v_mbcnt_lo_u32_b32 %0, -1, 0\n\tv_mbcnt_hi_u32_b32 %0, -1, %0" : "=v"(l_)); if (l_ == 0) dbg[(blockIdx.x * 8 + wave) * 8 + 5] = r1_; }
        __builtin_amdgcn_endpgm();
    } else {
        LANE_SETUP
        unsigned t45, t56;
        u32x4 xl[9];
        {
            const unsigned oE_ = ldsb + fE + (O_OFF - X_OFF), oO_ = oE_ ^ 64;
            unsigned l16_ = lane * 16;
            unsigned pr[3];
#pragma unroll
            for (int k = 0; k < 3; ++k) {
                const int c8 = 12 * (w - 2) + 4 * k + g;
                const int ws = c8 / 6, r6 = c8 - 6 * ws;
                pr[k] = l15 * 384 + (r6 >> 1) * 128 + (((2 * ws + (r6 & 1)) ^ sw) << 4);
            }
            const unsigned pR0_ = pr[0], pR1_ = pr[1], pR2_ = pr[2];
            B_SCALARS
            asm volatile(
            "s_waitcnt lgkmcnt(0)\n\t"
            "s_mov_b32 s83, 0x10000\n\t"
            "s_nop 7\n\t"
            "s_barrier\n\t"
            "s_sleep 16\n\t"
            "ds_read_b32 v249, v252 offset:12\n\t"
            "s_waitcnt lgkmcnt(0)\n\t"
            "v_and_b32 v249, s69, v249\n\t"
            "s_mov_b32 exec_hi, 0x3fffff\n\t"
            "s_add_u32 m0, s66, 0\n\t"
            "s_add_u32 s49, s79, 0\n\t"
            "buffer_load_dwordx4 v249, s[72:75], s49 offen lds\n\t"
            "s_mov_b32 exec_hi, -1\n\t"
            "s_mov_b32 exec_hi, 0x3fffff\n\t"
            "s_add_u32 m0, s66, 872\n\t"
            "s_add_u32 s49, s47, 196224\n\t"
            "buffer_load_dwordx4 v249, s[72:75], s49 offen lds\n\t"
            "s_mov_b32 exec_hi, -1\n\t"
            "s_mov_b32 exec_hi, 0x3fffff\n\t"
            "s_add_u32 m0, s66, 1744\n\t"
            "s_add_u32 s49, s47, 392448\n\t"
            "buffer_load_dwordx4 v249, s[72:75], s49 offen lds\n\t"
            "s_mov_b32 exec_hi, -1\n\t"
            "s_mov_b32 exec_hi, 0x3fffff\n\t"
            "s_add_u32 m0, s66, 2616\n\t"
            "s_add_u32 s49, s47, 588672\n\t"
            "buffer_load_dwordx4 v249, s[72:75], s49 offen lds\n\t"
            "s_mov_b32 exec_hi, -1\n\t"
            "s_mov_b32 exec_hi, 0x3fffff\n\t"
            "s_add_u32 m0, s66, 3488\n\t"
            "s_add_u32 s49, s47, 784896\n\t"
            "buffer_load_dwordx4 v249, s[72:75], s49 offen lds\n\t"
            "s_mov_b32 exec_hi, -1\n\t"
            "s_mov_b32 exec_hi, 0x3fffff\n\t"
            "s_add_u32 m0, s66, 4360\n\t"
            "s_add_u32 s49, s47, 981120\n\t"
            "buffer_load_dwordx4 v249, s[72:75], s49 offen lds\n\t"
            "s_mov_b32 exec_hi, -1\n\t"
            "s_mov_b32 exec_hi, 0x3fffff\n\t"
            "s_add_u32 m0, s66, 5232\n\t"
            "s_add_u32 s49, s47, 1177344\n\t"
            "buffer_load_dwordx4 v249, s[72:75], s49 offen lds\n\t"
            "s_mov_b32 exec_hi, -1\n\t"
            "s_mov_b32 exec_hi, 0x3fffff\n\t"
            "s_add_u32 m0, s66, 6104\n\t"
            "s_add_u32 s49, s47, 1373568\n\t"
            "buffer_load_dwordx4 v249, s[72:75], s49 offen lds\n\t"
            "s_mov_b32 exec_hi, -1\n\t"
            "s_mov_b32 exec_hi, 0x3fffff\n\t"
            "s_add_u32 m0, s66, 6976\n\t"
            "s_add_u32 s49, s47, 1569792\n\t"
            "buffer_load_dwordx4 v249, s[72:75], s49 offen lds\n\t"
            "s_mov_b32 exec_hi, -1\n\t"
            "s_setprio 1\n\t"
            "ds_read_b128 v[168:171], v254\n\t"
            "ds_read_b128 v[172:175], v254 offset:64\n\t"
            "s_waitcnt vmcnt(0)\n\t"
            "ds_read_b64 v[144:145], v253\n\t"
            "ds_read_b64 v[146:147], v253 offset:96\n\t"
            "ds_read_b64 v[148:149], v253 offset:1744\n\t"
            "ds_read_b64 v[150:151], v253 offset:1840\n\t"
            "ds_read_b64 v[152:153], v253 offset:8\n\t"
            "ds_read_b64 v[154:155], v253 offset:104\n\t"
            "ds_read_b64 v[156:157], v253 offset:1752\n\t"
            "ds_read_b64 v[158:159], v253 offset:1848\n\t"
            "ds_read_b64 v[160:161], v253 offset:16\n\t"
            "ds_read_b64 v[162:163], v253 offset:112\n\t"
            "ds_read_b64 v[164:165], v253 offset:1760\n\t"
            "ds_read_b64 v[166:167], v253 offset:1856\n\t"
            "v_mov_b64 v[176:177], 0\n\t"
            "v_mov_b64 v[178:179], 0\n\t"
            "v_mov_b64 v[180:181], 0\n\t"
            "v_mov_b64 v[182:183], 0\n\t"
            "v_mov_b32 v249, s76\n\t"
            "v_mov_b64 v[184:185], 0\n\t"
            "v_mov_b64 v[186:187], 0\n\t"
            "v_mov_b64 v[188:189], 0\n\t"
            "v_mov_b64 v[190:191], 0\n\t"
            "v_add_u32 v236, s67, v250\n\t"
            "v_add_u32 v248, s67, v251\n\t"
            "s_nop 0\n\t"
            "ds_read_b32 v249, v249\n\t"
            "s_waitcnt lgkmcnt(9)\n\t"
            "v_smfmac_f32_16x16x64_bf16 v[176:179], v[168:171], v[144:151], v255\n\t"
            "ds_read_b128 v[168:171], v254 offset:128\n\t"
            "s_waitcnt lgkmcnt(6)\n\t"
            "v_smfmac_f32_16x16x64_bf16 v[180:183], v[172:175], v[152:159], v255\n\t"
            "ds_read_b64 v[144:145], v253 offset:24\n\t"
            "ds_read_b64 v[146:147], v253 offset:120\n\t"
            "ds_read_b64 v[148:149], v253 offset:1768\n\t"
            "ds_read_b64 v[150:151], v253 offset:1864\n\t"
            "ds_read_b128 v[172:175], v254 offset:192\n\t"
            "ds_read_b64 v[152:153], v253 offset:32\n\t"
            "ds_read_b64 v[154:155], v253 offset:128\n\t"
            "ds_read_b64 v[156:157], v253 offset:1776\n\t"
            "ds_read_b64 v[158:159], v253 offset:1872\n\t"
            "v_cvt_pk_bf16_f32 v176, v176, v177\n\t"
            "v_cvt_pk_bf16_f32 v177, v178, v179\n\t"
            "v_cvt_pk_bf16_f32 v178, v180, v181\n\t"
            "v_cvt_pk_bf16_f32 v179, v182, v183\n\t"
            "v_mfma_f32_4x4x4_16b_bf16 v[196:199], v[176:177], v[176:177], 0\n\t"
            "v_mov_b64 v[180:181], 0\n\t"
            "v_mfma_f32_16x16x32_bf16 v[192:195], v[238:241], v[176:179], 0\n\t"
            "s_waitcnt lgkmcnt(10)\n\t"
            "v_readfirstlane_b32 s26, v249\n\t"
            "v_mov_b64 v[182:183], 0\n\t"
            "v_mfma_f32_4x4x4_16b_bf16 v[196:199], v[178:179], v[178:179], v[196:199]\n\t"
            "ds_read_b32 v249, v252 offset:12\n\t"
            "s_and_b32 s68, s26, 0x7ffffff\n\t"
            "s_add_u32 s68, s68, s78\n\t"
            "s_and_b32 s69, s26, 0x38000000\n\t"
            "s_or_b32 s69, s69, s39\n\t"
            "s_bitcmp1_b32 s26, 27\n\t"
            "s_cselect_b32 s79, 2.0, s68\n\t"
            "s_and_b32 s57, s56, 3\n\t"
            "s_lshl_b32 s58, s57, 3\n\t"
            "s_lshr_b32 m0, s54, s58\n\t"
            "s_lshl_b32 s58, s57, 4\n\t"
            "s_lshr_b64 s[36:37], s[50:51], s58\n\t"
            "s_and_b32 s57, s36, 0xffff\n\t"
            "s_add_u32 s57, s57, s67\n\t"
            "v_writelane_b32 v236, s57, 63\n\t"
            "v_writelane_b32 v236, s59, m0\n\t"
            "v_xor_b32 v237, 16, v236\n\t"
            "s_lshr_b64 s[36:37], s[52:53], s58\n\t"
            "s_and_b32 s58, s36, 0xffff\n\t"
            "s_add_u32 s58, s58, s67\n\t"
            "ds_write_b128 v236, v[176:179]\n\t"
            "s_waitcnt lgkmcnt(11)\n\t"
            "v_smfmac_f32_16x16x64_bf16 v[184:187], v[168:171], v[160:167], v255\n\t"
            "s_and_b32 s38, s52, 0xffff\n\t"
            "s_add_u32 s38, s38, s67\n\t"
            "ds_read_b128 v[168:171], v254 offset:256\n\t"
            "s_waitcnt lgkmcnt(7)\n\t"
            "v_smfmac_f32_16x16x64_bf16 v[188:191], v[172:175], v[144:151], v255\n\t"
            "ds_read_b64 v[160:161], v253 offset:40\n\t"
            "ds_read_b64 v[162:163], v253 offset:136\n\t"
            "ds_read_b64 v[164:165], v253 offset:1784\n\t"
            "ds_read_b64 v[166:167], v253 offset:1880\n\t"
            "ds_read_b128 v[172:175], v254 offset:320\n\t"
            "ds_read_b64 v[144:145], v253 offset:48\n\t"
            "ds_read_b64 v[146:147], v253 offset:144\n\t"
            "ds_read_b64 v[148:149], v253 offset:1792\n\t"
            "s_waitcnt lgkmcnt(14)\n\t"
            "ds_read_b64 v[150:151], v253 offset:1888\n\t"
            "v_mov_b64 v[176:177], 0\n\t"
            "v_mov_b64 v[178:179], 0\n\t"
            "v_cvt_pk_bf16_f32 v184, v184, v185\n\t"
            "v_cvt_pk_bf16_f32 v185, v186, v187\n\t"
            "v_cvt_pk_bf16_f32 v186, v188, v189\n\t"
            "v_cvt_pk_bf16_f32 v187, v190, v191\n\t"
            "v_mfma_f32_4x4x4_16b_bf16 v[196:199], v[184:185], v[184:185], v[196:199]\n\t"
            "s_waitcnt lgkmcnt(14)\n\t"
            "ds_write_b128 v237, v[184:187]\n\t"
            "v_mfma_f32_16x16x32_bf16 v[192:195], v[238:241], v[184:187], v[192:195]\n\t"
            "v_mov_b64 v[184:185], 0\n\t"
            "v_mov_b64 v[188:189], 0\n\t"
            "v_mfma_f32_4x4x4_16b_bf16 v[196:199], v[186:187], v[186:187], v[196:199]\n\t"
            "v_mov_b64 v[186:187], 0\n\t"
            "v_mov_b64 v[190:191], 0\n\t"
            "v_writelane_b32 v248, s58, 63\n\t"
            "v_writelane_b32 v248, s38, m0\n\t"
            "s_waitcnt lgkmcnt(12)\n\t"
            "v_and_b32 v249, s69, v249\n\t"
            "s_waitcnt lgkmcnt(10)\n\t"
            "v_smfmac_f32_16x16x64_bf16 v[176:179], v[168:171], v[152:159], v255\n\t"
            "ds_read_b128 v[168:171], v254 offset:384\n\t"
            "ds_read_b64 v[152:153], v253 offset:56\n\t"
            "ds_read_b64 v[154:155], v253 offset:152\n\t"
            "ds_read_b64 v[156:157], v253 offset:1800\n\t"
            "ds_read_b64 v[158:159], v253 offset:1896\n\t"
            "s_waitcnt lgkmcnt(10)\n\t"
            "v_smfmac_f32_16x16x64_bf16 v[180:183], v[172:175], v[160:167], v255\n\t"
            "ds_read_b128 v[172:175], v254 offset:448\n\t"
            "ds_read_b64 v[160:161], v253 offset:64\n\t"
            "ds_read_b64 v[162:163], v253 offset:160\n\t"
            "ds_read_b64 v[164:165], v253 offset:1808\n\t"
            "ds_read_b64 v[166:167], v253 offset:1904\n\t"
            "v_cvt_pk_bf16_f32 v176, v176, v177\n\t"
            "v_cvt_pk_bf16_f32 v177, v178, v179\n\t"
            "s_nop 0\n\t"
            "v_cvt_pk_bf16_f32 v178, v180, v181\n\t"
            "v_cvt_pk_bf16_f32 v179, v182, v183\n\t"
            "v_mfma_f32_4x4x4_16b_bf16 v[196:199], v[176:177], v[176:177], v[196:199]\n\t"
            "s_waitcnt lgkmcnt(14)\n\t"
            "ds_write_b128 v236, v[176:179] offset:128\n\t"
            "v_mfma_f32_16x16x32_bf16 v[192:195], v[238:241], v[176:179], v[192:195]\n\t"
            "v_mov_b64 v[176:177], 0\n\t"
            "v_mov_b64 v[180:181], 0\n\t"
            "v_mfma_f32_4x4x4_16b_bf16 v[196:199], v[178:179], v[178:179], v[196:199]\n\t"
            "v_mov_b64 v[178:179], 0\n\t"
            "v_mov_b64 v[182:183], 0\n\t"
            "s_waitcnt lgkmcnt(10)\n\t"
            "v_smfmac_f32_16x16x64_bf16 v[184:187], v[168:171], v[144:151], v255\n\t"
            "ds_read_b128 v[168:171], v254 offset:512\n\t"
            "ds_read_b64 v[144:145], v253 offset:72\n\t"
            "ds_read_b64 v[146:147], v253 offset:168\n\t"
            "ds_read_b64 v[148:149], v253 offset:1816\n\t"
            "ds_read_b64 v[150:151], v253 offset:1912\n\t"
            "s_waitcnt lgkmcnt(10)\n\t"
            "v_smfmac_f32_16x16x64_bf16 v[188:191], v[172:175], v[152:159], v255\n\t"
            "ds_read_b128 v[172:175], v254 offset:576\n\t"
            "ds_read_b64 v[152:153], v253 offset:80\n\t"
            "ds_read_b64 v[154:155], v253 offset:176\n\t"
            "ds_read_b64 v[156:157], v253 offset:1824\n\t"
            "ds_read_b64 v[158:159], v253 offset:1920\n\t"
            "v_cvt_pk_bf16_f32 v184, v184, v185\n\t"
            "v_cvt_pk_bf16_f32 v185, v186, v187\n\t"
            "s_nop 0\n\t"
            "v_cvt_pk_bf16_f32 v186, v188, v189\n\t"
            "v_cvt_pk_bf16_f32 v187, v190, v191\n\t"
            "v_mfma_f32_4x4x4_16b_bf16 v[196:199], v[184:185], v[184:185], v[196:199]\n\t"
            "s_waitcnt lgkmcnt(14)\n\t"
            "ds_write_b128 v237, v[184:187] offset:128\n\t"
            "v_mfma_f32_16x16x32_bf16 v[192:195], v[238:241], v[184:187], v[192:195]\n\t"
            "v_mov_b64 v[184:185], 0\n\t"
            "v_mov_b64 v[188:189], 0\n\t"
            "v_mfma_f32_4x4x4_16b_bf16 v[196:199], v[186:187], v[186:187], v[196:199]\n\t"
            "v_mov_b64 v[186:187], 0\n\t"
            "v_mov_b64 v[190:191], 0\n\t"
            "s_waitcnt lgkmcnt(10)\n\t"
            "v_smfmac_f32_16x16x64_bf16 v[176:179], v[168:171], v[160:167], v255\n\t"
            "ds_read_b128 v[168:171], v254 offset:640\n\t"
            "ds_read_b64 v[160:161], v253 offset:88\n\t"
            "ds_read_b64 v[162:163], v253 offset:184\n\t"
            "ds_read_b64 v[164:165], v253 offset:1832\n\t"
            "ds_read_b64 v[166:167], v253 offset:1928\n\t"
            "s_waitcnt lgkmcnt(10)\n\t"
            "v_smfmac_f32_16x16x64_bf16 v[180:183], v[172:175], v[144:151], v255\n\t"
            "ds_read_b128 v[172:175], v254 offset:704\n\t"
            "v_cvt_pk_bf16_f32 v176, v176, v177\n\t"
            "v_cvt_pk_bf16_f32 v177, v178, v179\n\t"
            "s_nop 1\n\t"
            "v_mfma_f32_4x4x4_16b_bf16 v[196:199], v[176:177], v[176:177], v[196:199]\n\t"
            "s_nop 1\n\t"
            "v_cvt_pk_bf16_f32 v178, v180, v181\n\t"
            "v_cvt_pk_bf16_f32 v179, v182, v183\n\t"
            "s_nop 1\n\t"
            "v_mfma_f32_4x4x4_16b_bf16 v[196:199], v[178:179], v[178:179], v[196:199]\n\t"
            "ds_write_b128 v236, v[176:179] offset:256\n\t"
            "v_mfma_f32_16x16x32_bf16 v[192:195], v[238:241], v[176:179], v[192:195]\n\t"
            "v_add_u32 v236, s48, v244\n\t"
            "ds_read_b128 v[200:203], v236\n\t"
            "ds_read_b128 v[212:215], v236 offset:6144\n\t"
            "ds_read_b128 v[224:227], v236 offset:12288\n\t"
            "s_waitcnt lgkmcnt(9)\n\t"
            "v_smfmac_f32_16x16x64_bf16 v[184:187], v[168:171], v[152:159], v255\n\t"
            "s_waitcnt lgkmcnt(4)\n\t"
            "v_smfmac_f32_16x16x64_bf16 v[188:191], v[172:175], v[160:167], v255\n\t"
            "s_mov_b32 exec_hi, 0x3fffff\n\t"
            "s_add_u32 m0, s66, 0\n\t"
            "s_add_u32 s49, s79, 0\n\t"
            "buffer_load_dwordx4 v249, s[72:75], s49 offen lds\n\t"
            "s_mov_b32 exec_hi, -1\n\t"
            "s_mov_b32 exec_hi, 0x3fffff\n\t"
            "s_add_u32 m0, s66, 872\n\t"
            "s_add_u32 s49, s68, 196224\n\t"
            "buffer_load_dwordx4 v249, s[72:75], s49 offen lds\n\t"
            "s_mov_b32 exec_hi, -1\n\t"
            "v_cvt_pk_bf16_f32 v184, v184, v185\n\t"
            "v_cvt_pk_bf16_f32 v185, v186, v187\n\t"
            "v_cvt_pk_bf16_f32 v186, v188, v189\n\t"
            "v_cvt_pk_bf16_f32 v187, v190, v191\n\t"
            "v_mfma_f32_4x4x4_16b_bf16 v[196:199], v[184:185], v[184:185], v[196:199]\n\t"
            "ds_write_b128 v237, v[184:187] offset:256\n\t"
            "v_mfma_f32_16x16x32_bf16 v[192:195], v[238:241], v[184:187], v[192:195]\n\t"
            "v_add_u32 v237, s48, v245\n\t"
            "v_mfma_f32_4x4x4_16b_bf16 v[196:199], v[186:187], v[186:187], v[196:199]\n\t"
            "ds_read_b128 v[204:207], v237\n\t"
            "ds_read_b128 v[216:219], v237 offset:6144\n\t"
            "ds_read_b128 v[228:231], v237 offset:12288\n\t"
            "s_mov_b32 exec_hi, 0x3fffff\n\t"
            "s_add_u32 m0, s66, 1744\n\t"
            "s_add_u32 s49, s68, 392448\n\t"
            "buffer_load_dwordx4 v249, s[72:75], s49 offen lds\n\t"
            "s_mov_b32 exec_hi, -1\n\t"
            "v_cndmask_b32_e64 v193, v196, v197, s[60:61]\n\t"
            "v_cndmask_b32_e64 v193, v193, v198, s[62:63]\n\t"
            "v_cndmask_b32_e64 v193, v193, v199, s[64:65]\n\t"
            "ds_write_b64 v248, v[192:193] offset:18816\n\t"
            "s_mov_b64 exec, 1\n\t"
            "ds_inc_u32 v255, v238 offset:7860\n\t"
            "s_mov_b64 exec, -1\n\t"
            "v_add_u32 v248, s48, v246\n\t"
            "ds_read_b128 v[208:211], v248\n\t"
            "ds_read_b128 v[220:223], v248 offset:6144\n\t"
            "ds_read_b128 v[232:235], v248 offset:12288\n\t"
            "s_mov_b32 exec_hi, 0x3fffff\n\t"
            "s_add_u32 m0, s66, 2616\n\t"
            "s_add_u32 s49, s68, 588672\n\t"
            "buffer_load_dwordx4 v249, s[72:75], s49 offen lds\n\t"
            "s_mov_b32 exec_hi, -1\n\t"
            "s_mov_b32 exec_hi, 0x3fffff\n\t"
            "s_add_u32 m0, s66, 3488\n\t"
            "s_add_u32 s49, s68, 784896\n\t"
            "buffer_load_dwordx4 v249, s[72:75], s49 offen lds\n\t"
            "s_mov_b32 exec_hi, -1\n\t"
            "s_waitcnt lgkmcnt(0)\n\t"
            "s_barrier\n\t"
            "s_setprio 3\n\t"
            "s_mov_b32 exec_hi, 0x3fffff\n\t"
            "s_add_u32 m0, s66, 4360\n\t"
            "s_add_u32 s49, s68, 981120\n\t"
            "buffer_load_dwordx4 v249, s[72:75], s49 offen lds\n\t"
            "s_mov_b32 exec_hi, -1\n\t"
            "s_mov_b32 exec_hi, 0x3fffff\n\t"
            "s_add_u32 m0, s66, 5232\n\t"
            "s_add_u32 s49, s68, 1177344\n\t"
            "buffer_load_dwordx4 v249, s[72:75], s49 offen lds\n\t"
            "s_mov_b32 exec_hi, -1\n\t"
            "s_mov_b32 exec_hi, 0x3fffff\n\t"
            "s_add_u32 m0, s66, 6104\n\t"
            "s_add_u32 s49, s68, 1373568\n\t"
            "buffer_load_dwordx4 v249, s[72:75], s49 offen lds\n\t"
            "s_mov_b32 exec_hi, -1\n\t"
            "s_mov_b32 exec_hi, 0x3fffff\n\t"
            "s_add_u32 m0, s66, 6976\n\t"
            "s_add_u32 s49, s68, 1569792\n\t"
            "buffer_load_dwordx4 v249, s[72:75], s49 offen lds\n\t"
            "s_mov_b32 exec_hi, -1\n\t"
            "s_add_u32 s49, s33, 0\n\t"
            "buffer_load_dwordx4 v[0:3], v247, s[28:31], s49 offen sc1\n\t"
            "s_add_u32 s49, s33, 1024\n\t"
            "buffer_load_dwordx4 v[4:7], v247, s[28:31], s49 offen sc1\n\t"
            "s_add_u32 s49, s33, 2048\n\t"
            "buffer_load_dwordx4 v[8:11], v247, s[28:31], s49 offen sc1\n\t"
            "s_add_u32 s49, s33, 3072\n\t"
            "buffer_load_dwordx4 v[12:15], v247, s[28:31], s49 offen sc1\n\t"
            "s_add_u32 s49, s33, 4096\n\t"
            "buffer_load_dwordx4 v[16:19], v247, s[28:31], s49 offen sc1\n\t"
            "s_add_u32 s49, s33, 5120\n\t"
            "buffer_load_dwordx4 v[20:23], v247, s[28:31], s49 offen sc1\n\t"
            "s_add_u32 s49, s33, 6144\n\t"
            "buffer_load_dwordx4 v[24:27], v247, s[28:31], s49 offen sc1\n\t"
            "s_add_u32 s49, s33, 7168\n\t"
            "buffer_load_dwordx4 v[28:31], v247, s[28:31], s49 offen sc1\n\t"
            "s_add_u32 s49, s33, 8192\n\t"
            "buffer_load_dwordx4 v[32:35], v247, s[28:31], s49 offen sc1\n\t"
            "s_add_u32 s49, s33, 9216\n\t"
            "buffer_load_dwordx4 v[36:39], v247, s[28:31], s49 offen sc1\n\t"
            "s_add_u32 s49, s33, 10240\n\t"
            "buffer_load_dwordx4 v[40:43], v247, s[28:31], s49 offen sc1\n\t"
            "s_add_u32 s49, s33, 11264\n\t"
            "buffer_load_dwordx4 v[44:47], v247, s[28:31], s49 offen sc1\n\t"
            "s_add_u32 s49, s33, 12288\n\t"
            "buffer_load_dwordx4 v[48:51], v247, s[28:31], s49 offen sc1\n\t"
            "s_add_u32 s49, s33, 13312\n\t"
            "buffer_load_dwordx4 v[52:55], v247, s[28:31], s49 offen sc1\n\t"
            "s_add_u32 s49, s33, 14336\n\t"
            "buffer_load_dwordx4 v[56:59], v247, s[28:31], s49 offen sc1\n\t"
            "s_add_u32 s49, s33, 15360\n\t"
            "buffer_load_dwordx4 v[60:63], v247, s[28:31], s49 offen sc1\n\t"
            "s_add_u32 s49, s33, 16384\n\t"
            "buffer_load_dwordx4 v[64:67], v247, s[28:31], s49 offen sc1\n\t"
            "s_add_u32 s49, s33, 17408\n\t"
            "buffer_load_dwordx4 v[68:71], v247, s[28:31], s49 offen sc1\n\t"
            "s_add_u32 s49, s33, 18432\n\t"
            "buffer_load_dwordx4 v[72:75], v247, s[28:31], s49 offen sc1\n\t"
            "s_add_u32 s49, s33, 19456\n\t"
            "buffer_load_dwordx4 v[76:79], v247, s[28:31], s49 offen sc1\n\t"
            "s_add_u32 s49, s33, 20480\n\t"
            "buffer_load_dwordx4 v[80:83], v247, s[28:31], s49 offen sc1\n\t"
            "s_add_u32 s49, s33, 21504\n\t"
            "buffer_load_dwordx4 v[84:87], v247, s[28:31], s49 offen sc1\n\t"
            "s_add_u32 s49, s33, 22528\n\t"
            "buffer_load_dwordx4 v[88:91], v247, s[28:31], s49 offen sc1\n\t"
            "s_add_u32 s49, s33, 23552\n\t"
            "buffer_load_dwordx4 v[92:95], v247, s[28:31], s49 offen sc1\n\t"
            "s_add_u32 s49, s33, 24576\n\t"
            "buffer_load_dwordx4 v[96:99], v247, s[28:31], s49 offen sc1\n\t"
            "s_add_u32 s49, s33, 25600\n\t"
            "buffer_load_dwordx4 v[100:103], v247, s[28:31], s49 offen sc1\n\t"
            "s_add_u32 s49, s33, 26624\n\t"
            "buffer_load_dwordx4 v[104:107], v247, s[28:31], s49 offen sc1\n\t"
            "s_add_u32 s49, s33, 27648\n\t"
            "buffer_load_dwordx4 v[108:111], v247, s[28:31], s49 offen sc1\n\t"
            "s_add_u32 s49, s33, 28672\n\t"
            "buffer_load_dwordx4 v[112:115], v247, s[28:31], s49 offen sc1\n\t"
            "s_add_u32 s49, s33, 29696\n\t"
            "buffer_load_dwordx4 v[116:119], v247, s[28:31], s49 offen sc1\n\t"
            "s_add_u32 s49, s33, 30720\n\t"
            "buffer_load_dwordx4 v[120:123], v247, s[28:31], s49 offen sc1\n\t"
            "s_add_u32 s49, s33, 31744\n\t"
            "buffer_load_dwordx4 v[124:127], v247, s[28:31], s49 offen sc1\n\t"
            "s_add_u32 s49, s33, 32768\n\t"
            "buffer_load_dwordx4 v[128:131], v247, s[28:31], s49 offen sc1\n\t"
            "s_add_u32 s49, s33, 33792\n\t"
            "buffer_load_dwordx4 v[132:135], v247, s[28:31], s49 offen sc1\n\t"
            "s_add_u32 s49, s33, 34816\n\t"
            "buffer_load_dwordx4 v[136:139], v247, s[28:31], s49 offen sc1\n\t"
            "s_add_u32 s49, s33, 35840\n\t"
            "buffer_load_dwordx4 v[140:143], v247, s[28:31], s49 offen sc1\n\t"
            "s_add_u32 s59, s59, -47104\n\t"
            "s_add_u32 s45, s46, s27\n\t"
            "s_mov_b32 s46, s47\n\t"
            "s_mov_b32 s47, s68\n\t"
            "s_xor_b32 s48, s48, s71\n\t"
            "s_xor_b32 s67, s67, s71\n\t"
            "s_add_u32 s76, s76, 4\n\t"
            "s_add_u32 s70, s70, 4\n\t"
            "s_lshl_b32 s56, s56, 2\n\t"
            "s_lshr_b32 s49, s26, 30\n\t"
            "s_or_b32 s56, s56, s49\n\t"
            "s_waitcnt vmcnt(0)\n\t"
            "s_nop 7\n\t"
            ".p2align 6\n\t"
            "s_nop 0\n\t"
            "s_nop 0\n\t"
            "s_nop 0\n\t"
            "s_nop 0\n\t"
            "s_nop 0\n\t"
            "s_nop 0\n\t"
            "s_nop 0\n\t"
            ".Lwb_loop%=:\n\t"
            "s_setprio 1\n\t"
            "ds_read_b128 v[168:171], v254\n\t"
            "ds_read_b128 v[172:175], v254 offset:64\n\t"
            "s_waitcnt vmcnt(9)\n\t"
            "ds_read_b64 v[144:145], v253\n\t"
            "ds_read_b64 v[146:147], v253 offset:96\n\t"
            "ds_read_b64 v[148:149], v253 offset:1744\n\t"
            "ds_read_b64 v[150:151], v253 offset:1840\n\t"
            "ds_read_b64 v[152:153], v253 offset:8\n\t"
            "ds_read_b64 v[154:155], v253 offset:104\n\t"
            "ds_read_b64 v[156:157], v253 offset:1752\n\t"
            "ds_read_b64 v[158:159], v253 offset:1848\n\t"
            "ds_read_b64 v[160:161], v253 offset:16\n\t"
            "ds_read_b64 v[162:163], v253 offset:112\n\t"
            "ds_read_b64 v[164:165], v253 offset:1760\n\t"
            "ds_read_b64 v[166:167], v253 offset:1856\n\t"
            "v_mov_b64 v[176:177], 0\n\t"
            "v_mov_b64 v[178:179], 0\n\t"
            "v_mov_b64 v[180:181], 0\n\t"
            "v_mov_b64 v[182:183], 0\n\t"
            "v_mov_b32 v249, s76\n\t"
            "v_mov_b64 v[184:185], 0\n\t"
            "v_mov_b64 v[186:187], 0\n\t"
            "v_mov_b64 v[188:189], 0\n\t"
            "v_mov_b64 v[190:191], 0\n\t"
            "v_add_u32 v236, s67, v250\n\t"
            "v_add_u32 v248, s67, v251\n\t"
            "v_mov_b32 v247, 0\n\t"
            "ds_read_b32 v249, v249\n\t"
            "s_setprio 3\n\t"
            "s_waitcnt lgkmcnt(9)\n\t"
            "v_smfmac_f32_16x16x64_bf16 v[176:179], v[168:171], v[144:151], v255\n\t"
            "ds_read_b128 v[168:171], v254 offset:128\n\t"
            "s_waitcnt lgkmcnt(6)\n\t"
            "v_smfmac_f32_16x16x64_bf16 v[180:183], v[172:175], v[152:159], v255\n\t"
            "s_setprio 1\n\t"
            "ds_read_b64 v[144:145], v253 offset:24\n\t"
            "ds_read_b64 v[146:147], v253 offset:120\n\t"
            "ds_read_b64 v[148:149], v253 offset:1768\n\t"
            "ds_read_b64 v[150:151], v253 offset:1864\n\t"
            "ds_read_b128 v[172:175], v254 offset:192\n\t"
            "ds_read_b64 v[152:153], v253 offset:32\n\t"
            "ds_read_b64 v[154:155], v253 offset:128\n\t"
            "ds_read_b64 v[156:157], v253 offset:1776\n\t"
            "ds_read_b64 v[158:159], v253 offset:1872\n\t"
            "v_cvt_pk_bf16_f32 v176, v176, v177\n\t"
            "v_cvt_pk_bf16_f32 v177, v178, v179\n\t"
            "v_cvt_pk_bf16_f32 v178, v180, v181\n\t"
            "v_cvt_pk_bf16_f32 v179, v182, v183\n\t"
            "s_setprio 3\n\t"
            "v_mfma_f32_4x4x4_16b_bf16 v[196:199], v[176:177], v[176:177], 0\n\t"
            "v_mov_b64 v[180:181], 0\n\t"
            "v_mfma_f32_16x16x32_bf16 v[192:195], v[238:241], v[176:179], 0\n\t"
            "s_waitcnt lgkmcnt(10)\n\t"
            "v_readfirstlane_b32 s26, v249\n\t"
            "v_mov_b64 v[182:183], 0\n\t"
            "v_mfma_f32_4x4x4_16b_bf16 v[196:199], v[178:179], v[178:179], v[196:199]\n\t"
            "ds_read_b32 v249, v252 offset:12\n\t"
            "s_and_b32 s68, s26, 0x7ffffff\n\t"
            "s_add_u32 s68, s68, s78\n\t"
            "s_and_b32 s69, s26, 0x38000000\n\t"
            "s_or_b32 s69, s69, s39\n\t"
            "s_bitcmp1_b32 s26, 27\n\t"
            "s_cselect_b32 s79, 2.0, s68\n\t"
            "s_and_b32 s57, s56, 3\n\t"
            "s_lshl_b32 s58, s57, 3\n\t"
            "s_lshr_b32 m0, s54, s58\n\t"
            "s_lshl_b32 s58, s57, 4\n\t"
            "s_lshr_b64 s[36:37], s[50:51], s58\n\t"
            "s_and_b32 s57, s36, 0xffff\n\t"
            "s_add_u32 s57, s57, s67\n\t"
            "v_writelane_b32 v236, s57, 63\n\t"
            "v_writelane_b32 v236, s59, m0\n\t"
            "v_xor_b32 v237, 16, v236\n\t"
            "s_lshr_b64 s[36:37], s[52:53], s58\n\t"
            "s_and_b32 s58, s36, 0xffff\n\t"
            "s_add_u32 s58, s58, s67\n\t"
            "ds_write_b128 v236, v[176:179]\n\t"
            "s_waitcnt lgkmcnt(11)\n\t"
            "v_smfmac_f32_16x16x64_bf16 v[184:187], v[168:171], v[160:167], v255\n\t"
            "s_and_b32 s38, s52, 0xffff\n\t"
            "s_add_u32 s38, s38, s67\n\t"
            "ds_read_b128 v[168:171], v254 offset:256\n\t"
            "s_waitcnt lgkmcnt(7)\n\t"
            "v_smfmac_f32_16x16x64_bf16 v[188:191], v[172:175], v[144:151], v255\n\t"
            "s_setprio 1\n\t"
            "ds_read_b64 v[160:161], v253 offset:40\n\t"
            "ds_read_b64 v[162:163], v253 offset:136\n\t"
            "ds_read_b64 v[164:165], v253 offset:1784\n\t"
            "ds_read_b64 v[166:167], v253 offset:1880\n\t"
            "ds_read_b128 v[172:175], v254 offset:320\n\t"
            "ds_read_b64 v[144:145], v253 offset:48\n\t"
            "ds_read_b64 v[146:147], v253 offset:144\n\t"
            "ds_read_b64 v[148:149], v253 offset:1792\n\t"
            "s_waitcnt lgkmcnt(14)\n\t"
            "ds_read_b64 v[150:151], v253 offset:1888\n\t"
            "v_mov_b64 v[176:177], 0\n\t"
            "v_mov_b64 v[178:179], 0\n\t"
            "v_cvt_pk_bf16_f32 v184, v184, v185\n\t"
            "v_cvt_pk_bf16_f32 v185, v186, v187\n\t"
            "v_cvt_pk_bf16_f32 v186, v188, v189\n\t"
            "v_cvt_pk_bf16_f32 v187, v190, v191\n\t"
            "s_setprio 3\n\t"
            "v_mfma_f32_4x4x4_16b_bf16 v[196:199], v[184:185], v[184:185], v[196:199]\n\t"
            "s_waitcnt lgkmcnt(14)\n\t"
            "ds_write_b128 v237, v[184:187]\n\t"
            "v_mfma_f32_16x16x32_bf16 v[192:195], v[238:241], v[184:187], v[192:195]\n\t"
            "v_mov_b64 v[184:185], 0\n\t"
            "v_mov_b64 v[188:189], 0\n\t"
            "v_mfma_f32_4x4x4_16b_bf16 v[196:199], v[186:187], v[186:187], v[196:199]\n\t"
            "v_mov_b64 v[186:187], 0\n\t"
            "v_mov_b64 v[190:191], 0\n\t"
            "v_writelane_b32 v248, s58, 63\n\t"
            "v_writelane_b32 v248, s38, m0\n\t"
            "s_waitcnt lgkmcnt(12)\n\t"
            "v_and_b32 v249, s69, v249\n\t"
            "s_waitcnt lgkmcnt(10)\n\t"
            "v_smfmac_f32_16x16x64_bf16 v[176:179], v[168:171], v[152:159], v255\n\t"
            "ds_read_b128 v[168:171], v254 offset:384\n\t"
            "ds_read_b64 v[152:153], v253 offset:56\n\t"
            "ds_read_b64 v[154:155], v253 offset:152\n\t"
            "ds_read_b64 v[156:157], v253 offset:1800\n\t"
            "ds_read_b64 v[158:159], v253 offset:1896\n\t"
            "s_waitcnt lgkmcnt(10)\n\t"
            "v_smfmac_f32_16x16x64_bf16 v[180:183], v[172:175], v[160:167], v255\n\t"
            "s_setprio 1\n\t"
            "ds_read_b128 v[172:175], v254 offset:448\n\t"
            "ds_read_b64 v[160:161], v253 offset:64\n\t"
            "ds_read_b64 v[162:163], v253 offset:160\n\t"
            "ds_read_b64 v[164:165], v253 offset:1808\n\t"
            "ds_read_b64 v[166:167], v253 offset:1904\n\t"
            "v_cvt_pk_bf16_f32 v176, v176, v177\n\t"
            "v_cvt_pk_bf16_f32 v177, v178, v179\n\t"
            "v_cvt_pk_bf16_f32 v178, v180, v181\n\t"
            "v_cvt_pk_bf16_f32 v179, v182, v183\n\t"
            "s_setprio 3\n\t"
            "v_mfma_f32_4x4x4_16b_bf16 v[196:199], v[176:177], v[176:177], v[196:199]\n\t"
            "s_waitcnt lgkmcnt(14)\n\t"
            "ds_write_b128 v236, v[176:179] offset:128\n\t"
            "v_mfma_f32_16x16x32_bf16 v[192:195], v[238:241], v[176:179], v[192:195]\n\t"
            "v_mov_b64 v[176:177], 0\n\t"
            "v_mov_b64 v[180:181], 0\n\t"
            "v_mfma_f32_4x4x4_16b_bf16 v[196:199], v[178:179], v[178:179], v[196:199]\n\t"
            "v_mov_b64 v[178:179], 0\n\t"
            "v_mov_b64 v[182:183], 0\n\t"
            "s_waitcnt lgkmcnt(10)\n\t"
            "v_smfmac_f32_16x16x64_bf16 v[184:187], v[168:171], v[144:151], v255\n\t"
            "ds_read_b128 v[168:171], v254 offset:512\n\t"
            "ds_read_b64 v[144:145], v253 offset:72\n\t"
            "ds_read_b64 v[146:147], v253 offset:168\n\t"
            "ds_read_b64 v[148:149], v253 offset:1816\n\t"
            "ds_read_b64 v[150:151], v253 offset:1912\n\t"
            "s_waitcnt lgkmcnt(10)\n\t"
            "v_smfmac_f32_16x16x64_bf16 v[188:191], v[172:175], v[152:159], v255\n\t"
            "s_setprio 1\n\t"
            "ds_read_b128 v[172:175], v254 offset:576\n\t"
            "ds_read_b64 v[152:153], v253 offset:80\n\t"
            "ds_read_b64 v[154:155], v253 offset:176\n\t"
            "ds_read_b64 v[156:157], v253 offset:1824\n\t"
            "ds_read_b64 v[158:159], v253 offset:1920\n\t"
            "v_cvt_pk_bf16_f32 v184, v184, v185\n\t"
            "v_cvt_pk_bf16_f32 v185, v186, v187\n\t"
            "v_cvt_pk_bf16_f32 v186, v188, v189\n\t"
            "v_cvt_pk_bf16_f32 v187, v190, v191\n\t"
            "s_setprio 3\n\t"
            "v_mfma_f32_4x4x4_16b_bf16 v[196:199], v[184:185], v[184:185], v[196:199]\n\t"
            "s_waitcnt lgkmcnt(14)\n\t"
            "ds_write_b128 v237, v[184:187] offset:128\n\t"
            "v_mfma_f32_16x16x32_bf16 v[192:195], v[238:241], v[184:187], v[192:195]\n\t"
            "v_mov_b64 v[184:185], 0\n\t"
            "v_mov_b64 v[188:189], 0\n\t"
            "v_mfma_f32_4x4x4_16b_bf16 v[196:199], v[186:187], v[186:187], v[196:199]\n\t"
            "v_mov_b64 v[186:187], 0\n\t"
            "v_mov_b64 v[190:191], 0\n\t"
            "s_waitcnt lgkmcnt(10)\n\t"
            "v_smfmac_f32_16x16x64_bf16 v[176:179], v[168:171], v[160:167], v255\n\t"
            "ds_read_b128 v[168:171], v254 offset:640\n\t"
            "ds_read_b64 v[160:161], v253 offset:88\n\t"
            "ds_read_b64 v[162:163], v253 offset:184\n\t"
            "ds_read_b64 v[164:165], v253 offset:1832\n\t"
            "ds_read_b64 v[166:167], v253 offset:1928\n\t"
            "s_waitcnt lgkmcnt(10)\n\t"
            "v_smfmac_f32_16x16x64_bf16 v[180:183], v[172:175], v[144:151], v255\n\t"
            "ds_read_b128 v[172:175], v254 offset:704\n\t"
            "ds_read_b32 v247, v247 offset:7856\n\t"
            "v_cvt_pk_bf16_f32 v176, v176, v177\n\t"
            "v_cvt_pk_bf16_f32 v177, v178, v179\n\t"
            "v_lshlrev_b32 v144, 16, v212\n\t"
            "v_and_b32 v145, 0xffff0000, v212\n\t"
            "v_mfma_f32_4x4x4_16b_bf16 v[196:199], v[176:177], v[176:177], v[196:199]\n\t"
            "v_lshlrev_b32 v146, 16, v213\n\t"
            "v_cvt_pk_bf16_f32 v178, v180, v181\n\t"
            "v_cvt_pk_bf16_f32 v179, v182, v183\n\t"
            "v_lshlrev_b32 v180, 16, v210\n\t"
            "v_and_b32 v181, 0xffff0000, v210\n\t"
            "v_mfma_f32_4x4x4_16b_bf16 v[196:199], v[178:179], v[178:179], v[196:199]\n\t"
            "ds_write_b128 v236, v[176:179] offset:256\n\t"
            "v_mfma_f32_16x16x32_bf16 v[192:195], v[238:241], v[176:179], v[192:195]\n\t"
            "s_setprio 1\n\t"
            "v_lshlrev_b32 v176, 16, v208\n\t"
            "v_and_b32 v177, 0xffff0000, v208\n\t"
            "v_lshlrev_b32 v178, 16, v209\n\t"
            "v_and_b32 v179, 0xffff0000, v209\n\t"
            "v_lshlrev_b32 v182, 16, v211\n\t"
            "v_and_b32 v183, 0xffff0000, v211\n\t"
            "v_and_b32 v147, 0xffff0000, v213\n\t"
            "v_lshlrev_b32 v148, 16, v214\n\t"
            "v_and_b32 v149, 0xffff0000, v214\n\t"
            "v_lshlrev_b32 v150, 16, v215\n\t"
            "v_and_b32 v151, 0xffff0000, v215\n\t"
            "v_add_u32 v236, s48, v244\n\t"
            "ds_read_b128 v[212:215], v236 offset:6144\n\t"
            "s_setprio 3\n\t"
            "s_waitcnt lgkmcnt(8)\n\t"
            "v_smfmac_f32_16x16x64_bf16 v[184:187], v[168:171], v[152:159], v255\n\t"
            "v_lshlrev_b32 v152, 16, v200\n\t"
            "v_and_b32 v153, 0xffff0000, v200\n\t"
            "v_lshlrev_b32 v154, 16, v201\n\t"
            "v_and_b32 v155, 0xffff0000, v201\n\t"
            "v_lshlrev_b32 v156, 16, v202\n\t"
            "s_waitcnt lgkmcnt(3)\n\t"
            "v_smfmac_f32_16x16x64_bf16 v[188:191], v[172:175], v[160:167], v255\n\t"
            "s_setprio 1\n\t"
            "s_mov_b32 exec_hi, 0x3fffff\n\t"
            "s_add_u32 m0, s66, 0\n\t"
            "s_add_u32 s49, s79, 0\n\t"
            "buffer_load_dwordx4 v249, s[72:75], s49 offen lds\n\t"
            "s_mov_b32 exec_hi, -1\n\t"
            "s_mov_b32 exec_hi, 0x3fffff\n\t"
            "s_add_u32 m0, s66, 872\n\t"
            "s_add_u32 s49, s68, 196224\n\t"
            "buffer_load_dwordx4 v249, s[72:75], s49 offen lds\n\t"
            "s_mov_b32 exec_hi, -1\n\t"
            "s_mov_b32 exec_hi, 0x3fffff\n\t"
            "s_add_u32 m0, s66, 1744\n\t"
            "s_add_u32 s49, s68, 392448\n\t"
            "buffer_load_dwordx4 v249, s[72:75], s49 offen lds\n\t"
            "s_mov_b32 exec_hi, -1\n\t"
            "s_mov_b32 exec_hi, 0x3fffff\n\t"
            "s_add_u32 m0, s66, 2616\n\t"
            "s_add_u32 s49, s68, 588672\n\t"
            "buffer_load_dwordx4 v249, s[72:75], s49 offen lds\n\t"
            "s_mov_b32 exec_hi, -1\n\t"
            "s_mov_b32 exec_hi, 0x3fffff\n\t"
            "s_add_u32 m0, s66, 3488\n\t"
            "s_add_u32 s49, s68, 784896\n\t"
            "buffer_load_dwordx4 v249, s[72:75], s49 offen lds\n\t"
            "s_mov_b32 exec_hi, -1\n\t"
            "s_mov_b32 exec_hi, 0x3fffff\n\t"
            "s_add_u32 m0, s66, 4360\n\t"
            "s_add_u32 s49, s68, 981120\n\t"
            "buffer_load_dwordx4 v249, s[72:75], s49 offen lds\n\t"
            "s_mov_b32 exec_hi, -1\n\t"
            "s_mov_b32 exec_hi, 0x3fffff\n\t"
            "s_add_u32 m0, s66, 5232\n\t"
            "s_add_u32 s49, s68, 1177344\n\t"
            "buffer_load_dwordx4 v249, s[72:75], s49 offen lds\n\t"
            "s_mov_b32 exec_hi, -1\n\t"
            "s_mov_b32 exec_hi, 0x3fffff\n\t"
            "s_add_u32 m0, s66, 6104\n\t"
            "s_add_u32 s49, s68, 1373568\n\t"
            "buffer_load_dwordx4 v249, s[72:75], s49 offen lds\n\t"
            "s_mov_b32 exec_hi, -1\n\t"
            "s_mov_b32 exec_hi, 0x3fffff\n\t"
            "s_add_u32 m0, s66, 6976\n\t"
            "s_add_u32 s49, s68, 1569792\n\t"
            "buffer_load_dwordx4 v249, s[72:75], s49 offen lds\n\t"
            "s_mov_b32 exec_hi, -1\n\t"
            "v_cvt_pk_bf16_f32 v184, v184, v185\n\t"
            "v_cvt_pk_bf16_f32 v185, v186, v187\n\t"
            "v_cvt_pk_bf16_f32 v186, v188, v189\n\t"
            "v_cvt_pk_bf16_f32 v187, v190, v191\n\t"
            "s_setprio 3\n\t"
            "v_mfma_f32_4x4x4_16b_bf16 v[196:199], v[184:185], v[184:185], v[196:199]\n\t"
            "ds_write_b128 v237, v[184:187] offset:256\n\t"
            "v_mfma_f32_16x16x32_bf16 v[192:195], v[238:241], v[184:187], v[192:195]\n\t"
            "s_waitcnt lgkmcnt(3)\n\t"
            "v_readfirstlane_b32 s77, v247\n\t"
            "s_cmp_ge_u32 s77, s70\n\t"
            "s_cbranch_scc0 .Lwb_spin%=\n\t"
            ".Lwb_go%=:\n\t"
            "v_mfma_f32_4x4x4_16b_bf16 v[196:199], v[186:187], v[186:187], v[196:199]\n\t"
            "s_setprio 1\n\t"
            "ds_read_b128 v[184:187], v242\n\t"
            "ds_read_b128 v[188:191], v243\n\t"
            "ds_read_b128 v[172:175], v243 offset:128\n\t"
            "v_and_b32 v157, 0xffff0000, v202\n\t"
            "v_lshlrev_b32 v158, 16, v203\n\t"
            "v_and_b32 v159, 0xffff0000, v203\n\t"
            "v_lshlrev_b32 v160, 16, v204\n\t"
            "v_and_b32 v161, 0xffff0000, v204\n\t"
            "v_lshlrev_b32 v162, 16, v205\n\t"
            "v_cndmask_b32_e64 v193, v196, v197, s[60:61]\n\t"
            "v_cndmask_b32_e64 v193, v193, v198, s[62:63]\n\t"
            "v_cndmask_b32_e64 v193, v193, v199, s[64:65]\n\t"
            "v_and_b32 v163, 0xffff0000, v205\n\t"
            "v_lshlrev_b32 v164, 16, v206\n\t"
            "v_and_b32 v165, 0xffff0000, v206\n\t"
            "v_lshlrev_b32 v166, 16, v207\n\t"
            "v_and_b32 v167, 0xffff0000, v207\n\t"
            "v_lshlrev_b32 v168, 16, v216\n\t"
            "v_and_b32 v169, 0xffff0000, v216\n\t"
            "v_lshlrev_b32 v170, 16, v217\n\t"
            "v_and_b32 v171, 0xffff0000, v217\n\t"
            "s_bfe_u32 s57, s56, 0x20004\n\t"
            "s_lshl_b32 s58, s57, 3\n\t"
            "s_lshr_b32 s58, s55, s58\n\t"
            "s_lshl_b64 s[36:37], s[34:35], s58\n\t"
            "ds_read_b128 v[196:199], v252\n\t"
            "ds_read_b128 v[200:203], v236\n\t"
            "ds_write_b64 v248, v[192:193] offset:18816\n\t"
            "ds_read_b128 v[192:195], v242 offset:128\n\t"
            "s_bitcmp1_b32 s57, 1\n\t"
            "s_cselect_b32 s58, 1177344, 0\n\t"
            "s_bitcmp1_b32 s57, 0\n\t"
            "s_cselect_b32 s57, 2304, 0\n\t"
            "s_add_u32 s57, s57, s58\n\t"
            "s_mov_b64 exec, 1\n\t"
            "ds_inc_u32 v255, v238 offset:7860\n\t"
            "s_mov_b64 exec, -1\n\t"
            "v_add_u32 v237, s48, v245\n\t"
            "v_add_u32 v248, s48, v246\n\t"
            "s_add_u32 s59, s59, 800\n\t"
            "s_setprio 1\n\t"
            "ds_read_b128 v[204:207], v237\n\t"
            "s_waitcnt lgkmcnt(7)\n\t"
            "v_mfma_f32_16x16x32_bf16 v[152:155], v[0:3], v[184:187], v[152:155]\n\t"
            "ds_read_b128 v[208:211], v248\n\t"
            "v_mfma_f32_16x16x32_bf16 v[152:155], v[4:7], v[188:191], v[152:155]\n\t"
            "v_mfma_f32_16x16x32_bf16 v[156:159], v[28:31], v[188:191], v[156:159]\n\t"
            "v_mfma_f32_16x16x32_bf16 v[156:159], v[24:27], v[184:187], v[156:159]\n\t"
            "v_mfma_f32_16x16x32_bf16 v[160:163], v[48:51], v[184:187], v[160:163]\n\t"
            "v_mfma_f32_16x16x32_bf16 v[160:163], v[52:55], v[188:191], v[160:163]\n\t"
            "v_mfma_f32_16x16x32_bf16 v[164:167], v[76:79], v[188:191], v[164:167]\n\t"
            "s_waitcnt lgkmcnt(6)\n\t"
            "s_mov_b64 exec, s[36:37]\n\t"
            "v_add_u32 v196, s57, v196\n\t"
            "s_mov_b64 exec, -1\n\t"
            "v_mfma_f32_16x16x32_bf16 v[164:167], v[72:75], v[184:187], v[164:167]\n\t"
            "v_mfma_f32_16x16x32_bf16 v[176:179], v[96:99], v[184:187], v[176:179]\n\t"
            "v_mfma_f32_16x16x32_bf16 v[176:179], v[100:103], v[188:191], v[176:179]\n\t"
            "v_mfma_f32_16x16x32_bf16 v[180:183], v[124:127], v[188:191], v[180:183]\n\t"
            "ds_read_b128 v[188:191], v243 offset:256\n\t"
            "v_mfma_f32_16x16x32_bf16 v[180:183], v[120:123], v[184:187], v[180:183]\n\t"
            "ds_read_b128 v[184:187], v242 offset:256\n\t"
            "s_waitcnt lgkmcnt(5)\n\t"
            "v_mfma_f32_16x16x32_bf16 v[152:155], v[8:11], v[192:195], v[152:155]\n\t"
            "v_mfma_f32_16x16x32_bf16 v[152:155], v[12:15], v[172:175], v[152:155]\n\t"
            "v_mfma_f32_16x16x32_bf16 v[156:159], v[36:39], v[172:175], v[156:159]\n\t"
            "v_mfma_f32_16x16x32_bf16 v[156:159], v[32:35], v[192:195], v[156:159]\n\t"
            "v_mfma_f32_16x16x32_bf16 v[160:163], v[56:59], v[192:195], v[160:163]\n\t"
            "v_mfma_f32_16x16x32_bf16 v[160:163], v[60:63], v[172:175], v[160:163]\n\t"
            "v_mfma_f32_16x16x32_bf16 v[164:167], v[84:87], v[172:175], v[164:167]\n\t"
            "v_mfma_f32_16x16x32_bf16 v[164:167], v[80:83], v[192:195], v[164:167]\n\t"
            "v_mfma_f32_16x16x32_bf16 v[176:179], v[104:107], v[192:195], v[176:179]\n\t"
            "v_mfma_f32_16x16x32_bf16 v[176:179], v[108:111], v[172:175], v[176:179]\n\t"
            "v_mfma_f32_16x16x32_bf16 v[180:183], v[132:135], v[172:175], v[180:183]\n\t"
            "ds_read_b128 v[172:175], v243 offset:6144\n\t"
            "v_mfma_f32_16x16x32_bf16 v[180:183], v[128:131], v[192:195], v[180:183]\n\t"
            "ds_read_b128 v[192:195], v242 offset:6144\n\t"
            "s_waitcnt lgkmcnt(2)\n\t"
            "v_mfma_f32_16x16x32_bf16 v[152:155], v[16:19], v[184:187], v[152:155]\n\t"
            "v_mfma_f32_16x16x32_bf16 v[152:155], v[20:23], v[188:191], v[152:155]\n\t"
            "v_mfma_f32_16x16x32_bf16 v[156:159], v[44:47], v[188:191], v[156:159]\n\t"
            "v_mfma_f32_16x16x32_bf16 v[156:159], v[40:43], v[184:187], v[156:159]\n\t"
            "v_mfma_f32_16x16x32_bf16 v[160:163], v[64:67], v[184:187], v[160:163]\n\t"
            "v_mfma_f32_16x16x32_bf16 v[160:163], v[68:71], v[188:191], v[160:163]\n\t"
            "v_mfma_f32_16x16x32_bf16 v[164:167], v[92:95], v[188:191], v[164:167]\n\t"
            "v_mfma_f32_16x16x32_bf16 v[164:167], v[88:91], v[184:187], v[164:167]\n\t"
            "s_nop 1\n\t"
            "v_cvt_pk_bf16_f32 v152, v152, v153\n\t"
            "v_mfma_f32_16x16x32_bf16 v[176:179], v[112:115], v[184:187], v[176:179]\n\t"
            "v_cvt_pk_bf16_f32 v153, v154, v155\n\t"
            "v_mfma_f32_16x16x32_bf16 v[176:179], v[116:119], v[188:191], v[176:179]\n\t"
            "v_cvt_pk_bf16_f32 v154, v156, v157\n\t"
            "v_cvt_pk_bf16_f32 v155, v158, v159\n\t"
            "v_mfma_f32_16x16x32_bf16 v[180:183], v[140:143], v[188:191], v[180:183]\n\t"
            "ds_read_b128 v[188:191], v243 offset:6272\n\t"
            "v_cvt_pk_bf16_f32 v160, v160, v161\n\t"
            "v_mfma_f32_16x16x32_bf16 v[180:183], v[136:139], v[184:187], v[180:183]\n\t"
            "ds_read_b128 v[184:187], v242 offset:6272\n\t"
            "v_cvt_pk_bf16_f32 v161, v162, v163\n\t"
            "s_waitcnt lgkmcnt(2)\n\t"
            "v_mfma_f32_16x16x32_bf16 v[144:147], v[0:3], v[192:195], v[144:147]\n\t"
            "v_cvt_pk_bf16_f32 v176, v176, v177\n\t"
            "v_cvt_pk_bf16_f32 v177, v178, v179\n\t"
            "v_mfma_f32_16x16x32_bf16 v[144:147], v[4:7], v[172:175], v[144:147]\n\t"
            "v_cvt_pk_bf16_f32 v162, v164, v165\n\t"
            "v_cvt_pk_bf16_f32 v163, v166, v167\n\t"
            "v_mfma_f32_16x16x32_bf16 v[148:151], v[28:31], v[172:175], v[148:151]\n\t"
            "v_cvt_pk_bf16_f32 v178, v180, v181\n\t"
            "v_cvt_pk_bf16_f32 v179, v182, v183\n\t"
            "v_mfma_f32_16x16x32_bf16 v[148:151], v[24:27], v[192:195], v[148:151]\n\t"
            "v_lshlrev_b32 v156, 16, v218\n\t"
            "v_and_b32 v157, 0xffff0000, v218\n\t"
            "v_mfma_f32_16x16x32_bf16 v[168:171], v[48:51], v[192:195], v[168:171]\n\t"
            "v_lshlrev_b32 v158, 16, v219\n\t"
            "v_and_b32 v159, 0xffff0000, v219\n\t"
            "v_mfma_f32_16x16x32_bf16 v[168:171], v[52:55], v[172:175], v[168:171]\n\t"
            "ds_read_b128 v[216:219], v237 offset:6144\n\t"
            "v_lshlrev_b32 v164, 16, v220\n\t"
            "v_mfma_f32_16x16x32_bf16 v[156:159], v[76:79], v[172:175], v[156:159]\n\t"
            "v_and_b32 v165, 0xffff0000, v220\n\t"
            "v_lshlrev_b32 v166, 16, v221\n\t"
            "v_mfma_f32_16x16x32_bf16 v[156:159], v[72:75], v[192:195], v[156:159]\n\t"
            "v_and_b32 v167, 0xffff0000, v221\n\t"
            "v_lshlrev_b32 v180, 16, v222\n\t"
            "v_and_b32 v181, 0xffff0000, v222\n\t"
            "v_mfma_f32_16x16x32_bf16 v[164:167], v[96:99], v[192:195], v[164:167]\n\t"
            "v_lshlrev_b32 v182, 16, v223\n\t"
            "v_and_b32 v183, 0xffff0000, v223\n\t"
            "v_mfma_f32_16x16x32_bf16 v[164:167], v[100:103], v[172:175], v[164:167]\n\t"
            "ds_read_b128 v[220:223], v248 offset:6144\n\t"
            "buffer_store_dwordx4 v[152:155], v196, s[40:43], s45 offen\n\t"
            "v_mfma_f32_16x16x32_bf16 v[180:183], v[124:127], v[172:175], v[180:183]\n\t"
            "ds_read_b128 v[172:175], v243 offset:6400\n\t"
            "v_lshlrev_b32 v152, 16, v224\n\t"
            "v_mfma_f32_16x16x32_bf16 v[180:183], v[120:123], v[192:195], v[180:183]\n\t"
            "ds_read_b128 v[192:195], v242 offset:6400\n\t"
            "buffer_store_dwordx4 v[160:163], v196, s[40:43], s45 offen offset:64\n\t"
            "s_waitcnt lgkmcnt(4)\n\t"
            "v_mfma_f32_16x16x32_bf16 v[144:147], v[8:11], v[184:187], v[144:147]\n\t"
            "v_and_b32 v153, 0xffff0000, v224\n\t"
            "v_lshlrev_b32 v154, 16, v225\n\t"
            "v_mfma_f32_16x16x32_bf16 v[144:147], v[12:15], v[188:191], v[144:147]\n\t"
            "buffer_store_dwordx4 v[176:179], v196, s[40:43], s45 offen offset:128\n\t"
            "v_mfma_f32_16x16x32_bf16 v[148:151], v[36:39], v[188:191], v[148:151]\n\t"
            "v_and_b32 v155, 0xffff0000, v225\n\t"
            "v_lshlrev_b32 v160, 16, v226\n\t"
            "v_mfma_f32_16x16x32_bf16 v[148:151], v[32:35], v[184:187], v[148:151]\n\t"
            "v_and_b32 v161, 0xffff0000, v226\n\t"
            "v_lshlrev_b32 v162, 16, v227\n\t"
            "v_mfma_f32_16x16x32_bf16 v[168:171], v[56:59], v[184:187], v[168:171]\n\t"
            "v_and_b32 v163, 0xffff0000, v227\n\t"
            "v_lshlrev_b32 v176, 16, v228\n\t"
            "v_mfma_f32_16x16x32_bf16 v[168:171], v[60:63], v[188:191], v[168:171]\n\t"
            "ds_read_b128 v[224:227], v236 offset:12288\n\t"
            "v_and_b32 v177, 0xffff0000, v228\n\t"
            "v_mfma_f32_16x16x32_bf16 v[156:159], v[84:87], v[188:191], v[156:159]\n\t"
            "v_lshlrev_b32 v178, 16, v229\n\t"
            "v_and_b32 v179, 0xffff0000, v229\n\t"
            "v_mfma_f32_16x16x32_bf16 v[156:159], v[80:83], v[184:187], v[156:159]\n\t"
            "v_mfma_f32_16x16x32_bf16 v[164:167], v[104:107], v[184:187], v[164:167]\n\t"
            "v_mfma_f32_16x16x32_bf16 v[164:167], v[108:111], v[188:191], v[164:167]\n\t"
            "v_mfma_f32_16x16x32_bf16 v[180:183], v[132:135], v[188:191], v[180:183]\n\t"
            "ds_read_b128 v[188:191], v243 offset:12288\n\t"
            "v_mfma_f32_16x16x32_bf16 v[180:183], v[128:131], v[184:187], v[180:183]\n\t"
            "ds_read_b128 v[184:187], v242 offset:12288\n\t"
            "s_waitcnt lgkmcnt(3)\n\t"
            "v_mfma_f32_16x16x32_bf16 v[144:147], v[16:19], v[192:195], v[144:147]\n\t"
            "v_mfma_f32_16x16x32_bf16 v[144:147], v[20:23], v[172:175], v[144:147]\n\t"
            "v_mfma_f32_16x16x32_bf16 v[168:171], v[64:67], v[192:195], v[168:171]\n\t"
            "v_mfma_f32_16x16x32_bf16 v[168:171], v[68:71], v[172:175], v[168:171]\n\t"
            "v_mfma_f32_16x16x32_bf16 v[164:167], v[112:115], v[192:195], v[164:167]\n\t"
            "v_mfma_f32_16x16x32_bf16 v[164:167], v[116:119], v[172:175], v[164:167]\n\t"
            "v_mfma_f32_16x16x32_bf16 v[148:151], v[44:47], v[172:175], v[148:151]\n\t"
            "v_mfma_f32_16x16x32_bf16 v[148:151], v[40:43], v[192:195], v[148:151]\n\t"
            "s_nop 1\n\t"
            "v_cvt_pk_bf16_f32 v144, v144, v145\n\t"
            "v_mfma_f32_16x16x32_bf16 v[156:159], v[92:95], v[172:175], v[156:159]\n\t"
            "v_cvt_pk_bf16_f32 v145, v146, v147\n\t"
            "v_mfma_f32_16x16x32_bf16 v[156:159], v[88:91], v[192:195], v[156:159]\n\t"
            "v_cvt_pk_bf16_f32 v168, v168, v169\n\t"
            "v_cvt_pk_bf16_f32 v169, v170, v171\n\t"
            "v_mfma_f32_16x16x32_bf16 v[180:183], v[140:143], v[172:175], v[180:183]\n\t"
            "ds_read_b128 v[172:175], v243 offset:12416\n\t"
            "v_cvt_pk_bf16_f32 v164, v164, v165\n\t"
            "v_mfma_f32_16x16x32_bf16 v[180:183], v[136:139], v[192:195], v[180:183]\n\t"
            "ds_read_b128 v[192:195], v242 offset:12416\n\t"
            "v_cvt_pk_bf16_f32 v165, v166, v167\n\t"
            "s_waitcnt lgkmcnt(2)\n\t"
            "v_mfma_f32_16x16x32_bf16 v[152:155], v[0:3], v[184:187], v[152:155]\n\t"
            "v_cvt_pk_bf16_f32 v146, v148, v149\n\t"
            "v_cvt_pk_bf16_f32 v147, v150, v151\n\t"
            "v_mfma_f32_16x16x32_bf16 v[152:155], v[4:7], v[188:191], v[152:155]\n\t"
            "v_cvt_pk_bf16_f32 v170, v156, v157\n\t"
            "v_cvt_pk_bf16_f32 v171, v158, v159\n\t"
            "v_mfma_f32_16x16x32_bf16 v[160:163], v[28:31], v[188:191], v[160:163]\n\t"
            "v_cvt_pk_bf16_f32 v166, v180, v181\n\t"
            "v_cvt_pk_bf16_f32 v167, v182, v183\n\t"
            "v_mfma_f32_16x16x32_bf16 v[160:163], v[24:27], v[184:187], v[160:163]\n\t"
            "v_lshlrev_b32 v148, 16, v230\n\t"
            "v_and_b32 v149, 0xffff0000, v230\n\t"
            "v_mfma_f32_16x16x32_bf16 v[176:179], v[48:51], v[184:187], v[176:179]\n\t"
            "v_lshlrev_b32 v150, 16, v231\n\t"
            "v_and_b32 v151, 0xffff0000, v231\n\t"
            "v_mfma_f32_16x16x32_bf16 v[176:179], v[52:55], v[188:191], v[176:179]\n\t"
            "ds_read_b128 v[228:231], v237 offset:12288\n\t"
            "v_lshlrev_b32 v156, 16, v232\n\t"
            "v_mfma_f32_16x16x32_bf16 v[148:151], v[76:79], v[188:191], v[148:151]\n\t"
            "v_and_b32 v157, 0xffff0000, v232\n\t"
            "v_lshlrev_b32 v158, 16, v233\n\t"
            "v_mfma_f32_16x16x32_bf16 v[148:151], v[72:75], v[184:187], v[148:151]\n\t"
            "v_and_b32 v159, 0xffff0000, v233\n\t"
            "v_lshlrev_b32 v180, 16, v234\n\t"
            "v_and_b32 v181, 0xffff0000, v234\n\t"
            "v_mfma_f32_16x16x32_bf16 v[156:159], v[96:99], v[184:187], v[156:159]\n\t"
            "v_lshlrev_b32 v182, 16, v235\n\t"
            "v_and_b32 v183, 0xffff0000, v235\n\t"
            "v_mfma_f32_16x16x32_bf16 v[156:159], v[100:103], v[188:191], v[156:159]\n\t"
            "ds_read_b128 v[232:235], v248 offset:12288\n\t"
            "buffer_store_dwordx4 v[144:147], v197, s[40:43], s45 offen\n\t"
            "v_mfma_f32_16x16x32_bf16 v[180:183], v[124:127], v[188:191], v[180:183]\n\t"
            "ds_read_b128 v[188:191], v243 offset:12544\n\t"
            "v_mfma_f32_16x16x32_bf16 v[180:183], v[120:123], v[184:187], v[180:183]\n\t"
            "ds_read_b128 v[184:187], v242 offset:12544\n\t"
            "buffer_store_dwordx4 v[168:171], v197, s[40:43], s45 offen offset:64\n\t"
            "s_waitcnt lgkmcnt(4)\n\t"
            "v_mfma_f32_16x16x32_bf16 v[152:155], v[8:11], v[192:195], v[152:155]\n\t"
            "v_mfma_f32_16x16x32_bf16 v[152:155], v[12:15], v[172:175], v[152:155]\n\t"
            "buffer_store_dwordx4 v[164:167], v197, s[40:43], s45 offen offset:128\n\t"
            "v_mfma_f32_16x16x32_bf16 v[160:163], v[36:39], v[172:175], v[160:163]\n\t"
            "v_mfma_f32_16x16x32_bf16 v[160:163], v[32:35], v[192:195], v[160:163]\n\t"
            "s_waitcnt lgkmcnt(0)\n\t"
            "s_barrier\n\t"
            "s_setprio 3\n\t"
            "v_mfma_f32_16x16x32_bf16 v[176:179], v[56:59], v[192:195], v[176:179]\n\t"
            "v_mfma_f32_16x16x32_bf16 v[176:179], v[60:63], v[172:175], v[176:179]\n\t"
            "v_mfma_f32_16x16x32_bf16 v[148:151], v[84:87], v[172:175], v[148:151]\n\t"
            "v_mfma_f32_16x16x32_bf16 v[148:151], v[80:83], v[192:195], v[148:151]\n\t"
            "v_mfma_f32_16x16x32_bf16 v[156:159], v[104:107], v[192:195], v[156:159]\n\t"
            "v_mfma_f32_16x16x32_bf16 v[156:159], v[108:111], v[172:175], v[156:159]\n\t"
            "v_mfma_f32_16x16x32_bf16 v[152:155], v[16:19], v[184:187], v[152:155]\n\t"
            "v_mfma_f32_16x16x32_bf16 v[152:155], v[20:23], v[188:191], v[152:155]\n\t"
            "v_mfma_f32_16x16x32_bf16 v[160:163], v[44:47], v[188:191], v[160:163]\n\t"
            "v_mfma_f32_16x16x32_bf16 v[160:163], v[40:43], v[184:187], v[160:163]\n\t"
            "v_mfma_f32_16x16x32_bf16 v[180:183], v[132:135], v[172:175], v[180:183]\n\t"
            "v_mfma_f32_16x16x32_bf16 v[180:183], v[128:131], v[192:195], v[180:183]\n\t"
            "v_mfma_f32_16x16x32_bf16 v[176:179], v[64:67], v[184:187], v[176:179]\n\t"
            "v_mfma_f32_16x16x32_bf16 v[176:179], v[68:71], v[188:191], v[176:179]\n\t"
            "s_nop 1\n\t"
            "v_cvt_pk_bf16_f32 v152, v152, v153\n\t"
            "v_mfma_f32_16x16x32_bf16 v[148:151], v[92:95], v[188:191], v[148:151]\n\t"
            "v_cvt_pk_bf16_f32 v153, v154, v155\n\t"
            "v_mfma_f32_16x16x32_bf16 v[148:151], v[88:91], v[184:187], v[148:151]\n\t"
            "v_cvt_pk_bf16_f32 v154, v160, v161\n\t"
            "v_cvt_pk_bf16_f32 v155, v162, v163\n\t"
            "v_mfma_f32_16x16x32_bf16 v[156:159], v[112:115], v[184:187], v[156:159]\n\t"
            "v_cvt_pk_bf16_f32 v176, v176, v177\n\t"
            "v_cvt_pk_bf16_f32 v177, v178, v179\n\t"
            "v_mfma_f32_16x16x32_bf16 v[156:159], v[116:119], v[188:191], v[156:159]\n\t"
            "buffer_store_dwordx4 v[152:155], v198, s[40:43], s45 offen\n\t"
            "v_mfma_f32_16x16x32_bf16 v[180:183], v[140:143], v[188:191], v[180:183]\n\t"
            "v_cvt_pk_bf16_f32 v178, v148, v149\n\t"
            "v_cvt_pk_bf16_f32 v179, v150, v151\n\t"
            "v_mfma_f32_16x16x32_bf16 v[180:183], v[136:139], v[184:187], v[180:183]\n\t"
            "buffer_store_dwordx4 v[176:179], v198, s[40:43], s45 offen offset:64\n\t"
            "s_nop 1\n\t"
            "v_cvt_pk_bf16_f32 v156, v156, v157\n\t"
            "v_cvt_pk_bf16_f32 v157, v158, v159\n\t"
            "s_nop 2\n\t"
            "v_cvt_pk_bf16_f32 v158, v180, v181\n\t"
            "v_cvt_pk_bf16_f32 v159, v182, v183\n\t"
            "buffer_store_dwordx4 v[156:159], v198, s[40:43], s45 offen offset:128\n\t"
            "s_add_u32 s45, s46, s27\n\t"
            "s_mov_b32 s46, s47\n\t"
            "s_mov_b32 s47, s68\n\t"
            "s_xor_b32 s48, s48, s71\n\t"
            "s_xor_b32 s67, s67, s71\n\t"
            "s_add_u32 s76, s76, 4\n\t"
            "s_add_u32 s70, s70, 4\n\t"
            "s_lshl_b32 s56, s56, 2\n\t"
            "s_lshr_b32 s49, s26, 30\n\t"
            "s_or_b32 s56, s56, s49\n\t"
            "s_cmp_lt_u32 s46, 0x7000000\n\t"
            "s_cbranch_scc1 .Lwb_loop%=\n\t"
            "s_branch .Lwb_end%=\n\t"
            ".Lwb_spin%=:\n\t"
            "s_cmp_eq_u32 s83, 0\n\t"
            "s_cbranch_scc1 .Lwb_go%=\n\t"
            "s_sub_u32 s83, s83, 1\n\t"
            "s_sleep 2\n\t"
            "v_mov_b32 v247, 0\n\t"
            "ds_read_b32 v247, v247 offset:7856\n\t"
            "s_waitcnt lgkmcnt(0)\n\t"
            "v_readfirstlane_b32 s77, v247\n\t"
            "s_cmp_ge_u32 s77, s70\n\t"
            "s_cbranch_scc0 .Lwb_spin%=\n\t"
            "s_branch .Lwb_go%=\n\t"
            ".Lwb_end%=:\n\t"
            "s_waitcnt lgkmcnt(0)\n\t"
            "s_nop 7\n\t"
            : "={v[0:3]}"(W[0]), "={v[4:7]}"(W[1]), "={v[8:11]}"(W[2]), "={v[12:15]}"(W[3]), "={v[16:19]}"(W[4]), "={v[20:23]}"(W[5]), "={v[24:27]}"(W[6]), "={v[28:31]}"(W[7]), "={v[32:35]}"(W[8]), "={v[36:39]}"(W[9]), "={v[40:43]}"(W[10]), "={v[44:47]}"(W[11]), "={v[48:51]}"(W[12]), "={v[52:55]}"(W[13]), "={v[56:59]}"(W[14]), "={v[60:63]}"(W[15]), "={v[64:67]}"(W[16]), "={v[68:71]}"(W[17]), "={v[72:75]}"(W[18]), "={v[76:79]}"(W[19]), "={v[80:83]}"(W[20]), "={v[84:87]}"(W[21]), "={v[88:91]}"(W[22]), "={v[92:95]}"(W[23]), "={v[96:99]}"(W[24]), "={v[100:103]}"(W[25]), "={v[104:107]}"(W[26]), "={v[108:111]}"(W[27]), "={v[112:115]}"(W[28]), "={v[116:119]}"(W[29]), "={v[120:123]}"(W[30]), "={v[124:127]}"(W[31]), "={v[128:131]}"(W[32]), "={v[132:135]}"(W[33]), "={v[136:139]}"(W[34]), "={v[140:143]}"(W[35]), "={v[200:203]}"(xl[0]), "={v[204:207]}"(xl[1]), "={v[208:211]}"(xl[2]), "={v[212:215]}"(xl[3]), "={v[216:219]}"(xl[4]), "={v[220:223]}"(xl[5]), "={v[224:227]}"(xl[6]), "={v[228:231]}"(xl[7]), "={v[232:235]}"(xl[8]), B_SIO, "+{v247}"(l16_)
            : "{v242}"(oE_), "{v243}"(oO_), "{v244}"(pR0_), "{v245}"(pR1_), "{v246}"(pR2_), "{v250}"(pXw_), "{v251}"(pSTw_), "{v252}"(aCA_), "{v253}"(aB_), "{v254}"(aA_), "{v255}"(idx_), B_SIN
            : "v144", "v145", "v146", "v147", "v148", "v149", "v150", "v151", "v152", "v153", "v154", "v155", "v156", "v157", "v158", "v159", "v160", "v161", "v162", "v163", "v164", "v165", "v166", "v167", "v168", "v169", "v170", "v171", "v172", "v173", "v174", "v175", "v176", "v177", "v178", "v179", "v180", "v181", "v182", "v183", "v184", "v185", "v186", "v187", "v188", "v189", "v190", "v191", "v192", "v193", "v194", "v195", "v196", "v197", "v198", "v199", "v236", "v237", "v248", "v249", B_SCLOB);
            t45 = s45; t56 = s56;
        }
        {
            int ln_;
            asm volatile("v_mbcnt_lo_u32_b32 %0, -1, 0\n\tv_mbcnt_hi_u32_b32 %0, -1, %0" : "=v"(ln_));
            const int l15_ = ln_ & 15;
            const int g_ = ln_ >> 4;
            KARGS
            const auto orsrc = __builtin_amdgcn_make_buffer_rsrc((void*)ob, 0, IMG * IMG * CH * 2, 0x00020000);
#pragma unroll
            for (int lt = 0; lt < 2; ++lt) {
                int i = 16 * lt + l15_;
                const unsigned en = *(const unsigned*)(smem + WL_OFF + i * 4);
                const bool ok = (en & 0x7ffffffu) != 0x7ffffffu;
                i = ok ? i : 0;
                const int lE = OL_OFF + i * 384 + ((g_ ^ ((i >> 1) & 7)) << 4);
                const unsigned o = ok ? (en & 0x7ffffffu) + LPOS(en >> 31) * ROWBYTES + LPOS(en >> 30) * (CH * 2) + 192 * (w - 2) + 16 * g_ : OOB;
                u32x4 rl[3];
#pragma unroll
                for (int k = 0; k < 3; ++k) {
                    const int c8 = 12 * (w - 2) + 4 * k + g_, ws = (c8 * 43) >> 8, r6 = c8 - 6 * ws;
                    rl[k] = *(const u32x4*)(smem + LX_OFF(i) + (r6 >> 1) * 128 + ((2 * ws + (r6 & 1)) << 4));
                }
                if (lt == 1) {
                    const unsigned oE_ = ldsb + O_OFF + l15_ * 384 + ((g_ ^ (l15_ >> 1)) << 4), oO_ = oE_ ^ 64, aCA_ = ldsb + CT_OFF + ln_ * 16;
                    asm volatile(
            "s_waitcnt lgkmcnt(0)\n\t"
            "s_barrier\n\t"
            "s_nop 7\n\t"
            "ds_read_b128 v[184:187], v242\n\t"
            "ds_read_b128 v[188:191], v243\n\t"
            "ds_read_b128 v[192:195], v242 offset:128\n\t"
            "ds_read_b128 v[172:175], v243 offset:128\n\t"
            "ds_read_b128 v[196:199], v252\n\t"
            "v_lshlrev_b32 v152, 16, v200\n\t"
            "v_and_b32 v153, 0xffff0000, v200\n\t"
            "v_lshlrev_b32 v154, 16, v201\n\t"
            "v_and_b32 v155, 0xffff0000, v201\n\t"
            "v_lshlrev_b32 v156, 16, v202\n\t"
            "v_and_b32 v157, 0xffff0000, v202\n\t"
            "v_lshlrev_b32 v158, 16, v203\n\t"
            "v_and_b32 v159, 0xffff0000, v203\n\t"
            "v_lshlrev_b32 v160, 16, v204\n\t"
            "v_and_b32 v161, 0xffff0000, v204\n\t"
            "v_lshlrev_b32 v162, 16, v205\n\t"
            "v_and_b32 v163, 0xffff0000, v205\n\t"
            "v_lshlrev_b32 v164, 16, v206\n\t"
            "v_and_b32 v165, 0xffff0000, v206\n\t"
            "v_lshlrev_b32 v166, 16, v207\n\t"
            "v_and_b32 v167, 0xffff0000, v207\n\t"
            "v_lshlrev_b32 v176, 16, v208\n\t"
            "v_and_b32 v177, 0xffff0000, v208\n\t"
            "v_lshlrev_b32 v178, 16, v209\n\t"
            "v_and_b32 v179, 0xffff0000, v209\n\t"
            "v_lshlrev_b32 v180, 16, v210\n\t"
            "v_and_b32 v181, 0xffff0000, v210\n\t"
            "v_lshlrev_b32 v182, 16, v211\n\t"
            "v_and_b32 v183, 0xffff0000, v211\n\t"
            "v_lshlrev_b32 v144, 16, v212\n\t"
            "v_and_b32 v145, 0xffff0000, v212\n\t"
            "v_lshlrev_b32 v146, 16, v213\n\t"
            "v_and_b32 v147, 0xffff0000, v213\n\t"
            "v_lshlrev_b32 v148, 16, v214\n\t"
            "v_and_b32 v149, 0xffff0000, v214\n\t"
            "v_lshlrev_b32 v150, 16, v215\n\t"
            "v_and_b32 v151, 0xffff0000, v215\n\t"
            "v_lshlrev_b32 v168, 16, v216\n\t"
            "v_and_b32 v169, 0xffff0000, v216\n\t"
            "v_lshlrev_b32 v170, 16, v217\n\t"
            "v_and_b32 v171, 0xffff0000, v217\n\t"
            "s_bfe_u32 s57, s56, 0x20004\n\t"
            "s_lshl_b32 s58, s57, 3\n\t"
            "s_lshr_b32 s58, s55, s58\n\t"
            "s_lshl_b64 s[36:37], s[34:35], s58\n\t"
            "s_bitcmp1_b32 s57, 1\n\t"
            "s_cselect_b32 s58, 1177344, 0\n\t"
            "s_bitcmp1_b32 s57, 0\n\t"
            "s_cselect_b32 s57, 2304, 0\n\t"
            "s_add_u32 s57, s57, s58\n\t"
            "s_waitcnt lgkmcnt(3)\n\t"
            "v_mfma_f32_16x16x32_bf16 v[152:155], v[0:3], v[184:187], v[152:155]\n\t"
            "s_waitcnt lgkmcnt(0)\n\t"
            "s_mov_b64 exec, s[36:37]\n\t"
            "v_add_u32 v196, s57, v196\n\t"
            "s_mov_b64 exec, -1\n\t"
            "v_mfma_f32_16x16x32_bf16 v[152:155], v[4:7], v[188:191], v[152:155]\n\t"
            "v_mfma_f32_16x16x32_bf16 v[156:159], v[28:31], v[188:191], v[156:159]\n\t"
            "v_mfma_f32_16x16x32_bf16 v[156:159], v[24:27], v[184:187], v[156:159]\n\t"
            "v_mfma_f32_16x16x32_bf16 v[160:163], v[48:51], v[184:187], v[160:163]\n\t"
            "v_mfma_f32_16x16x32_bf16 v[160:163], v[52:55], v[188:191], v[160:163]\n\t"
            "v_mfma_f32_16x16x32_bf16 v[164:167], v[76:79], v[188:191], v[164:167]\n\t"
            "v_mfma_f32_16x16x32_bf16 v[164:167], v[72:75], v[184:187], v[164:167]\n\t"
            "v_mfma_f32_16x16x32_bf16 v[176:179], v[96:99], v[184:187], v[176:179]\n\t"
            "v_mfma_f32_16x16x32_bf16 v[176:179], v[100:103], v[188:191], v[176:179]\n\t"
            "v_mfma_f32_16x16x32_bf16 v[180:183], v[124:127], v[188:191], v[180:183]\n\t"
            "ds_read_b128 v[188:191], v243 offset:256\n\t"
            "v_mfma_f32_16x16x32_bf16 v[180:183], v[120:123], v[184:187], v[180:183]\n\t"
            "ds_read_b128 v[184:187], v242 offset:256\n\t"
            "v_mfma_f32_16x16x32_bf16 v[152:155], v[8:11], v[192:195], v[152:155]\n\t"
            "v_mfma_f32_16x16x32_bf16 v[152:155], v[12:15], v[172:175], v[152:155]\n\t"
            "v_mfma_f32_16x16x32_bf16 v[156:159], v[36:39], v[172:175], v[156:159]\n\t"
            "v_mfma_f32_16x16x32_bf16 v[156:159], v[32:35], v[192:195], v[156:159]\n\t"
            "v_mfma_f32_16x16x32_bf16 v[160:163], v[56:59], v[192:195], v[160:163]\n\t"
            "v_mfma_f32_16x16x32_bf16 v[160:163], v[60:63], v[172:175], v[160:163]\n\t"
            "v_mfma_f32_16x16x32_bf16 v[164:167], v[84:87], v[172:175], v[164:167]\n\t"
            "v_mfma_f32_16x16x32_bf16 v[164:167], v[80:83], v[192:195], v[164:167]\n\t"
            "v_mfma_f32_16x16x32_bf16 v[176:179], v[104:107], v[192:195], v[176:179]\n\t"
            "v_mfma_f32_16x16x32_bf16 v[176:179], v[108:111], v[172:175], v[176:179]\n\t"
            "v_mfma_f32_16x16x32_bf16 v[180:183], v[132:135], v[172:175], v[180:183]\n\t"
            "ds_read_b128 v[172:175], v243 offset:6144\n\t"
            "v_mfma_f32_16x16x32_bf16 v[180:183], v[128:131], v[192:195], v[180:183]\n\t"
            "ds_read_b128 v[192:195], v242 offset:6144\n\t"
            "s_waitcnt lgkmcnt(2)\n\t"
            "v_mfma_f32_16x16x32_bf16 v[152:155], v[16:19], v[184:187], v[152:155]\n\t"
            "v_mfma_f32_16x16x32_bf16 v[152:155], v[20:23], v[188:191], v[152:155]\n\t"
            "v_mfma_f32_16x16x32_bf16 v[156:159], v[44:47], v[188:191], v[156:159]\n\t"
            "v_mfma_f32_16x16x32_bf16 v[156:159], v[40:43], v[184:187], v[156:159]\n\t"
            "v_mfma_f32_16x16x32_bf16 v[160:163], v[64:67], v[184:187], v[160:163]\n\t"
            "v_mfma_f32_16x16x32_bf16 v[160:163], v[68:71], v[188:191], v[160:163]\n\t"
            "v_mfma_f32_16x16x32_bf16 v[164:167], v[92:95], v[188:191], v[164:167]\n\t"
            "v_mfma_f32_16x16x32_bf16 v[164:167], v[88:91], v[184:187], v[164:167]\n\t"
            "s_nop 1\n\t"
            "v_cvt_pk_bf16_f32 v152, v152, v153\n\t"
            "v_mfma_f32_16x16x32_bf16 v[176:179], v[112:115], v[184:187], v[176:179]\n\t"
            "v_cvt_pk_bf16_f32 v153, v154, v155\n\t"
            "v_mfma_f32_16x16x32_bf16 v[176:179], v[116:119], v[188:191], v[176:179]\n\t"
            "v_cvt_pk_bf16_f32 v154, v156, v157\n\t"
            "v_cvt_pk_bf16_f32 v155, v158, v159\n\t"
            "v_mfma_f32_16x16x32_bf16 v[180:183], v[140:143], v[188:191], v[180:183]\n\t"
            "ds_read_b128 v[188:191], v243 offset:6272\n\t"
            "v_cvt_pk_bf16_f32 v160, v160, v161\n\t"
            "v_mfma_f32_16x16x32_bf16 v[180:183], v[136:139], v[184:187], v[180:183]\n\t"
            "ds_read_b128 v[184:187], v242 offset:6272\n\t"
            "v_cvt_pk_bf16_f32 v161, v162, v163\n\t"
            "s_waitcnt lgkmcnt(2)\n\t"
            "v_mfma_f32_16x16x32_bf16 v[144:147], v[0:3], v[192:195], v[144:147]\n\t"
            "v_cvt_pk_bf16_f32 v176, v176, v177\n\t"
            "v_cvt_pk_bf16_f32 v177, v178, v179\n\t"
            "v_mfma_f32_16x16x32_bf16 v[144:147], v[4:7], v[172:175], v[144:147]\n\t"
            "v_cvt_pk_bf16_f32 v162, v164, v165\n\t"
            "v_cvt_pk_bf16_f32 v163, v166, v167\n\t"
            "v_mfma_f32_16x16x32_bf16 v[148:151], v[28:31], v[172:175], v[148:151]\n\t"
            "v_cvt_pk_bf16_f32 v178, v180, v181\n\t"
            "v_cvt_pk_bf16_f32 v179, v182, v183\n\t"
            "v_mfma_f32_16x16x32_bf16 v[148:151], v[24:27], v[192:195], v[148:151]\n\t"
            "v_lshlrev_b32 v156, 16, v218\n\t"
            "v_and_b32 v157, 0xffff0000, v218\n\t"
            "v_mfma_f32_16x16x32_bf16 v[168:171], v[48:51], v[192:195], v[168:171]\n\t"
            "v_lshlrev_b32 v158, 16, v219\n\t"
            "v_and_b32 v159, 0xffff0000, v219\n\t"
            "v_mfma_f32_16x16x32_bf16 v[168:171], v[52:55], v[172:175], v[168:171]\n\t"
            "v_lshlrev_b32 v164, 16, v220\n\t"
            "v_and_b32 v165, 0xffff0000, v220\n\t"
            "v_mfma_f32_16x16x32_bf16 v[156:159], v[76:79], v[172:175], v[156:159]\n\t"
            "v_lshlrev_b32 v166, 16, v221\n\t"
            "v_and_b32 v167, 0xffff0000, v221\n\t"
            "v_mfma_f32_16x16x32_bf16 v[156:159], v[72:75], v[192:195], v[156:159]\n\t"
            "v_lshlrev_b32 v180, 16, v222\n\t"
            "v_and_b32 v181, 0xffff0000, v222\n\t"
            "v_mfma_f32_16x16x32_bf16 v[164:167], v[96:99], v[192:195], v[164:167]\n\t"
            "v_lshlrev_b32 v182, 16, v223\n\t"
            "v_and_b32 v183, 0xffff0000, v223\n\t"
            "v_mfma_f32_16x16x32_bf16 v[164:167], v[100:103], v[172:175], v[164:167]\n\t"
            "buffer_store_dwordx4 v[152:155], v196, s[40:43], s45 offen\n\t"
            "v_mfma_f32_16x16x32_bf16 v[180:183], v[124:127], v[172:175], v[180:183]\n\t"
            "ds_read_b128 v[172:175], v243 offset:6400\n\t"
            "v_lshlrev_b32 v152, 16, v224\n\t"
            "v_mfma_f32_16x16x32_bf16 v[180:183], v[120:123], v[192:195], v[180:183]\n\t"
            "ds_read_b128 v[192:195], v242 offset:6400\n\t"
            "buffer_store_dwordx4 v[160:163], v196, s[40:43], s45 offen offset:64\n\t"
            "s_waitcnt lgkmcnt(2)\n\t"
            "v_mfma_f32_16x16x32_bf16 v[144:147], v[8:11], v[184:187], v[144:147]\n\t"
            "v_and_b32 v153, 0xffff0000, v224\n\t"
            "v_lshlrev_b32 v154, 16, v225\n\t"
            "v_mfma_f32_16x16x32_bf16 v[144:147], v[12:15], v[188:191], v[144:147]\n\t"
            "buffer_store_dwordx4 v[176:179], v196, s[40:43], s45 offen offset:128\n\t"
            "v_mfma_f32_16x16x32_bf16 v[148:151], v[36:39], v[188:191], v[148:151]\n\t"
            "v_and_b32 v155, 0xffff0000, v225\n\t"
            "v_lshlrev_b32 v160, 16, v226\n\t"
            "v_mfma_f32_16x16x32_bf16 v[148:151], v[32:35], v[184:187], v[148:151]\n\t"
            "v_and_b32 v161, 0xffff0000, v226\n\t"
            "v_lshlrev_b32 v162, 16, v227\n\t"
            "v_mfma_f32_16x16x32_bf16 v[168:171], v[56:59], v[184:187], v[168:171]\n\t"
            "v_and_b32 v163, 0xffff0000, v227\n\t"
            "v_lshlrev_b32 v176, 16, v228\n\t"
            "v_mfma_f32_16x16x32_bf16 v[168:171], v[60:63], v[188:191], v[168:171]\n\t"
            "v_and_b32 v177, 0xffff0000, v228\n\t"
            "v_lshlrev_b32 v178, 16, v229\n\t"
            "v_mfma_f32_16x16x32_bf16 v[156:159], v[84:87], v[188:191], v[156:159]\n\t"
            "v_and_b32 v179, 0xffff0000, v229\n\t"
            "v_mfma_f32_16x16x32_bf16 v[156:159], v[80:83], v[184:187], v[156:159]\n\t"
            "v_mfma_f32_16x16x32_bf16 v[164:167], v[104:107], v[184:187], v[164:167]\n\t"
            "v_mfma_f32_16x16x32_bf16 v[164:167], v[108:111], v[188:191], v[164:167]\n\t"
            "v_mfma_f32_16x16x32_bf16 v[180:183], v[132:135], v[188:191], v[180:183]\n\t"
            "ds_read_b128 v[188:191], v243 offset:12288\n\t"
            "v_mfma_f32_16x16x32_bf16 v[180:183], v[128:131], v[184:187], v[180:183]\n\t"
            "ds_read_b128 v[184:187], v242 offset:12288\n\t"
            "s_waitcnt lgkmcnt(2)\n\t"
            "v_mfma_f32_16x16x32_bf16 v[144:147], v[16:19], v[192:195], v[144:147]\n\t"
            "v_mfma_f32_16x16x32_bf16 v[144:147], v[20:23], v[172:175], v[144:147]\n\t"
            "v_mfma_f32_16x16x32_bf16 v[168:171], v[64:67], v[192:195], v[168:171]\n\t"
            "v_mfma_f32_16x16x32_bf16 v[168:171], v[68:71], v[172:175], v[168:171]\n\t"
            "v_mfma_f32_16x16x32_bf16 v[164:167], v[112:115], v[192:195], v[164:167]\n\t"
            "v_mfma_f32_16x16x32_bf16 v[164:167], v[116:119], v[172:175], v[164:167]\n\t"
            "v_mfma_f32_16x16x32_bf16 v[148:151], v[44:47], v[172:175], v[148:151]\n\t"
            "v_mfma_f32_16x16x32_bf16 v[148:151], v[40:43], v[192:195], v[148:151]\n\t"
            "s_nop 1\n\t"
            "v_cvt_pk_bf16_f32 v144, v144, v145\n\t"
            "v_mfma_f32_16x16x32_bf16 v[156:159], v[92:95], v[172:175], v[156:159]\n\t"
            "v_cvt_pk_bf16_f32 v145, v146, v147\n\t"
            "v_mfma_f32_16x16x32_bf16 v[156:159], v[88:91], v[192:195], v[156:159]\n\t"
            "v_cvt_pk_bf16_f32 v168, v168, v169\n\t"
            "v_cvt_pk_bf16_f32 v169, v170, v171\n\t"
            "v_mfma_f32_16x16x32_bf16 v[180:183], v[140:143], v[172:175], v[180:183]\n\t"
            "ds_read_b128 v[172:175], v243 offset:12416\n\t"
            "v_cvt_pk_bf16_f32 v164, v164, v165\n\t"
            "v_mfma_f32_16x16x32_bf16 v[180:183], v[136:139], v[192:195], v[180:183]\n\t"
            "ds_read_b128 v[192:195], v242 offset:12416\n\t"
            "v_cvt_pk_bf16_f32 v165, v166, v167\n\t"
            "s_waitcnt lgkmcnt(2)\n\t"
            "v_mfma_f32_16x16x32_bf16 v[152:155], v[0:3], v[184:187], v[152:155]\n\t"
            "v_cvt_pk_bf16_f32 v146, v148, v149\n\t"
            "v_cvt_pk_bf16_f32 v147, v150, v151\n\t"
            "v_mfma_f32_16x16x32_bf16 v[152:155], v[4:7], v[188:191], v[152:155]\n\t"
            "v_cvt_pk_bf16_f32 v170, v156, v157\n\t"
            "v_cvt_pk_bf16_f32 v171, v158, v159\n\t"
            "v_mfma_f32_16x16x32_bf16 v[160:163], v[28:31], v[188:191], v[160:163]\n\t"
            "v_cvt_pk_bf16_f32 v166, v180, v181\n\t"
            "v_cvt_pk_bf16_f32 v167, v182, v183\n\t"
            "v_mfma_f32_16x16x32_bf16 v[160:163], v[24:27], v[184:187], v[160:163]\n\t"
            "v_lshlrev_b32 v148, 16, v230\n\t"
            "v_and_b32 v149, 0xffff0000, v230\n\t"
            "v_mfma_f32_16x16x32_bf16 v[176:179], v[48:51], v[184:187], v[176:179]\n\t"
            "v_lshlrev_b32 v150, 16, v231\n\t"
            "v_and_b32 v151, 0xffff0000, v231\n\t"
            "v_mfma_f32_16x16x32_bf16 v[176:179], v[52:55], v[188:191], v[176:179]\n\t"
            "v_lshlrev_b32 v156, 16, v232\n\t"
            "v_and_b32 v157, 0xffff0000, v232\n\t"
            "v_mfma_f32_16x16x32_bf16 v[148:151], v[76:79], v[188:191], v[148:151]\n\t"
            "v_lshlrev_b32 v158, 16, v233\n\t"
            "v_and_b32 v159, 0xffff0000, v233\n\t"
            "v_mfma_f32_16x16x32_bf16 v[148:151], v[72:75], v[184:187], v[148:151]\n\t"
            "v_lshlrev_b32 v180, 16, v234\n\t"
            "v_and_b32 v181, 0xffff0000, v234\n\t"
            "v_mfma_f32_16x16x32_bf16 v[156:159], v[96:99], v[184:187], v[156:159]\n\t"
            "v_lshlrev_b32 v182, 16, v235\n\t"
            "v_and_b32 v183, 0xffff0000, v235\n\t"
            "v_mfma_f32_16x16x32_bf16 v[156:159], v[100:103], v[188:191], v[156:159]\n\t"
            "buffer_store_dwordx4 v[144:147], v197, s[40:43], s45 offen\n\t"
            "v_mfma_f32_16x16x32_bf16 v[180:183], v[124:127], v[188:191], v[180:183]\n\t"
            "ds_read_b128 v[188:191], v243 offset:12544\n\t"
            "v_mfma_f32_16x16x32_bf16 v[180:183], v[120:123], v[184:187], v[180:183]\n\t"
            "ds_read_b128 v[184:187], v242 offset:12544\n\t"
            "buffer_store_dwordx4 v[168:171], v197, s[40:43], s45 offen offset:64\n\t"
            "s_waitcnt lgkmcnt(2)\n\t"
            "v_mfma_f32_16x16x32_bf16 v[152:155], v[8:11], v[192:195], v[152:155]\n\t"
            "v_mfma_f32_16x16x32_bf16 v[152:155], v[12:15], v[172:175], v[152:155]\n\t"
            "buffer_store_dwordx4 v[164:167], v197, s[40:43], s45 offen offset:128\n\t"
            "v_mfma_f32_16x16x32_bf16 v[160:163], v[36:39], v[172:175], v[160:163]\n\t"
            "v_mfma_f32_16x16x32_bf16 v[160:163], v[32:35], v[192:195], v[160:163]\n\t"
            "v_mfma_f32_16x16x32_bf16 v[176:179], v[56:59], v[192:195], v[176:179]\n\t"
            "v_mfma_f32_16x16x32_bf16 v[176:179], v[60:63], v[172:175], v[176:179]\n\t"
            "v_mfma_f32_16x16x32_bf16 v[148:151], v[84:87], v[172:175], v[148:151]\n\t"
            "v_mfma_f32_16x16x32_bf16 v[148:151], v[80:83], v[192:195], v[148:151]\n\t"
            "v_mfma_f32_16x16x32_bf16 v[156:159], v[104:107], v[192:195], v[156:159]\n\t"
            "v_mfma_f32_16x16x32_bf16 v[156:159], v[108:111], v[172:175], v[156:159]\n\t"
            "s_waitcnt lgkmcnt(0)\n\t"
            "v_mfma_f32_16x16x32_bf16 v[152:155], v[16:19], v[184:187], v[152:155]\n\t"
            "v_mfma_f32_16x16x32_bf16 v[152:155], v[20:23], v[188:191], v[152:155]\n\t"
            "v_mfma_f32_16x16x32_bf16 v[160:163], v[44:47], v[188:191], v[160:163]\n\t"
            "v_mfma_f32_16x16x32_bf16 v[160:163], v[40:43], v[184:187], v[160:163]\n\t"
            "v_mfma_f32_16x16x32_bf16 v[180:183], v[132:135], v[172:175], v[180:183]\n\t"
            "v_mfma_f32_16x16x32_bf16 v[180:183], v[128:131], v[192:195], v[180:183]\n\t"
            "v_mfma_f32_16x16x32_bf16 v[176:179], v[64:67], v[184:187], v[176:179]\n\t"
            "v_mfma_f32_16x16x32_bf16 v[176:179], v[68:71], v[188:191], v[176:179]\n\t"
            "s_nop 1\n\t"
            "v_cvt_pk_bf16_f32 v152, v152, v153\n\t"
            "v_mfma_f32_16x16x32_bf16 v[148:151], v[92:95], v[188:191], v[148:151]\n\t"
            "v_cvt_pk_bf16_f32 v153, v154, v155\n\t"
            "v_mfma_f32_16x16x32_bf16 v[148:151], v[88:91], v[184:187], v[148:151]\n\t"
            "v_cvt_pk_bf16_f32 v154, v160, v161\n\t"
            "v_cvt_pk_bf16_f32 v155, v162, v163\n\t"
            "v_mfma_f32_16x16x32_bf16 v[156:159], v[112:115], v[184:187], v[156:159]\n\t"
            "v_cvt_pk_bf16_f32 v176, v176, v177\n\t"
            "v_cvt_pk_bf16_f32 v177, v178, v179\n\t"
            "v_mfma_f32_16x16x32_bf16 v[156:159], v[116:119], v[188:191], v[156:159]\n\t"
            "buffer_store_dwordx4 v[152:155], v198, s[40:43], s45 offen\n\t"
            "v_mfma_f32_16x16x32_bf16 v[180:183], v[140:143], v[188:191], v[180:183]\n\t"
            "v_cvt_pk_bf16_f32 v178, v148, v149\n\t"
            "v_cvt_pk_bf16_f32 v179, v150, v151\n\t"
            "v_mfma_f32_16x16x32_bf16 v[180:183], v[136:139], v[184:187], v[180:183]\n\t"
            "buffer_store_dwordx4 v[176:179], v198, s[40:43], s45 offen offset:64\n\t"
            "s_nop 1\n\t"
            "v_cvt_pk_bf16_f32 v156, v156, v157\n\t"
            "v_cvt_pk_bf16_f32 v157, v158, v159\n\t"
            "s_nop 2\n\t"
            "v_cvt_pk_bf16_f32 v158, v180, v181\n\t"
            "v_cvt_pk_bf16_f32 v159, v182, v183\n\t"
            "buffer_store_dwordx4 v[156:159], v198, s[40:43], s45 offen offset:128\n\t"
            "s_nop 7\n\t"
                    :: "{v[0:3]}"(W[0]), "{v[4:7]}"(W[1]), "{v[8:11]}"(W[2]), "{v[12:15]}"(W[3]), "{v[16:19]}"(W[4]), "{v[20:23]}"(W[5]), "{v[24:27]}"(W[6]), "{v[28:31]}"(W[7]), "{v[32:35]}"(W[8]), "{v[36:39]}"(W[9]), "{v[40:43]}"(W[10]), "{v[44:47]}"(W[11]), "{v[48:51]}"(W[12]), "{v[52:55]}"(W[13]), "{v[56:59]}"(W[14]), "{v[60:63]}"(W[15]), "{v[64:67]}"(W[16]), "{v[68:71]}"(W[17]), "{v[72:75]}"(W[18]), "{v[76:79]}"(W[19]), "{v[80:83]}"(W[20]), "{v[84:87]}"(W[21]), "{v[88:91]}"(W[22]), "{v[92:95]}"(W[23]), "{v[96:99]}"(W[24]), "{v[100:103]}"(W[25]), "{v[104:107]}"(W[26]), "{v[108:111]}"(W[27]), "{v[112:115]}"(W[28]), "{v[116:119]}"(W[29]), "{v[120:123]}"(W[30]), "{v[124:127]}"(W[31]), "{v[128:131]}"(W[32]), "{v[132:135]}"(W[33]), "{v[136:139]}"(W[34]), "{v[140:143]}"(W[35]), "{v[200:203]}"(xl[0]), "{v[204:207]}"(xl[1]), "{v[208:211]}"(xl[2]), "{v[212:215]}"(xl[3]), "{v[216:219]}"(xl[4]), "{v[220:223]}"(xl[5]), "{v[224:227]}"(xl[6]), "{v[228:231]}"(xl[7]), "{v[232:235]}"(xl[8]), "{v242}"(oE_), "{v243}"(oO_), "{v252}"(aCA_),
                       "{s[40:43]}"(orr), "{s45}"(t45), "{s56}"(t56), "{s55}"(CROW & 0x0F0F0F0Fu), "{s[34:35]}"(0x0001000100010001ull)
                    : "v144", "v145", "v146", "v147", "v148", "v149", "v150", "v151", "v152", "v153", "v154", "v155", "v156", "v157", "v158", "v159", "v160", "v161", "v162", "v163", "v164", "v165", "v166", "v167", "v168", "v169", "v170", "v171", "v172", "v173", "v174", "v175", "v176", "v177", "v178", "v179", "v180", "v181", "v182", "v183", "v184", "v185", "v186", "v187", "v188", "v189", "v190", "v191", "v192", "v193", "v194", "v195", "v196", "v197", "v198", "v199", "s36", "s37", "s57", "s58", "scc", "memory");
                }
                f32x4 acc[6];
#pragma unroll
                for (int ks = 0; ks < 6; ++ks) {
                    const bf16x8 of = *(const bf16x8*)(smem + ((ks & 1) ? (lE ^ 64) : lE) + (ks >> 1) * 128);
#pragma unroll
                    for (int j = 0; j < 6; ++j) acc[j] = MFMA32(W[j * 6 + ks], of, ks == 0 ? ZERO4 : acc[j]);
                }
#pragma unroll
                for (int k = 0; k < 3; ++k) {
                    const f32x4 a = acc[2 * k], b = acc[2 * k + 1];
                    const u32x4 ov = {pk2(a[0] + bflo(rl[k][0]), a[1] + bfhi(rl[k][0])), pk2(a[2] + bflo(rl[k][1]), a[3] + bfhi(rl[k][1])),
                                      pk2(b[0] + bflo(rl[k][2]), b[1] + bfhi(rl[k][2])), pk2(b[2] + bflo(rl[k][3]), b[3] + bfhi(rl[k][3]))};
                    __builtin_amdgcn_raw_buffer_store_b128(ov, orsrc, o + 64 * k, 0, 0);
                }
                SB();
            }
        }
        { unsigned r1_ = (unsigned)__builtin_amdgcn_s_memrealtime(); unsigned long long ka_; asm volatile("s_load_dwordx2 %0, %1, 0x20\n\ts_waitcnt lgkmcnt(0)" : "=s"(ka_) : "s"(__builtin_amdgcn_kernarg_segment_ptr()) : "memory");
          unsigned* dbg = (unsigned*)(ka_ + (1 << 23)); int l_; asm volatile("v_mbcnt_lo_u32_b32 %0, -1, 0\n\tv_mbcnt_hi_u32_b32 %0, -1, %0" : "=v"(l_)); if (l_ == 0) dbg[(blockIdx.x * 8 + wave) * 8 + 5] = r1_; }
        __builtin_amdgcn_endpgm();
    }
}

extern "C" void kernel_launch(void* const* d_in, const int* in_sizes, int n_in,
                              void* d_out, int out_size, void* d_ws, size_t ws_size,
                              hipStream_t stream) {
    const unsigned short* x = (const unsigned short*)d_in[0];
    const unsigned short* dw = (const unsigned short*)d_in[1];
    const unsigned short* wqkv = (const unsigned short*)d_in[2];
    const unsigned short* wo = (const unsigned short*)d_in[3];
    unsigned short* out = (unsigned short*)d_out;
    u32x4* wf = (u32x4*)d_ws;
    win_attn_kernel<<<GRID, 512, 0, stream>>>(x, dw, wqkv, wo, wf, out);
}
```

```cpp
#include <hip/hip_runtime.h>
#include <hip/hip_bf16.h>
#include <stdint.h>

typedef short bf16x8 __attribute__((ext_vector_type(8)));
typedef float f32x4 __attribute__((ext_vector_type(4)));
typedef float f32x2 __attribute__((ext_vector_type(2)));
typedef unsigned u32x2 __attribute__((ext_vector_type(2)));
typedef unsigned u32x4 __attribute__((ext_vector_type(4)));

constexpr int IMG = 511;
constexpr int CH = 192;
constexpr int NWX = 73;
constexpr int NWIN = NWX * NWX;
constexpr int GRID = 256;
constexpr int ROWBYTES = IMG * CH * 2;
constexpr int XBIAS = (IMG + 1) * CH * 2;
constexpr unsigned OOB = 0x40000000u;

constexpr int FLAG_OFF = 7856;
constexpr int X_OFF = 32768;
constexpr int X1_OFF = 53632;
constexpr int O_OFF = 74496;
constexpr int V_OFF = 92928;
constexpr int KQL_OFF = 111360;
constexpr int OL_OFF = 128256;
constexpr int AT_OFF = 137472;
constexpr int CT_OFF = AT_OFF + 19584;
constexpr int RS_OFF = CT_OFF + 4096;
constexpr int WL_OFF = RS_OFF + 2304 + 64;
constexpr int LDS_BYTES = WL_OFF + 128;
#define RS_BASE (RS_OFF + (wave < 4 ? wave * 384 : 768 + wave * 192))
constexpr int STL_OFF = O_OFF + 12288;
constexpr int DW_OFF = O_OFF + 8448;
#define LX_OFF(i) ((i) < 2 ? CT_OFF + 1024 + (i) * 384 : KQL_OFF + ((i) - 2) * 800)
constexpr int PF_OFF = CT_OFF + 2048;
constexpr int PSLEEP = 4;
#define LB_OFF (w < 3 ? V_OFF + w * 13952 : X1_OFF)
#define LPOS(wc) (((wc) & 1) ? 0 : 6)
constexpr unsigned CROW = 0x060a0230u;
constexpr unsigned CLANE = 0x00132C3Fu;

constexpr unsigned long long TOKMAP[6] = {0x20001c190f2a2202ull, 0x052517121f061a13ull, 0x181e08151d0b260cull, 0x1b2b2903102d0901ull, 0x04162e0e2824142cull, 0x110a072127232f0dull};
constexpr unsigned long long ROWMAP[8] = {0x08321b240f1c0006ull, 0x23310e2a09170d14ull, 0x210c2f030a273418ull, 0x1d2b11221f043739ull, 0x3234363a103e192dull, 0x291a20022c3f1605ull, 0x303a3c2628122e15ull, 0x39253d1e01070b13ull};

constexpr int WF_BYTES = 8 * 36 * 1024;
constexpr int FLG_OFF = WF_BYTES;
constexpr int ARR_OFF = FLG_OFF + 512;
constexpr int EPO_OFF = ARR_OFF + 8192;
constexpr int WS_BYTES = EPO_OFF + 128;
constexpr int SC1 = 16;

#define MFMA32(a, b, c) __builtin_amdgcn_mfma_f32_16x16x32_bf16((a), (b), (c), 0, 0, 0)
#define BC8(v) __builtin_bit_cast(bf16x8, (v))
#define BAR() asm volatile("s_waitcnt lgkmcnt(0)\n\ts_barrier" ::: "memory")
#define SB() __builtin_amdgcn_sched_barrier(0)
#define OPQ(x) asm volatile("" : "+v"(x))
#define ZERO4 ((f32x4){0.f, 0.f, 0.f, 0.f})

typedef __bf16 nbf16x2 __attribute__((ext_vector_type(2)));
__device__ __forceinline__ unsigned pk2(float a, float b) {
    f32x2 v = {a, b};
    nbf16x2 r = __builtin_convertvector(v, nbf16x2);
    return __builtin_bit_cast(unsigned, r);
}
__device__ __forceinline__ float bflo(unsigned u) { return __uint_as_float(u << 16); }
__device__ __forceinline__ float bfhi(unsigned u) { return __uint_as_float(u & 0xffff0000u); }

__device__ __forceinline__ float bf2f(unsigned short v) { return __uint_as_float((unsigned)v << 16); }
__device__ __forceinline__ unsigned byte_of(unsigned long long a, unsigned long long b, int i) {
    return (unsigned)(((i & 8) ? b : a) >> (8 * (i & 7))) & 0xFFu;
}
#define M24(a, b) ((unsigned)__mul24((int)(a), (int)(b)))
__device__ __forceinline__ void seq_to_win(int seq, int& wy, int& wx) {
    const bool last = seq >= 5110;
    const int s = last ? 35 : M24(seq, 28729) >> 22, r = seq - M24(146, s);
    const int kx = last ? M24(r, 171) >> 10 : r >> 2, pos = r - M24(last ? 6 : 4, kx);
    const bool low = last && pos >= (kx == 36 ? 2 : 4);
    wy = low ? 72 : 2 * s + (pos & 1);
    wx = 2 * kx + (low ? (kx == 36 ? 0 : pos - 4) : pos >> 1);
}
__device__ __forceinline__ int win_to_seq(int wy, int wx) {
    const int kx = wx >> 1;
    return M24(146, wy == 72 ? 35 : wy >> 1) + M24(wy >= 70 ? 6 : 4, kx) + (wy == 72 ? (kx == 36 ? 2 : 4 + (wx & 1)) : 2 * (wx & 1) + (wy & 1));
}
__device__ __forceinline__ int block_of(int wy, int wx) { return wy >= 70 ? 1295 + (wx & ~1) + (wy == 72) : M24(37, wy >> 1) + (wx >> 1); }
__device__ __forceinline__ void block_pos(int b, int& ky, int& kx) {
    const int s = M24(b, 1772) >> 16, l = b - 1295;
    kx = l >= 0 ? l >> 1 : b - M24(37, s);
    ky = l >= 0 ? 70 + 2 * (l & 1) : 2 * s;
}
__device__ __forceinline__ unsigned win_org(int wy, int wx) { return M24(wy, 7 * ROWBYTES) + M24(wx, 7 * CH * 2); }
__device__ __forceinline__ unsigned wl_entry(int wy, int wx) {
    return win_org(wy, wx) | (wy == 0 ? 0x08000000u : 0u) | (wx == 0 ? 0x10000000u : 0u) |
           (wx == NWX - 1 ? 0x20000000u : 0u) | ((unsigned)(wx & 1) << 30) | ((unsigned)(wy & 1) << 31);
}
__device__ __forceinline__ unsigned dma_off(int lane) {
    const int col = (lane * 43) >> 8;
    return (unsigned)(col * (CH * 2 - 96) + 16 * lane) | (col == 0 ? 0x10000000u : 0u) | (col == 8 ? 0x20000000u : 0u);
}

#if defined(__HIP_DEVICE_COMPILE__)
#define NOPK __attribute__((target("no-packed-fp32-ops")))
#else
#define NOPK
#endif
__global__ __launch_bounds__(512, 2) NOPK void win_attn_kernel(const unsigned short* __restrict__ x,
                                                          const unsigned short* __restrict__ dw, const unsigned short* __restrict__ wqkv,
                                                          const unsigned short* __restrict__ wo, u32x4* wf,
                                                          unsigned short* __restrict__ out) {
    __shared__ __attribute__((aligned(16))) char smem[LDS_BYTES];

    asm volatile("wk_code_begin:");
    const int tid = threadIdx.x;
    const int wave = __builtin_amdgcn_readfirstlane(tid >> 6);
    const int w = wave & 3;
    const unsigned ldsb = (unsigned)(size_t)smem;
    {
        unsigned long long pc;
        unsigned len, rel;
        asm volatile("s_getpc_b64 %0\n.Lwk_here%=:\n\ts_mov_b32 %1, wk_code_end-wk_code_begin\n\ts_mov_b32 %2, .Lwk_here%=-wk_code_begin" : "=s"(pc), "=s"(len), "=s"(rel));
        pc -= rel;
        const u32x4 cr = {(unsigned)pc, (unsigned)(pc >> 32) & 0xffffu, len, 0x00020000u};
        asm volatile("s_mov_b32 m0, %0\n\ts_nop 0\n\tbuffer_load_dwordx4 %1, %2, %3 offen lds"
                     :: "s"(ldsb + X1_OFF + 14336 + w * 1024), "v"((tid & 63) * 16), "s"(cr), "s"((wave * 32 + (blockIdx.x >> 3)) * 1024) : "m0", "memory");
    }
    asm volatile("" :: "s"(x), "s"(dw), "s"(wqkv), "s"(wo), "s"(wf));
    const auto wrsrc = __builtin_amdgcn_make_buffer_rsrc((void*)wf, 0, WS_BYTES, 0x00020000);
    unsigned tag_lo = 0, tag_hi = 0;
#define TAG_READY                                                                                  \
    {                                                                                              \
        unsigned long long tag_;                                                                   \
        asm volatile("s_load_dwordx2 %0, %1, %2\n\ts_waitcnt lgkmcnt(0)" : "=s"(tag_) : "s"(wf), "n"(EPO_OFF) : "memory"); \
        tag_lo = (unsigned)tag_ + 1u; tag_hi = (unsigned)(tag_ >> 32);                             \
    }
    if (blockIdx.x < 36) {
        const int lane = tid & 63, n = lane & 15, g = lane >> 4;
        const bool isO = wave >= 6;
        const int fr = isO ? 216 + 2 * blockIdx.x + (wave - 6) : 6 * blockIdx.x + wave;
        const int tile = fr / 6, ks = fr - 6 * tile, tw = tile / 6, mdt = tile - 6 * tw;
        const int col = isO ? 96 * (tw - 6) + 32 * (mdt >> 1) + 8 * (n >> 2) + 4 * (mdt & 1) + (n & 3)
                            : (tw < 4 ? (mdt < 3 ? 192 : -48) + 48 * tw : 96 * tw) + 16 * mdt + n;
        const int rb = isO ? 384 : 1152;
        const auto rr = __builtin_amdgcn_make_buffer_rsrc((void*)(isO ? wo : wqkv), 0, 192 * rb, 0x00020000);
        const int vo = (48 * (g >> 1) + 8 * (g & 1)) * rb + 2 * col;
        float v[8];
#pragma unroll
        for (int j = 0; j < 8; ++j) v[j] = bf2f(__builtin_amdgcn_raw_buffer_load_b16(rr, vo, (96 * (ks & 1) + 16 * (ks >> 1) + j) * rb, 0));
        TAG_READY
        float* red = (float*)smem;
        red[n * 36 + wave * 4 + g] = ((v[0] + v[1]) + (v[2] + v[3])) + ((v[4] + v[5]) + (v[6] + v[7]));
        BAR();
        float mean = 0.f;
#pragma unroll
        for (int i = 0; i < 6; ++i) {
            const f32x4 p = *(const f32x4*)(red + n * 36 + 4 * i);
            mean += (p[0] + p[1]) + (p[2] + p[3]);
        }
        mean = isO ? 0.f : mean * (1.f / 192.f);
        u32x4 o;
#pragma unroll
        for (int j = 0; j < 4; ++j) o[j] = pk2(v[2 * j] - mean, v[2 * j + 1] - mean);
        __builtin_amdgcn_raw_buffer_store_b128(o, wrsrc, lane * 16, fr * 1024, SC1);
        asm volatile("s_waitcnt vmcnt(0)" ::: "memory");
        BAR();
        if (tid == 0) __builtin_amdgcn_raw_buffer_store_b64((u32x2){tag_lo, tag_hi}, wrsrc, FLG_OFF + blockIdx.x * 8, 0, SC1);
    }
    const int xi = blockIdx.x >> 3, nshort = (blockIdx.x & 7) == 7 ? 5 : 6;
    const int start = 666 * (blockIdx.x & 7) + 21 * xi - (xi < nshort ? xi : nshort);
    const int cnt = xi < nshort ? 20 : 21;

    const auto xrsrc = __builtin_amdgcn_make_buffer_rsrc((void*)((const char*)x - XBIAS), 0, IMG * IMG * CH * 2 + XBIAS, 0x00020000);
    bf16x8 W[36];
#define POLL_ISSUE                                                                                  \
    asm volatile("s_mov_b32 m0, %0\n\ts_mov_b64 exec, 0x3ffff\n\tbuffer_load_dwordx4 %1, %2, %3 offen sc1 lds\n\ts_mov_b64 exec, -1" \
                 :: "s"(ldsb + PF_OFF + w * 512), "v"(lane * 16), "s"(wrsrc), "s"(FLG_OFF) : "m0", "memory");
#define POLL_W                                                                                      \
    {                                                                                               \
        const int fi_ = lane < 36 ? lane : 0;                                                       \
        for (int it_ = 0; it_ < 100000; ++it_) {                                                    \
            u32x2 f_;                                                                               \
            if (it_ < 48) asm volatile("ds_read_b64 %0, %1\n\ts_waitcnt lgkmcnt(0)" : "=v"(f_) : "v"(ldsb + PF_OFF + w * 512 + fi_ * 8) : "memory"); \
            else f_ = __builtin_amdgcn_raw_buffer_load_b64(wrsrc, FLG_OFF + fi_ * 8, 0, SC1);       \
            if (__builtin_amdgcn_ballot_w64(f_[0] != tag_lo || f_[1] != tag_hi) == 0ull) break;     \
            if (it_ < 47) POLL_ISSUE                                                                \
            __builtin_amdgcn_s_sleep(PSLEEP);                                                       \
        }                                                                                           \
        __builtin_amdgcn_fence(__ATOMIC_ACQUIRE, "wavefront");                                      \
        SB();                                                                                       \
    }
#define LANE_SETUP                                                                                 \
    int lane;                                                                                      \
    asm volatile("v_mbcnt_lo_u32_b32 %0, -1, 0\n\tv_mbcnt_hi_u32_b32 %0, -1, %0" : "=v"(lane));    \
    __builtin_assume(lane >= 0 && lane < 64);                                                      \
    const int g = lane >> 4, l15 = lane & 15, sw = l15 >> 1;                                       \
    int fE = X_OFF + l15 * 384 + ((g ^ sw) << 4);                                                  \
    OPQ(fE);

#define DMA_SLICE(en)                                                                              \
        {                                                                                          \
            const unsigned vo_ = dma_off(lane) & (0x401fffffu | ((en) & 0x38000000u));             \
            const unsigned so_ = ((en) & 0x7ffffffu) + 96 * w;                                     \
            _Pragma("unroll") for (int r = 0; r < 9; ++r) {                                        \
                unsigned m0_ = ldsb + w * 8192 + r * 872;                                          \
                unsigned sr_ = (r == 0 && ((en) & 0x08000000u)) ? OOB : so_ + r * ROWBYTES;        \
                asm volatile("s_mov_b32 m0, %0\n\ts_mov_b32 exec_hi, 0x3fffff\n\tbuffer_load_dwordx4 %1, %2, %3 offen lds\n\ts_mov_b32 exec_hi, -1" \
                             :: "s"(m0_), "v"(vo_), "s"(xrsrc), "s"(sr_) : "m0", "memory");        \
            }                                                                                      \
        }
#define XPAT(r) (((r) < 48 ? (r) : 48) * 384 + (((2 * w) ^ (((r) >> 1) & 7)) << 4))
#define SPAT(r) ((w >> 1) * 1024 + (w & 1) * 8 + (r) * 16)
#define CONV_CONSTS(var) \
            const unsigned long long rlo = g == 0 ? ROWMAP[0] : (g == 1 ? ROWMAP[2] : (g == 2 ? ROWMAP[4] : ROWMAP[6])); \
            const unsigned long long rhi = g == 0 ? ROWMAP[1] : (g == 1 ? ROWMAP[3] : (g == 2 ? ROWMAP[5] : ROWMAP[7])); \
            int xrow = byte_of(rlo, rhi, l15); \
            if (lane == (int)((CLANE >> (8 * (var))) & 0xFF)) xrow = 48; \
            if (lane == 63) xrow = (CROW >> (8 * (var))) & 0xFF; \
            const unsigned pXw_ = XPAT(xrow); \
            const unsigned pSTw_ = (w >> 1) * 1024 + (w & 1) * 8 + xrow * 16; \
            const int oy = (0x5320 >> (4 * (l15 >> 2))) & 15, ox = (0x5320 >> (4 * (l15 & 3))) & 15; \
            const unsigned aB_ = ldsb + w * 8192 + (oy + (g >> 1)) * 872 + (ox + 2 * (g & 1)) * 96; \
            const int dyi = g - (l15 >> 3); \
            const unsigned aA_ = ldsb + AT_OFF + (l15 & 3) * 16 + \
                                 ((dyi < 0 || dyi > 2) ? 18816 : (dyi == 0 ? 0 : (dyi == 1 ? 6208 : 12544)) + ((l15 >> 2) & 1) * 3072 + w * 768); \
            const unsigned idx_ = (l15 & 3) * 0x5555u; \
            const unsigned se = (l15 == 4 * g) ? 0x3F803F80u : 0u; \
            const u32x4 sel4 = {se, se, se, se};

    unsigned en0, en1;
    {
        LANE_SETUP
        const int px = (lane * 43) >> 8;

        {
            const auto dr = __builtin_amdgcn_make_buffer_rsrc((void*)dw, 0, 9 * 384, 0x00020000);
            unsigned m0_ = ldsb + DW_OFF + w * 864, vo_ = px * 288 + lane * 16;
            asm volatile("s_mov_b32 m0, %0\n\ts_mov_b32 exec_hi, 0x3fffff\n\tbuffer_load_dwordx4 %1, %2, %3 offen lds\n\ts_mov_b32 exec_hi, -1"
                         :: "s"(m0_), "v"(vo_), "s"(dr), "s"(96 * w) : "m0", "memory");
        }
        int wy, wx;
        seq_to_win(start + (lane < cnt ? lane : cnt - 1), wy, wx);
        const unsigned en = wl_entry(wy, wx);
        const int bk = block_of(wy, wx);
        en0 = __builtin_amdgcn_readlane(en, 0);
        en1 = __builtin_amdgcn_readlane(en, 1);
        const int key0 = __builtin_amdgcn_readlane(bk, 0), nblk = __builtin_amdgcn_readlane(bk, 63) - key0 + 1;
        int ky, kx;
        block_pos(key0 + (l15 < nblk ? l15 : 0), ky, kx);
        if (wave < 4) {
            DMA_SLICE(en0)
        } else {
            const unsigned org = l15 < nblk ? (win_org(ky, 2 * kx) + (5 * ROWBYTES + 5 * CH * 2 + XBIAS)) | (kx == 36 ? OOB : 0u) : OOB + 0x8000000u;
            const unsigned lo = (px & 3) * (CH * 2) + (lane - 6 * px) * 16, keep = (px & 2) ? ~0u : ~OOB;
#pragma unroll
            for (int p = 0; p < 4; ++p) {
                if (p < 3 || nblk > 6) {
                    const unsigned oa = __builtin_amdgcn_readlane(org, 2 * p), ob = __builtin_amdgcn_readlane(org, 2 * p + 1);
                    const unsigned vo_ = ((lane < 24 ? oa : ob) & keep) + lo;
#pragma unroll
                    for (int r = 0; r < 4; ++r) {
                        unsigned m0_ = ldsb + LB_OFF + (4 * p + r) * 872;
                        asm volatile("s_mov_b32 m0, %0\n\ts_mov_b32 exec_hi, 0xffff\n\tbuffer_load_dwordx4 %1, %2, %3 offen lds\n\ts_mov_b32 exec_hi, -1"
                                     :: "s"(m0_), "v"(vo_), "s"(xrsrc), "s"(96 * w + r * ROWBYTES) : "m0", "memory");
                    }
                }
            }
        }
        SB();
        if (wave == 2) {
            u32x4 so;
#pragma unroll
            for (int e = 0; e < 3; ++e) {
                const int tk = byte_of(TOKMAP[2 * e], TOKMAP[2 * e + 1], l15);
                const int ty = (tk * 37) >> 8, tx = tk - 7 * ty;
                so[e] = (unsigned)(ty * ROWBYTES + tx * (CH * 2)) + 16 * g;
            }
            so[3] = dma_off(lane);
            *(u32x4*)(smem + CT_OFF + lane * 16) = so;
        }
        if (wave == 0 && lane < 32) *(unsigned*)(smem + WL_OFF + lane * 4) = lane < cnt ? en : 0x7ffffffu;
        if (wave == 1 && lane == 0) *(u32x2*)(smem + FLAG_OFF) = (u32x2){0u, 0u};
        if (lane < 48) *(u32x4*)(smem + AT_OFF + 18816 + lane * 16) = (u32x4){0u, 0u, 0u, 0u};
        CONV_CONSTS(en0 >> 30)
        const int ly = ky + (g >> 1), lx = 2 * kx + (g & 1), li = win_to_seq(ly, lx) - start;
        const bool ok = ly < NWX && lx < NWX && li >= 0 && li < cnt && l15 < nblk;
        const int row = ok ? li : 47, b = l15 < nblk ? l15 : 0;
        const unsigned cB = wave < 4 ? aB_ : ldsb + LB_OFF + (4 * (b >> 1) + (g >> 1)) * 872 + (b & 1) * 384 + (g & 1) * 192;
        const unsigned cX = wave < 4 ? (lane == (int)((CLANE >> (8 * (en0 >> 30))) & 0xFF) ? LX_OFF(0) - X_OFF + 32 * w : pXw_) : XPAT(row), cS = wave < 4 ? pSTw_ : SPAT(row) + (unsigned)(STL_OFF - O_OFF - 18816);
        if (wave < 4) {
            TAG_READY
            *(u32x2*)(smem + PF_OFF + w * 512 + lane * 8) = (u32x2){~tag_lo, 0u};
        }
        asm volatile("s_waitcnt vmcnt(9)" ::: "memory");
        if (lane < 48) {
            unsigned short dv[9];
#pragma unroll
            for (int i = 0; i < 9; ++i) dv[i] = *(const unsigned short*)(smem + DW_OFF + w * 864 + i * 96 + lane * 2);
#pragma unroll
            for (int dy = 0; dy < 3; ++dy) {
                const unsigned a = dv[3 * dy], b2 = dv[3 * dy + 1] | (dy == 1 ? 0x3F800000u : 0u), c = dv[3 * dy + 2];
                char* p = smem + AT_OFF + (dy == 0 ? 0 : (dy == 1 ? 6208 : 12544)) + w * 768 + lane * 16;
                *(u32x4*)p = (u32x4){a, b2, c, 0u};
                *(u32x4*)(p + 3072) = (u32x4){0u, a, b2, c};
            }
        }
        asm volatile("s_waitcnt vmcnt(0) lgkmcnt(0)" ::: "memory");
        if (wave < 4) POLL_ISSUE
        asm volatile(
            "s_mov_b32 m0, s73\n\t"
            "s_cmp_eq_u32 s73, 0\n\t"
            "s_nop 7\n\t"
            "ds_read_b128 v[168:171], v254\n\t"
            "ds_read_b64 v[144:145], v253\n\t"
            "ds_read_b64 v[146:147], v253 offset:96\n\t"
            "ds_read_b64 v[148:149], v253 offset:1744\n\t"
            "ds_read_b64 v[150:151], v253 offset:1840\n\t"
            "ds_read_b128 v[172:175], v254 offset:64\n\t"
            "ds_read_b64 v[152:153], v253 offset:8\n\t"
            "ds_read_b64 v[154:155], v253 offset:104\n\t"
            "ds_read_b64 v[156:157], v253 offset:1752\n\t"
            "ds_read_b64 v[158:159], v253 offset:1848\n\t"
            "ds_read_b64 v[160:161], v253 offset:16\n\t"
            "ds_read_b64 v[162:163], v253 offset:112\n\t"
            "ds_read_b64 v[164:165], v253 offset:1760\n\t"
            "ds_read_b64 v[166:167], v253 offset:1856\n\t"
            "v_mov_b64 v[176:177], 0\n\t"
            "v_mov_b64 v[178:179], 0\n\t"
            "v_mov_b64 v[180:181], 0\n\t"
            "v_mov_b64 v[182:183], 0\n\t"
            "v_mov_b64 v[184:185], 0\n\t"
            "v_mov_b64 v[186:187], 0\n\t"
            "v_mov_b64 v[188:189], 0\n\t"
            "v_mov_b64 v[190:191], 0\n\t"
            "v_add_u32 v236, s67, v250\n\t"
            "v_xor_b32 v237, 16, v236\n\t"
            "v_add_u32 v248, s67, v251\n\t"
            "s_waitcnt lgkmcnt(9)\n\t"
            "v_smfmac_f32_16x16x64_bf16 v[176:179], v[168:171], v[144:151], v255\n\t"
            "ds_read_b128 v[168:171], v254 offset:128\n\t"
            "ds_read_b64 v[144:145], v253 offset:24\n\t"
            "s_waitcnt lgkmcnt(6)\n\t"
            "v_smfmac_f32_16x16x64_bf16 v[180:183], v[172:175], v[152:159], v255\n\t"
            "ds_read_b64 v[146:147], v253 offset:120\n\t"
            "ds_read_b64 v[148:149], v253 offset:1768\n\t"
            "ds_read_b64 v[150:151], v253 offset:1864\n\t"
            "ds_read_b128 v[172:175], v254 offset:192\n\t"
            "ds_read_b64 v[152:153], v253 offset:32\n\t"
            "ds_read_b64 v[154:155], v253 offset:128\n\t"
            "ds_read_b64 v[156:157], v253 offset:1776\n\t"
            "ds_read_b64 v[158:159], v253 offset:1872\n\t"
            "v_cvt_pk_bf16_f32 v176, v176, v177\n\t"
            "v_cvt_pk_bf16_f32 v177, v178, v179\n\t"
            "v_cvt_pk_bf16_f32 v178, v180, v181\n\t"
            "v_cvt_pk_bf16_f32 v179, v182, v183\n\t"
            "v_mfma_f32_4x4x4_16b_bf16 v[196:199], v[176:177], v[176:177], 0\n\t"
            "ds_write_b128 v236, v[176:179]\n\t"
            "v_mfma_f32_16x16x32_bf16 v[192:195], v[238:241], v[176:179], 0\n\t"
            "v_mov_b64 v[176:177], 0\n\t"
            "v_mov_b64 v[180:181], 0\n\t"
            "v_mfma_f32_4x4x4_16b_bf16 v[196:199], v[178:179], v[178:179], v[196:199]\n\t"
            "v_mov_b64 v[178:179], 0\n\t"
            "v_mov_b64 v[182:183], 0\n\t"
            "s_waitcnt lgkmcnt(10)\n\t"
            "v_smfmac_f32_16x16x64_bf16 v[184:187], v[168:171], v[160:167], v255\n\t"
            "ds_read_b128 v[168:171], v254 offset:256\n\t"
            "ds_read_b64 v[160:161], v253 offset:40\n\t"
            "ds_read_b64 v[162:163], v253 offset:136\n\t"
            "ds_read_b64 v[164:165], v253 offset:1784\n\t"
            "ds_read_b64 v[166:167], v253 offset:1880\n\t"
            "s_waitcnt lgkmcnt(10)\n\t"
            "v_smfmac_f32_16x16x64_bf16 v[188:191], v[172:175], v[144:151], v255\n\t"
            "ds_read_b128 v[172:175], v254 offset:320\n\t"
            "ds_read_b64 v[144:145], v253 offset:48\n\t"
            "ds_read_b64 v[146:147], v253 offset:144\n\t"
            "ds_read_b64 v[148:149], v253 offset:1792\n\t"
            "ds_read_b64 v[150:151], v253 offset:1888\n\t"
            "v_cvt_pk_bf16_f32 v184, v184, v185\n\t"
            "v_cvt_pk_bf16_f32 v185, v186, v187\n\t"
            "s_nop 0\n\t"
            "v_cvt_pk_bf16_f32 v186, v188, v189\n\t"
            "v_cvt_pk_bf16_f32 v187, v190, v191\n\t"
            "v_mfma_f32_4x4x4_16b_bf16 v[196:199], v[184:185], v[184:185], v[196:199]\n\t"
            "s_waitcnt lgkmcnt(14)\n\t"
            "ds_write_b128 v237, v[184:187]\n\t"
            "v_mfma_f32_16x16x32_bf16 v[192:195], v[238:241], v[184:187], v[192:195]\n\t"
            "v_mov_b64 v[184:185], 0\n\t"
            "v_mov_b64 v[188:189], 0\n\t"
            "v_mfma_f32_4x4x4_16b_bf16 v[196:199], v[186:187], v[186:187], v[196:199]\n\t"
            "v_mov_b64 v[186:187], 0\n\t"
            "v_mov_b64 v[190:191], 0\n\t"
            "s_waitcnt lgkmcnt(10)\n\t"
            "v_smfmac_f32_16x16x64_bf16 v[176:179], v[168:171], v[152:159], v255\n\t"
            "ds_read_b128 v[168:171], v254 offset:384\n\t"
            "ds_read_b64 v[152:153], v253 offset:56\n\t"
            "ds_read_b64 v[154:155], v253 offset:152\n\t"
            "ds_read_b64 v[156:157], v253 offset:1800\n\t"
            "ds_read_b64 v[158:159], v253 offset:1896\n\t"
            "s_waitcnt lgkmcnt(10)\n\t"
            "v_smfmac_f32_16x16x64_bf16 v[180:183], v[172:175], v[160:167], v255\n\t"
            "ds_read_b128 v[172:175], v254 offset:448\n\t"
            "ds_read_b64 v[160:161], v253 offset:64\n\t"
            "ds_read_b64 v[162:163], v253 offset:160\n\t"
            "ds_read_b64 v[164:165], v253 offset:1808\n\t"
            "ds_read_b64 v[166:167], v253 offset:1904\n\t"
            "v_cvt_pk_bf16_f32 v176, v176, v177\n\t"
            "v_cvt_pk_bf16_f32 v177, v178, v179\n\t"
            "s_nop 0\n\t"
            "v_cvt_pk_bf16_f32 v178, v180, v181\n\t"
            "v_cvt_pk_bf16_f32 v179, v182, v183\n\t"
            "v_mfma_f32_4x4x4_16b_bf16 v[196:199], v[176:177], v[176:177], v[196:199]\n\t"
            "s_waitcnt lgkmcnt(14)\n\t"
            "ds_write_b128 v236, v[176:179] offset:128\n\t"
            "v_mfma_f32_16x16x32_bf16 v[192:195], v[238:241], v[176:179], v[192:195]\n\t"
            "v_mov_b64 v[176:177], 0\n\t"
            "v_mov_b64 v[180:181], 0\n\t"
            "v_mfma_f32_4x4x4_16b_bf16 v[196:199], v[178:179], v[178:179], v[196:199]\n\t"
            "v_mov_b64 v[178:179], 0\n\t"
            "v_mov_b64 v[182:183], 0\n\t"
            "s_waitcnt lgkmcnt(10)\n\t"
            "v_smfmac_f32_16x16x64_bf16 v[184:187], v[168:171], v[144:151], v255\n\t"
            "ds_read_b128 v[168:171], v254 offset:512\n\t"
            "ds_read_b64 v[144:145], v253 offset:72\n\t"
            "ds_read_b64 v[146:147], v253 offset:168\n\t"
            "ds_read_b64 v[148:149], v253 offset:1816\n\t"
            "ds_read_b64 v[150:151], v253 offset:1912\n\t"
            "s_waitcnt lgkmcnt(10)\n\t"
            "v_smfmac_f32_16x16x64_bf16 v[188:191], v[172:175], v[152:159], v255\n\t"
            "ds_read_b128 v[172:175], v254 offset:576\n\t"
            "ds_read_b64 v[152:153], v253 offset:80\n\t"
            "ds_read_b64 v[154:155], v253 offset:176\n\t"
            "ds_read_b64 v[156:157], v253 offset:1824\n\t"
            "ds_read_b64 v[158:159], v253 offset:1920\n\t"
            "v_cvt_pk_bf16_f32 v184, v184, v185\n\t"
            "v_cvt_pk_bf16_f32 v185, v186, v187\n\t"
            "s_nop 0\n\t"
            "v_cvt_pk_bf16_f32 v186, v188, v189\n\t"
            "v_cvt_pk_bf16_f32 v187, v190, v191\n\t"
            "v_mfma_f32_4x4x4_16b_bf16 v[196:199], v[184:185], v[184:185], v[196:199]\n\t"
            "s_waitcnt lgkmcnt(14)\n\t"
            "ds_write_b128 v237, v[184:187] offset:128\n\t"
            "v_mfma_f32_16x16x32_bf16 v[192:195], v[238:241], v[184:187], v[192:195]\n\t"
            "v_mov_b64 v[184:185], 0\n\t"
            "v_mov_b64 v[188:189], 0\n\t"
            "v_mfma_f32_4x4x4_16b_bf16 v[196:199], v[186:187], v[186:187], v[196:199]\n\t"
            "v_mov_b64 v[186:187], 0\n\t"
            "v_mov_b64 v[190:191], 0\n\t"
            "s_waitcnt lgkmcnt(10)\n\t"
            "v_smfmac_f32_16x16x64_bf16 v[176:179], v[168:171], v[160:167], v255\n\t"
            "ds_read_b128 v[168:171], v254 offset:640\n\t"
            "ds_read_b64 v[160:161], v253 offset:88\n\t"
            "ds_read_b64 v[162:163], v253 offset:184\n\t"
            "ds_read_b64 v[164:165], v253 offset:1832\n\t"
            "ds_read_b64 v[166:167], v253 offset:1928\n\t"
            "s_waitcnt lgkmcnt(10)\n\t"
            "v_smfmac_f32_16x16x64_bf16 v[180:183], v[172:175], v[144:151], v255\n\t"
            "ds_read_b128 v[172:175], v254 offset:704\n\t"
            "v_cvt_pk_bf16_f32 v176, v176, v177\n\t"
            "v_cvt_pk_bf16_f32 v177, v178, v179\n\t"
            "s_nop 1\n\t"
            "v_mfma_f32_4x4x4_16b_bf16 v[196:199], v[176:177], v[176:177], v[196:199]\n\t"
            "s_nop 1\n\t"
            "v_cvt_pk_bf16_f32 v178, v180, v181\n\t"
            "v_cvt_pk_bf16_f32 v179, v182, v183\n\t"
            "s_nop 1\n\t"
            "v_mfma_f32_4x4x4_16b_bf16 v[196:199], v[178:179], v[178:179], v[196:199]\n\t"
            "ds_write_b128 v236, v[176:179] offset:256\n\t"
            "v_mfma_f32_16x16x32_bf16 v[192:195], v[238:241], v[176:179], v[192:195]\n\t"
            "s_waitcnt lgkmcnt(6)\n\t"
            "v_smfmac_f32_16x16x64_bf16 v[184:187], v[168:171], v[152:159], v255\n\t"
            "s_waitcnt lgkmcnt(1)\n\t"
            "v_smfmac_f32_16x16x64_bf16 v[188:191], v[172:175], v[160:167], v255\n\t"
            "s_nop 5\n\t"
            "v_cvt_pk_bf16_f32 v184, v184, v185\n\t"
            "v_cvt_pk_bf16_f32 v185, v186, v187\n\t"
            "s_nop 1\n\t"
            "v_mfma_f32_4x4x4_16b_bf16 v[196:199], v[184:185], v[184:185], v[196:199]\n\t"
            "v_cvt_pk_bf16_f32 v186, v188, v189\n\t"
            "v_cvt_pk_bf16_f32 v187, v190, v191\n\t"
            "s_nop 1\n\t"
            "v_mfma_f32_4x4x4_16b_bf16 v[196:199], v[186:187], v[186:187], v[196:199]\n\t"
            "v_mfma_f32_16x16x32_bf16 v[192:195], v[238:241], v[184:187], v[192:195]\n\t"
            "ds_write_b128 v237, v[184:187] offset:256\n\t"
            "s_nop 6\n\t"
            "v_cndmask_b32_e64 v193, v196, v197, s[60:61]\n\t"
            "v_cndmask_b32_e64 v193, v193, v198, s[62:63]\n\t"
            "v_cndmask_b32_e64 v193, v193, v199, s[64:65]\n\t"
            "ds_write_b64 v248, v[192:193] offset:18816\n\t"
            "s_waitcnt lgkmcnt(0)\n\t"
            "s_nop 7\n\t"
            :
            : "{v250}"(cX), "{v251}"(cS), "{v253}"(cB), "{v254}"(aA_), "{v255}"(idx_), "{v[238:241]}"(sel4), "{s67}"(ldsb + (wave < 4 ? X_OFF : O_OFF)),
              "{v252}"(lane * 16), "{s[68:71]}"(wrsrc), "{s72}"(FLG_OFF), "{s73}"(wave < 4 ? ldsb + PF_OFF + w * 512 : 0u),
              "{s[60:61]}"(0x2222222222222222ull), "{s[62:63]}"(0x4444444444444444ull), "{s[64:65]}"(0x8888888888888888ull)
            : "v144", "v145", "v146", "v147", "v148", "v149", "v150", "v151", "v152", "v153", "v154", "v155", "v156", "v157", "v158", "v159", "v160", "v161", "v162", "v163", "v164", "v165", "v166", "v167", "v168", "v169", "v170", "v171", "v172", "v173", "v174", "v175", "v176", "v177", "v178", "v179", "v180", "v181", "v182", "v183", "v184", "v185", "v186", "v187", "v188", "v189", "v190", "v191", "v192", "v193", "v194", "v195", "v196", "v197", "v198", "v199", "v236", "v237", "v248", "v249", "m0", "scc", "memory");
        if (wave < 4) {
            if (lane == 0) __builtin_amdgcn_raw_buffer_store_b64((u32x2){tag_lo, tag_hi}, wrsrc, ARR_OFF + (blockIdx.x * 4 + w) * 8, 0, SC1);
            POLL_W
        }
    }
    const unsigned long long mrow = 0x0001000100010001ull * ((1u << (cnt - 16)) - 1u);
#define KARGS                                                                                      \
    unsigned long long xb, wb, ob;                                                                 \
    asm volatile("s_load_dwordx2 %0, %3, 0x0\n\ts_load_dwordx2 %1, %3, 0x20\n\ts_load_dwordx2 %2, %3, 0x28\n\ts_waitcnt lgkmcnt(0)" \
                 : "=&s"(xb), "=&s"(wb), "=&s"(ob) : "s"(__builtin_amdgcn_kernarg_segment_ptr()) : "memory"); \
    xb -= XBIAS;                                                                                   \
    const u32x4 wr = {(unsigned)wb, (unsigned)(wb >> 32) & 0xffffu, (unsigned)WS_BYTES, 0x00020000u}; \
    const u32x4 orr = {(unsigned)ob, (unsigned)(ob >> 32) & 0xffffu, (unsigned)(IMG * IMG * CH * 2), 0x00020000u}; \
    const u32x4 xr = {(unsigned)xb, (unsigned)(xb >> 32) & 0xffffu, (unsigned)(IMG * IMG * CH * 2 + XBIAS), 0x00020000u};
    const int tk_ = (xi + 4) / 6;
    const unsigned touch = wave * 36864 + ((xi + 4 - 6 * tk_) * 6 + tk_) * 1024;
#define B_SCALARS \
            CONV_CONSTS(0) \
            const unsigned aCA_ = ldsb + CT_OFF + lane * 16; \
            KARGS \
            unsigned s45 = OOB, s46 = (en0 & 0x7ffffffu) + 96 * w, s47 = (en1 & 0x7ffffffu) + 96 * w; \
            unsigned s48 = ldsb + X_OFF, s67 = ldsb + X1_OFF, s76 = ldsb + WL_OFF + 8, s70 = 0; \
            unsigned s56 = __builtin_amdgcn_readfirstlane((en0 >> 30) * 4 + (en1 >> 30)); \
            unsigned s69 = 0x401fffffu | (en1 & 0x38000000u), s79 = (en1 & 0x08000000u) ? OOB : s47, s59 = ldsb + LX_OFF(1) + 32 * w; \
            unsigned long long xp = 0, sp = 0; \
            _Pragma("unroll") for (int v = 0; v < 4; ++v) { \
                const unsigned r = (CROW >> (8 * v)) & 0xFF; \
                xp |= (unsigned long long)XPAT(r) << (16 * v); \
                sp |= (unsigned long long)SPAT(r) << (16 * v); \
            }
#define B_SIO "+{s45}"(s45), "+{s46}"(s46), "+{s47}"(s47), "+{s48}"(s48), "+{s67}"(s67), "+{s76}"(s76), "+{s56}"(s56), "+{s70}"(s70), "+{s69}"(s69), "+{s79}"(s79), "+{s59}"(s59)
#define B_SIN "{v[238:241]}"(sel4), "{s[40:43]}"(orr), "{s[72:75]}"(xr), "{s39}"(0x401fffffu), "{s[28:31]}"(wr), "{s33}"(wave * 36864), "{s78}"(96 * w), \
              "{s27}"(96 * w - 384), "{s[50:51]}"(xp), "{s[52:53]}"(sp), "{s54}"(CLANE), "{s55}"(CROW & 0x0F0F0F0Fu), "{s[34:35]}"(0x0001000100010001ull), \
              "{s[60:61]}"(0x2222222222222222ull), "{s[62:63]}"(0x4444444444444444ull), "{s[64:65]}"(0x8888888888888888ull), \
              "{s66}"(ldsb + w * 8192), "{s71}"((ldsb + X_OFF) ^ (ldsb + X1_OFF))
#define B_SCLOB "s83", "s26", "s77", "s68", "s36", "s37", "s38", "s57", "s58", "s49", "vcc", "scc", "m0", "memory"

    if (wave < 4) {
        LANE_SETUP
        const int dW = X_OFF + l15 * 384 + (((2 * w + (g >> 1)) ^ sw) << 4) + 8 * (g & 1);
        const int vB = V_OFF + w * 4608 + 1024 + lane * 8, rsR = RS_BASE + l15 * 4;
        {
            const unsigned rsb = ldsb + RS_BASE;
            const unsigned pE_ = fE - X_OFF, aO_ = ldsb + dW + (O_OFF - X_OFF), sel_ = (l15 & 3) ? 0u : 1u, aVB_ = ldsb + vB;
            const unsigned pST_ = lane * 16, aRSw_ = rsb + lane * 4, aPLw_ = rsb + lane * 2, aRSr_ = ldsb + rsR;
            const unsigned g16_ = (l15 & 3) ? ldsb + AT_OFF + 18816 : g * 16, g8_ = (l15 & 3) ? ldsb + AT_OFF + 18816 : g * 8;
            const unsigned loVL_ = ((2 * w + (l15 >> 3)) << 4) + (l15 & 7) * 2, loOL_ = g * 128;
            const unsigned on = (l15 & 3) ? 0u : 0x3F803F80u;
            const u32x4 ones4 = {on, on, on, on};
            unsigned s44 = cnt, s48 = ldsb + X_OFF, s70 = 0;
            unsigned s72 = ldsb + KQL_OFF + w * 192, s73 = ldsb + OL_OFF, s74 = ldsb + KQL_OFF + 784, s77 = ldsb + OL_OFF, s76 = ldsb + KQL_OFF + 768 + w * 4;
            KARGS
            asm volatile(
            "s_nop 4\n\t"
            "buffer_load_dwordx4 v[140:143], v248, s[84:87], s88 offen sc1\n\t"
            "s_add_u32 s49, s46, 0\n\t"
            "buffer_load_dwordx4 v[0:3], v248, s[84:87], s49 offen sc1\n\t"
            "s_add_u32 s49, s46, 6144\n\t"
            "buffer_load_dwordx4 v[24:27], v248, s[84:87], s49 offen sc1\n\t"
            "s_add_u32 s49, s46, 12288\n\t"
            "buffer_load_dwordx4 v[48:51], v248, s[84:87], s49 offen sc1\n\t"
            "s_add_u32 s49, s46, 18432\n\t"
            "buffer_load_dwordx4 v[72:75], v248, s[84:87], s49 offen sc1\n\t"
            "s_add_u32 s49, s46, 24576\n\t"
            "buffer_load_dwordx4 v[96:99], v248, s[84:87], s49 offen sc1\n\t"
            "s_add_u32 s49, s46, 30720\n\t"
            "buffer_load_dwordx4 v[120:123], v248, s[84:87], s49 offen sc1\n\t"
            "s_add_u32 s49, s46, 1024\n\t"
            "buffer_load_dwordx4 v[4:7], v248, s[84:87], s49 offen sc1\n\t"
            "s_add_u32 s49, s46, 7168\n\t"
            "buffer_load_dwordx4 v[28:31], v248, s[84:87], s49 offen sc1\n\t"
            "s_add_u32 s49, s46, 13312\n\t"
            "buffer_load_dwordx4 v[52:55], v248, s[84:87], s49 offen sc1\n\t"
            "s_add_u32 s49, s46, 19456\n\t"
            "buffer_load_dwordx4 v[76:79], v248, s[84:87], s49 offen sc1\n\t"
            "s_add_u32 s49, s46, 25600\n\t"
            "buffer_load_dwordx4 v[100:103], v248, s[84:87], s49 offen sc1\n\t"
            "s_add_u32 s49, s46, 31744\n\t"
            "buffer_load_dwordx4 v[124:127], v248, s[84:87], s49 offen sc1\n\t"
            "s_add_u32 s49, s46, 2048\n\t"
            "buffer_load_dwordx4 v[8:11], v248, s[84:87], s49 offen sc1\n\t"
            "s_add_u32 s49, s46, 8192\n\t"
            "buffer_load_dwordx4 v[32:35], v248, s[84:87], s49 offen sc1\n\t"
            "s_add_u32 s49, s46, 14336\n\t"
            "buffer_load_dwordx4 v[56:59], v248, s[84:87], s49 offen sc1\n\t"
            "s_add_u32 s49, s46, 20480\n\t"
            "buffer_load_dwordx4 v[80:83], v248, s[84:87], s49 offen sc1\n\t"
            "s_add_u32 s49, s46, 26624\n\t"
            "buffer_load_dwordx4 v[104:107], v248, s[84:87], s49 offen sc1\n\t"
            "s_add_u32 s49, s46, 32768\n\t"
            "buffer_load_dwordx4 v[128:131], v248, s[84:87], s49 offen sc1\n\t"
            "s_add_u32 s49, s46, 3072\n\t"
            "buffer_load_dwordx4 v[12:15], v248, s[84:87], s49 offen sc1\n\t"
            "s_add_u32 s49, s46, 9216\n\t"
            "buffer_load_dwordx4 v[36:39], v248, s[84:87], s49 offen sc1\n\t"
            "s_add_u32 s49, s46, 15360\n\t"
            "buffer_load_dwordx4 v[60:63], v248, s[84:87], s49 offen sc1\n\t"
            "s_add_u32 s49, s46, 21504\n\t"
            "buffer_load_dwordx4 v[84:87], v248, s[84:87], s49 offen sc1\n\t"
            "s_add_u32 s49, s46, 27648\n\t"
            "buffer_load_dwordx4 v[108:111], v248, s[84:87], s49 offen sc1\n\t"
            "s_add_u32 s49, s46, 33792\n\t"
            "buffer_load_dwordx4 v[132:135], v248, s[84:87], s49 offen sc1\n\t"
            "s_add_u32 s49, s46, 4096\n\t"
            "buffer_load_dwordx4 v[16:19], v248, s[84:87], s49 offen sc1\n\t"
            "s_add_u32 s49, s46, 10240\n\t"
            "buffer_load_dwordx4 v[40:43], v248, s[84:87], s49 offen sc1\n\t"
            "s_add_u32 s49, s46, 16384\n\t"
            "buffer_load_dwordx4 v[64:67], v248, s[84:87], s49 offen sc1\n\t"
            "s_add_u32 s49, s46, 22528\n\t"
            "buffer_load_dwordx4 v[88:91], v248, s[84:87], s49 offen sc1\n\t"
            "s_add_u32 s49, s46, 28672\n\t"
            "buffer_load_dwordx4 v[112:115], v248, s[84:87], s49 offen sc1\n\t"
            "s_add_u32 s49, s46, 34816\n\t"
            "buffer_load_dwordx4 v[136:139], v248, s[84:87], s49 offen sc1\n\t"
            "s_add_u32 s49, s46, 5120\n\t"
            "buffer_load_dwordx4 v[20:23], v248, s[84:87], s49 offen sc1\n\t"
            "s_add_u32 s49, s46, 11264\n\t"
            "buffer_load_dwordx4 v[44:47], v248, s[84:87], s49 offen sc1\n\t"
            "s_add_u32 s49, s46, 17408\n\t"
            "buffer_load_dwordx4 v[68:71], v248, s[84:87], s49 offen sc1\n\t"
            "s_add_u32 s49, s46, 23552\n\t"
            "buffer_load_dwordx4 v[92:95], v248, s[84:87], s49 offen sc1\n\t"
            "s_add_u32 s49, s46, 29696\n\t"
            "buffer_load_dwordx4 v[116:119], v248, s[84:87], s49 offen sc1\n\t"
            "s_add_u32 s49, s46, 35840\n\t"
            "buffer_load_dwordx4 v[140:143], v248, s[84:87], s49 offen sc1\n\t"
            "s_waitcnt lgkmcnt(0)\n\t"
            "s_barrier\n\t"
            "s_setprio 2\n\t"
            "v_add_u32 v235, s71, v248\n\t"
            "v_add_u32 v232, s71, v244\n\t"
            "v_xor_b32 v233, 64, v232\n\t"
            "v_subrev_u32 v239, s75, v251\n\t"
            "v_mul_u32_u24 v239, 200, v239\n\t"
            "v_lshrrev_b32 v234, 3, v255\n\t"
            "v_add3_u32 v234, v234, v239, s72\n\t"
            "ds_read_b128 v[228:231], v235 offset:12288\n\t"
            "ds_read_b128 v[144:147], v232\n\t"
            "ds_read_b128 v[148:151], v233\n\t"
            "ds_read_b128 v[204:207], v232 offset:6144\n\t"
            "ds_read_b128 v[208:211], v233 offset:6144\n\t"
            "ds_read_b128 v[152:155], v232 offset:128\n\t"
            "ds_read_b128 v[156:159], v233 offset:128\n\t"
            "ds_read_b128 v[212:215], v232 offset:6272\n\t"
            "ds_read_b128 v[224:227], v233 offset:6272\n\t"
            "ds_read_b128 v[160:163], v232 offset:256\n\t"
            "ds_read_b128 v[164:167], v233 offset:256\n\t"
            "s_waitcnt vmcnt(24)\n\t"
            "s_waitcnt lgkmcnt(10)\n\t"
            "v_add_f32 v236, v228, v230\n\t"
            "v_add_f32 v237, v229, v231\n\t"
            "s_waitcnt lgkmcnt(8)\n\t"
            "v_mfma_f32_16x16x32_bf16 v[168:171], v[0:3], v[144:147], 0\n\t"
            "ds_read_b128 v[228:231], v235 offset:13312\n\t"
            "v_lshrrev_b32 v235, 4, v255\n\t"
            "v_mfma_f32_16x16x32_bf16 v[168:171], v[4:7], v[148:151], v[168:171]\n\t"
            "v_add3_u32 v235, v235, v239, s72\n\t"
            "v_mfma_f32_16x16x32_bf16 v[176:179], v[48:51], v[144:147], 0\n\t"
            "v_mfma_f32_16x16x32_bf16 v[176:179], v[52:55], v[148:151], v[176:179]\n\t"
            "v_mfma_f32_16x16x32_bf16 v[172:175], v[24:27], v[144:147], 0\n\t"
            "v_mfma_f32_16x16x32_bf16 v[172:175], v[28:31], v[148:151], v[172:175]\n\t"
            "v_mfma_f32_16x16x32_bf16 v[180:183], v[72:75], v[144:147], 0\n\t"
            "v_mfma_f32_16x16x32_bf16 v[180:183], v[76:79], v[148:151], v[180:183]\n\t"
            "v_mfma_f32_16x16x32_bf16 v[184:187], v[96:99], v[144:147], 0\n\t"
            "v_mfma_f32_16x16x32_bf16 v[184:187], v[100:103], v[148:151], v[184:187]\n\t"
            "v_mfma_f32_16x16x32_bf16 v[188:191], v[120:123], v[144:147], 0\n\t"
            "s_waitcnt lgkmcnt(0)\n\t"
            "v_add_f32 v228, v228, v230\n\t"
            "v_add_f32 v236, v236, v228\n\t"
            "v_mfma_f32_16x16x32_bf16 v[188:191], v[124:127], v[148:151], v[188:191]\n\t"
            "v_add_f32 v229, v229, v231\n\t"
            "v_mul_f32 v236, 0x3baaaaab, v236\n\t"
            "v_mfma_f32_16x16x32_bf16 v[192:195], v[0:3], v[204:207], 0\n\t"
            "v_add_f32 v237, v237, v229\n\t"
            "v_mul_f32_e64 v236, v236, -v236\n\t"
            "v_mfma_f32_16x16x32_bf16 v[192:195], v[4:7], v[208:211], v[192:195]\n\t"
            "v_fmac_f32 v236, 0x3baaaaab, v237\n\t"
            "v_max_f32 v236, 0, v236\n\t"
            "v_mfma_f32_16x16x32_bf16 v[196:199], v[24:27], v[204:207], 0\n\t"
            "v_add_f32 v236, 0x3727c5ac, v236\n\t"
            "v_rsq_f32 v236, v236\n\t"
            "v_mfma_f32_16x16x32_bf16 v[196:199], v[28:31], v[208:211], v[196:199]\n\t"
            "ds_write_b32 v249, v236\n\t"
            "v_mfma_f32_16x16x32_bf16 v[200:203], v[48:51], v[204:207], 0\n\t"
            "ds_read_b32 v236, v251\n\t"
            "ds_read_b32 v237, v251 offset:64\n\t"
            "v_mfma_f32_16x16x32_bf16 v[200:203], v[52:55], v[208:211], v[200:203]\n\t"
            "v_mfma_f32_16x16x32_bf16 v[216:219], v[72:75], v[204:207], 0\n\t"
            "v_mfma_f32_16x16x32_bf16 v[216:219], v[76:79], v[208:211], v[216:219]\n\t"
            "v_mfma_f32_16x16x32_bf16 v[220:223], v[96:99], v[204:207], 0\n\t"
            "v_mfma_f32_16x16x32_bf16 v[220:223], v[100:103], v[208:211], v[220:223]\n\t"
            "v_mfma_f32_16x16x32_bf16 v[228:231], v[120:123], v[204:207], 0\n\t"
            "ds_read_b128 v[204:207], v232 offset:6400\n\t"
            "v_add_u32 v232, s48, v244\n\t"
            "v_mfma_f32_16x16x32_bf16 v[228:231], v[124:127], v[208:211], v[228:231]\n\t"
            "ds_read_b128 v[208:211], v233 offset:6400\n\t"
            "s_waitcnt vmcnt(12)\n\t"
            "v_mfma_f32_16x16x32_bf16 v[168:171], v[8:11], v[152:155], v[168:171]\n\t"
            "v_xor_b32 v233, 64, v232\n\t"
            "v_mfma_f32_16x16x32_bf16 v[168:171], v[12:15], v[156:159], v[168:171]\n\t"
            "v_mfma_f32_16x16x32_bf16 v[176:179], v[56:59], v[152:155], v[176:179]\n\t"
            "v_mfma_f32_16x16x32_bf16 v[176:179], v[60:63], v[156:159], v[176:179]\n\t"
            "v_mfma_f32_16x16x32_bf16 v[172:175], v[32:35], v[152:155], v[172:175]\n\t"
            "v_mfma_f32_16x16x32_bf16 v[172:175], v[36:39], v[156:159], v[172:175]\n\t"
            "v_mfma_f32_16x16x32_bf16 v[180:183], v[80:83], v[152:155], v[180:183]\n\t"
            "v_mfma_f32_16x16x32_bf16 v[180:183], v[84:87], v[156:159], v[180:183]\n\t"
            "v_mfma_f32_16x16x32_bf16 v[184:187], v[104:107], v[152:155], v[184:187]\n\t"
            "v_mfma_f32_16x16x32_bf16 v[184:187], v[108:111], v[156:159], v[184:187]\n\t"
            "v_mfma_f32_16x16x32_bf16 v[188:191], v[128:131], v[152:155], v[188:191]\n\t"
            "v_mfma_f32_16x16x32_bf16 v[188:191], v[132:135], v[156:159], v[188:191]\n\t"
            "v_mfma_f32_16x16x32_bf16 v[192:195], v[8:11], v[212:215], v[192:195]\n\t"
            "v_mfma_f32_16x16x32_bf16 v[192:195], v[12:15], v[224:227], v[192:195]\n\t"
            "v_mfma_f32_16x16x32_bf16 v[196:199], v[32:35], v[212:215], v[196:199]\n\t"
            "v_mfma_f32_16x16x32_bf16 v[196:199], v[36:39], v[224:227], v[196:199]\n\t"
            "v_mfma_f32_16x16x32_bf16 v[200:203], v[56:59], v[212:215], v[200:203]\n\t"
            "v_mfma_f32_16x16x32_bf16 v[200:203], v[60:63], v[224:227], v[200:203]\n\t"
            "v_mfma_f32_16x16x32_bf16 v[216:219], v[80:83], v[212:215], v[216:219]\n\t"
            "v_mfma_f32_16x16x32_bf16 v[216:219], v[84:87], v[224:227], v[216:219]\n\t"
            "v_mfma_f32_16x16x32_bf16 v[220:223], v[104:107], v[212:215], v[220:223]\n\t"
            "v_mfma_f32_16x16x32_bf16 v[220:223], v[108:111], v[224:227], v[220:223]\n\t"
            "v_mfma_f32_16x16x32_bf16 v[228:231], v[128:131], v[212:215], v[228:231]\n\t"
            "ds_read_b128 v[212:215], v232 offset:256\n\t"
            "v_mfma_f32_16x16x32_bf16 v[228:231], v[132:135], v[224:227], v[228:231]\n\t"
            "ds_read_b128 v[224:227], v233 offset:256\n\t"
            "s_waitcnt vmcnt(0)\n\t"
            "v_mfma_f32_16x16x32_bf16 v[168:171], v[16:19], v[160:163], v[168:171]\n\t"
            "v_mfma_f32_16x16x32_bf16 v[168:171], v[20:23], v[164:167], v[168:171]\n\t"
            "v_mfma_f32_16x16x32_bf16 v[176:179], v[64:67], v[160:163], v[176:179]\n\t"
            "v_mfma_f32_16x16x32_bf16 v[176:179], v[68:71], v[164:167], v[176:179]\n\t"
            "v_mfma_f32_16x16x32_bf16 v[180:183], v[88:91], v[160:163], v[180:183]\n\t"
            "v_mfma_f32_16x16x32_bf16 v[180:183], v[92:95], v[164:167], v[180:183]\n\t"
            "v_mfma_f32_16x16x32_bf16 v[172:175], v[40:43], v[160:163], v[172:175]\n\t"
            "v_mfma_f32_16x16x32_bf16 v[172:175], v[44:47], v[164:167], v[172:175]\n\t"
            "s_waitcnt lgkmcnt(5)\n\t"
            "s_nop 0\n\t"
            "v_mul_f32 v168, v236, v168\n\t"
            "v_mfma_f32_16x16x32_bf16 v[184:187], v[112:115], v[160:163], v[184:187]\n\t"
            "v_mul_f32 v169, v236, v169\n\t"
            "v_mul_f32 v170, v236, v170\n\t"
            "v_mfma_f32_16x16x32_bf16 v[184:187], v[116:119], v[164:167], v[184:187]\n\t"
            "v_mul_f32 v176, v236, v176\n\t"
            "v_mul_f32 v177, v236, v177\n\t"
            "v_mfma_f32_16x16x32_bf16 v[188:191], v[136:139], v[160:163], v[188:191]\n\t"
            "v_mul_f32 v171, v236, v171\n\t"
            "v_mul_f32 v178, v236, v178\n\t"
            "v_mfma_f32_16x16x32_bf16 v[188:191], v[140:143], v[164:167], v[188:191]\n\t"
            "v_mul_f32 v179, v236, v179\n\t"
            "v_cvt_pk_bf16_f32 v168, v168, v169\n\t"
            "s_waitcnt lgkmcnt(2)\n\t"
            "v_mfma_f32_16x16x32_bf16 v[192:195], v[16:19], v[204:207], v[192:195]\n\t"
            "v_cvt_pk_bf16_f32 v176, v176, v177\n\t"
            "v_cvt_pk_bf16_f32 v180, v180, v181\n\t"
            "v_mfma_f32_16x16x32_bf16 v[192:195], v[20:23], v[208:211], v[192:195]\n\t"
            "v_mul_f32 v172, v236, v172\n\t"
            "v_mul_f32 v173, v236, v173\n\t"
            "v_mfma_f32_16x16x32_bf16 v[196:199], v[40:43], v[204:207], v[196:199]\n\t"
            "v_mul_f32 v174, v236, v174\n\t"
            "v_mul_f32 v175, v236, v175\n\t"
            "v_mfma_f32_16x16x32_bf16 v[196:199], v[44:47], v[208:211], v[196:199]\n\t"
            "v_cvt_pk_bf16_f32 v169, v170, v171\n\t"
            "v_cvt_pk_bf16_f32 v177, v178, v179\n\t"
            "v_mfma_f32_16x16x32_bf16 v[200:203], v[64:67], v[204:207], v[200:203]\n\t"
            "v_cvt_pk_bf16_f32 v181, v182, v183\n\t"
            "v_cvt_pk_bf16_f32 v170, v172, v173\n\t"
            "v_mfma_f32_16x16x32_bf16 v[200:203], v[68:71], v[208:211], v[200:203]\n\t"
            "v_cvt_pk_bf16_f32 v171, v174, v175\n\t"
            "v_cvt_pk_bf16_f32 v182, v184, v185\n\t"
            "v_mfma_f32_16x16x32_bf16 v[216:219], v[88:91], v[204:207], v[216:219]\n\t"
            "v_cvt_pk_bf16_f32 v183, v186, v187\n\t"
            "v_cvt_pk_bf16_f32 v178, v188, v189\n\t"
            "v_mfma_f32_16x16x32_bf16 v[216:219], v[92:95], v[208:211], v[216:219]\n\t"
            "v_cvt_pk_bf16_f32 v179, v190, v191\n\t"
            "v_mul_f32 v236, 0x3e553b94, v236\n\t"
            "v_mfma_f32_16x16x32_bf16 v[220:223], v[112:115], v[204:207], v[220:223]\n\t"
            "v_add_u32 v184, s76, v239\n\t"
            "v_add_u32 v185, s74, v239\n\t"
            "v_mfma_f32_16x16x32_bf16 v[220:223], v[116:119], v[208:211], v[220:223]\n\t"
            "s_mov_b64 exec, -1\n\t"
            "ds_write_b128 v234, v[168:171]\n\t"
            "ds_write_b128 v234, v[180:183] offset:64\n\t"
            "ds_write_b64 v235, v[176:177] offset:128\n\t"
            "ds_write_b64 v235, v[178:179] offset:160\n\t"
            "s_mov_b64 exec, -1\n\t"
            "v_mfma_f32_16x16x16_bf16 v[172:175], v[176:177], v[178:179], 0\n\t"
            "v_mul_f32 v192, v237, v192\n\t"
            "v_mul_f32 v193, v237, v193\n\t"
            "v_mul_f32 v200, v237, v200\n\t"
            "v_mfma_f32_16x16x32_bf16 v[228:231], v[136:139], v[204:207], v[228:231]\n\t"
            "ds_read_b128 v[204:207], v232\n\t"
            "v_mul_f32 v201, v237, v201\n\t"
            "v_mfma_f32_16x16x32_bf16 v[228:231], v[140:143], v[208:211], v[228:231]\n\t"
            "ds_read_b128 v[208:211], v233\n\t"
            "v_mul_f32 v194, v237, v194\n\t"
            "v_mfma_f32_16x16x32_bf16 v[172:175], v[168:171], v[180:183], v[172:175]\n\t"
            "v_mul_f32 v195, v237, v195\n\t"
            "v_mul_f32 v202, v237, v202\n\t"
            "v_mul_f32 v203, v237, v203\n\t"
            "v_cvt_pk_bf16_f32 v192, v192, v193\n\t"
            "v_cvt_pk_bf16_f32 v200, v200, v201\n\t"
            "v_cvt_pk_bf16_f32 v216, v216, v217\n\t"
            "v_mul_f32 v196, v237, v196\n\t"
            "v_mul_f32 v197, v237, v197\n\t"
            "v_mul_f32 v198, v237, v198\n\t"
            "v_mul_f32 v199, v237, v199\n\t"
            "v_cvt_pk_bf16_f32 v193, v194, v195\n\t"
            "v_cvt_pk_bf16_f32 v201, v202, v203\n\t"
            "v_cvt_pk_bf16_f32 v217, v218, v219\n\t"
            "v_cvt_pk_bf16_f32 v194, v196, v197\n\t"
            "v_cvt_pk_bf16_f32 v195, v198, v199\n\t"
            "v_cvt_pk_bf16_f32 v218, v220, v221\n\t"
            "v_cvt_pk_bf16_f32 v219, v222, v223\n\t"
            "v_cvt_pk_bf16_f32 v202, v228, v229\n\t"
            "v_cvt_pk_bf16_f32 v203, v230, v231\n\t"
            "v_mul_f32 v237, 0x3e553b94, v237\n\t"
            "v_add_u32 v220, s76, v239\n\t"
            "v_mfma_f32_16x16x16_bf16 v[196:199], v[200:201], v[202:203], 0\n\t"
            "s_and_b32 exec_lo, s56, 0x11111111\n\t"
            "s_and_b32 exec_hi, s57, 0x11111111\n\t"
            "ds_write_b32 v184, v172 offset:0\n\t"
            "s_and_b32 exec_lo, s56, 0x22222222\n\t"
            "s_and_b32 exec_hi, s57, 0x22222222\n\t"
            "ds_write_b32 v184, v173 offset:0\n\t"
            "s_and_b32 exec_lo, s56, 0x44444444\n\t"
            "s_and_b32 exec_hi, s57, 0x44444444\n\t"
            "ds_write_b32 v184, v174 offset:0\n\t"
            "s_and_b32 exec_lo, s56, 0x88888888\n\t"
            "s_and_b32 exec_hi, s57, 0x88888888\n\t"
            "ds_write_b32 v184, v175 offset:0\n\t"
            "s_mov_b64 exec, s[56:57]\n\t"
            "ds_write_b32 v185, v236 offset:0\n\t"
            "s_mov_b64 exec, -1\n\t"
            "s_waitcnt lgkmcnt(5)\n\t"
            "v_mfma_f32_16x16x32_bf16 v[144:147], v[0:3], v[204:207], 0\n\t"
            "v_add_u32 v221, s74, v239\n\t"
            "v_mfma_f32_16x16x32_bf16 v[144:147], v[4:7], v[208:211], v[144:147]\n\t"
            "s_mov_b64 exec, s[54:55]\n\t"
            "ds_write_b128 v234, v[192:195] offset:12800\n\t"
            "ds_write_b128 v234, v[216:219] offset:12864\n\t"
            "ds_write_b64 v235, v[200:201] offset:12928\n\t"
            "ds_write_b64 v235, v[202:203] offset:12960\n\t"
            "s_mov_b64 exec, -1\n\t"
            "v_mfma_f32_16x16x32_bf16 v[148:151], v[28:31], v[208:211], 0\n\t"
            "v_add_u32 v235, s48, v248\n\t"
            "v_mfma_f32_16x16x32_bf16 v[148:151], v[24:27], v[204:207], v[148:151]\n\t"
            "ds_read_b128 v[228:231], v235 offset:18816\n\t"
            "v_mfma_f32_16x16x32_bf16 v[152:155], v[48:51], v[204:207], 0\n\t"
            "v_mfma_f32_16x16x32_bf16 v[152:155], v[52:55], v[208:211], v[152:155]\n\t"
            "v_mfma_f32_16x16x32_bf16 v[156:159], v[76:79], v[208:211], 0\n\t"
            "v_mfma_f32_16x16x32_bf16 v[156:159], v[72:75], v[204:207], v[156:159]\n\t"
            "v_mfma_f32_16x16x32_bf16 v[160:163], v[96:99], v[204:207], 0\n\t"
            "v_mfma_f32_16x16x32_bf16 v[160:163], v[100:103], v[208:211], v[160:163]\n\t"
            "v_mfma_f32_16x16x32_bf16 v[164:167], v[124:127], v[208:211], 0\n\t"
            "ds_read_b128 v[208:211], v233 offset:128\n\t"
            "v_mfma_f32_16x16x32_bf16 v[164:167], v[120:123], v[204:207], v[164:167]\n\t"
            "ds_read_b128 v[204:207], v232 offset:128\n\t"
            "v_mfma_f32_16x16x32_bf16 v[196:199], v[192:195], v[216:219], v[196:199]\n\t"
            "s_waitcnt lgkmcnt(2)\n\t"
            "v_add_f32 v236, v228, v230\n\t"
            "s_nop 5\n\t"
            "s_and_b32 exec_lo, s78, 0x11111111\n\t"
            "s_and_b32 exec_hi, s79, 0x11111111\n\t"
            "ds_write_b32 v220, v196 offset:12800\n\t"
            "s_and_b32 exec_lo, s78, 0x22222222\n\t"
            "s_and_b32 exec_hi, s79, 0x22222222\n\t"
            "ds_write_b32 v220, v197 offset:12800\n\t"
            "s_and_b32 exec_lo, s78, 0x44444444\n\t"
            "s_and_b32 exec_hi, s79, 0x44444444\n\t"
            "ds_write_b32 v220, v198 offset:12800\n\t"
            "s_and_b32 exec_lo, s78, 0x88888888\n\t"
            "s_and_b32 exec_hi, s79, 0x88888888\n\t"
            "ds_write_b32 v220, v199 offset:12800\n\t"
            "s_mov_b64 exec, s[78:79]\n\t"
            "ds_write_b32 v221, v237 offset:12800\n\t"
            "s_mov_b64 exec, -1\n\t"
            "s_waitcnt lgkmcnt(5)\n\t"
            "v_mfma_f32_16x16x32_bf16 v[144:147], v[8:11], v[204:207], v[144:147]\n\t"
            "v_add_f32 v237, v229, v231\n\t"
            "v_mfma_f32_16x16x32_bf16 v[144:147], v[12:15], v[208:211], v[144:147]\n\t"
            "ds_read_b128 v[228:231], v235 offset:19840\n\t"
            "v_mfma_f32_16x16x32_bf16 v[148:151], v[36:39], v[208:211], v[148:151]\n\t"
            "v_mfma_f32_16x16x32_bf16 v[148:151], v[32:35], v[204:207], v[148:151]\n\t"
            "v_mfma_f32_16x16x32_bf16 v[152:155], v[56:59], v[204:207], v[152:155]\n\t"
            "v_mfma_f32_16x16x32_bf16 v[152:155], v[60:63], v[208:211], v[152:155]\n\t"
            "v_mfma_f32_16x16x32_bf16 v[156:159], v[84:87], v[208:211], v[156:159]\n\t"
            "v_mfma_f32_16x16x32_bf16 v[156:159], v[80:83], v[204:207], v[156:159]\n\t"
            "v_mfma_f32_16x16x32_bf16 v[160:163], v[104:107], v[204:207], v[160:163]\n\t"
            "v_mfma_f32_16x16x32_bf16 v[160:163], v[108:111], v[208:211], v[160:163]\n\t"
            "v_mfma_f32_16x16x32_bf16 v[164:167], v[132:135], v[208:211], v[164:167]\n\t"
            "ds_read_b128 v[208:211], v233 offset:6144\n\t"
            "v_mfma_f32_16x16x32_bf16 v[164:167], v[128:131], v[204:207], v[164:167]\n\t"
            "ds_read_b128 v[204:207], v232 offset:6144\n\t"
            "s_waitcnt lgkmcnt(2)\n\t"
            "v_add_f32 v228, v228, v230\n\t"
            "v_mfma_f32_16x16x32_bf16 v[144:147], v[16:19], v[212:215], v[144:147]\n\t"
            "v_add_f32 v236, v236, v228\n\t"
            "v_add_f32 v229, v229, v231\n\t"
            "v_mfma_f32_16x16x32_bf16 v[144:147], v[20:23], v[224:227], v[144:147]\n\t"
            "v_mul_f32 v236, 0x3baaaaab, v236\n\t"
            "v_add_f32 v237, v237, v229\n\t"
            "v_mfma_f32_16x16x32_bf16 v[148:151], v[44:47], v[224:227], v[148:151]\n\t"
            "v_mul_f32_e64 v236, v236, -v236\n\t"
            "v_fmac_f32 v236, 0x3baaaaab, v237\n\t"
            "v_mfma_f32_16x16x32_bf16 v[148:151], v[40:43], v[212:215], v[148:151]\n\t"
            "v_max_f32 v236, 0, v236\n\t"
            "v_add_f32 v236, 0x3727c5ac, v236\n\t"
            "v_mfma_f32_16x16x32_bf16 v[152:155], v[64:67], v[212:215], v[152:155]\n\t"
            "v_rsq_f32 v236, v236\n\t"
            "v_mfma_f32_16x16x32_bf16 v[152:155], v[68:71], v[224:227], v[152:155]\n\t"
            "ds_write_b32 v249, v236\n\t"
            "v_mfma_f32_16x16x32_bf16 v[156:159], v[92:95], v[224:227], v[156:159]\n\t"
            "ds_read_b32 v236, v251\n\t"
            "ds_read_b32 v237, v251 offset:64\n\t"
            "v_mfma_f32_16x16x32_bf16 v[156:159], v[88:91], v[212:215], v[156:159]\n\t"
            "ds_read_b32 v238, v251 offset:128\n\t"
            "v_mfma_f32_16x16x32_bf16 v[160:163], v[112:115], v[212:215], v[160:163]\n\t"
            "v_mfma_f32_16x16x32_bf16 v[160:163], v[116:119], v[224:227], v[160:163]\n\t"
            "v_mfma_f32_16x16x32_bf16 v[164:167], v[140:143], v[224:227], v[164:167]\n\t"
            "ds_read_b128 v[224:227], v233 offset:6272\n\t"
            "v_mfma_f32_16x16x32_bf16 v[164:167], v[136:139], v[212:215], v[164:167]\n\t"
            "ds_read_b128 v[212:215], v232 offset:6272\n\t"
            "s_waitcnt lgkmcnt(6)\n\t"
            "v_mfma_f32_16x16x32_bf16 v[168:171], v[0:3], v[204:207], 0\n\t"
            "v_cvt_pk_bf16_f32 v156, v156, v157\n\t"
            "v_cvt_pk_bf16_f32 v157, v158, v159\n\t"
            "v_mfma_f32_16x16x32_bf16 v[168:171], v[4:7], v[208:211], v[168:171]\n\t"
            "v_cvt_pk_bf16_f32 v158, v160, v161\n\t"
            "v_cvt_pk_bf16_f32 v159, v162, v163\n\t"
            "v_mfma_f32_16x16x32_bf16 v[172:175], v[28:31], v[208:211], 0\n\t"
            "v_mfma_f32_16x16x32_bf16 v[172:175], v[24:27], v[204:207], v[172:175]\n\t"
            "v_mfma_f32_16x16x32_bf16 v[176:179], v[48:51], v[204:207], 0\n\t"
            "s_waitcnt lgkmcnt(4)\n\t"
            "v_mul_f32 v144, v236, v144\n\t"
            "v_mul_f32 v145, v236, v145\n\t"
            "v_mfma_f32_16x16x32_bf16 v[176:179], v[52:55], v[208:211], v[176:179]\n\t"
            "v_mul_f32 v152, v236, v152\n\t"
            "v_mul_f32 v153, v236, v153\n\t"
            "v_mfma_f32_16x16x32_bf16 v[180:183], v[76:79], v[208:211], 0\n\t"
            "v_mul_f32 v146, v236, v146\n\t"
            "v_mul_f32 v147, v236, v147\n\t"
            "v_mfma_f32_16x16x32_bf16 v[180:183], v[72:75], v[204:207], v[180:183]\n\t"
            "v_mul_f32 v154, v236, v154\n\t"
            "v_mul_f32 v155, v236, v155\n\t"
            "v_mfma_f32_16x16x32_bf16 v[184:187], v[96:99], v[204:207], 0\n\t"
            "v_cvt_pk_bf16_f32 v144, v144, v145\n\t"
            "v_cvt_pk_bf16_f32 v152, v152, v153\n\t"
            "v_mfma_f32_16x16x32_bf16 v[184:187], v[100:103], v[208:211], v[184:187]\n\t"
            "v_mul_f32 v148, v236, v148\n\t"
            "v_mul_f32 v149, v236, v149\n\t"
            "v_mfma_f32_16x16x32_bf16 v[188:191], v[124:127], v[208:211], 0\n\t"
            "ds_read_b128 v[208:211], v233 offset:6400\n\t"
            "v_mul_f32 v150, v236, v150\n\t"
            "v_mfma_f32_16x16x32_bf16 v[188:191], v[120:123], v[204:207], v[188:191]\n\t"
            "ds_read_b128 v[204:207], v232 offset:6400\n\t"
            "v_mul_f32 v151, v236, v151\n\t"
            "s_waitcnt lgkmcnt(2)\n\t"
            "v_mfma_f32_16x16x32_bf16 v[168:171], v[8:11], v[212:215], v[168:171]\n\t"
            "v_cvt_pk_bf16_f32 v145, v146, v147\n\t"
            "v_cvt_pk_bf16_f32 v153, v154, v155\n\t"
            "v_mfma_f32_16x16x32_bf16 v[168:171], v[12:15], v[224:227], v[168:171]\n\t"
            "v_cvt_pk_bf16_f32 v146, v148, v149\n\t"
            "v_cvt_pk_bf16_f32 v147, v150, v151\n\t"
            "v_mfma_f32_16x16x32_bf16 v[172:175], v[36:39], v[224:227], v[172:175]\n\t"
            "v_cvt_pk_bf16_f32 v154, v164, v165\n\t"
            "v_cvt_pk_bf16_f32 v155, v166, v167\n\t"
            "v_mfma_f32_16x16x32_bf16 v[172:175], v[32:35], v[212:215], v[172:175]\n\t"
            "v_mul_f32 v236, 0x3e553b94, v236\n\t"
            "v_mfma_f32_16x16x32_bf16 v[176:179], v[56:59], v[212:215], v[176:179]\n\t"
            "v_mfma_f32_16x16x32_bf16 v[176:179], v[60:63], v[224:227], v[176:179]\n\t"
            "v_mfma_f32_16x16x32_bf16 v[180:183], v[84:87], v[224:227], v[180:183]\n\t"
            "v_mfma_f32_16x16x32_bf16 v[180:183], v[80:83], v[212:215], v[180:183]\n\t"
            "v_mfma_f32_16x16x32_bf16 v[184:187], v[104:107], v[212:215], v[184:187]\n\t"
            "v_mfma_f32_16x16x32_bf16 v[184:187], v[108:111], v[224:227], v[184:187]\n\t"
            "v_mfma_f32_16x16x32_bf16 v[188:191], v[132:135], v[224:227], v[188:191]\n\t"
            "ds_read_b128 v[224:227], v233 offset:12288\n\t"
            "v_mfma_f32_16x16x32_bf16 v[188:191], v[128:131], v[212:215], v[188:191]\n\t"
            "ds_read_b128 v[212:215], v232 offset:12288\n\t"
            "s_waitcnt lgkmcnt(2)\n\t"
            "v_mfma_f32_16x16x32_bf16 v[168:171], v[16:19], v[204:207], v[168:171]\n\t"
            "v_mfma_f32_16x16x32_bf16 v[168:171], v[20:23], v[208:211], v[168:171]\n\t"
            "v_mfma_f32_16x16x32_bf16 v[172:175], v[44:47], v[208:211], v[172:175]\n\t"
            "v_mfma_f32_16x16x32_bf16 v[172:175], v[40:43], v[204:207], v[172:175]\n\t"
            "v_mfma_f32_16x16x32_bf16 v[176:179], v[64:67], v[204:207], v[176:179]\n\t"
            "v_mfma_f32_16x16x32_bf16 v[176:179], v[68:71], v[208:211], v[176:179]\n\t"
            "v_mfma_f32_16x16x32_bf16 v[180:183], v[92:95], v[208:211], v[180:183]\n\t"
            "v_mfma_f32_16x16x32_bf16 v[180:183], v[88:91], v[204:207], v[180:183]\n\t"
            "v_mfma_f32_16x16x32_bf16 v[184:187], v[112:115], v[204:207], v[184:187]\n\t"
            "v_mfma_f32_16x16x32_bf16 v[184:187], v[116:119], v[208:211], v[184:187]\n\t"
            "v_mfma_f32_16x16x32_bf16 v[188:191], v[140:143], v[208:211], v[188:191]\n\t"
            "ds_read_b128 v[208:211], v233 offset:12416\n\t"
            "v_mfma_f32_16x16x32_bf16 v[188:191], v[136:139], v[204:207], v[188:191]\n\t"
            "ds_read_b128 v[204:207], v232 offset:12416\n\t"
            "s_waitcnt lgkmcnt(0)\n\t"
            "s_nop 7\n\t"
            ".p2align 6\n\t"
            "s_nop 0\n\t"
            "s_nop 0\n\t"
            "s_nop 0\n\t"
            ".Lwa_loop%=:\n\t"
            "s_waitcnt lgkmcnt(2)\n\t"
            "v_mfma_f32_16x16x32_bf16 v[192:195], v[0:3], v[212:215], 0\n\t"
            "s_bfe_u32 s49, s70, 0x30001\n\t"
            "s_lshl_b32 s49, s49, 4\n\t"
            "s_or_b32 s73, s77, s49\n\t"
            "v_mfma_f32_16x16x32_bf16 v[192:195], v[4:7], v[224:227], v[192:195]\n\t"
            "v_mad_u32_u24 v220, v246, s72, v253\n\t"
            "v_mad_u32_u24 v216, v246, s72, v252\n\t"
            "v_mfma_f32_16x16x32_bf16 v[148:151], v[28:31], v[224:227], 0\n\t"
            "ds_read_b64 v[222:223], v220 offset:160\n\t"
            "ds_read_b64 v[220:221], v220 offset:128\n\t"
            "v_mfma_f32_16x16x32_bf16 v[148:151], v[24:27], v[212:215], v[148:151]\n\t"
            "v_mul_f32 v176, v237, v176\n\t"
            "v_mul_f32 v177, v237, v177\n\t"
            "v_mfma_f32_16x16x32_bf16 v[196:199], v[48:51], v[212:215], 0\n\t"
            "v_mul_f32 v178, v237, v178\n\t"
            "v_mul_f32 v179, v237, v179\n\t"
            "v_mfma_f32_16x16x32_bf16 v[196:199], v[52:55], v[224:227], v[196:199]\n\t"
            "v_cvt_pk_bf16_f32 v176, v176, v177\n\t"
            "v_mul_f32 v168, v237, v168\n\t"
            "v_mfma_f32_16x16x32_bf16 v[200:203], v[76:79], v[224:227], 0\n\t"
            "v_mul_f32 v169, v237, v169\n\t"
            "v_cvt_pk_bf16_f32 v177, v178, v179\n\t"
            "v_mfma_f32_16x16x32_bf16 v[200:203], v[72:75], v[212:215], v[200:203]\n\t"
            "v_mul_f32 v170, v237, v170\n\t"
            "v_mul_f32 v171, v237, v171\n\t"
            "v_mfma_f32_16x16x32_bf16 v[160:163], v[96:99], v[212:215], 0\n\t"
            "v_cvt_pk_bf16_f32 v168, v168, v169\n\t"
            "v_cvt_pk_bf16_f32 v180, v180, v181\n\t"
            "v_mfma_f32_16x16x32_bf16 v[160:163], v[100:103], v[224:227], v[160:163]\n\t"
            "v_cvt_pk_bf16_f32 v178, v188, v189\n\t"
            "v_cvt_pk_bf16_f32 v179, v190, v191\n\t"
            "v_mfma_f32_16x16x32_bf16 v[164:167], v[124:127], v[224:227], 0\n\t"
            "ds_read_b128 v[224:227], v233 offset:12544\n\t"
            "v_mul_f32 v172, v237, v172\n\t"
            "v_mfma_f32_16x16x32_bf16 v[164:167], v[120:123], v[212:215], v[164:167]\n\t"
            "ds_read_b128 v[212:215], v232 offset:12544\n\t"
            "v_mul_f32 v173, v237, v173\n\t"
            "s_waitcnt lgkmcnt(4)\n\t"
            "v_mfma_f32_16x16x32_bf16 v[192:195], v[8:11], v[204:207], v[192:195]\n\t"
            "v_mul_f32 v174, v237, v174\n\t"
            "v_mul_f32 v175, v237, v175\n\t"
            "v_mfma_f32_16x16x32_bf16 v[192:195], v[12:15], v[208:211], v[192:195]\n\t"
            "v_cvt_pk_bf16_f32 v169, v170, v171\n\t"
            "v_cvt_pk_bf16_f32 v181, v182, v183\n\t"
            "v_mfma_f32_16x16x32_bf16 v[196:199], v[56:59], v[204:207], v[196:199]\n\t"
            "v_cvt_pk_bf16_f32 v170, v172, v173\n\t"
            "v_cvt_pk_bf16_f32 v171, v174, v175\n\t"
            "v_mfma_f32_16x16x32_bf16 v[196:199], v[60:63], v[208:211], v[196:199]\n\t"
            "v_cvt_pk_bf16_f32 v182, v184, v185\n\t"
            "v_cvt_pk_bf16_f32 v183, v186, v187\n\t"
            "v_mfma_f32_16x16x32_bf16 v[160:163], v[104:107], v[204:207], v[160:163]\n\t"
            "v_mov_b32 v230, s76\n\t"
            "v_mov_b32 v239, s74\n\t"
            "v_mfma_f32_16x16x32_bf16 v[160:163], v[108:111], v[208:211], v[160:163]\n\t"
            "s_add_u32 s70, s70, 1\n\t"
            "s_add_u32 s72, s72, 800\n\t"
            "s_add_u32 s74, s74, 800\n\t"
            "s_add_u32 s76, s76, 800\n\t"
            "v_mfma_f32_16x16x32_bf16 v[148:151], v[36:39], v[208:211], v[148:151]\n\t"
            "s_add_u32 s77, s77, 384\n\t"
            "s_xor_b32 s48, s48, s47\n\t"
            "s_sub_u32 s44, s44, 1\n\t"
            "v_mfma_f32_16x16x32_bf16 v[148:151], v[32:35], v[204:207], v[148:151]\n\t"
            "ds_read_b32 v230, v230\n\t"
            "ds_read_b32 v239, v239\n\t"
            "v_mfma_f32_16x16x32_bf16 v[200:203], v[84:87], v[208:211], v[200:203]\n\t"
            "v_mul_f32 v237, 0x3e553b94, v237\n\t"
            "v_mfma_f32_16x16x32_bf16 v[200:203], v[80:83], v[204:207], v[200:203]\n\t"
            "v_mfma_f32_16x16x32_bf16 v[164:167], v[132:135], v[208:211], v[164:167]\n\t"
            "v_mfma_f32_16x16x32_bf16 v[164:167], v[128:131], v[204:207], v[164:167]\n\t"
            "s_waitcnt lgkmcnt(2)\n\t"
            "v_mfma_f32_16x16x32_bf16 v[192:195], v[16:19], v[212:215], v[192:195]\n\t"
            "v_mfma_f32_16x16x32_bf16 v[192:195], v[20:23], v[224:227], v[192:195]\n\t"
            "v_mfma_f32_16x16x32_bf16 v[196:199], v[64:67], v[212:215], v[196:199]\n\t"
            "v_mfma_f32_16x16x32_bf16 v[196:199], v[68:71], v[224:227], v[196:199]\n\t"
            "v_mfma_f32_16x16x32_bf16 v[160:163], v[112:115], v[212:215], v[160:163]\n\t"
            "v_mfma_f32_16x16x32_bf16 v[160:163], v[116:119], v[224:227], v[160:163]\n\t"
            "v_mfma_f32_16x16x32_bf16 v[148:151], v[44:47], v[224:227], v[148:151]\n\t"
            "v_mfma_f32_16x16x32_bf16 v[148:151], v[40:43], v[212:215], v[148:151]\n\t"
            "s_nop 1\n\t"
            "v_mul_f32 v192, v238, v192\n\t"
            "v_mfma_f32_16x16x32_bf16 v[200:203], v[92:95], v[224:227], v[200:203]\n\t"
            "v_mul_f32 v193, v238, v193\n\t"
            "v_mul_f32 v194, v238, v194\n\t"
            "v_mfma_f32_16x16x32_bf16 v[200:203], v[88:91], v[212:215], v[200:203]\n\t"
            "v_mul_f32 v195, v238, v195\n\t"
            "v_cvt_pk_bf16_f32 v192, v192, v193\n\t"
            "v_mfma_f32_16x16x32_bf16 v[164:167], v[140:143], v[224:227], v[164:167]\n\t"
            "ds_read_b128 v[224:227], v216 offset:64\n\t"
            "ds_read_b128 v[216:219], v216\n\t"
            "v_mfma_f32_16x16x32_bf16 v[164:167], v[136:139], v[212:215], v[164:167]\n\t"
            "v_mul_f32 v148, v238, v148\n\t"
            "v_mul_f32 v149, v238, v149\n\t"
            "v_mfma_f32_16x16x16_bf16 v[172:175], v[220:221], v[154:155], 0\n\t"
            "v_mul_f32 v150, v238, v150\n\t"
            "v_mul_f32 v151, v238, v151\n\t"
            "v_cvt_pk_bf16_f32 v193, v194, v195\n\t"
            "v_mfma_f32_16x16x16_bf16 v[184:187], v[152:153], v[178:179], 0\n\t"
            "v_mul_f32 v196, v238, v196\n\t"
            "v_mul_f32 v197, v238, v197\n\t"
            "v_cvt_pk_bf16_f32 v194, v148, v149\n\t"
            "v_mfma_f32_16x16x16_bf16 v[208:211], v[220:221], v[178:179], 0\n\t"
            "v_cvt_pk_bf16_f32 v195, v150, v151\n\t"
            "v_cvt_pk_bf16_f32 v200, v200, v201\n\t"
            "v_mul_f32 v198, v238, v198\n\t"
            "v_mfma_f32_16x16x16_bf16 v[148:151], v[152:153], v[154:155], 0\n\t"
            "v_mul_f32 v199, v238, v199\n\t"
            "v_cvt_pk_bf16_f32 v196, v196, v197\n\t"
            "v_cvt_pk_bf16_f32 v201, v202, v203\n\t"
            "v_mfma_f32_16x16x16_bf16 v[204:207], v[176:177], v[178:179], 0\n\t"
            "v_cvt_pk_bf16_f32 v197, v198, v199\n\t"
            "v_cvt_pk_bf16_f32 v202, v160, v161\n\t"
            "v_cvt_pk_bf16_f32 v203, v162, v163\n\t"
            "v_mfma_f32_16x16x16_bf16 v[160:163], v[176:177], v[154:155], 0\n\t"
            "v_cvt_pk_bf16_f32 v198, v164, v165\n\t"
            "v_cvt_pk_bf16_f32 v199, v166, v167\n\t"
            "s_setprio 0\n\t"
            "s_setprio 3\n\t"
            "v_mfma_f32_16x16x16_bf16 v[164:167], v[196:197], v[154:155], 0\n\t"
            "v_mul_f32 v238, 0x3e553b94, v238\n\t"
            "v_mfma_f32_16x16x16_bf16 v[188:191], v[196:197], v[178:179], 0\n\t"
            "v_mfma_f32_16x16x32_bf16 v[148:151], v[144:147], v[156:159], v[148:151]\n\t"
            "s_waitcnt lgkmcnt(0)\n\t"
            "v_mfma_f32_16x16x32_bf16 v[172:175], v[216:219], v[156:159], v[172:175]\n\t"
            "v_mfma_f32_16x16x32_bf16 v[184:187], v[144:147], v[180:183], v[184:187]\n\t"
            "v_mfma_f32_16x16x32_bf16 v[208:211], v[216:219], v[180:183], v[208:211]\n\t"
            "v_mfma_f32_16x16x32_bf16 v[160:163], v[168:171], v[156:159], v[160:163]\n\t"
            "v_mfma_f32_16x16x32_bf16 v[204:207], v[168:171], v[180:183], v[204:207]\n\t"
            "v_mfma_f32_16x16x32_bf16 v[164:167], v[192:195], v[156:159], v[164:167]\n\t"
            "v_mfma_f32_16x16x32_bf16 v[188:191], v[192:195], v[180:183], v[188:191]\n\t"
            "s_nop 1\n\t"
            "v_max3_f32 v228, v172, v148, v149\n\t"
            "v_mfma_f32_16x16x16_bf16 v[212:215], v[222:223], v[152:153], 0\n\t"
            "v_max3_f32 v228, v228, v150, v151\n\t"
            "v_max3_f32 v229, v208, v184, v185\n\t"
            "v_mfma_f32_16x16x16_bf16 v[156:159], v[222:223], v[196:197], 0\n\t"
            "v_max3_f32 v229, v229, v186, v187\n\t"
            "v_max3_f32 v228, v228, v160, v161\n\t"
            "v_max3_f32 v229, v229, v204, v205\n\t"
            "v_mfma_f32_16x16x32_bf16 v[212:215], v[224:227], v[144:147], v[212:215]\n\t"
            "v_max3_f32 v228, v228, v162, v163\n\t"
            "v_max3_f32 v229, v229, v206, v207\n\t"
            "v_mfma_f32_16x16x32_bf16 v[156:159], v[224:227], v[192:195], v[156:159]\n\t"
            "v_max3_f32 v228, v228, v164, v165\n\t"
            "v_max3_f32 v229, v229, v188, v189\n\t"
            "v_mfma_f32_16x16x16_bf16 v[180:183], v[152:153], v[198:199], 0\n\t"
            "v_max3_f32 v228, v228, v166, v167\n\t"
            "v_max3_f32 v229, v229, v190, v191\n\t"
            "s_nop 1\n\t"
            "v_permlane32_swap_b32 v228, v229\n\t"
            "v_max_f32 v228, v228, v229\n\t"
            "v_mov_b32 v231, v228\n\t"
            "v_mfma_f32_16x16x32_bf16 v[180:183], v[144:147], v[200:203], v[180:183]\n\t"
            "s_setprio 0\n\t"
            "v_permlane16_swap_b32 v228, v231\n\t"
            "v_max_f32 v228, v228, v231\n\t"
            "v_mov_b32 v229, v228\n\t"
            "s_nop 1\n\t"
            "v_permlane32_swap_b32 v228, v229\n\t"
            "v_mul_f32_e64 v228, -v228, v236\n\t"
            "v_fma_f32 v148, v148, v236, v228\n\t"
            "v_fma_f32 v149, v149, v236, v228\n\t"
            "v_exp_f32 v148, v148\n\t"
            "v_exp_f32 v149, v149\n\t"
            "v_fma_f32 v150, v150, v236, v228\n\t"
            "v_fma_f32 v151, v151, v236, v228\n\t"
            "v_exp_f32 v150, v150\n\t"
            "v_exp_f32 v151, v151\n\t"
            "v_fma_f32 v160, v160, v236, v228\n\t"
            "v_fma_f32 v161, v161, v236, v228\n\t"
            "v_fma_f32 v162, v162, v236, v228\n\t"
            "v_fma_f32 v163, v163, v236, v228\n\t"
            "v_exp_f32 v160, v160\n\t"
            "v_exp_f32 v161, v161\n\t"
            "v_exp_f32 v162, v162\n\t"
            "v_exp_f32 v163, v163\n\t"
            "v_cvt_pk_bf16_f32 v148, v148, v149\n\t"
            "v_cvt_pk_bf16_f32 v149, v150, v151\n\t"
            "v_cvt_pk_bf16_f32 v150, v160, v161\n\t"
            "v_cvt_pk_bf16_f32 v151, v162, v163\n\t"
            "s_setprio 3\n\t"
            "v_mfma_f32_16x16x16_bf16 v[160:163], v[222:223], v[176:177], 0\n\t"
            "s_setprio 0\n\t"
            "v_mul_f32_e64 v229, -v229, v237\n\t"
            "v_fma_f32 v188, v188, v237, v229\n\t"
            "v_fma_f32 v189, v189, v237, v229\n\t"
            "v_exp_f32 v188, v188\n\t"
            "v_exp_f32 v189, v189\n\t"
            "v_fma_f32 v190, v190, v237, v229\n\t"
            "s_setprio 3\n\t"
            "v_mfma_f32_16x16x32_bf16 v[160:163], v[224:227], v[168:171], v[160:163]\n\t"
            "s_setprio 0\n\t"
            "v_fma_f32 v191, v191, v237, v229\n\t"
            "v_fma_f32 v208, v208, v237, v229\n\t"
            "v_fma_f32 v184, v184, v237, v229\n\t"
            "v_fma_f32 v185, v185, v237, v229\n\t"
            "v_exp_f32 v190, v190\n\t"
            "v_exp_f32 v191, v191\n\t"
            "v_exp_f32 v208, v208\n\t"
            "v_exp_f32 v184, v184\n\t"
            "v_max3_f32 v234, v212, v160, v156\n\t"
            "v_max_f32 v234, v234, v230\n\t"
            "v_exp_f32 v185, v185\n\t"
            "v_fma_f32 v186, v186, v237, v229\n\t"
            "v_fma_f32 v187, v187, v237, v229\n\t"
            "v_fma_f32 v164, v164, v236, v228\n\t"
            "v_fma_f32 v165, v165, v236, v228\n\t"
            "v_exp_f32 v186, v186\n\t"
            "v_exp_f32 v187, v187\n\t"
            "v_fma_f32 v204, v204, v237, v229\n\t"
            "v_fma_f32 v205, v205, v237, v229\n\t"
            "v_fma_f32 v206, v206, v237, v229\n\t"
            "v_fma_f32 v207, v207, v237, v229\n\t"
            "v_cvt_pk_bf16_f32 v188, v188, v189\n\t"
            "v_exp_f32 v164, v164\n\t"
            "v_exp_f32 v165, v165\n\t"
            "v_fma_f32 v166, v166, v236, v228\n\t"
            "v_fma_f32 v167, v167, v236, v228\n\t"
            "v_fma_f32 v172, v172, v236, v228\n\t"
            "v_exp_f32 v204, v204\n\t"
            "v_exp_f32 v205, v205\n\t"
            "v_exp_f32 v206, v206\n\t"
            "v_exp_f32 v207, v207\n\t"
            "v_cvt_pk_bf16_f32 v189, v190, v191\n\t"
            "v_cvt_pk_bf16_f32 v208, v208, 0\n\t"
            "v_max_f32_dpp v234, v234, v234 row_ror:8 row_mask:0xf bank_mask:0xf\n\t"
            "v_exp_f32 v166, v166\n\t"
            "v_exp_f32 v167, v167\n\t"
            "v_exp_f32 v172, v172\n\t"
            "v_cvt_pk_bf16_f32 v184, v184, v185\n\t"
            "v_cndmask_b32_e64 v190, 0, v208, s[58:59]\n\t"
            "s_setprio 3\n\t"
            "v_mfma_f32_16x16x16_bf16 v[208:211], v[220:221], v[198:199], 0\n\t"
            "v_cvt_pk_bf16_f32 v185, v186, v187\n\t"
            "v_cvt_pk_bf16_f32 v164, v164, v165\n\t"
            "v_cvt_pk_bf16_f32 v186, v204, v205\n\t"
            "v_cvt_pk_bf16_f32 v187, v206, v207\n\t"
            "v_mfma_f32_16x16x16_bf16 v[204:207], v[176:177], v[198:199], 0\n\t"
            "v_max_f32_dpp v234, v234, v234 row_ror:4 row_mask:0xf bank_mask:0xf\n\t"
            "v_cvt_pk_bf16_f32 v165, v166, v167\n\t"
            "v_cvt_pk_bf16_f32 v172, v172, 0\n\t"
            "v_mfma_f32_16x16x32_bf16 v[208:211], v[216:219], v[200:203], v[208:211]\n\t"
            "v_cndmask_b32_e64 v166, 0, v172, s[58:59]\n\t"
            "v_max_f32_dpp v234, v234, v234 row_ror:2 row_mask:0xf bank_mask:0xf\n\t"
            "v_mfma_f32_16x16x16_bf16 v[172:175], v[196:197], v[198:199], 0\n\t"
            "v_cndmask_b32_e64 v235, v212, v160, s[60:61]\n\t"
            "v_cndmask_b32_e64 v235, v235, v156, s[62:63]\n\t"
            "v_max_f32_dpp v234, v234, v234 row_ror:1 row_mask:0xf bank_mask:0xf\n\t"
            "v_mfma_f32_16x16x32_bf16 v[204:207], v[168:171], v[200:203], v[204:207]\n\t"
            "v_cndmask_b32_e64 v235, v235, v230, s[64:65]\n\t"
            "v_sub_f32 v235, v235, v234\n\t"
            "v_mfma_f32_16x16x32_bf16 v[172:175], v[192:195], v[200:203], v[172:175]\n\t"
            "s_setprio 0\n\t"
            "v_mul_f32 v235, v239, v235\n\t"
            "v_exp_f32 v235, v235\n\t"
            "v_max3_f32 v230, v208, v180, v181\n\t"
            "v_cvt_pk_bf16_f32 v235, v235, 0\n\t"
            "v_cndmask_b32_e64 v235, 0, v235, s[66:67]\n\t"
            "v_max3_f32 v230, v230, v182, v183\n\t"
            "v_max3_f32 v230, v230, v204, v205\n\t"
            "v_max3_f32 v230, v230, v206, v207\n\t"
            "v_max3_f32 v230, v230, v172, v173\n\t"
            "v_max3_f32 v230, v230, v174, v175\n\t"
            "v_mov_b32 v231, v230\n\t"
            "v_mov_b32 v167, 0\n\t"
            "v_mov_b32 v191, 0\n\t"
            "v_permlane32_swap_b32 v230, v231\n\t"
            "v_max_f32 v230, v230, v231\n\t"
            "v_mov_b32 v231, v230\n\t"
            "ds_write_b16 v250, v235 offset:256\n\t"
            "s_nop 0\n\t"
            "v_permlane16_swap_b32 v230, v231\n\t"
            "v_max_f32 v230, v230, v231\n\t"
            "v_mul_f32_e64 v230, -v230, v238\n\t"
            "v_fma_f32 v180, v180, v238, v230\n\t"
            "v_fma_f32 v181, v181, v238, v230\n\t"
            "v_fma_f32 v172, v172, v238, v230\n\t"
            "v_fma_f32 v173, v173, v238, v230\n\t"
            "v_exp_f32 v180, v180\n\t"
            "v_exp_f32 v181, v181\n\t"
            "v_fma_f32 v182, v182, v238, v230\n\t"
            "v_fma_f32 v183, v183, v238, v230\n\t"
            "v_exp_f32 v172, v172\n\t"
            "v_exp_f32 v173, v173\n\t"
            "v_fma_f32 v174, v174, v238, v230\n\t"
            "v_fma_f32 v175, v175, v238, v230\n\t"
            "v_fma_f32 v208, v208, v238, v230\n\t"
            "v_exp_f32 v182, v182\n\t"
            "v_exp_f32 v183, v183\n\t"
            "v_fma_f32 v204, v204, v238, v230\n\t"
            "v_fma_f32 v205, v205, v238, v230\n\t"
            "v_fma_f32 v206, v206, v238, v230\n\t"
            "v_fma_f32 v207, v207, v238, v230\n\t"
            "v_exp_f32 v174, v174\n\t"
            "v_exp_f32 v175, v175\n\t"
            "v_exp_f32 v208, v208\n\t"
            "v_exp_f32 v204, v204\n\t"
            "v_exp_f32 v205, v205\n\t"
            "v_exp_f32 v206, v206\n\t"
            "v_exp_f32 v207, v207\n\t"
            "v_cvt_pk_bf16_f32 v180, v180, v181\n\t"
            "v_cvt_pk_bf16_f32 v172, v172, v173\n\t"
            "v_cvt_pk_bf16_f32 v181, v182, v183\n\t"
            "v_cvt_pk_bf16_f32 v173, v174, v175\n\t"
            "v_cvt_pk_bf16_f32 v208, v208, 0\n\t"
            "v_cvt_pk_bf16_f32 v182, v204, v205\n\t"
            "v_cvt_pk_bf16_f32 v183, v206, v207\n\t"
            "v_cndmask_b32_e64 v174, 0, v208, s[58:59]\n\t"
            "v_mov_b32 v175, 0\n\t"
            "s_barrier\n\t"
            "s_setprio 2\n\t"
            "v_mfma_f32_16x16x32_bf16 v[200:203], v[240:243], v[148:151], 0\n\t"
            "ds_read_b64 v[176:177], v247\n\t"
            "ds_read_b64 v[196:197], v247 offset:1536\n\t"
            "v_mfma_f32_16x16x32_bf16 v[200:203], v[240:243], v[164:167], v[200:203]\n\t"
            "ds_read_b64 v[220:221], v247 offset:3072\n\t"
            "v_xor_b32 v234, s73, v254\n\t"
            "v_add_u32 v235, s45, v248\n\t"
            "v_add_u32 v232, s48, v244\n\t"
            "v_xor_b32 v233, 64, v232\n\t"
            "v_mov_b32 v179, 0\n\t"
            "v_mov_b32 v199, 0\n\t"
            "v_mov_b32 v223, 0\n\t"
            "v_rcp_f32 v201, v200\n\t"
            "ds_read_b128 v[168:171], v235\n\t"
            "ds_read_u16 v178, v234\n\t"
            "ds_read_b128 v[192:195], v235 offset:1536\n\t"
            "ds_read_u16 v198, v234 offset:128\n\t"
            "ds_read_b128 v[216:219], v235 offset:3072\n\t"
            "ds_read_u16 v222, v234 offset:256\n\t"
            "ds_read_b128 v[204:207], v232\n\t"
            "ds_read_b128 v[208:211], v233\n\t"
            "v_add_u32 v235, s48, v248\n\t"
            "v_mad_u32_u24 v234, v246, s75, v253\n\t"
            "ds_read_b128 v[228:231], v235 offset:18816\n\t"
            "s_waitcnt lgkmcnt(7)\n\t"
            "v_mfma_f32_16x16x32_bf16 v[224:227], v[168:171], v[148:151], 0\n\t"
            "v_mfma_f32_16x16x32_bf16 v[224:227], v[176:179], v[164:167], v[224:227]\n\t"
            "s_waitcnt lgkmcnt(5)\n\t"
            "v_mfma_f32_16x16x32_bf16 v[212:215], v[192:195], v[148:151], 0\n\t"
            "s_waitcnt lgkmcnt(0)\n\t"
            "v_add_f32 v236, v228, v230\n\t"
            "v_mfma_f32_16x16x32_bf16 v[212:215], v[196:199], v[164:167], v[212:215]\n\t"
            "v_add_f32 v237, v229, v231\n\t"
            "v_mfma_f32_16x16x32_bf16 v[148:151], v[216:219], v[148:151], 0\n\t"
            "ds_read_b128 v[228:231], v235 offset:19840\n\t"
            "v_mfma_f32_16x16x32_bf16 v[148:151], v[220:223], v[164:167], v[148:151]\n\t"
            "v_mul_f32 v224, v201, v224\n\t"
            "v_mul_f32 v225, v201, v225\n\t"
            "v_mfma_f32_16x16x32_bf16 v[144:147], v[0:3], v[204:207], 0\n\t"
            "s_nop 0\n\t"
            "v_mul_f32 v212, v201, v212\n\t"
            "v_mul_f32 v213, v201, v213\n\t"
            "v_mfma_f32_16x16x32_bf16 v[144:147], v[4:7], v[208:211], v[144:147]\n\t"
            "v_mul_f32 v226, v201, v226\n\t"
            "v_mul_f32 v227, v201, v227\n\t"
            "v_mfma_f32_16x16x32_bf16 v[152:155], v[48:51], v[204:207], 0\n\t"
            "v_cvt_pk_bf16_f32 v224, v224, v225\n\t"
            "v_mul_f32 v214, v201, v214\n\t"
            "v_mfma_f32_16x16x32_bf16 v[152:155], v[52:55], v[208:211], v[152:155]\n\t"
            "v_mul_f32 v215, v201, v215\n\t"
            "v_cvt_pk_bf16_f32 v212, v212, v213\n\t"
            "v_mfma_f32_16x16x32_bf16 v[156:159], v[76:79], v[208:211], 0\n\t"
            "v_cvt_pk_bf16_f32 v225, v226, v227\n\t"
            "v_cvt_pk_bf16_f32 v213, v214, v215\n\t"
            "v_mfma_f32_16x16x32_bf16 v[156:159], v[72:75], v[204:207], v[156:159]\n\t"
            "ds_write_b64 v245, v[224:225]\n\t"
            "v_mfma_f32_16x16x32_bf16 v[224:227], v[168:171], v[184:187], 0\n\t"
            "v_mul_f32 v148, v201, v148\n\t"
            "v_mul_f32 v149, v201, v149\n\t"
            "v_mfma_f32_16x16x32_bf16 v[224:227], v[176:179], v[188:191], v[224:227]\n\t"
            "ds_write_b64 v245, v[212:213] offset:128\n\t"
            "v_mfma_f32_16x16x32_bf16 v[212:215], v[192:195], v[184:187], 0\n\t"
            "v_mul_f32 v150, v201, v150\n\t"
            "v_mul_f32 v151, v201, v151\n\t"
            "v_mfma_f32_16x16x32_bf16 v[212:215], v[196:199], v[188:191], v[212:215]\n\t"
            "v_cvt_pk_bf16_f32 v148, v148, v149\n\t"
            "v_cvt_pk_bf16_f32 v149, v150, v151\n\t"
            "v_mfma_f32_16x16x32_bf16 v[200:203], v[240:243], v[184:187], 0\n\t"
            "s_waitcnt lgkmcnt(2)\n\t"
            "v_add_f32 v228, v228, v230\n\t"
            "v_add_f32 v236, v236, v228\n\t"
            "v_mfma_f32_16x16x32_bf16 v[200:203], v[240:243], v[188:191], v[200:203]\n\t"
            "ds_write_b64 v245, v[148:149] offset:256\n\t"
            "v_mfma_f32_16x16x32_bf16 v[184:187], v[216:219], v[184:187], 0\n\t"
            "v_add_f32 v229, v229, v231\n\t"
            "v_mul_f32 v236, 0x3baaaaab, v236\n\t"
            "v_mfma_f32_16x16x32_bf16 v[184:187], v[220:223], v[188:191], v[184:187]\n\t"
            "v_add_f32 v237, v237, v229\n\t"
            "v_mul_f32_e64 v236, v236, -v236\n\t"
            "v_mfma_f32_16x16x32_bf16 v[148:151], v[28:31], v[208:211], 0\n\t"
            "v_rcp_f32 v201, v200\n\t"
            "v_mfma_f32_16x16x32_bf16 v[148:151], v[24:27], v[204:207], v[148:151]\n\t"
            "v_mul_f32 v224, v201, v224\n\t"
            "v_mul_f32 v225, v201, v225\n\t"
            "v_mfma_f32_16x16x32_bf16 v[160:163], v[96:99], v[204:207], 0\n\t"
            "v_mul_f32 v226, v201, v226\n\t"
            "v_mul_f32 v227, v201, v227\n\t"
            "v_mfma_f32_16x16x32_bf16 v[160:163], v[100:103], v[208:211], v[160:163]\n\t"
            "v_mul_f32 v212, v201, v212\n\t"
            "v_mul_f32 v213, v201, v213\n\t"
            "v_mfma_f32_16x16x32_bf16 v[164:167], v[124:127], v[208:211], 0\n\t"
            "ds_read_b128 v[208:211], v233 offset:128\n\t"
            "v_mul_f32 v214, v201, v214\n\t"
            "v_mfma_f32_16x16x32_bf16 v[164:167], v[120:123], v[204:207], v[164:167]\n\t"
            "ds_read_b128 v[204:207], v232 offset:128\n\t"
            "v_mul_f32 v215, v201, v215\n\t"
            "v_mfma_f32_16x16x32_bf16 v[188:191], v[192:195], v[180:183], 0\n\t"
            "v_mul_f32 v184, v201, v184\n\t"
            "v_mul_f32 v185, v201, v185\n\t"
            "v_mfma_f32_16x16x32_bf16 v[188:191], v[196:199], v[172:175], v[188:191]\n\t"
            "v_mul_f32 v186, v201, v186\n\t"
            "v_mul_f32 v187, v201, v187\n\t"
            "v_mfma_f32_16x16x32_bf16 v[200:203], v[240:243], v[180:183], 0\n\t"
            "v_cvt_pk_bf16_f32 v224, v224, v225\n\t"
            "v_cvt_pk_bf16_f32 v225, v226, v227\n\t"
            "v_mfma_f32_16x16x32_bf16 v[200:203], v[240:243], v[172:175], v[200:203]\n\t"
            "ds_write_b64 v245, v[224:225] offset:6144\n\t"
            "v_mfma_f32_16x16x32_bf16 v[224:227], v[168:171], v[180:183], 0\n\t"
            "v_cvt_pk_bf16_f32 v184, v184, v185\n\t"
            "v_cvt_pk_bf16_f32 v185, v186, v187\n\t"
            "v_mfma_f32_16x16x32_bf16 v[224:227], v[176:179], v[172:175], v[224:227]\n\t"
            "ds_read_b64 v[186:187], v234 offset:288\n\t"
            "ds_write_b64 v245, v[184:185] offset:6400\n\t"
            "v_mfma_f32_16x16x32_bf16 v[180:183], v[216:219], v[180:183], 0\n\t"
            "ds_read_b64 v[184:185], v234 offset:256\n\t"
            "v_rcp_f32 v201, v200\n\t"
            "v_mfma_f32_16x16x32_bf16 v[180:183], v[220:223], v[172:175], v[180:183]\n\t"
            "v_mul_f32 v188, v201, v188\n\t"
            "v_mul_f32 v189, v201, v189\n\t"
            "s_waitcnt lgkmcnt(4)\n\t"
            "v_mfma_f32_16x16x32_bf16 v[144:147], v[8:11], v[204:207], v[144:147]\n\t"
            "v_mul_f32 v190, v201, v190\n\t"
            "v_mul_f32 v191, v201, v191\n\t"
            "v_mfma_f32_16x16x32_bf16 v[144:147], v[12:15], v[208:211], v[144:147]\n\t"
            "v_cvt_pk_bf16_f32 v188, v188, v189\n\t"
            "v_cvt_pk_bf16_f32 v189, v190, v191\n\t"
            "v_mfma_f32_16x16x32_bf16 v[148:151], v[36:39], v[208:211], v[148:151]\n\t"
            "ds_read_b64 v[190:191], v234 offset:352\n\t"
            "v_mul_f32 v224, v201, v224\n\t"
            "v_mfma_f32_16x16x32_bf16 v[148:151], v[32:35], v[204:207], v[148:151]\n\t"
            "ds_write_b64 v245, v[188:189] offset:12416\n\t"
            "v_mfma_f32_16x16x32_bf16 v[152:155], v[56:59], v[204:207], v[152:155]\n\t"
            "ds_read_b64 v[188:189], v234 offset:320\n\t"
            "v_mul_f32 v225, v201, v225\n\t"
            "v_mfma_f32_16x16x32_bf16 v[152:155], v[60:63], v[208:211], v[152:155]\n\t"
            "v_cvt_pk_bf16_f32 v212, v212, v213\n\t"
            "v_mul_f32 v226, v201, v226\n\t"
            "v_mfma_f32_16x16x32_bf16 v[156:159], v[84:87], v[208:211], v[156:159]\n\t"
            "v_mul_f32 v227, v201, v227\n\t"
            "v_cvt_pk_bf16_f32 v224, v224, v225\n\t"
            "v_mfma_f32_16x16x32_bf16 v[156:159], v[80:83], v[204:207], v[156:159]\n\t"
            "v_cvt_pk_bf16_f32 v213, v214, v215\n\t"
            "v_cvt_pk_bf16_f32 v225, v226, v227\n\t"
            "v_mfma_f32_16x16x32_bf16 v[160:163], v[104:107], v[204:207], v[160:163]\n\t"
            "v_mul_f32 v180, v201, v180\n\t"
            "v_mul_f32 v181, v201, v181\n\t"
            "v_mfma_f32_16x16x32_bf16 v[160:163], v[108:111], v[208:211], v[160:163]\n\t"
            "ds_write_b64 v245, v[212:213] offset:6272\n\t"
            "v_mfma_f32_16x16x32_bf16 v[164:167], v[132:135], v[208:211], v[164:167]\n\t"
            "ds_read_b128 v[212:215], v232 offset:256\n\t"
            "ds_read_b128 v[208:211], v233 offset:6144\n\t"
            "v_mfma_f32_16x16x32_bf16 v[164:167], v[128:131], v[204:207], v[164:167]\n\t"
            "ds_read_b128 v[204:207], v232 offset:6144\n\t"
            "ds_write_b64 v245, v[224:225] offset:12288\n\t"
            "ds_read_b128 v[224:227], v233 offset:256\n\t"
            "v_mul_f32 v182, v201, v182\n\t"
            "v_mul_f32 v183, v201, v183\n\t"
            "v_cvt_pk_bf16_f32 v180, v180, v181\n\t"
            "s_waitcnt lgkmcnt(6)\n\t"
            "v_mfma_f32_16x16x32_bf16 v[200:203], v[240:243], v[184:187], 0\n\t"
            "v_cvt_pk_bf16_f32 v181, v182, v183\n\t"
            "v_fmac_f32 v236, 0x3baaaaab, v237\n\t"
            "v_mfma_f32_16x16x32_bf16 v[200:203], v[240:243], v[188:191], v[200:203]\n\t"
            "ds_write_b64 v245, v[180:181] offset:12544\n\t"
            "v_mfma_f32_16x16x32_bf16 v[168:171], v[184:187], v[168:171], 0\n\t"
            "v_max_f32 v236, 0, v236\n\t"
            "v_add_f32 v236, 0x3727c5ac, v236\n\t"
            "v_mfma_f32_16x16x32_bf16 v[168:171], v[188:191], v[176:179], v[168:171]\n\t"
            "v_rsq_f32 v236, v236\n\t"
            "v_mfma_f32_16x16x32_bf16 v[192:195], v[184:187], v[192:195], 0\n\t"
            "s_nop 0\n\t"
            "v_rcp_f32 v201, v200\n\t"
            "v_mfma_f32_16x16x32_bf16 v[192:195], v[188:191], v[196:199], v[192:195]\n\t"
            "ds_write_b32 v249, v236\n\t"
            "v_mfma_f32_16x16x32_bf16 v[216:219], v[184:187], v[216:219], 0\n\t"
            "ds_read_b32 v236, v251\n\t"
            "ds_read_b32 v237, v251 offset:64\n\t"
            "v_mfma_f32_16x16x32_bf16 v[216:219], v[188:191], v[220:223], v[216:219]\n\t"
            "ds_read_b32 v238, v251 offset:128\n\t"
            "v_xad_u32 v202, s73, v254, v255\n\t"
            "s_waitcnt lgkmcnt(5)\n\t"
            "v_mfma_f32_16x16x32_bf16 v[144:147], v[16:19], v[212:215], v[144:147]\n\t"
            "v_cndmask_b32_e64 v203, v168, v192, s[60:61]\n\t"
            "v_mfma_f32_16x16x32_bf16 v[144:147], v[20:23], v[224:227], v[144:147]\n\t"
            "v_mfma_f32_16x16x32_bf16 v[148:151], v[44:47], v[224:227], v[148:151]\n\t"
            "s_nop 0\n\t"
            "v_cndmask_b32_e64 v203, v203, v216, s[62:63]\n\t"
            "v_mfma_f32_16x16x32_bf16 v[148:151], v[40:43], v[212:215], v[148:151]\n\t"
            "s_nop 0\n\t"
            "v_mul_f32_dpp v203, v201, v203 quad_perm:[0,0,0,0] row_mask:0xf bank_mask:0xf\n\t"
            "v_cvt_pk_bf16_f32 v203, v203, 0\n\t"
            "v_mfma_f32_16x16x32_bf16 v[152:155], v[64:67], v[212:215], v[152:155]\n\t"
            "v_mfma_f32_16x16x32_bf16 v[152:155], v[68:71], v[224:227], v[152:155]\n\t"
            "s_mov_b64 exec, s[68:69]\n\t"
            "ds_write_b16 v202, v203\n\t"
            "s_mov_b64 exec, 1\n\t"
            "ds_add_u32 v253, v246 offset:7856\n\t"
            "s_mov_b64 exec, -1\n\t"
            "v_mfma_f32_16x16x32_bf16 v[156:159], v[92:95], v[224:227], v[156:159]\n\t"
            "s_waitcnt lgkmcnt(4)\n\t"
            "v_mul_f32 v144, v236, v144\n\t"
            "v_mul_f32 v145, v236, v145\n\t"
            "v_mfma_f32_16x16x32_bf16 v[156:159], v[88:91], v[212:215], v[156:159]\n\t"
            "s_cmp_eq_u32 s44, 0\n\t"
            "s_cbranch_scc1 .Lwa_done%=\n\t"
            "v_mul_f32 v152, v236, v152\n\t"
            "v_mul_f32 v153, v236, v153\n\t"
            "v_mfma_f32_16x16x32_bf16 v[160:163], v[112:115], v[212:215], v[160:163]\n\t"
            "v_mul_f32 v146, v236, v146\n\t"
            "v_mul_f32 v147, v236, v147\n\t"
            "v_mfma_f32_16x16x32_bf16 v[160:163], v[116:119], v[224:227], v[160:163]\n\t"
            "v_mul_f32 v154, v236, v154\n\t"
            "v_mul_f32 v155, v236, v155\n\t"
            "v_mfma_f32_16x16x32_bf16 v[164:167], v[140:143], v[224:227], v[164:167]\n\t"
            "ds_read_b128 v[224:227], v233 offset:6272\n\t"
            "v_cvt_pk_bf16_f32 v144, v144, v145\n\t"
            "v_mfma_f32_16x16x32_bf16 v[164:167], v[136:139], v[212:215], v[164:167]\n\t"
            "ds_read_b128 v[212:215], v232 offset:6272\n\t"
            "v_cvt_pk_bf16_f32 v152, v152, v153\n\t"
            "v_mfma_f32_16x16x32_bf16 v[168:171], v[0:3], v[204:207], 0\n\t"
            "v_cvt_pk_bf16_f32 v156, v156, v157\n\t"
            "v_mul_f32 v148, v236, v148\n\t"
            "v_mfma_f32_16x16x32_bf16 v[168:171], v[4:7], v[208:211], v[168:171]\n\t"
            "v_mul_f32 v149, v236, v149\n\t"
            "v_mul_f32 v150, v236, v150\n\t"
            "v_mfma_f32_16x16x32_bf16 v[172:175], v[28:31], v[208:211], 0\n\t"
            "v_mul_f32 v151, v236, v151\n\t"
            "v_cvt_pk_bf16_f32 v145, v146, v147\n\t"
            "v_mfma_f32_16x16x32_bf16 v[172:175], v[24:27], v[204:207], v[172:175]\n\t"
            "v_cvt_pk_bf16_f32 v153, v154, v155\n\t"
            "v_cvt_pk_bf16_f32 v157, v158, v159\n\t"
            "v_mfma_f32_16x16x32_bf16 v[176:179], v[48:51], v[204:207], 0\n\t"
            "v_cvt_pk_bf16_f32 v146, v148, v149\n\t"
            "v_cvt_pk_bf16_f32 v147, v150, v151\n\t"
            "v_mfma_f32_16x16x32_bf16 v[176:179], v[52:55], v[208:211], v[176:179]\n\t"
            "v_cvt_pk_bf16_f32 v158, v160, v161\n\t"
            "v_cvt_pk_bf16_f32 v159, v162, v163\n\t"
            "v_mfma_f32_16x16x32_bf16 v[180:183], v[76:79], v[208:211], 0\n\t"
            "v_cvt_pk_bf16_f32 v154, v164, v165\n\t"
            "v_cvt_pk_bf16_f32 v155, v166, v167\n\t"
            "v_mfma_f32_16x16x32_bf16 v[180:183], v[72:75], v[204:207], v[180:183]\n\t"
            "v_mul_f32 v236, 0x3e553b94, v236\n\t"
            "v_mfma_f32_16x16x32_bf16 v[184:187], v[96:99], v[204:207], 0\n\t"
            "v_mfma_f32_16x16x32_bf16 v[184:187], v[100:103], v[208:211], v[184:187]\n\t"
            "v_mfma_f32_16x16x32_bf16 v[188:191], v[124:127], v[208:211], 0\n\t"
            "ds_read_b128 v[208:211], v233 offset:6400\n\t"
            "v_mfma_f32_16x16x32_bf16 v[188:191], v[120:123], v[204:207], v[188:191]\n\t"
            "ds_read_b128 v[204:207], v232 offset:6400\n\t"
            "s_waitcnt lgkmcnt(2)\n\t"
            "v_mfma_f32_16x16x32_bf16 v[168:171], v[8:11], v[212:215], v[168:171]\n\t"
            "v_mfma_f32_16x16x32_bf16 v[168:171], v[12:15], v[224:227], v[168:171]\n\t"
            "v_mfma_f32_16x16x32_bf16 v[172:175], v[36:39], v[224:227], v[172:175]\n\t"
            "v_mfma_f32_16x16x32_bf16 v[172:175], v[32:35], v[212:215], v[172:175]\n\t"
            "v_mfma_f32_16x16x32_bf16 v[176:179], v[56:59], v[212:215], v[176:179]\n\t"
            "v_mfma_f32_16x16x32_bf16 v[176:179], v[60:63], v[224:227], v[176:179]\n\t"
            "v_mfma_f32_16x16x32_bf16 v[180:183], v[84:87], v[224:227], v[180:183]\n\t"
            "v_mfma_f32_16x16x32_bf16 v[180:183], v[80:83], v[212:215], v[180:183]\n\t"
            "v_mfma_f32_16x16x32_bf16 v[184:187], v[104:107], v[212:215], v[184:187]\n\t"
            "v_mfma_f32_16x16x32_bf16 v[184:187], v[108:111], v[224:227], v[184:187]\n\t"
            "v_mfma_f32_16x16x32_bf16 v[188:191], v[132:135], v[224:227], v[188:191]\n\t"
            "ds_read_b128 v[224:227], v233 offset:12288\n\t"
            "v_mfma_f32_16x16x32_bf16 v[188:191], v[128:131], v[212:215], v[188:191]\n\t"
            "ds_read_b128 v[212:215], v232 offset:12288\n\t"
            "s_waitcnt lgkmcnt(2)\n\t"
            "v_mfma_f32_16x16x32_bf16 v[168:171], v[16:19], v[204:207], v[168:171]\n\t"
            "v_mfma_f32_16x16x32_bf16 v[168:171], v[20:23], v[208:211], v[168:171]\n\t"
            "v_mfma_f32_16x16x32_bf16 v[172:175], v[44:47], v[208:211], v[172:175]\n\t"
            "v_mfma_f32_16x16x32_bf16 v[172:175], v[40:43], v[204:207], v[172:175]\n\t"
            "v_mfma_f32_16x16x32_bf16 v[176:179], v[64:67], v[204:207], v[176:179]\n\t"
            "v_mfma_f32_16x16x32_bf16 v[176:179], v[68:71], v[208:211], v[176:179]\n\t"
            "v_mfma_f32_16x16x32_bf16 v[180:183], v[92:95], v[208:211], v[180:183]\n\t"
            "v_mfma_f32_16x16x32_bf16 v[180:183], v[88:91], v[204:207], v[180:183]\n\t"
            "v_mfma_f32_16x16x32_bf16 v[184:187], v[112:115], v[204:207], v[184:187]\n\t"
            "v_mfma_f32_16x16x32_bf16 v[184:187], v[116:119], v[208:211], v[184:187]\n\t"
            "v_mfma_f32_16x16x32_bf16 v[188:191], v[140:143], v[208:211], v[188:191]\n\t"
            "ds_read_b128 v[208:211], v233 offset:12416\n\t"
            "v_mfma_f32_16x16x32_bf16 v[188:191], v[136:139], v[204:207], v[188:191]\n\t"
            "ds_read_b128 v[204:207], v232 offset:12416\n\t"
            "s_branch .Lwa_loop%=\n\t"
            ".Lwa_done%=:\n\t"
            "s_waitcnt lgkmcnt(0)\n\t"
            "s_nop 7\n\t"
            "s_barrier\n\t"
            : "+{s44}"(s44), "+{s48}"(s48), "+{s70}"(s70), "+{s72}"(s72), "+{s73}"(s73), "+{s74}"(s74), "+{s76}"(s76), "+{s77}"(s77)
            : "{v244}"(pE_), "{v245}"(aO_), "{v246}"(sel_), "{v247}"(aVB_), "{v248}"(pST_), "{v249}"(aRSw_), "{v250}"(aPLw_), "{v251}"(aRSr_), "{v252}"(g16_), "{v253}"(g8_), "{v254}"(loVL_), "{v255}"(loOL_), "{v[240:243]}"(ones4), "{s47}"((ldsb + X_OFF) ^ (ldsb + X1_OFF)),
              "{s[58:59]}"(0xFFFFull), "{s[60:61]}"(0xFFFF0000ull), "{s[62:63]}"(0xFFFF00000000ull), "{s[64:65]}"(0xFFFF000000000000ull),
              "{s[66:67]}"(0x1FFFFFFFFFFFFull), "{s75}"(rsb), "{s45}"(ldsb + V_OFF + w * 4608), "{s[68:69]}"(0xFFFFFFFFFFFFull),
              "{s[84:87]}"(wr), "{s46}"(wave * 36864), "{s88}"(touch), "{s71}"(ldsb + O_OFF), "{s[54:55]}"(mrow), "{s[56:57]}"(0xF0000F0000F0000Full),
              "{s[78:79]}"(mrow & 0xF0000F0000F0000Full)
            : "v144", "v145", "v146", "v147", "v148", "v149", "v150", "v151", "v152", "v153", "v154", "v155", "v156", "v157", "v158", "v159", "v160", "v161", "v162", "v163", "v164", "v165", "v166", "v167", "v168", "v169", "v170", "v171", "v172", "v173", "v174", "v175", "v176", "v177", "v178", "v179", "v180", "v181", "v182", "v183", "v184", "v185", "v186", "v187", "v188", "v189", "v190", "v191", "v192", "v193", "v194", "v195", "v196", "v197", "v198", "v199", "v200", "v201", "v202", "v203", "v204", "v205", "v206", "v207", "v208", "v209", "v210", "v211", "v212", "v213", "v214", "v215", "v216", "v217", "v218", "v219", "v220", "v221", "v222", "v223", "v224", "v225", "v226", "v227", "v228", "v229", "v230", "v231", "v232", "v233", "v234", "v235", "v236", "v237", "v238", "v239", "v0", "v1", "v2", "v3", "v4", "v5", "v6", "v7", "v8", "v9", "v10", "v11", "v12", "v13", "v14", "v15", "v16", "v17", "v18", "v19", "v20", "v21", "v22", "v23", "v24", "v25", "v26", "v27", "v28", "v29", "v30", "v31", "v32", "v33", "v34", "v35", "v36", "v37", "v38", "v39", "v40", "v41", "v42", "v43", "v44", "v45", "v46", "v47", "v48", "v49", "v50", "v51", "v52", "v53", "v54", "v55", "v56", "v57", "v58", "v59", "v60", "v61", "v62", "v63", "v64", "v65", "v66", "v67", "v68", "v69", "v70", "v71", "v72", "v73", "v74", "v75", "v76", "v77", "v78", "v79", "v80", "v81", "v82", "v83", "v84", "v85", "v86", "v87", "v88", "v89", "v90", "v91", "v92", "v93", "v94", "v95", "v96", "v97", "v98", "v99", "v100", "v101", "v102", "v103", "v104", "v105", "v106", "v107", "v108", "v109", "v110", "v111", "v112", "v113", "v114", "v115", "v116", "v117", "v118", "v119", "v120", "v121", "v122", "v123", "v124", "v125", "v126", "v127", "v128", "v129", "v130", "v131", "v132", "v133", "v134", "v135", "v136", "v137", "v138", "v139", "v140", "v141", "v142", "v143",  "m0", "s49", "vcc", "scc", "memory");

        }
        if (wave == 0 && blockIdx.x == 0) {
            int ln_;
            asm volatile("v_mbcnt_lo_u32_b32 %0, -1, 0\n\tv_mbcnt_hi_u32_b32 %0, -1, %0" : "=v"(ln_));
            KARGS
            unsigned long long tg;
            asm volatile("s_load_dwordx2 %0, %1, %2\n\ts_waitcnt lgkmcnt(0)" : "=s"(tg) : "s"(wb), "n"(EPO_OFF) : "memory");
            const unsigned lo = (unsigned)tg + 1u, hi = (unsigned)(tg >> 32);
            const auto wq = __builtin_amdgcn_make_buffer_rsrc((void*)wb, 0, WS_BYTES, 0x00020000);
            for (int it_ = 0; it_ < 100000; ++it_) {
                bool bad = false;
#pragma unroll
                for (int k = 0; k < 8; ++k) {
                    const u32x4 a_ = __builtin_amdgcn_raw_buffer_load_b128(wq, ARR_OFF + ln_ * 16, k * 1024, SC1);
                    bad |= a_[0] != lo || a_[1] != hi || a_[2] != lo || a_[3] != hi;
                }
                if (__builtin_amdgcn_ballot_w64(bad) == 0ull) break;
                __builtin_amdgcn_s_sleep(2);
            }
            if (ln_ == 0) __builtin_amdgcn_raw_buffer_store_b64((u32x2){lo, hi}, wq, EPO_OFF, 0, SC1);
        }
        asm volatile("wk_code_end:");
        __builtin_amdgcn_endpgm();
    } else if (wave < 6) {
        LANE_SETUP
        {
            const unsigned rsb = ldsb + RS_BASE;
            const unsigned pE_ = fE - X_OFF;
            const unsigned aVAw_ = ldsb + V_OFF + w * 9216 + lane * 16, aVBw_ = ldsb + V_OFF + w * 9216 + 1024 + lane * 8;
            const unsigned pST_ = lane * 16, aRSw_ = (lane < 48 ? rsb : ldsb + RS_OFF + 2304 - 192) + lane * 4, aRSr_ = rsb + (lane & 48);
            B_SCALARS
            unsigned s44 = ldsb + O_OFF, q0 = ldsb + O_OFF + pE_, q1 = rsb + l15 * 4, q2 = ldsb + OL_OFF + l15 * 384 + (((4 * w + (g >> 1)) ^ sw) << 4) + 8 * (g & 1);
            asm volatile(
            "s_waitcnt lgkmcnt(0)\n\t"
            "s_mov_b32 s83, 0x10000\n\t"
            "s_nop 7\n\t"
            "s_setprio 3\n\t"
            "s_barrier\n\t"
            "buffer_load_dwordx4 v[140:143], v245, s[28:31], s82 offen sc1\n\t"
            "s_add_u32 s49, s33, 0\n\t"
            "buffer_load_dwordx4 v[0:3], v245, s[28:31], s49 offen sc1\n\t"
            "s_add_u32 s49, s33, 6144\n\t"
            "buffer_load_dwordx4 v[24:27], v245, s[28:31], s49 offen sc1\n\t"
            "s_add_u32 s49, s33, 12288\n\t"
            "buffer_load_dwordx4 v[48:51], v245, s[28:31], s49 offen sc1\n\t"
            "s_add_u32 s49, s33, 18432\n\t"
            "buffer_load_dwordx4 v[72:75], v245, s[28:31], s49 offen sc1\n\t"
            "s_add_u32 s49, s33, 24576\n\t"
            "buffer_load_dwordx4 v[96:99], v245, s[28:31], s49 offen sc1\n\t"
            "s_add_u32 s49, s33, 30720\n\t"
            "buffer_load_dwordx4 v[120:123], v245, s[28:31], s49 offen sc1\n\t"
            "s_add_u32 s49, s33, 1024\n\t"
            "buffer_load_dwordx4 v[4:7], v245, s[28:31], s49 offen sc1\n\t"
            "s_add_u32 s49, s33, 7168\n\t"
            "buffer_load_dwordx4 v[28:31], v245, s[28:31], s49 offen sc1\n\t"
            "s_add_u32 s49, s33, 13312\n\t"
            "buffer_load_dwordx4 v[52:55], v245, s[28:31], s49 offen sc1\n\t"
            "s_add_u32 s49, s33, 19456\n\t"
            "buffer_load_dwordx4 v[76:79], v245, s[28:31], s49 offen sc1\n\t"
            "s_add_u32 s49, s33, 25600\n\t"
            "buffer_load_dwordx4 v[100:103], v245, s[28:31], s49 offen sc1\n\t"
            "s_add_u32 s49, s33, 31744\n\t"
            "buffer_load_dwordx4 v[124:127], v245, s[28:31], s49 offen sc1\n\t"
            "s_add_u32 s49, s33, 2048\n\t"
            "buffer_load_dwordx4 v[8:11], v245, s[28:31], s49 offen sc1\n\t"
            "s_add_u32 s49, s33, 8192\n\t"
            "buffer_load_dwordx4 v[32:35], v245, s[28:31], s49 offen sc1\n\t"
            "s_add_u32 s49, s33, 14336\n\t"
            "buffer_load_dwordx4 v[56:59], v245, s[28:31], s49 offen sc1\n\t"
            "s_add_u32 s49, s33, 20480\n\t"
            "buffer_load_dwordx4 v[80:83], v245, s[28:31], s49 offen sc1\n\t"
            "s_add_u32 s49, s33, 26624\n\t"
            "buffer_load_dwordx4 v[104:107], v245, s[28:31], s49 offen sc1\n\t"
            "s_add_u32 s49, s33, 32768\n\t"
            "buffer_load_dwordx4 v[128:131], v245, s[28:31], s49 offen sc1\n\t"
            "s_add_u32 s49, s33, 3072\n\t"
            "buffer_load_dwordx4 v[12:15], v245, s[28:31], s49 offen sc1\n\t"
            "s_add_u32 s49, s33, 9216\n\t"
            "buffer_load_dwordx4 v[36:39], v245, s[28:31], s49 offen sc1\n\t"
            "s_add_u32 s49, s33, 15360\n\t"
            "buffer_load_dwordx4 v[60:63], v245, s[28:31], s49 offen sc1\n\t"
            "s_add_u32 s49, s33, 21504\n\t"
            "buffer_load_dwordx4 v[84:87], v245, s[28:31], s49 offen sc1\n\t"
            "s_add_u32 s49, s33, 27648\n\t"
            "buffer_load_dwordx4 v[108:111], v245, s[28:31], s49 offen sc1\n\t"
            "s_add_u32 s49, s33, 33792\n\t"
            "buffer_load_dwordx4 v[132:135], v245, s[28:31], s49 offen sc1\n\t"
            "s_add_u32 s49, s33, 4096\n\t"
            "buffer_load_dwordx4 v[16:19], v245, s[28:31], s49 offen sc1\n\t"
            "s_add_u32 s49, s33, 10240\n\t"
            "buffer_load_dwordx4 v[40:43], v245, s[28:31], s49 offen sc1\n\t"
            "s_add_u32 s49, s33, 16384\n\t"
            "buffer_load_dwordx4 v[64:67], v245, s[28:31], s49 offen sc1\n\t"
            "s_add_u32 s49, s33, 22528\n\t"
            "buffer_load_dwordx4 v[88:91], v245, s[28:31], s49 offen sc1\n\t"
            "s_add_u32 s49, s33, 28672\n\t"
            "buffer_load_dwordx4 v[112:115], v245, s[28:31], s49 offen sc1\n\t"
            "s_add_u32 s49, s33, 34816\n\t"
            "buffer_load_dwordx4 v[136:139], v245, s[28:31], s49 offen sc1\n\t"
            "s_add_u32 s49, s33, 5120\n\t"
            "buffer_load_dwordx4 v[20:23], v245, s[28:31], s49 offen sc1\n\t"
            "s_add_u32 s49, s33, 11264\n\t"
            "buffer_load_dwordx4 v[44:47], v245, s[28:31], s49 offen sc1\n\t"
            "s_add_u32 s49, s33, 17408\n\t"
            "buffer_load_dwordx4 v[68:71], v245, s[28:31], s49 offen sc1\n\t"
            "s_add_u32 s49, s33, 23552\n\t"
            "buffer_load_dwordx4 v[92:95], v245, s[28:31], s49 offen sc1\n\t"
            "s_add_u32 s49, s33, 29696\n\t"
            "buffer_load_dwordx4 v[116:119], v245, s[28:31], s49 offen sc1\n\t"
            "s_add_u32 s49, s33, 35840\n\t"
            "buffer_load_dwordx4 v[140:143], v245, s[28:31], s49 offen sc1\n\t"
            "ds_read_b32 v249, v252 offset:12\n\t"
            "s_waitcnt lgkmcnt(0)\n\t"
            "v_and_b32 v249, s69, v249\n\t"
            "s_mov_b32 exec_hi, 0x3fffff\n\t"
            "s_add_u32 m0, s66, 0\n\t"
            "s_add_u32 s49, s79, 0\n\t"
            "buffer_load_dwordx4 v249, s[72:75], s49 offen lds\n\t"
            "s_mov_b32 exec_hi, -1\n\t"
            "s_mov_b32 exec_hi, 0x3fffff\n\t"
            "s_add_u32 m0, s66, 872\n\t"
            "s_add_u32 s49, s47, 196224\n\t"
            "buffer_load_dwordx4 v249, s[72:75], s49 offen lds\n\t"
            "s_mov_b32 exec_hi, -1\n\t"
            "s_mov_b32 exec_hi, 0x3fffff\n\t"
            "s_add_u32 m0, s66, 1744\n\t"
            "s_add_u32 s49, s47, 392448\n\t"
            "buffer_load_dwordx4 v249, s[72:75], s49 offen lds\n\t"
            "s_mov_b32 exec_hi, -1\n\t"
            "s_mov_b32 exec_hi, 0x3fffff\n\t"
            "s_add_u32 m0, s66, 2616\n\t"
            "s_add_u32 s49, s47, 588672\n\t"
            "buffer_load_dwordx4 v249, s[72:75], s49 offen lds\n\t"
            "s_mov_b32 exec_hi, -1\n\t"
            "s_mov_b32 exec_hi, 0x3fffff\n\t"
            "s_add_u32 m0, s66, 3488\n\t"
            "s_add_u32 s49, s47, 784896\n\t"
            "buffer_load_dwordx4 v249, s[72:75], s49 offen lds\n\t"
            "s_mov_b32 exec_hi, -1\n\t"
            "s_mov_b32 exec_hi, 0x3fffff\n\t"
            "s_add_u32 m0, s66, 4360\n\t"
            "s_add_u32 s49, s47, 981120\n\t"
            "buffer_load_dwordx4 v249, s[72:75], s49 offen lds\n\t"
            "s_mov_b32 exec_hi, -1\n\t"
            "s_mov_b32 exec_hi, 0x3fffff\n\t"
            "s_add_u32 m0, s66, 5232\n\t"
            "s_add_u32 s49, s47, 1177344\n\t"
            "buffer_load_dwordx4 v249, s[72:75], s49 offen lds\n\t"
            "s_mov_b32 exec_hi, -1\n\t"
            "s_mov_b32 exec_hi, 0x3fffff\n\t"
            "s_add_u32 m0, s66, 6104\n\t"
            "s_add_u32 s49, s47, 1373568\n\t"
            "buffer_load_dwordx4 v249, s[72:75], s49 offen lds\n\t"
            "s_mov_b32 exec_hi, -1\n\t"
            "s_mov_b32 exec_hi, 0x3fffff\n\t"
            "s_add_u32 m0, s66, 6976\n\t"
            "s_add_u32 s49, s47, 1569792\n\t"
            "buffer_load_dwordx4 v249, s[72:75], s49 offen lds\n\t"
            "s_mov_b32 exec_hi, -1\n\t"
            "s_setprio 3\n\t"
            "ds_read_b128 v[200:203], v232\n\t"
            "ds_read_b128 v[192:195], v232 offset:6272\n\t"
            "ds_read_b128 v[208:211], v232 offset:128\n\t"
            "ds_read_b128 v[216:219], v232 offset:256\n\t"
            "v_add_u32 v228, s44, v245\n\t"
            "v_xor_b32 v235, 64, v232\n\t"
            "s_setprio 3\n\t"
            "s_waitcnt vmcnt(9)\n\t"
            "s_nop 0\n\t"
            "ds_read_b128 v[224:227], v228 offset:12288\n\t"
            "ds_read_b128 v[228:231], v228 offset:13312\n\t"
            "ds_read_b128 v[204:207], v235\n\t"
            "ds_read_b128 v[196:199], v235 offset:6272\n\t"
            "ds_read_b128 v[212:215], v235 offset:128\n\t"
            "ds_read_b128 v[220:223], v235 offset:256\n\t"
            "v_xor_b32 v248, 32, v234\n\t"
            "s_waitcnt lgkmcnt(5)\n\t"
            "v_add_f32 v224, v224, v226\n\t"
            "s_waitcnt lgkmcnt(4)\n\t"
            "v_add_f32 v228, v228, v230\n\t"
            "s_waitcnt lgkmcnt(3)\n\t"
            "v_mfma_f32_16x16x32_bf16 v[144:147], v[0:3], v[200:203], 0\n\t"
            "v_add_f32 v224, v224, v228\n\t"
            "v_add_f32 v225, v225, v227\n\t"
            "v_mfma_f32_16x16x32_bf16 v[144:147], v[4:7], v[204:207], v[144:147]\n\t"
            "v_add_f32 v229, v229, v231\n\t"
            "v_mul_f32 v224, 0x3baaaaab, v224\n\t"
            "v_mfma_f32_16x16x32_bf16 v[148:151], v[24:27], v[200:203], 0\n\t"
            "v_add_f32 v225, v225, v229\n\t"
            "v_mul_f32_e64 v224, v224, -v224\n\t"
            "v_mfma_f32_16x16x32_bf16 v[148:151], v[28:31], v[204:207], v[148:151]\n\t"
            "ds_read_b128 v[228:231], v235 offset:6144\n\t"
            "v_fmac_f32 v224, 0x3baaaaab, v225\n\t"
            "v_mfma_f32_16x16x32_bf16 v[168:171], v[48:51], v[200:203], 0\n\t"
            "v_max_f32 v224, 0, v224\n\t"
            "v_add_f32 v224, 0x3727c5ac, v224\n\t"
            "v_mfma_f32_16x16x32_bf16 v[168:171], v[52:55], v[204:207], v[168:171]\n\t"
            "v_rsq_f32 v224, v224\n\t"
            "v_mfma_f32_16x16x32_bf16 v[172:175], v[72:75], v[200:203], 0\n\t"
            "v_mfma_f32_16x16x32_bf16 v[172:175], v[76:79], v[204:207], v[172:175]\n\t"
            "ds_write_b32 v246, v224\n\t"
            "v_mfma_f32_16x16x32_bf16 v[152:155], v[96:99], v[200:203], 0\n\t"
            "ds_read_b128 v[224:227], v232 offset:6144\n\t"
            "ds_read_b32 v236, v233\n\t"
            "v_mfma_f32_16x16x32_bf16 v[152:155], v[100:103], v[204:207], v[152:155]\n\t"
            "ds_read_b32 v237, v233 offset:64\n\t"
            "v_mfma_f32_16x16x32_bf16 v[156:159], v[120:123], v[200:203], 0\n\t"
            "v_mfma_f32_16x16x32_bf16 v[156:159], v[124:127], v[204:207], v[156:159]\n\t"
            "v_add_u32 v204, s48, v245\n\t"
            "s_waitcnt lgkmcnt(6)\n\t"
            "v_mfma_f32_16x16x32_bf16 v[144:147], v[8:11], v[208:211], v[144:147]\n\t"
            "ds_read_b128 v[200:203], v204 offset:18816\n\t"
            "ds_read_b128 v[204:207], v204 offset:19840\n\t"
            "v_mfma_f32_16x16x32_bf16 v[144:147], v[12:15], v[212:215], v[144:147]\n\t"
            "v_mfma_f32_16x16x32_bf16 v[148:151], v[32:35], v[208:211], v[148:151]\n\t"
            "v_mfma_f32_16x16x32_bf16 v[148:151], v[36:39], v[212:215], v[148:151]\n\t"
            "v_mfma_f32_16x16x32_bf16 v[168:171], v[56:59], v[208:211], v[168:171]\n\t"
            "v_mfma_f32_16x16x32_bf16 v[168:171], v[60:63], v[212:215], v[168:171]\n\t"
            "s_waitcnt lgkmcnt(4)\n\t"
            "v_mfma_f32_16x16x32_bf16 v[160:163], v[0:3], v[224:227], 0\n\t"
            "v_mfma_f32_16x16x32_bf16 v[160:163], v[4:7], v[228:231], v[160:163]\n\t"
            "v_mfma_f32_16x16x32_bf16 v[164:167], v[24:27], v[224:227], 0\n\t"
            "s_waitcnt lgkmcnt(1)\n\t"
            "v_add_f32 v200, v200, v202\n\t"
            "s_waitcnt lgkmcnt(0)\n\t"
            "v_add_f32 v204, v204, v206\n\t"
            "v_mfma_f32_16x16x32_bf16 v[164:167], v[28:31], v[228:231], v[164:167]\n\t"
            "v_add_f32 v200, v200, v204\n\t"
            "v_add_f32 v201, v201, v203\n\t"
            "v_mfma_f32_16x16x32_bf16 v[176:179], v[48:51], v[224:227], 0\n\t"
            "v_add_f32 v205, v205, v207\n\t"
            "v_mul_f32 v200, 0x3baaaaab, v200\n\t"
            "v_mfma_f32_16x16x32_bf16 v[176:179], v[52:55], v[228:231], v[176:179]\n\t"
            "v_add_f32 v201, v201, v205\n\t"
            "v_mul_f32_e64 v200, v200, -v200\n\t"
            "v_mfma_f32_16x16x32_bf16 v[180:183], v[72:75], v[224:227], 0\n\t"
            "v_fmac_f32 v200, 0x3baaaaab, v201\n\t"
            "v_max_f32 v200, 0, v200\n\t"
            "v_mfma_f32_16x16x32_bf16 v[180:183], v[76:79], v[228:231], v[180:183]\n\t"
            "v_add_f32 v200, 0x3727c5ac, v200\n\t"
            "v_rsq_f32 v200, v200\n\t"
            "v_mfma_f32_16x16x32_bf16 v[184:187], v[96:99], v[224:227], 0\n\t"
            "v_mfma_f32_16x16x32_bf16 v[184:187], v[100:103], v[228:231], v[184:187]\n\t"
            "ds_write_b32 v246, v200\n\t"
            "v_mfma_f32_16x16x32_bf16 v[188:191], v[120:123], v[224:227], 0\n\t"
            "ds_read_b128 v[224:227], v232 offset:6400\n\t"
            "v_add_u32 v232, s48, v242\n\t"
            "v_mfma_f32_16x16x32_bf16 v[188:191], v[124:127], v[228:231], v[188:191]\n\t"
            "ds_read_b128 v[228:231], v235 offset:6400\n\t"
            "v_xor_b32 v233, 64, v232\n\t"
            "v_mfma_f32_16x16x32_bf16 v[160:163], v[8:11], v[192:195], v[160:163]\n\t"
            "v_mfma_f32_16x16x32_bf16 v[160:163], v[12:15], v[196:199], v[160:163]\n\t"
            "v_mfma_f32_16x16x32_bf16 v[164:167], v[32:35], v[192:195], v[164:167]\n\t"
            "v_mfma_f32_16x16x32_bf16 v[164:167], v[36:39], v[196:199], v[164:167]\n\t"
            "v_mfma_f32_16x16x32_bf16 v[176:179], v[56:59], v[192:195], v[176:179]\n\t"
            "v_mfma_f32_16x16x32_bf16 v[176:179], v[60:63], v[196:199], v[176:179]\n\t"
            "v_mfma_f32_16x16x32_bf16 v[180:183], v[80:83], v[192:195], v[180:183]\n\t"
            "v_mfma_f32_16x16x32_bf16 v[180:183], v[84:87], v[196:199], v[180:183]\n\t"
            "v_mfma_f32_16x16x32_bf16 v[184:187], v[104:107], v[192:195], v[184:187]\n\t"
            "v_mfma_f32_16x16x32_bf16 v[184:187], v[108:111], v[196:199], v[184:187]\n\t"
            "v_mfma_f32_16x16x32_bf16 v[188:191], v[128:131], v[192:195], v[188:191]\n\t"
            "ds_read_b128 v[192:195], v232 offset:128\n\t"
            "v_mfma_f32_16x16x32_bf16 v[188:191], v[132:135], v[196:199], v[188:191]\n\t"
            "ds_read_b128 v[196:199], v233 offset:128\n\t"
            "s_waitcnt lgkmcnt(2)\n\t"
            "v_mfma_f32_16x16x32_bf16 v[160:163], v[16:19], v[224:227], v[160:163]\n\t"
            "v_mfma_f32_16x16x32_bf16 v[160:163], v[20:23], v[228:231], v[160:163]\n\t"
            "v_mfma_f32_16x16x32_bf16 v[164:167], v[40:43], v[224:227], v[164:167]\n\t"
            "v_mfma_f32_16x16x32_bf16 v[164:167], v[44:47], v[228:231], v[164:167]\n\t"
            "v_mfma_f32_16x16x32_bf16 v[176:179], v[64:67], v[224:227], v[176:179]\n\t"
            "v_mfma_f32_16x16x32_bf16 v[176:179], v[68:71], v[228:231], v[176:179]\n\t"
            "v_mfma_f32_16x16x32_bf16 v[180:183], v[88:91], v[224:227], v[180:183]\n\t"
            "v_mfma_f32_16x16x32_bf16 v[180:183], v[92:95], v[228:231], v[180:183]\n\t"
            "s_nop 1\n\t"
            "v_mul_f32 v160, v237, v160\n\t"
            "v_mfma_f32_16x16x32_bf16 v[184:187], v[112:115], v[224:227], v[184:187]\n\t"
            "v_mul_f32 v161, v237, v161\n\t"
            "v_mul_f32 v162, v237, v162\n\t"
            "v_mfma_f32_16x16x32_bf16 v[184:187], v[116:119], v[228:231], v[184:187]\n\t"
            "v_mul_f32 v164, v237, v164\n\t"
            "v_mul_f32 v165, v237, v165\n\t"
            "v_mfma_f32_16x16x32_bf16 v[188:191], v[136:139], v[224:227], v[188:191]\n\t"
            "ds_read_b128 v[224:227], v232\n\t"
            "v_mul_f32 v176, v237, v176\n\t"
            "v_mfma_f32_16x16x32_bf16 v[188:191], v[140:143], v[228:231], v[188:191]\n\t"
            "ds_read_b128 v[228:231], v233\n\t"
            "v_mul_f32 v177, v237, v177\n\t"
            "v_mfma_f32_16x16x32_bf16 v[172:175], v[80:83], v[208:211], v[172:175]\n\t"
            "v_mul_f32 v180, v237, v180\n\t"
            "v_mul_f32 v181, v237, v181\n\t"
            "v_mfma_f32_16x16x32_bf16 v[172:175], v[84:87], v[212:215], v[172:175]\n\t"
            "v_mul_f32 v184, v237, v184\n\t"
            "v_mul_f32 v185, v237, v185\n\t"
            "v_mfma_f32_16x16x32_bf16 v[152:155], v[104:107], v[208:211], v[152:155]\n\t"
            "v_mul_f32 v188, v237, v188\n\t"
            "v_mul_f32 v189, v237, v189\n\t"
            "v_mfma_f32_16x16x32_bf16 v[152:155], v[108:111], v[212:215], v[152:155]\n\t"
            "v_mul_f32 v163, v237, v163\n\t"
            "v_cvt_pk_bf16_f32 v160, v160, v161\n\t"
            "v_mfma_f32_16x16x32_bf16 v[156:159], v[128:131], v[208:211], v[156:159]\n\t"
            "v_mul_f32 v166, v237, v166\n\t"
            "v_mul_f32 v167, v237, v167\n\t"
            "v_mfma_f32_16x16x32_bf16 v[156:159], v[132:135], v[212:215], v[156:159]\n\t"
            "v_cvt_pk_bf16_f32 v164, v164, v165\n\t"
            "v_mul_f32 v178, v237, v178\n\t"
            "v_mfma_f32_16x16x32_bf16 v[144:147], v[16:19], v[216:219], v[144:147]\n\t"
            "v_mul_f32 v179, v237, v179\n\t"
            "v_cvt_pk_bf16_f32 v176, v176, v177\n\t"
            "v_mfma_f32_16x16x32_bf16 v[144:147], v[20:23], v[220:223], v[144:147]\n\t"
            "v_mul_f32 v182, v237, v182\n\t"
            "v_mul_f32 v183, v237, v183\n\t"
            "v_mfma_f32_16x16x32_bf16 v[148:151], v[40:43], v[216:219], v[148:151]\n\t"
            "v_cvt_pk_bf16_f32 v180, v180, v181\n\t"
            "v_mul_f32 v186, v237, v186\n\t"
            "v_mfma_f32_16x16x32_bf16 v[148:151], v[44:47], v[220:223], v[148:151]\n\t"
            "v_mul_f32 v187, v237, v187\n\t"
            "v_cvt_pk_bf16_f32 v184, v184, v185\n\t"
            "v_mfma_f32_16x16x32_bf16 v[168:171], v[64:67], v[216:219], v[168:171]\n\t"
            "v_mul_f32 v144, v236, v144\n\t"
            "v_mul_f32 v145, v236, v145\n\t"
            "v_mfma_f32_16x16x32_bf16 v[168:171], v[68:71], v[220:223], v[168:171]\n\t"
            "v_mul_f32 v146, v236, v146\n\t"
            "v_mul_f32 v147, v236, v147\n\t"
            "v_mfma_f32_16x16x32_bf16 v[172:175], v[88:91], v[216:219], v[172:175]\n\t"
            "v_mul_f32 v148, v236, v148\n\t"
            "v_mul_f32 v149, v236, v149\n\t"
            "v_mfma_f32_16x16x32_bf16 v[172:175], v[92:95], v[220:223], v[172:175]\n\t"
            "v_cvt_pk_bf16_f32 v144, v144, v145\n\t"
            "v_mul_f32 v150, v236, v150\n\t"
            "v_mfma_f32_16x16x32_bf16 v[152:155], v[112:115], v[216:219], v[152:155]\n\t"
            "v_mul_f32 v168, v236, v168\n\t"
            "v_mul_f32 v169, v236, v169\n\t"
            "v_mfma_f32_16x16x32_bf16 v[152:155], v[116:119], v[220:223], v[152:155]\n\t"
            "v_mul_f32 v151, v236, v151\n\t"
            "v_cvt_pk_bf16_f32 v148, v148, v149\n\t"
            "v_mfma_f32_16x16x32_bf16 v[156:159], v[136:139], v[216:219], v[156:159]\n\t"
            "v_mul_f32 v172, v236, v172\n\t"
            "v_mul_f32 v173, v236, v173\n\t"
            "v_mfma_f32_16x16x32_bf16 v[156:159], v[140:143], v[220:223], v[156:159]\n\t"
            "v_mul_f32 v170, v236, v170\n\t"
            "v_mul_f32 v171, v236, v171\n\t"
            "s_waitcnt lgkmcnt(0)\n\t"
            "v_mfma_f32_16x16x32_bf16 v[200:203], v[224:227], v[0:3], 0\n\t"
            "v_mul_f32 v152, v236, v152\n\t"
            "v_mul_f32 v153, v236, v153\n\t"
            "v_mfma_f32_16x16x32_bf16 v[200:203], v[228:231], v[4:7], v[200:203]\n\t"
            "v_cvt_pk_bf16_f32 v168, v168, v169\n\t"
            "v_mul_f32 v174, v236, v174\n\t"
            "v_mfma_f32_16x16x32_bf16 v[204:207], v[228:231], v[28:31], 0\n\t"
            "v_mul_f32 v156, v236, v156\n\t"
            "v_mul_f32 v157, v236, v157\n\t"
            "v_mfma_f32_16x16x32_bf16 v[204:207], v[224:227], v[24:27], v[204:207]\n\t"
            "v_mul_f32 v175, v236, v175\n\t"
            "v_cvt_pk_bf16_f32 v172, v172, v173\n\t"
            "v_mfma_f32_16x16x32_bf16 v[208:211], v[224:227], v[48:51], 0\n\t"
            "v_mul_f32 v154, v236, v154\n\t"
            "v_mul_f32 v155, v236, v155\n\t"
            "v_mfma_f32_16x16x32_bf16 v[208:211], v[228:231], v[52:55], v[208:211]\n\t"
            "v_cvt_pk_bf16_f32 v152, v152, v153\n\t"
            "v_mul_f32 v158, v236, v158\n\t"
            "v_mfma_f32_16x16x32_bf16 v[212:215], v[228:231], v[76:79], 0\n\t"
            "v_mul_f32 v159, v236, v159\n\t"
            "v_cvt_pk_bf16_f32 v156, v156, v157\n\t"
            "v_mfma_f32_16x16x32_bf16 v[212:215], v[224:227], v[72:75], v[212:215]\n\t"
            "v_cvt_pk_bf16_f32 v145, v146, v147\n\t"
            "v_cvt_pk_bf16_f32 v149, v150, v151\n\t"
            "v_mfma_f32_16x16x32_bf16 v[216:219], v[224:227], v[96:99], 0\n\t"
            "v_cvt_pk_bf16_f32 v169, v170, v171\n\t"
            "v_cvt_pk_bf16_f32 v173, v174, v175\n\t"
            "v_mfma_f32_16x16x32_bf16 v[216:219], v[228:231], v[100:103], v[216:219]\n\t"
            "v_cvt_pk_bf16_f32 v153, v154, v155\n\t"
            "v_cvt_pk_bf16_f32 v157, v158, v159\n\t"
            "v_mfma_f32_16x16x32_bf16 v[220:223], v[228:231], v[124:127], 0\n\t"
            "ds_read_b128 v[228:231], v233 offset:256\n\t"
            "v_mul_f32 v190, v237, v190\n\t"
            "v_mfma_f32_16x16x32_bf16 v[220:223], v[224:227], v[120:123], v[220:223]\n\t"
            "ds_read_b128 v[224:227], v232 offset:256\n\t"
            "s_mov_b64 exec, -1\n\t"
            "ds_write_b64 v234, v[144:145]\n\t"
            "ds_write_b64 v234, v[148:149] offset:128\n\t"
            "ds_write_b64 v234, v[168:169] offset:256\n\t"
            "ds_write_b64 v248, v[172:173]\n\t"
            "ds_write_b64 v248, v[152:153] offset:128\n\t"
            "ds_write_b64 v248, v[156:157] offset:256\n\t"
            "s_mov_b64 exec, -1\n\t"
            "v_mfma_f32_16x16x32_bf16 v[200:203], v[192:195], v[8:11], v[200:203]\n\t"
            "v_mul_f32 v191, v237, v191\n\t"
            "v_cvt_pk_bf16_f32 v188, v188, v189\n\t"
            "v_mfma_f32_16x16x32_bf16 v[200:203], v[196:199], v[12:15], v[200:203]\n\t"
            "ds_read_b64 v[236:237], v247\n\t"
            "v_cvt_pk_bf16_f32 v161, v162, v163\n\t"
            "v_mfma_f32_16x16x32_bf16 v[204:207], v[196:199], v[36:39], v[204:207]\n\t"
            "v_cvt_pk_bf16_f32 v165, v166, v167\n\t"
            "v_cvt_pk_bf16_f32 v177, v178, v179\n\t"
            "v_mfma_f32_16x16x32_bf16 v[204:207], v[192:195], v[32:35], v[204:207]\n\t"
            "v_cvt_pk_bf16_f32 v181, v182, v183\n\t"
            "v_cvt_pk_bf16_f32 v185, v186, v187\n\t"
            "v_mfma_f32_16x16x32_bf16 v[208:211], v[192:195], v[56:59], v[208:211]\n\t"
            "v_cvt_pk_bf16_f32 v189, v190, v191\n\t"
            "v_mfma_f32_16x16x32_bf16 v[208:211], v[196:199], v[60:63], v[208:211]\n\t"
            "s_mov_b64 exec, s[80:81]\n\t"
            "ds_write_b64 v234, v[160:161] offset:6144\n\t"
            "ds_write_b64 v234, v[164:165] offset:6272\n\t"
            "ds_write_b64 v234, v[176:177] offset:6400\n\t"
            "ds_write_b64 v248, v[180:181] offset:6144\n\t"
            "ds_write_b64 v248, v[184:185] offset:6272\n\t"
            "ds_write_b64 v248, v[188:189] offset:6400\n\t"
            "s_mov_b64 exec, -1\n\t"
            "v_mfma_f32_16x16x32_bf16 v[212:215], v[196:199], v[84:87], v[212:215]\n\t"
            "s_waitcnt lgkmcnt(14)\n\t"
            "ds_read_b64 v[248:249], v247 offset:8\n\t"
            "v_mfma_f32_16x16x32_bf16 v[212:215], v[192:195], v[80:83], v[212:215]\n\t"
            "v_mfma_f32_16x16x32_bf16 v[216:219], v[192:195], v[104:107], v[216:219]\n\t"
            "v_mfma_f32_16x16x32_bf16 v[216:219], v[196:199], v[108:111], v[216:219]\n\t"
            "v_mfma_f32_16x16x32_bf16 v[220:223], v[196:199], v[132:135], v[220:223]\n\t"
            "s_waitcnt lgkmcnt(14)\n\t"
            "ds_read_b128 v[196:199], v233 offset:6144\n\t"
            "v_mfma_f32_16x16x32_bf16 v[220:223], v[192:195], v[128:131], v[220:223]\n\t"
            "s_waitcnt lgkmcnt(14)\n\t"
            "ds_read_b128 v[192:195], v232 offset:6144\n\t"
            "v_mfma_f32_16x16x32_bf16 v[200:203], v[224:227], v[16:19], v[200:203]\n\t"
            "v_mfma_f32_16x16x32_bf16 v[200:203], v[228:231], v[20:23], v[200:203]\n\t"
            "v_mfma_f32_16x16x32_bf16 v[204:207], v[228:231], v[44:47], v[204:207]\n\t"
            "v_mfma_f32_16x16x32_bf16 v[204:207], v[224:227], v[40:43], v[204:207]\n\t"
            "v_mfma_f32_16x16x32_bf16 v[208:211], v[224:227], v[64:67], v[208:211]\n\t"
            "v_mfma_f32_16x16x32_bf16 v[208:211], v[228:231], v[68:71], v[208:211]\n\t"
            "v_mfma_f32_16x16x32_bf16 v[212:215], v[228:231], v[92:95], v[212:215]\n\t"
            "v_mfma_f32_16x16x32_bf16 v[212:215], v[224:227], v[88:91], v[212:215]\n\t"
            "s_waitcnt lgkmcnt(9)\n\t"
            "s_nop 0\n\t"
            "v_mul_f32 v200, v236, v200\n\t"
            "v_mfma_f32_16x16x32_bf16 v[216:219], v[224:227], v[112:115], v[216:219]\n\t"
            "v_mul_f32 v201, v237, v201\n\t"
            "s_waitcnt lgkmcnt(2)\n\t"
            "v_mul_f32 v202, v248, v202\n\t"
            "v_mfma_f32_16x16x32_bf16 v[216:219], v[228:231], v[116:119], v[216:219]\n\t"
            "v_mul_f32 v204, v236, v204\n\t"
            "v_mul_f32 v205, v237, v205\n\t"
            "v_mfma_f32_16x16x32_bf16 v[220:223], v[228:231], v[140:143], v[220:223]\n\t"
            "ds_read_b128 v[228:231], v233 offset:6272\n\t"
            "v_mul_f32 v208, v236, v208\n\t"
            "v_mfma_f32_16x16x32_bf16 v[220:223], v[224:227], v[136:139], v[220:223]\n\t"
            "ds_read_b128 v[224:227], v232 offset:6272\n\t"
            "v_mul_f32 v209, v237, v209\n\t"
            "s_waitcnt lgkmcnt(2)\n\t"
            "v_mfma_f32_16x16x32_bf16 v[144:147], v[192:195], v[0:3], 0\n\t"
            "v_mul_f32 v212, v236, v212\n\t"
            "v_mul_f32 v213, v237, v213\n\t"
            "v_mfma_f32_16x16x32_bf16 v[144:147], v[196:199], v[4:7], v[144:147]\n\t"
            "v_mul_f32 v216, v236, v216\n\t"
            "v_mul_f32 v217, v237, v217\n\t"
            "v_mfma_f32_16x16x32_bf16 v[148:151], v[196:199], v[28:31], 0\n\t"
            "v_mul_f32 v220, v236, v220\n\t"
            "v_mul_f32 v221, v237, v221\n\t"
            "v_mfma_f32_16x16x32_bf16 v[148:151], v[192:195], v[24:27], v[148:151]\n\t"
            "ds_read_b64 v[236:237], v247 offset:64\n\t"
            "v_mul_f32 v203, v249, v203\n\t"
            "v_mfma_f32_16x16x32_bf16 v[168:171], v[192:195], v[48:51], 0\n\t"
            "v_mul_f32 v206, v248, v206\n\t"
            "v_mul_f32 v207, v249, v207\n\t"
            "v_mfma_f32_16x16x32_bf16 v[168:171], v[196:199], v[52:55], v[168:171]\n\t"
            "v_mul_f32 v210, v248, v210\n\t"
            "v_mul_f32 v211, v249, v211\n\t"
            "v_mfma_f32_16x16x32_bf16 v[172:175], v[196:199], v[76:79], 0\n\t"
            "v_mul_f32 v214, v248, v214\n\t"
            "v_mul_f32 v215, v249, v215\n\t"
            "v_mfma_f32_16x16x32_bf16 v[172:175], v[192:195], v[72:75], v[172:175]\n\t"
            "v_mul_f32 v218, v248, v218\n\t"
            "v_mul_f32 v219, v249, v219\n\t"
            "v_mfma_f32_16x16x32_bf16 v[152:155], v[192:195], v[96:99], 0\n\t"
            "v_mul_f32 v222, v248, v222\n\t"
            "v_mul_f32 v223, v249, v223\n\t"
            "v_mfma_f32_16x16x32_bf16 v[152:155], v[196:199], v[100:103], v[152:155]\n\t"
            "ds_read_b64 v[248:249], v247 offset:72\n\t"
            "v_cvt_pk_bf16_f32 v200, v200, v201\n\t"
            "v_mfma_f32_16x16x32_bf16 v[156:159], v[196:199], v[124:127], 0\n\t"
            "ds_read_b128 v[196:199], v233 offset:6400\n\t"
            "v_cvt_pk_bf16_f32 v204, v204, v205\n\t"
            "v_mfma_f32_16x16x32_bf16 v[156:159], v[192:195], v[120:123], v[156:159]\n\t"
            "ds_read_b128 v[192:195], v232 offset:6400\n\t"
            "v_cvt_pk_bf16_f32 v208, v208, v209\n\t"
            "s_waitcnt lgkmcnt(4)\n\t"
            "v_mfma_f32_16x16x32_bf16 v[144:147], v[224:227], v[8:11], v[144:147]\n\t"
            "v_cvt_pk_bf16_f32 v201, v202, v203\n\t"
            "v_cvt_pk_bf16_f32 v205, v206, v207\n\t"
            "v_mfma_f32_16x16x32_bf16 v[144:147], v[228:231], v[12:15], v[144:147]\n\t"
            "v_cvt_pk_bf16_f32 v209, v210, v211\n\t"
            "v_cvt_pk_bf16_f32 v212, v212, v213\n\t"
            "v_mfma_f32_16x16x32_bf16 v[148:151], v[228:231], v[36:39], v[148:151]\n\t"
            "v_cvt_pk_bf16_f32 v216, v216, v217\n\t"
            "v_cvt_pk_bf16_f32 v220, v220, v221\n\t"
            "v_mfma_f32_16x16x32_bf16 v[148:151], v[224:227], v[32:35], v[148:151]\n\t"
            "v_cvt_pk_bf16_f32 v213, v214, v215\n\t"
            "v_cvt_pk_bf16_f32 v217, v218, v219\n\t"
            "v_mfma_f32_16x16x32_bf16 v[168:171], v[224:227], v[56:59], v[168:171]\n\t"
            "v_cvt_pk_bf16_f32 v221, v222, v223\n\t"
            "v_mfma_f32_16x16x32_bf16 v[168:171], v[228:231], v[60:63], v[168:171]\n\t"
            "v_mfma_f32_16x16x32_bf16 v[172:175], v[228:231], v[84:87], v[172:175]\n\t"
            "v_mfma_f32_16x16x32_bf16 v[172:175], v[224:227], v[80:83], v[172:175]\n\t"
            "v_mfma_f32_16x16x32_bf16 v[152:155], v[224:227], v[104:107], v[152:155]\n\t"
            "v_mfma_f32_16x16x32_bf16 v[152:155], v[228:231], v[108:111], v[152:155]\n\t"
            "v_mfma_f32_16x16x32_bf16 v[156:159], v[228:231], v[132:135], v[156:159]\n\t"
            "ds_read_b128 v[228:231], v233 offset:12288\n\t"
            "v_mfma_f32_16x16x32_bf16 v[156:159], v[224:227], v[128:131], v[156:159]\n\t"
            "ds_read_b128 v[224:227], v232 offset:12288\n\t"
            "s_waitcnt lgkmcnt(2)\n\t"
            "v_mfma_f32_16x16x32_bf16 v[144:147], v[192:195], v[16:19], v[144:147]\n\t"
            "v_mfma_f32_16x16x32_bf16 v[144:147], v[196:199], v[20:23], v[144:147]\n\t"
            "v_mfma_f32_16x16x32_bf16 v[148:151], v[196:199], v[44:47], v[148:151]\n\t"
            "v_mfma_f32_16x16x32_bf16 v[148:151], v[192:195], v[40:43], v[148:151]\n\t"
            "v_mfma_f32_16x16x32_bf16 v[168:171], v[192:195], v[64:67], v[168:171]\n\t"
            "v_mfma_f32_16x16x32_bf16 v[168:171], v[196:199], v[68:71], v[168:171]\n\t"
            "v_mfma_f32_16x16x32_bf16 v[172:175], v[196:199], v[92:95], v[172:175]\n\t"
            "v_mfma_f32_16x16x32_bf16 v[172:175], v[192:195], v[88:91], v[172:175]\n\t"
            "s_nop 1\n\t"
            "v_mul_f32 v144, v236, v144\n\t"
            "v_mfma_f32_16x16x32_bf16 v[152:155], v[192:195], v[112:115], v[152:155]\n\t"
            "v_mul_f32 v145, v237, v145\n\t"
            "v_mul_f32 v146, v248, v146\n\t"
            "v_mfma_f32_16x16x32_bf16 v[152:155], v[196:199], v[116:119], v[152:155]\n\t"
            "v_mul_f32 v148, v236, v148\n\t"
            "v_mul_f32 v149, v237, v149\n\t"
            "v_mfma_f32_16x16x32_bf16 v[156:159], v[196:199], v[140:143], v[156:159]\n\t"
            "ds_read_b128 v[196:199], v233 offset:12416\n\t"
            "v_mul_f32 v168, v236, v168\n\t"
            "v_mfma_f32_16x16x32_bf16 v[156:159], v[192:195], v[136:139], v[156:159]\n\t"
            "ds_read_b128 v[192:195], v232 offset:12416\n\t"
            "v_mul_f32 v169, v237, v169\n\t"
            "s_waitcnt lgkmcnt(2)\n\t"
            "v_mfma_f32_16x16x32_bf16 v[160:163], v[224:227], v[0:3], 0\n\t"
            "v_mul_f32 v172, v236, v172\n\t"
            "v_mul_f32 v173, v237, v173\n\t"
            "v_mfma_f32_16x16x32_bf16 v[160:163], v[228:231], v[4:7], v[160:163]\n\t"
            "v_mul_f32 v152, v236, v152\n\t"
            "v_mul_f32 v153, v237, v153\n\t"
            "v_mfma_f32_16x16x32_bf16 v[164:167], v[228:231], v[28:31], 0\n\t"
            "v_mul_f32 v156, v236, v156\n\t"
            "v_mul_f32 v157, v237, v157\n\t"
            "v_mfma_f32_16x16x32_bf16 v[164:167], v[224:227], v[24:27], v[164:167]\n\t"
            "ds_read_b64 v[236:237], v247 offset:128\n\t"
            "v_mul_f32 v147, v249, v147\n\t"
            "v_mfma_f32_16x16x32_bf16 v[176:179], v[224:227], v[48:51], 0\n\t"
            "v_mul_f32 v150, v248, v150\n\t"
            "v_mul_f32 v151, v249, v151\n\t"
            "v_mfma_f32_16x16x32_bf16 v[176:179], v[228:231], v[52:55], v[176:179]\n\t"
            "v_mul_f32 v170, v248, v170\n\t"
            "v_mul_f32 v171, v249, v171\n\t"
            "v_mfma_f32_16x16x32_bf16 v[180:183], v[228:231], v[76:79], 0\n\t"
            "v_mul_f32 v174, v248, v174\n\t"
            "v_mul_f32 v175, v249, v175\n\t"
            "v_mfma_f32_16x16x32_bf16 v[180:183], v[224:227], v[72:75], v[180:183]\n\t"
            "v_mul_f32 v154, v248, v154\n\t"
            "v_mul_f32 v155, v249, v155\n\t"
            "v_mfma_f32_16x16x32_bf16 v[184:187], v[224:227], v[96:99], 0\n\t"
            "v_mul_f32 v158, v248, v158\n\t"
            "v_mul_f32 v159, v249, v159\n\t"
            "v_mfma_f32_16x16x32_bf16 v[184:187], v[228:231], v[100:103], v[184:187]\n\t"
            "ds_read_b64 v[248:249], v247 offset:136\n\t"
            "v_cvt_pk_bf16_f32 v202, v144, v145\n\t"
            "v_mfma_f32_16x16x32_bf16 v[188:191], v[228:231], v[124:127], 0\n\t"
            "ds_read_b128 v[228:231], v233 offset:12544\n\t"
            "v_cvt_pk_bf16_f32 v203, v146, v147\n\t"
            "v_mfma_f32_16x16x32_bf16 v[188:191], v[224:227], v[120:123], v[188:191]\n\t"
            "ds_read_b128 v[224:227], v232 offset:12544\n\t"
            "v_cvt_pk_bf16_f32 v206, v148, v149\n\t"
            "s_waitcnt lgkmcnt(4)\n\t"
            "v_mfma_f32_16x16x32_bf16 v[188:191], v[196:199], v[132:135], v[188:191]\n\t"
            "v_cvt_pk_bf16_f32 v207, v150, v151\n\t"
            "v_cvt_pk_bf16_f32 v210, v168, v169\n\t"
            "v_mfma_f32_16x16x32_bf16 v[188:191], v[192:195], v[128:131], v[188:191]\n\t"
            "v_cvt_pk_bf16_f32 v211, v170, v171\n\t"
            "v_cvt_pk_bf16_f32 v214, v172, v173\n\t"
            "v_mfma_f32_16x16x32_bf16 v[176:179], v[192:195], v[56:59], v[176:179]\n\t"
            "ds_read_b128 v[168:171], v254\n\t"
            "v_cvt_pk_bf16_f32 v215, v174, v175\n\t"
            "v_mfma_f32_16x16x32_bf16 v[176:179], v[196:199], v[60:63], v[176:179]\n\t"
            "ds_read_b128 v[172:175], v254 offset:64\n\t"
            "v_cvt_pk_bf16_f32 v218, v152, v153\n\t"
            "v_mfma_f32_16x16x32_bf16 v[180:183], v[196:199], v[84:87], v[180:183]\n\t"
            "v_cvt_pk_bf16_f32 v219, v154, v155\n\t"
            "v_cvt_pk_bf16_f32 v222, v156, v157\n\t"
            "v_mfma_f32_16x16x32_bf16 v[180:183], v[192:195], v[80:83], v[180:183]\n\t"
            "v_cvt_pk_bf16_f32 v223, v158, v159\n\t"
            "v_mfma_f32_16x16x32_bf16 v[160:163], v[192:195], v[8:11], v[160:163]\n\t"
            "v_mfma_f32_16x16x32_bf16 v[160:163], v[196:199], v[12:15], v[160:163]\n\t"
            "ds_write_b128 v243, v[200:203]\n\t"
            "s_waitcnt lgkmcnt(3)\n\t"
            "v_mfma_f32_16x16x32_bf16 v[188:191], v[228:231], v[140:143], v[188:191]\n\t"
            "v_mfma_f32_16x16x32_bf16 v[188:191], v[224:227], v[136:139], v[188:191]\n\t"
            "s_waitcnt vmcnt(0)\n\t"
            "ds_read_b64 v[144:145], v253\n\t"
            "v_mfma_f32_16x16x32_bf16 v[164:167], v[196:199], v[36:39], v[164:167]\n\t"
            "ds_read_b64 v[146:147], v253 offset:96\n\t"
            "ds_read_b64 v[148:149], v253 offset:1744\n\t"
            "v_mfma_f32_16x16x32_bf16 v[164:167], v[192:195], v[32:35], v[164:167]\n\t"
            "ds_read_b64 v[150:151], v253 offset:1840\n\t"
            "ds_read_b64 v[152:153], v253 offset:8\n\t"
            "v_mfma_f32_16x16x32_bf16 v[176:179], v[224:227], v[64:67], v[176:179]\n\t"
            "ds_read_b64 v[154:155], v253 offset:104\n\t"
            "ds_read_b64 v[156:157], v253 offset:1752\n\t"
            "v_mfma_f32_16x16x32_bf16 v[176:179], v[228:231], v[68:71], v[176:179]\n\t"
            "ds_read_b64 v[158:159], v253 offset:1848\n\t"
            "v_mul_f32 v191, v249, v191\n\t"
            "v_mfma_f32_16x16x32_bf16 v[180:183], v[228:231], v[92:95], v[180:183]\n\t"
            "v_mul_f32 v188, v236, v188\n\t"
            "v_mul_f32 v189, v237, v189\n\t"
            "v_mfma_f32_16x16x32_bf16 v[180:183], v[224:227], v[88:91], v[180:183]\n\t"
            "v_mul_f32 v190, v248, v190\n\t"
            "v_cvt_pk_bf16_f32 v188, v188, v189\n\t"
            "v_mfma_f32_16x16x32_bf16 v[160:163], v[224:227], v[16:19], v[160:163]\n\t"
            "v_mul_f32 v176, v236, v176\n\t"
            "v_mul_f32 v177, v237, v177\n\t"
            "v_mfma_f32_16x16x32_bf16 v[160:163], v[228:231], v[20:23], v[160:163]\n\t"
            "v_mul_f32 v178, v248, v178\n\t"
            "v_mul_f32 v179, v249, v179\n\t"
            "v_mfma_f32_16x16x32_bf16 v[164:167], v[228:231], v[44:47], v[164:167]\n\t"
            "v_cvt_pk_bf16_f32 v176, v176, v177\n\t"
            "v_cvt_pk_bf16_f32 v177, v178, v179\n\t"
            "v_mfma_f32_16x16x32_bf16 v[164:167], v[224:227], v[40:43], v[164:167]\n\t"
            "ds_write_b64 v244, v[176:177] offset:3072\n\t"
            "v_mfma_f32_16x16x32_bf16 v[184:187], v[192:195], v[104:107], v[184:187]\n\t"
            "v_mul_f32 v180, v236, v180\n\t"
            "v_mul_f32 v181, v237, v181\n\t"
            "v_mfma_f32_16x16x32_bf16 v[184:187], v[196:199], v[108:111], v[184:187]\n\t"
            "v_mul_f32 v160, v236, v160\n\t"
            "v_mul_f32 v161, v237, v161\n\t"
            "v_mfma_f32_16x16x32_bf16 v[184:187], v[224:227], v[112:115], v[184:187]\n\t"
            "v_mul_f32 v164, v236, v164\n\t"
            "v_mul_f32 v165, v237, v165\n\t"
            "v_mfma_f32_16x16x32_bf16 v[184:187], v[228:231], v[116:119], v[184:187]\n\t"
            "v_mul_f32 v182, v248, v182\n\t"
            "v_mul_f32 v183, v249, v183\n\t"
            "v_cvt_pk_bf16_f32 v180, v180, v181\n\t"
            "v_mov_b64 v[176:177], 0\n\t"
            "v_mov_b64 v[178:179], 0\n\t"
            "v_mul_f32 v162, v248, v162\n\t"
            "v_mul_f32 v163, v249, v163\n\t"
            "s_waitcnt lgkmcnt(5)\n\t"
            "v_smfmac_f32_16x16x64_bf16 v[176:179], v[168:171], v[144:151], v255\n\t"
            "ds_read_b128 v[168:171], v254 offset:128\n\t"
            "ds_read_b64 v[144:145], v253 offset:24\n\t"
            "ds_read_b64 v[146:147], v253 offset:120\n\t"
            "ds_read_b64 v[148:149], v253 offset:1768\n\t"
            "ds_read_b64 v[150:151], v253 offset:1864\n\t"
            "v_cvt_pk_bf16_f32 v160, v160, v161\n\t"
            "v_mul_f32 v166, v248, v166\n\t"
            "v_mul_f32 v167, v249, v167\n\t"
            "v_cvt_pk_bf16_f32 v164, v164, v165\n\t"
            "v_cvt_pk_bf16_f32 v181, v182, v183\n\t"
            "v_cvt_pk_bf16_f32 v161, v162, v163\n\t"
            "v_cvt_pk_bf16_f32 v165, v166, v167\n\t"
            "v_mov_b64 v[182:183], 0\n\t"
            "v_mul_f32 v187, v249, v187\n\t"
            "v_mov_b32 v249, s76\n\t"
            "v_mul_f32 v184, v236, v184\n\t"
            "v_mul_f32 v185, v237, v185\n\t"
            "v_mul_f32 v186, v248, v186\n\t"
            "v_cvt_pk_bf16_f32 v184, v184, v185\n\t"
            "v_cvt_pk_bf16_f32 v185, v186, v187\n\t"
            "v_mov_b64 v[186:187], 0\n\t"
            "v_cvt_pk_bf16_f32 v189, v190, v191\n\t"
            "v_mov_b64 v[190:191], 0\n\t"
            "v_cvt_pk_bf16_f32 v176, v176, v177\n\t"
            "v_add_u32 v236, s67, v250\n\t"
            "v_cvt_pk_bf16_f32 v177, v178, v179\n\t"
            "v_add_u32 v248, s67, v251\n\t"
            "s_nop 0\n\t"
            "v_mfma_f32_4x4x4_16b_bf16 v[196:199], v[176:177], v[176:177], 0\n\t"
            "ds_read_b64 v[162:163], v253 offset:112\n\t"
            "ds_read_b64 v[166:167], v253 offset:1856\n\t"
            "ds_read_b32 v249, v249\n\t"
            "ds_write_b64 v244, v[180:181] offset:4608\n\t"
            "ds_write_b64 v244, v[160:161]\n\t"
            "s_waitcnt lgkmcnt(14)\n\t"
            "ds_read_b64 v[160:161], v253 offset:16\n\t"
            "s_waitcnt lgkmcnt(14)\n\t"
            "ds_write_b64 v244, v[164:165] offset:1536\n\t"
            "s_waitcnt lgkmcnt(14)\n\t"
            "ds_read_b64 v[164:165], v253 offset:1760\n\t"
            "v_mov_b64 v[180:181], 0\n\t"
            "s_waitcnt lgkmcnt(14)\n\t"
            "s_nop 0\n\t"
            "v_smfmac_f32_16x16x64_bf16 v[180:183], v[172:175], v[152:159], v255\n\t"
            "ds_read_b128 v[172:175], v254 offset:192\n\t"
            "s_waitcnt lgkmcnt(14)\n\t"
            "ds_read_b64 v[152:153], v253 offset:32\n\t"
            "s_waitcnt lgkmcnt(14)\n\t"
            "ds_read_b64 v[154:155], v253 offset:128\n\t"
            "s_waitcnt lgkmcnt(14)\n\t"
            "ds_read_b64 v[156:157], v253 offset:1776\n\t"
            "s_waitcnt lgkmcnt(14)\n\t"
            "ds_read_b64 v[158:159], v253 offset:1872\n\t"
            "s_waitcnt lgkmcnt(14)\n\t"
            "ds_write_b64 v244, v[184:185] offset:6144\n\t"
            "v_mov_b64 v[184:185], 0\n\t"
            "v_cvt_pk_bf16_f32 v178, v180, v181\n\t"
            "v_cvt_pk_bf16_f32 v179, v182, v183\n\t"
            "v_mov_b64 v[180:181], 0\n\t"
            "s_waitcnt lgkmcnt(11)\n\t"
            "v_readfirstlane_b32 s26, v249\n\t"
            "v_mfma_f32_16x16x32_bf16 v[192:195], v[238:241], v[176:179], 0\n\t"
            "ds_read_b32 v249, v252 offset:12\n\t"
            "ds_write_b64 v244, v[188:189] offset:7680\n\t"
            "v_mfma_f32_4x4x4_16b_bf16 v[196:199], v[178:179], v[178:179], v[196:199]\n\t"
            "v_mov_b64 v[188:189], 0\n\t"
            "s_and_b32 s68, s26, 0x7ffffff\n\t"
            "s_add_u32 s68, s68, s78\n\t"
            "s_and_b32 s69, s26, 0x38000000\n\t"
            "s_or_b32 s69, s69, s39\n\t"
            "s_bitcmp1_b32 s26, 27\n\t"
            "s_cselect_b32 s79, 2.0, s68\n\t"
            "s_waitcnt lgkmcnt(8)\n\t"
            "v_smfmac_f32_16x16x64_bf16 v[184:187], v[168:171], v[160:167], v255\n\t"
            "s_and_b32 s57, s56, 3\n\t"
            "s_lshl_b32 s58, s57, 3\n\t"
            "s_lshr_b32 m0, s54, s58\n\t"
            "s_waitcnt lgkmcnt(7)\n\t"
            "v_smfmac_f32_16x16x64_bf16 v[188:191], v[172:175], v[144:151], v255\n\t"
            "s_lshl_b32 s58, s57, 4\n\t"
            "s_lshr_b64 s[36:37], s[50:51], s58\n\t"
            "s_and_b32 s57, s36, 0xffff\n\t"
            "s_add_u32 s57, s57, s67\n\t"
            "ds_read_b128 v[168:171], v254 offset:256\n\t"
            "ds_read_b64 v[160:161], v253 offset:40\n\t"
            "ds_read_b64 v[162:163], v253 offset:136\n\t"
            "ds_read_b64 v[164:165], v253 offset:1784\n\t"
            "ds_read_b64 v[166:167], v253 offset:1880\n\t"
            "ds_read_b128 v[172:175], v254 offset:320\n\t"
            "ds_read_b64 v[144:145], v253 offset:48\n\t"
            "ds_read_b64 v[146:147], v253 offset:144\n\t"
            "s_waitcnt lgkmcnt(14)\n\t"
            "ds_read_b64 v[148:149], v253 offset:1792\n\t"
            "s_waitcnt lgkmcnt(14)\n\t"
            "ds_read_b64 v[150:151], v253 offset:1888\n\t"
            "v_writelane_b32 v236, s57, 63\n\t"
            "v_writelane_b32 v236, s59, m0\n\t"
            "v_mov_b64 v[182:183], 0\n\t"
            "v_cvt_pk_bf16_f32 v184, v184, v185\n\t"
            "v_cvt_pk_bf16_f32 v185, v186, v187\n\t"
            "v_xor_b32 v237, 16, v236\n\t"
            "v_cvt_pk_bf16_f32 v186, v188, v189\n\t"
            "v_mfma_f32_4x4x4_16b_bf16 v[196:199], v[184:185], v[184:185], v[196:199]\n\t"
            "s_waitcnt lgkmcnt(14)\n\t"
            "ds_write_b128 v236, v[176:179]\n\t"
            "v_mov_b64 v[176:177], 0\n\t"
            "v_mov_b64 v[178:179], 0\n\t"
            "v_cvt_pk_bf16_f32 v187, v190, v191\n\t"
            "v_mov_b64 v[188:189], 0\n\t"
            "v_mov_b64 v[190:191], 0\n\t"
            "v_mfma_f32_16x16x32_bf16 v[192:195], v[238:241], v[184:187], v[192:195]\n\t"
            "s_lshr_b64 s[36:37], s[52:53], s58\n\t"
            "s_and_b32 s58, s36, 0xffff\n\t"
            "s_add_u32 s58, s58, s67\n\t"
            "s_waitcnt lgkmcnt(10)\n\t"
            "v_smfmac_f32_16x16x64_bf16 v[176:179], v[168:171], v[152:159], v255\n\t"
            "s_and_b32 s38, s52, 0xffff\n\t"
            "s_add_u32 s38, s38, s67\n\t"
            "ds_read_b128 v[168:171], v254 offset:384\n\t"
            "s_waitcnt lgkmcnt(6)\n\t"
            "v_smfmac_f32_16x16x64_bf16 v[180:183], v[172:175], v[160:167], v255\n\t"
            "ds_read_b64 v[152:153], v253 offset:56\n\t"
            "v_mfma_f32_4x4x4_16b_bf16 v[196:199], v[186:187], v[186:187], v[196:199]\n\t"
            "ds_read_b64 v[154:155], v253 offset:152\n\t"
            "ds_read_b64 v[156:157], v253 offset:1800\n\t"
            "ds_read_b64 v[158:159], v253 offset:1896\n\t"
            "ds_read_b128 v[172:175], v254 offset:448\n\t"
            "ds_read_b64 v[160:161], v253 offset:64\n\t"
            "ds_read_b64 v[162:163], v253 offset:160\n\t"
            "ds_read_b64 v[164:165], v253 offset:1808\n\t"
            "ds_read_b64 v[166:167], v253 offset:1904\n\t"
            "s_waitcnt lgkmcnt(14)\n\t"
            "ds_write_b128 v237, v[184:187]\n\t"
            "v_mov_b64 v[184:185], 0\n\t"
            "v_mov_b64 v[186:187], 0\n\t"
            "v_cvt_pk_bf16_f32 v176, v176, v177\n\t"
            "v_cvt_pk_bf16_f32 v177, v178, v179\n\t"
            "v_cvt_pk_bf16_f32 v178, v180, v181\n\t"
            "v_cvt_pk_bf16_f32 v179, v182, v183\n\t"
            "v_mfma_f32_4x4x4_16b_bf16 v[196:199], v[176:177], v[176:177], v[196:199]\n\t"
            "s_waitcnt lgkmcnt(14)\n\t"
            "ds_write_b128 v236, v[176:179] offset:128\n\t"
            "s_waitcnt lgkmcnt(11)\n\t"
            "v_smfmac_f32_16x16x64_bf16 v[184:187], v[168:171], v[144:151], v255\n\t"
            "ds_read_b128 v[168:171], v254 offset:512\n\t"
            "v_mfma_f32_16x16x32_bf16 v[192:195], v[238:241], v[176:179], v[192:195]\n\t"
            "ds_read_b64 v[144:145], v253 offset:72\n\t"
            "ds_read_b64 v[146:147], v253 offset:168\n\t"
            "v_mfma_f32_4x4x4_16b_bf16 v[196:199], v[178:179], v[178:179], v[196:199]\n\t"
            "ds_read_b64 v[148:149], v253 offset:1816\n\t"
            "s_waitcnt lgkmcnt(10)\n\t"
            "v_smfmac_f32_16x16x64_bf16 v[188:191], v[172:175], v[152:159], v255\n\t"
            "ds_read_b64 v[150:151], v253 offset:1912\n\t"
            "ds_read_b128 v[172:175], v254 offset:576\n\t"
            "ds_read_b64 v[152:153], v253 offset:80\n\t"
            "ds_read_b64 v[154:155], v253 offset:176\n\t"
            "ds_read_b64 v[156:157], v253 offset:1824\n\t"
            "s_waitcnt lgkmcnt(14)\n\t"
            "ds_read_b64 v[158:159], v253 offset:1920\n\t"
            "v_mov_b64 v[176:177], 0\n\t"
            "v_mov_b64 v[178:179], 0\n\t"
            "v_mov_b64 v[180:181], 0\n\t"
            "v_mov_b64 v[182:183], 0\n\t"
            "v_cvt_pk_bf16_f32 v184, v184, v185\n\t"
            "v_cvt_pk_bf16_f32 v185, v186, v187\n\t"
            "v_cvt_pk_bf16_f32 v186, v188, v189\n\t"
            "v_cvt_pk_bf16_f32 v187, v190, v191\n\t"
            "v_mfma_f32_4x4x4_16b_bf16 v[196:199], v[184:185], v[184:185], v[196:199]\n\t"
            "s_waitcnt lgkmcnt(14)\n\t"
            "ds_write_b128 v237, v[184:187] offset:128\n\t"
            "v_mfma_f32_16x16x32_bf16 v[192:195], v[238:241], v[184:187], v[192:195]\n\t"
            "v_mov_b64 v[184:185], 0\n\t"
            "v_mov_b64 v[188:189], 0\n\t"
            "s_waitcnt lgkmcnt(10)\n\t"
            "v_smfmac_f32_16x16x64_bf16 v[176:179], v[168:171], v[160:167], v255\n\t"
            "ds_read_b128 v[168:171], v254 offset:640\n\t"
            "v_mfma_f32_4x4x4_16b_bf16 v[196:199], v[186:187], v[186:187], v[196:199]\n\t"
            "ds_read_b64 v[160:161], v253 offset:88\n\t"
            "ds_read_b64 v[162:163], v253 offset:184\n\t"
            "ds_read_b64 v[164:165], v253 offset:1832\n\t"
            "s_waitcnt lgkmcnt(9)\n\t"
            "v_smfmac_f32_16x16x64_bf16 v[180:183], v[172:175], v[144:151], v255\n\t"
            "ds_read_b64 v[166:167], v253 offset:1928\n\t"
            "ds_read_b128 v[172:175], v254 offset:704\n\t"
            "v_mov_b64 v[186:187], 0\n\t"
            "v_cvt_pk_bf16_f32 v176, v176, v177\n\t"
            "v_mov_b64 v[190:191], 0\n\t"
            "v_cvt_pk_bf16_f32 v177, v178, v179\n\t"
            "v_writelane_b32 v248, s58, 63\n\t"
            "v_writelane_b32 v248, s38, m0\n\t"
            "v_mfma_f32_4x4x4_16b_bf16 v[196:199], v[176:177], v[176:177], v[196:199]\n\t"
            "v_cvt_pk_bf16_f32 v178, v180, v181\n\t"
            "v_cvt_pk_bf16_f32 v179, v182, v183\n\t"
            "v_and_b32 v249, s69, v249\n\t"
            "s_nop 0\n\t"
            "v_mfma_f32_4x4x4_16b_bf16 v[196:199], v[178:179], v[178:179], v[196:199]\n\t"
            "ds_write_b128 v236, v[176:179] offset:256\n\t"
            "v_mfma_f32_16x16x32_bf16 v[192:195], v[238:241], v[176:179], v[192:195]\n\t"
            "ds_write_b128 v243, v[204:207] offset:1536\n\t"
            "s_waitcnt lgkmcnt(7)\n\t"
            "v_smfmac_f32_16x16x64_bf16 v[184:187], v[168:171], v[152:159], v255\n\t"
            "ds_write_b128 v243, v[208:211] offset:3072\n\t"
            "s_waitcnt lgkmcnt(3)\n\t"
            "v_smfmac_f32_16x16x64_bf16 v[188:191], v[172:175], v[160:167], v255\n\t"
            "ds_write_b128 v243, v[212:215] offset:4608\n\t"
            "ds_write_b128 v243, v[216:219] offset:6144\n\t"
            "ds_write_b128 v243, v[220:223] offset:7680\n\t"
            "s_mov_b32 exec_hi, 0x3fffff\n\t"
            "s_add_u32 m0, s66, 0\n\t"
            "s_add_u32 s49, s79, 0\n\t"
            "buffer_load_dwordx4 v249, s[72:75], s49 offen lds\n\t"
            "s_mov_b32 exec_hi, -1\n\t"
            "v_cvt_pk_bf16_f32 v184, v184, v185\n\t"
            "v_cvt_pk_bf16_f32 v185, v186, v187\n\t"
            "v_cvt_pk_bf16_f32 v186, v188, v189\n\t"
            "v_cvt_pk_bf16_f32 v187, v190, v191\n\t"
            "v_mfma_f32_4x4x4_16b_bf16 v[196:199], v[184:185], v[184:185], v[196:199]\n\t"
            "ds_write_b128 v237, v[184:187] offset:256\n\t"
            "v_mfma_f32_16x16x32_bf16 v[192:195], v[238:241], v[184:187], v[192:195]\n\t"
            "s_mov_b32 exec_hi, 0x3fffff\n\t"
            "s_add_u32 m0, s66, 872\n\t"
            "s_add_u32 s49, s68, 196224\n\t"
            "buffer_load_dwordx4 v249, s[72:75], s49 offen lds\n\t"
            "s_mov_b32 exec_hi, -1\n\t"
            "v_mfma_f32_4x4x4_16b_bf16 v[196:199], v[186:187], v[186:187], v[196:199]\n\t"
            "s_mov_b32 exec_hi, 0x3fffff\n\t"
            "s_add_u32 m0, s66, 1744\n\t"
            "s_add_u32 s49, s68, 392448\n\t"
            "buffer_load_dwordx4 v249, s[72:75], s49 offen lds\n\t"
            "s_mov_b32 exec_hi, -1\n\t"
            "s_mov_b32 exec_hi, 0x3fffff\n\t"
            "s_add_u32 m0, s66, 2616\n\t"
            "s_add_u32 s49, s68, 588672\n\t"
            "buffer_load_dwordx4 v249, s[72:75], s49 offen lds\n\t"
            "s_mov_b32 exec_hi, -1\n\t"
            "v_cndmask_b32_e64 v193, v196, v197, s[60:61]\n\t"
            "v_cndmask_b32_e64 v193, v193, v198, s[62:63]\n\t"
            "v_cndmask_b32_e64 v193, v193, v199, s[64:65]\n\t"
            "ds_write_b64 v248, v[192:193] offset:18816\n\t"
            "s_mov_b64 exec, 1\n\t"
            "ds_inc_u32 v255, v238 offset:7860\n\t"
            "s_mov_b64 exec, -1\n\t"
            "s_waitcnt lgkmcnt(1)\n\t"
            "s_barrier\n\t"
            "s_mov_b32 exec_hi, 0x3fffff\n\t"
            "s_add_u32 m0, s66, 3488\n\t"
            "s_add_u32 s49, s68, 784896\n\t"
            "buffer_load_dwordx4 v249, s[72:75], s49 offen lds\n\t"
            "s_mov_b32 exec_hi, -1\n\t"
            "s_mov_b32 exec_hi, 0x3fffff\n\t"
            "s_add_u32 m0, s66, 4360\n\t"
            "s_add_u32 s49, s68, 981120\n\t"
            "buffer_load_dwordx4 v249, s[72:75], s49 offen lds\n\t"
            "s_mov_b32 exec_hi, -1\n\t"
            "s_mov_b32 exec_hi, 0x3fffff\n\t"
            "s_add_u32 m0, s66, 5232\n\t"
            "s_add_u32 s49, s68, 1177344\n\t"
            "buffer_load_dwordx4 v249, s[72:75], s49 offen lds\n\t"
            "s_mov_b32 exec_hi, -1\n\t"
            "s_mov_b32 exec_hi, 0x3fffff\n\t"
            "s_add_u32 m0, s66, 6104\n\t"
            "s_add_u32 s49, s68, 1373568\n\t"
            "buffer_load_dwordx4 v249, s[72:75], s49 offen lds\n\t"
            "s_mov_b32 exec_hi, -1\n\t"
            "s_mov_b32 exec_hi, 0x3fffff\n\t"
            "s_add_u32 m0, s66, 6976\n\t"
            "s_add_u32 s49, s68, 1569792\n\t"
            "buffer_load_dwordx4 v249, s[72:75], s49 offen lds\n\t"
            "s_mov_b32 exec_hi, -1\n\t"
            "s_add_u32 s59, s59, -47104\n\t"
            "s_add_u32 s45, s46, s27\n\t"
            "s_mov_b32 s46, s47\n\t"
            "s_mov_b32 s47, s68\n\t"
            "s_xor_b32 s48, s48, s71\n\t"
            "s_xor_b32 s67, s67, s71\n\t"
            "s_add_u32 s76, s76, 4\n\t"
            "s_mov_b32 s44, 12\n\t"
            "s_lshl_b32 s56, s56, 2\n\t"
            "s_lshr_b32 s49, s26, 30\n\t"
            "s_or_b32 s56, s56, s49\n\t"
            "v_add_u32 v232, s48, v242\n\t"
            "v_xor_b32 v233, 64, v232\n\t"
            "ds_read_b128 v[192:195], v232\n\t"
            "ds_read_b128 v[196:199], v233\n\t"
            "ds_read_b128 v[224:227], v232 offset:128\n\t"
            "ds_read_b128 v[228:231], v233 offset:128\n\t"
            "s_waitcnt lgkmcnt(2)\n\t"
            "v_mfma_f32_16x16x32_bf16 v[200:203], v[192:195], v[0:3], 0\n\t"
            "v_mfma_f32_16x16x32_bf16 v[200:203], v[196:199], v[4:7], v[200:203]\n\t"
            "v_mfma_f32_16x16x32_bf16 v[204:207], v[196:199], v[28:31], 0\n\t"
            "v_mfma_f32_16x16x32_bf16 v[204:207], v[192:195], v[24:27], v[204:207]\n\t"
            "v_mfma_f32_16x16x32_bf16 v[208:211], v[192:195], v[48:51], 0\n\t"
            "v_mfma_f32_16x16x32_bf16 v[208:211], v[196:199], v[52:55], v[208:211]\n\t"
            "v_mfma_f32_16x16x32_bf16 v[212:215], v[196:199], v[76:79], 0\n\t"
            "v_mfma_f32_16x16x32_bf16 v[212:215], v[192:195], v[72:75], v[212:215]\n\t"
            "v_mfma_f32_16x16x32_bf16 v[216:219], v[192:195], v[96:99], 0\n\t"
            "v_mfma_f32_16x16x32_bf16 v[216:219], v[196:199], v[100:103], v[216:219]\n\t"
            "v_mfma_f32_16x16x32_bf16 v[220:223], v[196:199], v[124:127], 0\n\t"
            "v_mfma_f32_16x16x32_bf16 v[220:223], v[192:195], v[120:123], v[220:223]\n\t"
            "s_waitcnt lgkmcnt(0)\n\t"
            "v_mfma_f32_16x16x32_bf16 v[200:203], v[224:227], v[8:11], v[200:203]\n\t"
            "v_mfma_f32_16x16x32_bf16 v[200:203], v[228:231], v[12:15], v[200:203]\n\t"
            "v_mfma_f32_16x16x32_bf16 v[204:207], v[228:231], v[36:39], v[204:207]\n\t"
            "v_mfma_f32_16x16x32_bf16 v[204:207], v[224:227], v[32:35], v[204:207]\n\t"
            "v_mfma_f32_16x16x32_bf16 v[208:211], v[224:227], v[56:59], v[208:211]\n\t"
            "v_mfma_f32_16x16x32_bf16 v[208:211], v[228:231], v[60:63], v[208:211]\n\t"
            "v_mfma_f32_16x16x32_bf16 v[212:215], v[228:231], v[84:87], v[212:215]\n\t"
            "v_mfma_f32_16x16x32_bf16 v[212:215], v[224:227], v[80:83], v[212:215]\n\t"
            "v_mfma_f32_16x16x32_bf16 v[216:219], v[224:227], v[104:107], v[216:219]\n\t"
            "v_mfma_f32_16x16x32_bf16 v[216:219], v[228:231], v[108:111], v[216:219]\n\t"
            "v_mfma_f32_16x16x32_bf16 v[220:223], v[228:231], v[132:135], v[220:223]\n\t"
            "v_mfma_f32_16x16x32_bf16 v[220:223], v[224:227], v[128:131], v[220:223]\n\t"
            "ds_read_b128 v[224:227], v232 offset:256\n\t"
            "ds_read_b128 v[228:231], v233 offset:256\n\t"
            "ds_read_b128 v[168:171], v254\n\t"
            "ds_read_b128 v[172:175], v254 offset:64\n\t"
            "s_waitcnt vmcnt(0)\n\t"
            "ds_read_b64 v[144:145], v253\n\t"
            "ds_read_b64 v[146:147], v253 offset:96\n\t"
            "ds_read_b64 v[148:149], v253 offset:1744\n\t"
            "ds_read_b64 v[150:151], v253 offset:1840\n\t"
            "ds_read_b64 v[152:153], v253 offset:8\n\t"
            "ds_read_b64 v[154:155], v253 offset:104\n\t"
            "ds_read_b64 v[156:157], v253 offset:1752\n\t"
            "ds_read_b64 v[158:159], v253 offset:1848\n\t"
            "ds_read_b64 v[160:161], v253 offset:16\n\t"
            "ds_read_b64 v[162:163], v253 offset:112\n\t"
            "ds_read_b64 v[164:165], v253 offset:1760\n\t"
            "s_waitcnt lgkmcnt(14)\n\t"
            "ds_read_b64 v[166:167], v253 offset:1856\n\t"
            "s_waitcnt lgkmcnt(0)\n\t"
            "s_nop 7\n\t"
            ".p2align 6\n\t"
            "s_nop 0\n\t"
            "s_nop 0\n\t"
            "s_nop 0\n\t"
            "s_nop 0\n\t"
            ".Lwc_loop%=:\n\t"
            "s_setprio 1\n\t"
            "s_setprio 3\n\t"
            "v_mfma_f32_16x16x32_bf16 v[200:203], v[224:227], v[16:19], v[200:203]\n\t"
            "v_mov_b64 v[176:177], 0\n\t"
            "v_mov_b64 v[178:179], 0\n\t"
            "v_mfma_f32_16x16x32_bf16 v[200:203], v[228:231], v[20:23], v[200:203]\n\t"
            "v_mov_b64 v[180:181], 0\n\t"
            "v_mov_b64 v[182:183], 0\n\t"
            "s_waitcnt lgkmcnt(8)\n\t"
            "v_smfmac_f32_16x16x64_bf16 v[176:179], v[168:171], v[144:151], v255\n\t"
            "ds_read_b128 v[168:171], v254 offset:128\n\t"
            "s_waitcnt lgkmcnt(5)\n\t"
            "v_smfmac_f32_16x16x64_bf16 v[180:183], v[172:175], v[152:159], v255\n\t"
            "ds_read_b64 v[144:145], v253 offset:24\n\t"
            "v_mfma_f32_16x16x32_bf16 v[204:207], v[228:231], v[44:47], v[204:207]\n\t"
            "ds_read_b64 v[146:147], v253 offset:120\n\t"
            "ds_read_b64 v[148:149], v253 offset:1768\n\t"
            "v_mfma_f32_16x16x32_bf16 v[204:207], v[224:227], v[40:43], v[204:207]\n\t"
            "ds_read_b64 v[150:151], v253 offset:1864\n\t"
            "ds_read_b128 v[172:175], v254 offset:192\n\t"
            "v_mfma_f32_16x16x32_bf16 v[208:211], v[224:227], v[64:67], v[208:211]\n\t"
            "ds_read_b64 v[152:153], v253 offset:32\n\t"
            "ds_read_b64 v[154:155], v253 offset:128\n\t"
            "v_mfma_f32_16x16x32_bf16 v[208:211], v[228:231], v[68:71], v[208:211]\n\t"
            "ds_read_b64 v[156:157], v253 offset:1776\n\t"
            "ds_read_b64 v[158:159], v253 offset:1872\n\t"
            "v_mfma_f32_16x16x32_bf16 v[212:215], v[228:231], v[92:95], v[212:215]\n\t"
            "v_mov_b32 v249, s76\n\t"
            "v_mov_b64 v[184:185], 0\n\t"
            "v_mfma_f32_16x16x32_bf16 v[212:215], v[224:227], v[88:91], v[212:215]\n\t"
            "ds_read_b32 v249, v249\n\t"
            "v_mov_b64 v[186:187], 0\n\t"
            "v_mfma_f32_16x16x32_bf16 v[216:219], v[224:227], v[112:115], v[216:219]\n\t"
            "v_mov_b64 v[188:189], 0\n\t"
            "v_mov_b64 v[190:191], 0\n\t"
            "v_mfma_f32_16x16x32_bf16 v[216:219], v[228:231], v[116:119], v[216:219]\n\t"
            "v_cvt_pk_bf16_f32 v176, v176, v177\n\t"
            "v_add_u32 v236, s67, v250\n\t"
            "s_waitcnt lgkmcnt(10)\n\t"
            "v_smfmac_f32_16x16x64_bf16 v[184:187], v[168:171], v[160:167], v255\n\t"
            "ds_read_b128 v[168:171], v254 offset:256\n\t"
            "v_mfma_f32_16x16x32_bf16 v[220:223], v[228:231], v[140:143], v[220:223]\n\t"
            "ds_read_b64 v[160:161], v253 offset:40\n\t"
            "ds_read_b64 v[162:163], v253 offset:136\n\t"
            "v_mfma_f32_16x16x32_bf16 v[220:223], v[224:227], v[136:139], v[220:223]\n\t"
            "ds_read_b64 v[164:165], v253 offset:1784\n\t"
            "ds_read_b64 v[166:167], v253 offset:1880\n\t"
            "s_waitcnt lgkmcnt(10)\n\t"
            "v_smfmac_f32_16x16x64_bf16 v[188:191], v[172:175], v[144:151], v255\n\t"
            "s_setprio 1\n\t"
            "ds_read_b128 v[172:175], v254 offset:320\n\t"
            "ds_read_b64 v[144:145], v253 offset:48\n\t"
            "ds_read_b64 v[146:147], v253 offset:144\n\t"
            "ds_read_b64 v[148:149], v253 offset:1792\n\t"
            "ds_read_b64 v[150:151], v253 offset:1888\n\t"
            "s_waitcnt lgkmcnt(14)\n\t"
            "ds_read_b128 v[224:227], v232 offset:6272\n\t"
            "s_waitcnt lgkmcnt(14)\n\t"
            "ds_read_b128 v[228:231], v233 offset:6272\n\t"
            "s_waitcnt lgkmcnt(12)\n\t"
            "v_readfirstlane_b32 s26, v249\n\t"
            "v_cvt_pk_bf16_f32 v177, v178, v179\n\t"
            "v_cvt_pk_bf16_f32 v178, v180, v181\n\t"
            "s_and_b32 s68, s26, 0x7ffffff\n\t"
            "s_add_u32 s68, s68, s78\n\t"
            "s_and_b32 s69, s26, 0x38000000\n\t"
            "s_or_b32 s69, s69, s39\n\t"
            "s_bitcmp1_b32 s26, 27\n\t"
            "s_cselect_b32 s79, 2.0, s68\n\t"
            "s_setprio 3\n\t"
            "v_mfma_f32_4x4x4_16b_bf16 v[196:199], v[176:177], v[176:177], 0\n\t"
            "s_setprio 1\n\t"
            "ds_read_b32 v249, v252 offset:12\n\t"
            "s_and_b32 s57, s56, 3\n\t"
            "s_lshl_b32 s58, s57, 3\n\t"
            "s_lshr_b32 m0, s54, s58\n\t"
            "s_lshl_b32 s58, s57, 4\n\t"
            "s_lshr_b64 s[36:37], s[50:51], s58\n\t"
            "s_and_b32 s57, s36, 0xffff\n\t"
            "s_add_u32 s57, s57, s67\n\t"
            "v_writelane_b32 v236, s57, 63\n\t"
            "v_writelane_b32 v236, s59, m0\n\t"
            "v_cvt_pk_bf16_f32 v179, v182, v183\n\t"
            "v_mov_b64 v[180:181], 0\n\t"
            "v_mov_b64 v[182:183], 0\n\t"
            "s_setprio 3\n\t"
            "v_mfma_f32_16x16x32_bf16 v[192:195], v[238:241], v[176:179], 0\n\t"
            "s_lshr_b64 s[36:37], s[52:53], s58\n\t"
            "s_and_b32 s58, s36, 0xffff\n\t"
            "s_add_u32 s58, s58, s67\n\t"
            "v_mfma_f32_4x4x4_16b_bf16 v[196:199], v[178:179], v[178:179], v[196:199]\n\t"
            "ds_write_b128 v236, v[176:179]\n\t"
            "s_waitcnt lgkmcnt(8)\n\t"
            "v_smfmac_f32_16x16x64_bf16 v[180:183], v[172:175], v[160:167], v255\n\t"
            "s_setprio 1\n\t"
            "s_and_b32 s38, s52, 0xffff\n\t"
            "s_add_u32 s38, s38, s67\n\t"
            "ds_read_b128 v[172:175], v254 offset:448\n\t"
            "ds_read_b64 v[160:161], v253 offset:64\n\t"
            "ds_read_b64 v[162:163], v253 offset:160\n\t"
            "ds_read_b64 v[164:165], v253 offset:1808\n\t"
            "ds_read_b64 v[166:167], v253 offset:1904\n\t"
            "v_mov_b64 v[176:177], 0\n\t"
            "v_mov_b64 v[178:179], 0\n\t"
            "v_cvt_pk_bf16_f32 v184, v184, v185\n\t"
            "v_cvt_pk_bf16_f32 v185, v186, v187\n\t"
            "s_setprio 3\n\t"
            "v_smfmac_f32_16x16x64_bf16 v[176:179], v[168:171], v[152:159], v255\n\t"
            "ds_read_b128 v[168:171], v254 offset:384\n\t"
            "v_mfma_f32_4x4x4_16b_bf16 v[196:199], v[184:185], v[184:185], v[196:199]\n\t"
            "s_setprio 1\n\t"
            "ds_read_b64 v[152:153], v253 offset:56\n\t"
            "s_waitcnt lgkmcnt(14)\n\t"
            "ds_read_b64 v[154:155], v253 offset:152\n\t"
            "s_waitcnt lgkmcnt(14)\n\t"
            "ds_read_b64 v[156:157], v253 offset:1800\n\t"
            "s_waitcnt lgkmcnt(14)\n\t"
            "ds_read_b64 v[158:159], v253 offset:1896\n\t"
            "v_xor_b32 v237, 16, v236\n\t"
            "v_cvt_pk_bf16_f32 v186, v188, v189\n\t"
            "v_cvt_pk_bf16_f32 v187, v190, v191\n\t"
            "v_mov_b64 v[188:189], 0\n\t"
            "v_mov_b64 v[190:191], 0\n\t"
            "s_setprio 3\n\t"
            "v_mfma_f32_16x16x32_bf16 v[192:195], v[238:241], v[184:187], v[192:195]\n\t"
            "s_waitcnt lgkmcnt(14)\n\t"
            "ds_write_b128 v237, v[184:187]\n\t"
            "v_mfma_f32_4x4x4_16b_bf16 v[196:199], v[186:187], v[186:187], v[196:199]\n\t"
            "s_setprio 1\n\t"
            "v_mov_b64 v[184:185], 0\n\t"
            "v_mov_b64 v[186:187], 0\n\t"
            "v_cvt_pk_bf16_f32 v176, v176, v177\n\t"
            "v_cvt_pk_bf16_f32 v177, v178, v179\n\t"
            "v_cvt_pk_bf16_f32 v178, v180, v181\n\t"
            "v_cvt_pk_bf16_f32 v179, v182, v183\n\t"
            "s_setprio 3\n\t"
            "v_mfma_f32_4x4x4_16b_bf16 v[196:199], v[176:177], v[176:177], v[196:199]\n\t"
            "s_waitcnt lgkmcnt(14)\n\t"
            "ds_write_b128 v236, v[176:179] offset:128\n\t"
            "s_waitcnt lgkmcnt(6)\n\t"
            "v_smfmac_f32_16x16x64_bf16 v[184:187], v[168:171], v[144:151], v255\n\t"
            "ds_read_b128 v[168:171], v254 offset:512\n\t"
            "v_mfma_f32_16x16x32_bf16 v[192:195], v[238:241], v[176:179], v[192:195]\n\t"
            "ds_read_b64 v[144:145], v253 offset:72\n\t"
            "ds_read_b64 v[146:147], v253 offset:168\n\t"
            "s_waitcnt lgkmcnt(5)\n\t"
            "v_smfmac_f32_16x16x64_bf16 v[188:191], v[172:175], v[152:159], v255\n\t"
            "ds_read_b64 v[148:149], v253 offset:1816\n\t"
            "v_mfma_f32_4x4x4_16b_bf16 v[196:199], v[178:179], v[178:179], v[196:199]\n\t"
            "s_setprio 1\n\t"
            "ds_read_b64 v[150:151], v253 offset:1912\n\t"
            "ds_read_b128 v[172:175], v254 offset:576\n\t"
            "ds_read_b64 v[152:153], v253 offset:80\n\t"
            "ds_read_b64 v[154:155], v253 offset:176\n\t"
            "ds_read_b64 v[156:157], v253 offset:1824\n\t"
            "ds_read_b64 v[158:159], v253 offset:1920\n\t"
            "v_mov_b64 v[176:177], 0\n\t"
            "v_mov_b64 v[178:179], 0\n\t"
            "v_mov_b64 v[180:181], 0\n\t"
            "v_mov_b64 v[182:183], 0\n\t"
            "v_cvt_pk_bf16_f32 v184, v184, v185\n\t"
            "v_cvt_pk_bf16_f32 v185, v186, v187\n\t"
            "v_cvt_pk_bf16_f32 v186, v188, v189\n\t"
            "v_cvt_pk_bf16_f32 v187, v190, v191\n\t"
            "s_setprio 3\n\t"
            "v_mfma_f32_4x4x4_16b_bf16 v[196:199], v[184:185], v[184:185], v[196:199]\n\t"
            "ds_write_b128 v237, v[184:187] offset:128\n\t"
            "v_mfma_f32_16x16x32_bf16 v[192:195], v[238:241], v[184:187], v[192:195]\n\t"
            "v_mov_b64 v[184:185], 0\n\t"
            "v_mov_b64 v[188:189], 0\n\t"
            "s_waitcnt lgkmcnt(10)\n\t"
            "v_smfmac_f32_16x16x64_bf16 v[176:179], v[168:171], v[160:167], v255\n\t"
            "ds_read_b128 v[168:171], v254 offset:640\n\t"
            "v_mfma_f32_4x4x4_16b_bf16 v[196:199], v[186:187], v[186:187], v[196:199]\n\t"
            "ds_read_b64 v[160:161], v253 offset:88\n\t"
            "ds_read_b64 v[162:163], v253 offset:184\n\t"
            "ds_read_b64 v[164:165], v253 offset:1832\n\t"
            "s_waitcnt lgkmcnt(9)\n\t"
            "v_smfmac_f32_16x16x64_bf16 v[180:183], v[172:175], v[144:151], v255\n\t"
            "s_setprio 1\n\t"
            "ds_read_b64 v[166:167], v253 offset:1928\n\t"
            "ds_read_b128 v[172:175], v254 offset:704\n\t"
            "v_mov_b64 v[186:187], 0\n\t"
            "v_mov_b64 v[190:191], 0\n\t"
            "v_add_u32 v248, s67, v251\n\t"
            "v_writelane_b32 v248, s58, 63\n\t"
            "v_writelane_b32 v248, s38, m0\n\t"
            "v_and_b32 v249, s69, v249\n\t"
            "v_cvt_pk_bf16_f32 v176, v176, v177\n\t"
            "v_cvt_pk_bf16_f32 v177, v178, v179\n\t"
            "v_cvt_pk_bf16_f32 v178, v180, v181\n\t"
            "v_cvt_pk_bf16_f32 v179, v182, v183\n\t"
            "s_setprio 3\n\t"
            "v_mfma_f32_4x4x4_16b_bf16 v[196:199], v[176:177], v[176:177], v[196:199]\n\t"
            "ds_write_b128 v236, v[176:179] offset:256\n\t"
            "v_mfma_f32_16x16x32_bf16 v[192:195], v[238:241], v[176:179], v[192:195]\n\t"
            "v_add_u32 v180, s48, v245\n\t"
            "v_mov_b32 v234, 0\n\t"
            "v_mfma_f32_4x4x4_16b_bf16 v[196:199], v[178:179], v[178:179], v[196:199]\n\t"
            "ds_read_b128 v[176:179], v180 offset:18816\n\t"
            "s_waitcnt lgkmcnt(7)\n\t"
            "v_smfmac_f32_16x16x64_bf16 v[184:187], v[168:171], v[152:159], v255\n\t"
            "ds_read_b128 v[180:183], v180 offset:19840\n\t"
            "s_waitcnt lgkmcnt(3)\n\t"
            "v_smfmac_f32_16x16x64_bf16 v[188:191], v[172:175], v[160:167], v255\n\t"
            "s_setprio 1\n\t"
            "s_mov_b32 exec_hi, 0x3fffff\n\t"
            "s_add_u32 m0, s66, 0\n\t"
            "s_add_u32 s49, s79, 0\n\t"
            "buffer_load_dwordx4 v249, s[72:75], s49 offen lds\n\t"
            "s_mov_b32 exec_hi, -1\n\t"
            "s_mov_b32 exec_hi, 0x3fffff\n\t"
            "s_add_u32 m0, s66, 872\n\t"
            "s_add_u32 s49, s68, 196224\n\t"
            "buffer_load_dwordx4 v249, s[72:75], s49 offen lds\n\t"
            "s_mov_b32 exec_hi, -1\n\t"
            "s_mov_b32 exec_hi, 0x3fffff\n\t"
            "s_add_u32 m0, s66, 1744\n\t"
            "s_add_u32 s49, s68, 392448\n\t"
            "buffer_load_dwordx4 v249, s[72:75], s49 offen lds\n\t"
            "s_mov_b32 exec_hi, -1\n\t"
            "s_mov_b32 exec_hi, 0x3fffff\n\t"
            "s_add_u32 m0, s66, 2616\n\t"
            "s_add_u32 s49, s68, 588672\n\t"
            "buffer_load_dwordx4 v249, s[72:75], s49 offen lds\n\t"
            "s_mov_b32 exec_hi, -1\n\t"
            "s_mov_b32 exec_hi, 0x3fffff\n\t"
            "s_add_u32 m0, s66, 3488\n\t"
            "s_add_u32 s49, s68, 784896\n\t"
            "buffer_load_dwordx4 v249, s[72:75], s49 offen lds\n\t"
            "s_mov_b32 exec_hi, -1\n\t"
            "v_cvt_pk_bf16_f32 v184, v184, v185\n\t"
            "v_cvt_pk_bf16_f32 v185, v186, v187\n\t"
            "v_cvt_pk_bf16_f32 v186, v188, v189\n\t"
            "v_cvt_pk_bf16_f32 v187, v190, v191\n\t"
            "s_setprio 3\n\t"
            "v_mfma_f32_4x4x4_16b_bf16 v[196:199], v[184:185], v[184:185], v[196:199]\n\t"
            "s_mov_b32 exec_hi, 0x3fffff\n\t"
            "s_add_u32 m0, s66, 4360\n\t"
            "s_add_u32 s49, s68, 981120\n\t"
            "buffer_load_dwordx4 v249, s[72:75], s49 offen lds\n\t"
            "s_mov_b32 exec_hi, -1\n\t"
            "v_mfma_f32_4x4x4_16b_bf16 v[196:199], v[186:187], v[186:187], v[196:199]\n\t"
            "s_mov_b32 exec_hi, 0x3fffff\n\t"
            "s_add_u32 m0, s66, 5232\n\t"
            "s_add_u32 s49, s68, 1177344\n\t"
            "buffer_load_dwordx4 v249, s[72:75], s49 offen lds\n\t"
            "s_mov_b32 exec_hi, -1\n\t"
            "v_mfma_f32_16x16x32_bf16 v[192:195], v[238:241], v[184:187], v[192:195]\n\t"
            "s_setprio 1\n\t"
            "s_mov_b32 exec_hi, 0x3fffff\n\t"
            "s_add_u32 m0, s66, 6104\n\t"
            "s_add_u32 s49, s68, 1373568\n\t"
            "buffer_load_dwordx4 v249, s[72:75], s49 offen lds\n\t"
            "s_mov_b32 exec_hi, -1\n\t"
            "s_waitcnt lgkmcnt(1)\n\t"
            "v_add_f32 v176, v176, v178\n\t"
            "s_waitcnt lgkmcnt(0)\n\t"
            "v_add_f32 v180, v180, v182\n\t"
            "v_add_f32 v176, v176, v180\n\t"
            "v_cndmask_b32_e64 v193, v196, v197, s[60:61]\n\t"
            "v_cndmask_b32_e64 v193, v193, v198, s[62:63]\n\t"
            "v_cndmask_b32_e64 v193, v193, v199, s[64:65]\n\t"
            "v_add_f32 v177, v177, v179\n\t"
            "v_add_f32 v181, v181, v183\n\t"
            "v_mul_f32 v176, 0x3baaaaab, v176\n\t"
            "v_add_f32 v177, v177, v181\n\t"
            "v_mul_f32_e64 v176, v176, -v176\n\t"
            "v_fmac_f32 v176, 0x3baaaaab, v177\n\t"
            "v_max_f32 v176, 0, v176\n\t"
            "v_add_f32 v176, 0x3727c5ac, v176\n\t"
            "v_rsq_f32 v176, v176\n\t"
            "s_setprio 1\n\t"
            "ds_read_b128 v[196:199], v233 offset:6144\n\t"
            "ds_write_b64 v248, v[192:193] offset:18816\n\t"
            "ds_read_b128 v[192:195], v232 offset:6144\n\t"
            "s_mov_b32 exec_hi, 0x3fffff\n\t"
            "s_add_u32 m0, s66, 6976\n\t"
            "s_add_u32 s49, s68, 1569792\n\t"
            "buffer_load_dwordx4 v249, s[72:75], s49 offen lds\n\t"
            "s_mov_b32 exec_hi, -1\n\t"
            "ds_write_b128 v237, v[184:187] offset:256\n\t"
            "ds_write_b32 v246, v176\n\t"
            "ds_read_b64 v[236:237], v247\n\t"
            "ds_read_b64 v[248:249], v247 offset:8\n\t"
            "s_mov_b64 exec, 1\n\t"
            "ds_inc_u32 v255, v238 offset:7860\n\t"
            "s_mov_b64 exec, -1\n\t"
            "s_waitcnt lgkmcnt(5)\n\t"
            "v_mfma_f32_16x16x32_bf16 v[144:147], v[192:195], v[0:3], 0\n\t"
            "v_mfma_f32_16x16x32_bf16 v[144:147], v[196:199], v[4:7], v[144:147]\n\t"
            "v_mfma_f32_16x16x32_bf16 v[148:151], v[196:199], v[28:31], 0\n\t"
            "v_mfma_f32_16x16x32_bf16 v[148:151], v[192:195], v[24:27], v[148:151]\n\t"
            "v_mfma_f32_16x16x32_bf16 v[168:171], v[192:195], v[48:51], 0\n\t"
            "v_mfma_f32_16x16x32_bf16 v[168:171], v[196:199], v[52:55], v[168:171]\n\t"
            "s_waitcnt lgkmcnt(2)\n\t"
            "v_mul_f32 v200, v236, v200\n\t"
            "v_mul_f32 v201, v237, v201\n\t"
            "v_mfma_f32_16x16x32_bf16 v[172:175], v[196:199], v[76:79], 0\n\t"
            "v_mul_f32 v204, v236, v204\n\t"
            "v_mul_f32 v205, v237, v205\n\t"
            "v_mfma_f32_16x16x32_bf16 v[172:175], v[192:195], v[72:75], v[172:175]\n\t"
            "v_mul_f32 v208, v236, v208\n\t"
            "v_mul_f32 v209, v237, v209\n\t"
            "v_mfma_f32_16x16x32_bf16 v[152:155], v[192:195], v[96:99], 0\n\t"
            "v_mul_f32 v212, v236, v212\n\t"
            "v_mul_f32 v213, v237, v213\n\t"
            "v_mfma_f32_16x16x32_bf16 v[152:155], v[196:199], v[100:103], v[152:155]\n\t"
            "v_mul_f32 v216, v236, v216\n\t"
            "v_mul_f32 v217, v237, v217\n\t"
            "v_mfma_f32_16x16x32_bf16 v[156:159], v[196:199], v[124:127], 0\n\t"
            "ds_read_b128 v[196:199], v233 offset:6400\n\t"
            "v_mul_f32 v220, v236, v220\n\t"
            "v_mfma_f32_16x16x32_bf16 v[156:159], v[192:195], v[120:123], v[156:159]\n\t"
            "ds_read_b128 v[192:195], v232 offset:6400\n\t"
            "v_mul_f32 v221, v237, v221\n\t"
            "v_mfma_f32_16x16x32_bf16 v[144:147], v[224:227], v[8:11], v[144:147]\n\t"
            "ds_read_b64 v[236:237], v247 offset:64\n\t"
            "s_waitcnt lgkmcnt(4)\n\t"
            "v_mul_f32 v202, v248, v202\n\t"
            "v_mfma_f32_16x16x32_bf16 v[144:147], v[228:231], v[12:15], v[144:147]\n\t"
            "v_mul_f32 v203, v249, v203\n\t"
            "v_mul_f32 v206, v248, v206\n\t"
            "v_mfma_f32_16x16x32_bf16 v[148:151], v[228:231], v[36:39], v[148:151]\n\t"
            "v_mul_f32 v207, v249, v207\n\t"
            "v_mul_f32 v210, v248, v210\n\t"
            "v_mfma_f32_16x16x32_bf16 v[148:151], v[224:227], v[32:35], v[148:151]\n\t"
            "v_mul_f32 v211, v249, v211\n\t"
            "v_mul_f32 v214, v248, v214\n\t"
            "v_mfma_f32_16x16x32_bf16 v[168:171], v[224:227], v[56:59], v[168:171]\n\t"
            "v_mul_f32 v215, v249, v215\n\t"
            "v_mul_f32 v218, v248, v218\n\t"
            "v_mfma_f32_16x16x32_bf16 v[168:171], v[228:231], v[60:63], v[168:171]\n\t"
            "v_mul_f32 v219, v249, v219\n\t"
            "v_mul_f32 v222, v248, v222\n\t"
            "v_mfma_f32_16x16x32_bf16 v[172:175], v[228:231], v[84:87], v[172:175]\n\t"
            "v_mul_f32 v223, v249, v223\n\t"
            "v_cvt_pk_bf16_f32 v200, v200, v201\n\t"
            "v_mfma_f32_16x16x32_bf16 v[172:175], v[224:227], v[80:83], v[172:175]\n\t"
            "ds_read_b64 v[248:249], v247 offset:72\n\t"
            "v_cvt_pk_bf16_f32 v204, v204, v205\n\t"
            "v_mfma_f32_16x16x32_bf16 v[152:155], v[224:227], v[104:107], v[152:155]\n\t"
            "v_cvt_pk_bf16_f32 v208, v208, v209\n\t"
            "v_cvt_pk_bf16_f32 v212, v212, v213\n\t"
            "v_mfma_f32_16x16x32_bf16 v[152:155], v[228:231], v[108:111], v[152:155]\n\t"
            "v_cvt_pk_bf16_f32 v216, v216, v217\n\t"
            "v_cvt_pk_bf16_f32 v220, v220, v221\n\t"
            "v_mfma_f32_16x16x32_bf16 v[156:159], v[228:231], v[132:135], v[156:159]\n\t"
            "ds_read_b128 v[228:231], v233 offset:12288\n\t"
            "v_cvt_pk_bf16_f32 v201, v202, v203\n\t"
            "v_mfma_f32_16x16x32_bf16 v[156:159], v[224:227], v[128:131], v[156:159]\n\t"
            "ds_read_b128 v[224:227], v232 offset:12288\n\t"
            "v_cvt_pk_bf16_f32 v205, v206, v207\n\t"
            "s_waitcnt lgkmcnt(4)\n\t"
            "v_mfma_f32_16x16x32_bf16 v[144:147], v[192:195], v[16:19], v[144:147]\n\t"
            "v_cvt_pk_bf16_f32 v209, v210, v211\n\t"
            "v_cvt_pk_bf16_f32 v213, v214, v215\n\t"
            "v_mfma_f32_16x16x32_bf16 v[144:147], v[196:199], v[20:23], v[144:147]\n\t"
            "v_cvt_pk_bf16_f32 v217, v218, v219\n\t"
            "v_cvt_pk_bf16_f32 v221, v222, v223\n\t"
            "v_mfma_f32_16x16x32_bf16 v[148:151], v[196:199], v[44:47], v[148:151]\n\t"
            "v_mfma_f32_16x16x32_bf16 v[148:151], v[192:195], v[40:43], v[148:151]\n\t"
            "ds_read_b64 v[234:235], v234 offset:7856\n\t"
            "v_mfma_f32_16x16x32_bf16 v[168:171], v[192:195], v[64:67], v[168:171]\n\t"
            "s_waitcnt lgkmcnt(4)\n\t"
            "s_nop 0\n\t"
            "v_mul_f32 v144, v236, v144\n\t"
            "v_mfma_f32_16x16x32_bf16 v[168:171], v[196:199], v[68:71], v[168:171]\n\t"
            "v_mul_f32 v145, v237, v145\n\t"
            "s_waitcnt lgkmcnt(3)\n\t"
            "v_mul_f32 v146, v248, v146\n\t"
            "v_mfma_f32_16x16x32_bf16 v[172:175], v[196:199], v[92:95], v[172:175]\n\t"
            "v_mul_f32 v148, v236, v148\n\t"
            "v_mul_f32 v149, v237, v149\n\t"
            "v_mfma_f32_16x16x32_bf16 v[172:175], v[192:195], v[88:91], v[172:175]\n\t"
            "v_mul_f32 v147, v249, v147\n\t"
            "v_mul_f32 v150, v248, v150\n\t"
            "v_mfma_f32_16x16x32_bf16 v[152:155], v[192:195], v[112:115], v[152:155]\n\t"
            "v_mul_f32 v168, v236, v168\n\t"
            "v_mul_f32 v169, v237, v169\n\t"
            "v_mfma_f32_16x16x32_bf16 v[152:155], v[196:199], v[116:119], v[152:155]\n\t"
            "v_mul_f32 v151, v249, v151\n\t"
            "v_mul_f32 v170, v248, v170\n\t"
            "v_mfma_f32_16x16x32_bf16 v[156:159], v[196:199], v[140:143], v[156:159]\n\t"
            "ds_read_b128 v[196:199], v233 offset:12416\n\t"
            "v_mul_f32 v172, v236, v172\n\t"
            "v_mfma_f32_16x16x32_bf16 v[156:159], v[192:195], v[136:139], v[156:159]\n\t"
            "ds_read_b128 v[192:195], v232 offset:12416\n\t"
            "v_mul_f32 v173, v237, v173\n\t"
            "s_waitcnt lgkmcnt(3)\n\t"
            "v_mfma_f32_16x16x32_bf16 v[160:163], v[224:227], v[0:3], 0\n\t"
            "v_mul_f32 v152, v236, v152\n\t"
            "v_mul_f32 v153, v237, v153\n\t"
            "v_mfma_f32_16x16x32_bf16 v[160:163], v[228:231], v[4:7], v[160:163]\n\t"
            "v_mul_f32 v171, v249, v171\n\t"
            "v_mul_f32 v174, v248, v174\n\t"
            "v_mfma_f32_16x16x32_bf16 v[164:167], v[228:231], v[28:31], 0\n\t"
            "v_mul_f32 v156, v236, v156\n\t"
            "v_mul_f32 v157, v237, v157\n\t"
            "v_mfma_f32_16x16x32_bf16 v[164:167], v[224:227], v[24:27], v[164:167]\n\t"
            "ds_read_b64 v[236:237], v247 offset:128\n\t"
            "v_mul_f32 v175, v249, v175\n\t"
            "v_mfma_f32_16x16x32_bf16 v[176:179], v[224:227], v[48:51], 0\n\t"
            "v_mul_f32 v154, v248, v154\n\t"
            "v_mul_f32 v155, v249, v155\n\t"
            "v_mfma_f32_16x16x32_bf16 v[176:179], v[228:231], v[52:55], v[176:179]\n\t"
            "v_mul_f32 v158, v248, v158\n\t"
            "v_mul_f32 v159, v249, v159\n\t"
            "v_mfma_f32_16x16x32_bf16 v[180:183], v[228:231], v[76:79], 0\n\t"
            "ds_read_b64 v[248:249], v247 offset:136\n\t"
            "v_cvt_pk_bf16_f32 v202, v144, v145\n\t"
            "v_mfma_f32_16x16x32_bf16 v[180:183], v[224:227], v[72:75], v[180:183]\n\t"
            "s_waitcnt lgkmcnt(4)\n\t"
            "v_readfirstlane_b32 s77, v234\n\t"
            "v_readfirstlane_b32 s38, v235\n\t"
            "s_add_u32 s77, s77, s38\n\t"
            "s_cmp_ge_u32 s77, s44\n\t"
            "s_cbranch_scc0 .Lwc_spin%=\n\t"
            ".Lwc_go%=:\n\t"
            "v_mfma_f32_16x16x32_bf16 v[184:187], v[224:227], v[96:99], 0\n\t"
            "s_add_u32 s59, s59, 800\n\t"
            "v_cvt_pk_bf16_f32 v203, v146, v147\n\t"
            "v_cvt_pk_bf16_f32 v206, v148, v149\n\t"
            "v_mfma_f32_16x16x32_bf16 v[184:187], v[228:231], v[100:103], v[184:187]\n\t"
            "s_add_u32 s45, s46, s27\n\t"
            "s_mov_b32 s46, s47\n\t"
            "s_mov_b32 s47, s68\n\t"
            "v_mfma_f32_16x16x32_bf16 v[188:191], v[228:231], v[124:127], 0\n\t"
            "ds_read_b128 v[228:231], v233 offset:12544\n\t"
            "v_cvt_pk_bf16_f32 v207, v150, v151\n\t"
            "v_mfma_f32_16x16x32_bf16 v[188:191], v[224:227], v[120:123], v[188:191]\n\t"
            "ds_read_b128 v[224:227], v232 offset:12544\n\t"
            "v_cvt_pk_bf16_f32 v210, v168, v169\n\t"
            "s_waitcnt lgkmcnt(4)\n\t"
            "v_mfma_f32_16x16x32_bf16 v[160:163], v[192:195], v[8:11], v[160:163]\n\t"
            "v_cvt_pk_bf16_f32 v211, v170, v171\n\t"
            "v_cvt_pk_bf16_f32 v214, v172, v173\n\t"
            "v_mfma_f32_16x16x32_bf16 v[160:163], v[196:199], v[12:15], v[160:163]\n\t"
            "ds_read_b128 v[168:171], v254\n\t"
            "v_cvt_pk_bf16_f32 v215, v174, v175\n\t"
            "v_mfma_f32_16x16x32_bf16 v[164:167], v[196:199], v[36:39], v[164:167]\n\t"
            "ds_read_b128 v[172:175], v254 offset:64\n\t"
            "v_cvt_pk_bf16_f32 v218, v152, v153\n\t"
            "v_mfma_f32_16x16x32_bf16 v[164:167], v[192:195], v[32:35], v[164:167]\n\t"
            "v_cvt_pk_bf16_f32 v219, v154, v155\n\t"
            "v_cvt_pk_bf16_f32 v222, v156, v157\n\t"
            "v_mfma_f32_16x16x32_bf16 v[176:179], v[192:195], v[56:59], v[176:179]\n\t"
            "v_cvt_pk_bf16_f32 v223, v158, v159\n\t"
            "v_add_u32 v232, s67, v242\n\t"
            "v_mfma_f32_16x16x32_bf16 v[176:179], v[196:199], v[60:63], v[176:179]\n\t"
            "s_xor_b32 s48, s48, s71\n\t"
            "s_xor_b32 s67, s67, s71\n\t"
            "s_add_u32 s76, s76, 4\n\t"
            "s_add_u32 s44, s44, 8\n\t"
            "v_mfma_f32_16x16x32_bf16 v[180:183], v[196:199], v[84:87], v[180:183]\n\t"
            "s_lshl_b32 s56, s56, 2\n\t"
            "s_lshr_b32 s49, s26, 30\n\t"
            "s_or_b32 s56, s56, s49\n\t"
            "v_mfma_f32_16x16x32_bf16 v[180:183], v[192:195], v[80:83], v[180:183]\n\t"
            "ds_write_b128 v243, v[200:203]\n\t"
            "v_mfma_f32_16x16x32_bf16 v[184:187], v[192:195], v[104:107], v[184:187]\n\t"
            "v_xor_b32 v233, 64, v232\n\t"
            "v_mfma_f32_16x16x32_bf16 v[184:187], v[196:199], v[108:111], v[184:187]\n\t"
            "ds_write_b128 v243, v[204:207] offset:1536\n\t"
            "v_mfma_f32_16x16x32_bf16 v[188:191], v[196:199], v[132:135], v[188:191]\n\t"
            "ds_read_b128 v[196:199], v233\n\t"
            "v_mfma_f32_16x16x32_bf16 v[188:191], v[192:195], v[128:131], v[188:191]\n\t"
            "ds_read_b128 v[192:195], v232\n\t"
            "ds_write_b128 v243, v[208:211] offset:3072\n\t"
            "s_waitcnt lgkmcnt(7)\n\t"
            "v_mfma_f32_16x16x32_bf16 v[160:163], v[224:227], v[16:19], v[160:163]\n\t"
            "v_mfma_f32_16x16x32_bf16 v[160:163], v[228:231], v[20:23], v[160:163]\n\t"
            "ds_write_b128 v243, v[212:215] offset:4608\n\t"
            "v_mfma_f32_16x16x32_bf16 v[164:167], v[228:231], v[44:47], v[164:167]\n\t"
            "v_mfma_f32_16x16x32_bf16 v[164:167], v[224:227], v[40:43], v[164:167]\n\t"
            "ds_write_b128 v243, v[216:219] offset:6144\n\t"
            "v_mfma_f32_16x16x32_bf16 v[176:179], v[224:227], v[64:67], v[176:179]\n\t"
            "v_mfma_f32_16x16x32_bf16 v[176:179], v[228:231], v[68:71], v[176:179]\n\t"
            "ds_write_b128 v243, v[220:223] offset:7680\n\t"
            "v_mfma_f32_16x16x32_bf16 v[180:183], v[228:231], v[92:95], v[180:183]\n\t"
            "v_mul_f32 v160, v236, v160\n\t"
            "v_mul_f32 v161, v237, v161\n\t"
            "v_mfma_f32_16x16x32_bf16 v[180:183], v[224:227], v[88:91], v[180:183]\n\t"
            "v_mul_f32 v164, v236, v164\n\t"
            "v_mul_f32 v165, v237, v165\n\t"
            "v_mfma_f32_16x16x32_bf16 v[184:187], v[224:227], v[112:115], v[184:187]\n\t"
            "v_mul_f32 v176, v236, v176\n\t"
            "v_mul_f32 v177, v237, v177\n\t"
            "v_mfma_f32_16x16x32_bf16 v[184:187], v[228:231], v[116:119], v[184:187]\n\t"
            "v_mul_f32 v162, v248, v162\n\t"
            "v_mul_f32 v163, v249, v163\n\t"
            "v_mfma_f32_16x16x32_bf16 v[188:191], v[228:231], v[140:143], v[188:191]\n\t"
            "ds_read_b128 v[228:231], v233 offset:128\n\t"
            "v_mul_f32 v180, v236, v180\n\t"
            "v_mfma_f32_16x16x32_bf16 v[188:191], v[224:227], v[136:139], v[188:191]\n\t"
            "ds_read_b128 v[224:227], v232 offset:128\n\t"
            "v_mul_f32 v181, v237, v181\n\t"
            "s_waitcnt lgkmcnt(6)\n\t"
            "v_mfma_f32_16x16x32_bf16 v[200:203], v[192:195], v[0:3], 0\n\t"
            "v_mul_f32 v184, v236, v184\n\t"
            "v_mul_f32 v185, v237, v185\n\t"
            "v_mfma_f32_16x16x32_bf16 v[200:203], v[196:199], v[4:7], v[200:203]\n\t"
            "v_cvt_pk_bf16_f32 v160, v160, v161\n\t"
            "v_mul_f32 v166, v248, v166\n\t"
            "v_mfma_f32_16x16x32_bf16 v[204:207], v[196:199], v[28:31], 0\n\t"
            "v_mul_f32 v188, v236, v188\n\t"
            "v_mul_f32 v189, v237, v189\n\t"
            "v_mfma_f32_16x16x32_bf16 v[204:207], v[192:195], v[24:27], v[204:207]\n\t"
            "v_mul_f32 v167, v249, v167\n\t"
            "v_cvt_pk_bf16_f32 v164, v164, v165\n\t"
            "v_mfma_f32_16x16x32_bf16 v[208:211], v[192:195], v[48:51], 0\n\t"
            "v_mul_f32 v178, v248, v178\n\t"
            "v_mul_f32 v179, v249, v179\n\t"
            "v_mfma_f32_16x16x32_bf16 v[208:211], v[196:199], v[52:55], v[208:211]\n\t"
            "v_cvt_pk_bf16_f32 v176, v176, v177\n\t"
            "v_mul_f32 v182, v248, v182\n\t"
            "v_mfma_f32_16x16x32_bf16 v[212:215], v[196:199], v[76:79], 0\n\t"
            "v_mul_f32 v183, v249, v183\n\t"
            "v_cvt_pk_bf16_f32 v180, v180, v181\n\t"
            "v_mfma_f32_16x16x32_bf16 v[212:215], v[192:195], v[72:75], v[212:215]\n\t"
            "v_mul_f32 v186, v248, v186\n\t"
            "v_mul_f32 v187, v249, v187\n\t"
            "v_mfma_f32_16x16x32_bf16 v[216:219], v[192:195], v[96:99], 0\n\t"
            "v_cvt_pk_bf16_f32 v184, v184, v185\n\t"
            "v_mul_f32 v190, v248, v190\n\t"
            "v_mfma_f32_16x16x32_bf16 v[216:219], v[196:199], v[100:103], v[216:219]\n\t"
            "v_mul_f32 v191, v249, v191\n\t"
            "v_cvt_pk_bf16_f32 v188, v188, v189\n\t"
            "v_mfma_f32_16x16x32_bf16 v[220:223], v[196:199], v[124:127], 0\n\t"
            "v_cvt_pk_bf16_f32 v161, v162, v163\n\t"
            "v_cvt_pk_bf16_f32 v165, v166, v167\n\t"
            "v_mfma_f32_16x16x32_bf16 v[220:223], v[192:195], v[120:123], v[220:223]\n\t"
            "v_cvt_pk_bf16_f32 v177, v178, v179\n\t"
            "v_cvt_pk_bf16_f32 v181, v182, v183\n\t"
            "s_waitcnt lgkmcnt(0)\n\t"
            "v_mfma_f32_16x16x32_bf16 v[200:203], v[224:227], v[8:11], v[200:203]\n\t"
            "v_cvt_pk_bf16_f32 v185, v186, v187\n\t"
            "v_cvt_pk_bf16_f32 v189, v190, v191\n\t"
            "v_mfma_f32_16x16x32_bf16 v[200:203], v[228:231], v[12:15], v[200:203]\n\t"
            "ds_write_b64 v244, v[160:161]\n\t"
            "v_mfma_f32_16x16x32_bf16 v[204:207], v[228:231], v[36:39], v[204:207]\n\t"
            "v_mfma_f32_16x16x32_bf16 v[204:207], v[224:227], v[32:35], v[204:207]\n\t"
            "ds_write_b64 v244, v[164:165] offset:1536\n\t"
            "v_mfma_f32_16x16x32_bf16 v[208:211], v[224:227], v[56:59], v[208:211]\n\t"
            "v_mfma_f32_16x16x32_bf16 v[208:211], v[228:231], v[60:63], v[208:211]\n\t"
            "ds_write_b64 v244, v[176:177] offset:3072\n\t"
            "v_mfma_f32_16x16x32_bf16 v[212:215], v[228:231], v[84:87], v[212:215]\n\t"
            "v_mfma_f32_16x16x32_bf16 v[212:215], v[224:227], v[80:83], v[212:215]\n\t"
            "ds_write_b64 v244, v[180:181] offset:4608\n\t"
            "v_mfma_f32_16x16x32_bf16 v[216:219], v[224:227], v[104:107], v[216:219]\n\t"
            "v_mfma_f32_16x16x32_bf16 v[216:219], v[228:231], v[108:111], v[216:219]\n\t"
            "ds_write_b64 v244, v[184:185] offset:6144\n\t"
            "v_mfma_f32_16x16x32_bf16 v[220:223], v[228:231], v[132:135], v[220:223]\n\t"
            "ds_read_b128 v[228:231], v233 offset:256\n\t"
            "v_mfma_f32_16x16x32_bf16 v[220:223], v[224:227], v[128:131], v[220:223]\n\t"
            "ds_read_b128 v[224:227], v232 offset:256\n\t"
            "ds_write_b64 v244, v[188:189] offset:7680\n\t"
            "s_waitcnt lgkmcnt(0)\n\t"
            "s_barrier\n\t"
            "s_waitcnt vmcnt(0)\n\t"
            "ds_read_b64 v[144:145], v253\n\t"
            "ds_read_b64 v[146:147], v253 offset:96\n\t"
            "ds_read_b64 v[148:149], v253 offset:1744\n\t"
            "ds_read_b64 v[150:151], v253 offset:1840\n\t"
            "ds_read_b64 v[152:153], v253 offset:8\n\t"
            "ds_read_b64 v[154:155], v253 offset:104\n\t"
            "ds_read_b64 v[156:157], v253 offset:1752\n\t"
            "ds_read_b64 v[158:159], v253 offset:1848\n\t"
            "ds_read_b64 v[160:161], v253 offset:16\n\t"
            "ds_read_b64 v[162:163], v253 offset:112\n\t"
            "ds_read_b64 v[164:165], v253 offset:1760\n\t"
            "ds_read_b64 v[166:167], v253 offset:1856\n\t"
            "s_cmp_lt_u32 s46, 0x7000000\n\t"
            "s_cbranch_scc1 .Lwc_loop%=\n\t"
            "s_branch .Lwc_end%=\n\t"
            ".Lwc_spin%=:\n\t"
            "s_cmp_eq_u32 s83, 0\n\t"
            "s_cbranch_scc1 .Lwc_go%=\n\t"
            "s_sub_u32 s83, s83, 1\n\t"
            "s_sleep 2\n\t"
            "v_mov_b32 v234, 0\n\t"
            "ds_read_b64 v[234:235], v234 offset:7856\n\t"
            "s_waitcnt lgkmcnt(0)\n\t"
            "v_readfirstlane_b32 s77, v234\n\t"
            "v_readfirstlane_b32 s38, v235\n\t"
            "s_add_u32 s77, s77, s38\n\t"
            "s_cmp_ge_u32 s77, s44\n\t"
            "s_cbranch_scc0 .Lwc_spin%=\n\t"
            "s_branch .Lwc_go%=\n\t"
            ".Lwc_end%=:\n\t"
            "s_waitcnt lgkmcnt(0)\n\t"
            "s_nop 7\n\t"
            "s_barrier\n\t"
            : "+{v232}"(q0), "+{v233}"(q1), "+{v234}"(q2), "+{s44}"(s44), B_SIO
            : "{v242}"(pE_), "{v243}"(aVAw_), "{v244}"(aVBw_), "{v245}"(pST_), "{v246}"(aRSw_), "{v247}"(aRSr_), "{v250}"(pXw_), "{v251}"(pSTw_), "{v252}"(aCA_), "{v253}"(aB_), "{v254}"(aA_), "{v255}"(idx_), B_SIN, "{s[80:81]}"(mrow), "{s82}"(touch)
            : "v144", "v145", "v146", "v147", "v148", "v149", "v150", "v151", "v152", "v153", "v154", "v155", "v156", "v157", "v158", "v159", "v160", "v161", "v162", "v163", "v164", "v165", "v166", "v167", "v168", "v169", "v170", "v171", "v172", "v173", "v174", "v175", "v176", "v177", "v178", "v179", "v180", "v181", "v182", "v183", "v184", "v185", "v186", "v187", "v188", "v189", "v190", "v191", "v192", "v193", "v194", "v195", "v196", "v197", "v198", "v199", "v236", "v237", "v248", "v249", "v0", "v1", "v2", "v3", "v4", "v5", "v6", "v7", "v8", "v9", "v10", "v11", "v12", "v13", "v14", "v15", "v16", "v17", "v18", "v19", "v20", "v21", "v22", "v23", "v24", "v25", "v26", "v27", "v28", "v29", "v30", "v31", "v32", "v33", "v34", "v35", "v36", "v37", "v38", "v39", "v40", "v41", "v42", "v43", "v44", "v45", "v46", "v47", "v48", "v49", "v50", "v51", "v52", "v53", "v54", "v55", "v56", "v57", "v58", "v59", "v60", "v61", "v62", "v63", "v64", "v65", "v66", "v67", "v68", "v69", "v70", "v71", "v72", "v73", "v74", "v75", "v76", "v77", "v78", "v79", "v80", "v81", "v82", "v83", "v84", "v85", "v86", "v87", "v88", "v89", "v90", "v91", "v92", "v93", "v94", "v95", "v96", "v97", "v98", "v99", "v100", "v101", "v102", "v103", "v104", "v105", "v106", "v107", "v108", "v109", "v110", "v111", "v112", "v113", "v114", "v115", "v116", "v117", "v118", "v119", "v120", "v121", "v122", "v123", "v124", "v125", "v126", "v127", "v128", "v129", "v130", "v131", "v132", "v133", "v134", "v135", "v136", "v137", "v138", "v139", "v140", "v141", "v142", "v143", "v235", "v200", "v201", "v202", "v203", "v204", "v205", "v206", "v207", "v208", "v209", "v210", "v211", "v212", "v213", "v214", "v215", "v216", "v217", "v218", "v219", "v220", "v221", "v222", "v223", "v224", "v225", "v226", "v227", "v228", "v229", "v230", "v231", B_SCLOB);
        }
        __builtin_amdgcn_endpgm();
    } else {
        LANE_SETUP
        unsigned t45, t56;
        u32x4 xl[9];
        {
            const unsigned oE_ = ldsb + fE + (O_OFF - X_OFF), oO_ = oE_ ^ 64;
            unsigned l16_ = lane * 16;
            unsigned pr[3];
#pragma unroll
            for (int k = 0; k < 3; ++k) {
                const int c8 = 12 * (w - 2) + 4 * k + g;
                const int ws = c8 / 6, r6 = c8 - 6 * ws;
                pr[k] = l15 * 384 + (r6 >> 1) * 128 + (((2 * ws + (r6 & 1)) ^ sw) << 4);
            }
            const unsigned pR0_ = pr[0], pR1_ = pr[1], pR2_ = pr[2];
            B_SCALARS
            asm volatile(
            "s_waitcnt lgkmcnt(0)\n\t"
            "s_mov_b32 s83, 0x10000\n\t"
            "s_nop 7\n\t"
            "s_barrier\n\t"
            "s_sleep 16\n\t"
            "ds_read_b32 v249, v252 offset:12\n\t"
            "s_waitcnt lgkmcnt(0)\n\t"
            "v_and_b32 v249, s69, v249\n\t"
            "s_mov_b32 exec_hi, 0x3fffff\n\t"
            "s_add_u32 m0, s66, 0\n\t"
            "s_add_u32 s49, s79, 0\n\t"
            "buffer_load_dwordx4 v249, s[72:75], s49 offen lds\n\t"
            "s_mov_b32 exec_hi, -1\n\t"
            "s_mov_b32 exec_hi, 0x3fffff\n\t"
            "s_add_u32 m0, s66, 872\n\t"
            "s_add_u32 s49, s47, 196224\n\t"
            "buffer_load_dwordx4 v249, s[72:75], s49 offen lds\n\t"
            "s_mov_b32 exec_hi, -1\n\t"
            "s_mov_b32 exec_hi, 0x3fffff\n\t"
            "s_add_u32 m0, s66, 1744\n\t"
            "s_add_u32 s49, s47, 392448\n\t"
            "buffer_load_dwordx4 v249, s[72:75], s49 offen lds\n\t"
            "s_mov_b32 exec_hi, -1\n\t"
            "s_mov_b32 exec_hi, 0x3fffff\n\t"
            "s_add_u32 m0, s66, 2616\n\t"
            "s_add_u32 s49, s47, 588672\n\t"
            "buffer_load_dwordx4 v249, s[72:75], s49 offen lds\n\t"
            "s_mov_b32 exec_hi, -1\n\t"
            "s_mov_b32 exec_hi, 0x3fffff\n\t"
            "s_add_u32 m0, s66, 3488\n\t"
            "s_add_u32 s49, s47, 784896\n\t"
            "buffer_load_dwordx4 v249, s[72:75], s49 offen lds\n\t"
            "s_mov_b32 exec_hi, -1\n\t"
            "s_mov_b32 exec_hi, 0x3fffff\n\t"
            "s_add_u32 m0, s66, 4360\n\t"
            "s_add_u32 s49, s47, 981120\n\t"
            "buffer_load_dwordx4 v249, s[72:75], s49 offen lds\n\t"
            "s_mov_b32 exec_hi, -1\n\t"
            "s_mov_b32 exec_hi, 0x3fffff\n\t"
            "s_add_u32 m0, s66, 5232\n\t"
            "s_add_u32 s49, s47, 1177344\n\t"
            "buffer_load_dwordx4 v249, s[72:75], s49 offen lds\n\t"
            "s_mov_b32 exec_hi, -1\n\t"
            "s_mov_b32 exec_hi, 0x3fffff\n\t"
            "s_add_u32 m0, s66, 6104\n\t"
            "s_add_u32 s49, s47, 1373568\n\t"
            "buffer_load_dwordx4 v249, s[72:75], s49 offen lds\n\t"
            "s_mov_b32 exec_hi, -1\n\t"
            "s_mov_b32 exec_hi, 0x3fffff\n\t"
            "s_add_u32 m0, s66, 6976\n\t"
            "s_add_u32 s49, s47, 1569792\n\t"
            "buffer_load_dwordx4 v249, s[72:75], s49 offen lds\n\t"
            "s_mov_b32 exec_hi, -1\n\t"
            "s_setprio 1\n\t"
            "ds_read_b128 v[168:171], v254\n\t"
            "ds_read_b128 v[172:175], v254 offset:64\n\t"
            "s_waitcnt vmcnt(0)\n\t"
            "ds_read_b64 v[144:145], v253\n\t"
            "ds_read_b64 v[146:147], v253 offset:96\n\t"
            "ds_read_b64 v[148:149], v253 offset:1744\n\t"
            "ds_read_b64 v[150:151], v253 offset:1840\n\t"
            "ds_read_b64 v[152:153], v253 offset:8\n\t"
            "ds_read_b64 v[154:155], v253 offset:104\n\t"
            "ds_read_b64 v[156:157], v253 offset:1752\n\t"
            "ds_read_b64 v[158:159], v253 offset:1848\n\t"
            "ds_read_b64 v[160:161], v253 offset:16\n\t"
            "ds_read_b64 v[162:163], v253 offset:112\n\t"
            "ds_read_b64 v[164:165], v253 offset:1760\n\t"
            "ds_read_b64 v[166:167], v253 offset:1856\n\t"
            "v_mov_b64 v[176:177], 0\n\t"
            "v_mov_b64 v[178:179], 0\n\t"
            "v_mov_b64 v[180:181], 0\n\t"
            "v_mov_b64 v[182:183], 0\n\t"
            "v_mov_b32 v249, s76\n\t"
            "v_mov_b64 v[184:185], 0\n\t"
            "v_mov_b64 v[186:187], 0\n\t"
            "v_mov_b64 v[188:189], 0\n\t"
            "v_mov_b64 v[190:191], 0\n\t"
            "v_add_u32 v236, s67, v250\n\t"
            "v_add_u32 v248, s67, v251\n\t"
            "s_nop 0\n\t"
            "ds_read_b32 v249, v249\n\t"
            "s_waitcnt lgkmcnt(9)\n\t"
            "v_smfmac_f32_16x16x64_bf16 v[176:179], v[168:171], v[144:151], v255\n\t"
            "ds_read_b128 v[168:171], v254 offset:128\n\t"
            "s_waitcnt lgkmcnt(6)\n\t"
            "v_smfmac_f32_16x16x64_bf16 v[180:183], v[172:175], v[152:159], v255\n\t"
            "ds_read_b64 v[144:145], v253 offset:24\n\t"
            "ds_read_b64 v[146:147], v253 offset:120\n\t"
            "ds_read_b64 v[148:149], v253 offset:1768\n\t"
            "ds_read_b64 v[150:151], v253 offset:1864\n\t"
            "ds_read_b128 v[172:175], v254 offset:192\n\t"
            "ds_read_b64 v[152:153], v253 offset:32\n\t"
            "ds_read_b64 v[154:155], v253 offset:128\n\t"
            "ds_read_b64 v[156:157], v253 offset:1776\n\t"
            "ds_read_b64 v[158:159], v253 offset:1872\n\t"
            "v_cvt_pk_bf16_f32 v176, v176, v177\n\t"
            "v_cvt_pk_bf16_f32 v177, v178, v179\n\t"
            "v_cvt_pk_bf16_f32 v178, v180, v181\n\t"
            "v_cvt_pk_bf16_f32 v179, v182, v183\n\t"
            "v_mfma_f32_4x4x4_16b_bf16 v[196:199], v[176:177], v[176:177], 0\n\t"
            "v_mov_b64 v[180:181], 0\n\t"
            "v_mfma_f32_16x16x32_bf16 v[192:195], v[238:241], v[176:179], 0\n\t"
            "s_waitcnt lgkmcnt(10)\n\t"
            "v_readfirstlane_b32 s26, v249\n\t"
            "v_mov_b64 v[182:183], 0\n\t"
            "v_mfma_f32_4x4x4_16b_bf16 v[196:199], v[178:179], v[178:179], v[196:199]\n\t"
            "ds_read_b32 v249, v252 offset:12\n\t"
            "s_and_b32 s68, s26, 0x7ffffff\n\t"
            "s_add_u32 s68, s68, s78\n\t"
            "s_and_b32 s69, s26, 0x38000000\n\t"
            "s_or_b32 s69, s69, s39\n\t"
            "s_bitcmp1_b32 s26, 27\n\t"
            "s_cselect_b32 s79, 2.0, s68\n\t"
            "s_and_b32 s57, s56, 3\n\t"
            "s_lshl_b32 s58, s57, 3\n\t"
            "s_lshr_b32 m0, s54, s58\n\t"
            "s_lshl_b32 s58, s57, 4\n\t"
            "s_lshr_b64 s[36:37], s[50:51], s58\n\t"
            "s_and_b32 s57, s36, 0xffff\n\t"
            "s_add_u32 s57, s57, s67\n\t"
            "v_writelane_b32 v236, s57, 63\n\t"
            "v_writelane_b32 v236, s59, m0\n\t"
            "v_xor_b32 v237, 16, v236\n\t"
            "s_lshr_b64 s[36:37], s[52:53], s58\n\t"
            "s_and_b32 s58, s36, 0xffff\n\t"
            "s_add_u32 s58, s58, s67\n\t"
            "ds_write_b128 v236, v[176:179]\n\t"
            "s_waitcnt lgkmcnt(11)\n\t"
            "v_smfmac_f32_16x16x64_bf16 v[184:187], v[168:171], v[160:167], v255\n\t"
            "s_and_b32 s38, s52, 0xffff\n\t"
            "s_add_u32 s38, s38, s67\n\t"
            "ds_read_b128 v[168:171], v254 offset:256\n\t"
            "s_waitcnt lgkmcnt(7)\n\t"
            "v_smfmac_f32_16x16x64_bf16 v[188:191], v[172:175], v[144:151], v255\n\t"
            "ds_read_b64 v[160:161], v253 offset:40\n\t"
            "ds_read_b64 v[162:163], v253 offset:136\n\t"
            "ds_read_b64 v[164:165], v253 offset:1784\n\t"
            "ds_read_b64 v[166:167], v253 offset:1880\n\t"
            "ds_read_b128 v[172:175], v254 offset:320\n\t"
            "ds_read_b64 v[144:145], v253 offset:48\n\t"
            "ds_read_b64 v[146:147], v253 offset:144\n\t"
            "ds_read_b64 v[148:149], v253 offset:1792\n\t"
            "s_waitcnt lgkmcnt(14)\n\t"
            "ds_read_b64 v[150:151], v253 offset:1888\n\t"
            "v_mov_b64 v[176:177], 0\n\t"
            "v_mov_b64 v[178:179], 0\n\t"
            "v_cvt_pk_bf16_f32 v184, v184, v185\n\t"
            "v_cvt_pk_bf16_f32 v185, v186, v187\n\t"
            "v_cvt_pk_bf16_f32 v186, v188, v189\n\t"
            "v_cvt_pk_bf16_f32 v187, v190, v191\n\t"
            "v_mfma_f32_4x4x4_16b_bf16 v[196:199], v[184:185], v[184:185], v[196:199]\n\t"
            "s_waitcnt lgkmcnt(14)\n\t"
            "ds_write_b128 v237, v[184:187]\n\t"
            "v_mfma_f32_16x16x32_bf16 v[192:195], v[238:241], v[184:187], v[192:195]\n\t"
            "v_mov_b64 v[184:185], 0\n\t"
            "v_mov_b64 v[188:189], 0\n\t"
            "v_mfma_f32_4x4x4_16b_bf16 v[196:199], v[186:187], v[186:187], v[196:199]\n\t"
            "v_mov_b64 v[186:187], 0\n\t"
            "v_mov_b64 v[190:191], 0\n\t"
            "v_writelane_b32 v248, s58, 63\n\t"
            "v_writelane_b32 v248, s38, m0\n\t"
            "s_waitcnt lgkmcnt(12)\n\t"
            "v_and_b32 v249, s69, v249\n\t"
            "s_waitcnt lgkmcnt(10)\n\t"
            "v_smfmac_f32_16x16x64_bf16 v[176:179], v[168:171], v[152:159], v255\n\t"
            "ds_read_b128 v[168:171], v254 offset:384\n\t"
            "ds_read_b64 v[152:153], v253 offset:56\n\t"
            "ds_read_b64 v[154:155], v253 offset:152\n\t"
            "ds_read_b64 v[156:157], v253 offset:1800\n\t"
            "ds_read_b64 v[158:159], v253 offset:1896\n\t"
            "s_waitcnt lgkmcnt(10)\n\t"
            "v_smfmac_f32_16x16x64_bf16 v[180:183], v[172:175], v[160:167], v255\n\t"
            "ds_read_b128 v[172:175], v254 offset:448\n\t"
            "ds_read_b64 v[160:161], v253 offset:64\n\t"
            "ds_read_b64 v[162:163], v253 offset:160\n\t"
            "ds_read_b64 v[164:165], v253 offset:1808\n\t"
            "ds_read_b64 v[166:167], v253 offset:1904\n\t"
            "v_cvt_pk_bf16_f32 v176, v176, v177\n\t"
            "v_cvt_pk_bf16_f32 v177, v178, v179\n\t"
            "s_nop 0\n\t"
            "v_cvt_pk_bf16_f32 v178, v180, v181\n\t"
            "v_cvt_pk_bf16_f32 v179, v182, v183\n\t"
            "v_mfma_f32_4x4x4_16b_bf16 v[196:199], v[176:177], v[176:177], v[196:199]\n\t"
            "s_waitcnt lgkmcnt(14)\n\t"
            "ds_write_b128 v236, v[176:179] offset:128\n\t"
            "v_mfma_f32_16x16x32_bf16 v[192:195], v[238:241], v[176:179], v[192:195]\n\t"
            "v_mov_b64 v[176:177], 0\n\t"
            "v_mov_b64 v[180:181], 0\n\t"
            "v_mfma_f32_4x4x4_16b_bf16 v[196:199], v[178:179], v[178:179], v[196:199]\n\t"
            "v_mov_b64 v[178:179], 0\n\t"
            "v_mov_b64 v[182:183], 0\n\t"
            "s_waitcnt lgkmcnt(10)\n\t"
            "v_smfmac_f32_16x16x64_bf16 v[184:187], v[168:171], v[144:151], v255\n\t"
            "ds_read_b128 v[168:171], v254 offset:512\n\t"
            "ds_read_b64 v[144:145], v253 offset:72\n\t"
            "ds_read_b64 v[146:147], v253 offset:168\n\t"
            "ds_read_b64 v[148:149], v253 offset:1816\n\t"
            "ds_read_b64 v[150:151], v253 offset:1912\n\t"
            "s_waitcnt lgkmcnt(10)\n\t"
            "v_smfmac_f32_16x16x64_bf16 v[188:191], v[172:175], v[152:159], v255\n\t"
            "ds_read_b128 v[172:175], v254 offset:576\n\t"
            "ds_read_b64 v[152:153], v253 offset:80\n\t"
            "ds_read_b64 v[154:155], v253 offset:176\n\t"
            "ds_read_b64 v[156:157], v253 offset:1824\n\t"
            "ds_read_b64 v[158:159], v253 offset:1920\n\t"
            "v_cvt_pk_bf16_f32 v184, v184, v185\n\t"
            "v_cvt_pk_bf16_f32 v185, v186, v187\n\t"
            "s_nop 0\n\t"
            "v_cvt_pk_bf16_f32 v186, v188, v189\n\t"
            "v_cvt_pk_bf16_f32 v187, v190, v191\n\t"
            "v_mfma_f32_4x4x4_16b_bf16 v[196:199], v[184:185], v[184:185], v[196:199]\n\t"
            "s_waitcnt lgkmcnt(14)\n\t"
            "ds_write_b128 v237, v[184:187] offset:128\n\t"
            "v_mfma_f32_16x16x32_bf16 v[192:195], v[238:241], v[184:187], v[192:195]\n\t"
            "v_mov_b64 v[184:185], 0\n\t"
            "v_mov_b64 v[188:189], 0\n\t"
            "v_mfma_f32_4x4x4_16b_bf16 v[196:199], v[186:187], v[186:187], v[196:199]\n\t"
            "v_mov_b64 v[186:187], 0\n\t"
            "v_mov_b64 v[190:191], 0\n\t"
            "s_waitcnt lgkmcnt(10)\n\t"
            "v_smfmac_f32_16x16x64_bf16 v[176:179], v[168:171], v[160:167], v255\n\t"
            "ds_read_b128 v[168:171], v254 offset:640\n\t"
            "ds_read_b64 v[160:161], v253 offset:88\n\t"
            "ds_read_b64 v[162:163], v253 offset:184\n\t"
            "ds_read_b64 v[164:165], v253 offset:1832\n\t"
            "ds_read_b64 v[166:167], v253 offset:1928\n\t"
            "s_waitcnt lgkmcnt(10)\n\t"
            "v_smfmac_f32_16x16x64_bf16 v[180:183], v[172:175], v[144:151], v255\n\t"
            "ds_read_b128 v[172:175], v254 offset:704\n\t"
            "v_cvt_pk_bf16_f32 v176, v176, v177\n\t"
            "v_cvt_pk_bf16_f32 v177, v178, v179\n\t"
            "s_nop 1\n\t"
            "v_mfma_f32_4x4x4_16b_bf16 v[196:199], v[176:177], v[176:177], v[196:199]\n\t"
            "s_nop 1\n\t"
            "v_cvt_pk_bf16_f32 v178, v180, v181\n\t"
            "v_cvt_pk_bf16_f32 v179, v182, v183\n\t"
            "s_nop 1\n\t"
            "v_mfma_f32_4x4x4_16b_bf16 v[196:199], v[178:179], v[178:179], v[196:199]\n\t"
            "ds_write_b128 v236, v[176:179] offset:256\n\t"
            "v_mfma_f32_16x16x32_bf16 v[192:195], v[238:241], v[176:179], v[192:195]\n\t"
            "v_add_u32 v236, s48, v244\n\t"
            "ds_read_b128 v[200:203], v236\n\t"
            "ds_read_b128 v[212:215], v236 offset:6144\n\t"
            "ds_read_b128 v[224:227], v236 offset:12288\n\t"
            "s_waitcnt lgkmcnt(9)\n\t"
            "v_smfmac_f32_16x16x64_bf16 v[184:187], v[168:171], v[152:159], v255\n\t"
            "s_waitcnt lgkmcnt(4)\n\t"
            "v_smfmac_f32_16x16x64_bf16 v[188:191], v[172:175], v[160:167], v255\n\t"
            "s_mov_b32 exec_hi, 0x3fffff\n\t"
            "s_add_u32 m0, s66, 0\n\t"
            "s_add_u32 s49, s79, 0\n\t"
            "buffer_load_dwordx4 v249, s[72:75], s49 offen lds\n\t"
            "s_mov_b32 exec_hi, -1\n\t"
            "s_mov_b32 exec_hi, 0x3fffff\n\t"
            "s_add_u32 m0, s66, 872\n\t"
            "s_add_u32 s49, s68, 196224\n\t"
            "buffer_load_dwordx4 v249, s[72:75], s49 offen lds\n\t"
            "s_mov_b32 exec_hi, -1\n\t"
            "v_cvt_pk_bf16_f32 v184, v184, v185\n\t"
            "v_cvt_pk_bf16_f32 v185, v186, v187\n\t"
            "v_cvt_pk_bf16_f32 v186, v188, v189\n\t"
            "v_cvt_pk_bf16_f32 v187, v190, v191\n\t"
            "v_mfma_f32_4x4x4_16b_bf16 v[196:199], v[184:185], v[184:185], v[196:199]\n\t"
            "ds_write_b128 v237, v[184:187] offset:256\n\t"
            "v_mfma_f32_16x16x32_bf16 v[192:195], v[238:241], v[184:187], v[192:195]\n\t"
            "v_add_u32 v237, s48, v245\n\t"
            "v_mfma_f32_4x4x4_16b_bf16 v[196:199], v[186:187], v[186:187], v[196:199]\n\t"
            "ds_read_b128 v[204:207], v237\n\t"
            "ds_read_b128 v[216:219], v237 offset:6144\n\t"
            "ds_read_b128 v[228:231], v237 offset:12288\n\t"
            "s_mov_b32 exec_hi, 0x3fffff\n\t"
            "s_add_u32 m0, s66, 1744\n\t"
            "s_add_u32 s49, s68, 392448\n\t"
            "buffer_load_dwordx4 v249, s[72:75], s49 offen lds\n\t"
            "s_mov_b32 exec_hi, -1\n\t"
            "v_cndmask_b32_e64 v193, v196, v197, s[60:61]\n\t"
            "v_cndmask_b32_e64 v193, v193, v198, s[62:63]\n\t"
            "v_cndmask_b32_e64 v193, v193, v199, s[64:65]\n\t"
            "ds_write_b64 v248, v[192:193] offset:18816\n\t"
            "s_mov_b64 exec, 1\n\t"
            "ds_inc_u32 v255, v238 offset:7860\n\t"
            "s_mov_b64 exec, -1\n\t"
            "v_add_u32 v248, s48, v246\n\t"
            "ds_read_b128 v[208:211], v248\n\t"
            "ds_read_b128 v[220:223], v248 offset:6144\n\t"
            "ds_read_b128 v[232:235], v248 offset:12288\n\t"
            "s_mov_b32 exec_hi, 0x3fffff\n\t"
            "s_add_u32 m0, s66, 2616\n\t"
            "s_add_u32 s49, s68, 588672\n\t"
            "buffer_load_dwordx4 v249, s[72:75], s49 offen lds\n\t"
            "s_mov_b32 exec_hi, -1\n\t"
            "s_mov_b32 exec_hi, 0x3fffff\n\t"
            "s_add_u32 m0, s66, 3488\n\t"
            "s_add_u32 s49, s68, 784896\n\t"
            "buffer_load_dwordx4 v249, s[72:75], s49 offen lds\n\t"
            "s_mov_b32 exec_hi, -1\n\t"
            "s_waitcnt lgkmcnt(0)\n\t"
            "s_barrier\n\t"
            "s_setprio 3\n\t"
            "s_mov_b32 exec_hi, 0x3fffff\n\t"
            "s_add_u32 m0, s66, 4360\n\t"
            "s_add_u32 s49, s68, 981120\n\t"
            "buffer_load_dwordx4 v249, s[72:75], s49 offen lds\n\t"
            "s_mov_b32 exec_hi, -1\n\t"
            "s_mov_b32 exec_hi, 0x3fffff\n\t"
            "s_add_u32 m0, s66, 5232\n\t"
            "s_add_u32 s49, s68, 1177344\n\t"
            "buffer_load_dwordx4 v249, s[72:75], s49 offen lds\n\t"
            "s_mov_b32 exec_hi, -1\n\t"
            "s_mov_b32 exec_hi, 0x3fffff\n\t"
            "s_add_u32 m0, s66, 6104\n\t"
            "s_add_u32 s49, s68, 1373568\n\t"
            "buffer_load_dwordx4 v249, s[72:75], s49 offen lds\n\t"
            "s_mov_b32 exec_hi, -1\n\t"
            "s_mov_b32 exec_hi, 0x3fffff\n\t"
            "s_add_u32 m0, s66, 6976\n\t"
            "s_add_u32 s49, s68, 1569792\n\t"
            "buffer_load_dwordx4 v249, s[72:75], s49 offen lds\n\t"
            "s_mov_b32 exec_hi, -1\n\t"
            "s_add_u32 s49, s33, 0\n\t"
            "buffer_load_dwordx4 v[0:3], v247, s[28:31], s49 offen sc1\n\t"
            "s_add_u32 s49, s33, 1024\n\t"
            "buffer_load_dwordx4 v[4:7], v247, s[28:31], s49 offen sc1\n\t"
            "s_add_u32 s49, s33, 2048\n\t"
            "buffer_load_dwordx4 v[8:11], v247, s[28:31], s49 offen sc1\n\t"
            "s_add_u32 s49, s33, 3072\n\t"
            "buffer_load_dwordx4 v[12:15], v247, s[28:31], s49 offen sc1\n\t"
            "s_add_u32 s49, s33, 4096\n\t"
            "buffer_load_dwordx4 v[16:19], v247, s[28:31], s49 offen sc1\n\t"
            "s_add_u32 s49, s33, 5120\n\t"
            "buffer_load_dwordx4 v[20:23], v247, s[28:31], s49 offen sc1\n\t"
            "s_add_u32 s49, s33, 6144\n\t"
            "buffer_load_dwordx4 v[24:27], v247, s[28:31], s49 offen sc1\n\t"
            "s_add_u32 s49, s33, 7168\n\t"
            "buffer_load_dwordx4 v[28:31], v247, s[28:31], s49 offen sc1\n\t"
            "s_add_u32 s49, s33, 8192\n\t"
            "buffer_load_dwordx4 v[32:35], v247, s[28:31], s49 offen sc1\n\t"
            "s_add_u32 s49, s33, 9216\n\t"
            "buffer_load_dwordx4 v[36:39], v247, s[28:31], s49 offen sc1\n\t"
            "s_add_u32 s49, s33, 10240\n\t"
            "buffer_load_dwordx4 v[40:43], v247, s[28:31], s49 offen sc1\n\t"
            "s_add_u32 s49, s33, 11264\n\t"
            "buffer_load_dwordx4 v[44:47], v247, s[28:31], s49 offen sc1\n\t"
            "s_add_u32 s49, s33, 12288\n\t"
            "buffer_load_dwordx4 v[48:51], v247, s[28:31], s49 offen sc1\n\t"
            "s_add_u32 s49, s33, 13312\n\t"
            "buffer_load_dwordx4 v[52:55], v247, s[28:31], s49 offen sc1\n\t"
            "s_add_u32 s49, s33, 14336\n\t"
            "buffer_load_dwordx4 v[56:59], v247, s[28:31], s49 offen sc1\n\t"
            "s_add_u32 s49, s33, 15360\n\t"
            "buffer_load_dwordx4 v[60:63], v247, s[28:31], s49 offen sc1\n\t"
            "s_add_u32 s49, s33, 16384\n\t"
            "buffer_load_dwordx4 v[64:67], v247, s[28:31], s49 offen sc1\n\t"
            "s_add_u32 s49, s33, 17408\n\t"
            "buffer_load_dwordx4 v[68:71], v247, s[28:31], s49 offen sc1\n\t"
            "s_add_u32 s49, s33, 18432\n\t"
            "buffer_load_dwordx4 v[72:75], v247, s[28:31], s49 offen sc1\n\t"
            "s_add_u32 s49, s33, 19456\n\t"
            "buffer_load_dwordx4 v[76:79], v247, s[28:31], s49 offen sc1\n\t"
            "s_add_u32 s49, s33, 20480\n\t"
            "buffer_load_dwordx4 v[80:83], v247, s[28:31], s49 offen sc1\n\t"
            "s_add_u32 s49, s33, 21504\n\t"
            "buffer_load_dwordx4 v[84:87], v247, s[28:31], s49 offen sc1\n\t"
            "s_add_u32 s49, s33, 22528\n\t"
            "buffer_load_dwordx4 v[88:91], v247, s[28:31], s49 offen sc1\n\t"
            "s_add_u32 s49, s33, 23552\n\t"
            "buffer_load_dwordx4 v[92:95], v247, s[28:31], s49 offen sc1\n\t"
            "s_add_u32 s49, s33, 24576\n\t"
            "buffer_load_dwordx4 v[96:99], v247, s[28:31], s49 offen sc1\n\t"
            "s_add_u32 s49, s33, 25600\n\t"
            "buffer_load_dwordx4 v[100:103], v247, s[28:31], s49 offen sc1\n\t"
            "s_add_u32 s49, s33, 26624\n\t"
            "buffer_load_dwordx4 v[104:107], v247, s[28:31], s49 offen sc1\n\t"
            "s_add_u32 s49, s33, 27648\n\t"
            "buffer_load_dwordx4 v[108:111], v247, s[28:31], s49 offen sc1\n\t"
            "s_add_u32 s49, s33, 28672\n\t"
            "buffer_load_dwordx4 v[112:115], v247, s[28:31], s49 offen sc1\n\t"
            "s_add_u32 s49, s33, 29696\n\t"
            "buffer_load_dwordx4 v[116:119], v247, s[28:31], s49 offen sc1\n\t"
            "s_add_u32 s49, s33, 30720\n\t"
            "buffer_load_dwordx4 v[120:123], v247, s[28:31], s49 offen sc1\n\t"
            "s_add_u32 s49, s33, 31744\n\t"
            "buffer_load_dwordx4 v[124:127], v247, s[28:31], s49 offen sc1\n\t"
            "s_add_u32 s49, s33, 32768\n\t"
            "buffer_load_dwordx4 v[128:131], v247, s[28:31], s49 offen sc1\n\t"
            "s_add_u32 s49, s33, 33792\n\t"
            "buffer_load_dwordx4 v[132:135], v247, s[28:31], s49 offen sc1\n\t"
            "s_add_u32 s49, s33, 34816\n\t"
            "buffer_load_dwordx4 v[136:139], v247, s[28:31], s49 offen sc1\n\t"
            "s_add_u32 s49, s33, 35840\n\t"
            "buffer_load_dwordx4 v[140:143], v247, s[28:31], s49 offen sc1\n\t"
            "s_add_u32 s59, s59, -47104\n\t"
            "s_add_u32 s45, s46, s27\n\t"
            "s_mov_b32 s46, s47\n\t"
            "s_mov_b32 s47, s68\n\t"
            "s_xor_b32 s48, s48, s71\n\t"
            "s_xor_b32 s67, s67, s71\n\t"
            "s_add_u32 s76, s76, 4\n\t"
            "s_add_u32 s70, s70, 4\n\t"
            "s_lshl_b32 s56, s56, 2\n\t"
            "s_lshr_b32 s49, s26, 30\n\t"
            "s_or_b32 s56, s56, s49\n\t"
            "s_waitcnt vmcnt(0)\n\t"
            "s_nop 7\n\t"
            ".p2align 6\n\t"
            "s_nop 0\n\t"
            "s_nop 0\n\t"
            "s_nop 0\n\t"
            "s_nop 0\n\t"
            "s_nop 0\n\t"
            "s_nop 0\n\t"
            "s_nop 0\n\t"
            ".Lwb_loop%=:\n\t"
            "s_setprio 1\n\t"
            "ds_read_b128 v[168:171], v254\n\t"
            "ds_read_b128 v[172:175], v254 offset:64\n\t"
            "s_waitcnt vmcnt(9)\n\t"
            "ds_read_b64 v[144:145], v253\n\t"
            "ds_read_b64 v[146:147], v253 offset:96\n\t"
            "ds_read_b64 v[148:149], v253 offset:1744\n\t"
            "ds_read_b64 v[150:151], v253 offset:1840\n\t"
            "ds_read_b64 v[152:153], v253 offset:8\n\t"
            "ds_read_b64 v[154:155], v253 offset:104\n\t"
            "ds_read_b64 v[156:157], v253 offset:1752\n\t"
            "ds_read_b64 v[158:159], v253 offset:1848\n\t"
            "ds_read_b64 v[160:161], v253 offset:16\n\t"
            "ds_read_b64 v[162:163], v253 offset:112\n\t"
            "ds_read_b64 v[164:165], v253 offset:1760\n\t"
            "ds_read_b64 v[166:167], v253 offset:1856\n\t"
            "v_mov_b64 v[176:177], 0\n\t"
            "v_mov_b64 v[178:179], 0\n\t"
            "v_mov_b64 v[180:181], 0\n\t"
            "v_mov_b64 v[182:183], 0\n\t"
            "v_mov_b32 v249, s76\n\t"
            "v_mov_b64 v[184:185], 0\n\t"
            "v_mov_b64 v[186:187], 0\n\t"
            "v_mov_b64 v[188:189], 0\n\t"
            "v_mov_b64 v[190:191], 0\n\t"
            "v_add_u32 v236, s67, v250\n\t"
            "v_add_u32 v248, s67, v251\n\t"
            "v_mov_b32 v247, 0\n\t"
            "ds_read_b32 v249, v249\n\t"
            "s_setprio 3\n\t"
            "s_waitcnt lgkmcnt(9)\n\t"
            "v_smfmac_f32_16x16x64_bf16 v[176:179], v[168:171], v[144:151], v255\n\t"
            "ds_read_b128 v[168:171], v254 offset:128\n\t"
            "s_waitcnt lgkmcnt(6)\n\t"
            "v_smfmac_f32_16x16x64_bf16 v[180:183], v[172:175], v[152:159], v255\n\t"
            "s_setprio 1\n\t"
            "ds_read_b64 v[144:145], v253 offset:24\n\t"
            "ds_read_b64 v[146:147], v253 offset:120\n\t"
            "ds_read_b64 v[148:149], v253 offset:1768\n\t"
            "ds_read_b64 v[150:151], v253 offset:1864\n\t"
            "ds_read_b128 v[172:175], v254 offset:192\n\t"
            "ds_read_b64 v[152:153], v253 offset:32\n\t"
            "ds_read_b64 v[154:155], v253 offset:128\n\t"
            "ds_read_b64 v[156:157], v253 offset:1776\n\t"
            "ds_read_b64 v[158:159], v253 offset:1872\n\t"
            "v_cvt_pk_bf16_f32 v176, v176, v177\n\t"
            "v_cvt_pk_bf16_f32 v177, v178, v179\n\t"
            "v_cvt_pk_bf16_f32 v178, v180, v181\n\t"
            "v_cvt_pk_bf16_f32 v179, v182, v183\n\t"
            "s_setprio 3\n\t"
            "v_mfma_f32_4x4x4_16b_bf16 v[196:199], v[176:177], v[176:177], 0\n\t"
            "v_mov_b64 v[180:181], 0\n\t"
            "v_mfma_f32_16x16x32_bf16 v[192:195], v[238:241], v[176:179], 0\n\t"
            "s_waitcnt lgkmcnt(10)\n\t"
            "v_readfirstlane_b32 s26, v249\n\t"
            "v_mov_b64 v[182:183], 0\n\t"
            "v_mfma_f32_4x4x4_16b_bf16 v[196:199], v[178:179], v[178:179], v[196:199]\n\t"
            "ds_read_b32 v249, v252 offset:12\n\t"
            "s_and_b32 s68, s26, 0x7ffffff\n\t"
            "s_add_u32 s68, s68, s78\n\t"
            "s_and_b32 s69, s26, 0x38000000\n\t"
            "s_or_b32 s69, s69, s39\n\t"
            "s_bitcmp1_b32 s26, 27\n\t"
            "s_cselect_b32 s79, 2.0, s68\n\t"
            "s_and_b32 s57, s56, 3\n\t"
            "s_lshl_b32 s58, s57, 3\n\t"
            "s_lshr_b32 m0, s54, s58\n\t"
            "s_lshl_b32 s58, s57, 4\n\t"
            "s_lshr_b64 s[36:37], s[50:51], s58\n\t"
            "s_and_b32 s57, s36, 0xffff\n\t"
            "s_add_u32 s57, s57, s67\n\t"
            "v_writelane_b32 v236, s57, 63\n\t"
            "v_writelane_b32 v236, s59, m0\n\t"
            "v_xor_b32 v237, 16, v236\n\t"
            "s_lshr_b64 s[36:37], s[52:53], s58\n\t"
            "s_and_b32 s58, s36, 0xffff\n\t"
            "s_add_u32 s58, s58, s67\n\t"
            "ds_write_b128 v236, v[176:179]\n\t"
            "s_waitcnt lgkmcnt(11)\n\t"
            "v_smfmac_f32_16x16x64_bf16 v[184:187], v[168:171], v[160:167], v255\n\t"
            "s_and_b32 s38, s52, 0xffff\n\t"
            "s_add_u32 s38, s38, s67\n\t"
            "ds_read_b128 v[168:171], v254 offset:256\n\t"
            "s_waitcnt lgkmcnt(7)\n\t"
            "v_smfmac_f32_16x16x64_bf16 v[188:191], v[172:175], v[144:151], v255\n\t"
            "s_setprio 1\n\t"
            "ds_read_b64 v[160:161], v253 offset:40\n\t"
            "ds_read_b64 v[162:163], v253 offset:136\n\t"
            "ds_read_b64 v[164:165], v253 offset:1784\n\t"
            "ds_read_b64 v[166:167], v253 offset:1880\n\t"
            "ds_read_b128 v[172:175], v254 offset:320\n\t"
            "ds_read_b64 v[144:145], v253 offset:48\n\t"
            "ds_read_b64 v[146:147], v253 offset:144\n\t"
            "ds_read_b64 v[148:149], v253 offset:1792\n\t"
            "s_waitcnt lgkmcnt(14)\n\t"
            "ds_read_b64 v[150:151], v253 offset:1888\n\t"
            "v_mov_b64 v[176:177], 0\n\t"
            "v_mov_b64 v[178:179], 0\n\t"
            "v_cvt_pk_bf16_f32 v184, v184, v185\n\t"
            "v_cvt_pk_bf16_f32 v185, v186, v187\n\t"
            "v_cvt_pk_bf16_f32 v186, v188, v189\n\t"
            "v_cvt_pk_bf16_f32 v187, v190, v191\n\t"
            "s_setprio 3\n\t"
            "v_mfma_f32_4x4x4_16b_bf16 v[196:199], v[184:185], v[184:185], v[196:199]\n\t"
            "s_waitcnt lgkmcnt(14)\n\t"
            "ds_write_b128 v237, v[184:187]\n\t"
            "v_mfma_f32_16x16x32_bf16 v[192:195], v[238:241], v[184:187], v[192:195]\n\t"
            "v_mov_b64 v[184:185], 0\n\t"
            "v_mov_b64 v[188:189], 0\n\t"
            "v_mfma_f32_4x4x4_16b_bf16 v[196:199], v[186:187], v[186:187], v[196:199]\n\t"
            "v_mov_b64 v[186:187], 0\n\t"
            "v_mov_b64 v[190:191], 0\n\t"
            "v_writelane_b32 v248, s58, 63\n\t"
            "v_writelane_b32 v248, s38, m0\n\t"
            "s_waitcnt lgkmcnt(12)\n\t"
            "v_and_b32 v249, s69, v249\n\t"
            "s_waitcnt lgkmcnt(10)\n\t"
            "v_smfmac_f32_16x16x64_bf16 v[176:179], v[168:171], v[152:159], v255\n\t"
            "ds_read_b128 v[168:171], v254 offset:384\n\t"
            "ds_read_b64 v[152:153], v253 offset:56\n\t"
            "ds_read_b64 v[154:155], v253 offset:152\n\t"
            "ds_read_b64 v[156:157], v253 offset:1800\n\t"
            "ds_read_b64 v[158:159], v253 offset:1896\n\t"
            "s_waitcnt lgkmcnt(10)\n\t"
            "v_smfmac_f32_16x16x64_bf16 v[180:183], v[172:175], v[160:167], v255\n\t"
            "s_setprio 1\n\t"
            "ds_read_b128 v[172:175], v254 offset:448\n\t"
            "ds_read_b64 v[160:161], v253 offset:64\n\t"
            "ds_read_b64 v[162:163], v253 offset:160\n\t"
            "ds_read_b64 v[164:165], v253 offset:1808\n\t"
            "ds_read_b64 v[166:167], v253 offset:1904\n\t"
            "v_cvt_pk_bf16_f32 v176, v176, v177\n\t"
            "v_cvt_pk_bf16_f32 v177, v178, v179\n\t"
            "v_cvt_pk_bf16_f32 v178, v180, v181\n\t"
            "v_cvt_pk_bf16_f32 v179, v182, v183\n\t"
            "s_setprio 3\n\t"
            "v_mfma_f32_4x4x4_16b_bf16 v[196:199], v[176:177], v[176:177], v[196:199]\n\t"
            "s_waitcnt lgkmcnt(14)\n\t"
            "ds_write_b128 v236, v[176:179] offset:128\n\t"
            "v_mfma_f32_16x16x32_bf16 v[192:195], v[238:241], v[176:179], v[192:195]\n\t"
            "v_mov_b64 v[176:177], 0\n\t"
            "v_mov_b64 v[180:181], 0\n\t"
            "v_mfma_f32_4x4x4_16b_bf16 v[196:199], v[178:179], v[178:179], v[196:199]\n\t"
            "v_mov_b64 v[178:179], 0\n\t"
            "v_mov_b64 v[182:183], 0\n\t"
            "s_waitcnt lgkmcnt(10)\n\t"
            "v_smfmac_f32_16x16x64_bf16 v[184:187], v[168:171], v[144:151], v255\n\t"
            "ds_read_b128 v[168:171], v254 offset:512\n\t"
            "ds_read_b64 v[144:145], v253 offset:72\n\t"
            "ds_read_b64 v[146:147], v253 offset:168\n\t"
            "ds_read_b64 v[148:149], v253 offset:1816\n\t"
            "ds_read_b64 v[150:151], v253 offset:1912\n\t"
            "s_waitcnt lgkmcnt(10)\n\t"
            "v_smfmac_f32_16x16x64_bf16 v[188:191], v[172:175], v[152:159], v255\n\t"
            "s_setprio 1\n\t"
            "ds_read_b128 v[172:175], v254 offset:576\n\t"
            "ds_read_b64 v[152:153], v253 offset:80\n\t"
            "ds_read_b64 v[154:155], v253 offset:176\n\t"
            "ds_read_b64 v[156:157], v253 offset:1824\n\t"
            "ds_read_b64 v[158:159], v253 offset:1920\n\t"
            "v_cvt_pk_bf16_f32 v184, v184, v185\n\t"
            "v_cvt_pk_bf16_f32 v185, v186, v187\n\t"
            "v_cvt_pk_bf16_f32 v186, v188, v189\n\t"
            "v_cvt_pk_bf16_f32 v187, v190, v191\n\t"
            "s_setprio 3\n\t"
            "v_mfma_f32_4x4x4_16b_bf16 v[196:199], v[184:185], v[184:185], v[196:199]\n\t"
            "s_waitcnt lgkmcnt(14)\n\t"
            "ds_write_b128 v237, v[184:187] offset:128\n\t"
            "v_mfma_f32_16x16x32_bf16 v[192:195], v[238:241], v[184:187], v[192:195]\n\t"
            "v_mov_b64 v[184:185], 0\n\t"
            "v_mov_b64 v[188:189], 0\n\t"
            "v_mfma_f32_4x4x4_16b_bf16 v[196:199], v[186:187], v[186:187], v[196:199]\n\t"
            "v_mov_b64 v[186:187], 0\n\t"
            "v_mov_b64 v[190:191], 0\n\t"
            "s_waitcnt lgkmcnt(10)\n\t"
            "v_smfmac_f32_16x16x64_bf16 v[176:179], v[168:171], v[160:167], v255\n\t"
            "ds_read_b128 v[168:171], v254 offset:640\n\t"
            "ds_read_b64 v[160:161], v253 offset:88\n\t"
            "ds_read_b64 v[162:163], v253 offset:184\n\t"
            "ds_read_b64 v[164:165], v253 offset:1832\n\t"
            "ds_read_b64 v[166:167], v253 offset:1928\n\t"
            "s_waitcnt lgkmcnt(10)\n\t"
            "v_smfmac_f32_16x16x64_bf16 v[180:183], v[172:175], v[144:151], v255\n\t"
            "ds_read_b128 v[172:175], v254 offset:704\n\t"
            "ds_read_b32 v247, v247 offset:7856\n\t"
            "v_cvt_pk_bf16_f32 v176, v176, v177\n\t"
            "v_cvt_pk_bf16_f32 v177, v178, v179\n\t"
            "v_lshlrev_b32 v144, 16, v212\n\t"
            "v_and_b32 v145, 0xffff0000, v212\n\t"
            "v_mfma_f32_4x4x4_16b_bf16 v[196:199], v[176:177], v[176:177], v[196:199]\n\t"
            "v_lshlrev_b32 v146, 16, v213\n\t"
            "v_cvt_pk_bf16_f32 v178, v180, v181\n\t"
            "v_cvt_pk_bf16_f32 v179, v182, v183\n\t"
            "v_lshlrev_b32 v180, 16, v210\n\t"
            "v_and_b32 v181, 0xffff0000, v210\n\t"
            "v_mfma_f32_4x4x4_16b_bf16 v[196:199], v[178:179], v[178:179], v[196:199]\n\t"
            "ds_write_b128 v236, v[176:179] offset:256\n\t"
            "v_mfma_f32_16x16x32_bf16 v[192:195], v[238:241], v[176:179], v[192:195]\n\t"
            "s_setprio 1\n\t"
            "v_lshlrev_b32 v176, 16, v208\n\t"
            "v_and_b32 v177, 0xffff0000, v208\n\t"
            "v_lshlrev_b32 v178, 16, v209\n\t"
            "v_and_b32 v179, 0xffff0000, v209\n\t"
            "v_lshlrev_b32 v182, 16, v211\n\t"
            "v_and_b32 v183, 0xffff0000, v211\n\t"
            "v_and_b32 v147, 0xffff0000, v213\n\t"
            "v_lshlrev_b32 v148, 16, v214\n\t"
            "v_and_b32 v149, 0xffff0000, v214\n\t"
            "v_lshlrev_b32 v150, 16, v215\n\t"
            "v_and_b32 v151, 0xffff0000, v215\n\t"
            "v_add_u32 v236, s48, v244\n\t"
            "ds_read_b128 v[212:215], v236 offset:6144\n\t"
            "s_setprio 3\n\t"
            "s_waitcnt lgkmcnt(8)\n\t"
            "v_smfmac_f32_16x16x64_bf16 v[184:187], v[168:171], v[152:159], v255\n\t"
            "v_lshlrev_b32 v152, 16, v200\n\t"
            "v_and_b32 v153, 0xffff0000, v200\n\t"
            "v_lshlrev_b32 v154, 16, v201\n\t"
            "v_and_b32 v155, 0xffff0000, v201\n\t"
            "v_lshlrev_b32 v156, 16, v202\n\t"
            "s_waitcnt lgkmcnt(3)\n\t"
            "v_smfmac_f32_16x16x64_bf16 v[188:191], v[172:175], v[160:167], v255\n\t"
            "s_setprio 1\n\t"
            "s_mov_b32 exec_hi, 0x3fffff\n\t"
            "s_add_u32 m0, s66, 0\n\t"
            "s_add_u32 s49, s79, 0\n\t"
            "buffer_load_dwordx4 v249, s[72:75], s49 offen lds\n\t"
            "s_mov_b32 exec_hi, -1\n\t"
            "s_mov_b32 exec_hi, 0x3fffff\n\t"
            "s_add_u32 m0, s66, 872\n\t"
            "s_add_u32 s49, s68, 196224\n\t"
            "buffer_load_dwordx4 v249, s[72:75], s49 offen lds\n\t"
            "s_mov_b32 exec_hi, -1\n\t"
            "s_mov_b32 exec_hi, 0x3fffff\n\t"
            "s_add_u32 m0, s66, 1744\n\t"
            "s_add_u32 s49, s68, 392448\n\t"
            "buffer_load_dwordx4 v249, s[72:75], s49 offen lds\n\t"
            "s_mov_b32 exec_hi, -1\n\t"
            "s_mov_b32 exec_hi, 0x3fffff\n\t"
            "s_add_u32 m0, s66, 2616\n\t"
            "s_add_u32 s49, s68, 588672\n\t"
            "buffer_load_dwordx4 v249, s[72:75], s49 offen lds\n\t"
            "s_mov_b32 exec_hi, -1\n\t"
            "s_mov_b32 exec_hi, 0x3fffff\n\t"
            "s_add_u32 m0, s66, 3488\n\t"
            "s_add_u32 s49, s68, 784896\n\t"
            "buffer_load_dwordx4 v249, s[72:75], s49 offen lds\n\t"
            "s_mov_b32 exec_hi, -1\n\t"
            "s_mov_b32 exec_hi, 0x3fffff\n\t"
            "s_add_u32 m0, s66, 4360\n\t"
            "s_add_u32 s49, s68, 981120\n\t"
            "buffer_load_dwordx4 v249, s[72:75], s49 offen lds\n\t"
            "s_mov_b32 exec_hi, -1\n\t"
            "s_mov_b32 exec_hi, 0x3fffff\n\t"
            "s_add_u32 m0, s66, 5232\n\t"
            "s_add_u32 s49, s68, 1177344\n\t"
            "buffer_load_dwordx4 v249, s[72:75], s49 offen lds\n\t"
            "s_mov_b32 exec_hi, -1\n\t"
            "s_mov_b32 exec_hi, 0x3fffff\n\t"
            "s_add_u32 m0, s66, 6104\n\t"
            "s_add_u32 s49, s68, 1373568\n\t"
            "buffer_load_dwordx4 v249, s[72:75], s49 offen lds\n\t"
            "s_mov_b32 exec_hi, -1\n\t"
            "s_mov_b32 exec_hi, 0x3fffff\n\t"
            "s_add_u32 m0, s66, 6976\n\t"
            "s_add_u32 s49, s68, 1569792\n\t"
            "buffer_load_dwordx4 v249, s[72:75], s49 offen lds\n\t"
            "s_mov_b32 exec_hi, -1\n\t"
            "v_cvt_pk_bf16_f32 v184, v184, v185\n\t"
            "v_cvt_pk_bf16_f32 v185, v186, v187\n\t"
            "v_cvt_pk_bf16_f32 v186, v188, v189\n\t"
            "v_cvt_pk_bf16_f32 v187, v190, v191\n\t"
            "s_setprio 3\n\t"
            "v_mfma_f32_4x4x4_16b_bf16 v[196:199], v[184:185], v[184:185], v[196:199]\n\t"
            "ds_write_b128 v237, v[184:187] offset:256\n\t"
            "v_mfma_f32_16x16x32_bf16 v[192:195], v[238:241], v[184:187], v[192:195]\n\t"
            "s_waitcnt lgkmcnt(3)\n\t"
            "v_readfirstlane_b32 s77, v247\n\t"
            "s_cmp_ge_u32 s77, s70\n\t"
            "s_cbranch_scc0 .Lwb_spin%=\n\t"
            ".Lwb_go%=:\n\t"
            "v_mfma_f32_4x4x4_16b_bf16 v[196:199], v[186:187], v[186:187], v[196:199]\n\t"
            "s_setprio 1\n\t"
            "ds_read_b128 v[184:187], v242\n\t"
            "ds_read_b128 v[188:191], v243\n\t"
            "ds_read_b128 v[172:175], v243 offset:128\n\t"
            "v_and_b32 v157, 0xffff0000, v202\n\t"
            "v_lshlrev_b32 v158, 16, v203\n\t"
            "v_and_b32 v159, 0xffff0000, v203\n\t"
            "v_lshlrev_b32 v160, 16, v204\n\t"
            "v_and_b32 v161, 0xffff0000, v204\n\t"
            "v_lshlrev_b32 v162, 16, v205\n\t"
            "v_cndmask_b32_e64 v193, v196, v197, s[60:61]\n\t"
            "v_cndmask_b32_e64 v193, v193, v198, s[62:63]\n\t"
            "v_cndmask_b32_e64 v193, v193, v199, s[64:65]\n\t"
            "v_and_b32 v163, 0xffff0000, v205\n\t"
            "v_lshlrev_b32 v164, 16, v206\n\t"
            "v_and_b32 v165, 0xffff0000, v206\n\t"
            "v_lshlrev_b32 v166, 16, v207\n\t"
            "v_and_b32 v167, 0xffff0000, v207\n\t"
            "v_lshlrev_b32 v168, 16, v216\n\t"
            "v_and_b32 v169, 0xffff0000, v216\n\t"
            "v_lshlrev_b32 v170, 16, v217\n\t"
            "v_and_b32 v171, 0xffff0000, v217\n\t"
            "s_bfe_u32 s57, s56, 0x20004\n\t"
            "s_lshl_b32 s58, s57, 3\n\t"
            "s_lshr_b32 s58, s55, s58\n\t"
            "s_lshl_b64 s[36:37], s[34:35], s58\n\t"
            "ds_read_b128 v[196:199], v252\n\t"
            "ds_read_b128 v[200:203], v236\n\t"
            "ds_write_b64 v248, v[192:193] offset:18816\n\t"
            "ds_read_b128 v[192:195], v242 offset:128\n\t"
            "s_bitcmp1_b32 s57, 1\n\t"
            "s_cselect_b32 s58, 1177344, 0\n\t"
            "s_bitcmp1_b32 s57, 0\n\t"
            "s_cselect_b32 s57, 2304, 0\n\t"
            "s_add_u32 s57, s57, s58\n\t"
            "s_mov_b64 exec, 1\n\t"
            "ds_inc_u32 v255, v238 offset:7860\n\t"
            "s_mov_b64 exec, -1\n\t"
            "v_add_u32 v237, s48, v245\n\t"
            "v_add_u32 v248, s48, v246\n\t"
            "s_add_u32 s59, s59, 800\n\t"
            "s_setprio 1\n\t"
            "ds_read_b128 v[204:207], v237\n\t"
            "s_waitcnt lgkmcnt(7)\n\t"
            "v_mfma_f32_16x16x32_bf16 v[152:155], v[0:3], v[184:187], v[152:155]\n\t"
            "ds_read_b128 v[208:211], v248\n\t"
            "v_mfma_f32_16x16x32_bf16 v[152:155], v[4:7], v[188:191], v[152:155]\n\t"
            "v_mfma_f32_16x16x32_bf16 v[156:159], v[28:31], v[188:191], v[156:159]\n\t"
            "v_mfma_f32_16x16x32_bf16 v[156:159], v[24:27], v[184:187], v[156:159]\n\t"
            "v_mfma_f32_16x16x32_bf16 v[160:163], v[48:51], v[184:187], v[160:163]\n\t"
            "v_mfma_f32_16x16x32_bf16 v[160:163], v[52:55], v[188:191], v[160:163]\n\t"
            "v_mfma_f32_16x16x32_bf16 v[164:167], v[76:79], v[188:191], v[164:167]\n\t"
            "s_waitcnt lgkmcnt(6)\n\t"
            "s_mov_b64 exec, s[36:37]\n\t"
            "v_add_u32 v196, s57, v196\n\t"
            "s_mov_b64 exec, -1\n\t"
            "v_mfma_f32_16x16x32_bf16 v[164:167], v[72:75], v[184:187], v[164:167]\n\t"
            "v_mfma_f32_16x16x32_bf16 v[176:179], v[96:99], v[184:187], v[176:179]\n\t"
            "v_mfma_f32_16x16x32_bf16 v[176:179], v[100:103], v[188:191], v[176:179]\n\t"
            "v_mfma_f32_16x16x32_bf16 v[180:183], v[124:127], v[188:191], v[180:183]\n\t"
            "ds_read_b128 v[188:191], v243 offset:256\n\t"
            "v_mfma_f32_16x16x32_bf16 v[180:183], v[120:123], v[184:187], v[180:183]\n\t"
            "ds_read_b128 v[184:187], v242 offset:256\n\t"
            "s_waitcnt lgkmcnt(5)\n\t"
            "v_mfma_f32_16x16x32_bf16 v[152:155], v[8:11], v[192:195], v[152:155]\n\t"
            "v_mfma_f32_16x16x32_bf16 v[152:155], v[12:15], v[172:175], v[152:155]\n\t"
            "v_mfma_f32_16x16x32_bf16 v[156:159], v[36:39], v[172:175], v[156:159]\n\t"
            "v_mfma_f32_16x16x32_bf16 v[156:159], v[32:35], v[192:195], v[156:159]\n\t"
            "v_mfma_f32_16x16x32_bf16 v[160:163], v[56:59], v[192:195], v[160:163]\n\t"
            "v_mfma_f32_16x16x32_bf16 v[160:163], v[60:63], v[172:175], v[160:163]\n\t"
            "v_mfma_f32_16x16x32_bf16 v[164:167], v[84:87], v[172:175], v[164:167]\n\t"
            "v_mfma_f32_16x16x32_bf16 v[164:167], v[80:83], v[192:195], v[164:167]\n\t"
            "v_mfma_f32_16x16x32_bf16 v[176:179], v[104:107], v[192:195], v[176:179]\n\t"
            "v_mfma_f32_16x16x32_bf16 v[176:179], v[108:111], v[172:175], v[176:179]\n\t"
            "v_mfma_f32_16x16x32_bf16 v[180:183], v[132:135], v[172:175], v[180:183]\n\t"
            "ds_read_b128 v[172:175], v243 offset:6144\n\t"
            "v_mfma_f32_16x16x32_bf16 v[180:183], v[128:131], v[192:195], v[180:183]\n\t"
            "ds_read_b128 v[192:195], v242 offset:6144\n\t"
            "s_waitcnt lgkmcnt(2)\n\t"
            "v_mfma_f32_16x16x32_bf16 v[152:155], v[16:19], v[184:187], v[152:155]\n\t"
            "v_mfma_f32_16x16x32_bf16 v[152:155], v[20:23], v[188:191], v[152:155]\n\t"
            "v_mfma_f32_16x16x32_bf16 v[156:159], v[44:47], v[188:191], v[156:159]\n\t"
            "v_mfma_f32_16x16x32_bf16 v[156:159], v[40:43], v[184:187], v[156:159]\n\t"
            "v_mfma_f32_16x16x32_bf16 v[160:163], v[64:67], v[184:187], v[160:163]\n\t"
            "v_mfma_f32_16x16x32_bf16 v[160:163], v[68:71], v[188:191], v[160:163]\n\t"
            "v_mfma_f32_16x16x32_bf16 v[164:167], v[92:95], v[188:191], v[164:167]\n\t"
            "v_mfma_f32_16x16x32_bf16 v[164:167], v[88:91], v[184:187], v[164:167]\n\t"
            "s_nop 1\n\t"
            "v_cvt_pk_bf16_f32 v152, v152, v153\n\t"
            "v_mfma_f32_16x16x32_bf16 v[176:179], v[112:115], v[184:187], v[176:179]\n\t"
            "v_cvt_pk_bf16_f32 v153, v154, v155\n\t"
            "v_mfma_f32_16x16x32_bf16 v[176:179], v[116:119], v[188:191], v[176:179]\n\t"
            "v_cvt_pk_bf16_f32 v154, v156, v157\n\t"
            "v_cvt_pk_bf16_f32 v155, v158, v159\n\t"
            "v_mfma_f32_16x16x32_bf16 v[180:183], v[140:143], v[188:191], v[180:183]\n\t"
            "ds_read_b128 v[188:191], v243 offset:6272\n\t"
            "v_cvt_pk_bf16_f32 v160, v160, v161\n\t"
            "v_mfma_f32_16x16x32_bf16 v[180:183], v[136:139], v[184:187], v[180:183]\n\t"
            "ds_read_b128 v[184:187], v242 offset:6272\n\t"
            "v_cvt_pk_bf16_f32 v161, v162, v163\n\t"
            "s_waitcnt lgkmcnt(2)\n\t"
            "v_mfma_f32_16x16x32_bf16 v[144:147], v[0:3], v[192:195], v[144:147]\n\t"
            "v_cvt_pk_bf16_f32 v176, v176, v177\n\t"
            "v_cvt_pk_bf16_f32 v177, v178, v179\n\t"
            "v_mfma_f32_16x16x32_bf16 v[144:147], v[4:7], v[172:175], v[144:147]\n\t"
            "v_cvt_pk_bf16_f32 v162, v164, v165\n\t"
            "v_cvt_pk_bf16_f32 v163, v166, v167\n\t"
            "v_mfma_f32_16x16x32_bf16 v[148:151], v[28:31], v[172:175], v[148:151]\n\t"
            "v_cvt_pk_bf16_f32 v178, v180, v181\n\t"
            "v_cvt_pk_bf16_f32 v179, v182, v183\n\t"
            "v_mfma_f32_16x16x32_bf16 v[148:151], v[24:27], v[192:195], v[148:151]\n\t"
            "v_lshlrev_b32 v156, 16, v218\n\t"
            "v_and_b32 v157, 0xffff0000, v218\n\t"
            "v_mfma_f32_16x16x32_bf16 v[168:171], v[48:51], v[192:195], v[168:171]\n\t"
            "v_lshlrev_b32 v158, 16, v219\n\t"
            "v_and_b32 v159, 0xffff0000, v219\n\t"
            "v_mfma_f32_16x16x32_bf16 v[168:171], v[52:55], v[172:175], v[168:171]\n\t"
            "ds_read_b128 v[216:219], v237 offset:6144\n\t"
            "v_lshlrev_b32 v164, 16, v220\n\t"
            "v_mfma_f32_16x16x32_bf16 v[156:159], v[76:79], v[172:175], v[156:159]\n\t"
            "v_and_b32 v165, 0xffff0000, v220\n\t"
            "v_lshlrev_b32 v166, 16, v221\n\t"
            "v_mfma_f32_16x16x32_bf16 v[156:159], v[72:75], v[192:195], v[156:159]\n\t"
            "v_and_b32 v167, 0xffff0000, v221\n\t"
            "v_lshlrev_b32 v180, 16, v222\n\t"
            "v_and_b32 v181, 0xffff0000, v222\n\t"
            "v_mfma_f32_16x16x32_bf16 v[164:167], v[96:99], v[192:195], v[164:167]\n\t"
            "v_lshlrev_b32 v182, 16, v223\n\t"
            "v_and_b32 v183, 0xffff0000, v223\n\t"
            "v_mfma_f32_16x16x32_bf16 v[164:167], v[100:103], v[172:175], v[164:167]\n\t"
            "ds_read_b128 v[220:223], v248 offset:6144\n\t"
            "buffer_store_dwordx4 v[152:155], v196, s[40:43], s45 offen sc1\n\t"
            "v_mfma_f32_16x16x32_bf16 v[180:183], v[124:127], v[172:175], v[180:183]\n\t"
            "ds_read_b128 v[172:175], v243 offset:6400\n\t"
            "v_lshlrev_b32 v152, 16, v224\n\t"
            "v_mfma_f32_16x16x32_bf16 v[180:183], v[120:123], v[192:195], v[180:183]\n\t"
            "ds_read_b128 v[192:195], v242 offset:6400\n\t"
            "buffer_store_dwordx4 v[160:163], v196, s[40:43], s45 offen offset:64 sc1\n\t"
            "s_waitcnt lgkmcnt(4)\n\t"
            "v_mfma_f32_16x16x32_bf16 v[144:147], v[8:11], v[184:187], v[144:147]\n\t"
            "v_and_b32 v153, 0xffff0000, v224\n\t"
            "v_lshlrev_b32 v154, 16, v225\n\t"
            "v_mfma_f32_16x16x32_bf16 v[144:147], v[12:15], v[188:191], v[144:147]\n\t"
            "buffer_store_dwordx4 v[176:179], v196, s[40:43], s45 offen offset:128 sc1\n\t"
            "v_mfma_f32_16x16x32_bf16 v[148:151], v[36:39], v[188:191], v[148:151]\n\t"
            "v_and_b32 v155, 0xffff0000, v225\n\t"
            "v_lshlrev_b32 v160, 16, v226\n\t"
            "v_mfma_f32_16x16x32_bf16 v[148:151], v[32:35], v[184:187], v[148:151]\n\t"
            "v_and_b32 v161, 0xffff0000, v226\n\t"
            "v_lshlrev_b32 v162, 16, v227\n\t"
            "v_mfma_f32_16x16x32_bf16 v[168:171], v[56:59], v[184:187], v[168:171]\n\t"
            "v_and_b32 v163, 0xffff0000, v227\n\t"
            "v_lshlrev_b32 v176, 16, v228\n\t"
            "v_mfma_f32_16x16x32_bf16 v[168:171], v[60:63], v[188:191], v[168:171]\n\t"
            "ds_read_b128 v[224:227], v236 offset:12288\n\t"
            "v_and_b32 v177, 0xffff0000, v228\n\t"
            "v_mfma_f32_16x16x32_bf16 v[156:159], v[84:87], v[188:191], v[156:159]\n\t"
            "v_lshlrev_b32 v178, 16, v229\n\t"
            "v_and_b32 v179, 0xffff0000, v229\n\t"
            "v_mfma_f32_16x16x32_bf16 v[156:159], v[80:83], v[184:187], v[156:159]\n\t"
            "v_mfma_f32_16x16x32_bf16 v[164:167], v[104:107], v[184:187], v[164:167]\n\t"
            "v_mfma_f32_16x16x32_bf16 v[164:167], v[108:111], v[188:191], v[164:167]\n\t"
            "v_mfma_f32_16x16x32_bf16 v[180:183], v[132:135], v[188:191], v[180:183]\n\t"
            "ds_read_b128 v[188:191], v243 offset:12288\n\t"
            "v_mfma_f32_16x16x32_bf16 v[180:183], v[128:131], v[184:187], v[180:183]\n\t"
            "ds_read_b128 v[184:187], v242 offset:12288\n\t"
            "s_waitcnt lgkmcnt(3)\n\t"
            "v_mfma_f32_16x16x32_bf16 v[144:147], v[16:19], v[192:195], v[144:147]\n\t"
            "v_mfma_f32_16x16x32_bf16 v[144:147], v[20:23], v[172:175], v[144:147]\n\t"
            "v_mfma_f32_16x16x32_bf16 v[168:171], v[64:67], v[192:195], v[168:171]\n\t"
            "v_mfma_f32_16x16x32_bf16 v[168:171], v[68:71], v[172:175], v[168:171]\n\t"
            "v_mfma_f32_16x16x32_bf16 v[164:167], v[112:115], v[192:195], v[164:167]\n\t"
            "v_mfma_f32_16x16x32_bf16 v[164:167], v[116:119], v[172:175], v[164:167]\n\t"
            "v_mfma_f32_16x16x32_bf16 v[148:151], v[44:47], v[172:175], v[148:151]\n\t"
            "v_mfma_f32_16x16x32_bf16 v[148:151], v[40:43], v[192:195], v[148:151]\n\t"
            "s_nop 1\n\t"
            "v_cvt_pk_bf16_f32 v144, v144, v145\n\t"
            "v_mfma_f32_16x16x32_bf16 v[156:159], v[92:95], v[172:175], v[156:159]\n\t"
            "v_cvt_pk_bf16_f32 v145, v146, v147\n\t"
            "v_mfma_f32_16x16x32_bf16 v[156:159], v[88:91], v[192:195], v[156:159]\n\t"
            "v_cvt_pk_bf16_f32 v168, v168, v169\n\t"
            "v_cvt_pk_bf16_f32 v169, v170, v171\n\t"
            "v_mfma_f32_16x16x32_bf16 v[180:183], v[140:143], v[172:175], v[180:183]\n\t"
            "ds_read_b128 v[172:175], v243 offset:12416\n\t"
            "v_cvt_pk_bf16_f32 v164, v164, v165\n\t"
            "v_mfma_f32_16x16x32_bf16 v[180:183], v[136:139], v[192:195], v[180:183]\n\t"
            "ds_read_b128 v[192:195], v242 offset:12416\n\t"
            "v_cvt_pk_bf16_f32 v165, v166, v167\n\t"
            "s_waitcnt lgkmcnt(2)\n\t"
            "v_mfma_f32_16x16x32_bf16 v[152:155], v[0:3], v[184:187], v[152:155]\n\t"
            "v_cvt_pk_bf16_f32 v146, v148, v149\n\t"
            "v_cvt_pk_bf16_f32 v147, v150, v151\n\t"
            "v_mfma_f32_16x16x32_bf16 v[152:155], v[4:7], v[188:191], v[152:155]\n\t"
            "v_cvt_pk_bf16_f32 v170, v156, v157\n\t"
            "v_cvt_pk_bf16_f32 v171, v158, v159\n\t"
            "v_mfma_f32_16x16x32_bf16 v[160:163], v[28:31], v[188:191], v[160:163]\n\t"
            "v_cvt_pk_bf16_f32 v166, v180, v181\n\t"
            "v_cvt_pk_bf16_f32 v167, v182, v183\n\t"
            "v_mfma_f32_16x16x32_bf16 v[160:163], v[24:27], v[184:187], v[160:163]\n\t"
            "v_lshlrev_b32 v148, 16, v230\n\t"
            "v_and_b32 v149, 0xffff0000, v230\n\t"
            "v_mfma_f32_16x16x32_bf16 v[176:179], v[48:51], v[184:187], v[176:179]\n\t"
            "v_lshlrev_b32 v150, 16, v231\n\t"
            "v_and_b32 v151, 0xffff0000, v231\n\t"
            "v_mfma_f32_16x16x32_bf16 v[176:179], v[52:55], v[188:191], v[176:179]\n\t"
            "ds_read_b128 v[228:231], v237 offset:12288\n\t"
            "v_lshlrev_b32 v156, 16, v232\n\t"
            "v_mfma_f32_16x16x32_bf16 v[148:151], v[76:79], v[188:191], v[148:151]\n\t"
            "v_and_b32 v157, 0xffff0000, v232\n\t"
            "v_lshlrev_b32 v158, 16, v233\n\t"
            "v_mfma_f32_16x16x32_bf16 v[148:151], v[72:75], v[184:187], v[148:151]\n\t"
            "v_and_b32 v159, 0xffff0000, v233\n\t"
            "v_lshlrev_b32 v180, 16, v234\n\t"
            "v_and_b32 v181, 0xffff0000, v234\n\t"
            "v_mfma_f32_16x16x32_bf16 v[156:159], v[96:99], v[184:187], v[156:159]\n\t"
            "v_lshlrev_b32 v182, 16, v235\n\t"
            "v_and_b32 v183, 0xffff0000, v235\n\t"
            "v_mfma_f32_16x16x32_bf16 v[156:159], v[100:103], v[188:191], v[156:159]\n\t"
            "ds_read_b128 v[232:235], v248 offset:12288\n\t"
            "buffer_store_dwordx4 v[144:147], v197, s[40:43], s45 offen sc1\n\t"
            "v_mfma_f32_16x16x32_bf16 v[180:183], v[124:127], v[188:191], v[180:183]\n\t"
            "ds_read_b128 v[188:191], v243 offset:12544\n\t"
            "v_mfma_f32_16x16x32_bf16 v[180:183], v[120:123], v[184:187], v[180:183]\n\t"
            "ds_read_b128 v[184:187], v242 offset:12544\n\t"
            "buffer_store_dwordx4 v[168:171], v197, s[40:43], s45 offen offset:64 sc1\n\t"
            "s_waitcnt lgkmcnt(4)\n\t"
            "v_mfma_f32_16x16x32_bf16 v[152:155], v[8:11], v[192:195], v[152:155]\n\t"
            "v_mfma_f32_16x16x32_bf16 v[152:155], v[12:15], v[172:175], v[152:155]\n\t"
            "buffer_store_dwordx4 v[164:167], v197, s[40:43], s45 offen offset:128 sc1\n\t"
            "v_mfma_f32_16x16x32_bf16 v[160:163], v[36:39], v[172:175], v[160:163]\n\t"
            "v_mfma_f32_16x16x32_bf16 v[160:163], v[32:35], v[192:195], v[160:163]\n\t"
            "s_waitcnt lgkmcnt(0)\n\t"
            "s_barrier\n\t"
            "s_setprio 3\n\t"
            "v_mfma_f32_16x16x32_bf16 v[176:179], v[56:59], v[192:195], v[176:179]\n\t"
            "v_mfma_f32_16x16x32_bf16 v[176:179], v[60:63], v[172:175], v[176:179]\n\t"
            "v_mfma_f32_16x16x32_bf16 v[148:151], v[84:87], v[172:175], v[148:151]\n\t"
            "v_mfma_f32_16x16x32_bf16 v[148:151], v[80:83], v[192:195], v[148:151]\n\t"
            "v_mfma_f32_16x16x32_bf16 v[156:159], v[104:107], v[192:195], v[156:159]\n\t"
            "v_mfma_f32_16x16x32_bf16 v[156:159], v[108:111], v[172:175], v[156:159]\n\t"
            "v_mfma_f32_16x16x32_bf16 v[152:155], v[16:19], v[184:187], v[152:155]\n\t"
            "v_mfma_f32_16x16x32_bf16 v[152:155], v[20:23], v[188:191], v[152:155]\n\t"
            "v_mfma_f32_16x16x32_bf16 v[160:163], v[44:47], v[188:191], v[160:163]\n\t"
            "v_mfma_f32_16x16x32_bf16 v[160:163], v[40:43], v[184:187], v[160:163]\n\t"
            "v_mfma_f32_16x16x32_bf16 v[180:183], v[132:135], v[172:175], v[180:183]\n\t"
            "v_mfma_f32_16x16x32_bf16 v[180:183], v[128:131], v[192:195], v[180:183]\n\t"
            "v_mfma_f32_16x16x32_bf16 v[176:179], v[64:67], v[184:187], v[176:179]\n\t"
            "v_mfma_f32_16x16x32_bf16 v[176:179], v[68:71], v[188:191], v[176:179]\n\t"
            "s_nop 1\n\t"
            "v_cvt_pk_bf16_f32 v152, v152, v153\n\t"
            "v_mfma_f32_16x16x32_bf16 v[148:151], v[92:95], v[188:191], v[148:151]\n\t"
            "v_cvt_pk_bf16_f32 v153, v154, v155\n\t"
            "v_mfma_f32_16x16x32_bf16 v[148:151], v[88:91], v[184:187], v[148:151]\n\t"
            "v_cvt_pk_bf16_f32 v154, v160, v161\n\t"
            "v_cvt_pk_bf16_f32 v155, v162, v163\n\t"
            "v_mfma_f32_16x16x32_bf16 v[156:159], v[112:115], v[184:187], v[156:159]\n\t"
            "v_cvt_pk_bf16_f32 v176, v176, v177\n\t"
            "v_cvt_pk_bf16_f32 v177, v178, v179\n\t"
            "v_mfma_f32_16x16x32_bf16 v[156:159], v[116:119], v[188:191], v[156:159]\n\t"
            "buffer_store_dwordx4 v[152:155], v198, s[40:43], s45 offen sc1\n\t"
            "v_mfma_f32_16x16x32_bf16 v[180:183], v[140:143], v[188:191], v[180:183]\n\t"
            "v_cvt_pk_bf16_f32 v178, v148, v149\n\t"
            "v_cvt_pk_bf16_f32 v179, v150, v151\n\t"
            "v_mfma_f32_16x16x32_bf16 v[180:183], v[136:139], v[184:187], v[180:183]\n\t"
            "buffer_store_dwordx4 v[176:179], v198, s[40:43], s45 offen offset:64 sc1\n\t"
            "s_nop 1\n\t"
            "v_cvt_pk_bf16_f32 v156, v156, v157\n\t"
            "v_cvt_pk_bf16_f32 v157, v158, v159\n\t"
            "s_nop 2\n\t"
            "v_cvt_pk_bf16_f32 v158, v180, v181\n\t"
            "v_cvt_pk_bf16_f32 v159, v182, v183\n\t"
            "buffer_store_dwordx4 v[156:159], v198, s[40:43], s45 offen offset:128 sc1\n\t"
            "s_add_u32 s45, s46, s27\n\t"
            "s_mov_b32 s46, s47\n\t"
            "s_mov_b32 s47, s68\n\t"
            "s_xor_b32 s48, s48, s71\n\t"
            "s_xor_b32 s67, s67, s71\n\t"
            "s_add_u32 s76, s76, 4\n\t"
            "s_add_u32 s70, s70, 4\n\t"
            "s_lshl_b32 s56, s56, 2\n\t"
            "s_lshr_b32 s49, s26, 30\n\t"
            "s_or_b32 s56, s56, s49\n\t"
            "s_cmp_lt_u32 s46, 0x7000000\n\t"
            "s_cbranch_scc1 .Lwb_loop%=\n\t"
            "s_branch .Lwb_end%=\n\t"
            ".Lwb_spin%=:\n\t"
            "s_cmp_eq_u32 s83, 0\n\t"
            "s_cbranch_scc1 .Lwb_go%=\n\t"
            "s_sub_u32 s83, s83, 1\n\t"
            "s_sleep 2\n\t"
            "v_mov_b32 v247, 0\n\t"
            "ds_read_b32 v247, v247 offset:7856\n\t"
            "s_waitcnt lgkmcnt(0)\n\t"
            "v_readfirstlane_b32 s77, v247\n\t"
            "s_cmp_ge_u32 s77, s70\n\t"
            "s_cbranch_scc0 .Lwb_spin%=\n\t"
            "s_branch .Lwb_go%=\n\t"
            ".Lwb_end%=:\n\t"
            "s_waitcnt lgkmcnt(0)\n\t"
            "s_nop 7\n\t"
            : "={v[0:3]}"(W[0]), "={v[4:7]}"(W[1]), "={v[8:11]}"(W[2]), "={v[12:15]}"(W[3]), "={v[16:19]}"(W[4]), "={v[20:23]}"(W[5]), "={v[24:27]}"(W[6]), "={v[28:31]}"(W[7]), "={v[32:35]}"(W[8]), "={v[36:39]}"(W[9]), "={v[40:43]}"(W[10]), "={v[44:47]}"(W[11]), "={v[48:51]}"(W[12]), "={v[52:55]}"(W[13]), "={v[56:59]}"(W[14]), "={v[60:63]}"(W[15]), "={v[64:67]}"(W[16]), "={v[68:71]}"(W[17]), "={v[72:75]}"(W[18]), "={v[76:79]}"(W[19]), "={v[80:83]}"(W[20]), "={v[84:87]}"(W[21]), "={v[88:91]}"(W[22]), "={v[92:95]}"(W[23]), "={v[96:99]}"(W[24]), "={v[100:103]}"(W[25]), "={v[104:107]}"(W[26]), "={v[108:111]}"(W[27]), "={v[112:115]}"(W[28]), "={v[116:119]}"(W[29]), "={v[120:123]}"(W[30]), "={v[124:127]}"(W[31]), "={v[128:131]}"(W[32]), "={v[132:135]}"(W[33]), "={v[136:139]}"(W[34]), "={v[140:143]}"(W[35]), "={v[200:203]}"(xl[0]), "={v[204:207]}"(xl[1]), "={v[208:211]}"(xl[2]), "={v[212:215]}"(xl[3]), "={v[216:219]}"(xl[4]), "={v[220:223]}"(xl[5]), "={v[224:227]}"(xl[6]), "={v[228:231]}"(xl[7]), "={v[232:235]}"(xl[8]), B_SIO, "+{v247}"(l16_)
            : "{v242}"(oE_), "{v243}"(oO_), "{v244}"(pR0_), "{v245}"(pR1_), "{v246}"(pR2_), "{v250}"(pXw_), "{v251}"(pSTw_), "{v252}"(aCA_), "{v253}"(aB_), "{v254}"(aA_), "{v255}"(idx_), B_SIN
            : "v144", "v145", "v146", "v147", "v148", "v149", "v150", "v151", "v152", "v153", "v154", "v155", "v156", "v157", "v158", "v159", "v160", "v161", "v162", "v163", "v164", "v165", "v166", "v167", "v168", "v169", "v170", "v171", "v172", "v173", "v174", "v175", "v176", "v177", "v178", "v179", "v180", "v181", "v182", "v183", "v184", "v185", "v186", "v187", "v188", "v189", "v190", "v191", "v192", "v193", "v194", "v195", "v196", "v197", "v198", "v199", "v236", "v237", "v248", "v249", B_SCLOB);
            t45 = s45; t56 = s56;
        }
        {
            int ln_;
            asm volatile("v_mbcnt_lo_u32_b32 %0, -1, 0\n\tv_mbcnt_hi_u32_b32 %0, -1, %0" : "=v"(ln_));
            const int l15_ = ln_ & 15;
            const int g_ = ln_ >> 4;
            KARGS
            const auto orsrc = __builtin_amdgcn_make_buffer_rsrc((void*)ob, 0, IMG * IMG * CH * 2, 0x00020000);
#pragma unroll
            for (int lt = 0; lt < 2; ++lt) {
                int i = 16 * lt + l15_;
                const unsigned en = *(const unsigned*)(smem + WL_OFF + i * 4);
                const bool ok = (en & 0x7ffffffu) != 0x7ffffffu;
                i = ok ? i : 0;
                const int lE = OL_OFF + i * 384 + ((g_ ^ ((i >> 1) & 7)) << 4);
                const unsigned o = ok ? (en & 0x7ffffffu) + LPOS(en >> 31) * ROWBYTES + LPOS(en >> 30) * (CH * 2) + 192 * (w - 2) + 16 * g_ : OOB;
                u32x4 rl[3];
#pragma unroll
                for (int k = 0; k < 3; ++k) {
                    const int c8 = 12 * (w - 2) + 4 * k + g_, ws = (c8 * 43) >> 8, r6 = c8 - 6 * ws;
                    rl[k] = *(const u32x4*)(smem + LX_OFF(i) + (r6 >> 1) * 128 + ((2 * ws + (r6 & 1)) << 4));
                }
                if (lt == 1) {
                    const unsigned oE_ = ldsb + O_OFF + l15_ * 384 + ((g_ ^ (l15_ >> 1)) << 4), oO_ = oE_ ^ 64, aCA_ = ldsb + CT_OFF + ln_ * 16;
                    asm volatile(
            "s_waitcnt lgkmcnt(0)\n\t"
            "s_barrier\n\t"
            "s_nop 7\n\t"
            "ds_read_b128 v[184:187], v242\n\t"
            "ds_read_b128 v[188:191], v243\n\t"
            "ds_read_b128 v[192:195], v242 offset:128\n\t"
            "ds_read_b128 v[172:175], v243 offset:128\n\t"
            "ds_read_b128 v[196:199], v252\n\t"
            "v_lshlrev_b32 v152, 16, v200\n\t"
            "v_and_b32 v153, 0xffff0000, v200\n\t"
            "v_lshlrev_b32 v154, 16, v201\n\t"
            "v_and_b32 v155, 0xffff0000, v201\n\t"
            "v_lshlrev_b32 v156, 16, v202\n\t"
            "v_and_b32 v157, 0xffff0000, v202\n\t"
            "v_lshlrev_b32 v158, 16, v203\n\t"
            "v_and_b32 v159, 0xffff0000, v203\n\t"
            "v_lshlrev_b32 v160, 16, v204\n\t"
            "v_and_b32 v161, 0xffff0000, v204\n\t"
            "v_lshlrev_b32 v162, 16, v205\n\t"
            "v_and_b32 v163, 0xffff0000, v205\n\t"
            "v_lshlrev_b32 v164, 16, v206\n\t"
            "v_and_b32 v165, 0xffff0000, v206\n\t"
            "v_lshlrev_b32 v166, 16, v207\n\t"
            "v_and_b32 v167, 0xffff0000, v207\n\t"
            "v_lshlrev_b32 v176, 16, v208\n\t"
            "v_and_b32 v177, 0xffff0000, v208\n\t"
            "v_lshlrev_b32 v178, 16, v209\n\t"
            "v_and_b32 v179, 0xffff0000, v209\n\t"
            "v_lshlrev_b32 v180, 16, v210\n\t"
            "v_and_b32 v181, 0xffff0000, v210\n\t"
            "v_lshlrev_b32 v182, 16, v211\n\t"
            "v_and_b32 v183, 0xffff0000, v211\n\t"
            "v_lshlrev_b32 v144, 16, v212\n\t"
            "v_and_b32 v145, 0xffff0000, v212\n\t"
            "v_lshlrev_b32 v146, 16, v213\n\t"
            "v_and_b32 v147, 0xffff0000, v213\n\t"
            "v_lshlrev_b32 v148, 16, v214\n\t"
            "v_and_b32 v149, 0xffff0000, v214\n\t"
            "v_lshlrev_b32 v150, 16, v215\n\t"
            "v_and_b32 v151, 0xffff0000, v215\n\t"
            "v_lshlrev_b32 v168, 16, v216\n\t"
            "v_and_b32 v169, 0xffff0000, v216\n\t"
            "v_lshlrev_b32 v170, 16, v217\n\t"
            "v_and_b32 v171, 0xffff0000, v217\n\t"
            "s_bfe_u32 s57, s56, 0x20004\n\t"
            "s_lshl_b32 s58, s57, 3\n\t"
            "s_lshr_b32 s58, s55, s58\n\t"
            "s_lshl_b64 s[36:37], s[34:35], s58\n\t"
            "s_bitcmp1_b32 s57, 1\n\t"
            "s_cselect_b32 s58, 1177344, 0\n\t"
            "s_bitcmp1_b32 s57, 0\n\t"
            "s_cselect_b32 s57, 2304, 0\n\t"
            "s_add_u32 s57, s57, s58\n\t"
            "s_waitcnt lgkmcnt(3)\n\t"
            "v_mfma_f32_16x16x32_bf16 v[152:155], v[0:3], v[184:187], v[152:155]\n\t"
            "s_waitcnt lgkmcnt(0)\n\t"
            "s_mov_b64 exec, s[36:37]\n\t"
            "v_add_u32 v196, s57, v196\n\t"
            "s_mov_b64 exec, -1\n\t"
            "v_mfma_f32_16x16x32_bf16 v[152:155], v[4:7], v[188:191], v[152:155]\n\t"
            "v_mfma_f32_16x16x32_bf16 v[156:159], v[28:31], v[188:191], v[156:159]\n\t"
            "v_mfma_f32_16x16x32_bf16 v[156:159], v[24:27], v[184:187], v[156:159]\n\t"
            "v_mfma_f32_16x16x32_bf16 v[160:163], v[48:51], v[184:187], v[160:163]\n\t"
            "v_mfma_f32_16x16x32_bf16 v[160:163], v[52:55], v[188:191], v[160:163]\n\t"
            "v_mfma_f32_16x16x32_bf16 v[164:167], v[76:79], v[188:191], v[164:167]\n\t"
            "v_mfma_f32_16x16x32_bf16 v[164:167], v[72:75], v[184:187], v[164:167]\n\t"
            "v_mfma_f32_16x16x32_bf16 v[176:179], v[96:99], v[184:187], v[176:179]\n\t"
            "v_mfma_f32_16x16x32_bf16 v[176:179], v[100:103], v[188:191], v[176:179]\n\t"
            "v_mfma_f32_16x16x32_bf16 v[180:183], v[124:127], v[188:191], v[180:183]\n\t"
            "ds_read_b128 v[188:191], v243 offset:256\n\t"
            "v_mfma_f32_16x16x32_bf16 v[180:183], v[120:123], v[184:187], v[180:183]\n\t"
            "ds_read_b128 v[184:187], v242 offset:256\n\t"
            "v_mfma_f32_16x16x32_bf16 v[152:155], v[8:11], v[192:195], v[152:155]\n\t"
            "v_mfma_f32_16x16x32_bf16 v[152:155], v[12:15], v[172:175], v[152:155]\n\t"
            "v_mfma_f32_16x16x32_bf16 v[156:159], v[36:39], v[172:175], v[156:159]\n\t"
            "v_mfma_f32_16x16x32_bf16 v[156:159], v[32:35], v[192:195], v[156:159]\n\t"
            "v_mfma_f32_16x16x32_bf16 v[160:163], v[56:59], v[192:195], v[160:163]\n\t"
            "v_mfma_f32_16x16x32_bf16 v[160:163], v[60:63], v[172:175], v[160:163]\n\t"
            "v_mfma_f32_16x16x32_bf16 v[164:167], v[84:87], v[172:175], v[164:167]\n\t"
            "v_mfma_f32_16x16x32_bf16 v[164:167], v[80:83], v[192:195], v[164:167]\n\t"
            "v_mfma_f32_16x16x32_bf16 v[176:179], v[104:107], v[192:195], v[176:179]\n\t"
            "v_mfma_f32_16x16x32_bf16 v[176:179], v[108:111], v[172:175], v[176:179]\n\t"
            "v_mfma_f32_16x16x32_bf16 v[180:183], v[132:135], v[172:175], v[180:183]\n\t"
            "ds_read_b128 v[172:175], v243 offset:6144\n\t"
            "v_mfma_f32_16x16x32_bf16 v[180:183], v[128:131], v[192:195], v[180:183]\n\t"
            "ds_read_b128 v[192:195], v242 offset:6144\n\t"
            "s_waitcnt lgkmcnt(2)\n\t"
            "v_mfma_f32_16x16x32_bf16 v[152:155], v[16:19], v[184:187], v[152:155]\n\t"
            "v_mfma_f32_16x16x32_bf16 v[152:155], v[20:23], v[188:191], v[152:155]\n\t"
            "v_mfma_f32_16x16x32_bf16 v[156:159], v[44:47], v[188:191], v[156:159]\n\t"
            "v_mfma_f32_16x16x32_bf16 v[156:159], v[40:43], v[184:187], v[156:159]\n\t"
            "v_mfma_f32_16x16x32_bf16 v[160:163], v[64:67], v[184:187], v[160:163]\n\t"
            "v_mfma_f32_16x16x32_bf16 v[160:163], v[68:71], v[188:191], v[160:163]\n\t"
            "v_mfma_f32_16x16x32_bf16 v[164:167], v[92:95], v[188:191], v[164:167]\n\t"
            "v_mfma_f32_16x16x32_bf16 v[164:167], v[88:91], v[184:187], v[164:167]\n\t"
            "s_nop 1\n\t"
            "v_cvt_pk_bf16_f32 v152, v152, v153\n\t"
            "v_mfma_f32_16x16x32_bf16 v[176:179], v[112:115], v[184:187], v[176:179]\n\t"
            "v_cvt_pk_bf16_f32 v153, v154, v155\n\t"
            "v_mfma_f32_16x16x32_bf16 v[176:179], v[116:119], v[188:191], v[176:179]\n\t"
            "v_cvt_pk_bf16_f32 v154, v156, v157\n\t"
            "v_cvt_pk_bf16_f32 v155, v158, v159\n\t"
            "v_mfma_f32_16x16x32_bf16 v[180:183], v[140:143], v[188:191], v[180:183]\n\t"
            "ds_read_b128 v[188:191], v243 offset:6272\n\t"
            "v_cvt_pk_bf16_f32 v160, v160, v161\n\t"
            "v_mfma_f32_16x16x32_bf16 v[180:183], v[136:139], v[184:187], v[180:183]\n\t"
            "ds_read_b128 v[184:187], v242 offset:6272\n\t"
            "v_cvt_pk_bf16_f32 v161, v162, v163\n\t"
            "s_waitcnt lgkmcnt(2)\n\t"
            "v_mfma_f32_16x16x32_bf16 v[144:147], v[0:3], v[192:195], v[144:147]\n\t"
            "v_cvt_pk_bf16_f32 v176, v176, v177\n\t"
            "v_cvt_pk_bf16_f32 v177, v178, v179\n\t"
            "v_mfma_f32_16x16x32_bf16 v[144:147], v[4:7], v[172:175], v[144:147]\n\t"
            "v_cvt_pk_bf16_f32 v162, v164, v165\n\t"
            "v_cvt_pk_bf16_f32 v163, v166, v167\n\t"
            "v_mfma_f32_16x16x32_bf16 v[148:151], v[28:31], v[172:175], v[148:151]\n\t"
            "v_cvt_pk_bf16_f32 v178, v180, v181\n\t"
            "v_cvt_pk_bf16_f32 v179, v182, v183\n\t"
            "v_mfma_f32_16x16x32_bf16 v[148:151], v[24:27], v[192:195], v[148:151]\n\t"
            "v_lshlrev_b32 v156, 16, v218\n\t"
            "v_and_b32 v157, 0xffff0000, v218\n\t"
            "v_mfma_f32_16x16x32_bf16 v[168:171], v[48:51], v[192:195], v[168:171]\n\t"
            "v_lshlrev_b32 v158, 16, v219\n\t"
            "v_and_b32 v159, 0xffff0000, v219\n\t"
            "v_mfma_f32_16x16x32_bf16 v[168:171], v[52:55], v[172:175], v[168:171]\n\t"
            "v_lshlrev_b32 v164, 16, v220\n\t"
            "v_and_b32 v165, 0xffff0000, v220\n\t"
            "v_mfma_f32_16x16x32_bf16 v[156:159], v[76:79], v[172:175], v[156:159]\n\t"
            "v_lshlrev_b32 v166, 16, v221\n\t"
            "v_and_b32 v167, 0xffff0000, v221\n\t"
            "v_mfma_f32_16x16x32_bf16 v[156:159], v[72:75], v[192:195], v[156:159]\n\t"
            "v_lshlrev_b32 v180, 16, v222\n\t"
            "v_and_b32 v181, 0xffff0000, v222\n\t"
            "v_mfma_f32_16x16x32_bf16 v[164:167], v[96:99], v[192:195], v[164:167]\n\t"
            "v_lshlrev_b32 v182, 16, v223\n\t"
            "v_and_b32 v183, 0xffff0000, v223\n\t"
            "v_mfma_f32_16x16x32_bf16 v[164:167], v[100:103], v[172:175], v[164:167]\n\t"
            "buffer_store_dwordx4 v[152:155], v196, s[40:43], s45 offen sc1\n\t"
            "v_mfma_f32_16x16x32_bf16 v[180:183], v[124:127], v[172:175], v[180:183]\n\t"
            "ds_read_b128 v[172:175], v243 offset:6400\n\t"
            "v_lshlrev_b32 v152, 16, v224\n\t"
            "v_mfma_f32_16x16x32_bf16 v[180:183], v[120:123], v[192:195], v[180:183]\n\t"
            "ds_read_b128 v[192:195], v242 offset:6400\n\t"
            "buffer_store_dwordx4 v[160:163], v196, s[40:43], s45 offen offset:64 sc1\n\t"
            "s_waitcnt lgkmcnt(2)\n\t"
            "v_mfma_f32_16x16x32_bf16 v[144:147], v[8:11], v[184:187], v[144:147]\n\t"
            "v_and_b32 v153, 0xffff0000, v224\n\t"
            "v_lshlrev_b32 v154, 16, v225\n\t"
            "v_mfma_f32_16x16x32_bf16 v[144:147], v[12:15], v[188:191], v[144:147]\n\t"
            "buffer_store_dwordx4 v[176:179], v196, s[40:43], s45 offen offset:128 sc1\n\t"
            "v_mfma_f32_16x16x32_bf16 v[148:151], v[36:39], v[188:191], v[148:151]\n\t"
            "v_and_b32 v155, 0xffff0000, v225\n\t"
            "v_lshlrev_b32 v160, 16, v226\n\t"
            "v_mfma_f32_16x16x32_bf16 v[148:151], v[32:35], v[184:187], v[148:151]\n\t"
            "v_and_b32 v161, 0xffff0000, v226\n\t"
            "v_lshlrev_b32 v162, 16, v227\n\t"
            "v_mfma_f32_16x16x32_bf16 v[168:171], v[56:59], v[184:187], v[168:171]\n\t"
            "v_and_b32 v163, 0xffff0000, v227\n\t"
            "v_lshlrev_b32 v176, 16, v228\n\t"
            "v_mfma_f32_16x16x32_bf16 v[168:171], v[60:63], v[188:191], v[168:171]\n\t"
            "v_and_b32 v177, 0xffff0000, v228\n\t"
            "v_lshlrev_b32 v178, 16, v229\n\t"
            "v_mfma_f32_16x16x32_bf16 v[156:159], v[84:87], v[188:191], v[156:159]\n\t"
            "v_and_b32 v179, 0xffff0000, v229\n\t"
            "v_mfma_f32_16x16x32_bf16 v[156:159], v[80:83], v[184:187], v[156:159]\n\t"
            "v_mfma_f32_16x16x32_bf16 v[164:167], v[104:107], v[184:187], v[164:167]\n\t"
            "v_mfma_f32_16x16x32_bf16 v[164:167], v[108:111], v[188:191], v[164:167]\n\t"
            "v_mfma_f32_16x16x32_bf16 v[180:183], v[132:135], v[188:191], v[180:183]\n\t"
            "ds_read_b128 v[188:191], v243 offset:12288\n\t"
            "v_mfma_f32_16x16x32_bf16 v[180:183], v[128:131], v[184:187], v[180:183]\n\t"
            "ds_read_b128 v[184:187], v242 offset:12288\n\t"
            "s_waitcnt lgkmcnt(2)\n\t"
            "v_mfma_f32_16x16x32_bf16 v[144:147], v[16:19], v[192:195], v[144:147]\n\t"
            "v_mfma_f32_16x16x32_bf16 v[144:147], v[20:23], v[172:175], v[144:147]\n\t"
            "v_mfma_f32_16x16x32_bf16 v[168:171], v[64:67], v[192:195], v[168:171]\n\t"
            "v_mfma_f32_16x16x32_bf16 v[168:171], v[68:71], v[172:175], v[168:171]\n\t"
            "v_mfma_f32_16x16x32_bf16 v[164:167], v[112:115], v[192:195], v[164:167]\n\t"
            "v_mfma_f32_16x16x32_bf16 v[164:167], v[116:119], v[172:175], v[164:167]\n\t"
            "v_mfma_f32_16x16x32_bf16 v[148:151], v[44:47], v[172:175], v[148:151]\n\t"
            "v_mfma_f32_16x16x32_bf16 v[148:151], v[40:43], v[192:195], v[148:151]\n\t"
            "s_nop 1\n\t"
            "v_cvt_pk_bf16_f32 v144, v144, v145\n\t"
            "v_mfma_f32_16x16x32_bf16 v[156:159], v[92:95], v[172:175], v[156:159]\n\t"
            "v_cvt_pk_bf16_f32 v145, v146, v147\n\t"
            "v_mfma_f32_16x16x32_bf16 v[156:159], v[88:91], v[192:195], v[156:159]\n\t"
            "v_cvt_pk_bf16_f32 v168, v168, v169\n\t"
            "v_cvt_pk_bf16_f32 v169, v170, v171\n\t"
            "v_mfma_f32_16x16x32_bf16 v[180:183], v[140:143], v[172:175], v[180:183]\n\t"
            "ds_read_b128 v[172:175], v243 offset:12416\n\t"
            "v_cvt_pk_bf16_f32 v164, v164, v165\n\t"
            "v_mfma_f32_16x16x32_bf16 v[180:183], v[136:139], v[192:195], v[180:183]\n\t"
            "ds_read_b128 v[192:195], v242 offset:12416\n\t"
            "v_cvt_pk_bf16_f32 v165, v166, v167\n\t"
            "s_waitcnt lgkmcnt(2)\n\t"
            "v_mfma_f32_16x16x32_bf16 v[152:155], v[0:3], v[184:187], v[152:155]\n\t"
            "v_cvt_pk_bf16_f32 v146, v148, v149\n\t"
            "v_cvt_pk_bf16_f32 v147, v150, v151\n\t"
            "v_mfma_f32_16x16x32_bf16 v[152:155], v[4:7], v[188:191], v[152:155]\n\t"
            "v_cvt_pk_bf16_f32 v170, v156, v157\n\t"
            "v_cvt_pk_bf16_f32 v171, v158, v159\n\t"
            "v_mfma_f32_16x16x32_bf16 v[160:163], v[28:31], v[188:191], v[160:163]\n\t"
            "v_cvt_pk_bf16_f32 v166, v180, v181\n\t"
            "v_cvt_pk_bf16_f32 v167, v182, v183\n\t"
            "v_mfma_f32_16x16x32_bf16 v[160:163], v[24:27], v[184:187], v[160:163]\n\t"
            "v_lshlrev_b32 v148, 16, v230\n\t"
            "v_and_b32 v149, 0xffff0000, v230\n\t"
            "v_mfma_f32_16x16x32_bf16 v[176:179], v[48:51], v[184:187], v[176:179]\n\t"
            "v_lshlrev_b32 v150, 16, v231\n\t"
            "v_and_b32 v151, 0xffff0000, v231\n\t"
            "v_mfma_f32_16x16x32_bf16 v[176:179], v[52:55], v[188:191], v[176:179]\n\t"
            "v_lshlrev_b32 v156, 16, v232\n\t"
            "v_and_b32 v157, 0xffff0000, v232\n\t"
            "v_mfma_f32_16x16x32_bf16 v[148:151], v[76:79], v[188:191], v[148:151]\n\t"
            "v_lshlrev_b32 v158, 16, v233\n\t"
            "v_and_b32 v159, 0xffff0000, v233\n\t"
            "v_mfma_f32_16x16x32_bf16 v[148:151], v[72:75], v[184:187], v[148:151]\n\t"
            "v_lshlrev_b32 v180, 16, v234\n\t"
            "v_and_b32 v181, 0xffff0000, v234\n\t"
            "v_mfma_f32_16x16x32_bf16 v[156:159], v[96:99], v[184:187], v[156:159]\n\t"
            "v_lshlrev_b32 v182, 16, v235\n\t"
            "v_and_b32 v183, 0xffff0000, v235\n\t"
            "v_mfma_f32_16x16x32_bf16 v[156:159], v[100:103], v[188:191], v[156:159]\n\t"
            "buffer_store_dwordx4 v[144:147], v197, s[40:43], s45 offen sc1\n\t"
            "v_mfma_f32_16x16x32_bf16 v[180:183], v[124:127], v[188:191], v[180:183]\n\t"
            "ds_read_b128 v[188:191], v243 offset:12544\n\t"
            "v_mfma_f32_16x16x32_bf16 v[180:183], v[120:123], v[184:187], v[180:183]\n\t"
            "ds_read_b128 v[184:187], v242 offset:12544\n\t"
            "buffer_store_dwordx4 v[168:171], v197, s[40:43], s45 offen offset:64 sc1\n\t"
            "s_waitcnt lgkmcnt(2)\n\t"
            "v_mfma_f32_16x16x32_bf16 v[152:155], v[8:11], v[192:195], v[152:155]\n\t"
            "v_mfma_f32_16x16x32_bf16 v[152:155], v[12:15], v[172:175], v[152:155]\n\t"
            "buffer_store_dwordx4 v[164:167], v197, s[40:43], s45 offen offset:128 sc1\n\t"
            "v_mfma_f32_16x16x32_bf16 v[160:163], v[36:39], v[172:175], v[160:163]\n\t"
            "v_mfma_f32_16x16x32_bf16 v[160:163], v[32:35], v[192:195], v[160:163]\n\t"
            "v_mfma_f32_16x16x32_bf16 v[176:179], v[56:59], v[192:195], v[176:179]\n\t"
            "v_mfma_f32_16x16x32_bf16 v[176:179], v[60:63], v[172:175], v[176:179]\n\t"
            "v_mfma_f32_16x16x32_bf16 v[148:151], v[84:87], v[172:175], v[148:151]\n\t"
            "v_mfma_f32_16x16x32_bf16 v[148:151], v[80:83], v[192:195], v[148:151]\n\t"
            "v_mfma_f32_16x16x32_bf16 v[156:159], v[104:107], v[192:195], v[156:159]\n\t"
            "v_mfma_f32_16x16x32_bf16 v[156:159], v[108:111], v[172:175], v[156:159]\n\t"
            "s_waitcnt lgkmcnt(0)\n\t"
            "v_mfma_f32_16x16x32_bf16 v[152:155], v[16:19], v[184:187], v[152:155]\n\t"
            "v_mfma_f32_16x16x32_bf16 v[152:155], v[20:23], v[188:191], v[152:155]\n\t"
            "v_mfma_f32_16x16x32_bf16 v[160:163], v[44:47], v[188:191], v[160:163]\n\t"
            "v_mfma_f32_16x16x32_bf16 v[160:163], v[40:43], v[184:187], v[160:163]\n\t"
            "v_mfma_f32_16x16x32_bf16 v[180:183], v[132:135], v[172:175], v[180:183]\n\t"
            "v_mfma_f32_16x16x32_bf16 v[180:183], v[128:131], v[192:195], v[180:183]\n\t"
            "v_mfma_f32_16x16x32_bf16 v[176:179], v[64:67], v[184:187], v[176:179]\n\t"
            "v_mfma_f32_16x16x32_bf16 v[176:179], v[68:71], v[188:191], v[176:179]\n\t"
            "s_nop 1\n\t"
            "v_cvt_pk_bf16_f32 v152, v152, v153\n\t"
            "v_mfma_f32_16x16x32_bf16 v[148:151], v[92:95], v[188:191], v[148:151]\n\t"
            "v_cvt_pk_bf16_f32 v153, v154, v155\n\t"
            "v_mfma_f32_16x16x32_bf16 v[148:151], v[88:91], v[184:187], v[148:151]\n\t"
            "v_cvt_pk_bf16_f32 v154, v160, v161\n\t"
            "v_cvt_pk_bf16_f32 v155, v162, v163\n\t"
            "v_mfma_f32_16x16x32_bf16 v[156:159], v[112:115], v[184:187], v[156:159]\n\t"
            "v_cvt_pk_bf16_f32 v176, v176, v177\n\t"
            "v_cvt_pk_bf16_f32 v177, v178, v179\n\t"
            "v_mfma_f32_16x16x32_bf16 v[156:159], v[116:119], v[188:191], v[156:159]\n\t"
            "buffer_store_dwordx4 v[152:155], v198, s[40:43], s45 offen sc1\n\t"
            "v_mfma_f32_16x16x32_bf16 v[180:183], v[140:143], v[188:191], v[180:183]\n\t"
            "v_cvt_pk_bf16_f32 v178, v148, v149\n\t"
            "v_cvt_pk_bf16_f32 v179, v150, v151\n\t"
            "v_mfma_f32_16x16x32_bf16 v[180:183], v[136:139], v[184:187], v[180:183]\n\t"
            "buffer_store_dwordx4 v[176:179], v198, s[40:43], s45 offen offset:64 sc1\n\t"
            "s_nop 1\n\t"
            "v_cvt_pk_bf16_f32 v156, v156, v157\n\t"
            "v_cvt_pk_bf16_f32 v157, v158, v159\n\t"
            "s_nop 2\n\t"
            "v_cvt_pk_bf16_f32 v158, v180, v181\n\t"
            "v_cvt_pk_bf16_f32 v159, v182, v183\n\t"
            "buffer_store_dwordx4 v[156:159], v198, s[40:43], s45 offen offset:128 sc1\n\t"
            "s_nop 7\n\t"
                    :: "{v[0:3]}"(W[0]), "{v[4:7]}"(W[1]), "{v[8:11]}"(W[2]), "{v[12:15]}"(W[3]), "{v[16:19]}"(W[4]), "{v[20:23]}"(W[5]), "{v[24:27]}"(W[6]), "{v[28:31]}"(W[7]), "{v[32:35]}"(W[8]), "{v[36:39]}"(W[9]), "{v[40:43]}"(W[10]), "{v[44:47]}"(W[11]), "{v[48:51]}"(W[12]), "{v[52:55]}"(W[13]), "{v[56:59]}"(W[14]), "{v[60:63]}"(W[15]), "{v[64:67]}"(W[16]), "{v[68:71]}"(W[17]), "{v[72:75]}"(W[18]), "{v[76:79]}"(W[19]), "{v[80:83]}"(W[20]), "{v[84:87]}"(W[21]), "{v[88:91]}"(W[22]), "{v[92:95]}"(W[23]), "{v[96:99]}"(W[24]), "{v[100:103]}"(W[25]), "{v[104:107]}"(W[26]), "{v[108:111]}"(W[27]), "{v[112:115]}"(W[28]), "{v[116:119]}"(W[29]), "{v[120:123]}"(W[30]), "{v[124:127]}"(W[31]), "{v[128:131]}"(W[32]), "{v[132:135]}"(W[33]), "{v[136:139]}"(W[34]), "{v[140:143]}"(W[35]), "{v[200:203]}"(xl[0]), "{v[204:207]}"(xl[1]), "{v[208:211]}"(xl[2]), "{v[212:215]}"(xl[3]), "{v[216:219]}"(xl[4]), "{v[220:223]}"(xl[5]), "{v[224:227]}"(xl[6]), "{v[228:231]}"(xl[7]), "{v[232:235]}"(xl[8]), "{v242}"(oE_), "{v243}"(oO_), "{v252}"(aCA_),
                       "{s[40:43]}"(orr), "{s45}"(t45), "{s56}"(t56), "{s55}"(CROW & 0x0F0F0F0Fu), "{s[34:35]}"(0x0001000100010001ull)
                    : "v144", "v145", "v146", "v147", "v148", "v149", "v150", "v151", "v152", "v153", "v154", "v155", "v156", "v157", "v158", "v159", "v160", "v161", "v162", "v163", "v164", "v165", "v166", "v167", "v168", "v169", "v170", "v171", "v172", "v173", "v174", "v175", "v176", "v177", "v178", "v179", "v180", "v181", "v182", "v183", "v184", "v185", "v186", "v187", "v188", "v189", "v190", "v191", "v192", "v193", "v194", "v195", "v196", "v197", "v198", "v199", "s36", "s37", "s57", "s58", "scc", "memory");
                }
                f32x4 acc[6];
#pragma unroll
                for (int ks = 0; ks < 6; ++ks) {
                    const bf16x8 of = *(const bf16x8*)(smem + ((ks & 1) ? (lE ^ 64) : lE) + (ks >> 1) * 128);
#pragma unroll
                    for (int j = 0; j < 6; ++j) acc[j] = MFMA32(W[j * 6 + ks], of, ks == 0 ? ZERO4 : acc[j]);
                }
#pragma unroll
                for (int k = 0; k < 3; ++k) {
                    const f32x4 a = acc[2 * k], b = acc[2 * k + 1];
                    const u32x4 ov = {pk2(a[0] + bflo(rl[k][0]), a[1] + bfhi(rl[k][0])), pk2(a[2] + bflo(rl[k][1]), a[3] + bfhi(rl[k][1])),
                                      pk2(b[0] + bflo(rl[k][2]), b[1] + bfhi(rl[k][2])), pk2(b[2] + bflo(rl[k][3]), b[3] + bfhi(rl[k][3]))};
                    __builtin_amdgcn_raw_buffer_store_b128(ov, orsrc, o + 64 * k, 0, SC1);
                }
                SB();
            }
        }
        __builtin_amdgcn_endpgm();
    }
}

extern "C" void kernel_launch(void* const* d_in, const int* in_sizes, int n_in,
                              void* d_out, int out_size, void* d_ws, size_t ws_size,
                              hipStream_t stream) {
    const unsigned short* x = (const unsigned short*)d_in[0];
    const unsigned short* dw = (const unsigned short*)d_in[1];
    const unsigned short* wqkv = (const unsigned short*)d_in[2];
    const unsigned short* wo = (const unsigned short*)d_in[3];
    unsigned short* out = (unsigned short*)d_out;
    u32x4* wf = (u32x4*)d_ws;
    win_attn_kernel<<<GRID, 512, 0, stream>>>(x, dw, wqkv, wo, wf, out);
}
```

```cpp
#include <hip/hip_runtime.h>
#include <hip/hip_bf16.h>
#include <stdint.h>

typedef short bf16x8 __attribute__((ext_vector_type(8)));
typedef float f32x4 __attribute__((ext_vector_type(4)));
typedef float f32x2 __attribute__((ext_vector_type(2)));
typedef unsigned u32x2 __attribute__((ext_vector_type(2)));
typedef unsigned u32x4 __attribute__((ext_vector_type(4)));

constexpr int IMG = 511;
constexpr int CH = 192;
constexpr int NWX = 73;
constexpr int NWIN = NWX * NWX;
constexpr int GRID = 256;
constexpr int ROWBYTES = IMG * CH * 2;
constexpr int XBIAS = (IMG + 1) * CH * 2;
constexpr unsigned OOB = 0x40000000u;

constexpr int FLAG_OFF = 7856;
constexpr int X_OFF = 32768;
constexpr int X1_OFF = 53632;
constexpr int O_OFF = 74496;
constexpr int V_OFF = 92928;
constexpr int KQL_OFF = 111360;
constexpr int OL_OFF = 128256;
constexpr int AT_OFF = 137472;
constexpr int CT_OFF = AT_OFF + 19584;
constexpr int RS_OFF = CT_OFF + 4096;
constexpr int WL_OFF = RS_OFF + 2304 + 64;
constexpr int LDS_BYTES = WL_OFF + 128;
#define RS_BASE (RS_OFF + (wave < 4 ? wave * 384 : 768 + wave * 192))
constexpr int STL_OFF = O_OFF + 12288;
constexpr int DW_OFF = O_OFF + 8448;
#define LX_OFF(i) ((i) < 2 ? CT_OFF + 1024 + (i) * 384 : KQL_OFF + ((i) - 2) * 800)
constexpr int PF_OFF = CT_OFF + 2048;
constexpr int PSLEEP = 4;
#define LB_OFF (w < 3 ? V_OFF + w * 13952 : X1_OFF)
#define LPOS(wc) (((wc) & 1) ? 0 : 6)
constexpr unsigned CROW = 0x060a0230u;
constexpr unsigned CLANE = 0x00132C3Fu;

constexpr unsigned long long TOKMAP[6] = {0x20001c190f2a2202ull, 0x052517121f061a13ull, 0x181e08151d0b260cull, 0x1b2b2903102d0901ull, 0x04162e0e2824142cull, 0x110a072127232f0dull};
constexpr unsigned long long ROWMAP[8] = {0x08321b240f1c0006ull, 0x23310e2a09170d14ull, 0x210c2f030a273418ull, 0x1d2b11221f043739ull, 0x3234363a103e192dull, 0x291a20022c3f1605ull, 0x303a3c2628122e15ull, 0x39253d1e01070b13ull};

constexpr int WF_BYTES = 8 * 36 * 1024;
constexpr int FLG_OFF = WF_BYTES;
constexpr int ARR_OFF = FLG_OFF + 512;
constexpr int EPO_OFF = ARR_OFF + 8192;
constexpr int WS_BYTES = EPO_OFF + 128;
constexpr int SC1 = 16;

#define MFMA32(a, b, c) __builtin_amdgcn_mfma_f32_16x16x32_bf16((a), (b), (c), 0, 0, 0)
#define BC8(v) __builtin_bit_cast(bf16x8, (v))
#define BAR() asm volatile("s_waitcnt lgkmcnt(0)\n\ts_barrier" ::: "memory")
#define SB() __builtin_amdgcn_sched_barrier(0)
#define OPQ(x) asm volatile("" : "+v"(x))
#define ZERO4 ((f32x4){0.f, 0.f, 0.f, 0.f})

typedef __bf16 nbf16x2 __attribute__((ext_vector_type(2)));
__device__ __forceinline__ unsigned pk2(float a, float b) {
    f32x2 v = {a, b};
    nbf16x2 r = __builtin_convertvector(v, nbf16x2);
    return __builtin_bit_cast(unsigned, r);
}
__device__ __forceinline__ float bflo(unsigned u) { return __uint_as_float(u << 16); }
__device__ __forceinline__ float bfhi(unsigned u) { return __uint_as_float(u & 0xffff0000u); }

__device__ __forceinline__ float bf2f(unsigned short v) { return __uint_as_float((unsigned)v << 16); }
__device__ __forceinline__ unsigned byte_of(unsigned long long a, unsigned long long b, int i) {
    return (unsigned)(((i & 8) ? b : a) >> (8 * (i & 7))) & 0xFFu;
}
#define M24(a, b) ((unsigned)__mul24((int)(a), (int)(b)))
__device__ __forceinline__ void seq_to_win(int seq, int& wy, int& wx) {
    const bool last = seq >= 5110;
    const int s = last ? 35 : M24(seq, 28729) >> 22, r = seq - M24(146, s);
    const int kx = last ? M24(r, 171) >> 10 : r >> 2, pos = r - M24(last ? 6 : 4, kx);
    const bool low = last && pos >= (kx == 36 ? 2 : 4);
    wy = low ? 72 : 2 * s + (pos & 1);
    wx = 2 * kx + (low ? (kx == 36 ? 0 : pos - 4) : pos >> 1);
}
__device__ __forceinline__ int win_to_seq(int wy, int wx) {
    const int kx = wx >> 1;
    return M24(146, wy == 72 ? 35 : wy >> 1) + M24(wy >= 70 ? 6 : 4, kx) + (wy == 72 ? (kx == 36 ? 2 : 4 + (wx & 1)) : 2 * (wx & 1) + (wy & 1));
}
__device__ __forceinline__ int block_of(int wy, int wx) { return wy >= 70 ? 1295 + (wx & ~1) + (wy == 72) : M24(37, wy >> 1) + (wx >> 1); }
__device__ __forceinline__ void block_pos(int b, int& ky, int& kx) {
    const int s = M24(b, 1772) >> 16, l = b - 1295;
    kx = l >= 0 ? l >> 1 : b - M24(37, s);
    ky = l >= 0 ? 70 + 2 * (l & 1) : 2 * s;
}
__device__ __forceinline__ unsigned win_org(int wy, int wx) { return M24(wy, 7 * ROWBYTES) + M24(wx, 7 * CH * 2); }
__device__ __forceinline__ unsigned wl_entry(int wy, int wx) {
    return win_org(wy, wx) | (wy == 0 ? 0x08000000u : 0u) | (wx == 0 ? 0x10000000u : 0u) |
           (wx == NWX - 1 ? 0x20000000u : 0u) | ((unsigned)(wx & 1) << 30) | ((unsigned)(wy & 1) << 31);
}
__device__ __forceinline__ unsigned dma_off(int lane) {
    const int col = (lane * 43) >> 8;
    return (unsigned)(col * (CH * 2 - 96) + 16 * lane) | (col == 0 ? 0x10000000u : 0u) | (col == 8 ? 0x20000000u : 0u);
}

#if defined(__HIP_DEVICE_COMPILE__)
#define NOPK __attribute__((target("no-packed-fp32-ops")))
#else
#define NOPK
#endif
__global__ __launch_bounds__(512, 2) NOPK void win_attn_kernel(const unsigned short* __restrict__ x,
                                                          const unsigned short* __restrict__ dw, const unsigned short* __restrict__ wqkv,
                                                          const unsigned short* __restrict__ wo, u32x4* wf,
                                                          unsigned short* __restrict__ out) {
    __shared__ __attribute__((aligned(16))) char smem[LDS_BYTES];

    asm volatile("wk_code_begin:");
    const int tid = threadIdx.x;
    const int wave = __builtin_amdgcn_readfirstlane(tid >> 6);
    const int w = wave & 3;
    const unsigned ldsb = (unsigned)(size_t)smem;
    {
        unsigned long long pc;
        unsigned len, rel;
        asm volatile("s_getpc_b64 %0\n.Lwk_here%=:\n\ts_mov_b32 %1, wk_code_end-wk_code_begin\n\ts_mov_b32 %2, .Lwk_here%=-wk_code_begin" : "=s"(pc), "=s"(len), "=s"(rel));
        pc -= rel;
        const u32x4 cr = {(unsigned)pc, (unsigned)(pc >> 32) & 0xffffu, len, 0x00020000u};
        asm volatile("s_mov_b32 m0, %0\n\ts_nop 0\n\tbuffer_load_dwordx4 %1, %2, %3 offen lds"
                     :: "s"(ldsb + X1_OFF + 14336 + w * 1024), "v"((tid & 63) * 16), "s"(cr), "s"((wave * 32 + (blockIdx.x >> 3)) * 1024) : "m0", "memory");
    }
    asm volatile("" :: "s"(x), "s"(dw), "s"(wqkv), "s"(wo), "s"(wf));
    const auto wrsrc = __builtin_amdgcn_make_buffer_rsrc((void*)wf, 0, WS_BYTES, 0x00020000);
    unsigned tag_lo = 0, tag_hi = 0;
#define TAG_READY                                                                                  \
    {                                                                                              \
        unsigned long long tag_;                                                                   \
        asm volatile("s_load_dwordx2 %0, %1, %2\n\ts_waitcnt lgkmcnt(0)" : "=s"(tag_) : "s"(wf), "n"(EPO_OFF) : "memory"); \
        tag_lo = (unsigned)tag_ + 1u; tag_hi = (unsigned)(tag_ >> 32);                             \
    }
    const int pb = blockIdx.x;
    if (pb < 36) {
        const int lane = tid & 63, n = lane & 15, g = lane >> 4;
        const bool isO = wave >= 6;
        const int fr = isO ? 216 + 2 * pb + (wave - 6) : 6 * pb + wave;
        const int tile = fr / 6, ks = fr - 6 * tile, tw = tile / 6, mdt = tile - 6 * tw;
        const int col = isO ? 96 * (tw - 6) + 32 * (mdt >> 1) + 8 * (n >> 2) + 4 * (mdt & 1) + (n & 3)
                            : (tw < 4 ? (mdt < 3 ? 192 : -48) + 48 * tw : 96 * tw) + 16 * mdt + n;
        const int rb = isO ? 384 : 1152;
        const auto rr = __builtin_amdgcn_make_buffer_rsrc((void*)(isO ? wo : wqkv), 0, 192 * rb, 0x00020000);
        const int vo = (48 * (g >> 1) + 8 * (g & 1)) * rb + 2 * col;
        float v[8];
#pragma unroll
        for (int j = 0; j < 8; ++j) v[j] = bf2f(__builtin_amdgcn_raw_buffer_load_b16(rr, vo, (96 * (ks & 1) + 16 * (ks >> 1) + j) * rb, 0));
        TAG_READY
        float* red = (float*)smem;
        red[n * 36 + wave * 4 + g] = ((v[0] + v[1]) + (v[2] + v[3])) + ((v[4] + v[5]) + (v[6] + v[7]));
        BAR();
        float mean = 0.f;
#pragma unroll
        for (int i = 0; i < 6; ++i) {
            const f32x4 p = *(const f32x4*)(red + n * 36 + 4 * i);
            mean += (p[0] + p[1]) + (p[2] + p[3]);
        }
        mean = isO ? 0.f : mean * (1.f / 192.f);
        u32x4 o;
#pragma unroll
        for (int j = 0; j < 4; ++j) o[j] = pk2(v[2 * j] - mean, v[2 * j + 1] - mean);
        __builtin_amdgcn_raw_buffer_store_b128(o, wrsrc, lane * 16, fr * 1024, SC1);
        asm volatile("s_waitcnt vmcnt(0)" ::: "memory");
        BAR();
        if (tid == 0) __builtin_amdgcn_raw_buffer_store_b64((u32x2){tag_lo, tag_hi}, wrsrc, FLG_OFF + pb * 8, 0, SC1);
    }
    const int xi = blockIdx.x >> 3, nshort = (int)(0x0506060606060606ull >> (8 * (blockIdx.x & 7))) & 63;
    const int start = 672 * (blockIdx.x & 7) - ((int)(0x2a241e18120c0600ull >> (8 * (blockIdx.x & 7))) & 63) + 21 * xi - (xi < nshort ? xi : nshort);
    const int cnt = xi < nshort ? 20 : 21;

    const auto xrsrc = __builtin_amdgcn_make_buffer_rsrc((void*)((const char*)x - XBIAS), 0, IMG * IMG * CH * 2 + XBIAS, 0x00020000);
    bf16x8 W[36];
#define POLL_ISSUE                                                                                  \
    asm volatile("s_mov_b32 m0, %0\n\ts_mov_b64 exec, 0x3ffff\n\tbuffer_load_dwordx4 %1, %2, %3 offen sc1 lds\n\ts_mov_b64 exec, -1" \
                 :: "s"(ldsb + PF_OFF + w * 512), "v"(lane * 16), "s"(wrsrc), "s"(FLG_OFF) : "m0", "memory");
#define POLL_W                                                                                      \
    {                                                                                               \
        const int fi_ = lane < 36 ? lane : 0;                                                       \
        for (int it_ = 0; it_ < 100000; ++it_) {                                                    \
            u32x2 f_;                                                                               \
            if (it_ < 48) asm volatile("ds_read_b64 %0, %1\n\ts_waitcnt lgkmcnt(0)" : "=v"(f_) : "v"(ldsb + PF_OFF + w * 512 + fi_ * 8) : "memory"); \
            else f_ = __builtin_amdgcn_raw_buffer_load_b64(wrsrc, FLG_OFF + fi_ * 8, 0, SC1);       \
            if (__builtin_amdgcn_ballot_w64(f_[0] != tag_lo || f_[1] != tag_hi) == 0ull) break;     \
            if (it_ < 47) POLL_ISSUE                                                                \
            __builtin_amdgcn_s_sleep(PSLEEP);                                                       \
        }                                                                                           \
        __builtin_amdgcn_fence(__ATOMIC_ACQUIRE, "wavefront");                                      \
        SB();                                                                                       \
    }
#define LANE_SETUP                                                                                 \
    int lane;                                                                                      \
    asm volatile("v_mbcnt_lo_u32_b32 %0, -1, 0\n\tv_mbcnt_hi_u32_b32 %0, -1, %0" : "=v"(lane));    \
    __builtin_assume(lane >= 0 && lane < 64);                                                      \
    const int g = lane >> 4, l15 = lane & 15, sw = l15 >> 1;                                       \
    int fE = X_OFF + l15 * 384 + ((g ^ sw) << 4);                                                  \
    OPQ(fE);

#define DMA_SLICE(en)                                                                              \
        {                                                                                          \
            const unsigned vo_ = dma_off(lane) & (0x401fffffu | ((en) & 0x38000000u));             \
            const unsigned so_ = ((en) & 0x7ffffffu) + 96 * w;                                     \
            _Pragma("unroll") for (int r = 0; r < 9; ++r) {                                        \
                unsigned m0_ = ldsb + w * 8192 + r * 872;                                          \
                unsigned sr_ = (r == 0 && ((en) & 0x08000000u)) ? OOB : so_ + r * ROWBYTES;        \
                asm volatile("s_mov_b32 m0, %0\n\ts_mov_b32 exec_hi, 0x3fffff\n\tbuffer_load_dwordx4 %1, %2, %3 offen lds\n\ts_mov_b32 exec_hi, -1" \
                             :: "s"(m0_), "v"(vo_), "s"(xrsrc), "s"(sr_) : "m0", "memory");        \
            }                                                                                      \
        }
#define XPAT(r) (((r) < 48 ? (r) : 48) * 384 + (((2 * w) ^ (((r) >> 1) & 7)) << 4))
#define SPAT(r) ((w >> 1) * 1024 + (w & 1) * 8 + (r) * 16)
#define CONV_CONSTS(var) \
            const unsigned long long rlo = g == 0 ? ROWMAP[0] : (g == 1 ? ROWMAP[2] : (g == 2 ? ROWMAP[4] : ROWMAP[6])); \
            const unsigned long long rhi = g == 0 ? ROWMAP[1] : (g == 1 ? ROWMAP[3] : (g == 2 ? ROWMAP[5] : ROWMAP[7])); \
            int xrow = byte_of(rlo, rhi, l15); \
            if (lane == (int)((CLANE >> (8 * (var))) & 0xFF)) xrow = 48; \
            if (lane == 63) xrow = (CROW >> (8 * (var))) & 0xFF; \
            const unsigned pXw_ = XPAT(xrow); \
            const unsigned pSTw_ = (w >> 1) * 1024 + (w & 1) * 8 + xrow * 16; \
            const int oy = (0x5320 >> (4 * (l15 >> 2))) & 15, ox = (0x5320 >> (4 * (l15 & 3))) & 15; \
            const unsigned aB_ = ldsb + w * 8192 + (oy + (g >> 1)) * 872 + (ox + 2 * (g & 1)) * 96; \
            const int dyi = g - (l15 >> 3); \
            const unsigned aA_ = ldsb + AT_OFF + (l15 & 3) * 16 + \
                                 ((dyi < 0 || dyi > 2) ? 18816 : (dyi == 0 ? 0 : (dyi == 1 ? 6208 : 12544)) + ((l15 >> 2) & 1) * 3072 + w * 768); \
            const unsigned idx_ = (l15 & 3) * 0x5555u; \
            const unsigned se = (l15 == 4 * g) ? 0x3F803F80u : 0u; \
            const u32x4 sel4 = {se, se, se, se};

    unsigned en0, en1;
    {
        LANE_SETUP
        const int px = (lane * 43) >> 8;

        {
            const auto dr = __builtin_amdgcn_make_buffer_rsrc((void*)dw, 0, 9 * 384, 0x00020000);
            unsigned m0_ = ldsb + DW_OFF + w * 864, vo_ = px * 288 + lane * 16;
            asm volatile("s_mov_b32 m0, %0\n\ts_mov_b32 exec_hi, 0x3fffff\n\tbuffer_load_dwordx4 %1, %2, %3 offen lds\n\ts_mov_b32 exec_hi, -1"
                         :: "s"(m0_), "v"(vo_), "s"(dr), "s"(96 * w) : "m0", "memory");
        }
        int wy, wx;
        seq_to_win(start + (lane < cnt ? lane : cnt - 1), wy, wx);
        const unsigned en = wl_entry(wy, wx);
        const int bk = block_of(wy, wx);
        en0 = __builtin_amdgcn_readlane(en, 0);
        en1 = __builtin_amdgcn_readlane(en, 1);
        const int key0 = __builtin_amdgcn_readlane(bk, 0), nblk = __builtin_amdgcn_readlane(bk, 63) - key0 + 1;
        int ky, kx;
        block_pos(key0 + (l15 < nblk ? l15 : 0), ky, kx);
        if (wave < 4) {
            DMA_SLICE(en0)
        } else {
            const unsigned org = l15 < nblk ? (win_org(ky, 2 * kx) + (5 * ROWBYTES + 5 * CH * 2 + XBIAS)) | (kx == 36 ? OOB : 0u) : OOB + 0x8000000u;
            const unsigned lo = (px & 3) * (CH * 2) + (lane - 6 * px) * 16, keep = (px & 2) ? ~0u : ~OOB;
#pragma unroll
            for (int p = 0; p < 4; ++p) {
                if (p < 3 || nblk > 6) {
                    const unsigned oa = __builtin_amdgcn_readlane(org, 2 * p), ob = __builtin_amdgcn_readlane(org, 2 * p + 1);
                    const unsigned vo_ = ((lane < 24 ? oa : ob) & keep) + lo;
#pragma unroll
                    for (int r = 0; r < 4; ++r) {
                        unsigned m0_ = ldsb + LB_OFF + (4 * p + r) * 872;
                        asm volatile("s_mov_b32 m0, %0\n\ts_mov_b32 exec_hi, 0xffff\n\tbuffer_load_dwordx4 %1, %2, %3 offen lds\n\ts_mov_b32 exec_hi, -1"
                                     :: "s"(m0_), "v"(vo_), "s"(xrsrc), "s"(96 * w + r * ROWBYTES) : "m0", "memory");
                    }
                }
            }
        }
        SB();
        if (wave == 2) {
            u32x4 so;
#pragma unroll
            for (int e = 0; e < 3; ++e) {
                const int tk = byte_of(TOKMAP[2 * e], TOKMAP[2 * e + 1], l15);
                const int ty = (tk * 37) >> 8, tx = tk - 7 * ty;
                so[e] = (unsigned)(ty * ROWBYTES + tx * (CH * 2)) + 16 * g;
            }
            so[3] = dma_off(lane);
            *(u32x4*)(smem + CT_OFF + lane * 16) = so;
        }
        if (wave == 0 && lane < 32) *(unsigned*)(smem + WL_OFF + lane * 4) = lane < cnt ? en : 0x7ffffffu;
        if (wave == 1 && lane == 0) *(u32x2*)(smem + FLAG_OFF) = (u32x2){0u, 0u};
        if (lane < 48) *(u32x4*)(smem + AT_OFF + 18816 + lane * 16) = (u32x4){0u, 0u, 0u, 0u};
        CONV_CONSTS(en0 >> 30)
        const int ly = ky + (g >> 1), lx = 2 * kx + (g & 1), li = win_to_seq(ly, lx) - start;
        const bool ok = ly < NWX && lx < NWX && li >= 0 && li < cnt && l15 < nblk;
        const int row = ok ? li : 47, b = l15 < nblk ? l15 : 0;
        const unsigned cB = wave < 4 ? aB_ : ldsb + LB_OFF + (4 * (b >> 1) + (g >> 1)) * 872 + (b & 1) * 384 + (g & 1) * 192;
        const unsigned cX = wave < 4 ? (lane == (int)((CLANE >> (8 * (en0 >> 30))) & 0xFF) ? LX_OFF(0) - X_OFF + 32 * w : pXw_) : XPAT(row), cS = wave < 4 ? pSTw_ : SPAT(row) + (unsigned)(STL_OFF - O_OFF - 18816);
        if (wave < 4) {
            TAG_READY
            *(u32x2*)(smem + PF_OFF + w * 512 + lane * 8) = (u32x2){~tag_lo, 0u};
        }
        asm volatile("s_waitcnt vmcnt(9)" ::: "memory");
        if (lane < 48) {
            unsigned short dv[9];
#pragma unroll
            for (int i = 0; i < 9; ++i) dv[i] = *(const unsigned short*)(smem + DW_OFF + w * 864 + i * 96 + lane * 2);
#pragma unroll
            for (int dy = 0; dy < 3; ++dy) {
                const unsigned a = dv[3 * dy], b2 = dv[3 * dy + 1] | (dy == 1 ? 0x3F800000u : 0u), c = dv[3 * dy + 2];
                char* p = smem + AT_OFF + (dy == 0 ? 0 : (dy == 1 ? 6208 : 12544)) + w * 768 + lane * 16;
                *(u32x4*)p = (u32x4){a, b2, c, 0u};
                *(u32x4*)(p + 3072) = (u32x4){0u, a, b2, c};
            }
        }
        asm volatile("s_waitcnt vmcnt(0) lgkmcnt(0)" ::: "memory");
        if (wave < 4) POLL_ISSUE
        asm volatile(
            "s_mov_b32 m0, s73\n\t"
            "s_cmp_eq_u32 s73, 0\n\t"
            "s_nop 7\n\t"
            "ds_read_b128 v[168:171], v254\n\t"
            "ds_read_b64 v[144:145], v253\n\t"
            "ds_read_b64 v[146:147], v253 offset:96\n\t"
            "ds_read_b64 v[148:149], v253 offset:1744\n\t"
            "ds_read_b64 v[150:151], v253 offset:1840\n\t"
            "ds_read_b128 v[172:175], v254 offset:64\n\t"
            "ds_read_b64 v[152:153], v253 offset:8\n\t"
            "ds_read_b64 v[154:155], v253 offset:104\n\t"
            "ds_read_b64 v[156:157], v253 offset:1752\n\t"
            "ds_read_b64 v[158:159], v253 offset:1848\n\t"
            "ds_read_b64 v[160:161], v253 offset:16\n\t"
            "ds_read_b64 v[162:163], v253 offset:112\n\t"
            "ds_read_b64 v[164:165], v253 offset:1760\n\t"
            "ds_read_b64 v[166:167], v253 offset:1856\n\t"
            "v_mov_b64 v[176:177], 0\n\t"
            "v_mov_b64 v[178:179], 0\n\t"
            "v_mov_b64 v[180:181], 0\n\t"
            "v_mov_b64 v[182:183], 0\n\t"
            "v_mov_b64 v[184:185], 0\n\t"
            "v_mov_b64 v[186:187], 0\n\t"
            "v_mov_b64 v[188:189], 0\n\t"
            "v_mov_b64 v[190:191], 0\n\t"
            "v_add_u32 v236, s67, v250\n\t"
            "v_xor_b32 v237, 16, v236\n\t"
            "v_add_u32 v248, s67, v251\n\t"
            "s_waitcnt lgkmcnt(9)\n\t"
            "v_smfmac_f32_16x16x64_bf16 v[176:179], v[168:171], v[144:151], v255\n\t"
            "ds_read_b128 v[168:171], v254 offset:128\n\t"
            "ds_read_b64 v[144:145], v253 offset:24\n\t"
            "s_waitcnt lgkmcnt(6)\n\t"
            "v_smfmac_f32_16x16x64_bf16 v[180:183], v[172:175], v[152:159], v255\n\t"
            "ds_read_b64 v[146:147], v253 offset:120\n\t"
            "ds_read_b64 v[148:149], v253 offset:1768\n\t"
            "ds_read_b64 v[150:151], v253 offset:1864\n\t"
            "ds_read_b128 v[172:175], v254 offset:192\n\t"
            "ds_read_b64 v[152:153], v253 offset:32\n\t"
            "ds_read_b64 v[154:155], v253 offset:128\n\t"
            "ds_read_b64 v[156:157], v253 offset:1776\n\t"
            "ds_read_b64 v[158:159], v253 offset:1872\n\t"
            "v_cvt_pk_bf16_f32 v176, v176, v177\n\t"
            "v_cvt_pk_bf16_f32 v177, v178, v179\n\t"
            "v_cvt_pk_bf16_f32 v178, v180, v181\n\t"
            "v_cvt_pk_bf16_f32 v179, v182, v183\n\t"
            "v_mfma_f32_4x4x4_16b_bf16 v[196:199], v[176:177], v[176:177], 0\n\t"
            "ds_write_b128 v236, v[176:179]\n\t"
            "v_mfma_f32_16x16x32_bf16 v[192:195], v[238:241], v[176:179], 0\n\t"
            "v_mov_b64 v[176:177], 0\n\t"
            "v_mov_b64 v[180:181], 0\n\t"
            "v_mfma_f32_4x4x4_16b_bf16 v[196:199], v[178:179], v[178:179], v[196:199]\n\t"
            "v_mov_b64 v[178:179], 0\n\t"
            "v_mov_b64 v[182:183], 0\n\t"
            "s_waitcnt lgkmcnt(10)\n\t"
            "v_smfmac_f32_16x16x64_bf16 v[184:187], v[168:171], v[160:167], v255\n\t"
            "ds_read_b128 v[168:171], v254 offset:256\n\t"
            "ds_read_b64 v[160:161], v253 offset:40\n\t"
            "ds_read_b64 v[162:163], v253 offset:136\n\t"
            "ds_read_b64 v[164:165], v253 offset:1784\n\t"
            "ds_read_b64 v[166:167], v253 offset:1880\n\t"
            "s_waitcnt lgkmcnt(10)\n\t"
            "v_smfmac_f32_16x16x64_bf16 v[188:191], v[172:175], v[144:151], v255\n\t"
            "ds_read_b128 v[172:175], v254 offset:320\n\t"
            "ds_read_b64 v[144:145], v253 offset:48\n\t"
            "ds_read_b64 v[146:147], v253 offset:144\n\t"
            "ds_read_b64 v[148:149], v253 offset:1792\n\t"
            "ds_read_b64 v[150:151], v253 offset:1888\n\t"
            "v_cvt_pk_bf16_f32 v184, v184, v185\n\t"
            "v_cvt_pk_bf16_f32 v185, v186, v187\n\t"
            "s_nop 0\n\t"
            "v_cvt_pk_bf16_f32 v186, v188, v189\n\t"
            "v_cvt_pk_bf16_f32 v187, v190, v191\n\t"
            "v_mfma_f32_4x4x4_16b_bf16 v[196:199], v[184:185], v[184:185], v[196:199]\n\t"
            "s_waitcnt lgkmcnt(14)\n\t"
            "ds_write_b128 v237, v[184:187]\n\t"
            "v_mfma_f32_16x16x32_bf16 v[192:195], v[238:241], v[184:187], v[192:195]\n\t"
            "v_mov_b64 v[184:185], 0\n\t"
            "v_mov_b64 v[188:189], 0\n\t"
            "v_mfma_f32_4x4x4_16b_bf16 v[196:199], v[186:187], v[186:187], v[196:199]\n\t"
            "v_mov_b64 v[186:187], 0\n\t"
            "v_mov_b64 v[190:191], 0\n\t"
            "s_waitcnt lgkmcnt(10)\n\t"
            "v_smfmac_f32_16x16x64_bf16 v[176:179], v[168:171], v[152:159], v255\n\t"
            "ds_read_b128 v[168:171], v254 offset:384\n\t"
            "ds_read_b64 v[152:153], v253 offset:56\n\t"
            "ds_read_b64 v[154:155], v253 offset:152\n\t"
            "ds_read_b64 v[156:157], v253 offset:1800\n\t"
            "ds_read_b64 v[158:159], v253 offset:1896\n\t"
            "s_waitcnt lgkmcnt(10)\n\t"
            "v_smfmac_f32_16x16x64_bf16 v[180:183], v[172:175], v[160:167], v255\n\t"
            "ds_read_b128 v[172:175], v254 offset:448\n\t"
            "ds_read_b64 v[160:161], v253 offset:64\n\t"
            "ds_read_b64 v[162:163], v253 offset:160\n\t"
            "ds_read_b64 v[164:165], v253 offset:1808\n\t"
            "ds_read_b64 v[166:167], v253 offset:1904\n\t"
            "v_cvt_pk_bf16_f32 v176, v176, v177\n\t"
            "v_cvt_pk_bf16_f32 v177, v178, v179\n\t"
            "s_nop 0\n\t"
            "v_cvt_pk_bf16_f32 v178, v180, v181\n\t"
            "v_cvt_pk_bf16_f32 v179, v182, v183\n\t"
            "v_mfma_f32_4x4x4_16b_bf16 v[196:199], v[176:177], v[176:177], v[196:199]\n\t"
            "s_waitcnt lgkmcnt(14)\n\t"
            "ds_write_b128 v236, v[176:179] offset:128\n\t"
            "v_mfma_f32_16x16x32_bf16 v[192:195], v[238:241], v[176:179], v[192:195]\n\t"
            "v_mov_b64 v[176:177], 0\n\t"
            "v_mov_b64 v[180:181], 0\n\t"
            "v_mfma_f32_4x4x4_16b_bf16 v[196:199], v[178:179], v[178:179], v[196:199]\n\t"
            "v_mov_b64 v[178:179], 0\n\t"
            "v_mov_b64 v[182:183], 0\n\t"
            "s_waitcnt lgkmcnt(10)\n\t"
            "v_smfmac_f32_16x16x64_bf16 v[184:187], v[168:171], v[144:151], v255\n\t"
            "ds_read_b128 v[168:171], v254 offset:512\n\t"
            "ds_read_b64 v[144:145], v253 offset:72\n\t"
            "ds_read_b64 v[146:147], v253 offset:168\n\t"
            "ds_read_b64 v[148:149], v253 offset:1816\n\t"
            "ds_read_b64 v[150:151], v253 offset:1912\n\t"
            "s_waitcnt lgkmcnt(10)\n\t"
            "v_smfmac_f32_16x16x64_bf16 v[188:191], v[172:175], v[152:159], v255\n\t"
            "ds_read_b128 v[172:175], v254 offset:576\n\t"
            "ds_read_b64 v[152:153], v253 offset:80\n\t"
            "ds_read_b64 v[154:155], v253 offset:176\n\t"
            "ds_read_b64 v[156:157], v253 offset:1824\n\t"
            "ds_read_b64 v[158:159], v253 offset:1920\n\t"
            "v_cvt_pk_bf16_f32 v184, v184, v185\n\t"
            "v_cvt_pk_bf16_f32 v185, v186, v187\n\t"
            "s_nop 0\n\t"
            "v_cvt_pk_bf16_f32 v186, v188, v189\n\t"
            "v_cvt_pk_bf16_f32 v187, v190, v191\n\t"
            "v_mfma_f32_4x4x4_16b_bf16 v[196:199], v[184:185], v[184:185], v[196:199]\n\t"
            "s_waitcnt lgkmcnt(14)\n\t"
            "ds_write_b128 v237, v[184:187] offset:128\n\t"
            "v_mfma_f32_16x16x32_bf16 v[192:195], v[238:241], v[184:187], v[192:195]\n\t"
            "v_mov_b64 v[184:185], 0\n\t"
            "v_mov_b64 v[188:189], 0\n\t"
            "v_mfma_f32_4x4x4_16b_bf16 v[196:199], v[186:187], v[186:187], v[196:199]\n\t"
            "v_mov_b64 v[186:187], 0\n\t"
            "v_mov_b64 v[190:191], 0\n\t"
            "s_waitcnt lgkmcnt(10)\n\t"
            "v_smfmac_f32_16x16x64_bf16 v[176:179], v[168:171], v[160:167], v255\n\t"
            "ds_read_b128 v[168:171], v254 offset:640\n\t"
            "ds_read_b64 v[160:161], v253 offset:88\n\t"
            "ds_read_b64 v[162:163], v253 offset:184\n\t"
            "ds_read_b64 v[164:165], v253 offset:1832\n\t"
            "ds_read_b64 v[166:167], v253 offset:1928\n\t"
            "s_waitcnt lgkmcnt(10)\n\t"
            "v_smfmac_f32_16x16x64_bf16 v[180:183], v[172:175], v[144:151], v255\n\t"
            "ds_read_b128 v[172:175], v254 offset:704\n\t"
            "v_cvt_pk_bf16_f32 v176, v176, v177\n\t"
            "v_cvt_pk_bf16_f32 v177, v178, v179\n\t"
            "s_nop 1\n\t"
            "v_mfma_f32_4x4x4_16b_bf16 v[196:199], v[176:177], v[176:177], v[196:199]\n\t"
            "s_nop 1\n\t"
            "v_cvt_pk_bf16_f32 v178, v180, v181\n\t"
            "v_cvt_pk_bf16_f32 v179, v182, v183\n\t"
            "s_nop 1\n\t"
            "v_mfma_f32_4x4x4_16b_bf16 v[196:199], v[178:179], v[178:179], v[196:199]\n\t"
            "ds_write_b128 v236, v[176:179] offset:256\n\t"
            "v_mfma_f32_16x16x32_bf16 v[192:195], v[238:241], v[176:179], v[192:195]\n\t"
            "s_waitcnt lgkmcnt(6)\n\t"
            "v_smfmac_f32_16x16x64_bf16 v[184:187], v[168:171], v[152:159], v255\n\t"
            "s_waitcnt lgkmcnt(1)\n\t"
            "v_smfmac_f32_16x16x64_bf16 v[188:191], v[172:175], v[160:167], v255\n\t"
            "s_nop 5\n\t"
            "v_cvt_pk_bf16_f32 v184, v184, v185\n\t"
            "v_cvt_pk_bf16_f32 v185, v186, v187\n\t"
            "s_nop 1\n\t"
            "v_mfma_f32_4x4x4_16b_bf16 v[196:199], v[184:185], v[184:185], v[196:199]\n\t"
            "v_cvt_pk_bf16_f32 v186, v188, v189\n\t"
            "v_cvt_pk_bf16_f32 v187, v190, v191\n\t"
            "s_nop 1\n\t"
            "v_mfma_f32_4x4x4_16b_bf16 v[196:199], v[186:187], v[186:187], v[196:199]\n\t"
            "v_mfma_f32_16x16x32_bf16 v[192:195], v[238:241], v[184:187], v[192:195]\n\t"
            "ds_write_b128 v237, v[184:187] offset:256\n\t"
            "s_nop 6\n\t"
            "v_cndmask_b32_e64 v193, v196, v197, s[60:61]\n\t"
            "v_cndmask_b32_e64 v193, v193, v198, s[62:63]\n\t"
            "v_cndmask_b32_e64 v193, v193, v199, s[64:65]\n\t"
            "ds_write_b64 v248, v[192:193] offset:18816\n\t"
            "s_waitcnt lgkmcnt(0)\n\t"
            "s_nop 7\n\t"
            :
            : "{v250}"(cX), "{v251}"(cS), "{v253}"(cB), "{v254}"(aA_), "{v255}"(idx_), "{v[238:241]}"(sel4), "{s67}"(ldsb + (wave < 4 ? X_OFF : O_OFF)),
              "{v252}"(lane * 16), "{s[68:71]}"(wrsrc), "{s72}"(FLG_OFF), "{s73}"(wave < 4 ? ldsb + PF_OFF + w * 512 : 0u),
              "{s[60:61]}"(0x2222222222222222ull), "{s[62:63]}"(0x4444444444444444ull), "{s[64:65]}"(0x8888888888888888ull)
            : "v144", "v145", "v146", "v147", "v148", "v149", "v150", "v151", "v152", "v153", "v154", "v155", "v156", "v157", "v158", "v159", "v160", "v161", "v162", "v163", "v164", "v165", "v166", "v167", "v168", "v169", "v170", "v171", "v172", "v173", "v174", "v175", "v176", "v177", "v178", "v179", "v180", "v181", "v182", "v183", "v184", "v185", "v186", "v187", "v188", "v189", "v190", "v191", "v192", "v193", "v194", "v195", "v196", "v197", "v198", "v199", "v236", "v237", "v248", "v249", "m0", "scc", "memory");
        if (wave < 4) {
            if (lane == 0) __builtin_amdgcn_raw_buffer_store_b64((u32x2){tag_lo, tag_hi}, wrsrc, ARR_OFF + (blockIdx.x * 4 + w) * 8, 0, SC1);
            POLL_W
        }
    }
    const unsigned long long mrow = 0x0001000100010001ull * ((1u << (cnt - 16)) - 1u);
#define KARGS                                                                                      \
    unsigned long long xb, wb, ob;                                                                 \
    asm volatile("s_load_dwordx2 %0, %3, 0x0\n\ts_load_dwordx2 %1, %3, 0x20\n\ts_load_dwordx2 %2, %3, 0x28\n\ts_waitcnt lgkmcnt(0)" \
                 : "=&s"(xb), "=&s"(wb), "=&s"(ob) : "s"(__builtin_amdgcn_kernarg_segment_ptr()) : "memory"); \
    xb -= XBIAS;                                                                                   \
    const u32x4 wr = {(unsigned)wb, (unsigned)(wb >> 32) & 0xffffu, (unsigned)WS_BYTES, 0x00020000u}; \
    const u32x4 orr = {(unsigned)ob, (unsigned)(ob >> 32) & 0xffffu, (unsigned)(IMG * IMG * CH * 2), 0x00020000u}; \
    const u32x4 xr = {(unsigned)xb, (unsigned)(xb >> 32) & 0xffffu, (unsigned)(IMG * IMG * CH * 2 + XBIAS), 0x00020000u};
    const int tk_ = (xi + 4) / 6;
    const unsigned touch = wave * 36864 + ((xi + 4 - 6 * tk_) * 6 + tk_) * 1024;
#define B_SCALARS \
            CONV_CONSTS(0) \
            const unsigned aCA_ = ldsb + CT_OFF + lane * 16; \
            KARGS \
            unsigned s45 = OOB, s46 = (en0 & 0x7ffffffu) + 96 * w, s47 = (en1 & 0x7ffffffu) + 96 * w; \
            unsigned s48 = ldsb + X_OFF, s67 = ldsb + X1_OFF, s76 = ldsb + WL_OFF + 8, s70 = 0; \
            unsigned s56 = __builtin_amdgcn_readfirstlane((en0 >> 30) * 4 + (en1 >> 30)); \
            unsigned s69 = 0x401fffffu | (en1 & 0x38000000u), s79 = (en1 & 0x08000000u) ? OOB : s47, s59 = ldsb + LX_OFF(1) + 32 * w; \
            unsigned long long xp = 0, sp = 0; \
            _Pragma("unroll") for (int v = 0; v < 4; ++v) { \
                const unsigned r = (CROW >> (8 * v)) & 0xFF; \
                xp |= (unsigned long long)XPAT(r) << (16 * v); \
                sp |= (unsigned long long)SPAT(r) << (16 * v); \
            }
#define B_SIO "+{s45}"(s45), "+{s46}"(s46), "+{s47}"(s47), "+{s48}"(s48), "+{s67}"(s67), "+{s76}"(s76), "+{s56}"(s56), "+{s70}"(s70), "+{s69}"(s69), "+{s79}"(s79), "+{s59}"(s59)
#define B_SIN "{v[238:241]}"(sel4), "{s[40:43]}"(orr), "{s[72:75]}"(xr), "{s39}"(0x401fffffu), "{s[28:31]}"(wr), "{s33}"(wave * 36864), "{s78}"(96 * w), \
              "{s27}"(96 * w - 384), "{s[50:51]}"(xp), "{s[52:53]}"(sp), "{s54}"(CLANE), "{s55}"(CROW & 0x0F0F0F0Fu), "{s[34:35]}"(0x0001000100010001ull), \
              "{s[60:61]}"(0x2222222222222222ull), "{s[62:63]}"(0x4444444444444444ull), "{s[64:65]}"(0x8888888888888888ull), \
              "{s66}"(ldsb + w * 8192), "{s71}"((ldsb + X_OFF) ^ (ldsb + X1_OFF))
#define B_SCLOB "s83", "s26", "s77", "s68", "s36", "s37", "s38", "s57", "s58", "s49", "vcc", "scc", "m0", "memory"

    if (wave < 4) {
        LANE_SETUP
        const int dW = X_OFF + l15 * 384 + (((2 * w + (g >> 1)) ^ sw) << 4) + 8 * (g & 1);
        const int vB = V_OFF + w * 4608 + 1024 + lane * 8, rsR = RS_BASE + l15 * 4;
        {
            const unsigned rsb = ldsb + RS_BASE;
            const unsigned pE_ = fE - X_OFF, aO_ = ldsb + dW + (O_OFF - X_OFF), sel_ = (l15 & 3) ? 0u : 1u, aVB_ = ldsb + vB;
            const unsigned pST_ = lane * 16, aRSw_ = rsb + lane * 4, aPLw_ = rsb + lane * 2, aRSr_ = ldsb + rsR;
            const unsigned g16_ = (l15 & 3) ? ldsb + AT_OFF + 18816 : g * 16, g8_ = (l15 & 3) ? ldsb + AT_OFF + 18816 : g * 8;
            const unsigned loVL_ = ((2 * w + (l15 >> 3)) << 4) + (l15 & 7) * 2, loOL_ = g * 128;
            const unsigned on = (l15 & 3) ? 0u : 0x3F803F80u;
            const u32x4 ones4 = {on, on, on, on};
            unsigned s44 = cnt, s48 = ldsb + X_OFF, s70 = 0;
            unsigned s72 = ldsb + KQL_OFF + w * 192, s73 = ldsb + OL_OFF, s74 = ldsb + KQL_OFF + 784, s77 = ldsb + OL_OFF, s76 = ldsb + KQL_OFF + 768 + w * 4;
            KARGS
            asm volatile(
            "s_nop 4\n\t"
            "buffer_load_dwordx4 v[140:143], v248, s[84:87], s88 offen sc1\n\t"
            "s_add_u32 s49, s46, 0\n\t"
            "buffer_load_dwordx4 v[0:3], v248, s[84:87], s49 offen sc1\n\t"
            "s_add_u32 s49, s46, 6144\n\t"
            "buffer_load_dwordx4 v[24:27], v248, s[84:87], s49 offen sc1\n\t"
            "s_add_u32 s49, s46, 12288\n\t"
            "buffer_load_dwordx4 v[48:51], v248, s[84:87], s49 offen sc1\n\t"
            "s_add_u32 s49, s46, 18432\n\t"
            "buffer_load_dwordx4 v[72:75], v248, s[84:87], s49 offen sc1\n\t"
            "s_add_u32 s49, s46, 24576\n\t"
            "buffer_load_dwordx4 v[96:99], v248, s[84:87], s49 offen sc1\n\t"
            "s_add_u32 s49, s46, 30720\n\t"
            "buffer_load_dwordx4 v[120:123], v248, s[84:87], s49 offen sc1\n\t"
            "s_add_u32 s49, s46, 1024\n\t"
            "buffer_load_dwordx4 v[4:7], v248, s[84:87], s49 offen sc1\n\t"
            "s_add_u32 s49, s46, 7168\n\t"
            "buffer_load_dwordx4 v[28:31], v248, s[84:87], s49 offen sc1\n\t"
            "s_add_u32 s49, s46, 13312\n\t"
            "buffer_load_dwordx4 v[52:55], v248, s[84:87], s49 offen sc1\n\t"
            "s_add_u32 s49, s46, 19456\n\t"
            "buffer_load_dwordx4 v[76:79], v248, s[84:87], s49 offen sc1\n\t"
            "s_add_u32 s49, s46, 25600\n\t"
            "buffer_load_dwordx4 v[100:103], v248, s[84:87], s49 offen sc1\n\t"
            "s_add_u32 s49, s46, 31744\n\t"
            "buffer_load_dwordx4 v[124:127], v248, s[84:87], s49 offen sc1\n\t"
            "s_add_u32 s49, s46, 2048\n\t"
            "buffer_load_dwordx4 v[8:11], v248, s[84:87], s49 offen sc1\n\t"
            "s_add_u32 s49, s46, 8192\n\t"
            "buffer_load_dwordx4 v[32:35], v248, s[84:87], s49 offen sc1\n\t"
            "s_add_u32 s49, s46, 14336\n\t"
            "buffer_load_dwordx4 v[56:59], v248, s[84:87], s49 offen sc1\n\t"
            "s_add_u32 s49, s46, 20480\n\t"
            "buffer_load_dwordx4 v[80:83], v248, s[84:87], s49 offen sc1\n\t"
            "s_add_u32 s49, s46, 26624\n\t"
            "buffer_load_dwordx4 v[104:107], v248, s[84:87], s49 offen sc1\n\t"
            "s_add_u32 s49, s46, 32768\n\t"
            "buffer_load_dwordx4 v[128:131], v248, s[84:87], s49 offen sc1\n\t"
            "s_add_u32 s49, s46, 3072\n\t"
            "buffer_load_dwordx4 v[12:15], v248, s[84:87], s49 offen sc1\n\t"
            "s_add_u32 s49, s46, 9216\n\t"
            "buffer_load_dwordx4 v[36:39], v248, s[84:87], s49 offen sc1\n\t"
            "s_add_u32 s49, s46, 15360\n\t"
            "buffer_load_dwordx4 v[60:63], v248, s[84:87], s49 offen sc1\n\t"
            "s_add_u32 s49, s46, 21504\n\t"
            "buffer_load_dwordx4 v[84:87], v248, s[84:87], s49 offen sc1\n\t"
            "s_add_u32 s49, s46, 27648\n\t"
            "buffer_load_dwordx4 v[108:111], v248, s[84:87], s49 offen sc1\n\t"
            "s_add_u32 s49, s46, 33792\n\t"
            "buffer_load_dwordx4 v[132:135], v248, s[84:87], s49 offen sc1\n\t"
            "s_add_u32 s49, s46, 4096\n\t"
            "buffer_load_dwordx4 v[16:19], v248, s[84:87], s49 offen sc1\n\t"
            "s_add_u32 s49, s46, 10240\n\t"
            "buffer_load_dwordx4 v[40:43], v248, s[84:87], s49 offen sc1\n\t"
            "s_add_u32 s49, s46, 16384\n\t"
            "buffer_load_dwordx4 v[64:67], v248, s[84:87], s49 offen sc1\n\t"
            "s_add_u32 s49, s46, 22528\n\t"
            "buffer_load_dwordx4 v[88:91], v248, s[84:87], s49 offen sc1\n\t"
            "s_add_u32 s49, s46, 28672\n\t"
            "buffer_load_dwordx4 v[112:115], v248, s[84:87], s49 offen sc1\n\t"
            "s_add_u32 s49, s46, 34816\n\t"
            "buffer_load_dwordx4 v[136:139], v248, s[84:87], s49 offen sc1\n\t"
            "s_add_u32 s49, s46, 5120\n\t"
            "buffer_load_dwordx4 v[20:23], v248, s[84:87], s49 offen sc1\n\t"
            "s_add_u32 s49, s46, 11264\n\t"
            "buffer_load_dwordx4 v[44:47], v248, s[84:87], s49 offen sc1\n\t"
            "s_add_u32 s49, s46, 17408\n\t"
            "buffer_load_dwordx4 v[68:71], v248, s[84:87], s49 offen sc1\n\t"
            "s_add_u32 s49, s46, 23552\n\t"
            "buffer_load_dwordx4 v[92:95], v248, s[84:87], s49 offen sc1\n\t"
            "s_add_u32 s49, s46, 29696\n\t"
            "buffer_load_dwordx4 v[116:119], v248, s[84:87], s49 offen sc1\n\t"
            "s_add_u32 s49, s46, 35840\n\t"
            "buffer_load_dwordx4 v[140:143], v248, s[84:87], s49 offen sc1\n\t"
            "s_waitcnt lgkmcnt(0)\n\t"
            "s_barrier\n\t"
            "s_setprio 2\n\t"
            "v_add_u32 v235, s71, v248\n\t"
            "v_add_u32 v232, s71, v244\n\t"
            "v_xor_b32 v233, 64, v232\n\t"
            "v_subrev_u32 v239, s75, v251\n\t"
            "v_mul_u32_u24 v239, 200, v239\n\t"
            "v_lshrrev_b32 v234, 3, v255\n\t"
            "v_add3_u32 v234, v234, v239, s72\n\t"
            "ds_read_b128 v[228:231], v235 offset:12288\n\t"
            "ds_read_b128 v[144:147], v232\n\t"
            "ds_read_b128 v[148:151], v233\n\t"
            "ds_read_b128 v[204:207], v232 offset:6144\n\t"
            "ds_read_b128 v[208:211], v233 offset:6144\n\t"
            "ds_read_b128 v[152:155], v232 offset:128\n\t"
            "ds_read_b128 v[156:159], v233 offset:128\n\t"
            "ds_read_b128 v[212:215], v232 offset:6272\n\t"
            "ds_read_b128 v[224:227], v233 offset:6272\n\t"
            "ds_read_b128 v[160:163], v232 offset:256\n\t"
            "ds_read_b128 v[164:167], v233 offset:256\n\t"
            "s_waitcnt vmcnt(24)\n\t"
            "s_waitcnt lgkmcnt(10)\n\t"
            "v_add_f32 v236, v228, v230\n\t"
            "v_add_f32 v237, v229, v231\n\t"
            "s_waitcnt lgkmcnt(8)\n\t"
            "v_mfma_f32_16x16x32_bf16 v[168:171], v[0:3], v[144:147], 0\n\t"
            "ds_read_b128 v[228:231], v235 offset:13312\n\t"
            "v_lshrrev_b32 v235, 4, v255\n\t"
            "v_mfma_f32_16x16x32_bf16 v[168:171], v[4:7], v[148:151], v[168:171]\n\t"
            "v_add3_u32 v235, v235, v239, s72\n\t"
            "v_mfma_f32_16x16x32_bf16 v[176:179], v[48:51], v[144:147], 0\n\t"
            "v_mfma_f32_16x16x32_bf16 v[176:179], v[52:55], v[148:151], v[176:179]\n\t"
            "v_mfma_f32_16x16x32_bf16 v[172:175], v[24:27], v[144:147], 0\n\t"
            "v_mfma_f32_16x16x32_bf16 v[172:175], v[28:31], v[148:151], v[172:175]\n\t"
            "v_mfma_f32_16x16x32_bf16 v[180:183], v[72:75], v[144:147], 0\n\t"
            "v_mfma_f32_16x16x32_bf16 v[180:183], v[76:79], v[148:151], v[180:183]\n\t"
            "v_mfma_f32_16x16x32_bf16 v[184:187], v[96:99], v[144:147], 0\n\t"
            "v_mfma_f32_16x16x32_bf16 v[184:187], v[100:103], v[148:151], v[184:187]\n\t"
            "v_mfma_f32_16x16x32_bf16 v[188:191], v[120:123], v[144:147], 0\n\t"
            "s_waitcnt lgkmcnt(0)\n\t"
            "v_add_f32 v228, v228, v230\n\t"
            "v_add_f32 v236, v236, v228\n\t"
            "v_mfma_f32_16x16x32_bf16 v[188:191], v[124:127], v[148:151], v[188:191]\n\t"
            "v_add_f32 v229, v229, v231\n\t"
            "v_mul_f32 v236, 0x3baaaaab, v236\n\t"
            "v_mfma_f32_16x16x32_bf16 v[192:195], v[0:3], v[204:207], 0\n\t"
            "v_add_f32 v237, v237, v229\n\t"
            "v_mul_f32_e64 v236, v236, -v236\n\t"
            "v_mfma_f32_16x16x32_bf16 v[192:195], v[4:7], v[208:211], v[192:195]\n\t"
            "v_fmac_f32 v236, 0x3baaaaab, v237\n\t"
            "v_max_f32 v236, 0, v236\n\t"
            "v_mfma_f32_16x16x32_bf16 v[196:199], v[24:27], v[204:207], 0\n\t"
            "v_add_f32 v236, 0x3727c5ac, v236\n\t"
            "v_rsq_f32 v236, v236\n\t"
            "v_mfma_f32_16x16x32_bf16 v[196:199], v[28:31], v[208:211], v[196:199]\n\t"
            "ds_write_b32 v249, v236\n\t"
            "v_mfma_f32_16x16x32_bf16 v[200:203], v[48:51], v[204:207], 0\n\t"
            "ds_read_b32 v236, v251\n\t"
            "ds_read_b32 v237, v251 offset:64\n\t"
            "v_mfma_f32_16x16x32_bf16 v[200:203], v[52:55], v[208:211], v[200:203]\n\t"
            "v_mfma_f32_16x16x32_bf16 v[216:219], v[72:75], v[204:207], 0\n\t"
            "v_mfma_f32_16x16x32_bf16 v[216:219], v[76:79], v[208:211], v[216:219]\n\t"
            "v_mfma_f32_16x16x32_bf16 v[220:223], v[96:99], v[204:207], 0\n\t"
            "v_mfma_f32_16x16x32_bf16 v[220:223], v[100:103], v[208:211], v[220:223]\n\t"
            "v_mfma_f32_16x16x32_bf16 v[228:231], v[120:123], v[204:207], 0\n\t"
            "ds_read_b128 v[204:207], v232 offset:6400\n\t"
            "v_add_u32 v232, s48, v244\n\t"
            "v_mfma_f32_16x16x32_bf16 v[228:231], v[124:127], v[208:211], v[228:231]\n\t"
            "ds_read_b128 v[208:211], v233 offset:6400\n\t"
            "s_waitcnt vmcnt(12)\n\t"
            "v_mfma_f32_16x16x32_bf16 v[168:171], v[8:11], v[152:155], v[168:171]\n\t"
            "v_xor_b32 v233, 64, v232\n\t"
            "v_mfma_f32_16x16x32_bf16 v[168:171], v[12:15], v[156:159], v[168:171]\n\t"
            "v_mfma_f32_16x16x32_bf16 v[176:179], v[56:59], v[152:155], v[176:179]\n\t"
            "v_mfma_f32_16x16x32_bf16 v[176:179], v[60:63], v[156:159], v[176:179]\n\t"
            "v_mfma_f32_16x16x32_bf16 v[172:175], v[32:35], v[152:155], v[172:175]\n\t"
            "v_mfma_f32_16x16x32_bf16 v[172:175], v[36:39], v[156:159], v[172:175]\n\t"
            "v_mfma_f32_16x16x32_bf16 v[180:183], v[80:83], v[152:155], v[180:183]\n\t"
            "v_mfma_f32_16x16x32_bf16 v[180:183], v[84:87], v[156:159], v[180:183]\n\t"
            "v_mfma_f32_16x16x32_bf16 v[184:187], v[104:107], v[152:155], v[184:187]\n\t"
            "v_mfma_f32_16x16x32_bf16 v[184:187], v[108:111], v[156:159], v[184:187]\n\t"
            "v_mfma_f32_16x16x32_bf16 v[188:191], v[128:131], v[152:155], v[188:191]\n\t"
            "v_mfma_f32_16x16x32_bf16 v[188:191], v[132:135], v[156:159], v[188:191]\n\t"
            "v_mfma_f32_16x16x32_bf16 v[192:195], v[8:11], v[212:215], v[192:195]\n\t"
            "v_mfma_f32_16x16x32_bf16 v[192:195], v[12:15], v[224:227], v[192:195]\n\t"
            "v_mfma_f32_16x16x32_bf16 v[196:199], v[32:35], v[212:215], v[196:199]\n\t"
            "v_mfma_f32_16x16x32_bf16 v[196:199], v[36:39], v[224:227], v[196:199]\n\t"
            "v_mfma_f32_16x16x32_bf16 v[200:203], v[56:59], v[212:215], v[200:203]\n\t"
            "v_mfma_f32_16x16x32_bf16 v[200:203], v[60:63], v[224:227], v[200:203]\n\t"
            "v_mfma_f32_16x16x32_bf16 v[216:219], v[80:83], v[212:215], v[216:219]\n\t"
            "v_mfma_f32_16x16x32_bf16 v[216:219], v[84:87], v[224:227], v[216:219]\n\t"
            "v_mfma_f32_16x16x32_bf16 v[220:223], v[104:107], v[212:215], v[220:223]\n\t"
            "v_mfma_f32_16x16x32_bf16 v[220:223], v[108:111], v[224:227], v[220:223]\n\t"
            "v_mfma_f32_16x16x32_bf16 v[228:231], v[128:131], v[212:215], v[228:231]\n\t"
            "ds_read_b128 v[212:215], v232 offset:256\n\t"
            "v_mfma_f32_16x16x32_bf16 v[228:231], v[132:135], v[224:227], v[228:231]\n\t"
            "ds_read_b128 v[224:227], v233 offset:256\n\t"
            "s_waitcnt vmcnt(0)\n\t"
            "v_mfma_f32_16x16x32_bf16 v[168:171], v[16:19], v[160:163], v[168:171]\n\t"
            "v_mfma_f32_16x16x32_bf16 v[168:171], v[20:23], v[164:167], v[168:171]\n\t"
            "v_mfma_f32_16x16x32_bf16 v[176:179], v[64:67], v[160:163], v[176:179]\n\t"
            "v_mfma_f32_16x16x32_bf16 v[176:179], v[68:71], v[164:167], v[176:179]\n\t"
            "v_mfma_f32_16x16x32_bf16 v[180:183], v[88:91], v[160:163], v[180:183]\n\t"
            "v_mfma_f32_16x16x32_bf16 v[180:183], v[92:95], v[164:167], v[180:183]\n\t"
            "v_mfma_f32_16x16x32_bf16 v[172:175], v[40:43], v[160:163], v[172:175]\n\t"
            "v_mfma_f32_16x16x32_bf16 v[172:175], v[44:47], v[164:167], v[172:175]\n\t"
            "s_waitcnt lgkmcnt(5)\n\t"
            "s_nop 0\n\t"
            "v_mul_f32 v168, v236, v168\n\t"
            "v_mfma_f32_16x16x32_bf16 v[184:187], v[112:115], v[160:163], v[184:187]\n\t"
            "v_mul_f32 v169, v236, v169\n\t"
            "v_mul_f32 v170, v236, v170\n\t"
            "v_mfma_f32_16x16x32_bf16 v[184:187], v[116:119], v[164:167], v[184:187]\n\t"
            "v_mul_f32 v176, v236, v176\n\t"
            "v_mul_f32 v177, v236, v177\n\t"
            "v_mfma_f32_16x16x32_bf16 v[188:191], v[136:139], v[160:163], v[188:191]\n\t"
            "v_mul_f32 v171, v236, v171\n\t"
            "v_mul_f32 v178, v236, v178\n\t"
            "v_mfma_f32_16x16x32_bf16 v[188:191], v[140:143], v[164:167], v[188:191]\n\t"
            "v_mul_f32 v179, v236, v179\n\t"
            "v_cvt_pk_bf16_f32 v168, v168, v169\n\t"
            "s_waitcnt lgkmcnt(2)\n\t"
            "v_mfma_f32_16x16x32_bf16 v[192:195], v[16:19], v[204:207], v[192:195]\n\t"
            "v_cvt_pk_bf16_f32 v176, v176, v177\n\t"
            "v_cvt_pk_bf16_f32 v180, v180, v181\n\t"
            "v_mfma_f32_16x16x32_bf16 v[192:195], v[20:23], v[208:211], v[192:195]\n\t"
            "v_mul_f32 v172, v236, v172\n\t"
            "v_mul_f32 v173, v236, v173\n\t"
            "v_mfma_f32_16x16x32_bf16 v[196:199], v[40:43], v[204:207], v[196:199]\n\t"
            "v_mul_f32 v174, v236, v174\n\t"
            "v_mul_f32 v175, v236, v175\n\t"
            "v_mfma_f32_16x16x32_bf16 v[196:199], v[44:47], v[208:211], v[196:199]\n\t"
            "v_cvt_pk_bf16_f32 v169, v170, v171\n\t"
            "v_cvt_pk_bf16_f32 v177, v178, v179\n\t"
            "v_mfma_f32_16x16x32_bf16 v[200:203], v[64:67], v[204:207], v[200:203]\n\t"
            "v_cvt_pk_bf16_f32 v181, v182, v183\n\t"
            "v_cvt_pk_bf16_f32 v170, v172, v173\n\t"
            "v_mfma_f32_16x16x32_bf16 v[200:203], v[68:71], v[208:211], v[200:203]\n\t"
            "v_cvt_pk_bf16_f32 v171, v174, v175\n\t"
            "v_cvt_pk_bf16_f32 v182, v184, v185\n\t"
            "v_mfma_f32_16x16x32_bf16 v[216:219], v[88:91], v[204:207], v[216:219]\n\t"
            "v_cvt_pk_bf16_f32 v183, v186, v187\n\t"
            "v_cvt_pk_bf16_f32 v178, v188, v189\n\t"
            "v_mfma_f32_16x16x32_bf16 v[216:219], v[92:95], v[208:211], v[216:219]\n\t"
            "v_cvt_pk_bf16_f32 v179, v190, v191\n\t"
            "v_mul_f32 v236, 0x3e553b94, v236\n\t"
            "v_mfma_f32_16x16x32_bf16 v[220:223], v[112:115], v[204:207], v[220:223]\n\t"
            "v_add_u32 v184, s76, v239\n\t"
            "v_add_u32 v185, s74, v239\n\t"
            "v_mfma_f32_16x16x32_bf16 v[220:223], v[116:119], v[208:211], v[220:223]\n\t"
            "s_mov_b64 exec, -1\n\t"
            "ds_write_b128 v234, v[168:171]\n\t"
            "ds_write_b128 v234, v[180:183] offset:64\n\t"
            "ds_write_b64 v235, v[176:177] offset:128\n\t"
            "ds_write_b64 v235, v[178:179] offset:160\n\t"
            "s_mov_b64 exec, -1\n\t"
            "v_mfma_f32_16x16x16_bf16 v[172:175], v[176:177], v[178:179], 0\n\t"
            "v_mul_f32 v192, v237, v192\n\t"
            "v_mul_f32 v193, v237, v193\n\t"
            "v_mul_f32 v200, v237, v200\n\t"
            "v_mfma_f32_16x16x32_bf16 v[228:231], v[136:139], v[204:207], v[228:231]\n\t"
            "ds_read_b128 v[204:207], v232\n\t"
            "v_mul_f32 v201, v237, v201\n\t"
            "v_mfma_f32_16x16x32_bf16 v[228:231], v[140:143], v[208:211], v[228:231]\n\t"
            "ds_read_b128 v[208:211], v233\n\t"
            "v_mul_f32 v194, v237, v194\n\t"
            "v_mfma_f32_16x16x32_bf16 v[172:175], v[168:171], v[180:183], v[172:175]\n\t"
            "v_mul_f32 v195, v237, v195\n\t"
            "v_mul_f32 v202, v237, v202\n\t"
            "v_mul_f32 v203, v237, v203\n\t"
            "v_cvt_pk_bf16_f32 v192, v192, v193\n\t"
            "v_cvt_pk_bf16_f32 v200, v200, v201\n\t"
            "v_cvt_pk_bf16_f32 v216, v216, v217\n\t"
            "v_mul_f32 v196, v237, v196\n\t"
            "v_mul_f32 v197, v237, v197\n\t"
            "v_mul_f32 v198, v237, v198\n\t"
            "v_mul_f32 v199, v237, v199\n\t"
            "v_cvt_pk_bf16_f32 v193, v194, v195\n\t"
            "v_cvt_pk_bf16_f32 v201, v202, v203\n\t"
            "v_cvt_pk_bf16_f32 v217, v218, v219\n\t"
            "v_cvt_pk_bf16_f32 v194, v196, v197\n\t"
            "v_cvt_pk_bf16_f32 v195, v198, v199\n\t"
            "v_cvt_pk_bf16_f32 v218, v220, v221\n\t"
            "v_cvt_pk_bf16_f32 v219, v222, v223\n\t"
            "v_cvt_pk_bf16_f32 v202, v228, v229\n\t"
            "v_cvt_pk_bf16_f32 v203, v230, v231\n\t"
            "v_mul_f32 v237, 0x3e553b94, v237\n\t"
            "v_add_u32 v220, s76, v239\n\t"
            "v_mfma_f32_16x16x16_bf16 v[196:199], v[200:201], v[202:203], 0\n\t"
            "s_and_b32 exec_lo, s56, 0x11111111\n\t"
            "s_and_b32 exec_hi, s57, 0x11111111\n\t"
            "ds_write_b32 v184, v172 offset:0\n\t"
            "s_and_b32 exec_lo, s56, 0x22222222\n\t"
            "s_and_b32 exec_hi, s57, 0x22222222\n\t"
            "ds_write_b32 v184, v173 offset:0\n\t"
            "s_and_b32 exec_lo, s56, 0x44444444\n\t"
            "s_and_b32 exec_hi, s57, 0x44444444\n\t"
            "ds_write_b32 v184, v174 offset:0\n\t"
            "s_and_b32 exec_lo, s56, 0x88888888\n\t"
            "s_and_b32 exec_hi, s57, 0x88888888\n\t"
            "ds_write_b32 v184, v175 offset:0\n\t"
            "s_mov_b64 exec, s[56:57]\n\t"
            "ds_write_b32 v185, v236 offset:0\n\t"
            "s_mov_b64 exec, -1\n\t"
            "s_waitcnt lgkmcnt(5)\n\t"
            "v_mfma_f32_16x16x32_bf16 v[144:147], v[0:3], v[204:207], 0\n\t"
            "v_add_u32 v221, s74, v239\n\t"
            "v_mfma_f32_16x16x32_bf16 v[144:147], v[4:7], v[208:211], v[144:147]\n\t"
            "s_mov_b64 exec, s[54:55]\n\t"
            "ds_write_b128 v234, v[192:195] offset:12800\n\t"
            "ds_write_b128 v234, v[216:219] offset:12864\n\t"
            "ds_write_b64 v235, v[200:201] offset:12928\n\t"
            "ds_write_b64 v235, v[202:203] offset:12960\n\t"
            "s_mov_b64 exec, -1\n\t"
            "v_mfma_f32_16x16x32_bf16 v[148:151], v[28:31], v[208:211], 0\n\t"
            "v_add_u32 v235, s48, v248\n\t"
            "v_mfma_f32_16x16x32_bf16 v[148:151], v[24:27], v[204:207], v[148:151]\n\t"
            "ds_read_b128 v[228:231], v235 offset:18816\n\t"
            "v_mfma_f32_16x16x32_bf16 v[152:155], v[48:51], v[204:207], 0\n\t"
            "v_mfma_f32_16x16x32_bf16 v[152:155], v[52:55], v[208:211], v[152:155]\n\t"
            "v_mfma_f32_16x16x32_bf16 v[156:159], v[76:79], v[208:211], 0\n\t"
            "v_mfma_f32_16x16x32_bf16 v[156:159], v[72:75], v[204:207], v[156:159]\n\t"
            "v_mfma_f32_16x16x32_bf16 v[160:163], v[96:99], v[204:207], 0\n\t"
            "v_mfma_f32_16x16x32_bf16 v[160:163], v[100:103], v[208:211], v[160:163]\n\t"
            "v_mfma_f32_16x16x32_bf16 v[164:167], v[124:127], v[208:211], 0\n\t"
            "ds_read_b128 v[208:211], v233 offset:128\n\t"
            "v_mfma_f32_16x16x32_bf16 v[164:167], v[120:123], v[204:207], v[164:167]\n\t"
            "ds_read_b128 v[204:207], v232 offset:128\n\t"
            "v_mfma_f32_16x16x32_bf16 v[196:199], v[192:195], v[216:219], v[196:199]\n\t"
            "s_waitcnt lgkmcnt(2)\n\t"
            "v_add_f32 v236, v228, v230\n\t"
            "s_nop 5\n\t"
            "s_and_b32 exec_lo, s78, 0x11111111\n\t"
            "s_and_b32 exec_hi, s79, 0x11111111\n\t"
            "ds_write_b32 v220, v196 offset:12800\n\t"
            "s_and_b32 exec_lo, s78, 0x22222222\n\t"
            "s_and_b32 exec_hi, s79, 0x22222222\n\t"
            "ds_write_b32 v220, v197 offset:12800\n\t"
            "s_and_b32 exec_lo, s78, 0x44444444\n\t"
            "s_and_b32 exec_hi, s79, 0x44444444\n\t"
            "ds_write_b32 v220, v198 offset:12800\n\t"
            "s_and_b32 exec_lo, s78, 0x88888888\n\t"
            "s_and_b32 exec_hi, s79, 0x88888888\n\t"
            "ds_write_b32 v220, v199 offset:12800\n\t"
            "s_mov_b64 exec, s[78:79]\n\t"
            "ds_write_b32 v221, v237 offset:12800\n\t"
            "s_mov_b64 exec, -1\n\t"
            "s_waitcnt lgkmcnt(5)\n\t"
            "v_mfma_f32_16x16x32_bf16 v[144:147], v[8:11], v[204:207], v[144:147]\n\t"
            "v_add_f32 v237, v229, v231\n\t"
            "v_mfma_f32_16x16x32_bf16 v[144:147], v[12:15], v[208:211], v[144:147]\n\t"
            "ds_read_b128 v[228:231], v235 offset:19840\n\t"
            "v_mfma_f32_16x16x32_bf16 v[148:151], v[36:39], v[208:211], v[148:151]\n\t"
            "v_mfma_f32_16x16x32_bf16 v[148:151], v[32:35], v[204:207], v[148:151]\n\t"
            "v_mfma_f32_16x16x32_bf16 v[152:155], v[56:59], v[204:207], v[152:155]\n\t"
            "v_mfma_f32_16x16x32_bf16 v[152:155], v[60:63], v[208:211], v[152:155]\n\t"
            "v_mfma_f32_16x16x32_bf16 v[156:159], v[84:87], v[208:211], v[156:159]\n\t"
            "v_mfma_f32_16x16x32_bf16 v[156:159], v[80:83], v[204:207], v[156:159]\n\t"
            "v_mfma_f32_16x16x32_bf16 v[160:163], v[104:107], v[204:207], v[160:163]\n\t"
            "v_mfma_f32_16x16x32_bf16 v[160:163], v[108:111], v[208:211], v[160:163]\n\t"
            "v_mfma_f32_16x16x32_bf16 v[164:167], v[132:135], v[208:211], v[164:167]\n\t"
            "ds_read_b128 v[208:211], v233 offset:6144\n\t"
            "v_mfma_f32_16x16x32_bf16 v[164:167], v[128:131], v[204:207], v[164:167]\n\t"
            "ds_read_b128 v[204:207], v232 offset:6144\n\t"
            "s_waitcnt lgkmcnt(2)\n\t"
            "v_add_f32 v228, v228, v230\n\t"
            "v_mfma_f32_16x16x32_bf16 v[144:147], v[16:19], v[212:215], v[144:147]\n\t"
            "v_add_f32 v236, v236, v228\n\t"
            "v_add_f32 v229, v229, v231\n\t"
            "v_mfma_f32_16x16x32_bf16 v[144:147], v[20:23], v[224:227], v[144:147]\n\t"
            "v_mul_f32 v236, 0x3baaaaab, v236\n\t"
            "v_add_f32 v237, v237, v229\n\t"
            "v_mfma_f32_16x16x32_bf16 v[148:151], v[44:47], v[224:227], v[148:151]\n\t"
            "v_mul_f32_e64 v236, v236, -v236\n\t"
            "v_fmac_f32 v236, 0x3baaaaab, v237\n\t"
            "v_mfma_f32_16x16x32_bf16 v[148:151], v[40:43], v[212:215], v[148:151]\n\t"
            "v_max_f32 v236, 0, v236\n\t"
            "v_add_f32 v236, 0x3727c5ac, v236\n\t"
            "v_mfma_f32_16x16x32_bf16 v[152:155], v[64:67], v[212:215], v[152:155]\n\t"
            "v_rsq_f32 v236, v236\n\t"
            "v_mfma_f32_16x16x32_bf16 v[152:155], v[68:71], v[224:227], v[152:155]\n\t"
            "ds_write_b32 v249, v236\n\t"
            "v_mfma_f32_16x16x32_bf16 v[156:159], v[92:95], v[224:227], v[156:159]\n\t"
            "ds_read_b32 v236, v251\n\t"
            "ds_read_b32 v237, v251 offset:64\n\t"
            "v_mfma_f32_16x16x32_bf16 v[156:159], v[88:91], v[212:215], v[156:159]\n\t"
            "ds_read_b32 v238, v251 offset:128\n\t"
            "v_mfma_f32_16x16x32_bf16 v[160:163], v[112:115], v[212:215], v[160:163]\n\t"
            "v_mfma_f32_16x16x32_bf16 v[160:163], v[116:119], v[224:227], v[160:163]\n\t"
            "v_mfma_f32_16x16x32_bf16 v[164:167], v[140:143], v[224:227], v[164:167]\n\t"
            "ds_read_b128 v[224:227], v233 offset:6272\n\t"
            "v_mfma_f32_16x16x32_bf16 v[164:167], v[136:139], v[212:215], v[164:167]\n\t"
            "ds_read_b128 v[212:215], v232 offset:6272\n\t"
            "s_waitcnt lgkmcnt(6)\n\t"
            "v_mfma_f32_16x16x32_bf16 v[168:171], v[0:3], v[204:207], 0\n\t"
            "v_cvt_pk_bf16_f32 v156, v156, v157\n\t"
            "v_cvt_pk_bf16_f32 v157, v158, v159\n\t"
            "v_mfma_f32_16x16x32_bf16 v[168:171], v[4:7], v[208:211], v[168:171]\n\t"
            "v_cvt_pk_bf16_f32 v158, v160, v161\n\t"
            "v_cvt_pk_bf16_f32 v159, v162, v163\n\t"
            "v_mfma_f32_16x16x32_bf16 v[172:175], v[28:31], v[208:211], 0\n\t"
            "v_mfma_f32_16x16x32_bf16 v[172:175], v[24:27], v[204:207], v[172:175]\n\t"
            "v_mfma_f32_16x16x32_bf16 v[176:179], v[48:51], v[204:207], 0\n\t"
            "s_waitcnt lgkmcnt(4)\n\t"
            "v_mul_f32 v144, v236, v144\n\t"
            "v_mul_f32 v145, v236, v145\n\t"
            "v_mfma_f32_16x16x32_bf16 v[176:179], v[52:55], v[208:211], v[176:179]\n\t"
            "v_mul_f32 v152, v236, v152\n\t"
            "v_mul_f32 v153, v236, v153\n\t"
            "v_mfma_f32_16x16x32_bf16 v[180:183], v[76:79], v[208:211], 0\n\t"
            "v_mul_f32 v146, v236, v146\n\t"
            "v_mul_f32 v147, v236, v147\n\t"
            "v_mfma_f32_16x16x32_bf16 v[180:183], v[72:75], v[204:207], v[180:183]\n\t"
            "v_mul_f32 v154, v236, v154\n\t"
            "v_mul_f32 v155, v236, v155\n\t"
            "v_mfma_f32_16x16x32_bf16 v[184:187], v[96:99], v[204:207], 0\n\t"
            "v_cvt_pk_bf16_f32 v144, v144, v145\n\t"
            "v_cvt_pk_bf16_f32 v152, v152, v153\n\t"
            "v_mfma_f32_16x16x32_bf16 v[184:187], v[100:103], v[208:211], v[184:187]\n\t"
            "v_mul_f32 v148, v236, v148\n\t"
            "v_mul_f32 v149, v236, v149\n\t"
            "v_mfma_f32_16x16x32_bf16 v[188:191], v[124:127], v[208:211], 0\n\t"
            "ds_read_b128 v[208:211], v233 offset:6400\n\t"
            "v_mul_f32 v150, v236, v150\n\t"
            "v_mfma_f32_16x16x32_bf16 v[188:191], v[120:123], v[204:207], v[188:191]\n\t"
            "ds_read_b128 v[204:207], v232 offset:6400\n\t"
            "v_mul_f32 v151, v236, v151\n\t"
            "s_waitcnt lgkmcnt(2)\n\t"
            "v_mfma_f32_16x16x32_bf16 v[168:171], v[8:11], v[212:215], v[168:171]\n\t"
            "v_cvt_pk_bf16_f32 v145, v146, v147\n\t"
            "v_cvt_pk_bf16_f32 v153, v154, v155\n\t"
            "v_mfma_f32_16x16x32_bf16 v[168:171], v[12:15], v[224:227], v[168:171]\n\t"
            "v_cvt_pk_bf16_f32 v146, v148, v149\n\t"
            "v_cvt_pk_bf16_f32 v147, v150, v151\n\t"
            "v_mfma_f32_16x16x32_bf16 v[172:175], v[36:39], v[224:227], v[172:175]\n\t"
            "v_cvt_pk_bf16_f32 v154, v164, v165\n\t"
            "v_cvt_pk_bf16_f32 v155, v166, v167\n\t"
            "v_mfma_f32_16x16x32_bf16 v[172:175], v[32:35], v[212:215], v[172:175]\n\t"
            "v_mul_f32 v236, 0x3e553b94, v236\n\t"
            "v_mfma_f32_16x16x32_bf16 v[176:179], v[56:59], v[212:215], v[176:179]\n\t"
            "v_mfma_f32_16x16x32_bf16 v[176:179], v[60:63], v[224:227], v[176:179]\n\t"
            "v_mfma_f32_16x16x32_bf16 v[180:183], v[84:87], v[224:227], v[180:183]\n\t"
            "v_mfma_f32_16x16x32_bf16 v[180:183], v[80:83], v[212:215], v[180:183]\n\t"
            "v_mfma_f32_16x16x32_bf16 v[184:187], v[104:107], v[212:215], v[184:187]\n\t"
            "v_mfma_f32_16x16x32_bf16 v[184:187], v[108:111], v[224:227], v[184:187]\n\t"
            "v_mfma_f32_16x16x32_bf16 v[188:191], v[132:135], v[224:227], v[188:191]\n\t"
            "ds_read_b128 v[224:227], v233 offset:12288\n\t"
            "v_mfma_f32_16x16x32_bf16 v[188:191], v[128:131], v[212:215], v[188:191]\n\t"
            "ds_read_b128 v[212:215], v232 offset:12288\n\t"
            "s_waitcnt lgkmcnt(2)\n\t"
            "v_mfma_f32_16x16x32_bf16 v[168:171], v[16:19], v[204:207], v[168:171]\n\t"
            "v_mfma_f32_16x16x32_bf16 v[168:171], v[20:23], v[208:211], v[168:171]\n\t"
            "v_mfma_f32_16x16x32_bf16 v[172:175], v[44:47], v[208:211], v[172:175]\n\t"
            "v_mfma_f32_16x16x32_bf16 v[172:175], v[40:43], v[204:207], v[172:175]\n\t"
            "v_mfma_f32_16x16x32_bf16 v[176:179], v[64:67], v[204:207], v[176:179]\n\t"
            "v_mfma_f32_16x16x32_bf16 v[176:179], v[68:71], v[208:211], v[176:179]\n\t"
            "v_mfma_f32_16x16x32_bf16 v[180:183], v[92:95], v[208:211], v[180:183]\n\t"
            "v_mfma_f32_16x16x32_bf16 v[180:183], v[88:91], v[204:207], v[180:183]\n\t"
            "v_mfma_f32_16x16x32_bf16 v[184:187], v[112:115], v[204:207], v[184:187]\n\t"
            "v_mfma_f32_16x16x32_bf16 v[184:187], v[116:119], v[208:211], v[184:187]\n\t"
            "v_mfma_f32_16x16x32_bf16 v[188:191], v[140:143], v[208:211], v[188:191]\n\t"
            "ds_read_b128 v[208:211], v233 offset:12416\n\t"
            "v_mfma_f32_16x16x32_bf16 v[188:191], v[136:139], v[204:207], v[188:191]\n\t"
            "ds_read_b128 v[204:207], v232 offset:12416\n\t"
            "s_waitcnt lgkmcnt(0)\n\t"
            "s_nop 7\n\t"
            ".p2align 6\n\t"
            "s_nop 0\n\t"
            "s_nop 0\n\t"
            "s_nop 0\n\t"
            ".Lwa_loop%=:\n\t"
            "s_waitcnt lgkmcnt(2)\n\t"
            "v_mfma_f32_16x16x32_bf16 v[192:195], v[0:3], v[212:215], 0\n\t"
            "s_bfe_u32 s49, s70, 0x30001\n\t"
            "s_lshl_b32 s49, s49, 4\n\t"
            "s_or_b32 s73, s77, s49\n\t"
            "v_mfma_f32_16x16x32_bf16 v[192:195], v[4:7], v[224:227], v[192:195]\n\t"
            "v_mad_u32_u24 v220, v246, s72, v253\n\t"
            "v_mad_u32_u24 v216, v246, s72, v252\n\t"
            "v_mfma_f32_16x16x32_bf16 v[148:151], v[28:31], v[224:227], 0\n\t"
            "ds_read_b64 v[222:223], v220 offset:160\n\t"
            "ds_read_b64 v[220:221], v220 offset:128\n\t"
            "v_mfma_f32_16x16x32_bf16 v[148:151], v[24:27], v[212:215], v[148:151]\n\t"
            "v_mul_f32 v176, v237, v176\n\t"
            "v_mul_f32 v177, v237, v177\n\t"
            "v_mfma_f32_16x16x32_bf16 v[196:199], v[48:51], v[212:215], 0\n\t"
            "v_mul_f32 v178, v237, v178\n\t"
            "v_mul_f32 v179, v237, v179\n\t"
            "v_mfma_f32_16x16x32_bf16 v[196:199], v[52:55], v[224:227], v[196:199]\n\t"
            "v_cvt_pk_bf16_f32 v176, v176, v177\n\t"
            "v_mul_f32 v168, v237, v168\n\t"
            "v_mfma_f32_16x16x32_bf16 v[200:203], v[76:79], v[224:227], 0\n\t"
            "v_mul_f32 v169, v237, v169\n\t"
            "v_cvt_pk_bf16_f32 v177, v178, v179\n\t"
            "v_mfma_f32_16x16x32_bf16 v[200:203], v[72:75], v[212:215], v[200:203]\n\t"
            "v_mul_f32 v170, v237, v170\n\t"
            "v_mul_f32 v171, v237, v171\n\t"
            "v_mfma_f32_16x16x32_bf16 v[160:163], v[96:99], v[212:215], 0\n\t"
            "v_cvt_pk_bf16_f32 v168, v168, v169\n\t"
            "v_cvt_pk_bf16_f32 v180, v180, v181\n\t"
            "v_mfma_f32_16x16x32_bf16 v[160:163], v[100:103], v[224:227], v[160:163]\n\t"
            "v_cvt_pk_bf16_f32 v178, v188, v189\n\t"
            "v_cvt_pk_bf16_f32 v179, v190, v191\n\t"
            "v_mfma_f32_16x16x32_bf16 v[164:167], v[124:127], v[224:227], 0\n\t"
            "ds_read_b128 v[224:227], v233 offset:12544\n\t"
            "v_mul_f32 v172, v237, v172\n\t"
            "v_mfma_f32_16x16x32_bf16 v[164:167], v[120:123], v[212:215], v[164:167]\n\t"
            "ds_read_b128 v[212:215], v232 offset:12544\n\t"
            "v_mul_f32 v173, v237, v173\n\t"
            "s_waitcnt lgkmcnt(4)\n\t"
            "v_mfma_f32_16x16x32_bf16 v[192:195], v[8:11], v[204:207], v[192:195]\n\t"
            "v_mul_f32 v174, v237, v174\n\t"
            "v_mul_f32 v175, v237, v175\n\t"
            "v_mfma_f32_16x16x32_bf16 v[192:195], v[12:15], v[208:211], v[192:195]\n\t"
            "v_cvt_pk_bf16_f32 v169, v170, v171\n\t"
            "v_cvt_pk_bf16_f32 v181, v182, v183\n\t"
            "v_mfma_f32_16x16x32_bf16 v[196:199], v[56:59], v[204:207], v[196:199]\n\t"
            "v_cvt_pk_bf16_f32 v170, v172, v173\n\t"
            "v_cvt_pk_bf16_f32 v171, v174, v175\n\t"
            "v_mfma_f32_16x16x32_bf16 v[196:199], v[60:63], v[208:211], v[196:199]\n\t"
            "v_cvt_pk_bf16_f32 v182, v184, v185\n\t"
            "v_cvt_pk_bf16_f32 v183, v186, v187\n\t"
            "v_mfma_f32_16x16x32_bf16 v[160:163], v[104:107], v[204:207], v[160:163]\n\t"
            "v_mov_b32 v230, s76\n\t"
            "v_mov_b32 v239, s74\n\t"
            "v_mfma_f32_16x16x32_bf16 v[160:163], v[108:111], v[208:211], v[160:163]\n\t"
            "s_add_u32 s70, s70, 1\n\t"
            "s_add_u32 s72, s72, 800\n\t"
            "s_add_u32 s74, s74, 800\n\t"
            "s_add_u32 s76, s76, 800\n\t"
            "v_mfma_f32_16x16x32_bf16 v[148:151], v[36:39], v[208:211], v[148:151]\n\t"
            "s_add_u32 s77, s77, 384\n\t"
            "s_xor_b32 s48, s48, s47\n\t"
            "s_sub_u32 s44, s44, 1\n\t"
            "v_mfma_f32_16x16x32_bf16 v[148:151], v[32:35], v[204:207], v[148:151]\n\t"
            "ds_read_b32 v230, v230\n\t"
            "ds_read_b32 v239, v239\n\t"
            "v_mfma_f32_16x16x32_bf16 v[200:203], v[84:87], v[208:211], v[200:203]\n\t"
            "v_mul_f32 v237, 0x3e553b94, v237\n\t"
            "v_mfma_f32_16x16x32_bf16 v[200:203], v[80:83], v[204:207], v[200:203]\n\t"
            "v_mfma_f32_16x16x32_bf16 v[164:167], v[132:135], v[208:211], v[164:167]\n\t"
            "v_mfma_f32_16x16x32_bf16 v[164:167], v[128:131], v[204:207], v[164:167]\n\t"
            "s_waitcnt lgkmcnt(2)\n\t"
            "v_mfma_f32_16x16x32_bf16 v[192:195], v[16:19], v[212:215], v[192:195]\n\t"
            "v_mfma_f32_16x16x32_bf16 v[192:195], v[20:23], v[224:227], v[192:195]\n\t"
            "v_mfma_f32_16x16x32_bf16 v[196:199], v[64:67], v[212:215], v[196:199]\n\t"
            "v_mfma_f32_16x16x32_bf16 v[196:199], v[68:71], v[224:227], v[196:199]\n\t"
            "v_mfma_f32_16x16x32_bf16 v[160:163], v[112:115], v[212:215], v[160:163]\n\t"
            "v_mfma_f32_16x16x32_bf16 v[160:163], v[116:119], v[224:227], v[160:163]\n\t"
            "v_mfma_f32_16x16x32_bf16 v[148:151], v[44:47], v[224:227], v[148:151]\n\t"
            "v_mfma_f32_16x16x32_bf16 v[148:151], v[40:43], v[212:215], v[148:151]\n\t"
            "s_nop 1\n\t"
            "v_mul_f32 v192, v238, v192\n\t"
            "v_mfma_f32_16x16x32_bf16 v[200:203], v[92:95], v[224:227], v[200:203]\n\t"
            "v_mul_f32 v193, v238, v193\n\t"
            "v_mul_f32 v194, v238, v194\n\t"
            "v_mfma_f32_16x16x32_bf16 v[200:203], v[88:91], v[212:215], v[200:203]\n\t"
            "v_mul_f32 v195, v238, v195\n\t"
            "v_cvt_pk_bf16_f32 v192, v192, v193\n\t"
            "v_mfma_f32_16x16x32_bf16 v[164:167], v[140:143], v[224:227], v[164:167]\n\t"
            "ds_read_b128 v[224:227], v216 offset:64\n\t"
            "ds_read_b128 v[216:219], v216\n\t"
            "v_mfma_f32_16x16x32_bf16 v[164:167], v[136:139], v[212:215], v[164:167]\n\t"
            "v_mul_f32 v148, v238, v148\n\t"
            "v_mul_f32 v149, v238, v149\n\t"
            "v_mfma_f32_16x16x16_bf16 v[172:175], v[220:221], v[154:155], 0\n\t"
            "v_mul_f32 v150, v238, v150\n\t"
            "v_mul_f32 v151, v238, v151\n\t"
            "v_cvt_pk_bf16_f32 v193, v194, v195\n\t"
            "v_mfma_f32_16x16x16_bf16 v[184:187], v[152:153], v[178:179], 0\n\t"
            "v_mul_f32 v196, v238, v196\n\t"
            "v_mul_f32 v197, v238, v197\n\t"
            "v_cvt_pk_bf16_f32 v194, v148, v149\n\t"
            "v_mfma_f32_16x16x16_bf16 v[208:211], v[220:221], v[178:179], 0\n\t"
            "v_cvt_pk_bf16_f32 v195, v150, v151\n\t"
            "v_cvt_pk_bf16_f32 v200, v200, v201\n\t"
            "v_mul_f32 v198, v238, v198\n\t"
            "v_mfma_f32_16x16x16_bf16 v[148:151], v[152:153], v[154:155], 0\n\t"
            "v_mul_f32 v199, v238, v199\n\t"
            "v_cvt_pk_bf16_f32 v196, v196, v197\n\t"
            "v_cvt_pk_bf16_f32 v201, v202, v203\n\t"
            "v_mfma_f32_16x16x16_bf16 v[204:207], v[176:177], v[178:179], 0\n\t"
            "v_cvt_pk_bf16_f32 v197, v198, v199\n\t"
            "v_cvt_pk_bf16_f32 v202, v160, v161\n\t"
            "v_cvt_pk_bf16_f32 v203, v162, v163\n\t"
            "v_mfma_f32_16x16x16_bf16 v[160:163], v[176:177], v[154:155], 0\n\t"
            "v_cvt_pk_bf16_f32 v198, v164, v165\n\t"
            "v_cvt_pk_bf16_f32 v199, v166, v167\n\t"
            "s_setprio 0\n\t"
            "s_setprio 3\n\t"
            "v_mfma_f32_16x16x16_bf16 v[164:167], v[196:197], v[154:155], 0\n\t"
            "v_mul_f32 v238, 0x3e553b94, v238\n\t"
            "v_mfma_f32_16x16x16_bf16 v[188:191], v[196:197], v[178:179], 0\n\t"
            "v_mfma_f32_16x16x32_bf16 v[148:151], v[144:147], v[156:159], v[148:151]\n\t"
            "s_waitcnt lgkmcnt(0)\n\t"
            "v_mfma_f32_16x16x32_bf16 v[172:175], v[216:219], v[156:159], v[172:175]\n\t"
            "v_mfma_f32_16x16x32_bf16 v[184:187], v[144:147], v[180:183], v[184:187]\n\t"
            "v_mfma_f32_16x16x32_bf16 v[208:211], v[216:219], v[180:183], v[208:211]\n\t"
            "v_mfma_f32_16x16x32_bf16 v[160:163], v[168:171], v[156:159], v[160:163]\n\t"
            "v_mfma_f32_16x16x32_bf16 v[204:207], v[168:171], v[180:183], v[204:207]\n\t"
            "v_mfma_f32_16x16x32_bf16 v[164:167], v[192:195], v[156:159], v[164:167]\n\t"
            "v_mfma_f32_16x16x32_bf16 v[188:191], v[192:195], v[180:183], v[188:191]\n\t"
            "s_nop 1\n\t"
            "v_max3_f32 v228, v172, v148, v149\n\t"
            "v_mfma_f32_16x16x16_bf16 v[212:215], v[222:223], v[152:153], 0\n\t"
            "v_max3_f32 v228, v228, v150, v151\n\t"
            "v_max3_f32 v229, v208, v184, v185\n\t"
            "v_mfma_f32_16x16x16_bf16 v[156:159], v[222:223], v[196:197], 0\n\t"
            "v_max3_f32 v229, v229, v186, v187\n\t"
            "v_max3_f32 v228, v228, v160, v161\n\t"
            "v_max3_f32 v229, v229, v204, v205\n\t"
            "v_mfma_f32_16x16x32_bf16 v[212:215], v[224:227], v[144:147], v[212:215]\n\t"
            "v_max3_f32 v228, v228, v162, v163\n\t"
            "v_max3_f32 v229, v229, v206, v207\n\t"
            "v_mfma_f32_16x16x32_bf16 v[156:159], v[224:227], v[192:195], v[156:159]\n\t"
            "v_max3_f32 v228, v228, v164, v165\n\t"
            "v_max3_f32 v229, v229, v188, v189\n\t"
            "v_mfma_f32_16x16x16_bf16 v[180:183], v[152:153], v[198:199], 0\n\t"
            "v_max3_f32 v228, v228, v166, v167\n\t"
            "v_max3_f32 v229, v229, v190, v191\n\t"
            "s_nop 1\n\t"
            "v_permlane32_swap_b32 v228, v229\n\t"
            "v_max_f32 v228, v228, v229\n\t"
            "v_mov_b32 v231, v228\n\t"
            "v_mfma_f32_16x16x32_bf16 v[180:183], v[144:147], v[200:203], v[180:183]\n\t"
            "s_setprio 0\n\t"
            "v_permlane16_swap_b32 v228, v231\n\t"
            "v_max_f32 v228, v228, v231\n\t"
            "v_mov_b32 v229, v228\n\t"
            "s_nop 1\n\t"
            "v_permlane32_swap_b32 v228, v229\n\t"
            "v_mul_f32_e64 v228, -v228, v236\n\t"
            "v_fma_f32 v148, v148, v236, v228\n\t"
            "v_fma_f32 v149, v149, v236, v228\n\t"
            "v_exp_f32 v148, v148\n\t"
            "v_exp_f32 v149, v149\n\t"
            "v_fma_f32 v150, v150, v236, v228\n\t"
            "v_fma_f32 v151, v151, v236, v228\n\t"
            "v_exp_f32 v150, v150\n\t"
            "v_exp_f32 v151, v151\n\t"
            "v_fma_f32 v160, v160, v236, v228\n\t"
            "v_fma_f32 v161, v161, v236, v228\n\t"
            "v_fma_f32 v162, v162, v236, v228\n\t"
            "v_fma_f32 v163, v163, v236, v228\n\t"
            "v_exp_f32 v160, v160\n\t"
            "v_exp_f32 v161, v161\n\t"
            "v_exp_f32 v162, v162\n\t"
            "v_exp_f32 v163, v163\n\t"
            "v_cvt_pk_bf16_f32 v148, v148, v149\n\t"
            "v_cvt_pk_bf16_f32 v149, v150, v151\n\t"
            "v_cvt_pk_bf16_f32 v150, v160, v161\n\t"
            "v_cvt_pk_bf16_f32 v151, v162, v163\n\t"
            "s_setprio 3\n\t"
            "v_mfma_f32_16x16x16_bf16 v[160:163], v[222:223], v[176:177], 0\n\t"
            "s_setprio 0\n\t"
            "v_mul_f32_e64 v229, -v229, v237\n\t"
            "v_fma_f32 v188, v188, v237, v229\n\t"
            "v_fma_f32 v189, v189, v237, v229\n\t"
            "v_exp_f32 v188, v188\n\t"
            "v_exp_f32 v189, v189\n\t"
            "v_fma_f32 v190, v190, v237, v229\n\t"
            "s_setprio 3\n\t"
            "v_mfma_f32_16x16x32_bf16 v[160:163], v[224:227], v[168:171], v[160:163]\n\t"
            "s_setprio 0\n\t"
            "v_fma_f32 v191, v191, v237, v229\n\t"
            "v_fma_f32 v208, v208, v237, v229\n\t"
            "v_fma_f32 v184, v184, v237, v229\n\t"
            "v_fma_f32 v185, v185, v237, v229\n\t"
            "v_exp_f32 v190, v190\n\t"
            "v_exp_f32 v191, v191\n\t"
            "v_exp_f32 v208, v208\n\t"
            "v_exp_f32 v184, v184\n\t"
            "v_max3_f32 v234, v212, v160, v156\n\t"
            "v_max_f32 v234, v234, v230\n\t"
            "v_exp_f32 v185, v185\n\t"
            "v_fma_f32 v186, v186, v237, v229\n\t"
            "v_fma_f32 v187, v187, v237, v229\n\t"
            "v_fma_f32 v164, v164, v236, v228\n\t"
            "v_fma_f32 v165, v165, v236, v228\n\t"
            "v_exp_f32 v186, v186\n\t"
            "v_exp_f32 v187, v187\n\t"
            "v_fma_f32 v204, v204, v237, v229\n\t"
            "v_fma_f32 v205, v205, v237, v229\n\t"
            "v_fma_f32 v206, v206, v237, v229\n\t"
            "v_fma_f32 v207, v207, v237, v229\n\t"
            "v_cvt_pk_bf16_f32 v188, v188, v189\n\t"
            "v_exp_f32 v164, v164\n\t"
            "v_exp_f32 v165, v165\n\t"
            "v_fma_f32 v166, v166, v236, v228\n\t"
            "v_fma_f32 v167, v167, v236, v228\n\t"
            "v_fma_f32 v172, v172, v236, v228\n\t"
            "v_exp_f32 v204, v204\n\t"
            "v_exp_f32 v205, v205\n\t"
            "v_exp_f32 v206, v206\n\t"
            "v_exp_f32 v207, v207\n\t"
            "v_cvt_pk_bf16_f32 v189, v190, v191\n\t"
            "v_cvt_pk_bf16_f32 v208, v208, 0\n\t"
            "v_max_f32_dpp v234, v234, v234 row_ror:8 row_mask:0xf bank_mask:0xf\n\t"
            "v_exp_f32 v166, v166\n\t"
            "v_exp_f32 v167, v167\n\t"
            "v_exp_f32 v172, v172\n\t"
            "v_cvt_pk_bf16_f32 v184, v184, v185\n\t"
            "v_cndmask_b32_e64 v190, 0, v208, s[58:59]\n\t"
            "s_setprio 3\n\t"
            "v_mfma_f32_16x16x16_bf16 v[208:211], v[220:221], v[198:199], 0\n\t"
            "v_cvt_pk_bf16_f32 v185, v186, v187\n\t"
            "v_cvt_pk_bf16_f32 v164, v164, v165\n\t"
            "v_cvt_pk_bf16_f32 v186, v204, v205\n\t"
            "v_cvt_pk_bf16_f32 v187, v206, v207\n\t"
            "v_mfma_f32_16x16x16_bf16 v[204:207], v[176:177], v[198:199], 0\n\t"
            "v_max_f32_dpp v234, v234, v234 row_ror:4 row_mask:0xf bank_mask:0xf\n\t"
            "v_cvt_pk_bf16_f32 v165, v166, v167\n\t"
            "v_cvt_pk_bf16_f32 v172, v172, 0\n\t"
            "v_mfma_f32_16x16x32_bf16 v[208:211], v[216:219], v[200:203], v[208:211]\n\t"
            "v_cndmask_b32_e64 v166, 0, v172, s[58:59]\n\t"
            "v_max_f32_dpp v234, v234, v234 row_ror:2 row_mask:0xf bank_mask:0xf\n\t"
            "v_mfma_f32_16x16x16_bf16 v[172:175], v[196:197], v[198:199], 0\n\t"
            "v_cndmask_b32_e64 v235, v212, v160, s[60:61]\n\t"
            "v_cndmask_b32_e64 v235, v235, v156, s[62:63]\n\t"
            "v_max_f32_dpp v234, v234, v234 row_ror:1 row_mask:0xf bank_mask:0xf\n\t"
            "v_mfma_f32_16x16x32_bf16 v[204:207], v[168:171], v[200:203], v[204:207]\n\t"
            "v_cndmask_b32_e64 v235, v235, v230, s[64:65]\n\t"
            "v_sub_f32 v235, v235, v234\n\t"
            "v_mfma_f32_16x16x32_bf16 v[172:175], v[192:195], v[200:203], v[172:175]\n\t"
            "s_setprio 0\n\t"
            "v_mul_f32 v235, v239, v235\n\t"
            "v_exp_f32 v235, v235\n\t"
            "v_max3_f32 v230, v208, v180, v181\n\t"
            "v_cvt_pk_bf16_f32 v235, v235, 0\n\t"
            "v_cndmask_b32_e64 v235, 0, v235, s[66:67]\n\t"
            "v_max3_f32 v230, v230, v182, v183\n\t"
            "v_max3_f32 v230, v230, v204, v205\n\t"
            "v_max3_f32 v230, v230, v206, v207\n\t"
            "v_max3_f32 v230, v230, v172, v173\n\t"
            "v_max3_f32 v230, v230, v174, v175\n\t"
            "v_mov_b32 v231, v230\n\t"
            "v_mov_b32 v167, 0\n\t"
            "v_mov_b32 v191, 0\n\t"
            "v_permlane32_swap_b32 v230, v231\n\t"
            "v_max_f32 v230, v230, v231\n\t"
            "v_mov_b32 v231, v230\n\t"
            "ds_write_b16 v250, v235 offset:256\n\t"
            "s_nop 0\n\t"
            "v_permlane16_swap_b32 v230, v231\n\t"
            "v_max_f32 v230, v230, v231\n\t"
            "v_mul_f32_e64 v230, -v230, v238\n\t"
            "v_fma_f32 v180, v180, v238, v230\n\t"
            "v_fma_f32 v181, v181, v238, v230\n\t"
            "v_fma_f32 v172, v172, v238, v230\n\t"
            "v_fma_f32 v173, v173, v238, v230\n\t"
            "v_exp_f32 v180, v180\n\t"
            "v_exp_f32 v181, v181\n\t"
            "v_fma_f32 v182, v182, v238, v230\n\t"
            "v_fma_f32 v183, v183, v238, v230\n\t"
            "v_exp_f32 v172, v172\n\t"
            "v_exp_f32 v173, v173\n\t"
            "v_fma_f32 v174, v174, v238, v230\n\t"
            "v_fma_f32 v175, v175, v238, v230\n\t"
            "v_fma_f32 v208, v208, v238, v230\n\t"
            "v_exp_f32 v182, v182\n\t"
            "v_exp_f32 v183, v183\n\t"
            "v_fma_f32 v204, v204, v238, v230\n\t"
            "v_fma_f32 v205, v205, v238, v230\n\t"
            "v_fma_f32 v206, v206, v238, v230\n\t"
            "v_fma_f32 v207, v207, v238, v230\n\t"
            "v_exp_f32 v174, v174\n\t"
            "v_exp_f32 v175, v175\n\t"
            "v_exp_f32 v208, v208\n\t"
            "v_exp_f32 v204, v204\n\t"
            "v_exp_f32 v205, v205\n\t"
            "v_exp_f32 v206, v206\n\t"
            "v_exp_f32 v207, v207\n\t"
            "v_cvt_pk_bf16_f32 v180, v180, v181\n\t"
            "v_cvt_pk_bf16_f32 v172, v172, v173\n\t"
            "v_cvt_pk_bf16_f32 v181, v182, v183\n\t"
            "v_cvt_pk_bf16_f32 v173, v174, v175\n\t"
            "v_cvt_pk_bf16_f32 v208, v208, 0\n\t"
            "v_cvt_pk_bf16_f32 v182, v204, v205\n\t"
            "v_cvt_pk_bf16_f32 v183, v206, v207\n\t"
            "v_cndmask_b32_e64 v174, 0, v208, s[58:59]\n\t"
            "v_mov_b32 v175, 0\n\t"
            "s_barrier\n\t"
            "s_setprio 2\n\t"
            "v_mfma_f32_16x16x32_bf16 v[200:203], v[240:243], v[148:151], 0\n\t"
            "ds_read_b64 v[176:177], v247\n\t"
            "ds_read_b64 v[196:197], v247 offset:1536\n\t"
            "v_mfma_f32_16x16x32_bf16 v[200:203], v[240:243], v[164:167], v[200:203]\n\t"
            "ds_read_b64 v[220:221], v247 offset:3072\n\t"
            "v_xor_b32 v234, s73, v254\n\t"
            "v_add_u32 v235, s45, v248\n\t"
            "v_add_u32 v232, s48, v244\n\t"
            "v_xor_b32 v233, 64, v232\n\t"
            "v_mov_b32 v179, 0\n\t"
            "v_mov_b32 v199, 0\n\t"
            "v_mov_b32 v223, 0\n\t"
            "v_rcp_f32 v201, v200\n\t"
            "ds_read_b128 v[168:171], v235\n\t"
            "ds_read_u16 v178, v234\n\t"
            "ds_read_b128 v[192:195], v235 offset:1536\n\t"
            "ds_read_u16 v198, v234 offset:128\n\t"
            "ds_read_b128 v[216:219], v235 offset:3072\n\t"
            "ds_read_u16 v222, v234 offset:256\n\t"
            "ds_read_b128 v[204:207], v232\n\t"
            "ds_read_b128 v[208:211], v233\n\t"
            "v_add_u32 v235, s48, v248\n\t"
            "v_mad_u32_u24 v234, v246, s75, v253\n\t"
            "ds_read_b128 v[228:231], v235 offset:18816\n\t"
            "s_waitcnt lgkmcnt(7)\n\t"
            "v_mfma_f32_16x16x32_bf16 v[224:227], v[168:171], v[148:151], 0\n\t"
            "v_mfma_f32_16x16x32_bf16 v[224:227], v[176:179], v[164:167], v[224:227]\n\t"
            "s_waitcnt lgkmcnt(5)\n\t"
            "v_mfma_f32_16x16x32_bf16 v[212:215], v[192:195], v[148:151], 0\n\t"
            "s_waitcnt lgkmcnt(0)\n\t"
            "v_add_f32 v236, v228, v230\n\t"
            "v_mfma_f32_16x16x32_bf16 v[212:215], v[196:199], v[164:167], v[212:215]\n\t"
            "v_add_f32 v237, v229, v231\n\t"
            "v_mfma_f32_16x16x32_bf16 v[148:151], v[216:219], v[148:151], 0\n\t"
            "ds_read_b128 v[228:231], v235 offset:19840\n\t"
            "v_mfma_f32_16x16x32_bf16 v[148:151], v[220:223], v[164:167], v[148:151]\n\t"
            "v_mul_f32 v224, v201, v224\n\t"
            "v_mul_f32 v225, v201, v225\n\t"
            "v_mfma_f32_16x16x32_bf16 v[144:147], v[0:3], v[204:207], 0\n\t"
            "s_nop 0\n\t"
            "v_mul_f32 v212, v201, v212\n\t"
            "v_mul_f32 v213, v201, v213\n\t"
            "v_mfma_f32_16x16x32_bf16 v[144:147], v[4:7], v[208:211], v[144:147]\n\t"
            "v_mul_f32 v226, v201, v226\n\t"
            "v_mul_f32 v227, v201, v227\n\t"
            "v_mfma_f32_16x16x32_bf16 v[152:155], v[48:51], v[204:207], 0\n\t"
            "v_cvt_pk_bf16_f32 v224, v224, v225\n\t"
            "v_mul_f32 v214, v201, v214\n\t"
            "v_mfma_f32_16x16x32_bf16 v[152:155], v[52:55], v[208:211], v[152:155]\n\t"
            "v_mul_f32 v215, v201, v215\n\t"
            "v_cvt_pk_bf16_f32 v212, v212, v213\n\t"
            "v_mfma_f32_16x16x32_bf16 v[156:159], v[76:79], v[208:211], 0\n\t"
            "v_cvt_pk_bf16_f32 v225, v226, v227\n\t"
            "v_cvt_pk_bf16_f32 v213, v214, v215\n\t"
            "v_mfma_f32_16x16x32_bf16 v[156:159], v[72:75], v[204:207], v[156:159]\n\t"
            "ds_write_b64 v245, v[224:225]\n\t"
            "v_mfma_f32_16x16x32_bf16 v[224:227], v[168:171], v[184:187], 0\n\t"
            "v_mul_f32 v148, v201, v148\n\t"
            "v_mul_f32 v149, v201, v149\n\t"
            "v_mfma_f32_16x16x32_bf16 v[224:227], v[176:179], v[188:191], v[224:227]\n\t"
            "ds_write_b64 v245, v[212:213] offset:128\n\t"
            "v_mfma_f32_16x16x32_bf16 v[212:215], v[192:195], v[184:187], 0\n\t"
            "v_mul_f32 v150, v201, v150\n\t"
            "v_mul_f32 v151, v201, v151\n\t"
            "v_mfma_f32_16x16x32_bf16 v[212:215], v[196:199], v[188:191], v[212:215]\n\t"
            "v_cvt_pk_bf16_f32 v148, v148, v149\n\t"
            "v_cvt_pk_bf16_f32 v149, v150, v151\n\t"
            "v_mfma_f32_16x16x32_bf16 v[200:203], v[240:243], v[184:187], 0\n\t"
            "s_waitcnt lgkmcnt(2)\n\t"
            "v_add_f32 v228, v228, v230\n\t"
            "v_add_f32 v236, v236, v228\n\t"
            "v_mfma_f32_16x16x32_bf16 v[200:203], v[240:243], v[188:191], v[200:203]\n\t"
            "ds_write_b64 v245, v[148:149] offset:256\n\t"
            "v_mfma_f32_16x16x32_bf16 v[184:187], v[216:219], v[184:187], 0\n\t"
            "v_add_f32 v229, v229, v231\n\t"
            "v_mul_f32 v236, 0x3baaaaab, v236\n\t"
            "v_mfma_f32_16x16x32_bf16 v[184:187], v[220:223], v[188:191], v[184:187]\n\t"
            "v_add_f32 v237, v237, v229\n\t"
            "v_mul_f32_e64 v236, v236, -v236\n\t"
            "v_mfma_f32_16x16x32_bf16 v[148:151], v[28:31], v[208:211], 0\n\t"
            "v_rcp_f32 v201, v200\n\t"
            "v_mfma_f32_16x16x32_bf16 v[148:151], v[24:27], v[204:207], v[148:151]\n\t"
            "v_mul_f32 v224, v201, v224\n\t"
            "v_mul_f32 v225, v201, v225\n\t"
            "v_mfma_f32_16x16x32_bf16 v[160:163], v[96:99], v[204:207], 0\n\t"
            "v_mul_f32 v226, v201, v226\n\t"
            "v_mul_f32 v227, v201, v227\n\t"
            "v_mfma_f32_16x16x32_bf16 v[160:163], v[100:103], v[208:211], v[160:163]\n\t"
            "v_mul_f32 v212, v201, v212\n\t"
            "v_mul_f32 v213, v201, v213\n\t"
            "v_mfma_f32_16x16x32_bf16 v[164:167], v[124:127], v[208:211], 0\n\t"
            "ds_read_b128 v[208:211], v233 offset:128\n\t"
            "v_mul_f32 v214, v201, v214\n\t"
            "v_mfma_f32_16x16x32_bf16 v[164:167], v[120:123], v[204:207], v[164:167]\n\t"
            "ds_read_b128 v[204:207], v232 offset:128\n\t"
            "v_mul_f32 v215, v201, v215\n\t"
            "v_mfma_f32_16x16x32_bf16 v[188:191], v[192:195], v[180:183], 0\n\t"
            "v_mul_f32 v184, v201, v184\n\t"
            "v_mul_f32 v185, v201, v185\n\t"
            "v_mfma_f32_16x16x32_bf16 v[188:191], v[196:199], v[172:175], v[188:191]\n\t"
            "v_mul_f32 v186, v201, v186\n\t"
            "v_mul_f32 v187, v201, v187\n\t"
            "v_mfma_f32_16x16x32_bf16 v[200:203], v[240:243], v[180:183], 0\n\t"
            "v_cvt_pk_bf16_f32 v224, v224, v225\n\t"
            "v_cvt_pk_bf16_f32 v225, v226, v227\n\t"
            "v_mfma_f32_16x16x32_bf16 v[200:203], v[240:243], v[172:175], v[200:203]\n\t"
            "ds_write_b64 v245, v[224:225] offset:6144\n\t"
            "v_mfma_f32_16x16x32_bf16 v[224:227], v[168:171], v[180:183], 0\n\t"
            "v_cvt_pk_bf16_f32 v184, v184, v185\n\t"
            "v_cvt_pk_bf16_f32 v185, v186, v187\n\t"
            "v_mfma_f32_16x16x32_bf16 v[224:227], v[176:179], v[172:175], v[224:227]\n\t"
            "ds_read_b64 v[186:187], v234 offset:288\n\t"
            "ds_write_b64 v245, v[184:185] offset:6400\n\t"
            "v_mfma_f32_16x16x32_bf16 v[180:183], v[216:219], v[180:183], 0\n\t"
            "ds_read_b64 v[184:185], v234 offset:256\n\t"
            "v_rcp_f32 v201, v200\n\t"
            "v_mfma_f32_16x16x32_bf16 v[180:183], v[220:223], v[172:175], v[180:183]\n\t"
            "v_mul_f32 v188, v201, v188\n\t"
            "v_mul_f32 v189, v201, v189\n\t"
            "s_waitcnt lgkmcnt(4)\n\t"
            "v_mfma_f32_16x16x32_bf16 v[144:147], v[8:11], v[204:207], v[144:147]\n\t"
            "v_mul_f32 v190, v201, v190\n\t"
            "v_mul_f32 v191, v201, v191\n\t"
            "v_mfma_f32_16x16x32_bf16 v[144:147], v[12:15], v[208:211], v[144:147]\n\t"
            "v_cvt_pk_bf16_f32 v188, v188, v189\n\t"
            "v_cvt_pk_bf16_f32 v189, v190, v191\n\t"
            "v_mfma_f32_16x16x32_bf16 v[148:151], v[36:39], v[208:211], v[148:151]\n\t"
            "ds_read_b64 v[190:191], v234 offset:352\n\t"
            "v_mul_f32 v224, v201, v224\n\t"
            "v_mfma_f32_16x16x32_bf16 v[148:151], v[32:35], v[204:207], v[148:151]\n\t"
            "ds_write_b64 v245, v[188:189] offset:12416\n\t"
            "v_mfma_f32_16x16x32_bf16 v[152:155], v[56:59], v[204:207], v[152:155]\n\t"
            "ds_read_b64 v[188:189], v234 offset:320\n\t"
            "v_mul_f32 v225, v201, v225\n\t"
            "v_mfma_f32_16x16x32_bf16 v[152:155], v[60:63], v[208:211], v[152:155]\n\t"
            "v_cvt_pk_bf16_f32 v212, v212, v213\n\t"
            "v_mul_f32 v226, v201, v226\n\t"
            "v_mfma_f32_16x16x32_bf16 v[156:159], v[84:87], v[208:211], v[156:159]\n\t"
            "v_mul_f32 v227, v201, v227\n\t"
            "v_cvt_pk_bf16_f32 v224, v224, v225\n\t"
            "v_mfma_f32_16x16x32_bf16 v[156:159], v[80:83], v[204:207], v[156:159]\n\t"
            "v_cvt_pk_bf16_f32 v213, v214, v215\n\t"
            "v_cvt_pk_bf16_f32 v225, v226, v227\n\t"
            "v_mfma_f32_16x16x32_bf16 v[160:163], v[104:107], v[204:207], v[160:163]\n\t"
            "v_mul_f32 v180, v201, v180\n\t"
            "v_mul_f32 v181, v201, v181\n\t"
            "v_mfma_f32_16x16x32_bf16 v[160:163], v[108:111], v[208:211], v[160:163]\n\t"
            "ds_write_b64 v245, v[212:213] offset:6272\n\t"
            "v_mfma_f32_16x16x32_bf16 v[164:167], v[132:135], v[208:211], v[164:167]\n\t"
            "ds_read_b128 v[212:215], v232 offset:256\n\t"
            "ds_read_b128 v[208:211], v233 offset:6144\n\t"
            "v_mfma_f32_16x16x32_bf16 v[164:167], v[128:131], v[204:207], v[164:167]\n\t"
            "ds_read_b128 v[204:207], v232 offset:6144\n\t"
            "ds_write_b64 v245, v[224:225] offset:12288\n\t"
            "ds_read_b128 v[224:227], v233 offset:256\n\t"
            "v_mul_f32 v182, v201, v182\n\t"
            "v_mul_f32 v183, v201, v183\n\t"
            "v_cvt_pk_bf16_f32 v180, v180, v181\n\t"
            "s_waitcnt lgkmcnt(6)\n\t"
            "v_mfma_f32_16x16x32_bf16 v[200:203], v[240:243], v[184:187], 0\n\t"
            "v_cvt_pk_bf16_f32 v181, v182, v183\n\t"
            "v_fmac_f32 v236, 0x3baaaaab, v237\n\t"
            "v_mfma_f32_16x16x32_bf16 v[200:203], v[240:243], v[188:191], v[200:203]\n\t"
            "ds_write_b64 v245, v[180:181] offset:12544\n\t"
            "v_mfma_f32_16x16x32_bf16 v[168:171], v[184:187], v[168:171], 0\n\t"
            "v_max_f32 v236, 0, v236\n\t"
            "v_add_f32 v236, 0x3727c5ac, v236\n\t"
            "v_mfma_f32_16x16x32_bf16 v[168:171], v[188:191], v[176:179], v[168:171]\n\t"
            "v_rsq_f32 v236, v236\n\t"
            "v_mfma_f32_16x16x32_bf16 v[192:195], v[184:187], v[192:195], 0\n\t"
            "s_nop 0\n\t"
            "v_rcp_f32 v201, v200\n\t"
            "v_mfma_f32_16x16x32_bf16 v[192:195], v[188:191], v[196:199], v[192:195]\n\t"
            "ds_write_b32 v249, v236\n\t"
            "v_mfma_f32_16x16x32_bf16 v[216:219], v[184:187], v[216:219], 0\n\t"
            "ds_read_b32 v236, v251\n\t"
            "ds_read_b32 v237, v251 offset:64\n\t"
            "v_mfma_f32_16x16x32_bf16 v[216:219], v[188:191], v[220:223], v[216:219]\n\t"
            "ds_read_b32 v238, v251 offset:128\n\t"
            "v_xad_u32 v202, s73, v254, v255\n\t"
            "s_waitcnt lgkmcnt(5)\n\t"
            "v_mfma_f32_16x16x32_bf16 v[144:147], v[16:19], v[212:215], v[144:147]\n\t"
            "v_cndmask_b32_e64 v203, v168, v192, s[60:61]\n\t"
            "v_mfma_f32_16x16x32_bf16 v[144:147], v[20:23], v[224:227], v[144:147]\n\t"
            "v_mfma_f32_16x16x32_bf16 v[148:151], v[44:47], v[224:227], v[148:151]\n\t"
            "s_nop 0\n\t"
            "v_cndmask_b32_e64 v203, v203, v216, s[62:63]\n\t"
            "v_mfma_f32_16x16x32_bf16 v[148:151], v[40:43], v[212:215], v[148:151]\n\t"
            "s_nop 0\n\t"
            "v_mul_f32_dpp v203, v201, v203 quad_perm:[0,0,0,0] row_mask:0xf bank_mask:0xf\n\t"
            "v_cvt_pk_bf16_f32 v203, v203, 0\n\t"
            "v_mfma_f32_16x16x32_bf16 v[152:155], v[64:67], v[212:215], v[152:155]\n\t"
            "v_mfma_f32_16x16x32_bf16 v[152:155], v[68:71], v[224:227], v[152:155]\n\t"
            "s_mov_b64 exec, s[68:69]\n\t"
            "ds_write_b16 v202, v203\n\t"
            "s_mov_b64 exec, 1\n\t"
            "ds_add_u32 v253, v246 offset:7856\n\t"
            "s_mov_b64 exec, -1\n\t"
            "v_mfma_f32_16x16x32_bf16 v[156:159], v[92:95], v[224:227], v[156:159]\n\t"
            "s_waitcnt lgkmcnt(4)\n\t"
            "v_mul_f32 v144, v236, v144\n\t"
            "v_mul_f32 v145, v236, v145\n\t"
            "v_mfma_f32_16x16x32_bf16 v[156:159], v[88:91], v[212:215], v[156:159]\n\t"
            "s_cmp_eq_u32 s44, 0\n\t"
            "s_cbranch_scc1 .Lwa_done%=\n\t"
            "v_mul_f32 v152, v236, v152\n\t"
            "v_mul_f32 v153, v236, v153\n\t"
            "v_mfma_f32_16x16x32_bf16 v[160:163], v[112:115], v[212:215], v[160:163]\n\t"
            "v_mul_f32 v146, v236, v146\n\t"
            "v_mul_f32 v147, v236, v147\n\t"
            "v_mfma_f32_16x16x32_bf16 v[160:163], v[116:119], v[224:227], v[160:163]\n\t"
            "v_mul_f32 v154, v236, v154\n\t"
            "v_mul_f32 v155, v236, v155\n\t"
            "v_mfma_f32_16x16x32_bf16 v[164:167], v[140:143], v[224:227], v[164:167]\n\t"
            "ds_read_b128 v[224:227], v233 offset:6272\n\t"
            "v_cvt_pk_bf16_f32 v144, v144, v145\n\t"
            "v_mfma_f32_16x16x32_bf16 v[164:167], v[136:139], v[212:215], v[164:167]\n\t"
            "ds_read_b128 v[212:215], v232 offset:6272\n\t"
            "v_cvt_pk_bf16_f32 v152, v152, v153\n\t"
            "v_mfma_f32_16x16x32_bf16 v[168:171], v[0:3], v[204:207], 0\n\t"
            "v_cvt_pk_bf16_f32 v156, v156, v157\n\t"
            "v_mul_f32 v148, v236, v148\n\t"
            "v_mfma_f32_16x16x32_bf16 v[168:171], v[4:7], v[208:211], v[168:171]\n\t"
            "v_mul_f32 v149, v236, v149\n\t"
            "v_mul_f32 v150, v236, v150\n\t"
            "v_mfma_f32_16x16x32_bf16 v[172:175], v[28:31], v[208:211], 0\n\t"
            "v_mul_f32 v151, v236, v151\n\t"
            "v_cvt_pk_bf16_f32 v145, v146, v147\n\t"
            "v_mfma_f32_16x16x32_bf16 v[172:175], v[24:27], v[204:207], v[172:175]\n\t"
            "v_cvt_pk_bf16_f32 v153, v154, v155\n\t"
            "v_cvt_pk_bf16_f32 v157, v158, v159\n\t"
            "v_mfma_f32_16x16x32_bf16 v[176:179], v[48:51], v[204:207], 0\n\t"
            "v_cvt_pk_bf16_f32 v146, v148, v149\n\t"
            "v_cvt_pk_bf16_f32 v147, v150, v151\n\t"
            "v_mfma_f32_16x16x32_bf16 v[176:179], v[52:55], v[208:211], v[176:179]\n\t"
            "v_cvt_pk_bf16_f32 v158, v160, v161\n\t"
            "v_cvt_pk_bf16_f32 v159, v162, v163\n\t"
            "v_mfma_f32_16x16x32_bf16 v[180:183], v[76:79], v[208:211], 0\n\t"
            "v_cvt_pk_bf16_f32 v154, v164, v165\n\t"
            "v_cvt_pk_bf16_f32 v155, v166, v167\n\t"
            "v_mfma_f32_16x16x32_bf16 v[180:183], v[72:75], v[204:207], v[180:183]\n\t"
            "v_mul_f32 v236, 0x3e553b94, v236\n\t"
            "v_mfma_f32_16x16x32_bf16 v[184:187], v[96:99], v[204:207], 0\n\t"
            "v_mfma_f32_16x16x32_bf16 v[184:187], v[100:103], v[208:211], v[184:187]\n\t"
            "v_mfma_f32_16x16x32_bf16 v[188:191], v[124:127], v[208:211], 0\n\t"
            "ds_read_b128 v[208:211], v233 offset:6400\n\t"
            "v_mfma_f32_16x16x32_bf16 v[188:191], v[120:123], v[204:207], v[188:191]\n\t"
            "ds_read_b128 v[204:207], v232 offset:6400\n\t"
            "s_waitcnt lgkmcnt(2)\n\t"
            "v_mfma_f32_16x16x32_bf16 v[168:171], v[8:11], v[212:215], v[168:171]\n\t"
            "v_mfma_f32_16x16x32_bf16 v[168:171], v[12:15], v[224:227], v[168:171]\n\t"
            "v_mfma_f32_16x16x32_bf16 v[172:175], v[36:39], v[224:227], v[172:175]\n\t"
            "v_mfma_f32_16x16x32_bf16 v[172:175], v[32:35], v[212:215], v[172:175]\n\t"
            "v_mfma_f32_16x16x32_bf16 v[176:179], v[56:59], v[212:215], v[176:179]\n\t"
            "v_mfma_f32_16x16x32_bf16 v[176:179], v[60:63], v[224:227], v[176:179]\n\t"
            "v_mfma_f32_16x16x32_bf16 v[180:183], v[84:87], v[224:227], v[180:183]\n\t"
            "v_mfma_f32_16x16x32_bf16 v[180:183], v[80:83], v[212:215], v[180:183]\n\t"
            "v_mfma_f32_16x16x32_bf16 v[184:187], v[104:107], v[212:215], v[184:187]\n\t"
            "v_mfma_f32_16x16x32_bf16 v[184:187], v[108:111], v[224:227], v[184:187]\n\t"
            "v_mfma_f32_16x16x32_bf16 v[188:191], v[132:135], v[224:227], v[188:191]\n\t"
            "ds_read_b128 v[224:227], v233 offset:12288\n\t"
            "v_mfma_f32_16x16x32_bf16 v[188:191], v[128:131], v[212:215], v[188:191]\n\t"
            "ds_read_b128 v[212:215], v232 offset:12288\n\t"
            "s_waitcnt lgkmcnt(2)\n\t"
            "v_mfma_f32_16x16x32_bf16 v[168:171], v[16:19], v[204:207], v[168:171]\n\t"
            "v_mfma_f32_16x16x32_bf16 v[168:171], v[20:23], v[208:211], v[168:171]\n\t"
            "v_mfma_f32_16x16x32_bf16 v[172:175], v[44:47], v[208:211], v[172:175]\n\t"
            "v_mfma_f32_16x16x32_bf16 v[172:175], v[40:43], v[204:207], v[172:175]\n\t"
            "v_mfma_f32_16x16x32_bf16 v[176:179], v[64:67], v[204:207], v[176:179]\n\t"
            "v_mfma_f32_16x16x32_bf16 v[176:179], v[68:71], v[208:211], v[176:179]\n\t"
            "v_mfma_f32_16x16x32_bf16 v[180:183], v[92:95], v[208:211], v[180:183]\n\t"
            "v_mfma_f32_16x16x32_bf16 v[180:183], v[88:91], v[204:207], v[180:183]\n\t"
            "v_mfma_f32_16x16x32_bf16 v[184:187], v[112:115], v[204:207], v[184:187]\n\t"
            "v_mfma_f32_16x16x32_bf16 v[184:187], v[116:119], v[208:211], v[184:187]\n\t"
            "v_mfma_f32_16x16x32_bf16 v[188:191], v[140:143], v[208:211], v[188:191]\n\t"
            "ds_read_b128 v[208:211], v233 offset:12416\n\t"
            "v_mfma_f32_16x16x32_bf16 v[188:191], v[136:139], v[204:207], v[188:191]\n\t"
            "ds_read_b128 v[204:207], v232 offset:12416\n\t"
            "s_branch .Lwa_loop%=\n\t"
            ".Lwa_done%=:\n\t"
            "s_waitcnt lgkmcnt(0)\n\t"
            "s_nop 7\n\t"
            "s_barrier\n\t"
            : "+{s44}"(s44), "+{s48}"(s48), "+{s70}"(s70), "+{s72}"(s72), "+{s73}"(s73), "+{s74}"(s74), "+{s76}"(s76), "+{s77}"(s77)
            : "{v244}"(pE_), "{v245}"(aO_), "{v246}"(sel_), "{v247}"(aVB_), "{v248}"(pST_), "{v249}"(aRSw_), "{v250}"(aPLw_), "{v251}"(aRSr_), "{v252}"(g16_), "{v253}"(g8_), "{v254}"(loVL_), "{v255}"(loOL_), "{v[240:243]}"(ones4), "{s47}"((ldsb + X_OFF) ^ (ldsb + X1_OFF)),
              "{s[58:59]}"(0xFFFFull), "{s[60:61]}"(0xFFFF0000ull), "{s[62:63]}"(0xFFFF00000000ull), "{s[64:65]}"(0xFFFF000000000000ull),
              "{s[66:67]}"(0x1FFFFFFFFFFFFull), "{s75}"(rsb), "{s45}"(ldsb + V_OFF + w * 4608), "{s[68:69]}"(0xFFFFFFFFFFFFull),
              "{s[84:87]}"(wr), "{s46}"(wave * 36864), "{s88}"(touch), "{s71}"(ldsb + O_OFF), "{s[54:55]}"(mrow), "{s[56:57]}"(0xF0000F0000F0000Full),
              "{s[78:79]}"(mrow & 0xF0000F0000F0000Full)
            : "v144", "v145", "v146", "v147", "v148", "v149", "v150", "v151", "v152", "v153", "v154", "v155", "v156", "v157", "v158", "v159", "v160", "v161", "v162", "v163", "v164", "v165", "v166", "v167", "v168", "v169", "v170", "v171", "v172", "v173", "v174", "v175", "v176", "v177", "v178", "v179", "v180", "v181", "v182", "v183", "v184", "v185", "v186", "v187", "v188", "v189", "v190", "v191", "v192", "v193", "v194", "v195", "v196", "v197", "v198", "v199", "v200", "v201", "v202", "v203", "v204", "v205", "v206", "v207", "v208", "v209", "v210", "v211", "v212", "v213", "v214", "v215", "v216", "v217", "v218", "v219", "v220", "v221", "v222", "v223", "v224", "v225", "v226", "v227", "v228", "v229", "v230", "v231", "v232", "v233", "v234", "v235", "v236", "v237", "v238", "v239", "v0", "v1", "v2", "v3", "v4", "v5", "v6", "v7", "v8", "v9", "v10", "v11", "v12", "v13", "v14", "v15", "v16", "v17", "v18", "v19", "v20", "v21", "v22", "v23", "v24", "v25", "v26", "v27", "v28", "v29", "v30", "v31", "v32", "v33", "v34", "v35", "v36", "v37", "v38", "v39", "v40", "v41", "v42", "v43", "v44", "v45", "v46", "v47", "v48", "v49", "v50", "v51", "v52", "v53", "v54", "v55", "v56", "v57", "v58", "v59", "v60", "v61", "v62", "v63", "v64", "v65", "v66", "v67", "v68", "v69", "v70", "v71", "v72", "v73", "v74", "v75", "v76", "v77", "v78", "v79", "v80", "v81", "v82", "v83", "v84", "v85", "v86", "v87", "v88", "v89", "v90", "v91", "v92", "v93", "v94", "v95", "v96", "v97", "v98", "v99", "v100", "v101", "v102", "v103", "v104", "v105", "v106", "v107", "v108", "v109", "v110", "v111", "v112", "v113", "v114", "v115", "v116", "v117", "v118", "v119", "v120", "v121", "v122", "v123", "v124", "v125", "v126", "v127", "v128", "v129", "v130", "v131", "v132", "v133", "v134", "v135", "v136", "v137", "v138", "v139", "v140", "v141", "v142", "v143",  "m0", "s49", "vcc", "scc", "memory");

        }
        if (wave == 0 && blockIdx.x == 0) {
            int ln_;
            asm volatile("v_mbcnt_lo_u32_b32 %0, -1, 0\n\tv_mbcnt_hi_u32_b32 %0, -1, %0" : "=v"(ln_));
            KARGS
            unsigned long long tg;
            asm volatile("s_load_dwordx2 %0, %1, %2\n\ts_waitcnt lgkmcnt(0)" : "=s"(tg) : "s"(wb), "n"(EPO_OFF) : "memory");
            const unsigned lo = (unsigned)tg + 1u, hi = (unsigned)(tg >> 32);
            const auto wq = __builtin_amdgcn_make_buffer_rsrc((void*)wb, 0, WS_BYTES, 0x00020000);
            for (int it_ = 0; it_ < 100000; ++it_) {
                bool bad = false;
#pragma unroll
                for (int k = 0; k < 8; ++k) {
                    const u32x4 a_ = __builtin_amdgcn_raw_buffer_load_b128(wq, ARR_OFF + ln_ * 16, k * 1024, SC1);
                    bad |= a_[0] != lo || a_[1] != hi || a_[2] != lo || a_[3] != hi;
                }
                if (__builtin_amdgcn_ballot_w64(bad) == 0ull) break;
                __builtin_amdgcn_s_sleep(2);
            }
            if (ln_ == 0) __builtin_amdgcn_raw_buffer_store_b64((u32x2){lo, hi}, wq, EPO_OFF, 0, SC1);
        }
        asm volatile("wk_code_end:");
        __builtin_amdgcn_endpgm();
    } else if (wave < 6) {
        LANE_SETUP
        {
            const unsigned rsb = ldsb + RS_BASE;
            const unsigned pE_ = fE - X_OFF;
            const unsigned aVAw_ = ldsb + V_OFF + w * 9216 + lane * 16, aVBw_ = ldsb + V_OFF + w * 9216 + 1024 + lane * 8;
            const unsigned pST_ = lane * 16, aRSw_ = (lane < 48 ? rsb : ldsb + RS_OFF + 2304 - 192) + lane * 4, aRSr_ = rsb + (lane & 48);
            B_SCALARS
            unsigned s44 = ldsb + O_OFF, q0 = ldsb + O_OFF + pE_, q1 = rsb + l15 * 4, q2 = ldsb + OL_OFF + l15 * 384 + (((4 * w + (g >> 1)) ^ sw) << 4) + 8 * (g & 1);
            asm volatile(
            "s_waitcnt lgkmcnt(0)\n\t"
            "s_mov_b32 s83, 0x10000\n\t"
            "s_nop 7\n\t"
            "s_setprio 3\n\t"
            "s_barrier\n\t"
            "buffer_load_dwordx4 v[140:143], v245, s[28:31], s82 offen sc1\n\t"
            "s_add_u32 s49, s33, 0\n\t"
            "buffer_load_dwordx4 v[0:3], v245, s[28:31], s49 offen sc1\n\t"
            "s_add_u32 s49, s33, 6144\n\t"
            "buffer_load_dwordx4 v[24:27], v245, s[28:31], s49 offen sc1\n\t"
            "s_add_u32 s49, s33, 12288\n\t"
            "buffer_load_dwordx4 v[48:51], v245, s[28:31], s49 offen sc1\n\t"
            "s_add_u32 s49, s33, 18432\n\t"
            "buffer_load_dwordx4 v[72:75], v245, s[28:31], s49 offen sc1\n\t"
            "s_add_u32 s49, s33, 24576\n\t"
            "buffer_load_dwordx4 v[96:99], v245, s[28:31], s49 offen sc1\n\t"
            "s_add_u32 s49, s33, 30720\n\t"
            "buffer_load_dwordx4 v[120:123], v245, s[28:31], s49 offen sc1\n\t"
            "s_add_u32 s49, s33, 1024\n\t"
            "buffer_load_dwordx4 v[4:7], v245, s[28:31], s49 offen sc1\n\t"
            "s_add_u32 s49, s33, 7168\n\t"
            "buffer_load_dwordx4 v[28:31], v245, s[28:31], s49 offen sc1\n\t"
            "s_add_u32 s49, s33, 13312\n\t"
            "buffer_load_dwordx4 v[52:55], v245, s[28:31], s49 offen sc1\n\t"
            "s_add_u32 s49, s33, 19456\n\t"
            "buffer_load_dwordx4 v[76:79], v245, s[28:31], s49 offen sc1\n\t"
            "s_add_u32 s49, s33, 25600\n\t"
            "buffer_load_dwordx4 v[100:103], v245, s[28:31], s49 offen sc1\n\t"
            "s_add_u32 s49, s33, 31744\n\t"
            "buffer_load_dwordx4 v[124:127], v245, s[28:31], s49 offen sc1\n\t"
            "s_add_u32 s49, s33, 2048\n\t"
            "buffer_load_dwordx4 v[8:11], v245, s[28:31], s49 offen sc1\n\t"
            "s_add_u32 s49, s33, 8192\n\t"
            "buffer_load_dwordx4 v[32:35], v245, s[28:31], s49 offen sc1\n\t"
            "s_add_u32 s49, s33, 14336\n\t"
            "buffer_load_dwordx4 v[56:59], v245, s[28:31], s49 offen sc1\n\t"
            "s_add_u32 s49, s33, 20480\n\t"
            "buffer_load_dwordx4 v[80:83], v245, s[28:31], s49 offen sc1\n\t"
            "s_add_u32 s49, s33, 26624\n\t"
            "buffer_load_dwordx4 v[104:107], v245, s[28:31], s49 offen sc1\n\t"
            "s_add_u32 s49, s33, 32768\n\t"
            "buffer_load_dwordx4 v[128:131], v245, s[28:31], s49 offen sc1\n\t"
            "s_add_u32 s49, s33, 3072\n\t"
            "buffer_load_dwordx4 v[12:15], v245, s[28:31], s49 offen sc1\n\t"
            "s_add_u32 s49, s33, 9216\n\t"
            "buffer_load_dwordx4 v[36:39], v245, s[28:31], s49 offen sc1\n\t"
            "s_add_u32 s49, s33, 15360\n\t"
            "buffer_load_dwordx4 v[60:63], v245, s[28:31], s49 offen sc1\n\t"
            "s_add_u32 s49, s33, 21504\n\t"
            "buffer_load_dwordx4 v[84:87], v245, s[28:31], s49 offen sc1\n\t"
            "s_add_u32 s49, s33, 27648\n\t"
            "buffer_load_dwordx4 v[108:111], v245, s[28:31], s49 offen sc1\n\t"
            "s_add_u32 s49, s33, 33792\n\t"
            "buffer_load_dwordx4 v[132:135], v245, s[28:31], s49 offen sc1\n\t"
            "s_add_u32 s49, s33, 4096\n\t"
            "buffer_load_dwordx4 v[16:19], v245, s[28:31], s49 offen sc1\n\t"
            "s_add_u32 s49, s33, 10240\n\t"
            "buffer_load_dwordx4 v[40:43], v245, s[28:31], s49 offen sc1\n\t"
            "s_add_u32 s49, s33, 16384\n\t"
            "buffer_load_dwordx4 v[64:67], v245, s[28:31], s49 offen sc1\n\t"
            "s_add_u32 s49, s33, 22528\n\t"
            "buffer_load_dwordx4 v[88:91], v245, s[28:31], s49 offen sc1\n\t"
            "s_add_u32 s49, s33, 28672\n\t"
            "buffer_load_dwordx4 v[112:115], v245, s[28:31], s49 offen sc1\n\t"
            "s_add_u32 s49, s33, 34816\n\t"
            "buffer_load_dwordx4 v[136:139], v245, s[28:31], s49 offen sc1\n\t"
            "s_add_u32 s49, s33, 5120\n\t"
            "buffer_load_dwordx4 v[20:23], v245, s[28:31], s49 offen sc1\n\t"
            "s_add_u32 s49, s33, 11264\n\t"
            "buffer_load_dwordx4 v[44:47], v245, s[28:31], s49 offen sc1\n\t"
            "s_add_u32 s49, s33, 17408\n\t"
            "buffer_load_dwordx4 v[68:71], v245, s[28:31], s49 offen sc1\n\t"
            "s_add_u32 s49, s33, 23552\n\t"
            "buffer_load_dwordx4 v[92:95], v245, s[28:31], s49 offen sc1\n\t"
            "s_add_u32 s49, s33, 29696\n\t"
            "buffer_load_dwordx4 v[116:119], v245, s[28:31], s49 offen sc1\n\t"
            "s_add_u32 s49, s33, 35840\n\t"
            "buffer_load_dwordx4 v[140:143], v245, s[28:31], s49 offen sc1\n\t"
            "ds_read_b32 v249, v252 offset:12\n\t"
            "s_waitcnt lgkmcnt(0)\n\t"
            "v_and_b32 v249, s69, v249\n\t"
            "s_mov_b32 exec_hi, 0x3fffff\n\t"
            "s_add_u32 m0, s66, 0\n\t"
            "s_add_u32 s49, s79, 0\n\t"
            "buffer_load_dwordx4 v249, s[72:75], s49 offen lds\n\t"
            "s_mov_b32 exec_hi, -1\n\t"
            "s_mov_b32 exec_hi, 0x3fffff\n\t"
            "s_add_u32 m0, s66, 872\n\t"
            "s_add_u32 s49, s47, 196224\n\t"
            "buffer_load_dwordx4 v249, s[72:75], s49 offen lds\n\t"
            "s_mov_b32 exec_hi, -1\n\t"
            "s_mov_b32 exec_hi, 0x3fffff\n\t"
            "s_add_u32 m0, s66, 1744\n\t"
            "s_add_u32 s49, s47, 392448\n\t"
            "buffer_load_dwordx4 v249, s[72:75], s49 offen lds\n\t"
            "s_mov_b32 exec_hi, -1\n\t"
            "s_mov_b32 exec_hi, 0x3fffff\n\t"
            "s_add_u32 m0, s66, 2616\n\t"
            "s_add_u32 s49, s47, 588672\n\t"
            "buffer_load_dwordx4 v249, s[72:75], s49 offen lds\n\t"
            "s_mov_b32 exec_hi, -1\n\t"
            "s_mov_b32 exec_hi, 0x3fffff\n\t"
            "s_add_u32 m0, s66, 3488\n\t"
            "s_add_u32 s49, s47, 784896\n\t"
            "buffer_load_dwordx4 v249, s[72:75], s49 offen lds\n\t"
            "s_mov_b32 exec_hi, -1\n\t"
            "s_mov_b32 exec_hi, 0x3fffff\n\t"
            "s_add_u32 m0, s66, 4360\n\t"
            "s_add_u32 s49, s47, 981120\n\t"
            "buffer_load_dwordx4 v249, s[72:75], s49 offen lds\n\t"
            "s_mov_b32 exec_hi, -1\n\t"
            "s_mov_b32 exec_hi, 0x3fffff\n\t"
            "s_add_u32 m0, s66, 5232\n\t"
            "s_add_u32 s49, s47, 1177344\n\t"
            "buffer_load_dwordx4 v249, s[72:75], s49 offen lds\n\t"
            "s_mov_b32 exec_hi, -1\n\t"
            "s_mov_b32 exec_hi, 0x3fffff\n\t"
            "s_add_u32 m0, s66, 6104\n\t"
            "s_add_u32 s49, s47, 1373568\n\t"
            "buffer_load_dwordx4 v249, s[72:75], s49 offen lds\n\t"
            "s_mov_b32 exec_hi, -1\n\t"
            "s_mov_b32 exec_hi, 0x3fffff\n\t"
            "s_add_u32 m0, s66, 6976\n\t"
            "s_add_u32 s49, s47, 1569792\n\t"
            "buffer_load_dwordx4 v249, s[72:75], s49 offen lds\n\t"
            "s_mov_b32 exec_hi, -1\n\t"
            "s_setprio 3\n\t"
            "ds_read_b128 v[200:203], v232\n\t"
            "ds_read_b128 v[208:211], v232 offset:128\n\t"
            "ds_read_b128 v[216:219], v232 offset:256\n\t"
            "ds_read_b128 v[192:195], v232 offset:6272\n\t"
            "v_add_u32 v228, s44, v245\n\t"
            "v_xor_b32 v235, 64, v232\n\t"
            "s_setprio 3\n\t"
            "s_waitcnt vmcnt(9)\n\t"
            "s_nop 0\n\t"
            "ds_read_b128 v[224:227], v228 offset:12288\n\t"
            "ds_read_b128 v[228:231], v228 offset:13312\n\t"
            "ds_read_b128 v[204:207], v235\n\t"
            "ds_read_b128 v[212:215], v235 offset:128\n\t"
            "ds_read_b128 v[220:223], v235 offset:256\n\t"
            "ds_read_b128 v[196:199], v235 offset:6272\n\t"
            "v_xor_b32 v248, 32, v234\n\t"
            "v_mov_b32 v249, s76\n\t"
            "ds_read_b32 v249, v249\n\t"
            "s_waitcnt lgkmcnt(6)\n\t"
            "v_add_f32 v224, v224, v226\n\t"
            "s_waitcnt lgkmcnt(5)\n\t"
            "v_add_f32 v228, v228, v230\n\t"
            "s_waitcnt lgkmcnt(4)\n\t"
            "v_mfma_f32_16x16x32_bf16 v[144:147], v[0:3], v[200:203], 0\n\t"
            "v_add_f32 v224, v224, v228\n\t"
            "v_add_f32 v225, v225, v227\n\t"
            "v_mfma_f32_16x16x32_bf16 v[144:147], v[4:7], v[204:207], v[144:147]\n\t"
            "v_add_f32 v229, v229, v231\n\t"
            "v_mul_f32 v224, 0x3baaaaab, v224\n\t"
            "v_mfma_f32_16x16x32_bf16 v[148:151], v[24:27], v[200:203], 0\n\t"
            "v_add_f32 v225, v225, v229\n\t"
            "v_mul_f32_e64 v224, v224, -v224\n\t"
            "v_mfma_f32_16x16x32_bf16 v[148:151], v[28:31], v[204:207], v[148:151]\n\t"
            "ds_read_b128 v[228:231], v235 offset:6144\n\t"
            "v_fmac_f32 v224, 0x3baaaaab, v225\n\t"
            "v_mfma_f32_16x16x32_bf16 v[168:171], v[48:51], v[200:203], 0\n\t"
            "v_max_f32 v224, 0, v224\n\t"
            "v_add_f32 v224, 0x3727c5ac, v224\n\t"
            "v_mfma_f32_16x16x32_bf16 v[168:171], v[52:55], v[204:207], v[168:171]\n\t"
            "v_rsq_f32 v224, v224\n\t"
            "v_mfma_f32_16x16x32_bf16 v[172:175], v[72:75], v[200:203], 0\n\t"
            "s_waitcnt lgkmcnt(1)\n\t"
            "v_readfirstlane_b32 s26, v249\n\t"
            "v_mfma_f32_16x16x32_bf16 v[172:175], v[76:79], v[204:207], v[172:175]\n\t"
            "s_and_b32 s68, s26, 0x7ffffff\n\t"
            "s_add_u32 s68, s68, s78\n\t"
            "s_and_b32 s69, s26, 0x38000000\n\t"
            "s_or_b32 s69, s69, s39\n\t"
            "s_bitcmp1_b32 s26, 27\n\t"
            "s_cselect_b32 s79, 2.0, s68\n\t"
            "v_mfma_f32_16x16x32_bf16 v[152:155], v[96:99], v[200:203], 0\n\t"
            "s_and_b32 s57, s56, 3\n\t"
            "s_lshl_b32 s58, s57, 3\n\t"
            "s_lshr_b32 m0, s54, s58\n\t"
            "v_mfma_f32_16x16x32_bf16 v[152:155], v[100:103], v[204:207], v[152:155]\n\t"
            "s_lshl_b32 s58, s57, 4\n\t"
            "s_lshr_b64 s[36:37], s[50:51], s58\n\t"
            "s_and_b32 s57, s36, 0xffff\n\t"
            "s_add_u32 s57, s57, s67\n\t"
            "v_mfma_f32_16x16x32_bf16 v[156:159], v[120:123], v[200:203], 0\n\t"
            "s_lshr_b64 s[36:37], s[52:53], s58\n\t"
            "s_and_b32 s58, s36, 0xffff\n\t"
            "s_add_u32 s58, s58, s67\n\t"
            "v_mfma_f32_16x16x32_bf16 v[156:159], v[124:127], v[204:207], v[156:159]\n\t"
            "s_and_b32 s38, s52, 0xffff\n\t"
            "s_add_u32 s38, s38, s67\n\t"
            "ds_read_b32 v249, v252 offset:12\n\t"
            "v_mfma_f32_16x16x32_bf16 v[144:147], v[8:11], v[208:211], v[144:147]\n\t"
            "v_add_u32 v204, s48, v245\n\t"
            "v_mfma_f32_16x16x32_bf16 v[144:147], v[12:15], v[212:215], v[144:147]\n\t"
            "ds_read_b128 v[200:203], v204 offset:18816\n\t"
            "ds_read_b128 v[204:207], v204 offset:19840\n\t"
            "v_mfma_f32_16x16x32_bf16 v[148:151], v[32:35], v[208:211], v[148:151]\n\t"
            "v_mfma_f32_16x16x32_bf16 v[148:151], v[36:39], v[212:215], v[148:151]\n\t"
            "ds_write_b32 v246, v224\n\t"
            "v_mfma_f32_16x16x32_bf16 v[168:171], v[56:59], v[208:211], v[168:171]\n\t"
            "ds_read_b128 v[224:227], v232 offset:6144\n\t"
            "ds_read_b32 v236, v233\n\t"
            "v_mfma_f32_16x16x32_bf16 v[168:171], v[60:63], v[212:215], v[168:171]\n\t"
            "ds_read_b32 v237, v233 offset:64\n\t"
            "v_mfma_f32_16x16x32_bf16 v[172:175], v[80:83], v[208:211], v[172:175]\n\t"
            "v_mfma_f32_16x16x32_bf16 v[172:175], v[84:87], v[212:215], v[172:175]\n\t"
            "s_waitcnt lgkmcnt(6)\n\t"
            "v_and_b32 v249, s69, v249\n\t"
            "v_mfma_f32_16x16x32_bf16 v[152:155], v[104:107], v[208:211], v[152:155]\n\t"
            "v_mfma_f32_16x16x32_bf16 v[152:155], v[108:111], v[212:215], v[152:155]\n\t"
            "s_waitcnt lgkmcnt(5)\n\t"
            "v_add_f32 v200, v200, v202\n\t"
            "s_waitcnt lgkmcnt(4)\n\t"
            "v_add_f32 v204, v204, v206\n\t"
            "v_mfma_f32_16x16x32_bf16 v[156:159], v[128:131], v[208:211], v[156:159]\n\t"
            "v_add_f32 v200, v200, v204\n\t"
            "v_add_f32 v201, v201, v203\n\t"
            "v_mfma_f32_16x16x32_bf16 v[156:159], v[132:135], v[212:215], v[156:159]\n\t"
            "v_add_f32 v205, v205, v207\n\t"
            "v_mul_f32 v200, 0x3baaaaab, v200\n\t"
            "v_mfma_f32_16x16x32_bf16 v[144:147], v[16:19], v[216:219], v[144:147]\n\t"
            "v_add_f32 v201, v201, v205\n\t"
            "v_mul_f32_e64 v200, v200, -v200\n\t"
            "v_mfma_f32_16x16x32_bf16 v[144:147], v[20:23], v[220:223], v[144:147]\n\t"
            "v_fmac_f32 v200, 0x3baaaaab, v201\n\t"
            "v_max_f32 v200, 0, v200\n\t"
            "s_waitcnt lgkmcnt(2)\n\t"
            "v_mfma_f32_16x16x32_bf16 v[160:163], v[0:3], v[224:227], 0\n\t"
            "v_add_f32 v200, 0x3727c5ac, v200\n\t"
            "v_rsq_f32 v200, v200\n\t"
            "v_mfma_f32_16x16x32_bf16 v[160:163], v[4:7], v[228:231], v[160:163]\n\t"
            "ds_write_b32 v246, v200\n\t"
            "v_mfma_f32_16x16x32_bf16 v[164:167], v[24:27], v[224:227], 0\n\t"
            "s_waitcnt lgkmcnt(2)\n\t"
            "v_mul_f32 v144, v236, v144\n\t"
            "v_mul_f32 v145, v236, v145\n\t"
            "v_mfma_f32_16x16x32_bf16 v[164:167], v[28:31], v[228:231], v[164:167]\n\t"
            "v_mul_f32 v146, v236, v146\n\t"
            "v_mul_f32 v147, v236, v147\n\t"
            "v_mfma_f32_16x16x32_bf16 v[176:179], v[48:51], v[224:227], 0\n\t"
            "v_cvt_pk_bf16_f32 v144, v144, v145\n\t"
            "v_cvt_pk_bf16_f32 v145, v146, v147\n\t"
            "v_mfma_f32_16x16x32_bf16 v[176:179], v[52:55], v[228:231], v[176:179]\n\t"
            "v_mfma_f32_16x16x32_bf16 v[180:183], v[72:75], v[224:227], 0\n\t"
            "v_mfma_f32_16x16x32_bf16 v[180:183], v[76:79], v[228:231], v[180:183]\n\t"
            "v_mfma_f32_16x16x32_bf16 v[184:187], v[96:99], v[224:227], 0\n\t"
            "v_mfma_f32_16x16x32_bf16 v[184:187], v[100:103], v[228:231], v[184:187]\n\t"
            "v_mfma_f32_16x16x32_bf16 v[188:191], v[120:123], v[224:227], 0\n\t"
            "ds_read_b128 v[224:227], v232 offset:6400\n\t"
            "v_add_u32 v232, s48, v242\n\t"
            "v_mfma_f32_16x16x32_bf16 v[188:191], v[124:127], v[228:231], v[188:191]\n\t"
            "ds_read_b128 v[228:231], v235 offset:6400\n\t"
            "v_xor_b32 v233, 64, v232\n\t"
            "v_mfma_f32_16x16x32_bf16 v[148:151], v[40:43], v[216:219], v[148:151]\n\t"
            "v_mfma_f32_16x16x32_bf16 v[148:151], v[44:47], v[220:223], v[148:151]\n\t"
            "v_mfma_f32_16x16x32_bf16 v[168:171], v[64:67], v[216:219], v[168:171]\n\t"
            "v_mfma_f32_16x16x32_bf16 v[168:171], v[68:71], v[220:223], v[168:171]\n\t"
            "v_mfma_f32_16x16x32_bf16 v[172:175], v[88:91], v[216:219], v[172:175]\n\t"
            "v_mfma_f32_16x16x32_bf16 v[172:175], v[92:95], v[220:223], v[172:175]\n\t"
            "v_mfma_f32_16x16x32_bf16 v[152:155], v[112:115], v[216:219], v[152:155]\n\t"
            "v_mfma_f32_16x16x32_bf16 v[152:155], v[116:119], v[220:223], v[152:155]\n\t"
            "s_nop 1\n\t"
            "v_mul_f32 v148, v236, v148\n\t"
            "v_mfma_f32_16x16x32_bf16 v[156:159], v[136:139], v[216:219], v[156:159]\n\t"
            "v_mul_f32 v149, v236, v149\n\t"
            "v_mul_f32 v150, v236, v150\n\t"
            "v_mfma_f32_16x16x32_bf16 v[156:159], v[140:143], v[220:223], v[156:159]\n\t"
            "v_mul_f32 v168, v236, v168\n\t"
            "v_mul_f32 v169, v236, v169\n\t"
            "v_mfma_f32_16x16x32_bf16 v[160:163], v[8:11], v[192:195], v[160:163]\n\t"
            "v_mul_f32 v172, v236, v172\n\t"
            "v_mul_f32 v173, v236, v173\n\t"
            "v_mfma_f32_16x16x32_bf16 v[160:163], v[12:15], v[196:199], v[160:163]\n\t"
            "v_mul_f32 v152, v236, v152\n\t"
            "v_mul_f32 v153, v236, v153\n\t"
            "v_mfma_f32_16x16x32_bf16 v[164:167], v[32:35], v[192:195], v[164:167]\n\t"
            "v_mul_f32 v156, v236, v156\n\t"
            "v_mul_f32 v157, v236, v157\n\t"
            "v_mfma_f32_16x16x32_bf16 v[164:167], v[36:39], v[196:199], v[164:167]\n\t"
            "s_waitcnt vmcnt(0)\n\t"
            "ds_read_b64 v[146:147], v253 offset:96\n\t"
            "v_mul_f32 v151, v236, v151\n\t"
            "v_mfma_f32_16x16x32_bf16 v[176:179], v[56:59], v[192:195], v[176:179]\n\t"
            "v_cvt_pk_bf16_f32 v148, v148, v149\n\t"
            "v_mul_f32 v170, v236, v170\n\t"
            "v_mfma_f32_16x16x32_bf16 v[176:179], v[60:63], v[196:199], v[176:179]\n\t"
            "v_mul_f32 v171, v236, v171\n\t"
            "v_cvt_pk_bf16_f32 v168, v168, v169\n\t"
            "v_mfma_f32_16x16x32_bf16 v[180:183], v[80:83], v[192:195], v[180:183]\n\t"
            "v_mul_f32 v174, v236, v174\n\t"
            "v_mul_f32 v175, v236, v175\n\t"
            "v_mfma_f32_16x16x32_bf16 v[180:183], v[84:87], v[196:199], v[180:183]\n\t"
            "v_cvt_pk_bf16_f32 v172, v172, v173\n\t"
            "v_mul_f32 v154, v236, v154\n\t"
            "v_mfma_f32_16x16x32_bf16 v[184:187], v[104:107], v[192:195], v[184:187]\n\t"
            "v_mul_f32 v155, v236, v155\n\t"
            "v_cvt_pk_bf16_f32 v152, v152, v153\n\t"
            "v_mfma_f32_16x16x32_bf16 v[184:187], v[108:111], v[196:199], v[184:187]\n\t"
            "v_mul_f32 v158, v236, v158\n\t"
            "v_mul_f32 v159, v236, v159\n\t"
            "v_mfma_f32_16x16x32_bf16 v[188:191], v[128:131], v[192:195], v[188:191]\n\t"
            "v_cvt_pk_bf16_f32 v156, v156, v157\n\t"
            "v_cvt_pk_bf16_f32 v149, v150, v151\n\t"
            "v_mfma_f32_16x16x32_bf16 v[188:191], v[132:135], v[196:199], v[188:191]\n\t"
            "ds_read_b64 v[150:151], v253 offset:1840\n\t"
            "v_cvt_pk_bf16_f32 v169, v170, v171\n\t"
            "s_waitcnt lgkmcnt(2)\n\t"
            "v_mfma_f32_16x16x32_bf16 v[160:163], v[16:19], v[224:227], v[160:163]\n\t"
            "v_cvt_pk_bf16_f32 v173, v174, v175\n\t"
            "v_cvt_pk_bf16_f32 v153, v154, v155\n\t"
            "v_mfma_f32_16x16x32_bf16 v[160:163], v[20:23], v[228:231], v[160:163]\n\t"
            "ds_read_b64 v[154:155], v253 offset:104\n\t"
            "v_cvt_pk_bf16_f32 v157, v158, v159\n\t"
            "v_mfma_f32_16x16x32_bf16 v[164:167], v[40:43], v[224:227], v[164:167]\n\t"
            "ds_read_b64 v[158:159], v253 offset:1848\n\t"
            "v_add_u32 v236, s67, v250\n\t"
            "v_mfma_f32_16x16x32_bf16 v[164:167], v[44:47], v[228:231], v[164:167]\n\t"
            "s_mov_b64 exec, -1\n\t"
            "ds_write_b64 v234, v[144:145]\n\t"
            "ds_write_b64 v234, v[148:149] offset:128\n\t"
            "ds_write_b64 v234, v[168:169] offset:256\n\t"
            "ds_write_b64 v248, v[172:173]\n\t"
            "ds_write_b64 v248, v[152:153] offset:128\n\t"
            "ds_write_b64 v248, v[156:157] offset:256\n\t"
            "s_mov_b64 exec, -1\n\t"
            "v_mfma_f32_16x16x32_bf16 v[176:179], v[64:67], v[224:227], v[176:179]\n\t"
            "ds_read_b128 v[168:171], v254\n\t"
            "ds_read_b128 v[172:175], v254 offset:64\n\t"
            "v_mfma_f32_16x16x32_bf16 v[176:179], v[68:71], v[228:231], v[176:179]\n\t"
            "ds_read_b64 v[144:145], v253\n\t"
            "ds_read_b64 v[148:149], v253 offset:1744\n\t"
            "v_mfma_f32_16x16x32_bf16 v[180:183], v[88:91], v[224:227], v[180:183]\n\t"
            "ds_read_b64 v[152:153], v253 offset:8\n\t"
            "s_waitcnt lgkmcnt(14)\n\t"
            "ds_read_b64 v[156:157], v253 offset:1752\n\t"
            "v_mfma_f32_16x16x32_bf16 v[180:183], v[92:95], v[228:231], v[180:183]\n\t"
            "v_mul_f32 v160, v237, v160\n\t"
            "v_mul_f32 v161, v237, v161\n\t"
            "v_mfma_f32_16x16x32_bf16 v[184:187], v[112:115], v[224:227], v[184:187]\n\t"
            "v_mul_f32 v164, v237, v164\n\t"
            "v_mul_f32 v165, v237, v165\n\t"
            "v_mfma_f32_16x16x32_bf16 v[184:187], v[116:119], v[228:231], v[184:187]\n\t"
            "v_mul_f32 v176, v237, v176\n\t"
            "v_mul_f32 v177, v237, v177\n\t"
            "v_mfma_f32_16x16x32_bf16 v[188:191], v[136:139], v[224:227], v[188:191]\n\t"
            "s_waitcnt lgkmcnt(14)\n\t"
            "ds_read_b128 v[224:227], v232\n\t"
            "v_mul_f32 v180, v237, v180\n\t"
            "v_mfma_f32_16x16x32_bf16 v[188:191], v[140:143], v[228:231], v[188:191]\n\t"
            "s_waitcnt lgkmcnt(14)\n\t"
            "ds_read_b128 v[228:231], v233\n\t"
            "v_mul_f32 v181, v237, v181\n\t"
            "v_mul_f32 v184, v237, v184\n\t"
            "v_mul_f32 v185, v237, v185\n\t"
            "v_mul_f32 v162, v237, v162\n\t"
            "v_mul_f32 v163, v237, v163\n\t"
            "v_cvt_pk_bf16_f32 v160, v160, v161\n\t"
            "v_mul_f32 v166, v237, v166\n\t"
            "v_mul_f32 v188, v237, v188\n\t"
            "v_mul_f32 v189, v237, v189\n\t"
            "v_mul_f32 v167, v237, v167\n\t"
            "v_cvt_pk_bf16_f32 v164, v164, v165\n\t"
            "v_mul_f32 v178, v237, v178\n\t"
            "v_mul_f32 v179, v237, v179\n\t"
            "v_cvt_pk_bf16_f32 v176, v176, v177\n\t"
            "v_mul_f32 v182, v237, v182\n\t"
            "v_mul_f32 v183, v237, v183\n\t"
            "v_cvt_pk_bf16_f32 v180, v180, v181\n\t"
            "v_mul_f32 v186, v237, v186\n\t"
            "v_mul_f32 v187, v237, v187\n\t"
            "v_cvt_pk_bf16_f32 v184, v184, v185\n\t"
            "v_mul_f32 v190, v237, v190\n\t"
            "v_mul_f32 v191, v237, v191\n\t"
            "v_cvt_pk_bf16_f32 v188, v188, v189\n\t"
            "v_cvt_pk_bf16_f32 v161, v162, v163\n\t"
            "v_cvt_pk_bf16_f32 v165, v166, v167\n\t"
            "v_cvt_pk_bf16_f32 v177, v178, v179\n\t"
            "v_cvt_pk_bf16_f32 v181, v182, v183\n\t"
            "v_cvt_pk_bf16_f32 v185, v186, v187\n\t"
            "v_cvt_pk_bf16_f32 v189, v190, v191\n\t"
            "v_mov_b64 v[178:179], 0\n\t"
            "s_waitcnt lgkmcnt(0)\n\t"
            "v_mfma_f32_16x16x32_bf16 v[200:203], v[224:227], v[0:3], 0\n\t"
            "ds_read_b64 v[162:163], v253 offset:112\n\t"
            "ds_read_b64 v[166:167], v253 offset:1856\n\t"
            "v_mfma_f32_16x16x32_bf16 v[200:203], v[228:231], v[4:7], v[200:203]\n\t"
            "s_mov_b64 exec, s[80:81]\n\t"
            "ds_write_b64 v234, v[160:161] offset:6144\n\t"
            "ds_write_b64 v234, v[164:165] offset:6272\n\t"
            "ds_write_b64 v234, v[176:177] offset:6400\n\t"
            "ds_write_b64 v248, v[180:181] offset:6144\n\t"
            "ds_write_b64 v248, v[184:185] offset:6272\n\t"
            "ds_write_b64 v248, v[188:189] offset:6400\n\t"
            "s_mov_b64 exec, -1\n\t"
            "v_mfma_f32_16x16x32_bf16 v[204:207], v[228:231], v[28:31], 0\n\t"
            "ds_read_b64 v[160:161], v253 offset:16\n\t"
            "ds_read_b64 v[164:165], v253 offset:1760\n\t"
            "v_mfma_f32_16x16x32_bf16 v[204:207], v[224:227], v[24:27], v[204:207]\n\t"
            "v_mov_b64 v[176:177], 0\n\t"
            "v_mov_b64 v[180:181], 0\n\t"
            "v_mfma_f32_16x16x32_bf16 v[208:211], v[224:227], v[48:51], 0\n\t"
            "v_mov_b64 v[182:183], 0\n\t"
            "v_mov_b64 v[184:185], 0\n\t"
            "v_mfma_f32_16x16x32_bf16 v[208:211], v[228:231], v[52:55], v[208:211]\n\t"
            "v_mov_b64 v[186:187], 0\n\t"
            "v_mov_b64 v[188:189], 0\n\t"
            "v_smfmac_f32_16x16x64_bf16 v[176:179], v[168:171], v[144:151], v255\n\t"
            "ds_read_b128 v[168:171], v254 offset:128\n\t"
            "v_smfmac_f32_16x16x64_bf16 v[180:183], v[172:175], v[152:159], v255\n\t"
            "ds_read_b64 v[144:145], v253 offset:24\n\t"
            "v_mfma_f32_16x16x32_bf16 v[212:215], v[228:231], v[76:79], 0\n\t"
            "ds_read_b64 v[146:147], v253 offset:120\n\t"
            "ds_read_b64 v[148:149], v253 offset:1768\n\t"
            "v_mfma_f32_16x16x32_bf16 v[212:215], v[224:227], v[72:75], v[212:215]\n\t"
            "ds_read_b64 v[150:151], v253 offset:1864\n\t"
            "s_waitcnt lgkmcnt(14)\n\t"
            "ds_read_b128 v[172:175], v254 offset:192\n\t"
            "v_mfma_f32_16x16x32_bf16 v[216:219], v[224:227], v[96:99], 0\n\t"
            "s_waitcnt lgkmcnt(14)\n\t"
            "ds_read_b64 v[152:153], v253 offset:32\n\t"
            "s_waitcnt lgkmcnt(14)\n\t"
            "ds_read_b64 v[154:155], v253 offset:128\n\t"
            "v_mfma_f32_16x16x32_bf16 v[216:219], v[228:231], v[100:103], v[216:219]\n\t"
            "s_waitcnt lgkmcnt(14)\n\t"
            "ds_read_b64 v[156:157], v253 offset:1776\n\t"
            "s_waitcnt lgkmcnt(14)\n\t"
            "ds_read_b64 v[158:159], v253 offset:1872\n\t"
            "v_mfma_f32_16x16x32_bf16 v[220:223], v[228:231], v[124:127], 0\n\t"
            "s_waitcnt lgkmcnt(14)\n\t"
            "ds_read_b128 v[228:231], v233 offset:256\n\t"
            "v_mov_b64 v[190:191], 0\n\t"
            "v_mfma_f32_16x16x32_bf16 v[220:223], v[224:227], v[120:123], v[220:223]\n\t"
            "s_waitcnt lgkmcnt(14)\n\t"
            "ds_read_b128 v[224:227], v232 offset:256\n\t"
            "v_cvt_pk_bf16_f32 v176, v176, v177\n\t"
            "v_cvt_pk_bf16_f32 v177, v178, v179\n\t"
            "v_writelane_b32 v236, s57, 63\n\t"
            "s_waitcnt lgkmcnt(11)\n\t"
            "v_smfmac_f32_16x16x64_bf16 v[184:187], v[168:171], v[160:167], v255\n\t"
            "ds_read_b128 v[168:171], v254 offset:256\n\t"
            "v_mfma_f32_4x4x4_16b_bf16 v[196:199], v[176:177], v[176:177], 0\n\t"
            "ds_read_b64 v[160:161], v253 offset:40\n\t"
            "ds_read_b64 v[162:163], v253 offset:136\n\t"
            "ds_read_b64 v[164:165], v253 offset:1784\n\t"
            "s_waitcnt lgkmcnt(14)\n\t"
            "ds_read_b64 v[166:167], v253 offset:1880\n\t"
            "v_writelane_b32 v236, s59, m0\n\t"
            "v_cvt_pk_bf16_f32 v178, v180, v181\n\t"
            "v_cvt_pk_bf16_f32 v179, v182, v183\n\t"
            "s_waitcnt lgkmcnt(11)\n\t"
            "v_smfmac_f32_16x16x64_bf16 v[188:191], v[172:175], v[144:151], v255\n\t"
            "ds_read_b128 v[172:175], v254 offset:320\n\t"
            "v_mfma_f32_16x16x32_bf16 v[192:195], v[238:241], v[176:179], 0\n\t"
            "ds_read_b64 v[144:145], v253 offset:48\n\t"
            "ds_read_b64 v[146:147], v253 offset:144\n\t"
            "v_mfma_f32_4x4x4_16b_bf16 v[196:199], v[178:179], v[178:179], v[196:199]\n\t"
            "ds_read_b64 v[148:149], v253 offset:1792\n\t"
            "s_waitcnt lgkmcnt(14)\n\t"
            "ds_read_b64 v[150:151], v253 offset:1888\n\t"
            "s_waitcnt lgkmcnt(14)\n\t"
            "ds_write_b128 v236, v[176:179]\n\t"
            "v_mov_b64 v[176:177], 0\n\t"
            "v_mov_b64 v[178:179], 0\n\t"
            "v_mov_b64 v[180:181], 0\n\t"
            "v_mov_b64 v[182:183], 0\n\t"
            "v_cvt_pk_bf16_f32 v184, v184, v185\n\t"
            "v_cvt_pk_bf16_f32 v185, v186, v187\n\t"
            "s_waitcnt lgkmcnt(10)\n\t"
            "v_smfmac_f32_16x16x64_bf16 v[176:179], v[168:171], v[152:159], v255\n\t"
            "ds_read_b128 v[168:171], v254 offset:384\n\t"
            "v_mfma_f32_4x4x4_16b_bf16 v[196:199], v[184:185], v[184:185], v[196:199]\n\t"
            "ds_read_b64 v[152:153], v253 offset:56\n\t"
            "ds_read_b64 v[154:155], v253 offset:152\n\t"
            "ds_read_b64 v[156:157], v253 offset:1800\n\t"
            "ds_read_b64 v[158:159], v253 offset:1896\n\t"
            "v_xor_b32 v237, 16, v236\n\t"
            "v_cvt_pk_bf16_f32 v186, v188, v189\n\t"
            "v_cvt_pk_bf16_f32 v187, v190, v191\n\t"
            "s_waitcnt lgkmcnt(10)\n\t"
            "v_smfmac_f32_16x16x64_bf16 v[180:183], v[172:175], v[160:167], v255\n\t"
            "ds_read_b128 v[172:175], v254 offset:448\n\t"
            "v_mfma_f32_16x16x32_bf16 v[192:195], v[238:241], v[184:187], v[192:195]\n\t"
            "ds_read_b64 v[160:161], v253 offset:64\n\t"
            "ds_read_b64 v[162:163], v253 offset:160\n\t"
            "v_mfma_f32_4x4x4_16b_bf16 v[196:199], v[186:187], v[186:187], v[196:199]\n\t"
            "ds_read_b64 v[164:165], v253 offset:1808\n\t"
            "ds_read_b64 v[166:167], v253 offset:1904\n\t"
            "s_waitcnt lgkmcnt(14)\n\t"
            "ds_write_b128 v237, v[184:187]\n\t"
            "v_mov_b64 v[184:185], 0\n\t"
            "v_mov_b64 v[186:187], 0\n\t"
            "v_mov_b64 v[188:189], 0\n\t"
            "v_mov_b64 v[190:191], 0\n\t"
            "v_cvt_pk_bf16_f32 v176, v176, v177\n\t"
            "v_cvt_pk_bf16_f32 v177, v178, v179\n\t"
            "s_waitcnt lgkmcnt(10)\n\t"
            "v_smfmac_f32_16x16x64_bf16 v[184:187], v[168:171], v[144:151], v255\n\t"
            "ds_read_b128 v[168:171], v254 offset:512\n\t"
            "v_mfma_f32_4x4x4_16b_bf16 v[196:199], v[176:177], v[176:177], v[196:199]\n\t"
            "ds_read_b64 v[144:145], v253 offset:72\n\t"
            "ds_read_b64 v[146:147], v253 offset:168\n\t"
            "ds_read_b64 v[148:149], v253 offset:1816\n\t"
            "ds_read_b64 v[150:151], v253 offset:1912\n\t"
            "v_cvt_pk_bf16_f32 v178, v180, v181\n\t"
            "v_cvt_pk_bf16_f32 v179, v182, v183\n\t"
            "v_mov_b64 v[180:181], 0\n\t"
            "s_waitcnt lgkmcnt(10)\n\t"
            "v_smfmac_f32_16x16x64_bf16 v[188:191], v[172:175], v[152:159], v255\n\t"
            "ds_read_b128 v[172:175], v254 offset:576\n\t"
            "v_mfma_f32_16x16x32_bf16 v[192:195], v[238:241], v[176:179], v[192:195]\n\t"
            "ds_read_b64 v[152:153], v253 offset:80\n\t"
            "ds_read_b64 v[154:155], v253 offset:176\n\t"
            "v_mfma_f32_4x4x4_16b_bf16 v[196:199], v[178:179], v[178:179], v[196:199]\n\t"
            "ds_read_b64 v[156:157], v253 offset:1824\n\t"
            "ds_read_b64 v[158:159], v253 offset:1920\n\t"
            "s_waitcnt lgkmcnt(14)\n\t"
            "ds_write_b128 v236, v[176:179] offset:128\n\t"
            "v_mov_b64 v[176:177], 0\n\t"
            "v_mov_b64 v[178:179], 0\n\t"
            "v_mov_b64 v[182:183], 0\n\t"
            "v_cvt_pk_bf16_f32 v184, v184, v185\n\t"
            "v_cvt_pk_bf16_f32 v185, v186, v187\n\t"
            "v_cvt_pk_bf16_f32 v186, v188, v189\n\t"
            "s_waitcnt lgkmcnt(10)\n\t"
            "v_smfmac_f32_16x16x64_bf16 v[176:179], v[168:171], v[160:167], v255\n\t"
            "ds_read_b128 v[168:171], v254 offset:640\n\t"
            "v_mfma_f32_4x4x4_16b_bf16 v[196:199], v[184:185], v[184:185], v[196:199]\n\t"
            "ds_read_b64 v[160:161], v253 offset:88\n\t"
            "ds_read_b64 v[162:163], v253 offset:184\n\t"
            "ds_read_b64 v[164:165], v253 offset:1832\n\t"
            "ds_read_b64 v[166:167], v253 offset:1928\n\t"
            "v_cvt_pk_bf16_f32 v187, v190, v191\n\t"
            "v_mov_b64 v[188:189], 0\n\t"
            "v_mov_b64 v[190:191], 0\n\t"
            "s_waitcnt lgkmcnt(10)\n\t"
            "v_smfmac_f32_16x16x64_bf16 v[180:183], v[172:175], v[144:151], v255\n\t"
            "ds_read_b128 v[172:175], v254 offset:704\n\t"
            "v_mfma_f32_16x16x32_bf16 v[192:195], v[238:241], v[184:187], v[192:195]\n\t"
            "ds_write_b128 v237, v[184:187] offset:128\n\t"
            "v_mfma_f32_4x4x4_16b_bf16 v[196:199], v[186:187], v[186:187], v[196:199]\n\t"
            "v_mov_b64 v[184:185], 0\n\t"
            "v_mov_b64 v[186:187], 0\n\t"
            "v_add_u32 v248, s67, v251\n\t"
            "v_writelane_b32 v248, s58, 63\n\t"
            "v_writelane_b32 v248, s38, m0\n\t"
            "v_cvt_pk_bf16_f32 v176, v176, v177\n\t"
            "v_cvt_pk_bf16_f32 v177, v178, v179\n\t"
            "v_cvt_pk_bf16_f32 v178, v180, v181\n\t"
            "v_cvt_pk_bf16_f32 v179, v182, v183\n\t"
            "v_mfma_f32_4x4x4_16b_bf16 v[196:199], v[176:177], v[176:177], v[196:199]\n\t"
            "ds_write_b128 v236, v[176:179] offset:256\n\t"
            "s_waitcnt lgkmcnt(7)\n\t"
            "v_smfmac_f32_16x16x64_bf16 v[184:187], v[168:171], v[152:159], v255\n\t"
            "v_mfma_f32_4x4x4_16b_bf16 v[196:199], v[178:179], v[178:179], v[196:199]\n\t"
            "v_mfma_f32_16x16x32_bf16 v[192:195], v[238:241], v[176:179], v[192:195]\n\t"
            "s_waitcnt lgkmcnt(2)\n\t"
            "v_smfmac_f32_16x16x64_bf16 v[188:191], v[172:175], v[160:167], v255\n\t"
            "s_mov_b32 exec_hi, 0x3fffff\n\t"
            "s_add_u32 m0, s66, 0\n\t"
            "s_add_u32 s49, s79, 0\n\t"
            "buffer_load_dwordx4 v249, s[72:75], s49 offen lds\n\t"
            "s_mov_b32 exec_hi, -1\n\t"
            "s_mov_b32 exec_hi, 0x3fffff\n\t"
            "s_add_u32 m0, s66, 872\n\t"
            "s_add_u32 s49, s68, 196224\n\t"
            "buffer_load_dwordx4 v249, s[72:75], s49 offen lds\n\t"
            "s_mov_b32 exec_hi, -1\n\t"
            "v_cvt_pk_bf16_f32 v184, v184, v185\n\t"
            "v_cvt_pk_bf16_f32 v185, v186, v187\n\t"
            "v_cvt_pk_bf16_f32 v186, v188, v189\n\t"
            "v_cvt_pk_bf16_f32 v187, v190, v191\n\t"
            "v_mfma_f32_4x4x4_16b_bf16 v[196:199], v[184:185], v[184:185], v[196:199]\n\t"
            "s_mov_b32 exec_hi, 0x3fffff\n\t"
            "s_add_u32 m0, s66, 1744\n\t"
            "s_add_u32 s49, s68, 392448\n\t"
            "buffer_load_dwordx4 v249, s[72:75], s49 offen lds\n\t"
            "s_mov_b32 exec_hi, -1\n\t"
            "v_mfma_f32_4x4x4_16b_bf16 v[196:199], v[186:187], v[186:187], v[196:199]\n\t"
            "s_mov_b32 exec_hi, 0x3fffff\n\t"
            "s_add_u32 m0, s66, 2616\n\t"
            "s_add_u32 s49, s68, 588672\n\t"
            "buffer_load_dwordx4 v249, s[72:75], s49 offen lds\n\t"
            "s_mov_b32 exec_hi, -1\n\t"
            "v_mfma_f32_16x16x32_bf16 v[192:195], v[238:241], v[184:187], v[192:195]\n\t"
            "s_mov_b32 exec_hi, 0x3fffff\n\t"
            "s_add_u32 m0, s66, 3488\n\t"
            "s_add_u32 s49, s68, 784896\n\t"
            "buffer_load_dwordx4 v249, s[72:75], s49 offen lds\n\t"
            "s_mov_b32 exec_hi, -1\n\t"
            "s_mov_b32 exec_hi, 0x3fffff\n\t"
            "s_add_u32 m0, s66, 4360\n\t"
            "s_add_u32 s49, s68, 981120\n\t"
            "buffer_load_dwordx4 v249, s[72:75], s49 offen lds\n\t"
            "s_mov_b32 exec_hi, -1\n\t"
            "v_cndmask_b32_e64 v193, v196, v197, s[60:61]\n\t"
            "v_cndmask_b32_e64 v193, v193, v198, s[62:63]\n\t"
            "v_cndmask_b32_e64 v193, v193, v199, s[64:65]\n\t"
            "ds_read_b128 v[196:199], v233 offset:128\n\t"
            "ds_write_b64 v248, v[192:193] offset:18816\n\t"
            "ds_read_b128 v[192:195], v232 offset:128\n\t"
            "s_mov_b32 exec_hi, 0x3fffff\n\t"
            "s_add_u32 m0, s66, 5232\n\t"
            "s_add_u32 s49, s68, 1177344\n\t"
            "buffer_load_dwordx4 v249, s[72:75], s49 offen lds\n\t"
            "s_mov_b32 exec_hi, -1\n\t"
            "s_mov_b32 exec_hi, 0x3fffff\n\t"
            "s_add_u32 m0, s66, 6104\n\t"
            "s_add_u32 s49, s68, 1373568\n\t"
            "buffer_load_dwordx4 v249, s[72:75], s49 offen lds\n\t"
            "s_mov_b32 exec_hi, -1\n\t"
            "s_mov_b32 exec_hi, 0x3fffff\n\t"
            "s_add_u32 m0, s66, 6976\n\t"
            "s_add_u32 s49, s68, 1569792\n\t"
            "buffer_load_dwordx4 v249, s[72:75], s49 offen lds\n\t"
            "s_mov_b32 exec_hi, -1\n\t"
            "s_waitcnt lgkmcnt(0)\n\t"
            "v_mfma_f32_16x16x32_bf16 v[200:203], v[192:195], v[8:11], v[200:203]\n\t"
            "s_add_u32 s59, s59, -47104\n\t"
            "ds_read_b64 v[248:249], v247 offset:8\n\t"
            "v_mfma_f32_16x16x32_bf16 v[200:203], v[196:199], v[12:15], v[200:203]\n\t"
            "s_add_u32 s45, s46, s27\n\t"
            "s_mov_b32 s46, s47\n\t"
            "s_mov_b32 s47, s68\n\t"
            "v_mfma_f32_16x16x32_bf16 v[204:207], v[196:199], v[36:39], v[204:207]\n\t"
            "s_xor_b32 s48, s48, s71\n\t"
            "s_xor_b32 s67, s67, s71\n\t"
            "s_add_u32 s76, s76, 4\n\t"
            "s_mov_b32 s44, 12\n\t"
            "v_mfma_f32_16x16x32_bf16 v[204:207], v[192:195], v[32:35], v[204:207]\n\t"
            "s_lshl_b32 s56, s56, 2\n\t"
            "s_lshr_b32 s49, s26, 30\n\t"
            "s_or_b32 s56, s56, s49\n\t"
            "v_mfma_f32_16x16x32_bf16 v[208:211], v[192:195], v[56:59], v[208:211]\n\t"
            "v_mfma_f32_16x16x32_bf16 v[208:211], v[196:199], v[60:63], v[208:211]\n\t"
            "ds_write_b128 v237, v[184:187] offset:256\n\t"
            "v_mfma_f32_16x16x32_bf16 v[212:215], v[196:199], v[84:87], v[212:215]\n\t"
            "ds_read_b64 v[236:237], v247\n\t"
            "v_mfma_f32_16x16x32_bf16 v[212:215], v[192:195], v[80:83], v[212:215]\n\t"
            "s_mov_b64 exec, 1\n\t"
            "ds_inc_u32 v255, v238 offset:7860\n\t"
            "s_mov_b64 exec, -1\n\t"
            "v_mfma_f32_16x16x32_bf16 v[216:219], v[192:195], v[104:107], v[216:219]\n\t"
            "v_mfma_f32_16x16x32_bf16 v[216:219], v[196:199], v[108:111], v[216:219]\n\t"
            "v_mfma_f32_16x16x32_bf16 v[220:223], v[196:199], v[132:135], v[220:223]\n\t"
            "ds_read_b128 v[196:199], v233 offset:6144\n\t"
            "v_mfma_f32_16x16x32_bf16 v[220:223], v[192:195], v[128:131], v[220:223]\n\t"
            "ds_read_b128 v[192:195], v232 offset:6144\n\t"
            "v_mfma_f32_16x16x32_bf16 v[200:203], v[224:227], v[16:19], v[200:203]\n\t"
            "v_mfma_f32_16x16x32_bf16 v[200:203], v[228:231], v[20:23], v[200:203]\n\t"
            "v_mfma_f32_16x16x32_bf16 v[204:207], v[228:231], v[44:47], v[204:207]\n\t"
            "v_mfma_f32_16x16x32_bf16 v[204:207], v[224:227], v[40:43], v[204:207]\n\t"
            "v_mfma_f32_16x16x32_bf16 v[208:211], v[224:227], v[64:67], v[208:211]\n\t"
            "v_mfma_f32_16x16x32_bf16 v[208:211], v[228:231], v[68:71], v[208:211]\n\t"
            "v_mfma_f32_16x16x32_bf16 v[212:215], v[228:231], v[92:95], v[212:215]\n\t"
            "v_mfma_f32_16x16x32_bf16 v[212:215], v[224:227], v[88:91], v[212:215]\n\t"
            "s_waitcnt lgkmcnt(3)\n\t"
            "s_nop 0\n\t"
            "v_mul_f32 v200, v236, v200\n\t"
            "v_mfma_f32_16x16x32_bf16 v[216:219], v[224:227], v[112:115], v[216:219]\n\t"
            "v_mul_f32 v201, v237, v201\n\t"
            "v_mul_f32 v202, v248, v202\n\t"
            "v_mfma_f32_16x16x32_bf16 v[216:219], v[228:231], v[116:119], v[216:219]\n\t"
            "v_mul_f32 v204, v236, v204\n\t"
            "v_mul_f32 v205, v237, v205\n\t"
            "v_mfma_f32_16x16x32_bf16 v[220:223], v[228:231], v[140:143], v[220:223]\n\t"
            "ds_read_b128 v[228:231], v233 offset:6272\n\t"
            "v_mul_f32 v208, v236, v208\n\t"
            "v_mfma_f32_16x16x32_bf16 v[220:223], v[224:227], v[136:139], v[220:223]\n\t"
            "ds_read_b128 v[224:227], v232 offset:6272\n\t"
            "v_mul_f32 v209, v237, v209\n\t"
            "s_waitcnt lgkmcnt(2)\n\t"
            "v_mfma_f32_16x16x32_bf16 v[144:147], v[192:195], v[0:3], 0\n\t"
            "v_mul_f32 v212, v236, v212\n\t"
            "v_mul_f32 v213, v237, v213\n\t"
            "v_mfma_f32_16x16x32_bf16 v[144:147], v[196:199], v[4:7], v[144:147]\n\t"
            "v_mul_f32 v216, v236, v216\n\t"
            "v_mul_f32 v217, v237, v217\n\t"
            "v_mfma_f32_16x16x32_bf16 v[148:151], v[196:199], v[28:31], 0\n\t"
            "v_mul_f32 v220, v236, v220\n\t"
            "v_mul_f32 v221, v237, v221\n\t"
            "v_mfma_f32_16x16x32_bf16 v[148:151], v[192:195], v[24:27], v[148:151]\n\t"
            "ds_read_b64 v[236:237], v247 offset:64\n\t"
            "v_mul_f32 v203, v249, v203\n\t"
            "v_mfma_f32_16x16x32_bf16 v[168:171], v[192:195], v[48:51], 0\n\t"
            "v_mul_f32 v206, v248, v206\n\t"
            "v_mul_f32 v207, v249, v207\n\t"
            "v_mfma_f32_16x16x32_bf16 v[168:171], v[196:199], v[52:55], v[168:171]\n\t"
            "v_mul_f32 v210, v248, v210\n\t"
            "v_mul_f32 v211, v249, v211\n\t"
            "v_mfma_f32_16x16x32_bf16 v[172:175], v[196:199], v[76:79], 0\n\t"
            "v_mul_f32 v214, v248, v214\n\t"
            "v_mul_f32 v215, v249, v215\n\t"
            "v_mfma_f32_16x16x32_bf16 v[172:175], v[192:195], v[72:75], v[172:175]\n\t"
            "v_mul_f32 v218, v248, v218\n\t"
            "v_mul_f32 v219, v249, v219\n\t"
            "v_mfma_f32_16x16x32_bf16 v[152:155], v[192:195], v[96:99], 0\n\t"
            "v_mul_f32 v222, v248, v222\n\t"
            "v_mul_f32 v223, v249, v223\n\t"
            "v_mfma_f32_16x16x32_bf16 v[152:155], v[196:199], v[100:103], v[152:155]\n\t"
            "ds_read_b64 v[248:249], v247 offset:72\n\t"
            "v_cvt_pk_bf16_f32 v200, v200, v201\n\t"
            "v_mfma_f32_16x16x32_bf16 v[156:159], v[196:199], v[124:127], 0\n\t"
            "ds_read_b128 v[196:199], v233 offset:6400\n\t"
            "v_cvt_pk_bf16_f32 v204, v204, v205\n\t"
            "v_mfma_f32_16x16x32_bf16 v[156:159], v[192:195], v[120:123], v[156:159]\n\t"
            "ds_read_b128 v[192:195], v232 offset:6400\n\t"
            "v_cvt_pk_bf16_f32 v208, v208, v209\n\t"
            "s_waitcnt lgkmcnt(4)\n\t"
            "v_mfma_f32_16x16x32_bf16 v[144:147], v[224:227], v[8:11], v[144:147]\n\t"
            "v_cvt_pk_bf16_f32 v212, v212, v213\n\t"
            "v_cvt_pk_bf16_f32 v216, v216, v217\n\t"
            "v_mfma_f32_16x16x32_bf16 v[144:147], v[228:231], v[12:15], v[144:147]\n\t"
            "v_cvt_pk_bf16_f32 v220, v220, v221\n\t"
            "v_cvt_pk_bf16_f32 v201, v202, v203\n\t"
            "v_mfma_f32_16x16x32_bf16 v[148:151], v[228:231], v[36:39], v[148:151]\n\t"
            "v_cvt_pk_bf16_f32 v205, v206, v207\n\t"
            "v_cvt_pk_bf16_f32 v209, v210, v211\n\t"
            "v_mfma_f32_16x16x32_bf16 v[148:151], v[224:227], v[32:35], v[148:151]\n\t"
            "v_cvt_pk_bf16_f32 v213, v214, v215\n\t"
            "v_cvt_pk_bf16_f32 v217, v218, v219\n\t"
            "v_mfma_f32_16x16x32_bf16 v[168:171], v[224:227], v[56:59], v[168:171]\n\t"
            "v_cvt_pk_bf16_f32 v221, v222, v223\n\t"
            "v_mfma_f32_16x16x32_bf16 v[168:171], v[228:231], v[60:63], v[168:171]\n\t"
            "v_mfma_f32_16x16x32_bf16 v[172:175], v[228:231], v[84:87], v[172:175]\n\t"
            "v_mfma_f32_16x16x32_bf16 v[172:175], v[224:227], v[80:83], v[172:175]\n\t"
            "v_mfma_f32_16x16x32_bf16 v[152:155], v[224:227], v[104:107], v[152:155]\n\t"
            "v_mfma_f32_16x16x32_bf16 v[152:155], v[228:231], v[108:111], v[152:155]\n\t"
            "v_mfma_f32_16x16x32_bf16 v[156:159], v[228:231], v[132:135], v[156:159]\n\t"
            "ds_read_b128 v[228:231], v233 offset:12288\n\t"
            "v_mfma_f32_16x16x32_bf16 v[156:159], v[224:227], v[128:131], v[156:159]\n\t"
            "ds_read_b128 v[224:227], v232 offset:12288\n\t"
            "s_waitcnt lgkmcnt(2)\n\t"
            "v_mfma_f32_16x16x32_bf16 v[144:147], v[192:195], v[16:19], v[144:147]\n\t"
            "v_mfma_f32_16x16x32_bf16 v[144:147], v[196:199], v[20:23], v[144:147]\n\t"
            "v_mfma_f32_16x16x32_bf16 v[148:151], v[196:199], v[44:47], v[148:151]\n\t"
            "v_mfma_f32_16x16x32_bf16 v[148:151], v[192:195], v[40:43], v[148:151]\n\t"
            "v_mfma_f32_16x16x32_bf16 v[168:171], v[192:195], v[64:67], v[168:171]\n\t"
            "v_mfma_f32_16x16x32_bf16 v[168:171], v[196:199], v[68:71], v[168:171]\n\t"
            "v_mfma_f32_16x16x32_bf16 v[172:175], v[196:199], v[92:95], v[172:175]\n\t"
            "v_mfma_f32_16x16x32_bf16 v[172:175], v[192:195], v[88:91], v[172:175]\n\t"
            "s_nop 1\n\t"
            "v_mul_f32 v144, v236, v144\n\t"
            "v_mfma_f32_16x16x32_bf16 v[152:155], v[192:195], v[112:115], v[152:155]\n\t"
            "v_mul_f32 v145, v237, v145\n\t"
            "v_mul_f32 v146, v248, v146\n\t"
            "v_mfma_f32_16x16x32_bf16 v[152:155], v[196:199], v[116:119], v[152:155]\n\t"
            "v_mul_f32 v148, v236, v148\n\t"
            "v_mul_f32 v149, v237, v149\n\t"
            "v_mfma_f32_16x16x32_bf16 v[156:159], v[196:199], v[140:143], v[156:159]\n\t"
            "ds_read_b128 v[196:199], v233 offset:12416\n\t"
            "v_mul_f32 v168, v236, v168\n\t"
            "v_mfma_f32_16x16x32_bf16 v[156:159], v[192:195], v[136:139], v[156:159]\n\t"
            "ds_read_b128 v[192:195], v232 offset:12416\n\t"
            "v_mul_f32 v169, v237, v169\n\t"
            "s_waitcnt lgkmcnt(2)\n\t"
            "v_mfma_f32_16x16x32_bf16 v[160:163], v[224:227], v[0:3], 0\n\t"
            "v_mul_f32 v172, v236, v172\n\t"
            "v_mul_f32 v173, v237, v173\n\t"
            "v_mfma_f32_16x16x32_bf16 v[160:163], v[228:231], v[4:7], v[160:163]\n\t"
            "v_mul_f32 v152, v236, v152\n\t"
            "v_mul_f32 v153, v237, v153\n\t"
            "v_mfma_f32_16x16x32_bf16 v[164:167], v[228:231], v[28:31], 0\n\t"
            "v_mul_f32 v156, v236, v156\n\t"
            "v_mul_f32 v157, v237, v157\n\t"
            "v_mfma_f32_16x16x32_bf16 v[164:167], v[224:227], v[24:27], v[164:167]\n\t"
            "ds_read_b64 v[236:237], v247 offset:128\n\t"
            "v_mul_f32 v147, v249, v147\n\t"
            "v_mfma_f32_16x16x32_bf16 v[176:179], v[224:227], v[48:51], 0\n\t"
            "v_mul_f32 v150, v248, v150\n\t"
            "v_mul_f32 v151, v249, v151\n\t"
            "v_mfma_f32_16x16x32_bf16 v[176:179], v[228:231], v[52:55], v[176:179]\n\t"
            "v_mul_f32 v170, v248, v170\n\t"
            "v_mul_f32 v171, v249, v171\n\t"
            "v_mfma_f32_16x16x32_bf16 v[180:183], v[228:231], v[76:79], 0\n\t"
            "v_mul_f32 v174, v248, v174\n\t"
            "v_mul_f32 v175, v249, v175\n\t"
            "v_mfma_f32_16x16x32_bf16 v[180:183], v[224:227], v[72:75], v[180:183]\n\t"
            "v_mul_f32 v154, v248, v154\n\t"
            "v_mul_f32 v155, v249, v155\n\t"
            "v_mfma_f32_16x16x32_bf16 v[184:187], v[224:227], v[96:99], 0\n\t"
            "v_mul_f32 v158, v248, v158\n\t"
            "v_mul_f32 v159, v249, v159\n\t"
            "v_mfma_f32_16x16x32_bf16 v[184:187], v[228:231], v[100:103], v[184:187]\n\t"
            "ds_read_b64 v[248:249], v247 offset:136\n\t"
            "v_cvt_pk_bf16_f32 v202, v144, v145\n\t"
            "v_mfma_f32_16x16x32_bf16 v[188:191], v[228:231], v[124:127], 0\n\t"
            "ds_read_b128 v[228:231], v233 offset:12544\n\t"
            "v_cvt_pk_bf16_f32 v203, v146, v147\n\t"
            "v_mfma_f32_16x16x32_bf16 v[188:191], v[224:227], v[120:123], v[188:191]\n\t"
            "ds_read_b128 v[224:227], v232 offset:12544\n\t"
            "v_cvt_pk_bf16_f32 v206, v148, v149\n\t"
            "s_waitcnt lgkmcnt(4)\n\t"
            "v_mfma_f32_16x16x32_bf16 v[160:163], v[192:195], v[8:11], v[160:163]\n\t"
            "v_cvt_pk_bf16_f32 v207, v150, v151\n\t"
            "v_cvt_pk_bf16_f32 v210, v168, v169\n\t"
            "v_mfma_f32_16x16x32_bf16 v[160:163], v[196:199], v[12:15], v[160:163]\n\t"
            "v_cvt_pk_bf16_f32 v211, v170, v171\n\t"
            "v_cvt_pk_bf16_f32 v214, v172, v173\n\t"
            "v_mfma_f32_16x16x32_bf16 v[164:167], v[196:199], v[36:39], v[164:167]\n\t"
            "v_cvt_pk_bf16_f32 v215, v174, v175\n\t"
            "v_cvt_pk_bf16_f32 v218, v152, v153\n\t"
            "v_mfma_f32_16x16x32_bf16 v[164:167], v[192:195], v[32:35], v[164:167]\n\t"
            "v_cvt_pk_bf16_f32 v219, v154, v155\n\t"
            "v_cvt_pk_bf16_f32 v222, v156, v157\n\t"
            "v_mfma_f32_16x16x32_bf16 v[176:179], v[192:195], v[56:59], v[176:179]\n\t"
            "v_cvt_pk_bf16_f32 v223, v158, v159\n\t"
            "v_mfma_f32_16x16x32_bf16 v[176:179], v[196:199], v[60:63], v[176:179]\n\t"
            "ds_write_b128 v243, v[200:203]\n\t"
            "v_mfma_f32_16x16x32_bf16 v[180:183], v[196:199], v[84:87], v[180:183]\n\t"
            "v_mfma_f32_16x16x32_bf16 v[180:183], v[192:195], v[80:83], v[180:183]\n\t"
            "ds_write_b128 v243, v[204:207] offset:1536\n\t"
            "v_mfma_f32_16x16x32_bf16 v[184:187], v[192:195], v[104:107], v[184:187]\n\t"
            "v_mfma_f32_16x16x32_bf16 v[184:187], v[196:199], v[108:111], v[184:187]\n\t"
            "ds_write_b128 v243, v[208:211] offset:3072\n\t"
            "v_mfma_f32_16x16x32_bf16 v[188:191], v[196:199], v[132:135], v[188:191]\n\t"
            "v_mfma_f32_16x16x32_bf16 v[188:191], v[192:195], v[128:131], v[188:191]\n\t"
            "ds_write_b128 v243, v[212:215] offset:4608\n\t"
            "s_waitcnt lgkmcnt(4)\n\t"
            "v_mfma_f32_16x16x32_bf16 v[160:163], v[224:227], v[16:19], v[160:163]\n\t"
            "v_mfma_f32_16x16x32_bf16 v[160:163], v[228:231], v[20:23], v[160:163]\n\t"
            "ds_write_b128 v243, v[216:219] offset:6144\n\t"
            "v_mfma_f32_16x16x32_bf16 v[164:167], v[228:231], v[44:47], v[164:167]\n\t"
            "v_mfma_f32_16x16x32_bf16 v[164:167], v[224:227], v[40:43], v[164:167]\n\t"
            "ds_write_b128 v243, v[220:223] offset:7680\n\t"
            "v_mfma_f32_16x16x32_bf16 v[176:179], v[224:227], v[64:67], v[176:179]\n\t"
            "v_mfma_f32_16x16x32_bf16 v[176:179], v[228:231], v[68:71], v[176:179]\n\t"
            "s_nop 1\n\t"
            "v_mul_f32 v160, v236, v160\n\t"
            "v_mfma_f32_16x16x32_bf16 v[180:183], v[228:231], v[92:95], v[180:183]\n\t"
            "v_mul_f32 v161, v237, v161\n\t"
            "v_mul_f32 v162, v248, v162\n\t"
            "v_mfma_f32_16x16x32_bf16 v[180:183], v[224:227], v[88:91], v[180:183]\n\t"
            "v_mul_f32 v164, v236, v164\n\t"
            "v_mul_f32 v165, v237, v165\n\t"
            "v_mfma_f32_16x16x32_bf16 v[184:187], v[224:227], v[112:115], v[184:187]\n\t"
            "v_mul_f32 v176, v236, v176\n\t"
            "v_mul_f32 v177, v237, v177\n\t"
            "v_mfma_f32_16x16x32_bf16 v[184:187], v[228:231], v[116:119], v[184:187]\n\t"
            "v_mul_f32 v163, v249, v163\n\t"
            "v_cvt_pk_bf16_f32 v160, v160, v161\n\t"
            "v_mfma_f32_16x16x32_bf16 v[188:191], v[228:231], v[140:143], v[188:191]\n\t"
            "v_mul_f32 v180, v236, v180\n\t"
            "v_mul_f32 v181, v237, v181\n\t"
            "v_mfma_f32_16x16x32_bf16 v[188:191], v[224:227], v[136:139], v[188:191]\n\t"
            "v_mul_f32 v166, v248, v166\n\t"
            "v_mul_f32 v167, v249, v167\n\t"
            "v_mul_f32 v184, v236, v184\n\t"
            "v_mul_f32 v185, v237, v185\n\t"
            "v_cvt_pk_bf16_f32 v164, v164, v165\n\t"
            "v_mul_f32 v178, v248, v178\n\t"
            "v_mul_f32 v179, v249, v179\n\t"
            "v_cvt_pk_bf16_f32 v176, v176, v177\n\t"
            "v_mul_f32 v188, v236, v188\n\t"
            "v_mul_f32 v189, v237, v189\n\t"
            "v_mul_f32 v182, v248, v182\n\t"
            "v_mul_f32 v183, v249, v183\n\t"
            "v_cvt_pk_bf16_f32 v180, v180, v181\n\t"
            "v_mul_f32 v186, v248, v186\n\t"
            "v_mul_f32 v187, v249, v187\n\t"
            "v_cvt_pk_bf16_f32 v184, v184, v185\n\t"
            "v_mul_f32 v190, v248, v190\n\t"
            "v_mul_f32 v191, v249, v191\n\t"
            "v_cvt_pk_bf16_f32 v188, v188, v189\n\t"
            "v_cvt_pk_bf16_f32 v161, v162, v163\n\t"
            "v_cvt_pk_bf16_f32 v165, v166, v167\n\t"
            "v_cvt_pk_bf16_f32 v177, v178, v179\n\t"
            "v_cvt_pk_bf16_f32 v181, v182, v183\n\t"
            "v_cvt_pk_bf16_f32 v185, v186, v187\n\t"
            "v_cvt_pk_bf16_f32 v189, v190, v191\n\t"
            "ds_write_b64 v244, v[160:161]\n\t"
            "ds_write_b64 v244, v[164:165] offset:1536\n\t"
            "ds_write_b64 v244, v[176:177] offset:3072\n\t"
            "ds_write_b64 v244, v[180:181] offset:4608\n\t"
            "ds_write_b64 v244, v[184:185] offset:6144\n\t"
            "ds_write_b64 v244, v[188:189] offset:7680\n\t"
            "s_waitcnt lgkmcnt(0)\n\t"
            "s_barrier\n\t"
            "v_add_u32 v232, s48, v242\n\t"
            "v_xor_b32 v233, 64, v232\n\t"
            "ds_read_b128 v[192:195], v232\n\t"
            "ds_read_b128 v[196:199], v233\n\t"
            "ds_read_b128 v[224:227], v232 offset:128\n\t"
            "ds_read_b128 v[228:231], v233 offset:128\n\t"
            "s_waitcnt lgkmcnt(2)\n\t"
            "v_mfma_f32_16x16x32_bf16 v[200:203], v[192:195], v[0:3], 0\n\t"
            "v_mfma_f32_16x16x32_bf16 v[200:203], v[196:199], v[4:7], v[200:203]\n\t"
            "v_mfma_f32_16x16x32_bf16 v[204:207], v[196:199], v[28:31], 0\n\t"
            "v_mfma_f32_16x16x32_bf16 v[204:207], v[192:195], v[24:27], v[204:207]\n\t"
            "v_mfma_f32_16x16x32_bf16 v[208:211], v[192:195], v[48:51], 0\n\t"
            "v_mfma_f32_16x16x32_bf16 v[208:211], v[196:199], v[52:55], v[208:211]\n\t"
            "v_mfma_f32_16x16x32_bf16 v[212:215], v[196:199], v[76:79], 0\n\t"
            "v_mfma_f32_16x16x32_bf16 v[212:215], v[192:195], v[72:75], v[212:215]\n\t"
            "v_mfma_f32_16x16x32_bf16 v[216:219], v[192:195], v[96:99], 0\n\t"
            "v_mfma_f32_16x16x32_bf16 v[216:219], v[196:199], v[100:103], v[216:219]\n\t"
            "v_mfma_f32_16x16x32_bf16 v[220:223], v[196:199], v[124:127], 0\n\t"
            "v_mfma_f32_16x16x32_bf16 v[220:223], v[192:195], v[120:123], v[220:223]\n\t"
            "s_waitcnt lgkmcnt(0)\n\t"
            "v_mfma_f32_16x16x32_bf16 v[200:203], v[224:227], v[8:11], v[200:203]\n\t"
            "v_mfma_f32_16x16x32_bf16 v[200:203], v[228:231], v[12:15], v[200:203]\n\t"
            "v_mfma_f32_16x16x32_bf16 v[204:207], v[228:231], v[36:39], v[204:207]\n\t"
            "v_mfma_f32_16x16x32_bf16 v[204:207], v[224:227], v[32:35], v[204:207]\n\t"
            "v_mfma_f32_16x16x32_bf16 v[208:211], v[224:227], v[56:59], v[208:211]\n\t"
            "v_mfma_f32_16x16x32_bf16 v[208:211], v[228:231], v[60:63], v[208:211]\n\t"
            "v_mfma_f32_16x16x32_bf16 v[212:215], v[228:231], v[84:87], v[212:215]\n\t"
            "v_mfma_f32_16x16x32_bf16 v[212:215], v[224:227], v[80:83], v[212:215]\n\t"
            "v_mfma_f32_16x16x32_bf16 v[216:219], v[224:227], v[104:107], v[216:219]\n\t"
            "v_mfma_f32_16x16x32_bf16 v[216:219], v[228:231], v[108:111], v[216:219]\n\t"
            "v_mfma_f32_16x16x32_bf16 v[220:223], v[228:231], v[132:135], v[220:223]\n\t"
            "v_mfma_f32_16x16x32_bf16 v[220:223], v[224:227], v[128:131], v[220:223]\n\t"
            "ds_read_b128 v[224:227], v232 offset:256\n\t"
            "ds_read_b128 v[228:231], v233 offset:256\n\t"
            "ds_read_b128 v[168:171], v254\n\t"
            "ds_read_b128 v[172:175], v254 offset:64\n\t"
            "s_waitcnt vmcnt(0)\n\t"
            "ds_read_b64 v[144:145], v253\n\t"
            "ds_read_b64 v[146:147], v253 offset:96\n\t"
            "ds_read_b64 v[148:149], v253 offset:1744\n\t"
            "ds_read_b64 v[150:151], v253 offset:1840\n\t"
            "ds_read_b64 v[152:153], v253 offset:8\n\t"
            "ds_read_b64 v[154:155], v253 offset:104\n\t"
            "ds_read_b64 v[156:157], v253 offset:1752\n\t"
            "ds_read_b64 v[158:159], v253 offset:1848\n\t"
            "ds_read_b64 v[160:161], v253 offset:16\n\t"
            "ds_read_b64 v[162:163], v253 offset:112\n\t"
            "ds_read_b64 v[164:165], v253 offset:1760\n\t"
            "s_waitcnt lgkmcnt(14)\n\t"
            "ds_read_b64 v[166:167], v253 offset:1856\n\t"
            "s_waitcnt lgkmcnt(0)\n\t"
            "s_nop 7\n\t"
            ".p2align 6\n\t"
            "s_nop 0\n\t"
            "s_nop 0\n\t"
            "s_nop 0\n\t"
            "s_nop 0\n\t"
            ".Lwc_loop%=:\n\t"
            "s_setprio 1\n\t"
            "s_setprio 3\n\t"
            "v_mfma_f32_16x16x32_bf16 v[200:203], v[224:227], v[16:19], v[200:203]\n\t"
            "v_mov_b64 v[176:177], 0\n\t"
            "v_mov_b64 v[178:179], 0\n\t"
            "v_mfma_f32_16x16x32_bf16 v[200:203], v[228:231], v[20:23], v[200:203]\n\t"
            "v_mov_b64 v[180:181], 0\n\t"
            "v_mov_b64 v[182:183], 0\n\t"
            "s_waitcnt lgkmcnt(8)\n\t"
            "v_smfmac_f32_16x16x64_bf16 v[176:179], v[168:171], v[144:151], v255\n\t"
            "ds_read_b128 v[168:171], v254 offset:128\n\t"
            "s_waitcnt lgkmcnt(5)\n\t"
            "v_smfmac_f32_16x16x64_bf16 v[180:183], v[172:175], v[152:159], v255\n\t"
            "ds_read_b64 v[144:145], v253 offset:24\n\t"
            "v_mfma_f32_16x16x32_bf16 v[204:207], v[228:231], v[44:47], v[204:207]\n\t"
            "ds_read_b64 v[146:147], v253 offset:120\n\t"
            "ds_read_b64 v[148:149], v253 offset:1768\n\t"
            "v_mfma_f32_16x16x32_bf16 v[204:207], v[224:227], v[40:43], v[204:207]\n\t"
            "ds_read_b64 v[150:151], v253 offset:1864\n\t"
            "ds_read_b128 v[172:175], v254 offset:192\n\t"
            "v_mfma_f32_16x16x32_bf16 v[208:211], v[224:227], v[64:67], v[208:211]\n\t"
            "ds_read_b64 v[152:153], v253 offset:32\n\t"
            "ds_read_b64 v[154:155], v253 offset:128\n\t"
            "v_mfma_f32_16x16x32_bf16 v[208:211], v[228:231], v[68:71], v[208:211]\n\t"
            "ds_read_b64 v[156:157], v253 offset:1776\n\t"
            "ds_read_b64 v[158:159], v253 offset:1872\n\t"
            "v_mfma_f32_16x16x32_bf16 v[212:215], v[228:231], v[92:95], v[212:215]\n\t"
            "v_mov_b32 v249, s76\n\t"
            "v_mov_b64 v[184:185], 0\n\t"
            "v_mfma_f32_16x16x32_bf16 v[212:215], v[224:227], v[88:91], v[212:215]\n\t"
            "ds_read_b32 v249, v249\n\t"
            "v_mov_b64 v[186:187], 0\n\t"
            "v_mfma_f32_16x16x32_bf16 v[216:219], v[224:227], v[112:115], v[216:219]\n\t"
            "v_mov_b64 v[188:189], 0\n\t"
            "v_mov_b64 v[190:191], 0\n\t"
            "v_mfma_f32_16x16x32_bf16 v[216:219], v[228:231], v[116:119], v[216:219]\n\t"
            "v_cvt_pk_bf16_f32 v176, v176, v177\n\t"
            "v_add_u32 v236, s67, v250\n\t"
            "s_waitcnt lgkmcnt(10)\n\t"
            "v_smfmac_f32_16x16x64_bf16 v[184:187], v[168:171], v[160:167], v255\n\t"
            "ds_read_b128 v[168:171], v254 offset:256\n\t"
            "v_mfma_f32_16x16x32_bf16 v[220:223], v[228:231], v[140:143], v[220:223]\n\t"
            "ds_read_b64 v[160:161], v253 offset:40\n\t"
            "ds_read_b64 v[162:163], v253 offset:136\n\t"
            "v_mfma_f32_16x16x32_bf16 v[220:223], v[224:227], v[136:139], v[220:223]\n\t"
            "ds_read_b64 v[164:165], v253 offset:1784\n\t"
            "ds_read_b64 v[166:167], v253 offset:1880\n\t"
            "s_waitcnt lgkmcnt(10)\n\t"
            "v_smfmac_f32_16x16x64_bf16 v[188:191], v[172:175], v[144:151], v255\n\t"
            "s_setprio 1\n\t"
            "ds_read_b128 v[172:175], v254 offset:320\n\t"
            "ds_read_b64 v[144:145], v253 offset:48\n\t"
            "ds_read_b64 v[146:147], v253 offset:144\n\t"
            "ds_read_b64 v[148:149], v253 offset:1792\n\t"
            "ds_read_b64 v[150:151], v253 offset:1888\n\t"
            "s_waitcnt lgkmcnt(14)\n\t"
            "ds_read_b128 v[224:227], v232 offset:6272\n\t"
            "s_waitcnt lgkmcnt(14)\n\t"
            "ds_read_b128 v[228:231], v233 offset:6272\n\t"
            "s_waitcnt lgkmcnt(12)\n\t"
            "v_readfirstlane_b32 s26, v249\n\t"
            "v_cvt_pk_bf16_f32 v177, v178, v179\n\t"
            "v_cvt_pk_bf16_f32 v178, v180, v181\n\t"
            "s_and_b32 s68, s26, 0x7ffffff\n\t"
            "s_add_u32 s68, s68, s78\n\t"
            "s_and_b32 s69, s26, 0x38000000\n\t"
            "s_or_b32 s69, s69, s39\n\t"
            "s_bitcmp1_b32 s26, 27\n\t"
            "s_cselect_b32 s79, 2.0, s68\n\t"
            "s_setprio 3\n\t"
            "v_mfma_f32_4x4x4_16b_bf16 v[196:199], v[176:177], v[176:177], 0\n\t"
            "s_setprio 1\n\t"
            "ds_read_b32 v249, v252 offset:12\n\t"
            "s_and_b32 s57, s56, 3\n\t"
            "s_lshl_b32 s58, s57, 3\n\t"
            "s_lshr_b32 m0, s54, s58\n\t"
            "s_lshl_b32 s58, s57, 4\n\t"
            "s_lshr_b64 s[36:37], s[50:51], s58\n\t"
            "s_and_b32 s57, s36, 0xffff\n\t"
            "s_add_u32 s57, s57, s67\n\t"
            "v_writelane_b32 v236, s57, 63\n\t"
            "v_writelane_b32 v236, s59, m0\n\t"
            "v_cvt_pk_bf16_f32 v179, v182, v183\n\t"
            "v_mov_b64 v[180:181], 0\n\t"
            "v_mov_b64 v[182:183], 0\n\t"
            "s_setprio 3\n\t"
            "v_mfma_f32_16x16x32_bf16 v[192:195], v[238:241], v[176:179], 0\n\t"
            "s_lshr_b64 s[36:37], s[52:53], s58\n\t"
            "s_and_b32 s58, s36, 0xffff\n\t"
            "s_add_u32 s58, s58, s67\n\t"
            "v_mfma_f32_4x4x4_16b_bf16 v[196:199], v[178:179], v[178:179], v[196:199]\n\t"
            "ds_write_b128 v236, v[176:179]\n\t"
            "s_waitcnt lgkmcnt(8)\n\t"
            "v_smfmac_f32_16x16x64_bf16 v[180:183], v[172:175], v[160:167], v255\n\t"
            "s_setprio 1\n\t"
            "s_and_b32 s38, s52, 0xffff\n\t"
            "s_add_u32 s38, s38, s67\n\t"
            "ds_read_b128 v[172:175], v254 offset:448\n\t"
            "ds_read_b64 v[160:161], v253 offset:64\n\t"
            "ds_read_b64 v[162:163], v253 offset:160\n\t"
            "ds_read_b64 v[164:165], v253 offset:1808\n\t"
            "ds_read_b64 v[166:167], v253 offset:1904\n\t"
            "v_mov_b64 v[176:177], 0\n\t"
            "v_mov_b64 v[178:179], 0\n\t"
            "v_cvt_pk_bf16_f32 v184, v184, v185\n\t"
            "v_cvt_pk_bf16_f32 v185, v186, v187\n\t"
            "s_setprio 3\n\t"
            "v_smfmac_f32_16x16x64_bf16 v[176:179], v[168:171], v[152:159], v255\n\t"
            "ds_read_b128 v[168:171], v254 offset:384\n\t"
            "v_mfma_f32_4x4x4_16b_bf16 v[196:199], v[184:185], v[184:185], v[196:199]\n\t"
            "s_setprio 1\n\t"
            "ds_read_b64 v[152:153], v253 offset:56\n\t"
            "s_waitcnt lgkmcnt(14)\n\t"
            "ds_read_b64 v[154:155], v253 offset:152\n\t"
            "s_waitcnt lgkmcnt(14)\n\t"
            "ds_read_b64 v[156:157], v253 offset:1800\n\t"
            "s_waitcnt lgkmcnt(14)\n\t"
            "ds_read_b64 v[158:159], v253 offset:1896\n\t"
            "v_xor_b32 v237, 16, v236\n\t"
            "v_cvt_pk_bf16_f32 v186, v188, v189\n\t"
            "v_cvt_pk_bf16_f32 v187, v190, v191\n\t"
            "v_mov_b64 v[188:189], 0\n\t"
            "v_mov_b64 v[190:191], 0\n\t"
            "s_setprio 3\n\t"
            "v_mfma_f32_16x16x32_bf16 v[192:195], v[238:241], v[184:187], v[192:195]\n\t"
            "s_waitcnt lgkmcnt(14)\n\t"
            "ds_write_b128 v237, v[184:187]\n\t"
            "v_mfma_f32_4x4x4_16b_bf16 v[196:199], v[186:187], v[186:187], v[196:199]\n\t"
            "s_setprio 1\n\t"
            "v_mov_b64 v[184:185], 0\n\t"
            "v_mov_b64 v[186:187], 0\n\t"
            "v_cvt_pk_bf16_f32 v176, v176, v177\n\t"
            "v_cvt_pk_bf16_f32 v177, v178, v179\n\t"
            "v_cvt_pk_bf16_f32 v178, v180, v181\n\t"
            "v_cvt_pk_bf16_f32 v179, v182, v183\n\t"
            "s_setprio 3\n\t"
            "v_mfma_f32_4x4x4_16b_bf16 v[196:199], v[176:177], v[176:177], v[196:199]\n\t"
            "s_waitcnt lgkmcnt(14)\n\t"
            "ds_write_b128 v236, v[176:179] offset:128\n\t"
            "s_waitcnt lgkmcnt(6)\n\t"
            "v_smfmac_f32_16x16x64_bf16 v[184:187], v[168:171], v[144:151], v255\n\t"
            "ds_read_b128 v[168:171], v254 offset:512\n\t"
            "v_mfma_f32_16x16x32_bf16 v[192:195], v[238:241], v[176:179], v[192:195]\n\t"
            "ds_read_b64 v[144:145], v253 offset:72\n\t"
            "ds_read_b64 v[146:147], v253 offset:168\n\t"
            "s_waitcnt lgkmcnt(5)\n\t"
            "v_smfmac_f32_16x16x64_bf16 v[188:191], v[172:175], v[152:159], v255\n\t"
            "ds_read_b64 v[148:149], v253 offset:1816\n\t"
            "v_mfma_f32_4x4x4_16b_bf16 v[196:199], v[178:179], v[178:179], v[196:199]\n\t"
            "s_setprio 1\n\t"
            "ds_read_b64 v[150:151], v253 offset:1912\n\t"
            "ds_read_b128 v[172:175], v254 offset:576\n\t"
            "ds_read_b64 v[152:153], v253 offset:80\n\t"
            "ds_read_b64 v[154:155], v253 offset:176\n\t"
            "ds_read_b64 v[156:157], v253 offset:1824\n\t"
            "ds_read_b64 v[158:159], v253 offset:1920\n\t"
            "v_mov_b64 v[176:177], 0\n\t"
            "v_mov_b64 v[178:179], 0\n\t"
            "v_mov_b64 v[180:181], 0\n\t"
            "v_mov_b64 v[182:183], 0\n\t"
            "v_cvt_pk_bf16_f32 v184, v184, v185\n\t"
            "v_cvt_pk_bf16_f32 v185, v186, v187\n\t"
            "v_cvt_pk_bf16_f32 v186, v188, v189\n\t"
            "v_cvt_pk_bf16_f32 v187, v190, v191\n\t"
            "s_setprio 3\n\t"
            "v_mfma_f32_4x4x4_16b_bf16 v[196:199], v[184:185], v[184:185], v[196:199]\n\t"
            "ds_write_b128 v237, v[184:187] offset:128\n\t"
            "v_mfma_f32_16x16x32_bf16 v[192:195], v[238:241], v[184:187], v[192:195]\n\t"
            "v_mov_b64 v[184:185], 0\n\t"
            "v_mov_b64 v[188:189], 0\n\t"
            "s_waitcnt lgkmcnt(10)\n\t"
            "v_smfmac_f32_16x16x64_bf16 v[176:179], v[168:171], v[160:167], v255\n\t"
            "ds_read_b128 v[168:171], v254 offset:640\n\t"
            "v_mfma_f32_4x4x4_16b_bf16 v[196:199], v[186:187], v[186:187], v[196:199]\n\t"
            "ds_read_b64 v[160:161], v253 offset:88\n\t"
            "ds_read_b64 v[162:163], v253 offset:184\n\t"
            "ds_read_b64 v[164:165], v253 offset:1832\n\t"
            "s_waitcnt lgkmcnt(9)\n\t"
            "v_smfmac_f32_16x16x64_bf16 v[180:183], v[172:175], v[144:151], v255\n\t"
            "s_setprio 1\n\t"
            "ds_read_b64 v[166:167], v253 offset:1928\n\t"
            "ds_read_b128 v[172:175], v254 offset:704\n\t"
            "v_mov_b64 v[186:187], 0\n\t"
            "v_mov_b64 v[190:191], 0\n\t"
            "v_add_u32 v248, s67, v251\n\t"
            "v_writelane_b32 v248, s58, 63\n\t"
            "v_writelane_b32 v248, s38, m0\n\t"
            "v_and_b32 v249, s69, v249\n\t"
            "v_cvt_pk_bf16_f32 v176, v176, v177\n\t"
            "v_cvt_pk_bf16_f32 v177, v178, v179\n\t"
            "v_cvt_pk_bf16_f32 v178, v180, v181\n\t"
            "v_cvt_pk_bf16_f32 v179, v182, v183\n\t"
            "s_setprio 3\n\t"
            "v_mfma_f32_4x4x4_16b_bf16 v[196:199], v[176:177], v[176:177], v[196:199]\n\t"
            "ds_write_b128 v236, v[176:179] offset:256\n\t"
            "v_mfma_f32_16x16x32_bf16 v[192:195], v[238:241], v[176:179], v[192:195]\n\t"
            "v_add_u32 v180, s48, v245\n\t"
            "v_mov_b32 v234, 0\n\t"
            "v_mfma_f32_4x4x4_16b_bf16 v[196:199], v[178:179], v[178:179], v[196:199]\n\t"
            "ds_read_b128 v[176:179], v180 offset:18816\n\t"
            "s_waitcnt lgkmcnt(7)\n\t"
            "v_smfmac_f32_16x16x64_bf16 v[184:187], v[168:171], v[152:159], v255\n\t"
            "ds_read_b128 v[180:183], v180 offset:19840\n\t"
            "s_waitcnt lgkmcnt(3)\n\t"
            "v_smfmac_f32_16x16x64_bf16 v[188:191], v[172:175], v[160:167], v255\n\t"
            "s_setprio 1\n\t"
            "s_mov_b32 exec_hi, 0x3fffff\n\t"
            "s_add_u32 m0, s66, 0\n\t"
            "s_add_u32 s49, s79, 0\n\t"
            "buffer_load_dwordx4 v249, s[72:75], s49 offen lds\n\t"
            "s_mov_b32 exec_hi, -1\n\t"
            "s_mov_b32 exec_hi, 0x3fffff\n\t"
            "s_add_u32 m0, s66, 872\n\t"
            "s_add_u32 s49, s68, 196224\n\t"
            "buffer_load_dwordx4 v249, s[72:75], s49 offen lds\n\t"
            "s_mov_b32 exec_hi, -1\n\t"
            "s_mov_b32 exec_hi, 0x3fffff\n\t"
            "s_add_u32 m0, s66, 1744\n\t"
            "s_add_u32 s49, s68, 392448\n\t"
            "buffer_load_dwordx4 v249, s[72:75], s49 offen lds\n\t"
            "s_mov_b32 exec_hi, -1\n\t"
            "s_mov_b32 exec_hi, 0x3fffff\n\t"
            "s_add_u32 m0, s66, 2616\n\t"
            "s_add_u32 s49, s68, 588672\n\t"
            "buffer_load_dwordx4 v249, s[72:75], s49 offen lds\n\t"
            "s_mov_b32 exec_hi, -1\n\t"
            "s_mov_b32 exec_hi, 0x3fffff\n\t"
            "s_add_u32 m0, s66, 3488\n\t"
            "s_add_u32 s49, s68, 784896\n\t"
            "buffer_load_dwordx4 v249, s[72:75], s49 offen lds\n\t"
            "s_mov_b32 exec_hi, -1\n\t"
            "v_cvt_pk_bf16_f32 v184, v184, v185\n\t"
            "v_cvt_pk_bf16_f32 v185, v186, v187\n\t"
            "v_cvt_pk_bf16_f32 v186, v188, v189\n\t"
            "v_cvt_pk_bf16_f32 v187, v190, v191\n\t"
            "s_setprio 3\n\t"
            "v_mfma_f32_4x4x4_16b_bf16 v[196:199], v[184:185], v[184:185], v[196:199]\n\t"
            "s_mov_b32 exec_hi, 0x3fffff\n\t"
            "s_add_u32 m0, s66, 4360\n\t"
            "s_add_u32 s49, s68, 981120\n\t"
            "buffer_load_dwordx4 v249, s[72:75], s49 offen lds\n\t"
            "s_mov_b32 exec_hi, -1\n\t"
            "v_mfma_f32_4x4x4_16b_bf16 v[196:199], v[186:187], v[186:187], v[196:199]\n\t"
            "s_mov_b32 exec_hi, 0x3fffff\n\t"
            "s_add_u32 m0, s66, 5232\n\t"
            "s_add_u32 s49, s68, 1177344\n\t"
            "buffer_load_dwordx4 v249, s[72:75], s49 offen lds\n\t"
            "s_mov_b32 exec_hi, -1\n\t"
            "v_mfma_f32_16x16x32_bf16 v[192:195], v[238:241], v[184:187], v[192:195]\n\t"
            "s_setprio 1\n\t"
            "s_mov_b32 exec_hi, 0x3fffff\n\t"
            "s_add_u32 m0, s66, 6104\n\t"
            "s_add_u32 s49, s68, 1373568\n\t"
            "buffer_load_dwordx4 v249, s[72:75], s49 offen lds\n\t"
            "s_mov_b32 exec_hi, -1\n\t"
            "s_waitcnt lgkmcnt(1)\n\t"
            "v_add_f32 v176, v176, v178\n\t"
            "s_waitcnt lgkmcnt(0)\n\t"
            "v_add_f32 v180, v180, v182\n\t"
            "v_add_f32 v176, v176, v180\n\t"
            "v_cndmask_b32_e64 v193, v196, v197, s[60:61]\n\t"
            "v_cndmask_b32_e64 v193, v193, v198, s[62:63]\n\t"
            "v_cndmask_b32_e64 v193, v193, v199, s[64:65]\n\t"
            "v_add_f32 v177, v177, v179\n\t"
            "v_add_f32 v181, v181, v183\n\t"
            "v_mul_f32 v176, 0x3baaaaab, v176\n\t"
            "v_add_f32 v177, v177, v181\n\t"
            "v_mul_f32_e64 v176, v176, -v176\n\t"
            "v_fmac_f32 v176, 0x3baaaaab, v177\n\t"
            "v_max_f32 v176, 0, v176\n\t"
            "v_add_f32 v176, 0x3727c5ac, v176\n\t"
            "v_rsq_f32 v176, v176\n\t"
            "s_setprio 1\n\t"
            "ds_read_b128 v[196:199], v233 offset:6144\n\t"
            "ds_write_b64 v248, v[192:193] offset:18816\n\t"
            "ds_read_b128 v[192:195], v232 offset:6144\n\t"
            "s_mov_b32 exec_hi, 0x3fffff\n\t"
            "s_add_u32 m0, s66, 6976\n\t"
            "s_add_u32 s49, s68, 1569792\n\t"
            "buffer_load_dwordx4 v249, s[72:75], s49 offen lds\n\t"
            "s_mov_b32 exec_hi, -1\n\t"
            "ds_write_b128 v237, v[184:187] offset:256\n\t"
            "ds_write_b32 v246, v176\n\t"
            "ds_read_b64 v[236:237], v247\n\t"
            "ds_read_b64 v[248:249], v247 offset:8\n\t"
            "s_mov_b64 exec, 1\n\t"
            "ds_inc_u32 v255, v238 offset:7860\n\t"
            "s_mov_b64 exec, -1\n\t"
            "s_waitcnt lgkmcnt(5)\n\t"
            "v_mfma_f32_16x16x32_bf16 v[144:147], v[192:195], v[0:3], 0\n\t"
            "v_mfma_f32_16x16x32_bf16 v[144:147], v[196:199], v[4:7], v[144:147]\n\t"
            "v_mfma_f32_16x16x32_bf16 v[148:151], v[196:199], v[28:31], 0\n\t"
            "v_mfma_f32_16x16x32_bf16 v[148:151], v[192:195], v[24:27], v[148:151]\n\t"
            "v_mfma_f32_16x16x32_bf16 v[168:171], v[192:195], v[48:51], 0\n\t"
            "v_mfma_f32_16x16x32_bf16 v[168:171], v[196:199], v[52:55], v[168:171]\n\t"
            "s_waitcnt lgkmcnt(2)\n\t"
            "v_mul_f32 v200, v236, v200\n\t"
            "v_mul_f32 v201, v237, v201\n\t"
            "v_mfma_f32_16x16x32_bf16 v[172:175], v[196:199], v[76:79], 0\n\t"
            "v_mul_f32 v204, v236, v204\n\t"
            "v_mul_f32 v205, v237, v205\n\t"
            "v_mfma_f32_16x16x32_bf16 v[172:175], v[192:195], v[72:75], v[172:175]\n\t"
            "v_mul_f32 v208, v236, v208\n\t"
            "v_mul_f32 v209, v237, v209\n\t"
            "v_mfma_f32_16x16x32_bf16 v[152:155], v[192:195], v[96:99], 0\n\t"
            "v_mul_f32 v212, v236, v212\n\t"
            "v_mul_f32 v213, v237, v213\n\t"
            "v_mfma_f32_16x16x32_bf16 v[152:155], v[196:199], v[100:103], v[152:155]\n\t"
            "v_mul_f32 v216, v236, v216\n\t"
            "v_mul_f32 v217, v237, v217\n\t"
            "v_mfma_f32_16x16x32_bf16 v[156:159], v[196:199], v[124:127], 0\n\t"
            "ds_read_b128 v[196:199], v233 offset:6400\n\t"
            "v_mul_f32 v220, v236, v220\n\t"
            "v_mfma_f32_16x16x32_bf16 v[156:159], v[192:195], v[120:123], v[156:159]\n\t"
            "ds_read_b128 v[192:195], v232 offset:6400\n\t"
            "v_mul_f32 v221, v237, v221\n\t"
            "v_mfma_f32_16x16x32_bf16 v[144:147], v[224:227], v[8:11], v[144:147]\n\t"
            "ds_read_b64 v[236:237], v247 offset:64\n\t"
            "s_waitcnt lgkmcnt(4)\n\t"
            "v_mul_f32 v202, v248, v202\n\t"
            "v_mfma_f32_16x16x32_bf16 v[144:147], v[228:231], v[12:15], v[144:147]\n\t"
            "v_mul_f32 v203, v249, v203\n\t"
            "v_mul_f32 v206, v248, v206\n\t"
            "v_mfma_f32_16x16x32_bf16 v[148:151], v[228:231], v[36:39], v[148:151]\n\t"
            "v_mul_f32 v207, v249, v207\n\t"
            "v_mul_f32 v210, v248, v210\n\t"
            "v_mfma_f32_16x16x32_bf16 v[148:151], v[224:227], v[32:35], v[148:151]\n\t"
            "v_mul_f32 v211, v249, v211\n\t"
            "v_mul_f32 v214, v248, v214\n\t"
            "v_mfma_f32_16x16x32_bf16 v[168:171], v[224:227], v[56:59], v[168:171]\n\t"
            "v_mul_f32 v215, v249, v215\n\t"
            "v_mul_f32 v218, v248, v218\n\t"
            "v_mfma_f32_16x16x32_bf16 v[168:171], v[228:231], v[60:63], v[168:171]\n\t"
            "v_mul_f32 v219, v249, v219\n\t"
            "v_mul_f32 v222, v248, v222\n\t"
            "v_mfma_f32_16x16x32_bf16 v[172:175], v[228:231], v[84:87], v[172:175]\n\t"
            "v_mul_f32 v223, v249, v223\n\t"
            "v_cvt_pk_bf16_f32 v200, v200, v201\n\t"
            "v_mfma_f32_16x16x32_bf16 v[172:175], v[224:227], v[80:83], v[172:175]\n\t"
            "ds_read_b64 v[248:249], v247 offset:72\n\t"
            "v_cvt_pk_bf16_f32 v204, v204, v205\n\t"
            "v_mfma_f32_16x16x32_bf16 v[152:155], v[224:227], v[104:107], v[152:155]\n\t"
            "v_cvt_pk_bf16_f32 v208, v208, v209\n\t"
            "v_cvt_pk_bf16_f32 v212, v212, v213\n\t"
            "v_mfma_f32_16x16x32_bf16 v[152:155], v[228:231], v[108:111], v[152:155]\n\t"
            "v_cvt_pk_bf16_f32 v216, v216, v217\n\t"
            "v_cvt_pk_bf16_f32 v220, v220, v221\n\t"
            "v_mfma_f32_16x16x32_bf16 v[156:159], v[228:231], v[132:135], v[156:159]\n\t"
            "ds_read_b128 v[228:231], v233 offset:12288\n\t"
            "v_cvt_pk_bf16_f32 v201, v202, v203\n\t"
            "v_mfma_f32_16x16x32_bf16 v[156:159], v[224:227], v[128:131], v[156:159]\n\t"
            "ds_read_b128 v[224:227], v232 offset:12288\n\t"
            "v_cvt_pk_bf16_f32 v205, v206, v207\n\t"
            "s_waitcnt lgkmcnt(4)\n\t"
            "v_mfma_f32_16x16x32_bf16 v[144:147], v[192:195], v[16:19], v[144:147]\n\t"
            "v_cvt_pk_bf16_f32 v209, v210, v211\n\t"
            "v_cvt_pk_bf16_f32 v213, v214, v215\n\t"
            "v_mfma_f32_16x16x32_bf16 v[144:147], v[196:199], v[20:23], v[144:147]\n\t"
            "v_cvt_pk_bf16_f32 v217, v218, v219\n\t"
            "v_cvt_pk_bf16_f32 v221, v222, v223\n\t"
            "v_mfma_f32_16x16x32_bf16 v[148:151], v[196:199], v[44:47], v[148:151]\n\t"
            "v_mfma_f32_16x16x32_bf16 v[148:151], v[192:195], v[40:43], v[148:151]\n\t"
            "ds_read_b64 v[234:235], v234 offset:7856\n\t"
            "v_mfma_f32_16x16x32_bf16 v[168:171], v[192:195], v[64:67], v[168:171]\n\t"
            "s_waitcnt lgkmcnt(4)\n\t"
            "s_nop 0\n\t"
            "v_mul_f32 v144, v236, v144\n\t"
            "v_mfma_f32_16x16x32_bf16 v[168:171], v[196:199], v[68:71], v[168:171]\n\t"
            "v_mul_f32 v145, v237, v145\n\t"
            "s_waitcnt lgkmcnt(3)\n\t"
            "v_mul_f32 v146, v248, v146\n\t"
            "v_mfma_f32_16x16x32_bf16 v[172:175], v[196:199], v[92:95], v[172:175]\n\t"
            "v_mul_f32 v148, v236, v148\n\t"
            "v_mul_f32 v149, v237, v149\n\t"
            "v_mfma_f32_16x16x32_bf16 v[172:175], v[192:195], v[88:91], v[172:175]\n\t"
            "v_mul_f32 v147, v249, v147\n\t"
            "v_mul_f32 v150, v248, v150\n\t"
            "v_mfma_f32_16x16x32_bf16 v[152:155], v[192:195], v[112:115], v[152:155]\n\t"
            "v_mul_f32 v168, v236, v168\n\t"
            "v_mul_f32 v169, v237, v169\n\t"
            "v_mfma_f32_16x16x32_bf16 v[152:155], v[196:199], v[116:119], v[152:155]\n\t"
            "v_mul_f32 v151, v249, v151\n\t"
            "v_mul_f32 v170, v248, v170\n\t"
            "v_mfma_f32_16x16x32_bf16 v[156:159], v[196:199], v[140:143], v[156:159]\n\t"
            "ds_read_b128 v[196:199], v233 offset:12416\n\t"
            "v_mul_f32 v172, v236, v172\n\t"
            "v_mfma_f32_16x16x32_bf16 v[156:159], v[192:195], v[136:139], v[156:159]\n\t"
            "ds_read_b128 v[192:195], v232 offset:12416\n\t"
            "v_mul_f32 v173, v237, v173\n\t"
            "s_waitcnt lgkmcnt(3)\n\t"
            "v_mfma_f32_16x16x32_bf16 v[160:163], v[224:227], v[0:3], 0\n\t"
            "v_mul_f32 v152, v236, v152\n\t"
            "v_mul_f32 v153, v237, v153\n\t"
            "v_mfma_f32_16x16x32_bf16 v[160:163], v[228:231], v[4:7], v[160:163]\n\t"
            "v_mul_f32 v171, v249, v171\n\t"
            "v_mul_f32 v174, v248, v174\n\t"
            "v_mfma_f32_16x16x32_bf16 v[164:167], v[228:231], v[28:31], 0\n\t"
            "v_mul_f32 v156, v236, v156\n\t"
            "v_mul_f32 v157, v237, v157\n\t"
            "v_mfma_f32_16x16x32_bf16 v[164:167], v[224:227], v[24:27], v[164:167]\n\t"
            "ds_read_b64 v[236:237], v247 offset:128\n\t"
            "v_mul_f32 v175, v249, v175\n\t"
            "v_mfma_f32_16x16x32_bf16 v[176:179], v[224:227], v[48:51], 0\n\t"
            "v_mul_f32 v154, v248, v154\n\t"
            "v_mul_f32 v155, v249, v155\n\t"
            "v_mfma_f32_16x16x32_bf16 v[176:179], v[228:231], v[52:55], v[176:179]\n\t"
            "v_mul_f32 v158, v248, v158\n\t"
            "v_mul_f32 v159, v249, v159\n\t"
            "v_mfma_f32_16x16x32_bf16 v[180:183], v[228:231], v[76:79], 0\n\t"
            "ds_read_b64 v[248:249], v247 offset:136\n\t"
            "v_cvt_pk_bf16_f32 v202, v144, v145\n\t"
            "v_mfma_f32_16x16x32_bf16 v[180:183], v[224:227], v[72:75], v[180:183]\n\t"
            "s_waitcnt lgkmcnt(4)\n\t"
            "v_readfirstlane_b32 s77, v234\n\t"
            "v_readfirstlane_b32 s38, v235\n\t"
            "s_add_u32 s77, s77, s38\n\t"
            "s_cmp_ge_u32 s77, s44\n\t"
            "s_cbranch_scc0 .Lwc_spin%=\n\t"
            ".Lwc_go%=:\n\t"
            "v_mfma_f32_16x16x32_bf16 v[184:187], v[224:227], v[96:99], 0\n\t"
            "s_add_u32 s59, s59, 800\n\t"
            "v_cvt_pk_bf16_f32 v203, v146, v147\n\t"
            "v_cvt_pk_bf16_f32 v206, v148, v149\n\t"
            "v_mfma_f32_16x16x32_bf16 v[184:187], v[228:231], v[100:103], v[184:187]\n\t"
            "s_add_u32 s45, s46, s27\n\t"
            "s_mov_b32 s46, s47\n\t"
            "s_mov_b32 s47, s68\n\t"
            "v_mfma_f32_16x16x32_bf16 v[188:191], v[228:231], v[124:127], 0\n\t"
            "ds_read_b128 v[228:231], v233 offset:12544\n\t"
            "v_cvt_pk_bf16_f32 v207, v150, v151\n\t"
            "v_mfma_f32_16x16x32_bf16 v[188:191], v[224:227], v[120:123], v[188:191]\n\t"
            "ds_read_b128 v[224:227], v232 offset:12544\n\t"
            "v_cvt_pk_bf16_f32 v210, v168, v169\n\t"
            "s_waitcnt lgkmcnt(4)\n\t"
            "v_mfma_f32_16x16x32_bf16 v[160:163], v[192:195], v[8:11], v[160:163]\n\t"
            "v_cvt_pk_bf16_f32 v211, v170, v171\n\t"
            "v_cvt_pk_bf16_f32 v214, v172, v173\n\t"
            "v_mfma_f32_16x16x32_bf16 v[160:163], v[196:199], v[12:15], v[160:163]\n\t"
            "ds_read_b128 v[168:171], v254\n\t"
            "v_cvt_pk_bf16_f32 v215, v174, v175\n\t"
            "v_mfma_f32_16x16x32_bf16 v[164:167], v[196:199], v[36:39], v[164:167]\n\t"
            "ds_read_b128 v[172:175], v254 offset:64\n\t"
            "v_cvt_pk_bf16_f32 v218, v152, v153\n\t"
            "v_mfma_f32_16x16x32_bf16 v[164:167], v[192:195], v[32:35], v[164:167]\n\t"
            "v_cvt_pk_bf16_f32 v219, v154, v155\n\t"
            "v_cvt_pk_bf16_f32 v222, v156, v157\n\t"
            "v_mfma_f32_16x16x32_bf16 v[176:179], v[192:195], v[56:59], v[176:179]\n\t"
            "v_cvt_pk_bf16_f32 v223, v158, v159\n\t"
            "v_add_u32 v232, s67, v242\n\t"
            "v_mfma_f32_16x16x32_bf16 v[176:179], v[196:199], v[60:63], v[176:179]\n\t"
            "s_xor_b32 s48, s48, s71\n\t"
            "s_xor_b32 s67, s67, s71\n\t"
            "s_add_u32 s76, s76, 4\n\t"
            "s_add_u32 s44, s44, 8\n\t"
            "v_mfma_f32_16x16x32_bf16 v[180:183], v[196:199], v[84:87], v[180:183]\n\t"
            "s_lshl_b32 s56, s56, 2\n\t"
            "s_lshr_b32 s49, s26, 30\n\t"
            "s_or_b32 s56, s56, s49\n\t"
            "v_mfma_f32_16x16x32_bf16 v[180:183], v[192:195], v[80:83], v[180:183]\n\t"
            "ds_write_b128 v243, v[200:203]\n\t"
            "v_mfma_f32_16x16x32_bf16 v[184:187], v[192:195], v[104:107], v[184:187]\n\t"
            "v_xor_b32 v233, 64, v232\n\t"
            "v_mfma_f32_16x16x32_bf16 v[184:187], v[196:199], v[108:111], v[184:187]\n\t"
            "ds_write_b128 v243, v[204:207] offset:1536\n\t"
            "v_mfma_f32_16x16x32_bf16 v[188:191], v[196:199], v[132:135], v[188:191]\n\t"
            "ds_read_b128 v[196:199], v233\n\t"
            "v_mfma_f32_16x16x32_bf16 v[188:191], v[192:195], v[128:131], v[188:191]\n\t"
            "ds_read_b128 v[192:195], v232\n\t"
            "ds_write_b128 v243, v[208:211] offset:3072\n\t"
            "s_waitcnt lgkmcnt(7)\n\t"
            "v_mfma_f32_16x16x32_bf16 v[160:163], v[224:227], v[16:19], v[160:163]\n\t"
            "v_mfma_f32_16x16x32_bf16 v[160:163], v[228:231], v[20:23], v[160:163]\n\t"
            "ds_write_b128 v243, v[212:215] offset:4608\n\t"
            "v_mfma_f32_16x16x32_bf16 v[164:167], v[228:231], v[44:47], v[164:167]\n\t"
            "v_mfma_f32_16x16x32_bf16 v[164:167], v[224:227], v[40:43], v[164:167]\n\t"
            "ds_write_b128 v243, v[216:219] offset:6144\n\t"
            "v_mfma_f32_16x16x32_bf16 v[176:179], v[224:227], v[64:67], v[176:179]\n\t"
            "v_mfma_f32_16x16x32_bf16 v[176:179], v[228:231], v[68:71], v[176:179]\n\t"
            "ds_write_b128 v243, v[220:223] offset:7680\n\t"
            "v_mfma_f32_16x16x32_bf16 v[180:183], v[228:231], v[92:95], v[180:183]\n\t"
            "v_mul_f32 v160, v236, v160\n\t"
            "v_mul_f32 v161, v237, v161\n\t"
            "v_mfma_f32_16x16x32_bf16 v[180:183], v[224:227], v[88:91], v[180:183]\n\t"
            "v_mul_f32 v164, v236, v164\n\t"
            "v_mul_f32 v165, v237, v165\n\t"
            "v_mfma_f32_16x16x32_bf16 v[184:187], v[224:227], v[112:115], v[184:187]\n\t"
            "v_mul_f32 v176, v236, v176\n\t"
            "v_mul_f32 v177, v237, v177\n\t"
            "v_mfma_f32_16x16x32_bf16 v[184:187], v[228:231], v[116:119], v[184:187]\n\t"
            "v_mul_f32 v162, v248, v162\n\t"
            "v_mul_f32 v163, v249, v163\n\t"
            "v_mfma_f32_16x16x32_bf16 v[188:191], v[228:231], v[140:143], v[188:191]\n\t"
            "ds_read_b128 v[228:231], v233 offset:128\n\t"
            "v_mul_f32 v180, v236, v180\n\t"
            "v_mfma_f32_16x16x32_bf16 v[188:191], v[224:227], v[136:139], v[188:191]\n\t"
            "ds_read_b128 v[224:227], v232 offset:128\n\t"
            "v_mul_f32 v181, v237, v181\n\t"
            "s_waitcnt lgkmcnt(6)\n\t"
            "v_mfma_f32_16x16x32_bf16 v[200:203], v[192:195], v[0:3], 0\n\t"
            "v_mul_f32 v184, v236, v184\n\t"
            "v_mul_f32 v185, v237, v185\n\t"
            "v_mfma_f32_16x16x32_bf16 v[200:203], v[196:199], v[4:7], v[200:203]\n\t"
            "v_cvt_pk_bf16_f32 v160, v160, v161\n\t"
            "v_mul_f32 v166, v248, v166\n\t"
            "v_mfma_f32_16x16x32_bf16 v[204:207], v[196:199], v[28:31], 0\n\t"
            "v_mul_f32 v188, v236, v188\n\t"
            "v_mul_f32 v189, v237, v189\n\t"
            "v_mfma_f32_16x16x32_bf16 v[204:207], v[192:195], v[24:27], v[204:207]\n\t"
            "v_mul_f32 v167, v249, v167\n\t"
            "v_cvt_pk_bf16_f32 v164, v164, v165\n\t"
            "v_mfma_f32_16x16x32_bf16 v[208:211], v[192:195], v[48:51], 0\n\t"
            "v_mul_f32 v178, v248, v178\n\t"
            "v_mul_f32 v179, v249, v179\n\t"
            "v_mfma_f32_16x16x32_bf16 v[208:211], v[196:199], v[52:55], v[208:211]\n\t"
            "v_cvt_pk_bf16_f32 v176, v176, v177\n\t"
            "v_mul_f32 v182, v248, v182\n\t"
            "v_mfma_f32_16x16x32_bf16 v[212:215], v[196:199], v[76:79], 0\n\t"
            "v_mul_f32 v183, v249, v183\n\t"
            "v_cvt_pk_bf16_f32 v180, v180, v181\n\t"
            "v_mfma_f32_16x16x32_bf16 v[212:215], v[192:195], v[72:75], v[212:215]\n\t"
            "v_mul_f32 v186, v248, v186\n\t"
            "v_mul_f32 v187, v249, v187\n\t"
            "v_mfma_f32_16x16x32_bf16 v[216:219], v[192:195], v[96:99], 0\n\t"
            "v_cvt_pk_bf16_f32 v184, v184, v185\n\t"
            "v_mul_f32 v190, v248, v190\n\t"
            "v_mfma_f32_16x16x32_bf16 v[216:219], v[196:199], v[100:103], v[216:219]\n\t"
            "v_mul_f32 v191, v249, v191\n\t"
            "v_cvt_pk_bf16_f32 v188, v188, v189\n\t"
            "v_mfma_f32_16x16x32_bf16 v[220:223], v[196:199], v[124:127], 0\n\t"
            "v_cvt_pk_bf16_f32 v161, v162, v163\n\t"
            "v_cvt_pk_bf16_f32 v165, v166, v167\n\t"
            "v_mfma_f32_16x16x32_bf16 v[220:223], v[192:195], v[120:123], v[220:223]\n\t"
            "v_cvt_pk_bf16_f32 v177, v178, v179\n\t"
            "v_cvt_pk_bf16_f32 v181, v182, v183\n\t"
            "s_waitcnt lgkmcnt(0)\n\t"
            "v_mfma_f32_16x16x32_bf16 v[200:203], v[224:227], v[8:11], v[200:203]\n\t"
            "v_cvt_pk_bf16_f32 v185, v186, v187\n\t"
            "v_cvt_pk_bf16_f32 v189, v190, v191\n\t"
            "v_mfma_f32_16x16x32_bf16 v[200:203], v[228:231], v[12:15], v[200:203]\n\t"
            "ds_write_b64 v244, v[160:161]\n\t"
            "v_mfma_f32_16x16x32_bf16 v[204:207], v[228:231], v[36:39], v[204:207]\n\t"
            "v_mfma_f32_16x16x32_bf16 v[204:207], v[224:227], v[32:35], v[204:207]\n\t"
            "ds_write_b64 v244, v[164:165] offset:1536\n\t"
            "v_mfma_f32_16x16x32_bf16 v[208:211], v[224:227], v[56:59], v[208:211]\n\t"
            "v_mfma_f32_16x16x32_bf16 v[208:211], v[228:231], v[60:63], v[208:211]\n\t"
            "ds_write_b64 v244, v[176:177] offset:3072\n\t"
            "v_mfma_f32_16x16x32_bf16 v[212:215], v[228:231], v[84:87], v[212:215]\n\t"
            "v_mfma_f32_16x16x32_bf16 v[212:215], v[224:227], v[80:83], v[212:215]\n\t"
            "ds_write_b64 v244, v[180:181] offset:4608\n\t"
            "v_mfma_f32_16x16x32_bf16 v[216:219], v[224:227], v[104:107], v[216:219]\n\t"
            "v_mfma_f32_16x16x32_bf16 v[216:219], v[228:231], v[108:111], v[216:219]\n\t"
            "ds_write_b64 v244, v[184:185] offset:6144\n\t"
            "v_mfma_f32_16x16x32_bf16 v[220:223], v[228:231], v[132:135], v[220:223]\n\t"
            "ds_read_b128 v[228:231], v233 offset:256\n\t"
            "v_mfma_f32_16x16x32_bf16 v[220:223], v[224:227], v[128:131], v[220:223]\n\t"
            "ds_read_b128 v[224:227], v232 offset:256\n\t"
            "ds_write_b64 v244, v[188:189] offset:7680\n\t"
            "s_waitcnt lgkmcnt(0)\n\t"
            "s_barrier\n\t"
            "s_waitcnt vmcnt(0)\n\t"
            "ds_read_b64 v[144:145], v253\n\t"
            "ds_read_b64 v[146:147], v253 offset:96\n\t"
            "ds_read_b64 v[148:149], v253 offset:1744\n\t"
            "ds_read_b64 v[150:151], v253 offset:1840\n\t"
            "ds_read_b64 v[152:153], v253 offset:8\n\t"
            "ds_read_b64 v[154:155], v253 offset:104\n\t"
            "ds_read_b64 v[156:157], v253 offset:1752\n\t"
            "ds_read_b64 v[158:159], v253 offset:1848\n\t"
            "ds_read_b64 v[160:161], v253 offset:16\n\t"
            "ds_read_b64 v[162:163], v253 offset:112\n\t"
            "ds_read_b64 v[164:165], v253 offset:1760\n\t"
            "ds_read_b64 v[166:167], v253 offset:1856\n\t"
            "s_cmp_lt_u32 s46, 0x7000000\n\t"
            "s_cbranch_scc1 .Lwc_loop%=\n\t"
            "s_branch .Lwc_end%=\n\t"
            ".Lwc_spin%=:\n\t"
            "s_cmp_eq_u32 s83, 0\n\t"
            "s_cbranch_scc1 .Lwc_go%=\n\t"
            "s_sub_u32 s83, s83, 1\n\t"
            "s_sleep 2\n\t"
            "v_mov_b32 v234, 0\n\t"
            "ds_read_b64 v[234:235], v234 offset:7856\n\t"
            "s_waitcnt lgkmcnt(0)\n\t"
            "v_readfirstlane_b32 s77, v234\n\t"
            "v_readfirstlane_b32 s38, v235\n\t"
            "s_add_u32 s77, s77, s38\n\t"
            "s_cmp_ge_u32 s77, s44\n\t"
            "s_cbranch_scc0 .Lwc_spin%=\n\t"
            "s_branch .Lwc_go%=\n\t"
            ".Lwc_end%=:\n\t"
            "s_waitcnt lgkmcnt(0)\n\t"
            "s_nop 7\n\t"
            "s_barrier\n\t"
            : "+{v232}"(q0), "+{v233}"(q1), "+{v234}"(q2), "+{s44}"(s44), B_SIO
            : "{v242}"(pE_), "{v243}"(aVAw_), "{v244}"(aVBw_), "{v245}"(pST_), "{v246}"(aRSw_), "{v247}"(aRSr_), "{v250}"(pXw_), "{v251}"(pSTw_), "{v252}"(aCA_), "{v253}"(aB_), "{v254}"(aA_), "{v255}"(idx_), B_SIN, "{s[80:81]}"(mrow), "{s82}"(touch)
            : "v144", "v145", "v146", "v147", "v148", "v149", "v150", "v151", "v152", "v153", "v154", "v155", "v156", "v157", "v158", "v159", "v160", "v161", "v162", "v163", "v164", "v165", "v166", "v167", "v168", "v169", "v170", "v171", "v172", "v173", "v174", "v175", "v176", "v177", "v178", "v179", "v180", "v181", "v182", "v183", "v184", "v185", "v186", "v187", "v188", "v189", "v190", "v191", "v192", "v193", "v194", "v195", "v196", "v197", "v198", "v199", "v236", "v237", "v248", "v249", "v0", "v1", "v2", "v3", "v4", "v5", "v6", "v7", "v8", "v9", "v10", "v11", "v12", "v13", "v14", "v15", "v16", "v17", "v18", "v19", "v20", "v21", "v22", "v23", "v24", "v25", "v26", "v27", "v28", "v29", "v30", "v31", "v32", "v33", "v34", "v35", "v36", "v37", "v38", "v39", "v40", "v41", "v42", "v43", "v44", "v45", "v46", "v47", "v48", "v49", "v50", "v51", "v52", "v53", "v54", "v55", "v56", "v57", "v58", "v59", "v60", "v61", "v62", "v63", "v64", "v65", "v66", "v67", "v68", "v69", "v70", "v71", "v72", "v73", "v74", "v75", "v76", "v77", "v78", "v79", "v80", "v81", "v82", "v83", "v84", "v85", "v86", "v87", "v88", "v89", "v90", "v91", "v92", "v93", "v94", "v95", "v96", "v97", "v98", "v99", "v100", "v101", "v102", "v103", "v104", "v105", "v106", "v107", "v108", "v109", "v110", "v111", "v112", "v113", "v114", "v115", "v116", "v117", "v118", "v119", "v120", "v121", "v122", "v123", "v124", "v125", "v126", "v127", "v128", "v129", "v130", "v131", "v132", "v133", "v134", "v135", "v136", "v137", "v138", "v139", "v140", "v141", "v142", "v143", "v235", "v200", "v201", "v202", "v203", "v204", "v205", "v206", "v207", "v208", "v209", "v210", "v211", "v212", "v213", "v214", "v215", "v216", "v217", "v218", "v219", "v220", "v221", "v222", "v223", "v224", "v225", "v226", "v227", "v228", "v229", "v230", "v231", B_SCLOB);
        }
        __builtin_amdgcn_endpgm();
    } else {
        LANE_SETUP
        unsigned t45, t56;
        u32x4 xl[9];
        {
            const unsigned oE_ = ldsb + fE + (O_OFF - X_OFF), oO_ = oE_ ^ 64;
            unsigned l16_ = lane * 16;
            unsigned pr[3];
#pragma unroll
            for (int k = 0; k < 3; ++k) {
                const int c8 = 12 * (w - 2) + 4 * k + g;
                const int ws = c8 / 6, r6 = c8 - 6 * ws;
                pr[k] = l15 * 384 + (r6 >> 1) * 128 + (((2 * ws + (r6 & 1)) ^ sw) << 4);
            }
            const unsigned pR0_ = pr[0], pR1_ = pr[1], pR2_ = pr[2];
            B_SCALARS
            asm volatile(
            "s_waitcnt lgkmcnt(0)\n\t"
            "s_mov_b32 s83, 0x10000\n\t"
            "s_nop 7\n\t"
            "s_barrier\n\t"
            "s_sleep 16\n\t"
            "ds_read_b32 v249, v252 offset:12\n\t"
            "s_waitcnt lgkmcnt(0)\n\t"
            "v_and_b32 v249, s69, v249\n\t"
            "s_mov_b32 exec_hi, 0x3fffff\n\t"
            "s_add_u32 m0, s66, 0\n\t"
            "s_add_u32 s49, s79, 0\n\t"
            "buffer_load_dwordx4 v249, s[72:75], s49 offen lds\n\t"
            "s_mov_b32 exec_hi, -1\n\t"
            "s_mov_b32 exec_hi, 0x3fffff\n\t"
            "s_add_u32 m0, s66, 872\n\t"
            "s_add_u32 s49, s47, 196224\n\t"
            "buffer_load_dwordx4 v249, s[72:75], s49 offen lds\n\t"
            "s_mov_b32 exec_hi, -1\n\t"
            "s_mov_b32 exec_hi, 0x3fffff\n\t"
            "s_add_u32 m0, s66, 1744\n\t"
            "s_add_u32 s49, s47, 392448\n\t"
            "buffer_load_dwordx4 v249, s[72:75], s49 offen lds\n\t"
            "s_mov_b32 exec_hi, -1\n\t"
            "s_mov_b32 exec_hi, 0x3fffff\n\t"
            "s_add_u32 m0, s66, 2616\n\t"
            "s_add_u32 s49, s47, 588672\n\t"
            "buffer_load_dwordx4 v249, s[72:75], s49 offen lds\n\t"
            "s_mov_b32 exec_hi, -1\n\t"
            "s_mov_b32 exec_hi, 0x3fffff\n\t"
            "s_add_u32 m0, s66, 3488\n\t"
            "s_add_u32 s49, s47, 784896\n\t"
            "buffer_load_dwordx4 v249, s[72:75], s49 offen lds\n\t"
            "s_mov_b32 exec_hi, -1\n\t"
            "s_mov_b32 exec_hi, 0x3fffff\n\t"
            "s_add_u32 m0, s66, 4360\n\t"
            "s_add_u32 s49, s47, 981120\n\t"
            "buffer_load_dwordx4 v249, s[72:75], s49 offen lds\n\t"
            "s_mov_b32 exec_hi, -1\n\t"
            "s_mov_b32 exec_hi, 0x3fffff\n\t"
            "s_add_u32 m0, s66, 5232\n\t"
            "s_add_u32 s49, s47, 1177344\n\t"
            "buffer_load_dwordx4 v249, s[72:75], s49 offen lds\n\t"
            "s_mov_b32 exec_hi, -1\n\t"
            "s_mov_b32 exec_hi, 0x3fffff\n\t"
            "s_add_u32 m0, s66, 6104\n\t"
            "s_add_u32 s49, s47, 1373568\n\t"
            "buffer_load_dwordx4 v249, s[72:75], s49 offen lds\n\t"
            "s_mov_b32 exec_hi, -1\n\t"
            "s_mov_b32 exec_hi, 0x3fffff\n\t"
            "s_add_u32 m0, s66, 6976\n\t"
            "s_add_u32 s49, s47, 1569792\n\t"
            "buffer_load_dwordx4 v249, s[72:75], s49 offen lds\n\t"
            "s_mov_b32 exec_hi, -1\n\t"
            "s_setprio 1\n\t"
            "ds_read_b128 v[168:171], v254\n\t"
            "ds_read_b128 v[172:175], v254 offset:64\n\t"
            "s_waitcnt vmcnt(0)\n\t"
            "ds_read_b64 v[144:145], v253\n\t"
            "ds_read_b64 v[146:147], v253 offset:96\n\t"
            "ds_read_b64 v[148:149], v253 offset:1744\n\t"
            "ds_read_b64 v[150:151], v253 offset:1840\n\t"
            "ds_read_b64 v[152:153], v253 offset:8\n\t"
            "ds_read_b64 v[154:155], v253 offset:104\n\t"
            "ds_read_b64 v[156:157], v253 offset:1752\n\t"
            "ds_read_b64 v[158:159], v253 offset:1848\n\t"
            "ds_read_b64 v[160:161], v253 offset:16\n\t"
            "ds_read_b64 v[162:163], v253 offset:112\n\t"
            "ds_read_b64 v[164:165], v253 offset:1760\n\t"
            "ds_read_b64 v[166:167], v253 offset:1856\n\t"
            "v_mov_b64 v[176:177], 0\n\t"
            "v_mov_b64 v[178:179], 0\n\t"
            "v_mov_b64 v[180:181], 0\n\t"
            "v_mov_b64 v[182:183], 0\n\t"
            "v_mov_b32 v249, s76\n\t"
            "v_mov_b64 v[184:185], 0\n\t"
            "v_mov_b64 v[186:187], 0\n\t"
            "v_mov_b64 v[188:189], 0\n\t"
            "v_mov_b64 v[190:191], 0\n\t"
            "v_add_u32 v236, s67, v250\n\t"
            "v_add_u32 v248, s67, v251\n\t"
            "s_nop 0\n\t"
            "ds_read_b32 v249, v249\n\t"
            "s_waitcnt lgkmcnt(9)\n\t"
            "v_smfmac_f32_16x16x64_bf16 v[176:179], v[168:171], v[144:151], v255\n\t"
            "ds_read_b128 v[168:171], v254 offset:128\n\t"
            "s_waitcnt lgkmcnt(6)\n\t"
            "v_smfmac_f32_16x16x64_bf16 v[180:183], v[172:175], v[152:159], v255\n\t"
            "ds_read_b64 v[144:145], v253 offset:24\n\t"
            "ds_read_b64 v[146:147], v253 offset:120\n\t"
            "ds_read_b64 v[148:149], v253 offset:1768\n\t"
            "ds_read_b64 v[150:151], v253 offset:1864\n\t"
            "ds_read_b128 v[172:175], v254 offset:192\n\t"
            "ds_read_b64 v[152:153], v253 offset:32\n\t"
            "ds_read_b64 v[154:155], v253 offset:128\n\t"
            "ds_read_b64 v[156:157], v253 offset:1776\n\t"
            "ds_read_b64 v[158:159], v253 offset:1872\n\t"
            "v_cvt_pk_bf16_f32 v176, v176, v177\n\t"
            "v_cvt_pk_bf16_f32 v177, v178, v179\n\t"
            "v_cvt_pk_bf16_f32 v178, v180, v181\n\t"
            "v_cvt_pk_bf16_f32 v179, v182, v183\n\t"
            "v_mfma_f32_4x4x4_16b_bf16 v[196:199], v[176:177], v[176:177], 0\n\t"
            "v_mov_b64 v[180:181], 0\n\t"
            "v_mfma_f32_16x16x32_bf16 v[192:195], v[238:241], v[176:179], 0\n\t"
            "s_waitcnt lgkmcnt(10)\n\t"
            "v_readfirstlane_b32 s26, v249\n\t"
            "v_mov_b64 v[182:183], 0\n\t"
            "v_mfma_f32_4x4x4_16b_bf16 v[196:199], v[178:179], v[178:179], v[196:199]\n\t"
            "ds_read_b32 v249, v252 offset:12\n\t"
            "s_and_b32 s68, s26, 0x7ffffff\n\t"
            "s_add_u32 s68, s68, s78\n\t"
            "s_and_b32 s69, s26, 0x38000000\n\t"
            "s_or_b32 s69, s69, s39\n\t"
            "s_bitcmp1_b32 s26, 27\n\t"
            "s_cselect_b32 s79, 2.0, s68\n\t"
            "s_and_b32 s57, s56, 3\n\t"
            "s_lshl_b32 s58, s57, 3\n\t"
            "s_lshr_b32 m0, s54, s58\n\t"
            "s_lshl_b32 s58, s57, 4\n\t"
            "s_lshr_b64 s[36:37], s[50:51], s58\n\t"
            "s_and_b32 s57, s36, 0xffff\n\t"
            "s_add_u32 s57, s57, s67\n\t"
            "v_writelane_b32 v236, s57, 63\n\t"
            "v_writelane_b32 v236, s59, m0\n\t"
            "v_xor_b32 v237, 16, v236\n\t"
            "s_lshr_b64 s[36:37], s[52:53], s58\n\t"
            "s_and_b32 s58, s36, 0xffff\n\t"
            "s_add_u32 s58, s58, s67\n\t"
            "ds_write_b128 v236, v[176:179]\n\t"
            "s_waitcnt lgkmcnt(11)\n\t"
            "v_smfmac_f32_16x16x64_bf16 v[184:187], v[168:171], v[160:167], v255\n\t"
            "s_and_b32 s38, s52, 0xffff\n\t"
            "s_add_u32 s38, s38, s67\n\t"
            "ds_read_b128 v[168:171], v254 offset:256\n\t"
            "s_waitcnt lgkmcnt(7)\n\t"
            "v_smfmac_f32_16x16x64_bf16 v[188:191], v[172:175], v[144:151], v255\n\t"
            "ds_read_b64 v[160:161], v253 offset:40\n\t"
            "ds_read_b64 v[162:163], v253 offset:136\n\t"
            "ds_read_b64 v[164:165], v253 offset:1784\n\t"
            "ds_read_b64 v[166:167], v253 offset:1880\n\t"
            "ds_read_b128 v[172:175], v254 offset:320\n\t"
            "ds_read_b64 v[144:145], v253 offset:48\n\t"
            "ds_read_b64 v[146:147], v253 offset:144\n\t"
            "ds_read_b64 v[148:149], v253 offset:1792\n\t"
            "s_waitcnt lgkmcnt(14)\n\t"
            "ds_read_b64 v[150:151], v253 offset:1888\n\t"
            "v_mov_b64 v[176:177], 0\n\t"
            "v_mov_b64 v[178:179], 0\n\t"
            "v_cvt_pk_bf16_f32 v184, v184, v185\n\t"
            "v_cvt_pk_bf16_f32 v185, v186, v187\n\t"
            "v_cvt_pk_bf16_f32 v186, v188, v189\n\t"
            "v_cvt_pk_bf16_f32 v187, v190, v191\n\t"
            "v_mfma_f32_4x4x4_16b_bf16 v[196:199], v[184:185], v[184:185], v[196:199]\n\t"
            "s_waitcnt lgkmcnt(14)\n\t"
            "ds_write_b128 v237, v[184:187]\n\t"
            "v_mfma_f32_16x16x32_bf16 v[192:195], v[238:241], v[184:187], v[192:195]\n\t"
            "v_mov_b64 v[184:185], 0\n\t"
            "v_mov_b64 v[188:189], 0\n\t"
            "v_mfma_f32_4x4x4_16b_bf16 v[196:199], v[186:187], v[186:187], v[196:199]\n\t"
            "v_mov_b64 v[186:187], 0\n\t"
            "v_mov_b64 v[190:191], 0\n\t"
            "v_writelane_b32 v248, s58, 63\n\t"
            "v_writelane_b32 v248, s38, m0\n\t"
            "s_waitcnt lgkmcnt(12)\n\t"
            "v_and_b32 v249, s69, v249\n\t"
            "s_waitcnt lgkmcnt(10)\n\t"
            "v_smfmac_f32_16x16x64_bf16 v[176:179], v[168:171], v[152:159], v255\n\t"
            "ds_read_b128 v[168:171], v254 offset:384\n\t"
            "ds_read_b64 v[152:153], v253 offset:56\n\t"
            "ds_read_b64 v[154:155], v253 offset:152\n\t"
            "ds_read_b64 v[156:157], v253 offset:1800\n\t"
            "ds_read_b64 v[158:159], v253 offset:1896\n\t"
            "s_waitcnt lgkmcnt(10)\n\t"
            "v_smfmac_f32_16x16x64_bf16 v[180:183], v[172:175], v[160:167], v255\n\t"
            "ds_read_b128 v[172:175], v254 offset:448\n\t"
            "ds_read_b64 v[160:161], v253 offset:64\n\t"
            "ds_read_b64 v[162:163], v253 offset:160\n\t"
            "ds_read_b64 v[164:165], v253 offset:1808\n\t"
            "ds_read_b64 v[166:167], v253 offset:1904\n\t"
            "v_cvt_pk_bf16_f32 v176, v176, v177\n\t"
            "v_cvt_pk_bf16_f32 v177, v178, v179\n\t"
            "s_nop 0\n\t"
            "v_cvt_pk_bf16_f32 v178, v180, v181\n\t"
            "v_cvt_pk_bf16_f32 v179, v182, v183\n\t"
            "v_mfma_f32_4x4x4_16b_bf16 v[196:199], v[176:177], v[176:177], v[196:199]\n\t"
            "s_waitcnt lgkmcnt(14)\n\t"
            "ds_write_b128 v236, v[176:179] offset:128\n\t"
            "v_mfma_f32_16x16x32_bf16 v[192:195], v[238:241], v[176:179], v[192:195]\n\t"
            "v_mov_b64 v[176:177], 0\n\t"
            "v_mov_b64 v[180:181], 0\n\t"
            "v_mfma_f32_4x4x4_16b_bf16 v[196:199], v[178:179], v[178:179], v[196:199]\n\t"
            "v_mov_b64 v[178:179], 0\n\t"
            "v_mov_b64 v[182:183], 0\n\t"
            "s_waitcnt lgkmcnt(10)\n\t"
            "v_smfmac_f32_16x16x64_bf16 v[184:187], v[168:171], v[144:151], v255\n\t"
            "ds_read_b128 v[168:171], v254 offset:512\n\t"
            "ds_read_b64 v[144:145], v253 offset:72\n\t"
            "ds_read_b64 v[146:147], v253 offset:168\n\t"
            "ds_read_b64 v[148:149], v253 offset:1816\n\t"
            "ds_read_b64 v[150:151], v253 offset:1912\n\t"
            "s_waitcnt lgkmcnt(10)\n\t"
            "v_smfmac_f32_16x16x64_bf16 v[188:191], v[172:175], v[152:159], v255\n\t"
            "ds_read_b128 v[172:175], v254 offset:576\n\t"
            "ds_read_b64 v[152:153], v253 offset:80\n\t"
            "ds_read_b64 v[154:155], v253 offset:176\n\t"
            "ds_read_b64 v[156:157], v253 offset:1824\n\t"
            "ds_read_b64 v[158:159], v253 offset:1920\n\t"
            "v_cvt_pk_bf16_f32 v184, v184, v185\n\t"
            "v_cvt_pk_bf16_f32 v185, v186, v187\n\t"
            "s_nop 0\n\t"
            "v_cvt_pk_bf16_f32 v186, v188, v189\n\t"
            "v_cvt_pk_bf16_f32 v187, v190, v191\n\t"
            "v_mfma_f32_4x4x4_16b_bf16 v[196:199], v[184:185], v[184:185], v[196:199]\n\t"
            "s_waitcnt lgkmcnt(14)\n\t"
            "ds_write_b128 v237, v[184:187] offset:128\n\t"
            "v_mfma_f32_16x16x32_bf16 v[192:195], v[238:241], v[184:187], v[192:195]\n\t"
            "v_mov_b64 v[184:185], 0\n\t"
            "v_mov_b64 v[188:189], 0\n\t"
            "v_mfma_f32_4x4x4_16b_bf16 v[196:199], v[186:187], v[186:187], v[196:199]\n\t"
            "v_mov_b64 v[186:187], 0\n\t"
            "v_mov_b64 v[190:191], 0\n\t"
            "s_waitcnt lgkmcnt(10)\n\t"
            "v_smfmac_f32_16x16x64_bf16 v[176:179], v[168:171], v[160:167], v255\n\t"
            "ds_read_b128 v[168:171], v254 offset:640\n\t"
            "ds_read_b64 v[160:161], v253 offset:88\n\t"
            "ds_read_b64 v[162:163], v253 offset:184\n\t"
            "ds_read_b64 v[164:165], v253 offset:1832\n\t"
            "ds_read_b64 v[166:167], v253 offset:1928\n\t"
            "s_waitcnt lgkmcnt(10)\n\t"
            "v_smfmac_f32_16x16x64_bf16 v[180:183], v[172:175], v[144:151], v255\n\t"
            "ds_read_b128 v[172:175], v254 offset:704\n\t"
            "v_cvt_pk_bf16_f32 v176, v176, v177\n\t"
            "v_cvt_pk_bf16_f32 v177, v178, v179\n\t"
            "s_nop 1\n\t"
            "v_mfma_f32_4x4x4_16b_bf16 v[196:199], v[176:177], v[176:177], v[196:199]\n\t"
            "s_nop 1\n\t"
            "v_cvt_pk_bf16_f32 v178, v180, v181\n\t"
            "v_cvt_pk_bf16_f32 v179, v182, v183\n\t"
            "s_nop 1\n\t"
            "v_mfma_f32_4x4x4_16b_bf16 v[196:199], v[178:179], v[178:179], v[196:199]\n\t"
            "ds_write_b128 v236, v[176:179] offset:256\n\t"
            "v_mfma_f32_16x16x32_bf16 v[192:195], v[238:241], v[176:179], v[192:195]\n\t"
            "v_add_u32 v236, s48, v244\n\t"
            "ds_read_b128 v[200:203], v236\n\t"
            "ds_read_b128 v[212:215], v236 offset:6144\n\t"
            "ds_read_b128 v[224:227], v236 offset:12288\n\t"
            "s_waitcnt lgkmcnt(9)\n\t"
            "v_smfmac_f32_16x16x64_bf16 v[184:187], v[168:171], v[152:159], v255\n\t"
            "s_waitcnt lgkmcnt(4)\n\t"
            "v_smfmac_f32_16x16x64_bf16 v[188:191], v[172:175], v[160:167], v255\n\t"
            "s_mov_b32 exec_hi, 0x3fffff\n\t"
            "s_add_u32 m0, s66, 0\n\t"
            "s_add_u32 s49, s79, 0\n\t"
            "buffer_load_dwordx4 v249, s[72:75], s49 offen lds\n\t"
            "s_mov_b32 exec_hi, -1\n\t"
            "s_mov_b32 exec_hi, 0x3fffff\n\t"
            "s_add_u32 m0, s66, 872\n\t"
            "s_add_u32 s49, s68, 196224\n\t"
            "buffer_load_dwordx4 v249, s[72:75], s49 offen lds\n\t"
            "s_mov_b32 exec_hi, -1\n\t"
            "v_cvt_pk_bf16_f32 v184, v184, v185\n\t"
            "v_cvt_pk_bf16_f32 v185, v186, v187\n\t"
            "v_cvt_pk_bf16_f32 v186, v188, v189\n\t"
            "v_cvt_pk_bf16_f32 v187, v190, v191\n\t"
            "v_mfma_f32_4x4x4_16b_bf16 v[196:199], v[184:185], v[184:185], v[196:199]\n\t"
            "ds_write_b128 v237, v[184:187] offset:256\n\t"
            "v_mfma_f32_16x16x32_bf16 v[192:195], v[238:241], v[184:187], v[192:195]\n\t"
            "v_add_u32 v237, s48, v245\n\t"
            "v_mfma_f32_4x4x4_16b_bf16 v[196:199], v[186:187], v[186:187], v[196:199]\n\t"
            "ds_read_b128 v[204:207], v237\n\t"
            "ds_read_b128 v[216:219], v237 offset:6144\n\t"
            "ds_read_b128 v[228:231], v237 offset:12288\n\t"
            "s_mov_b32 exec_hi, 0x3fffff\n\t"
            "s_add_u32 m0, s66, 1744\n\t"
            "s_add_u32 s49, s68, 392448\n\t"
            "buffer_load_dwordx4 v249, s[72:75], s49 offen lds\n\t"
            "s_mov_b32 exec_hi, -1\n\t"
            "v_cndmask_b32_e64 v193, v196, v197, s[60:61]\n\t"
            "v_cndmask_b32_e64 v193, v193, v198, s[62:63]\n\t"
            "v_cndmask_b32_e64 v193, v193, v199, s[64:65]\n\t"
            "ds_write_b64 v248, v[192:193] offset:18816\n\t"
            "s_mov_b64 exec, 1\n\t"
            "ds_inc_u32 v255, v238 offset:7860\n\t"
            "s_mov_b64 exec, -1\n\t"
            "v_add_u32 v248, s48, v246\n\t"
            "ds_read_b128 v[208:211], v248\n\t"
            "ds_read_b128 v[220:223], v248 offset:6144\n\t"
            "ds_read_b128 v[232:235], v248 offset:12288\n\t"
            "s_mov_b32 exec_hi, 0x3fffff\n\t"
            "s_add_u32 m0, s66, 2616\n\t"
            "s_add_u32 s49, s68, 588672\n\t"
            "buffer_load_dwordx4 v249, s[72:75], s49 offen lds\n\t"
            "s_mov_b32 exec_hi, -1\n\t"
            "s_mov_b32 exec_hi, 0x3fffff\n\t"
            "s_add_u32 m0, s66, 3488\n\t"
            "s_add_u32 s49, s68, 784896\n\t"
            "buffer_load_dwordx4 v249, s[72:75], s49 offen lds\n\t"
            "s_mov_b32 exec_hi, -1\n\t"
            "s_waitcnt lgkmcnt(0)\n\t"
            "s_barrier\n\t"
            "s_setprio 3\n\t"
            "s_mov_b32 exec_hi, 0x3fffff\n\t"
            "s_add_u32 m0, s66, 4360\n\t"
            "s_add_u32 s49, s68, 981120\n\t"
            "buffer_load_dwordx4 v249, s[72:75], s49 offen lds\n\t"
            "s_mov_b32 exec_hi, -1\n\t"
            "s_mov_b32 exec_hi, 0x3fffff\n\t"
            "s_add_u32 m0, s66, 5232\n\t"
            "s_add_u32 s49, s68, 1177344\n\t"
            "buffer_load_dwordx4 v249, s[72:75], s49 offen lds\n\t"
            "s_mov_b32 exec_hi, -1\n\t"
            "s_mov_b32 exec_hi, 0x3fffff\n\t"
            "s_add_u32 m0, s66, 6104\n\t"
            "s_add_u32 s49, s68, 1373568\n\t"
            "buffer_load_dwordx4 v249, s[72:75], s49 offen lds\n\t"
            "s_mov_b32 exec_hi, -1\n\t"
            "s_mov_b32 exec_hi, 0x3fffff\n\t"
            "s_add_u32 m0, s66, 6976\n\t"
            "s_add_u32 s49, s68, 1569792\n\t"
            "buffer_load_dwordx4 v249, s[72:75], s49 offen lds\n\t"
            "s_mov_b32 exec_hi, -1\n\t"
            "s_add_u32 s49, s33, 0\n\t"
            "buffer_load_dwordx4 v[0:3], v247, s[28:31], s49 offen sc1\n\t"
            "s_add_u32 s49, s33, 1024\n\t"
            "buffer_load_dwordx4 v[4:7], v247, s[28:31], s49 offen sc1\n\t"
            "s_add_u32 s49, s33, 2048\n\t"
            "buffer_load_dwordx4 v[8:11], v247, s[28:31], s49 offen sc1\n\t"
            "s_add_u32 s49, s33, 3072\n\t"
            "buffer_load_dwordx4 v[12:15], v247, s[28:31], s49 offen sc1\n\t"
            "s_add_u32 s49, s33, 4096\n\t"
            "buffer_load_dwordx4 v[16:19], v247, s[28:31], s49 offen sc1\n\t"
            "s_add_u32 s49, s33, 5120\n\t"
            "buffer_load_dwordx4 v[20:23], v247, s[28:31], s49 offen sc1\n\t"
            "s_add_u32 s49, s33, 6144\n\t"
            "buffer_load_dwordx4 v[24:27], v247, s[28:31], s49 offen sc1\n\t"
            "s_add_u32 s49, s33, 7168\n\t"
            "buffer_load_dwordx4 v[28:31], v247, s[28:31], s49 offen sc1\n\t"
            "s_add_u32 s49, s33, 8192\n\t"
            "buffer_load_dwordx4 v[32:35], v247, s[28:31], s49 offen sc1\n\t"
            "s_add_u32 s49, s33, 9216\n\t"
            "buffer_load_dwordx4 v[36:39], v247, s[28:31], s49 offen sc1\n\t"
            "s_add_u32 s49, s33, 10240\n\t"
            "buffer_load_dwordx4 v[40:43], v247, s[28:31], s49 offen sc1\n\t"
            "s_add_u32 s49, s33, 11264\n\t"
            "buffer_load_dwordx4 v[44:47], v247, s[28:31], s49 offen sc1\n\t"
            "s_add_u32 s49, s33, 12288\n\t"
            "buffer_load_dwordx4 v[48:51], v247, s[28:31], s49 offen sc1\n\t"
            "s_add_u32 s49, s33, 13312\n\t"
            "buffer_load_dwordx4 v[52:55], v247, s[28:31], s49 offen sc1\n\t"
            "s_add_u32 s49, s33, 14336\n\t"
            "buffer_load_dwordx4 v[56:59], v247, s[28:31], s49 offen sc1\n\t"
            "s_add_u32 s49, s33, 15360\n\t"
            "buffer_load_dwordx4 v[60:63], v247, s[28:31], s49 offen sc1\n\t"
            "s_add_u32 s49, s33, 16384\n\t"
            "buffer_load_dwordx4 v[64:67], v247, s[28:31], s49 offen sc1\n\t"
            "s_add_u32 s49, s33, 17408\n\t"
            "buffer_load_dwordx4 v[68:71], v247, s[28:31], s49 offen sc1\n\t"
            "s_add_u32 s49, s33, 18432\n\t"
            "buffer_load_dwordx4 v[72:75], v247, s[28:31], s49 offen sc1\n\t"
            "s_add_u32 s49, s33, 19456\n\t"
            "buffer_load_dwordx4 v[76:79], v247, s[28:31], s49 offen sc1\n\t"
            "s_add_u32 s49, s33, 20480\n\t"
            "buffer_load_dwordx4 v[80:83], v247, s[28:31], s49 offen sc1\n\t"
            "s_add_u32 s49, s33, 21504\n\t"
            "buffer_load_dwordx4 v[84:87], v247, s[28:31], s49 offen sc1\n\t"
            "s_add_u32 s49, s33, 22528\n\t"
            "buffer_load_dwordx4 v[88:91], v247, s[28:31], s49 offen sc1\n\t"
            "s_add_u32 s49, s33, 23552\n\t"
            "buffer_load_dwordx4 v[92:95], v247, s[28:31], s49 offen sc1\n\t"
            "s_add_u32 s49, s33, 24576\n\t"
            "buffer_load_dwordx4 v[96:99], v247, s[28:31], s49 offen sc1\n\t"
            "s_add_u32 s49, s33, 25600\n\t"
            "buffer_load_dwordx4 v[100:103], v247, s[28:31], s49 offen sc1\n\t"
            "s_add_u32 s49, s33, 26624\n\t"
            "buffer_load_dwordx4 v[104:107], v247, s[28:31], s49 offen sc1\n\t"
            "s_add_u32 s49, s33, 27648\n\t"
            "buffer_load_dwordx4 v[108:111], v247, s[28:31], s49 offen sc1\n\t"
            "s_add_u32 s49, s33, 28672\n\t"
            "buffer_load_dwordx4 v[112:115], v247, s[28:31], s49 offen sc1\n\t"
            "s_add_u32 s49, s33, 29696\n\t"
            "buffer_load_dwordx4 v[116:119], v247, s[28:31], s49 offen sc1\n\t"
            "s_add_u32 s49, s33, 30720\n\t"
            "buffer_load_dwordx4 v[120:123], v247, s[28:31], s49 offen sc1\n\t"
            "s_add_u32 s49, s33, 31744\n\t"
            "buffer_load_dwordx4 v[124:127], v247, s[28:31], s49 offen sc1\n\t"
            "s_add_u32 s49, s33, 32768\n\t"
            "buffer_load_dwordx4 v[128:131], v247, s[28:31], s49 offen sc1\n\t"
            "s_add_u32 s49, s33, 33792\n\t"
            "buffer_load_dwordx4 v[132:135], v247, s[28:31], s49 offen sc1\n\t"
            "s_add_u32 s49, s33, 34816\n\t"
            "buffer_load_dwordx4 v[136:139], v247, s[28:31], s49 offen sc1\n\t"
            "s_add_u32 s49, s33, 35840\n\t"
            "buffer_load_dwordx4 v[140:143], v247, s[28:31], s49 offen sc1\n\t"
            "s_add_u32 s59, s59, -47104\n\t"
            "s_add_u32 s45, s46, s27\n\t"
            "s_mov_b32 s46, s47\n\t"
            "s_mov_b32 s47, s68\n\t"
            "s_xor_b32 s48, s48, s71\n\t"
            "s_xor_b32 s67, s67, s71\n\t"
            "s_add_u32 s76, s76, 4\n\t"
            "s_add_u32 s70, s70, 4\n\t"
            "s_lshl_b32 s56, s56, 2\n\t"
            "s_lshr_b32 s49, s26, 30\n\t"
            "s_or_b32 s56, s56, s49\n\t"
            "s_waitcnt vmcnt(0)\n\t"
            "s_nop 7\n\t"
            ".p2align 6\n\t"
            "s_nop 0\n\t"
            "s_nop 0\n\t"
            "s_nop 0\n\t"
            "s_nop 0\n\t"
            "s_nop 0\n\t"
            "s_nop 0\n\t"
            "s_nop 0\n\t"
            ".Lwb_loop%=:\n\t"
            "s_setprio 1\n\t"
            "ds_read_b128 v[168:171], v254\n\t"
            "ds_read_b128 v[172:175], v254 offset:64\n\t"
            "s_waitcnt vmcnt(9)\n\t"
            "ds_read_b64 v[144:145], v253\n\t"
            "ds_read_b64 v[146:147], v253 offset:96\n\t"
            "ds_read_b64 v[148:149], v253 offset:1744\n\t"
            "ds_read_b64 v[150:151], v253 offset:1840\n\t"
            "ds_read_b64 v[152:153], v253 offset:8\n\t"
            "ds_read_b64 v[154:155], v253 offset:104\n\t"
            "ds_read_b64 v[156:157], v253 offset:1752\n\t"
            "ds_read_b64 v[158:159], v253 offset:1848\n\t"
            "ds_read_b64 v[160:161], v253 offset:16\n\t"
            "ds_read_b64 v[162:163], v253 offset:112\n\t"
            "ds_read_b64 v[164:165], v253 offset:1760\n\t"
            "ds_read_b64 v[166:167], v253 offset:1856\n\t"
            "v_mov_b64 v[176:177], 0\n\t"
            "v_mov_b64 v[178:179], 0\n\t"
            "v_mov_b64 v[180:181], 0\n\t"
            "v_mov_b64 v[182:183], 0\n\t"
            "v_mov_b32 v249, s76\n\t"
            "v_mov_b64 v[184:185], 0\n\t"
            "v_mov_b64 v[186:187], 0\n\t"
            "v_mov_b64 v[188:189], 0\n\t"
            "v_mov_b64 v[190:191], 0\n\t"
            "v_add_u32 v236, s67, v250\n\t"
            "v_add_u32 v248, s67, v251\n\t"
            "v_mov_b32 v247, 0\n\t"
            "ds_read_b32 v249, v249\n\t"
            "s_setprio 3\n\t"
            "s_waitcnt lgkmcnt(9)\n\t"
            "v_smfmac_f32_16x16x64_bf16 v[176:179], v[168:171], v[144:151], v255\n\t"
            "ds_read_b128 v[168:171], v254 offset:128\n\t"
            "s_waitcnt lgkmcnt(6)\n\t"
            "v_smfmac_f32_16x16x64_bf16 v[180:183], v[172:175], v[152:159], v255\n\t"
            "s_setprio 1\n\t"
            "ds_read_b64 v[144:145], v253 offset:24\n\t"
            "ds_read_b64 v[146:147], v253 offset:120\n\t"
            "ds_read_b64 v[148:149], v253 offset:1768\n\t"
            "ds_read_b64 v[150:151], v253 offset:1864\n\t"
            "ds_read_b128 v[172:175], v254 offset:192\n\t"
            "ds_read_b64 v[152:153], v253 offset:32\n\t"
            "ds_read_b64 v[154:155], v253 offset:128\n\t"
            "ds_read_b64 v[156:157], v253 offset:1776\n\t"
            "ds_read_b64 v[158:159], v253 offset:1872\n\t"
            "v_cvt_pk_bf16_f32 v176, v176, v177\n\t"
            "v_cvt_pk_bf16_f32 v177, v178, v179\n\t"
            "v_cvt_pk_bf16_f32 v178, v180, v181\n\t"
            "v_cvt_pk_bf16_f32 v179, v182, v183\n\t"
            "s_setprio 3\n\t"
            "v_mfma_f32_4x4x4_16b_bf16 v[196:199], v[176:177], v[176:177], 0\n\t"
            "v_mov_b64 v[180:181], 0\n\t"
            "v_mfma_f32_16x16x32_bf16 v[192:195], v[238:241], v[176:179], 0\n\t"
            "s_waitcnt lgkmcnt(10)\n\t"
            "v_readfirstlane_b32 s26, v249\n\t"
            "v_mov_b64 v[182:183], 0\n\t"
            "v_mfma_f32_4x4x4_16b_bf16 v[196:199], v[178:179], v[178:179], v[196:199]\n\t"
            "ds_read_b32 v249, v252 offset:12\n\t"
            "s_and_b32 s68, s26, 0x7ffffff\n\t"
            "s_add_u32 s68, s68, s78\n\t"
            "s_and_b32 s69, s26, 0x38000000\n\t"
            "s_or_b32 s69, s69, s39\n\t"
            "s_bitcmp1_b32 s26, 27\n\t"
            "s_cselect_b32 s79, 2.0, s68\n\t"
            "s_and_b32 s57, s56, 3\n\t"
            "s_lshl_b32 s58, s57, 3\n\t"
            "s_lshr_b32 m0, s54, s58\n\t"
            "s_lshl_b32 s58, s57, 4\n\t"
            "s_lshr_b64 s[36:37], s[50:51], s58\n\t"
            "s_and_b32 s57, s36, 0xffff\n\t"
            "s_add_u32 s57, s57, s67\n\t"
            "v_writelane_b32 v236, s57, 63\n\t"
            "v_writelane_b32 v236, s59, m0\n\t"
            "v_xor_b32 v237, 16, v236\n\t"
            "s_lshr_b64 s[36:37], s[52:53], s58\n\t"
            "s_and_b32 s58, s36, 0xffff\n\t"
            "s_add_u32 s58, s58, s67\n\t"
            "ds_write_b128 v236, v[176:179]\n\t"
            "s_waitcnt lgkmcnt(11)\n\t"
            "v_smfmac_f32_16x16x64_bf16 v[184:187], v[168:171], v[160:167], v255\n\t"
            "s_and_b32 s38, s52, 0xffff\n\t"
            "s_add_u32 s38, s38, s67\n\t"
            "ds_read_b128 v[168:171], v254 offset:256\n\t"
            "s_waitcnt lgkmcnt(7)\n\t"
            "v_smfmac_f32_16x16x64_bf16 v[188:191], v[172:175], v[144:151], v255\n\t"
            "s_setprio 1\n\t"
            "ds_read_b64 v[160:161], v253 offset:40\n\t"
            "ds_read_b64 v[162:163], v253 offset:136\n\t"
            "ds_read_b64 v[164:165], v253 offset:1784\n\t"
            "ds_read_b64 v[166:167], v253 offset:1880\n\t"
            "ds_read_b128 v[172:175], v254 offset:320\n\t"
            "ds_read_b64 v[144:145], v253 offset:48\n\t"
            "ds_read_b64 v[146:147], v253 offset:144\n\t"
            "ds_read_b64 v[148:149], v253 offset:1792\n\t"
            "s_waitcnt lgkmcnt(14)\n\t"
            "ds_read_b64 v[150:151], v253 offset:1888\n\t"
            "v_mov_b64 v[176:177], 0\n\t"
            "v_mov_b64 v[178:179], 0\n\t"
            "v_cvt_pk_bf16_f32 v184, v184, v185\n\t"
            "v_cvt_pk_bf16_f32 v185, v186, v187\n\t"
            "v_cvt_pk_bf16_f32 v186, v188, v189\n\t"
            "v_cvt_pk_bf16_f32 v187, v190, v191\n\t"
            "s_setprio 3\n\t"
            "v_mfma_f32_4x4x4_16b_bf16 v[196:199], v[184:185], v[184:185], v[196:199]\n\t"
            "s_waitcnt lgkmcnt(14)\n\t"
            "ds_write_b128 v237, v[184:187]\n\t"
            "v_mfma_f32_16x16x32_bf16 v[192:195], v[238:241], v[184:187], v[192:195]\n\t"
            "v_mov_b64 v[184:185], 0\n\t"
            "v_mov_b64 v[188:189], 0\n\t"
            "v_mfma_f32_4x4x4_16b_bf16 v[196:199], v[186:187], v[186:187], v[196:199]\n\t"
            "v_mov_b64 v[186:187], 0\n\t"
            "v_mov_b64 v[190:191], 0\n\t"
            "v_writelane_b32 v248, s58, 63\n\t"
            "v_writelane_b32 v248, s38, m0\n\t"
            "s_waitcnt lgkmcnt(12)\n\t"
            "v_and_b32 v249, s69, v249\n\t"
            "s_waitcnt lgkmcnt(10)\n\t"
            "v_smfmac_f32_16x16x64_bf16 v[176:179], v[168:171], v[152:159], v255\n\t"
            "ds_read_b128 v[168:171], v254 offset:384\n\t"
            "ds_read_b64 v[152:153], v253 offset:56\n\t"
            "ds_read_b64 v[154:155], v253 offset:152\n\t"
            "ds_read_b64 v[156:157], v253 offset:1800\n\t"
            "ds_read_b64 v[158:159], v253 offset:1896\n\t"
            "s_waitcnt lgkmcnt(10)\n\t"
            "v_smfmac_f32_16x16x64_bf16 v[180:183], v[172:175], v[160:167], v255\n\t"
            "s_setprio 1\n\t"
            "ds_read_b128 v[172:175], v254 offset:448\n\t"
            "ds_read_b64 v[160:161], v253 offset:64\n\t"
            "ds_read_b64 v[162:163], v253 offset:160\n\t"
            "ds_read_b64 v[164:165], v253 offset:1808\n\t"
            "ds_read_b64 v[166:167], v253 offset:1904\n\t"
            "v_cvt_pk_bf16_f32 v176, v176, v177\n\t"
            "v_cvt_pk_bf16_f32 v177, v178, v179\n\t"
            "v_cvt_pk_bf16_f32 v178, v180, v181\n\t"
            "v_cvt_pk_bf16_f32 v179, v182, v183\n\t"
            "s_setprio 3\n\t"
            "v_mfma_f32_4x4x4_16b_bf16 v[196:199], v[176:177], v[176:177], v[196:199]\n\t"
            "s_waitcnt lgkmcnt(14)\n\t"
            "ds_write_b128 v236, v[176:179] offset:128\n\t"
            "v_mfma_f32_16x16x32_bf16 v[192:195], v[238:241], v[176:179], v[192:195]\n\t"
            "v_mov_b64 v[176:177], 0\n\t"
            "v_mov_b64 v[180:181], 0\n\t"
            "v_mfma_f32_4x4x4_16b_bf16 v[196:199], v[178:179], v[178:179], v[196:199]\n\t"
            "v_mov_b64 v[178:179], 0\n\t"
            "v_mov_b64 v[182:183], 0\n\t"
            "s_waitcnt lgkmcnt(10)\n\t"
            "v_smfmac_f32_16x16x64_bf16 v[184:187], v[168:171], v[144:151], v255\n\t"
            "ds_read_b128 v[168:171], v254 offset:512\n\t"
            "ds_read_b64 v[144:145], v253 offset:72\n\t"
            "ds_read_b64 v[146:147], v253 offset:168\n\t"
            "ds_read_b64 v[148:149], v253 offset:1816\n\t"
            "ds_read_b64 v[150:151], v253 offset:1912\n\t"
            "s_waitcnt lgkmcnt(10)\n\t"
            "v_smfmac_f32_16x16x64_bf16 v[188:191], v[172:175], v[152:159], v255\n\t"
            "s_setprio 1\n\t"
            "ds_read_b128 v[172:175], v254 offset:576\n\t"
            "ds_read_b64 v[152:153], v253 offset:80\n\t"
            "ds_read_b64 v[154:155], v253 offset:176\n\t"
            "ds_read_b64 v[156:157], v253 offset:1824\n\t"
            "ds_read_b64 v[158:159], v253 offset:1920\n\t"
            "v_cvt_pk_bf16_f32 v184, v184, v185\n\t"
            "v_cvt_pk_bf16_f32 v185, v186, v187\n\t"
            "v_cvt_pk_bf16_f32 v186, v188, v189\n\t"
            "v_cvt_pk_bf16_f32 v187, v190, v191\n\t"
            "s_setprio 3\n\t"
            "v_mfma_f32_4x4x4_16b_bf16 v[196:199], v[184:185], v[184:185], v[196:199]\n\t"
            "s_waitcnt lgkmcnt(14)\n\t"
            "ds_write_b128 v237, v[184:187] offset:128\n\t"
            "v_mfma_f32_16x16x32_bf16 v[192:195], v[238:241], v[184:187], v[192:195]\n\t"
            "v_mov_b64 v[184:185], 0\n\t"
            "v_mov_b64 v[188:189], 0\n\t"
            "v_mfma_f32_4x4x4_16b_bf16 v[196:199], v[186:187], v[186:187], v[196:199]\n\t"
            "v_mov_b64 v[186:187], 0\n\t"
            "v_mov_b64 v[190:191], 0\n\t"
            "s_waitcnt lgkmcnt(10)\n\t"
            "v_smfmac_f32_16x16x64_bf16 v[176:179], v[168:171], v[160:167], v255\n\t"
            "ds_read_b128 v[168:171], v254 offset:640\n\t"
            "ds_read_b64 v[160:161], v253 offset:88\n\t"
            "ds_read_b64 v[162:163], v253 offset:184\n\t"
            "ds_read_b64 v[164:165], v253 offset:1832\n\t"
            "ds_read_b64 v[166:167], v253 offset:1928\n\t"
            "s_waitcnt lgkmcnt(10)\n\t"
            "v_smfmac_f32_16x16x64_bf16 v[180:183], v[172:175], v[144:151], v255\n\t"
            "ds_read_b128 v[172:175], v254 offset:704\n\t"
            "ds_read_b32 v247, v247 offset:7856\n\t"
            "v_cvt_pk_bf16_f32 v176, v176, v177\n\t"
            "v_cvt_pk_bf16_f32 v177, v178, v179\n\t"
            "v_lshlrev_b32 v144, 16, v212\n\t"
            "v_and_b32 v145, 0xffff0000, v212\n\t"
            "v_mfma_f32_4x4x4_16b_bf16 v[196:199], v[176:177], v[176:177], v[196:199]\n\t"
            "v_lshlrev_b32 v146, 16, v213\n\t"
            "v_cvt_pk_bf16_f32 v178, v180, v181\n\t"
            "v_cvt_pk_bf16_f32 v179, v182, v183\n\t"
            "v_lshlrev_b32 v180, 16, v210\n\t"
            "v_and_b32 v181, 0xffff0000, v210\n\t"
            "v_mfma_f32_4x4x4_16b_bf16 v[196:199], v[178:179], v[178:179], v[196:199]\n\t"
            "ds_write_b128 v236, v[176:179] offset:256\n\t"
            "v_mfma_f32_16x16x32_bf16 v[192:195], v[238:241], v[176:179], v[192:195]\n\t"
            "s_setprio 1\n\t"
            "v_lshlrev_b32 v176, 16, v208\n\t"
            "v_and_b32 v177, 0xffff0000, v208\n\t"
            "v_lshlrev_b32 v178, 16, v209\n\t"
            "v_and_b32 v179, 0xffff0000, v209\n\t"
            "v_lshlrev_b32 v182, 16, v211\n\t"
            "v_and_b32 v183, 0xffff0000, v211\n\t"
            "v_and_b32 v147, 0xffff0000, v213\n\t"
            "v_lshlrev_b32 v148, 16, v214\n\t"
            "v_and_b32 v149, 0xffff0000, v214\n\t"
            "v_lshlrev_b32 v150, 16, v215\n\t"
            "v_and_b32 v151, 0xffff0000, v215\n\t"
            "v_add_u32 v236, s48, v244\n\t"
            "ds_read_b128 v[212:215], v236 offset:6144\n\t"
            "s_setprio 3\n\t"
            "s_waitcnt lgkmcnt(8)\n\t"
            "v_smfmac_f32_16x16x64_bf16 v[184:187], v[168:171], v[152:159], v255\n\t"
            "v_lshlrev_b32 v152, 16, v200\n\t"
            "v_and_b32 v153, 0xffff0000, v200\n\t"
            "v_lshlrev_b32 v154, 16, v201\n\t"
            "v_and_b32 v155, 0xffff0000, v201\n\t"
            "v_lshlrev_b32 v156, 16, v202\n\t"
            "s_waitcnt lgkmcnt(3)\n\t"
            "v_smfmac_f32_16x16x64_bf16 v[188:191], v[172:175], v[160:167], v255\n\t"
            "s_setprio 1\n\t"
            "s_mov_b32 exec_hi, 0x3fffff\n\t"
            "s_add_u32 m0, s66, 0\n\t"
            "s_add_u32 s49, s79, 0\n\t"
            "buffer_load_dwordx4 v249, s[72:75], s49 offen lds\n\t"
            "s_mov_b32 exec_hi, -1\n\t"
            "s_mov_b32 exec_hi, 0x3fffff\n\t"
            "s_add_u32 m0, s66, 872\n\t"
            "s_add_u32 s49, s68, 196224\n\t"
            "buffer_load_dwordx4 v249, s[72:75], s49 offen lds\n\t"
            "s_mov_b32 exec_hi, -1\n\t"
            "s_mov_b32 exec_hi, 0x3fffff\n\t"
            "s_add_u32 m0, s66, 1744\n\t"
            "s_add_u32 s49, s68, 392448\n\t"
            "buffer_load_dwordx4 v249, s[72:75], s49 offen lds\n\t"
            "s_mov_b32 exec_hi, -1\n\t"
            "s_mov_b32 exec_hi, 0x3fffff\n\t"
            "s_add_u32 m0, s66, 2616\n\t"
            "s_add_u32 s49, s68, 588672\n\t"
            "buffer_load_dwordx4 v249, s[72:75], s49 offen lds\n\t"
            "s_mov_b32 exec_hi, -1\n\t"
            "s_mov_b32 exec_hi, 0x3fffff\n\t"
            "s_add_u32 m0, s66, 3488\n\t"
            "s_add_u32 s49, s68, 784896\n\t"
            "buffer_load_dwordx4 v249, s[72:75], s49 offen lds\n\t"
            "s_mov_b32 exec_hi, -1\n\t"
            "s_mov_b32 exec_hi, 0x3fffff\n\t"
            "s_add_u32 m0, s66, 4360\n\t"
            "s_add_u32 s49, s68, 981120\n\t"
            "buffer_load_dwordx4 v249, s[72:75], s49 offen lds\n\t"
            "s_mov_b32 exec_hi, -1\n\t"
            "s_mov_b32 exec_hi, 0x3fffff\n\t"
            "s_add_u32 m0, s66, 5232\n\t"
            "s_add_u32 s49, s68, 1177344\n\t"
            "buffer_load_dwordx4 v249, s[72:75], s49 offen lds\n\t"
            "s_mov_b32 exec_hi, -1\n\t"
            "s_mov_b32 exec_hi, 0x3fffff\n\t"
            "s_add_u32 m0, s66, 6104\n\t"
            "s_add_u32 s49, s68, 1373568\n\t"
            "buffer_load_dwordx4 v249, s[72:75], s49 offen lds\n\t"
            "s_mov_b32 exec_hi, -1\n\t"
            "s_mov_b32 exec_hi, 0x3fffff\n\t"
            "s_add_u32 m0, s66, 6976\n\t"
            "s_add_u32 s49, s68, 1569792\n\t"
            "buffer_load_dwordx4 v249, s[72:75], s49 offen lds\n\t"
            "s_mov_b32 exec_hi, -1\n\t"
            "v_cvt_pk_bf16_f32 v184, v184, v185\n\t"
            "v_cvt_pk_bf16_f32 v185, v186, v187\n\t"
            "v_cvt_pk_bf16_f32 v186, v188, v189\n\t"
            "v_cvt_pk_bf16_f32 v187, v190, v191\n\t"
            "s_setprio 3\n\t"
            "v_mfma_f32_4x4x4_16b_bf16 v[196:199], v[184:185], v[184:185], v[196:199]\n\t"
            "ds_write_b128 v237, v[184:187] offset:256\n\t"
            "v_mfma_f32_16x16x32_bf16 v[192:195], v[238:241], v[184:187], v[192:195]\n\t"
            "s_waitcnt lgkmcnt(3)\n\t"
            "v_readfirstlane_b32 s77, v247\n\t"
            "s_cmp_ge_u32 s77, s70\n\t"
            "s_cbranch_scc0 .Lwb_spin%=\n\t"
            ".Lwb_go%=:\n\t"
            "v_mfma_f32_4x4x4_16b_bf16 v[196:199], v[186:187], v[186:187], v[196:199]\n\t"
            "s_setprio 1\n\t"
            "ds_read_b128 v[184:187], v242\n\t"
            "ds_read_b128 v[188:191], v243\n\t"
            "ds_read_b128 v[172:175], v243 offset:128\n\t"
            "v_and_b32 v157, 0xffff0000, v202\n\t"
            "v_lshlrev_b32 v158, 16, v203\n\t"
            "v_and_b32 v159, 0xffff0000, v203\n\t"
            "v_lshlrev_b32 v160, 16, v204\n\t"
            "v_and_b32 v161, 0xffff0000, v204\n\t"
            "v_lshlrev_b32 v162, 16, v205\n\t"
            "v_cndmask_b32_e64 v193, v196, v197, s[60:61]\n\t"
            "v_cndmask_b32_e64 v193, v193, v198, s[62:63]\n\t"
            "v_cndmask_b32_e64 v193, v193, v199, s[64:65]\n\t"
            "v_and_b32 v163, 0xffff0000, v205\n\t"
            "v_lshlrev_b32 v164, 16, v206\n\t"
            "v_and_b32 v165, 0xffff0000, v206\n\t"
            "v_lshlrev_b32 v166, 16, v207\n\t"
            "v_and_b32 v167, 0xffff0000, v207\n\t"
            "v_lshlrev_b32 v168, 16, v216\n\t"
            "v_and_b32 v169, 0xffff0000, v216\n\t"
            "v_lshlrev_b32 v170, 16, v217\n\t"
            "v_and_b32 v171, 0xffff0000, v217\n\t"
            "s_bfe_u32 s57, s56, 0x20004\n\t"
            "s_lshl_b32 s58, s57, 3\n\t"
            "s_lshr_b32 s58, s55, s58\n\t"
            "s_lshl_b64 s[36:37], s[34:35], s58\n\t"
            "ds_read_b128 v[196:199], v252\n\t"
            "ds_read_b128 v[200:203], v236\n\t"
            "ds_write_b64 v248, v[192:193] offset:18816\n\t"
            "ds_read_b128 v[192:195], v242 offset:128\n\t"
            "s_bitcmp1_b32 s57, 1\n\t"
            "s_cselect_b32 s58, 1177344, 0\n\t"
            "s_bitcmp1_b32 s57, 0\n\t"
            "s_cselect_b32 s57, 2304, 0\n\t"
            "s_add_u32 s57, s57, s58\n\t"
            "s_mov_b64 exec, 1\n\t"
            "ds_inc_u32 v255, v238 offset:7860\n\t"
            "s_mov_b64 exec, -1\n\t"
            "v_add_u32 v237, s48, v245\n\t"
            "v_add_u32 v248, s48, v246\n\t"
            "s_add_u32 s59, s59, 800\n\t"
            "s_setprio 1\n\t"
            "ds_read_b128 v[204:207], v237\n\t"
            "s_waitcnt lgkmcnt(7)\n\t"
            "v_mfma_f32_16x16x32_bf16 v[152:155], v[0:3], v[184:187], v[152:155]\n\t"
            "ds_read_b128 v[208:211], v248\n\t"
            "v_mfma_f32_16x16x32_bf16 v[152:155], v[4:7], v[188:191], v[152:155]\n\t"
            "v_mfma_f32_16x16x32_bf16 v[156:159], v[28:31], v[188:191], v[156:159]\n\t"
            "v_mfma_f32_16x16x32_bf16 v[156:159], v[24:27], v[184:187], v[156:159]\n\t"
            "v_mfma_f32_16x16x32_bf16 v[160:163], v[48:51], v[184:187], v[160:163]\n\t"
            "v_mfma_f32_16x16x32_bf16 v[160:163], v[52:55], v[188:191], v[160:163]\n\t"
            "v_mfma_f32_16x16x32_bf16 v[164:167], v[76:79], v[188:191], v[164:167]\n\t"
            "s_waitcnt lgkmcnt(6)\n\t"
            "s_mov_b64 exec, s[36:37]\n\t"
            "v_add_u32 v196, s57, v196\n\t"
            "s_mov_b64 exec, -1\n\t"
            "v_mfma_f32_16x16x32_bf16 v[164:167], v[72:75], v[184:187], v[164:167]\n\t"
            "v_mfma_f32_16x16x32_bf16 v[176:179], v[96:99], v[184:187], v[176:179]\n\t"
            "v_mfma_f32_16x16x32_bf16 v[176:179], v[100:103], v[188:191], v[176:179]\n\t"
            "v_mfma_f32_16x16x32_bf16 v[180:183], v[124:127], v[188:191], v[180:183]\n\t"
            "ds_read_b128 v[188:191], v243 offset:256\n\t"
            "v_mfma_f32_16x16x32_bf16 v[180:183], v[120:123], v[184:187], v[180:183]\n\t"
            "ds_read_b128 v[184:187], v242 offset:256\n\t"
            "s_waitcnt lgkmcnt(5)\n\t"
            "v_mfma_f32_16x16x32_bf16 v[152:155], v[8:11], v[192:195], v[152:155]\n\t"
            "v_mfma_f32_16x16x32_bf16 v[152:155], v[12:15], v[172:175], v[152:155]\n\t"
            "v_mfma_f32_16x16x32_bf16 v[156:159], v[36:39], v[172:175], v[156:159]\n\t"
            "v_mfma_f32_16x16x32_bf16 v[156:159], v[32:35], v[192:195], v[156:159]\n\t"
            "v_mfma_f32_16x16x32_bf16 v[160:163], v[56:59], v[192:195], v[160:163]\n\t"
            "v_mfma_f32_16x16x32_bf16 v[160:163], v[60:63], v[172:175], v[160:163]\n\t"
            "v_mfma_f32_16x16x32_bf16 v[164:167], v[84:87], v[172:175], v[164:167]\n\t"
            "v_mfma_f32_16x16x32_bf16 v[164:167], v[80:83], v[192:195], v[164:167]\n\t"
            "v_mfma_f32_16x16x32_bf16 v[176:179], v[104:107], v[192:195], v[176:179]\n\t"
            "v_mfma_f32_16x16x32_bf16 v[176:179], v[108:111], v[172:175], v[176:179]\n\t"
            "v_mfma_f32_16x16x32_bf16 v[180:183], v[132:135], v[172:175], v[180:183]\n\t"
            "ds_read_b128 v[172:175], v243 offset:6144\n\t"
            "v_mfma_f32_16x16x32_bf16 v[180:183], v[128:131], v[192:195], v[180:183]\n\t"
            "ds_read_b128 v[192:195], v242 offset:6144\n\t"
            "s_waitcnt lgkmcnt(2)\n\t"
            "v_mfma_f32_16x16x32_bf16 v[152:155], v[16:19], v[184:187], v[152:155]\n\t"
            "v_mfma_f32_16x16x32_bf16 v[152:155], v[20:23], v[188:191], v[152:155]\n\t"
            "v_mfma_f32_16x16x32_bf16 v[156:159], v[44:47], v[188:191], v[156:159]\n\t"
            "v_mfma_f32_16x16x32_bf16 v[156:159], v[40:43], v[184:187], v[156:159]\n\t"
            "v_mfma_f32_16x16x32_bf16 v[160:163], v[64:67], v[184:187], v[160:163]\n\t"
            "v_mfma_f32_16x16x32_bf16 v[160:163], v[68:71], v[188:191], v[160:163]\n\t"
            "v_mfma_f32_16x16x32_bf16 v[164:167], v[92:95], v[188:191], v[164:167]\n\t"
            "v_mfma_f32_16x16x32_bf16 v[164:167], v[88:91], v[184:187], v[164:167]\n\t"
            "s_nop 1\n\t"
            "v_cvt_pk_bf16_f32 v152, v152, v153\n\t"
            "v_mfma_f32_16x16x32_bf16 v[176:179], v[112:115], v[184:187], v[176:179]\n\t"
            "v_cvt_pk_bf16_f32 v153, v154, v155\n\t"
            "v_mfma_f32_16x16x32_bf16 v[176:179], v[116:119], v[188:191], v[176:179]\n\t"
            "v_cvt_pk_bf16_f32 v154, v156, v157\n\t"
            "v_cvt_pk_bf16_f32 v155, v158, v159\n\t"
            "v_mfma_f32_16x16x32_bf16 v[180:183], v[140:143], v[188:191], v[180:183]\n\t"
            "ds_read_b128 v[188:191], v243 offset:6272\n\t"
            "v_cvt_pk_bf16_f32 v160, v160, v161\n\t"
            "v_mfma_f32_16x16x32_bf16 v[180:183], v[136:139], v[184:187], v[180:183]\n\t"
            "ds_read_b128 v[184:187], v242 offset:6272\n\t"
            "v_cvt_pk_bf16_f32 v161, v162, v163\n\t"
            "s_waitcnt lgkmcnt(2)\n\t"
            "v_mfma_f32_16x16x32_bf16 v[144:147], v[0:3], v[192:195], v[144:147]\n\t"
            "v_cvt_pk_bf16_f32 v176, v176, v177\n\t"
            "v_cvt_pk_bf16_f32 v177, v178, v179\n\t"
            "v_mfma_f32_16x16x32_bf16 v[144:147], v[4:7], v[172:175], v[144:147]\n\t"
            "v_cvt_pk_bf16_f32 v162, v164, v165\n\t"
            "v_cvt_pk_bf16_f32 v163, v166, v167\n\t"
            "v_mfma_f32_16x16x32_bf16 v[148:151], v[28:31], v[172:175], v[148:151]\n\t"
            "v_cvt_pk_bf16_f32 v178, v180, v181\n\t"
            "v_cvt_pk_bf16_f32 v179, v182, v183\n\t"
            "v_mfma_f32_16x16x32_bf16 v[148:151], v[24:27], v[192:195], v[148:151]\n\t"
            "v_lshlrev_b32 v156, 16, v218\n\t"
            "v_and_b32 v157, 0xffff0000, v218\n\t"
            "v_mfma_f32_16x16x32_bf16 v[168:171], v[48:51], v[192:195], v[168:171]\n\t"
            "v_lshlrev_b32 v158, 16, v219\n\t"
            "v_and_b32 v159, 0xffff0000, v219\n\t"
            "v_mfma_f32_16x16x32_bf16 v[168:171], v[52:55], v[172:175], v[168:171]\n\t"
            "ds_read_b128 v[216:219], v237 offset:6144\n\t"
            "v_lshlrev_b32 v164, 16, v220\n\t"
            "v_mfma_f32_16x16x32_bf16 v[156:159], v[76:79], v[172:175], v[156:159]\n\t"
            "v_and_b32 v165, 0xffff0000, v220\n\t"
            "v_lshlrev_b32 v166, 16, v221\n\t"
            "v_mfma_f32_16x16x32_bf16 v[156:159], v[72:75], v[192:195], v[156:159]\n\t"
            "v_and_b32 v167, 0xffff0000, v221\n\t"
            "v_lshlrev_b32 v180, 16, v222\n\t"
            "v_and_b32 v181, 0xffff0000, v222\n\t"
            "v_mfma_f32_16x16x32_bf16 v[164:167], v[96:99], v[192:195], v[164:167]\n\t"
            "v_lshlrev_b32 v182, 16, v223\n\t"
            "v_and_b32 v183, 0xffff0000, v223\n\t"
            "v_mfma_f32_16x16x32_bf16 v[164:167], v[100:103], v[172:175], v[164:167]\n\t"
            "ds_read_b128 v[220:223], v248 offset:6144\n\t"
            "buffer_store_dwordx4 v[152:155], v196, s[40:43], s45 offen sc1\n\t"
            "v_mfma_f32_16x16x32_bf16 v[180:183], v[124:127], v[172:175], v[180:183]\n\t"
            "ds_read_b128 v[172:175], v243 offset:6400\n\t"
            "v_lshlrev_b32 v152, 16, v224\n\t"
            "v_mfma_f32_16x16x32_bf16 v[180:183], v[120:123], v[192:195], v[180:183]\n\t"
            "ds_read_b128 v[192:195], v242 offset:6400\n\t"
            "buffer_store_dwordx4 v[160:163], v196, s[40:43], s45 offen offset:64 sc1\n\t"
            "s_waitcnt lgkmcnt(4)\n\t"
            "v_mfma_f32_16x16x32_bf16 v[144:147], v[8:11], v[184:187], v[144:147]\n\t"
            "v_and_b32 v153, 0xffff0000, v224\n\t"
            "v_lshlrev_b32 v154, 16, v225\n\t"
            "v_mfma_f32_16x16x32_bf16 v[144:147], v[12:15], v[188:191], v[144:147]\n\t"
            "buffer_store_dwordx4 v[176:179], v196, s[40:43], s45 offen offset:128 sc1\n\t"
            "v_mfma_f32_16x16x32_bf16 v[148:151], v[36:39], v[188:191], v[148:151]\n\t"
            "v_and_b32 v155, 0xffff0000, v225\n\t"
            "v_lshlrev_b32 v160, 16, v226\n\t"
            "v_mfma_f32_16x16x32_bf16 v[148:151], v[32:35], v[184:187], v[148:151]\n\t"
            "v_and_b32 v161, 0xffff0000, v226\n\t"
            "v_lshlrev_b32 v162, 16, v227\n\t"
            "v_mfma_f32_16x16x32_bf16 v[168:171], v[56:59], v[184:187], v[168:171]\n\t"
            "v_and_b32 v163, 0xffff0000, v227\n\t"
            "v_lshlrev_b32 v176, 16, v228\n\t"
            "v_mfma_f32_16x16x32_bf16 v[168:171], v[60:63], v[188:191], v[168:171]\n\t"
            "ds_read_b128 v[224:227], v236 offset:12288\n\t"
            "v_and_b32 v177, 0xffff0000, v228\n\t"
            "v_mfma_f32_16x16x32_bf16 v[156:159], v[84:87], v[188:191], v[156:159]\n\t"
            "v_lshlrev_b32 v178, 16, v229\n\t"
            "v_and_b32 v179, 0xffff0000, v229\n\t"
            "v_mfma_f32_16x16x32_bf16 v[156:159], v[80:83], v[184:187], v[156:159]\n\t"
            "v_mfma_f32_16x16x32_bf16 v[164:167], v[104:107], v[184:187], v[164:167]\n\t"
            "v_mfma_f32_16x16x32_bf16 v[164:167], v[108:111], v[188:191], v[164:167]\n\t"
            "v_mfma_f32_16x16x32_bf16 v[180:183], v[132:135], v[188:191], v[180:183]\n\t"
            "ds_read_b128 v[188:191], v243 offset:12288\n\t"
            "v_mfma_f32_16x16x32_bf16 v[180:183], v[128:131], v[184:187], v[180:183]\n\t"
            "ds_read_b128 v[184:187], v242 offset:12288\n\t"
            "s_waitcnt lgkmcnt(3)\n\t"
            "v_mfma_f32_16x16x32_bf16 v[144:147], v[16:19], v[192:195], v[144:147]\n\t"
            "v_mfma_f32_16x16x32_bf16 v[144:147], v[20:23], v[172:175], v[144:147]\n\t"
            "v_mfma_f32_16x16x32_bf16 v[168:171], v[64:67], v[192:195], v[168:171]\n\t"
            "v_mfma_f32_16x16x32_bf16 v[168:171], v[68:71], v[172:175], v[168:171]\n\t"
            "v_mfma_f32_16x16x32_bf16 v[164:167], v[112:115], v[192:195], v[164:167]\n\t"
            "v_mfma_f32_16x16x32_bf16 v[164:167], v[116:119], v[172:175], v[164:167]\n\t"
            "v_mfma_f32_16x16x32_bf16 v[148:151], v[44:47], v[172:175], v[148:151]\n\t"
            "v_mfma_f32_16x16x32_bf16 v[148:151], v[40:43], v[192:195], v[148:151]\n\t"
            "s_nop 1\n\t"
            "v_cvt_pk_bf16_f32 v144, v144, v145\n\t"
            "v_mfma_f32_16x16x32_bf16 v[156:159], v[92:95], v[172:175], v[156:159]\n\t"
            "v_cvt_pk_bf16_f32 v145, v146, v147\n\t"
            "v_mfma_f32_16x16x32_bf16 v[156:159], v[88:91], v[192:195], v[156:159]\n\t"
            "v_cvt_pk_bf16_f32 v168, v168, v169\n\t"
            "v_cvt_pk_bf16_f32 v169, v170, v171\n\t"
            "v_mfma_f32_16x16x32_bf16 v[180:183], v[140:143], v[172:175], v[180:183]\n\t"
            "ds_read_b128 v[172:175], v243 offset:12416\n\t"
            "v_cvt_pk_bf16_f32 v164, v164, v165\n\t"
            "v_mfma_f32_16x16x32_bf16 v[180:183], v[136:139], v[192:195], v[180:183]\n\t"
            "ds_read_b128 v[192:195], v242 offset:12416\n\t"
            "v_cvt_pk_bf16_f32 v165, v166, v167\n\t"
            "s_waitcnt lgkmcnt(2)\n\t"
            "v_mfma_f32_16x16x32_bf16 v[152:155], v[0:3], v[184:187], v[152:155]\n\t"
            "v_cvt_pk_bf16_f32 v146, v148, v149\n\t"
            "v_cvt_pk_bf16_f32 v147, v150, v151\n\t"
            "v_mfma_f32_16x16x32_bf16 v[152:155], v[4:7], v[188:191], v[152:155]\n\t"
            "v_cvt_pk_bf16_f32 v170, v156, v157\n\t"
            "v_cvt_pk_bf16_f32 v171, v158, v159\n\t"
            "v_mfma_f32_16x16x32_bf16 v[160:163], v[28:31], v[188:191], v[160:163]\n\t"
            "v_cvt_pk_bf16_f32 v166, v180, v181\n\t"
            "v_cvt_pk_bf16_f32 v167, v182, v183\n\t"
            "v_mfma_f32_16x16x32_bf16 v[160:163], v[24:27], v[184:187], v[160:163]\n\t"
            "v_lshlrev_b32 v148, 16, v230\n\t"
            "v_and_b32 v149, 0xffff0000, v230\n\t"
            "v_mfma_f32_16x16x32_bf16 v[176:179], v[48:51], v[184:187], v[176:179]\n\t"
            "v_lshlrev_b32 v150, 16, v231\n\t"
            "v_and_b32 v151, 0xffff0000, v231\n\t"
            "v_mfma_f32_16x16x32_bf16 v[176:179], v[52:55], v[188:191], v[176:179]\n\t"
            "ds_read_b128 v[228:231], v237 offset:12288\n\t"
            "v_lshlrev_b32 v156, 16, v232\n\t"
            "v_mfma_f32_16x16x32_bf16 v[148:151], v[76:79], v[188:191], v[148:151]\n\t"
            "v_and_b32 v157, 0xffff0000, v232\n\t"
            "v_lshlrev_b32 v158, 16, v233\n\t"
            "v_mfma_f32_16x16x32_bf16 v[148:151], v[72:75], v[184:187], v[148:151]\n\t"
            "v_and_b32 v159, 0xffff0000, v233\n\t"
            "v_lshlrev_b32 v180, 16, v234\n\t"
            "v_and_b32 v181, 0xffff0000, v234\n\t"
            "v_mfma_f32_16x16x32_bf16 v[156:159], v[96:99], v[184:187], v[156:159]\n\t"
            "v_lshlrev_b32 v182, 16, v235\n\t"
            "v_and_b32 v183, 0xffff0000, v235\n\t"
            "v_mfma_f32_16x16x32_bf16 v[156:159], v[100:103], v[188:191], v[156:159]\n\t"
            "ds_read_b128 v[232:235], v248 offset:12288\n\t"
            "buffer_store_dwordx4 v[144:147], v197, s[40:43], s45 offen sc1\n\t"
            "v_mfma_f32_16x16x32_bf16 v[180:183], v[124:127], v[188:191], v[180:183]\n\t"
            "ds_read_b128 v[188:191], v243 offset:12544\n\t"
            "v_mfma_f32_16x16x32_bf16 v[180:183], v[120:123], v[184:187], v[180:183]\n\t"
            "ds_read_b128 v[184:187], v242 offset:12544\n\t"
            "buffer_store_dwordx4 v[168:171], v197, s[40:43], s45 offen offset:64 sc1\n\t"
            "s_waitcnt lgkmcnt(4)\n\t"
            "v_mfma_f32_16x16x32_bf16 v[152:155], v[8:11], v[192:195], v[152:155]\n\t"
            "v_mfma_f32_16x16x32_bf16 v[152:155], v[12:15], v[172:175], v[152:155]\n\t"
            "buffer_store_dwordx4 v[164:167], v197, s[40:43], s45 offen offset:128 sc1\n\t"
            "v_mfma_f32_16x16x32_bf16 v[160:163], v[36:39], v[172:175], v[160:163]\n\t"
            "v_mfma_f32_16x16x32_bf16 v[160:163], v[32:35], v[192:195], v[160:163]\n\t"
            "s_waitcnt lgkmcnt(0)\n\t"
            "s_barrier\n\t"
            "s_setprio 3\n\t"
            "v_mfma_f32_16x16x32_bf16 v[176:179], v[56:59], v[192:195], v[176:179]\n\t"
            "v_mfma_f32_16x16x32_bf16 v[176:179], v[60:63], v[172:175], v[176:179]\n\t"
            "v_mfma_f32_16x16x32_bf16 v[148:151], v[84:87], v[172:175], v[148:151]\n\t"
            "v_mfma_f32_16x16x32_bf16 v[148:151], v[80:83], v[192:195], v[148:151]\n\t"
            "v_mfma_f32_16x16x32_bf16 v[156:159], v[104:107], v[192:195], v[156:159]\n\t"
            "v_mfma_f32_16x16x32_bf16 v[156:159], v[108:111], v[172:175], v[156:159]\n\t"
            "v_mfma_f32_16x16x32_bf16 v[152:155], v[16:19], v[184:187], v[152:155]\n\t"
            "v_mfma_f32_16x16x32_bf16 v[152:155], v[20:23], v[188:191], v[152:155]\n\t"
            "v_mfma_f32_16x16x32_bf16 v[160:163], v[44:47], v[188:191], v[160:163]\n\t"
            "v_mfma_f32_16x16x32_bf16 v[160:163], v[40:43], v[184:187], v[160:163]\n\t"
            "v_mfma_f32_16x16x32_bf16 v[180:183], v[132:135], v[172:175], v[180:183]\n\t"
            "v_mfma_f32_16x16x32_bf16 v[180:183], v[128:131], v[192:195], v[180:183]\n\t"
            "v_mfma_f32_16x16x32_bf16 v[176:179], v[64:67], v[184:187], v[176:179]\n\t"
            "v_mfma_f32_16x16x32_bf16 v[176:179], v[68:71], v[188:191], v[176:179]\n\t"
            "s_nop 1\n\t"
            "v_cvt_pk_bf16_f32 v152, v152, v153\n\t"
            "v_mfma_f32_16x16x32_bf16 v[148:151], v[92:95], v[188:191], v[148:151]\n\t"
            "v_cvt_pk_bf16_f32 v153, v154, v155\n\t"
            "v_mfma_f32_16x16x32_bf16 v[148:151], v[88:91], v[184:187], v[148:151]\n\t"
            "v_cvt_pk_bf16_f32 v154, v160, v161\n\t"
            "v_cvt_pk_bf16_f32 v155, v162, v163\n\t"
            "v_mfma_f32_16x16x32_bf16 v[156:159], v[112:115], v[184:187], v[156:159]\n\t"
            "v_cvt_pk_bf16_f32 v176, v176, v177\n\t"
            "v_cvt_pk_bf16_f32 v177, v178, v179\n\t"
            "v_mfma_f32_16x16x32_bf16 v[156:159], v[116:119], v[188:191], v[156:159]\n\t"
            "buffer_store_dwordx4 v[152:155], v198, s[40:43], s45 offen sc1\n\t"
            "v_mfma_f32_16x16x32_bf16 v[180:183], v[140:143], v[188:191], v[180:183]\n\t"
            "v_cvt_pk_bf16_f32 v178, v148, v149\n\t"
            "v_cvt_pk_bf16_f32 v179, v150, v151\n\t"
            "v_mfma_f32_16x16x32_bf16 v[180:183], v[136:139], v[184:187], v[180:183]\n\t"
            "buffer_store_dwordx4 v[176:179], v198, s[40:43], s45 offen offset:64 sc1\n\t"
            "s_nop 1\n\t"
            "v_cvt_pk_bf16_f32 v156, v156, v157\n\t"
            "v_cvt_pk_bf16_f32 v157, v158, v159\n\t"
            "s_nop 2\n\t"
            "v_cvt_pk_bf16_f32 v158, v180, v181\n\t"
            "v_cvt_pk_bf16_f32 v159, v182, v183\n\t"
            "buffer_store_dwordx4 v[156:159], v198, s[40:43], s45 offen offset:128 sc1\n\t"
            "s_add_u32 s45, s46, s27\n\t"
            "s_mov_b32 s46, s47\n\t"
            "s_mov_b32 s47, s68\n\t"
            "s_xor_b32 s48, s48, s71\n\t"
            "s_xor_b32 s67, s67, s71\n\t"
            "s_add_u32 s76, s76, 4\n\t"
            "s_add_u32 s70, s70, 4\n\t"
            "s_lshl_b32 s56, s56, 2\n\t"
            "s_lshr_b32 s49, s26, 30\n\t"
            "s_or_b32 s56, s56, s49\n\t"
            "s_cmp_lt_u32 s46, 0x7000000\n\t"
            "s_cbranch_scc1 .Lwb_loop%=\n\t"
            "s_branch .Lwb_end%=\n\t"
            ".Lwb_spin%=:\n\t"
            "s_cmp_eq_u32 s83, 0\n\t"
            "s_cbranch_scc1 .Lwb_go%=\n\t"
            "s_sub_u32 s83, s83, 1\n\t"
            "s_sleep 2\n\t"
            "v_mov_b32 v247, 0\n\t"
            "ds_read_b32 v247, v247 offset:7856\n\t"
            "s_waitcnt lgkmcnt(0)\n\t"
            "v_readfirstlane_b32 s77, v247\n\t"
            "s_cmp_ge_u32 s77, s70\n\t"
            "s_cbranch_scc0 .Lwb_spin%=\n\t"
            "s_branch .Lwb_go%=\n\t"
            ".Lwb_end%=:\n\t"
            "s_waitcnt lgkmcnt(0)\n\t"
            "s_nop 7\n\t"
            : "={v[0:3]}"(W[0]), "={v[4:7]}"(W[1]), "={v[8:11]}"(W[2]), "={v[12:15]}"(W[3]), "={v[16:19]}"(W[4]), "={v[20:23]}"(W[5]), "={v[24:27]}"(W[6]), "={v[28:31]}"(W[7]), "={v[32:35]}"(W[8]), "={v[36:39]}"(W[9]), "={v[40:43]}"(W[10]), "={v[44:47]}"(W[11]), "={v[48:51]}"(W[12]), "={v[52:55]}"(W[13]), "={v[56:59]}"(W[14]), "={v[60:63]}"(W[15]), "={v[64:67]}"(W[16]), "={v[68:71]}"(W[17]), "={v[72:75]}"(W[18]), "={v[76:79]}"(W[19]), "={v[80:83]}"(W[20]), "={v[84:87]}"(W[21]), "={v[88:91]}"(W[22]), "={v[92:95]}"(W[23]), "={v[96:99]}"(W[24]), "={v[100:103]}"(W[25]), "={v[104:107]}"(W[26]), "={v[108:111]}"(W[27]), "={v[112:115]}"(W[28]), "={v[116:119]}"(W[29]), "={v[120:123]}"(W[30]), "={v[124:127]}"(W[31]), "={v[128:131]}"(W[32]), "={v[132:135]}"(W[33]), "={v[136:139]}"(W[34]), "={v[140:143]}"(W[35]), "={v[200:203]}"(xl[0]), "={v[204:207]}"(xl[1]), "={v[208:211]}"(xl[2]), "={v[212:215]}"(xl[3]), "={v[216:219]}"(xl[4]), "={v[220:223]}"(xl[5]), "={v[224:227]}"(xl[6]), "={v[228:231]}"(xl[7]), "={v[232:235]}"(xl[8]), B_SIO, "+{v247}"(l16_)
            : "{v242}"(oE_), "{v243}"(oO_), "{v244}"(pR0_), "{v245}"(pR1_), "{v246}"(pR2_), "{v250}"(pXw_), "{v251}"(pSTw_), "{v252}"(aCA_), "{v253}"(aB_), "{v254}"(aA_), "{v255}"(idx_), B_SIN
            : "v144", "v145", "v146", "v147", "v148", "v149", "v150", "v151", "v152", "v153", "v154", "v155", "v156", "v157", "v158", "v159", "v160", "v161", "v162", "v163", "v164", "v165", "v166", "v167", "v168", "v169", "v170", "v171", "v172", "v173", "v174", "v175", "v176", "v177", "v178", "v179", "v180", "v181", "v182", "v183", "v184", "v185", "v186", "v187", "v188", "v189", "v190", "v191", "v192", "v193", "v194", "v195", "v196", "v197", "v198", "v199", "v236", "v237", "v248", "v249", B_SCLOB);
            t45 = s45; t56 = s56;
        }
        {
            int ln_;
            asm volatile("v_mbcnt_lo_u32_b32 %0, -1, 0\n\tv_mbcnt_hi_u32_b32 %0, -1, %0" : "=v"(ln_));
            const int l15_ = ln_ & 15;
            const int g_ = ln_ >> 4;
            KARGS
            const auto orsrc = __builtin_amdgcn_make_buffer_rsrc((void*)ob, 0, IMG * IMG * CH * 2, 0x00020000);
#pragma unroll
            for (int lt = 0; lt < 2; ++lt) {
                int i = 16 * lt + l15_;
                const unsigned en = *(const unsigned*)(smem + WL_OFF + i * 4);
                const bool ok = (en & 0x7ffffffu) != 0x7ffffffu;
                i = ok ? i : 0;
                const int lE = OL_OFF + i * 384 + ((g_ ^ ((i >> 1) & 7)) << 4);
                const unsigned o = ok ? (en & 0x7ffffffu) + LPOS(en >> 31) * ROWBYTES + LPOS(en >> 30) * (CH * 2) + 192 * (w - 2) + 16 * g_ : OOB;
                u32x4 rl[3];
#pragma unroll
                for (int k = 0; k < 3; ++k) {
                    const int c8 = 12 * (w - 2) + 4 * k + g_, ws = (c8 * 43) >> 8, r6 = c8 - 6 * ws;
                    rl[k] = *(const u32x4*)(smem + LX_OFF(i) + (r6 >> 1) * 128 + ((2 * ws + (r6 & 1)) << 4));
                }
                if (lt == 1) {
                    const unsigned oE_ = ldsb + O_OFF + l15_ * 384 + ((g_ ^ (l15_ >> 1)) << 4), oO_ = oE_ ^ 64, aCA_ = ldsb + CT_OFF + ln_ * 16;
                    asm volatile(
            "s_waitcnt lgkmcnt(0)\n\t"
            "s_barrier\n\t"
            "s_nop 7\n\t"
            "ds_read_b128 v[184:187], v242\n\t"
            "ds_read_b128 v[188:191], v243\n\t"
            "ds_read_b128 v[192:195], v242 offset:128\n\t"
            "ds_read_b128 v[172:175], v243 offset:128\n\t"
            "ds_read_b128 v[196:199], v252\n\t"
            "v_lshlrev_b32 v152, 16, v200\n\t"
            "v_and_b32 v153, 0xffff0000, v200\n\t"
            "v_lshlrev_b32 v154, 16, v201\n\t"
            "v_and_b32 v155, 0xffff0000, v201\n\t"
            "v_lshlrev_b32 v156, 16, v202\n\t"
            "v_and_b32 v157, 0xffff0000, v202\n\t"
            "v_lshlrev_b32 v158, 16, v203\n\t"
            "v_and_b32 v159, 0xffff0000, v203\n\t"
            "v_lshlrev_b32 v160, 16, v204\n\t"
            "v_and_b32 v161, 0xffff0000, v204\n\t"
            "v_lshlrev_b32 v162, 16, v205\n\t"
            "v_and_b32 v163, 0xffff0000, v205\n\t"
            "v_lshlrev_b32 v164, 16, v206\n\t"
            "v_and_b32 v165, 0xffff0000, v206\n\t"
            "v_lshlrev_b32 v166, 16, v207\n\t"
            "v_and_b32 v167, 0xffff0000, v207\n\t"
            "v_lshlrev_b32 v176, 16, v208\n\t"
            "v_and_b32 v177, 0xffff0000, v208\n\t"
            "v_lshlrev_b32 v178, 16, v209\n\t"
            "v_and_b32 v179, 0xffff0000, v209\n\t"
            "v_lshlrev_b32 v180, 16, v210\n\t"
            "v_and_b32 v181, 0xffff0000, v210\n\t"
            "v_lshlrev_b32 v182, 16, v211\n\t"
            "v_and_b32 v183, 0xffff0000, v211\n\t"
            "v_lshlrev_b32 v144, 16, v212\n\t"
            "v_and_b32 v145, 0xffff0000, v212\n\t"
            "v_lshlrev_b32 v146, 16, v213\n\t"
            "v_and_b32 v147, 0xffff0000, v213\n\t"
            "v_lshlrev_b32 v148, 16, v214\n\t"
            "v_and_b32 v149, 0xffff0000, v214\n\t"
            "v_lshlrev_b32 v150, 16, v215\n\t"
            "v_and_b32 v151, 0xffff0000, v215\n\t"
            "v_lshlrev_b32 v168, 16, v216\n\t"
            "v_and_b32 v169, 0xffff0000, v216\n\t"
            "v_lshlrev_b32 v170, 16, v217\n\t"
            "v_and_b32 v171, 0xffff0000, v217\n\t"
            "s_bfe_u32 s57, s56, 0x20004\n\t"
            "s_lshl_b32 s58, s57, 3\n\t"
            "s_lshr_b32 s58, s55, s58\n\t"
            "s_lshl_b64 s[36:37], s[34:35], s58\n\t"
            "s_bitcmp1_b32 s57, 1\n\t"
            "s_cselect_b32 s58, 1177344, 0\n\t"
            "s_bitcmp1_b32 s57, 0\n\t"
            "s_cselect_b32 s57, 2304, 0\n\t"
            "s_add_u32 s57, s57, s58\n\t"
            "s_waitcnt lgkmcnt(3)\n\t"
            "v_mfma_f32_16x16x32_bf16 v[152:155], v[0:3], v[184:187], v[152:155]\n\t"
            "s_waitcnt lgkmcnt(0)\n\t"
            "s_mov_b64 exec, s[36:37]\n\t"
            "v_add_u32 v196, s57, v196\n\t"
            "s_mov_b64 exec, -1\n\t"
            "v_mfma_f32_16x16x32_bf16 v[152:155], v[4:7], v[188:191], v[152:155]\n\t"
            "v_mfma_f32_16x16x32_bf16 v[156:159], v[28:31], v[188:191], v[156:159]\n\t"
            "v_mfma_f32_16x16x32_bf16 v[156:159], v[24:27], v[184:187], v[156:159]\n\t"
            "v_mfma_f32_16x16x32_bf16 v[160:163], v[48:51], v[184:187], v[160:163]\n\t"
            "v_mfma_f32_16x16x32_bf16 v[160:163], v[52:55], v[188:191], v[160:163]\n\t"
            "v_mfma_f32_16x16x32_bf16 v[164:167], v[76:79], v[188:191], v[164:167]\n\t"
            "v_mfma_f32_16x16x32_bf16 v[164:167], v[72:75], v[184:187], v[164:167]\n\t"
            "v_mfma_f32_16x16x32_bf16 v[176:179], v[96:99], v[184:187], v[176:179]\n\t"
            "v_mfma_f32_16x16x32_bf16 v[176:179], v[100:103], v[188:191], v[176:179]\n\t"
            "v_mfma_f32_16x16x32_bf16 v[180:183], v[124:127], v[188:191], v[180:183]\n\t"
            "ds_read_b128 v[188:191], v243 offset:256\n\t"
            "v_mfma_f32_16x16x32_bf16 v[180:183], v[120:123], v[184:187], v[180:183]\n\t"
            "ds_read_b128 v[184:187], v242 offset:256\n\t"
            "v_mfma_f32_16x16x32_bf16 v[152:155], v[8:11], v[192:195], v[152:155]\n\t"
            "v_mfma_f32_16x16x32_bf16 v[152:155], v[12:15], v[172:175], v[152:155]\n\t"
            "v_mfma_f32_16x16x32_bf16 v[156:159], v[36:39], v[172:175], v[156:159]\n\t"
            "v_mfma_f32_16x16x32_bf16 v[156:159], v[32:35], v[192:195], v[156:159]\n\t"
            "v_mfma_f32_16x16x32_bf16 v[160:163], v[56:59], v[192:195], v[160:163]\n\t"
            "v_mfma_f32_16x16x32_bf16 v[160:163], v[60:63], v[172:175], v[160:163]\n\t"
            "v_mfma_f32_16x16x32_bf16 v[164:167], v[84:87], v[172:175], v[164:167]\n\t"
            "v_mfma_f32_16x16x32_bf16 v[164:167], v[80:83], v[192:195], v[164:167]\n\t"
            "v_mfma_f32_16x16x32_bf16 v[176:179], v[104:107], v[192:195], v[176:179]\n\t"
            "v_mfma_f32_16x16x32_bf16 v[176:179], v[108:111], v[172:175], v[176:179]\n\t"
            "v_mfma_f32_16x16x32_bf16 v[180:183], v[132:135], v[172:175], v[180:183]\n\t"
            "ds_read_b128 v[172:175], v243 offset:6144\n\t"
            "v_mfma_f32_16x16x32_bf16 v[180:183], v[128:131], v[192:195], v[180:183]\n\t"
            "ds_read_b128 v[192:195], v242 offset:6144\n\t"
            "s_waitcnt lgkmcnt(2)\n\t"
            "v_mfma_f32_16x16x32_bf16 v[152:155], v[16:19], v[184:187], v[152:155]\n\t"
            "v_mfma_f32_16x16x32_bf16 v[152:155], v[20:23], v[188:191], v[152:155]\n\t"
            "v_mfma_f32_16x16x32_bf16 v[156:159], v[44:47], v[188:191], v[156:159]\n\t"
            "v_mfma_f32_16x16x32_bf16 v[156:159], v[40:43], v[184:187], v[156:159]\n\t"
            "v_mfma_f32_16x16x32_bf16 v[160:163], v[64:67], v[184:187], v[160:163]\n\t"
            "v_mfma_f32_16x16x32_bf16 v[160:163], v[68:71], v[188:191], v[160:163]\n\t"
            "v_mfma_f32_16x16x32_bf16 v[164:167], v[92:95], v[188:191], v[164:167]\n\t"
            "v_mfma_f32_16x16x32_bf16 v[164:167], v[88:91], v[184:187], v[164:167]\n\t"
            "s_nop 1\n\t"
            "v_cvt_pk_bf16_f32 v152, v152, v153\n\t"
            "v_mfma_f32_16x16x32_bf16 v[176:179], v[112:115], v[184:187], v[176:179]\n\t"
            "v_cvt_pk_bf16_f32 v153, v154, v155\n\t"
            "v_mfma_f32_16x16x32_bf16 v[176:179], v[116:119], v[188:191], v[176:179]\n\t"
            "v_cvt_pk_bf16_f32 v154, v156, v157\n\t"
            "v_cvt_pk_bf16_f32 v155, v158, v159\n\t"
            "v_mfma_f32_16x16x32_bf16 v[180:183], v[140:143], v[188:191], v[180:183]\n\t"
            "ds_read_b128 v[188:191], v243 offset:6272\n\t"
            "v_cvt_pk_bf16_f32 v160, v160, v161\n\t"
            "v_mfma_f32_16x16x32_bf16 v[180:183], v[136:139], v[184:187], v[180:183]\n\t"
            "ds_read_b128 v[184:187], v242 offset:6272\n\t"
            "v_cvt_pk_bf16_f32 v161, v162, v163\n\t"
            "s_waitcnt lgkmcnt(2)\n\t"
            "v_mfma_f32_16x16x32_bf16 v[144:147], v[0:3], v[192:195], v[144:147]\n\t"
            "v_cvt_pk_bf16_f32 v176, v176, v177\n\t"
            "v_cvt_pk_bf16_f32 v177, v178, v179\n\t"
            "v_mfma_f32_16x16x32_bf16 v[144:147], v[4:7], v[172:175], v[144:147]\n\t"
            "v_cvt_pk_bf16_f32 v162, v164, v165\n\t"
            "v_cvt_pk_bf16_f32 v163, v166, v167\n\t"
            "v_mfma_f32_16x16x32_bf16 v[148:151], v[28:31], v[172:175], v[148:151]\n\t"
            "v_cvt_pk_bf16_f32 v178, v180, v181\n\t"
            "v_cvt_pk_bf16_f32 v179, v182, v183\n\t"
            "v_mfma_f32_16x16x32_bf16 v[148:151], v[24:27], v[192:195], v[148:151]\n\t"
            "v_lshlrev_b32 v156, 16, v218\n\t"
            "v_and_b32 v157, 0xffff0000, v218\n\t"
            "v_mfma_f32_16x16x32_bf16 v[168:171], v[48:51], v[192:195], v[168:171]\n\t"
            "v_lshlrev_b32 v158, 16, v219\n\t"
            "v_and_b32 v159, 0xffff0000, v219\n\t"
            "v_mfma_f32_16x16x32_bf16 v[168:171], v[52:55], v[172:175], v[168:171]\n\t"
            "v_lshlrev_b32 v164, 16, v220\n\t"
            "v_and_b32 v165, 0xffff0000, v220\n\t"
            "v_mfma_f32_16x16x32_bf16 v[156:159], v[76:79], v[172:175], v[156:159]\n\t"
            "v_lshlrev_b32 v166, 16, v221\n\t"
            "v_and_b32 v167, 0xffff0000, v221\n\t"
            "v_mfma_f32_16x16x32_bf16 v[156:159], v[72:75], v[192:195], v[156:159]\n\t"
            "v_lshlrev_b32 v180, 16, v222\n\t"
            "v_and_b32 v181, 0xffff0000, v222\n\t"
            "v_mfma_f32_16x16x32_bf16 v[164:167], v[96:99], v[192:195], v[164:167]\n\t"
            "v_lshlrev_b32 v182, 16, v223\n\t"
            "v_and_b32 v183, 0xffff0000, v223\n\t"
            "v_mfma_f32_16x16x32_bf16 v[164:167], v[100:103], v[172:175], v[164:167]\n\t"
            "buffer_store_dwordx4 v[152:155], v196, s[40:43], s45 offen sc1\n\t"
            "v_mfma_f32_16x16x32_bf16 v[180:183], v[124:127], v[172:175], v[180:183]\n\t"
            "ds_read_b128 v[172:175], v243 offset:6400\n\t"
            "v_lshlrev_b32 v152, 16, v224\n\t"
            "v_mfma_f32_16x16x32_bf16 v[180:183], v[120:123], v[192:195], v[180:183]\n\t"
            "ds_read_b128 v[192:195], v242 offset:6400\n\t"
            "buffer_store_dwordx4 v[160:163], v196, s[40:43], s45 offen offset:64 sc1\n\t"
            "s_waitcnt lgkmcnt(2)\n\t"
            "v_mfma_f32_16x16x32_bf16 v[144:147], v[8:11], v[184:187], v[144:147]\n\t"
            "v_and_b32 v153, 0xffff0000, v224\n\t"
            "v_lshlrev_b32 v154, 16, v225\n\t"
            "v_mfma_f32_16x16x32_bf16 v[144:147], v[12:15], v[188:191], v[144:147]\n\t"
            "buffer_store_dwordx4 v[176:179], v196, s[40:43], s45 offen offset:128 sc1\n\t"
            "v_mfma_f32_16x16x32_bf16 v[148:151], v[36:39], v[188:191], v[148:151]\n\t"
            "v_and_b32 v155, 0xffff0000, v225\n\t"
            "v_lshlrev_b32 v160, 16, v226\n\t"
            "v_mfma_f32_16x16x32_bf16 v[148:151], v[32:35], v[184:187], v[148:151]\n\t"
            "v_and_b32 v161, 0xffff0000, v226\n\t"
            "v_lshlrev_b32 v162, 16, v227\n\t"
            "v_mfma_f32_16x16x32_bf16 v[168:171], v[56:59], v[184:187], v[168:171]\n\t"
            "v_and_b32 v163, 0xffff0000, v227\n\t"
            "v_lshlrev_b32 v176, 16, v228\n\t"
            "v_mfma_f32_16x16x32_bf16 v[168:171], v[60:63], v[188:191], v[168:171]\n\t"
            "v_and_b32 v177, 0xffff0000, v228\n\t"
            "v_lshlrev_b32 v178, 16, v229\n\t"
            "v_mfma_f32_16x16x32_bf16 v[156:159], v[84:87], v[188:191], v[156:159]\n\t"
            "v_and_b32 v179, 0xffff0000, v229\n\t"
            "v_mfma_f32_16x16x32_bf16 v[156:159], v[80:83], v[184:187], v[156:159]\n\t"
            "v_mfma_f32_16x16x32_bf16 v[164:167], v[104:107], v[184:187], v[164:167]\n\t"
            "v_mfma_f32_16x16x32_bf16 v[164:167], v[108:111], v[188:191], v[164:167]\n\t"
            "v_mfma_f32_16x16x32_bf16 v[180:183], v[132:135], v[188:191], v[180:183]\n\t"
            "ds_read_b128 v[188:191], v243 offset:12288\n\t"
            "v_mfma_f32_16x16x32_bf16 v[180:183], v[128:131], v[184:187], v[180:183]\n\t"
            "ds_read_b128 v[184:187], v242 offset:12288\n\t"
            "s_waitcnt lgkmcnt(2)\n\t"
            "v_mfma_f32_16x16x32_bf16 v[144:147], v[16:19], v[192:195], v[144:147]\n\t"
            "v_mfma_f32_16x16x32_bf16 v[144:147], v[20:23], v[172:175], v[144:147]\n\t"
            "v_mfma_f32_16x16x32_bf16 v[168:171], v[64:67], v[192:195], v[168:171]\n\t"
            "v_mfma_f32_16x16x32_bf16 v[168:171], v[68:71], v[172:175], v[168:171]\n\t"
            "v_mfma_f32_16x16x32_bf16 v[164:167], v[112:115], v[192:195], v[164:167]\n\t"
            "v_mfma_f32_16x16x32_bf16 v[164:167], v[116:119], v[172:175], v[164:167]\n\t"
            "v_mfma_f32_16x16x32_bf16 v[148:151], v[44:47], v[172:175], v[148:151]\n\t"
            "v_mfma_f32_16x16x32_bf16 v[148:151], v[40:43], v[192:195], v[148:151]\n\t"
            "s_nop 1\n\t"
            "v_cvt_pk_bf16_f32 v144, v144, v145\n\t"
            "v_mfma_f32_16x16x32_bf16 v[156:159], v[92:95], v[172:175], v[156:159]\n\t"
            "v_cvt_pk_bf16_f32 v145, v146, v147\n\t"
            "v_mfma_f32_16x16x32_bf16 v[156:159], v[88:91], v[192:195], v[156:159]\n\t"
            "v_cvt_pk_bf16_f32 v168, v168, v169\n\t"
            "v_cvt_pk_bf16_f32 v169, v170, v171\n\t"
            "v_mfma_f32_16x16x32_bf16 v[180:183], v[140:143], v[172:175], v[180:183]\n\t"
            "ds_read_b128 v[172:175], v243 offset:12416\n\t"
            "v_cvt_pk_bf16_f32 v164, v164, v165\n\t"
            "v_mfma_f32_16x16x32_bf16 v[180:183], v[136:139], v[192:195], v[180:183]\n\t"
            "ds_read_b128 v[192:195], v242 offset:12416\n\t"
            "v_cvt_pk_bf16_f32 v165, v166, v167\n\t"
            "s_waitcnt lgkmcnt(2)\n\t"
            "v_mfma_f32_16x16x32_bf16 v[152:155], v[0:3], v[184:187], v[152:155]\n\t"
            "v_cvt_pk_bf16_f32 v146, v148, v149\n\t"
            "v_cvt_pk_bf16_f32 v147, v150, v151\n\t"
            "v_mfma_f32_16x16x32_bf16 v[152:155], v[4:7], v[188:191], v[152:155]\n\t"
            "v_cvt_pk_bf16_f32 v170, v156, v157\n\t"
            "v_cvt_pk_bf16_f32 v171, v158, v159\n\t"
            "v_mfma_f32_16x16x32_bf16 v[160:163], v[28:31], v[188:191], v[160:163]\n\t"
            "v_cvt_pk_bf16_f32 v166, v180, v181\n\t"
            "v_cvt_pk_bf16_f32 v167, v182, v183\n\t"
            "v_mfma_f32_16x16x32_bf16 v[160:163], v[24:27], v[184:187], v[160:163]\n\t"
            "v_lshlrev_b32 v148, 16, v230\n\t"
            "v_and_b32 v149, 0xffff0000, v230\n\t"
            "v_mfma_f32_16x16x32_bf16 v[176:179], v[48:51], v[184:187], v[176:179]\n\t"
            "v_lshlrev_b32 v150, 16, v231\n\t"
            "v_and_b32 v151, 0xffff0000, v231\n\t"
            "v_mfma_f32_16x16x32_bf16 v[176:179], v[52:55], v[188:191], v[176:179]\n\t"
            "v_lshlrev_b32 v156, 16, v232\n\t"
            "v_and_b32 v157, 0xffff0000, v232\n\t"
            "v_mfma_f32_16x16x32_bf16 v[148:151], v[76:79], v[188:191], v[148:151]\n\t"
            "v_lshlrev_b32 v158, 16, v233\n\t"
            "v_and_b32 v159, 0xffff0000, v233\n\t"
            "v_mfma_f32_16x16x32_bf16 v[148:151], v[72:75], v[184:187], v[148:151]\n\t"
            "v_lshlrev_b32 v180, 16, v234\n\t"
            "v_and_b32 v181, 0xffff0000, v234\n\t"
            "v_mfma_f32_16x16x32_bf16 v[156:159], v[96:99], v[184:187], v[156:159]\n\t"
            "v_lshlrev_b32 v182, 16, v235\n\t"
            "v_and_b32 v183, 0xffff0000, v235\n\t"
            "v_mfma_f32_16x16x32_bf16 v[156:159], v[100:103], v[188:191], v[156:159]\n\t"
            "buffer_store_dwordx4 v[144:147], v197, s[40:43], s45 offen sc1\n\t"
            "v_mfma_f32_16x16x32_bf16 v[180:183], v[124:127], v[188:191], v[180:183]\n\t"
            "ds_read_b128 v[188:191], v243 offset:12544\n\t"
            "v_mfma_f32_16x16x32_bf16 v[180:183], v[120:123], v[184:187], v[180:183]\n\t"
            "ds_read_b128 v[184:187], v242 offset:12544\n\t"
            "buffer_store_dwordx4 v[168:171], v197, s[40:43], s45 offen offset:64 sc1\n\t"
            "s_waitcnt lgkmcnt(2)\n\t"
            "v_mfma_f32_16x16x32_bf16 v[152:155], v[8:11], v[192:195], v[152:155]\n\t"
            "v_mfma_f32_16x16x32_bf16 v[152:155], v[12:15], v[172:175], v[152:155]\n\t"
            "buffer_store_dwordx4 v[164:167], v197, s[40:43], s45 offen offset:128 sc1\n\t"
            "v_mfma_f32_16x16x32_bf16 v[160:163], v[36:39], v[172:175], v[160:163]\n\t"
            "v_mfma_f32_16x16x32_bf16 v[160:163], v[32:35], v[192:195], v[160:163]\n\t"
            "v_mfma_f32_16x16x32_bf16 v[176:179], v[56:59], v[192:195], v[176:179]\n\t"
            "v_mfma_f32_16x16x32_bf16 v[176:179], v[60:63], v[172:175], v[176:179]\n\t"
            "v_mfma_f32_16x16x32_bf16 v[148:151], v[84:87], v[172:175], v[148:151]\n\t"
            "v_mfma_f32_16x16x32_bf16 v[148:151], v[80:83], v[192:195], v[148:151]\n\t"
            "v_mfma_f32_16x16x32_bf16 v[156:159], v[104:107], v[192:195], v[156:159]\n\t"
            "v_mfma_f32_16x16x32_bf16 v[156:159], v[108:111], v[172:175], v[156:159]\n\t"
            "s_waitcnt lgkmcnt(0)\n\t"
            "v_mfma_f32_16x16x32_bf16 v[152:155], v[16:19], v[184:187], v[152:155]\n\t"
            "v_mfma_f32_16x16x32_bf16 v[152:155], v[20:23], v[188:191], v[152:155]\n\t"
            "v_mfma_f32_16x16x32_bf16 v[160:163], v[44:47], v[188:191], v[160:163]\n\t"
            "v_mfma_f32_16x16x32_bf16 v[160:163], v[40:43], v[184:187], v[160:163]\n\t"
            "v_mfma_f32_16x16x32_bf16 v[180:183], v[132:135], v[172:175], v[180:183]\n\t"
            "v_mfma_f32_16x16x32_bf16 v[180:183], v[128:131], v[192:195], v[180:183]\n\t"
            "v_mfma_f32_16x16x32_bf16 v[176:179], v[64:67], v[184:187], v[176:179]\n\t"
            "v_mfma_f32_16x16x32_bf16 v[176:179], v[68:71], v[188:191], v[176:179]\n\t"
            "s_nop 1\n\t"
            "v_cvt_pk_bf16_f32 v152, v152, v153\n\t"
            "v_mfma_f32_16x16x32_bf16 v[148:151], v[92:95], v[188:191], v[148:151]\n\t"
            "v_cvt_pk_bf16_f32 v153, v154, v155\n\t"
            "v_mfma_f32_16x16x32_bf16 v[148:151], v[88:91], v[184:187], v[148:151]\n\t"
            "v_cvt_pk_bf16_f32 v154, v160, v161\n\t"
            "v_cvt_pk_bf16_f32 v155, v162, v163\n\t"
            "v_mfma_f32_16x16x32_bf16 v[156:159], v[112:115], v[184:187], v[156:159]\n\t"
            "v_cvt_pk_bf16_f32 v176, v176, v177\n\t"
            "v_cvt_pk_bf16_f32 v177, v178, v179\n\t"
            "v_mfma_f32_16x16x32_bf16 v[156:159], v[116:119], v[188:191], v[156:159]\n\t"
            "buffer_store_dwordx4 v[152:155], v198, s[40:43], s45 offen sc1\n\t"
            "v_mfma_f32_16x16x32_bf16 v[180:183], v[140:143], v[188:191], v[180:183]\n\t"
            "v_cvt_pk_bf16_f32 v178, v148, v149\n\t"
            "v_cvt_pk_bf16_f32 v179, v150, v151\n\t"
            "v_mfma_f32_16x16x32_bf16 v[180:183], v[136:139], v[184:187], v[180:183]\n\t"
            "buffer_store_dwordx4 v[176:179], v198, s[40:43], s45 offen offset:64 sc1\n\t"
            "s_nop 1\n\t"
            "v_cvt_pk_bf16_f32 v156, v156, v157\n\t"
            "v_cvt_pk_bf16_f32 v157, v158, v159\n\t"
            "s_nop 2\n\t"
            "v_cvt_pk_bf16_f32 v158, v180, v181\n\t"
            "v_cvt_pk_bf16_f32 v159, v182, v183\n\t"
            "buffer_store_dwordx4 v[156:159], v198, s[40:43], s45 offen offset:128 sc1\n\t"
            "s_nop 7\n\t"
                    :: "{v[0:3]}"(W[0]), "{v[4:7]}"(W[1]), "{v[8:11]}"(W[2]), "{v[12:15]}"(W[3]), "{v[16:19]}"(W[4]), "{v[20:23]}"(W[5]), "{v[24:27]}"(W[6]), "{v[28:31]}"(W[7]), "{v[32:35]}"(W[8]), "{v[36:39]}"(W[9]), "{v[40:43]}"(W[10]), "{v[44:47]}"(W[11]), "{v[48:51]}"(W[12]), "{v[52:55]}"(W[13]), "{v[56:59]}"(W[14]), "{v[60:63]}"(W[15]), "{v[64:67]}"(W[16]), "{v[68:71]}"(W[17]), "{v[72:75]}"(W[18]), "{v[76:79]}"(W[19]), "{v[80:83]}"(W[20]), "{v[84:87]}"(W[21]), "{v[88:91]}"(W[22]), "{v[92:95]}"(W[23]), "{v[96:99]}"(W[24]), "{v[100:103]}"(W[25]), "{v[104:107]}"(W[26]), "{v[108:111]}"(W[27]), "{v[112:115]}"(W[28]), "{v[116:119]}"(W[29]), "{v[120:123]}"(W[30]), "{v[124:127]}"(W[31]), "{v[128:131]}"(W[32]), "{v[132:135]}"(W[33]), "{v[136:139]}"(W[34]), "{v[140:143]}"(W[35]), "{v[200:203]}"(xl[0]), "{v[204:207]}"(xl[1]), "{v[208:211]}"(xl[2]), "{v[212:215]}"(xl[3]), "{v[216:219]}"(xl[4]), "{v[220:223]}"(xl[5]), "{v[224:227]}"(xl[6]), "{v[228:231]}"(xl[7]), "{v[232:235]}"(xl[8]), "{v242}"(oE_), "{v243}"(oO_), "{v252}"(aCA_),
                       "{s[40:43]}"(orr), "{s45}"(t45), "{s56}"(t56), "{s55}"(CROW & 0x0F0F0F0Fu), "{s[34:35]}"(0x0001000100010001ull)
                    : "v144", "v145", "v146", "v147", "v148", "v149", "v150", "v151", "v152", "v153", "v154", "v155", "v156", "v157", "v158", "v159", "v160", "v161", "v162", "v163", "v164", "v165", "v166", "v167", "v168", "v169", "v170", "v171", "v172", "v173", "v174", "v175", "v176", "v177", "v178", "v179", "v180", "v181", "v182", "v183", "v184", "v185", "v186", "v187", "v188", "v189", "v190", "v191", "v192", "v193", "v194", "v195", "v196", "v197", "v198", "v199", "s36", "s37", "s57", "s58", "scc", "memory");
                }
                f32x4 acc[6];
#pragma unroll
                for (int ks = 0; ks < 6; ++ks) {
                    const bf16x8 of = *(const bf16x8*)(smem + ((ks & 1) ? (lE ^ 64) : lE) + (ks >> 1) * 128);
#pragma unroll
                    for (int j = 0; j < 6; ++j) acc[j] = MFMA32(W[j * 6 + ks], of, ks == 0 ? ZERO4 : acc[j]);
                }
#pragma unroll
                for (int k = 0; k < 3; ++k) {
                    const f32x4 a = acc[2 * k], b = acc[2 * k + 1];
                    const u32x4 ov = {pk2(a[0] + bflo(rl[k][0]), a[1] + bfhi(rl[k][0])), pk2(a[2] + bflo(rl[k][1]), a[3] + bfhi(rl[k][1])),
                                      pk2(b[0] + bflo(rl[k][2]), b[1] + bfhi(rl[k][2])), pk2(b[2] + bflo(rl[k][3]), b[3] + bfhi(rl[k][3]))};
                    __builtin_amdgcn_raw_buffer_store_b128(ov, orsrc, o + 64 * k, 0, SC1);
                }
                SB();
            }
        }
        __builtin_amdgcn_endpgm();
    }
}

extern "C" void kernel_launch(void* const* d_in, const int* in_sizes, int n_in,
                              void* d_out, int out_size, void* d_ws, size_t ws_size,
                              hipStream_t stream) {
    const unsigned short* x = (const unsigned short*)d_in[0];
    const unsigned short* dw = (const unsigned short*)d_in[1];
    const unsigned short* wqkv = (const unsigned short*)d_in[2];
    const unsigned short* wo = (const unsigned short*)d_in[3];
    unsigned short* out = (unsigned short*)d_out;
    u32x4* wf = (u32x4*)d_ws;
    win_attn_kernel<<<GRID, 512, 0, stream>>>(x, dw, wqkv, wo, wf, out);
}
```

```cpp
#include <hip/hip_runtime.h>
#include <hip/hip_bf16.h>
#include <stdint.h>

typedef short bf16x8 __attribute__((ext_vector_type(8)));
typedef float f32x4 __attribute__((ext_vector_type(4)));
typedef float f32x2 __attribute__((ext_vector_type(2)));
typedef unsigned u32x2 __attribute__((ext_vector_type(2)));
typedef unsigned u32x4 __attribute__((ext_vector_type(4)));

constexpr int IMG = 511;
constexpr int CH = 192;
constexpr int NWX = 73;
constexpr int NWIN = NWX * NWX;
constexpr int GRID = 256;
constexpr int ROWBYTES = IMG * CH * 2;
constexpr int XBIAS = (IMG + 1) * CH * 2;
constexpr unsigned OOB = 0x40000000u;

constexpr int FLAG_OFF = 7856;
constexpr int X_OFF = 32768;
constexpr int X1_OFF = 53632;
constexpr int O_OFF = 74496;
constexpr int V_OFF = 92928;
constexpr int KQL_OFF = 111360;
constexpr int OL_OFF = 128256;
constexpr int AT_OFF = 137472;
constexpr int CT_OFF = AT_OFF + 19584;
constexpr int RS_OFF = CT_OFF + 4096;
constexpr int RSX_OFF = 162816;
constexpr int WL_OFF = RS_OFF + 2304 + 64;
constexpr int LDS_BYTES = WL_OFF + 128;
#define RS_BASE (RS_OFF + wave * 384)
constexpr int STL_OFF = O_OFF + 12288;
constexpr int DW_OFF = O_OFF + 8448;
#define LX_OFF(i) ((i) < 2 ? CT_OFF + 1024 + (i) * 384 : KQL_OFF + ((i) - 2) * 800)
constexpr int PF_OFF = CT_OFF + 2048;
constexpr int PSLEEP = 4;
#define LB_OFF (w < 3 ? V_OFF + w * 13952 : X1_OFF)
#define LPOS(wc) (((wc) & 1) ? 0 : 6)
constexpr unsigned CROW = 0x060a0230u;
constexpr unsigned CLANE = 0x00132C3Fu;

constexpr unsigned long long TOKMAP[6] = {0x20001c190f2a2202ull, 0x052517121f061a13ull, 0x181e08151d0b260cull, 0x1b2b2903102d0901ull, 0x04162e0e2824142cull, 0x110a072127232f0dull};
constexpr unsigned long long ROWMAP[8] = {0x08321b240f1c0006ull, 0x23310e2a09170d14ull, 0x210c2f030a273418ull, 0x1d2b11221f043739ull, 0x3234363a103e192dull, 0x291a20022c3f1605ull, 0x303a3c2628122e15ull, 0x39253d1e01070b13ull};

constexpr int WF_BYTES = 8 * 36 * 1024;
constexpr int FLG_OFF = WF_BYTES;
constexpr int ARR_OFF = FLG_OFF + 4096;
constexpr int EPO_OFF = ARR_OFF + 2048;
constexpr int WS_BYTES = EPO_OFF + 128;
constexpr int SC1 = 16;

#define MFMA32(a, b, c) __builtin_amdgcn_mfma_f32_16x16x32_bf16((a), (b), (c), 0, 0, 0)
#define BC8(v) __builtin_bit_cast(bf16x8, (v))
#define BAR() asm volatile("s_waitcnt lgkmcnt(0)\n\ts_barrier" ::: "memory")
#define SB() __builtin_amdgcn_sched_barrier(0)
#define OPQ(x) asm volatile("" : "+v"(x))
#define ZERO4 ((f32x4){0.f, 0.f, 0.f, 0.f})

typedef __bf16 nbf16x2 __attribute__((ext_vector_type(2)));
__device__ __forceinline__ unsigned pk2(float a, float b) {
    f32x2 v = {a, b};
    nbf16x2 r = __builtin_convertvector(v, nbf16x2);
    return __builtin_bit_cast(unsigned, r);
}
__device__ __forceinline__ float bflo(unsigned u) { return __uint_as_float(u << 16); }
__device__ __forceinline__ float bfhi(unsigned u) { return __uint_as_float(u & 0xffff0000u); }

__device__ __forceinline__ float bf2f(unsigned short v) { return __uint_as_float((unsigned)v << 16); }
__device__ __forceinline__ unsigned byte_of(unsigned long long a, unsigned long long b, int i) {
    return (unsigned)(((i & 8) ? b : a) >> (8 * (i & 7))) & 0xFFu;
}
#define M24(a, b) ((unsigned)__mul24((int)(a), (int)(b)))
__device__ __forceinline__ void seq_to_win(int seq, int& wy, int& wx) {
    const bool last = seq >= 5110;
    const int s = last ? 35 : M24(seq, 28729) >> 22, r = seq - M24(146, s);
    const int kx = last ? M24(r, 171) >> 10 : r >> 2, pos = r - M24(last ? 6 : 4, kx);
    const bool low = last && pos >= (kx == 36 ? 2 : 4);
    wy = low ? 72 : 2 * s + (pos & 1);
    wx = 2 * kx + (low ? (kx == 36 ? 0 : pos - 4) : pos >> 1);
}
__device__ __forceinline__ int win_to_seq(int wy, int wx) {
    const int kx = wx >> 1;
    return M24(146, wy == 72 ? 35 : wy >> 1) + M24(wy >= 70 ? 6 : 4, kx) + (wy == 72 ? (kx == 36 ? 2 : 4 + (wx & 1)) : 2 * (wx & 1) + (wy & 1));
}
__device__ __forceinline__ int block_of(int wy, int wx) { return wy >= 70 ? 1295 + (wx & ~1) + (wy == 72) : M24(37, wy >> 1) + (wx >> 1); }
__device__ __forceinline__ void block_pos(int b, int& ky, int& kx) {
    const int s = M24(b, 1772) >> 16, l = b - 1295;
    kx = l >= 0 ? l >> 1 : b - M24(37, s);
    ky = l >= 0 ? 70 + 2 * (l & 1) : 2 * s;
}
__device__ __forceinline__ unsigned win_org(int wy, int wx) { return M24(wy, 7 * ROWBYTES) + M24(wx, 7 * CH * 2); }
__device__ __forceinline__ unsigned wl_entry(int wy, int wx) {
    return win_org(wy, wx) | (wy == 0 ? 0x08000000u : 0u) | (wx == 0 ? 0x10000000u : 0u) |
           (wx == NWX - 1 ? 0x20000000u : 0u) | ((unsigned)(wx & 1) << 30) | ((unsigned)(wy & 1) << 31);
}
__device__ __forceinline__ unsigned dma_off(int lane) {
    const int col = (lane * 43) >> 8;
    return (unsigned)(col * (CH * 2 - 96) + 16 * lane) | (col == 0 ? 0x10000000u : 0u) | (col == 8 ? 0x20000000u : 0u);
}

#if defined(__HIP_DEVICE_COMPILE__)
#define NOPK __attribute__((target("no-packed-fp32-ops")))
#else
#define NOPK
#endif
__global__ __launch_bounds__(512, 2) NOPK void win_attn_kernel(const unsigned short* __restrict__ x,
                                                          const unsigned short* __restrict__ dw, const unsigned short* __restrict__ wqkv,
                                                          const unsigned short* __restrict__ wo, u32x4* wf,
                                                          unsigned short* __restrict__ out) {
    __shared__ __attribute__((aligned(16))) char smem[LDS_BYTES];

    asm volatile("wk_code_begin:");
    const int tid = threadIdx.x;
    const int wave = __builtin_amdgcn_readfirstlane(tid >> 6);
    const int w = wave & 3;
    const unsigned ldsb = (unsigned)(size_t)smem;
    {
        unsigned long long pc;
        unsigned len, rel;
        asm volatile("s_getpc_b64 %0\n.Lwk_here%=:\n\ts_mov_b32 %1, wk_code_end-wk_code_begin\n\ts_mov_b32 %2, .Lwk_here%=-wk_code_begin" : "=s"(pc), "=s"(len), "=s"(rel));
        pc -= rel;
        const u32x4 cr = {(unsigned)pc, (unsigned)(pc >> 32) & 0xffffu, len, 0x00020000u};
        asm volatile("s_mov_b32 m0, %0\n\ts_nop 0\n\tbuffer_load_dwordx4 %1, %2, %3 offen lds"
                     :: "s"(ldsb + X1_OFF + 14336 + w * 1024), "v"((tid & 63) * 16), "s"(cr), "s"((wave * 32 + (blockIdx.x >> 3)) * 1024) : "m0", "memory");
    }
    *(unsigned*)(smem + PF_OFF + tid * 4) = 0x9E3779B9u * (tid + 1);
    *(unsigned*)(smem + (tid < 192 ? AT_OFF + 18816 + tid * 4 : FLAG_OFF + (tid & 3) * 4)) = 0u;
    BAR();
    const unsigned flg = FLG_OFF + (blockIdx.x & 7) * 512;
    asm volatile("" :: "s"(x), "s"(dw), "s"(wqkv), "s"(wo), "s"(wf));
    const auto wrsrc = __builtin_amdgcn_make_buffer_rsrc((void*)wf, 0, WS_BYTES, 0x00020000);
    unsigned tag_lo = 0, tag_hi = 0;
#define TAG_READY                                                                                  \
    {                                                                                              \
        unsigned long long tag_;                                                                   \
        asm volatile("s_load_dwordx2 %0, %1, %2\n\ts_waitcnt lgkmcnt(0)" : "=s"(tag_) : "s"(wf), "n"(EPO_OFF) : "memory"); \
        tag_lo = (unsigned)tag_ + 1u; tag_hi = (unsigned)(tag_ >> 32);                             \
    }
    const int pb = blockIdx.x;
    if (pb < 36) {
        const int lane = tid & 63, n = lane & 15, g = lane >> 4;
        const bool isO = wave >= 6;
        const int fr = isO ? 216 + 2 * pb + (wave - 6) : 6 * pb + wave;
        const int tile = fr / 6, ks = fr - 6 * tile, tw = tile / 6, mdt = tile - 6 * tw;
        const int col = isO ? 96 * (tw - 6) + 32 * (mdt >> 1) + 8 * (n >> 2) + 4 * (mdt & 1) + (n & 3)
                            : (tw < 4 ? (mdt < 3 ? 192 : -48) + 48 * tw : 96 * tw) + 16 * mdt + n;
        const int rb = isO ? 384 : 1152;
        const auto rr = __builtin_amdgcn_make_buffer_rsrc((void*)(isO ? wo : wqkv), 0, 192 * rb, 0x00020000);
        const int vo = (48 * (g >> 1) + 8 * (g & 1)) * rb + 2 * col;
        float v[8];
#pragma unroll
        for (int j = 0; j < 8; ++j) v[j] = bf2f(__builtin_amdgcn_raw_buffer_load_b16(rr, vo, (96 * (ks & 1) + 16 * (ks >> 1) + j) * rb, 0));
        TAG_READY
        float* red = (float*)smem;
        red[n * 36 + wave * 4 + g] = ((v[0] + v[1]) + (v[2] + v[3])) + ((v[4] + v[5]) + (v[6] + v[7]));
        BAR();
        float mean = 0.f;
#pragma unroll
        for (int i = 0; i < 6; ++i) {
            const f32x4 p = *(const f32x4*)(red + n * 36 + 4 * i);
            mean += (p[0] + p[1]) + (p[2] + p[3]);
        }
        mean = isO ? 0.f : mean * (1.f / 192.f);
        u32x4 o;
#pragma unroll
        for (int j = 0; j < 4; ++j) o[j] = pk2(v[2 * j] - mean, v[2 * j + 1] - mean);
        __builtin_amdgcn_raw_buffer_store_b128(o, wrsrc, lane * 16, fr * 1024, SC1);
        asm volatile("s_waitcnt vmcnt(0)" ::: "memory");
        BAR();
        if (tid < 8) __builtin_amdgcn_raw_buffer_store_b64((u32x2){tag_lo, tag_hi}, wrsrc, FLG_OFF + tid * 512 + pb * 8, 0, SC1);
    }
    const int xi = blockIdx.x >> 3, nshort = (int)(0x0506060606060606ull >> (8 * (blockIdx.x & 7))) & 63;
    const int start = 672 * (blockIdx.x & 7) - ((int)(0x2a241e18120c0600ull >> (8 * (blockIdx.x & 7))) & 63) + 21 * xi - (xi < nshort ? xi : nshort);
    const int cnt = xi < nshort ? 20 : 21;

    const auto xrsrc = __builtin_amdgcn_make_buffer_rsrc((void*)((const char*)x - XBIAS), 0, IMG * IMG * CH * 2 + XBIAS, 0x00020000);
    bf16x8 W[36];
#define POLL_ISSUE                                                                                  \
    asm volatile("s_mov_b32 m0, %0\n\ts_mov_b64 exec, 0x3ffff\n\tbuffer_load_dwordx4 %1, %2, %3 offen sc1 lds\n\ts_mov_b64 exec, -1" \
                 :: "s"(ldsb + PF_OFF), "v"(lane * 16), "s"(wrsrc), "s"(flg) : "m0", "memory");
#define POLL_W                                                                                      \
    {                                                                                               \
        const int fi_ = lane < 36 ? lane : 0;                                                       \
        for (int it_ = 0; it_ < 100000; ++it_) {                                                    \
            u32x2 f_;                                                                               \
            if (it_ < 48) asm volatile("ds_read_b64 %0, %1\n\ts_waitcnt lgkmcnt(0)" : "=v"(f_) : "v"(ldsb + PF_OFF + fi_ * 8) : "memory"); \
            else f_ = __builtin_amdgcn_raw_buffer_load_b64(wrsrc, fi_ * 8, flg, SC1);        \
            if (__builtin_amdgcn_ballot_w64(f_[0] != tag_lo || f_[1] != tag_hi) == 0ull) break;     \
            if (it_ < 47 && wave == 0) POLL_ISSUE                                                              \
            __builtin_amdgcn_s_sleep(PSLEEP);                                                       \
        }                                                                                           \
        *(u32x2*)(smem + PF_OFF + 512) = (u32x2){tag_lo, tag_hi};                                   \
        __builtin_amdgcn_fence(__ATOMIC_ACQUIRE, "wavefront");                                      \
        SB();                                                                                       \
    }
#define LANE_SETUP                                                                                 \
    int lane;                                                                                      \
    asm volatile("v_mbcnt_lo_u32_b32 %0, -1, 0\n\tv_mbcnt_hi_u32_b32 %0, -1, %0" : "=v"(lane));    \
    __builtin_assume(lane >= 0 && lane < 64);                                                      \
    const int g = lane >> 4, l15 = lane & 15, sw = l15 >> 1;                                       \
    int fE = X_OFF + l15 * 384 + ((g ^ sw) << 4);                                                  \
    OPQ(fE);

#define DMA_SLICE(en)                                                                              \
        {                                                                                          \
            const unsigned vo_ = dma_off(lane) & (0x401fffffu | ((en) & 0x38000000u));             \
            const unsigned so_ = ((en) & 0x7ffffffu) + 96 * w;                                     \
            _Pragma("unroll") for (int r = 0; r < 9; ++r) {                                        \
                unsigned m0_ = ldsb + w * 8192 + r * 872;                                          \
                unsigned sr_ = (r == 0 && ((en) & 0x08000000u)) ? OOB : so_ + r * ROWBYTES;        \
                asm volatile("s_mov_b32 m0, %0\n\ts_mov_b32 exec_hi, 0x3fffff\n\tbuffer_load_dwordx4 %1, %2, %3 offen lds\n\ts_mov_b32 exec_hi, -1" \
                             :: "s"(m0_), "v"(vo_), "s"(xrsrc), "s"(sr_) : "m0", "memory");        \
            }                                                                                      \
        }
#define XPAT(r) (((r) < 48 ? (r) : 48) * 384 + (((2 * w) ^ (((r) >> 1) & 7)) << 4))
#define SPAT(r) ((w >> 1) * 1024 + (w & 1) * 8 + (r) * 16)
#define CONV_CONSTS(var) CONV_ROWS(var) CONV_OPS
#define CONV_ROWS(var) \
            const unsigned long long rlo = g == 0 ? ROWMAP[0] : (g == 1 ? ROWMAP[2] : (g == 2 ? ROWMAP[4] : ROWMAP[6])); \
            const unsigned long long rhi = g == 0 ? ROWMAP[1] : (g == 1 ? ROWMAP[3] : (g == 2 ? ROWMAP[5] : ROWMAP[7])); \
            int xrow = byte_of(rlo, rhi, l15); \
            if (lane == (int)((CLANE >> (8 * (var))) & 0xFF)) xrow = 48; \
            if (lane == 63) xrow = (CROW >> (8 * (var))) & 0xFF; \
            const unsigned pXw_ = XPAT(xrow); \
            const unsigned pSTw_ = (w >> 1) * 1024 + (w & 1) * 8 + xrow * 16; \
            const int oy = (0x5320 >> (4 * (l15 >> 2))) & 15, ox = (0x5320 >> (4 * (l15 & 3))) & 15; \
            const unsigned aB_ = ldsb + w * 8192 + (oy + (g >> 1)) * 872 + (ox + 2 * (g & 1)) * 96;
#define CONV_OPS \
            const int dyi = g - (l15 >> 3); \
            const unsigned aA_ = ldsb + AT_OFF + (l15 & 3) * 16 + \
                                 ((dyi < 0 || dyi > 2) ? 18816 : (dyi == 0 ? 0 : (dyi == 1 ? 6208 : 12544)) + ((l15 >> 2) & 1) * 3072 + w * 768); \
            const unsigned idx_ = (l15 & 3) * 0x5555u; \
            const unsigned se = (l15 == 4 * g) ? 0x3F803F80u : 0u; \
            const u32x4 sel4 = {se, se, se, se};

    unsigned en0, en1;
    {
        LANE_SETUP
        const int px = (lane * 43) >> 8;

        int wy, wx;
        seq_to_win(start + (lane < cnt ? lane : cnt - 1), wy, wx);
        const unsigned en = wl_entry(wy, wx);
        en0 = __builtin_amdgcn_readlane(en, 0);
        en1 = __builtin_amdgcn_readlane(en, 1);
        unsigned cB, cX, cS;
        if (wave >= 4) {
            const int bk = block_of(wy, wx);
            const int key0 = __builtin_amdgcn_readlane(bk, 0), nblk = __builtin_amdgcn_readlane(bk, 63) - key0 + 1;
            const int b = l15 < nblk ? l15 : 0;
            int ky, kx;
            block_pos(key0 + b, ky, kx);
            const unsigned org = l15 < nblk ? (win_org(ky, 2 * kx) + (5 * ROWBYTES + 5 * CH * 2 + XBIAS)) | (kx == 36 ? OOB : 0u) : OOB + 0x8000000u;
            const unsigned lo = (px & 3) * (CH * 2) + (lane - 6 * px) * 16, keep = (px & 2) ? ~0u : ~OOB;
#pragma unroll
            for (int p = 0; p < 4; ++p) {
                if (p < 3 || nblk > 6) {
                    const unsigned oa = __builtin_amdgcn_readlane(org, 2 * p), ob = __builtin_amdgcn_readlane(org, 2 * p + 1);
                    const unsigned vo_ = ((lane < 24 ? oa : ob) & keep) + lo;
#pragma unroll
                    for (int r = 0; r < 4; ++r) {
                        unsigned m0_ = ldsb + LB_OFF + (4 * p + r) * 872;
                        asm volatile("s_mov_b32 m0, %0\n\ts_mov_b32 exec_hi, 0xffff\n\tbuffer_load_dwordx4 %1, %2, %3 offen lds\n\ts_mov_b32 exec_hi, -1"
                                     :: "s"(m0_), "v"(vo_), "s"(xrsrc), "s"(96 * w + r * ROWBYTES) : "m0", "memory");
                    }
                }
            }
            SB();
            const int ly = ky + (g >> 1), lx = 2 * kx + (g & 1), li = win_to_seq(ly, lx) - start;
            const bool ok = ly < NWX && lx < NWX && li >= 0 && li < cnt && l15 < nblk;
            const int row = ok ? li : 47;
            cB = ldsb + LB_OFF + (4 * (b >> 1) + (g >> 1)) * 872 + (b & 1) * 384 + (g & 1) * 192;
            cX = XPAT(row);
            cS = SPAT(row) + (unsigned)(STL_OFF - O_OFF - 18816);
            for (int it_ = 0; it_ < 100000; ++it_) {
                unsigned n_;
                asm volatile("ds_read_b32 %0, %1\n\ts_waitcnt lgkmcnt(0)" : "=v"(n_) : "v"(ldsb + FLAG_OFF + 8) : "memory");
                if (__builtin_amdgcn_readfirstlane(n_) >= 4) break;
                __builtin_amdgcn_s_sleep(1);
            }
        } else {
            {
                const auto dr = __builtin_amdgcn_make_buffer_rsrc((void*)dw, 0, 9 * 384, 0x00020000);
                unsigned m0_ = ldsb + DW_OFF + w * 864, vo_ = px * 288 + lane * 16;
                asm volatile("s_mov_b32 m0, %0\n\ts_mov_b32 exec_hi, 0x3fffff\n\tbuffer_load_dwordx4 %1, %2, %3 offen lds\n\ts_mov_b32 exec_hi, -1"
                             :: "s"(m0_), "v"(vo_), "s"(dr), "s"(96 * w) : "m0", "memory");
            }
            DMA_SLICE(en0)
            SB();
            if (wave == 2) {
                u32x4 so;
#pragma unroll
                for (int e = 0; e < 3; ++e) {
                    const int tk = byte_of(TOKMAP[2 * e], TOKMAP[2 * e + 1], l15);
                    const int ty = (tk * 37) >> 8, tx = tk - 7 * ty;
                    so[e] = (unsigned)(ty * ROWBYTES + tx * (CH * 2)) + 16 * g;
                }
                so[3] = dma_off(lane);
                *(u32x4*)(smem + CT_OFF + lane * 16) = so;
            }
            if (wave == 1 && lane < 32) *(unsigned*)(smem + WL_OFF + lane * 4) = lane < cnt ? en : 0x7ffffffu;
            CONV_ROWS(en0 >> 30)
            cB = aB_;
            cX = lane == (int)((CLANE >> (8 * (en0 >> 30))) & 0xFF) ? LX_OFF(0) - X_OFF + 32 * w : pXw_;
            cS = pSTw_;
            TAG_READY
            asm volatile("s_waitcnt vmcnt(9)" ::: "memory");
            if (lane < 48) {
                unsigned short dv[9];
#pragma unroll
                for (int i = 0; i < 9; ++i) dv[i] = *(const unsigned short*)(smem + DW_OFF + w * 864 + i * 96 + lane * 2);
#pragma unroll
                for (int dy = 0; dy < 3; ++dy) {
                    const unsigned a = dv[3 * dy], b2 = dv[3 * dy + 1] | (dy == 1 ? 0x3F800000u : 0u), c = dv[3 * dy + 2];
                    char* p = smem + AT_OFF + (dy == 0 ? 0 : (dy == 1 ? 6208 : 12544)) + w * 768 + lane * 16;
                    *(u32x4*)p = (u32x4){a, b2, c, 0u};
                    *(u32x4*)(p + 3072) = (u32x4){0u, a, b2, c};
                }
            }
            asm volatile("s_mov_b64 exec, 1\n\tds_add_u32 %0, %1\n\ts_mov_b64 exec, -1" :: "v"(ldsb + FLAG_OFF + 8), "v"(1) : "memory");
        }
        CONV_OPS
        asm volatile("s_waitcnt vmcnt(0) lgkmcnt(0)" ::: "memory");
        if (wave == 0) POLL_ISSUE
        asm volatile(
            "s_mov_b32 m0, s73\n\t"
            "s_cmp_eq_u32 s73, 0\n\t"
            "s_nop 7\n\t"
            "ds_read_b128 v[168:171], v254\n\t"
            "ds_read_b64 v[144:145], v253\n\t"
            "ds_read_b64 v[146:147], v253 offset:96\n\t"
            "ds_read_b64 v[148:149], v253 offset:1744\n\t"
            "ds_read_b64 v[150:151], v253 offset:1840\n\t"
            "ds_read_b128 v[172:175], v254 offset:64\n\t"
            "ds_read_b64 v[152:153], v253 offset:8\n\t"
            "ds_read_b64 v[154:155], v253 offset:104\n\t"
            "ds_read_b64 v[156:157], v253 offset:1752\n\t"
            "ds_read_b64 v[158:159], v253 offset:1848\n\t"
            "ds_read_b64 v[160:161], v253 offset:16\n\t"
            "ds_read_b64 v[162:163], v253 offset:112\n\t"
            "ds_read_b64 v[164:165], v253 offset:1760\n\t"
            "ds_read_b64 v[166:167], v253 offset:1856\n\t"
            "v_mov_b64 v[176:177], 0\n\t"
            "v_mov_b64 v[178:179], 0\n\t"
            "v_mov_b64 v[180:181], 0\n\t"
            "v_mov_b64 v[182:183], 0\n\t"
            "v_mov_b64 v[184:185], 0\n\t"
            "v_mov_b64 v[186:187], 0\n\t"
            "v_mov_b64 v[188:189], 0\n\t"
            "v_mov_b64 v[190:191], 0\n\t"
            "v_add_u32 v236, s67, v250\n\t"
            "v_xor_b32 v237, 16, v236\n\t"
            "v_add_u32 v248, s67, v251\n\t"
            "s_waitcnt lgkmcnt(9)\n\t"
            "v_smfmac_f32_16x16x64_bf16 v[176:179], v[168:171], v[144:151], v255\n\t"
            "ds_read_b128 v[168:171], v254 offset:128\n\t"
            "ds_read_b64 v[144:145], v253 offset:24\n\t"
            "s_waitcnt lgkmcnt(6)\n\t"
            "v_smfmac_f32_16x16x64_bf16 v[180:183], v[172:175], v[152:159], v255\n\t"
            "ds_read_b64 v[146:147], v253 offset:120\n\t"
            "ds_read_b64 v[148:149], v253 offset:1768\n\t"
            "ds_read_b64 v[150:151], v253 offset:1864\n\t"
            "ds_read_b128 v[172:175], v254 offset:192\n\t"
            "ds_read_b64 v[152:153], v253 offset:32\n\t"
            "ds_read_b64 v[154:155], v253 offset:128\n\t"
            "ds_read_b64 v[156:157], v253 offset:1776\n\t"
            "ds_read_b64 v[158:159], v253 offset:1872\n\t"
            "v_cvt_pk_bf16_f32 v176, v176, v177\n\t"
            "v_cvt_pk_bf16_f32 v177, v178, v179\n\t"
            "v_cvt_pk_bf16_f32 v178, v180, v181\n\t"
            "v_cvt_pk_bf16_f32 v179, v182, v183\n\t"
            "v_mfma_f32_4x4x4_16b_bf16 v[196:199], v[176:177], v[176:177], 0\n\t"
            "ds_write_b128 v236, v[176:179]\n\t"
            "v_mfma_f32_16x16x32_bf16 v[192:195], v[238:241], v[176:179], 0\n\t"
            "v_mov_b64 v[176:177], 0\n\t"
            "v_mov_b64 v[180:181], 0\n\t"
            "v_mfma_f32_4x4x4_16b_bf16 v[196:199], v[178:179], v[178:179], v[196:199]\n\t"
            "v_mov_b64 v[178:179], 0\n\t"
            "v_mov_b64 v[182:183], 0\n\t"
            "s_waitcnt lgkmcnt(10)\n\t"
            "v_smfmac_f32_16x16x64_bf16 v[184:187], v[168:171], v[160:167], v255\n\t"
            "ds_read_b128 v[168:171], v254 offset:256\n\t"
            "ds_read_b64 v[160:161], v253 offset:40\n\t"
            "ds_read_b64 v[162:163], v253 offset:136\n\t"
            "ds_read_b64 v[164:165], v253 offset:1784\n\t"
            "ds_read_b64 v[166:167], v253 offset:1880\n\t"
            "s_waitcnt lgkmcnt(10)\n\t"
            "v_smfmac_f32_16x16x64_bf16 v[188:191], v[172:175], v[144:151], v255\n\t"
            "ds_read_b128 v[172:175], v254 offset:320\n\t"
            "ds_read_b64 v[144:145], v253 offset:48\n\t"
            "ds_read_b64 v[146:147], v253 offset:144\n\t"
            "s_cbranch_scc1 .Lwk_np1%=\n\t"
            "s_mov_b64 exec, 0x3ffff\n\t"
            "buffer_load_dwordx4 v252, s[68:71], s72 offen sc1 lds\n\t"
            "s_mov_b64 exec, -1\n\t"
            ".Lwk_np1%=:\n\t"
            "ds_read_b64 v[148:149], v253 offset:1792\n\t"
            "ds_read_b64 v[150:151], v253 offset:1888\n\t"
            "v_cvt_pk_bf16_f32 v184, v184, v185\n\t"
            "v_cvt_pk_bf16_f32 v185, v186, v187\n\t"
            "s_nop 0\n\t"
            "v_cvt_pk_bf16_f32 v186, v188, v189\n\t"
            "v_cvt_pk_bf16_f32 v187, v190, v191\n\t"
            "v_mfma_f32_4x4x4_16b_bf16 v[196:199], v[184:185], v[184:185], v[196:199]\n\t"
            "s_waitcnt lgkmcnt(14)\n\t"
            "ds_write_b128 v237, v[184:187]\n\t"
            "v_mfma_f32_16x16x32_bf16 v[192:195], v[238:241], v[184:187], v[192:195]\n\t"
            "v_mov_b64 v[184:185], 0\n\t"
            "v_mov_b64 v[188:189], 0\n\t"
            "v_mfma_f32_4x4x4_16b_bf16 v[196:199], v[186:187], v[186:187], v[196:199]\n\t"
            "v_mov_b64 v[186:187], 0\n\t"
            "v_mov_b64 v[190:191], 0\n\t"
            "s_waitcnt lgkmcnt(10)\n\t"
            "v_smfmac_f32_16x16x64_bf16 v[176:179], v[168:171], v[152:159], v255\n\t"
            "ds_read_b128 v[168:171], v254 offset:384\n\t"
            "ds_read_b64 v[152:153], v253 offset:56\n\t"
            "ds_read_b64 v[154:155], v253 offset:152\n\t"
            "ds_read_b64 v[156:157], v253 offset:1800\n\t"
            "ds_read_b64 v[158:159], v253 offset:1896\n\t"
            "s_waitcnt lgkmcnt(10)\n\t"
            "v_smfmac_f32_16x16x64_bf16 v[180:183], v[172:175], v[160:167], v255\n\t"
            "ds_read_b128 v[172:175], v254 offset:448\n\t"
            "ds_read_b64 v[160:161], v253 offset:64\n\t"
            "ds_read_b64 v[162:163], v253 offset:160\n\t"
            "ds_read_b64 v[164:165], v253 offset:1808\n\t"
            "ds_read_b64 v[166:167], v253 offset:1904\n\t"
            "v_cvt_pk_bf16_f32 v176, v176, v177\n\t"
            "v_cvt_pk_bf16_f32 v177, v178, v179\n\t"
            "s_nop 0\n\t"
            "v_cvt_pk_bf16_f32 v178, v180, v181\n\t"
            "v_cvt_pk_bf16_f32 v179, v182, v183\n\t"
            "v_mfma_f32_4x4x4_16b_bf16 v[196:199], v[176:177], v[176:177], v[196:199]\n\t"
            "s_waitcnt lgkmcnt(14)\n\t"
            "ds_write_b128 v236, v[176:179] offset:128\n\t"
            "v_mfma_f32_16x16x32_bf16 v[192:195], v[238:241], v[176:179], v[192:195]\n\t"
            "v_mov_b64 v[176:177], 0\n\t"
            "v_mov_b64 v[180:181], 0\n\t"
            "v_mfma_f32_4x4x4_16b_bf16 v[196:199], v[178:179], v[178:179], v[196:199]\n\t"
            "v_mov_b64 v[178:179], 0\n\t"
            "v_mov_b64 v[182:183], 0\n\t"
            "s_waitcnt lgkmcnt(10)\n\t"
            "v_smfmac_f32_16x16x64_bf16 v[184:187], v[168:171], v[144:151], v255\n\t"
            "ds_read_b128 v[168:171], v254 offset:512\n\t"
            "ds_read_b64 v[144:145], v253 offset:72\n\t"
            "ds_read_b64 v[146:147], v253 offset:168\n\t"
            "ds_read_b64 v[148:149], v253 offset:1816\n\t"
            "ds_read_b64 v[150:151], v253 offset:1912\n\t"
            "s_waitcnt lgkmcnt(10)\n\t"
            "v_smfmac_f32_16x16x64_bf16 v[188:191], v[172:175], v[152:159], v255\n\t"
            "ds_read_b128 v[172:175], v254 offset:576\n\t"
            "ds_read_b64 v[152:153], v253 offset:80\n\t"
            "ds_read_b64 v[154:155], v253 offset:176\n\t"
            "ds_read_b64 v[156:157], v253 offset:1824\n\t"
            "ds_read_b64 v[158:159], v253 offset:1920\n\t"
            "v_cvt_pk_bf16_f32 v184, v184, v185\n\t"
            "v_cvt_pk_bf16_f32 v185, v186, v187\n\t"
            "s_cbranch_scc1 .Lwk_np0%=\n\t"
            "s_mov_b64 exec, 0x3ffff\n\t"
            "buffer_load_dwordx4 v252, s[68:71], s72 offen sc1 lds\n\t"
            "s_mov_b64 exec, -1\n\t"
            ".Lwk_np0%=:\n\t"
            "s_nop 0\n\t"
            "v_cvt_pk_bf16_f32 v186, v188, v189\n\t"
            "v_cvt_pk_bf16_f32 v187, v190, v191\n\t"
            "v_mfma_f32_4x4x4_16b_bf16 v[196:199], v[184:185], v[184:185], v[196:199]\n\t"
            "s_waitcnt lgkmcnt(14)\n\t"
            "ds_write_b128 v237, v[184:187] offset:128\n\t"
            "v_mfma_f32_16x16x32_bf16 v[192:195], v[238:241], v[184:187], v[192:195]\n\t"
            "v_mov_b64 v[184:185], 0\n\t"
            "v_mov_b64 v[188:189], 0\n\t"
            "v_mfma_f32_4x4x4_16b_bf16 v[196:199], v[186:187], v[186:187], v[196:199]\n\t"
            "v_mov_b64 v[186:187], 0\n\t"
            "v_mov_b64 v[190:191], 0\n\t"
            "s_waitcnt lgkmcnt(10)\n\t"
            "v_smfmac_f32_16x16x64_bf16 v[176:179], v[168:171], v[160:167], v255\n\t"
            "ds_read_b128 v[168:171], v254 offset:640\n\t"
            "ds_read_b64 v[160:161], v253 offset:88\n\t"
            "ds_read_b64 v[162:163], v253 offset:184\n\t"
            "ds_read_b64 v[164:165], v253 offset:1832\n\t"
            "ds_read_b64 v[166:167], v253 offset:1928\n\t"
            "s_waitcnt lgkmcnt(10)\n\t"
            "v_smfmac_f32_16x16x64_bf16 v[180:183], v[172:175], v[144:151], v255\n\t"
            "ds_read_b128 v[172:175], v254 offset:704\n\t"
            "v_cvt_pk_bf16_f32 v176, v176, v177\n\t"
            "v_cvt_pk_bf16_f32 v177, v178, v179\n\t"
            "s_nop 1\n\t"
            "v_mfma_f32_4x4x4_16b_bf16 v[196:199], v[176:177], v[176:177], v[196:199]\n\t"
            "s_nop 1\n\t"
            "v_cvt_pk_bf16_f32 v178, v180, v181\n\t"
            "v_cvt_pk_bf16_f32 v179, v182, v183\n\t"
            "s_nop 1\n\t"
            "v_mfma_f32_4x4x4_16b_bf16 v[196:199], v[178:179], v[178:179], v[196:199]\n\t"
            "ds_write_b128 v236, v[176:179] offset:256\n\t"
            "v_mfma_f32_16x16x32_bf16 v[192:195], v[238:241], v[176:179], v[192:195]\n\t"
            "s_waitcnt lgkmcnt(6)\n\t"
            "v_smfmac_f32_16x16x64_bf16 v[184:187], v[168:171], v[152:159], v255\n\t"
            "s_waitcnt lgkmcnt(1)\n\t"
            "v_smfmac_f32_16x16x64_bf16 v[188:191], v[172:175], v[160:167], v255\n\t"
            "s_nop 5\n\t"
            "v_cvt_pk_bf16_f32 v184, v184, v185\n\t"
            "v_cvt_pk_bf16_f32 v185, v186, v187\n\t"
            "s_nop 1\n\t"
            "v_mfma_f32_4x4x4_16b_bf16 v[196:199], v[184:185], v[184:185], v[196:199]\n\t"
            "v_cvt_pk_bf16_f32 v186, v188, v189\n\t"
            "v_cvt_pk_bf16_f32 v187, v190, v191\n\t"
            "s_nop 1\n\t"
            "v_mfma_f32_4x4x4_16b_bf16 v[196:199], v[186:187], v[186:187], v[196:199]\n\t"
            "v_mfma_f32_16x16x32_bf16 v[192:195], v[238:241], v[184:187], v[192:195]\n\t"
            "ds_write_b128 v237, v[184:187] offset:256\n\t"
            "s_nop 6\n\t"
            "v_cndmask_b32_e64 v193, v196, v197, s[60:61]\n\t"
            "v_cndmask_b32_e64 v193, v193, v198, s[62:63]\n\t"
            "v_cndmask_b32_e64 v193, v193, v199, s[64:65]\n\t"
            "ds_write_b64 v248, v[192:193] offset:18816\n\t"
            "s_waitcnt lgkmcnt(0)\n\t"
            "s_nop 7\n\t"
            :
            : "{v250}"(cX), "{v251}"(cS), "{v253}"(cB), "{v254}"(aA_), "{v255}"(idx_), "{v[238:241]}"(sel4), "{s67}"(ldsb + (wave < 4 ? X_OFF : O_OFF)),
              "{v252}"(lane * 16), "{s[68:71]}"(wrsrc), "{s72}"(flg), "{s73}"(wave == 0 ? ldsb + PF_OFF : 0u),
              "{s[60:61]}"(0x2222222222222222ull), "{s[62:63]}"(0x4444444444444444ull), "{s[64:65]}"(0x8888888888888888ull)
            : "v144", "v145", "v146", "v147", "v148", "v149", "v150", "v151", "v152", "v153", "v154", "v155", "v156", "v157", "v158", "v159", "v160", "v161", "v162", "v163", "v164", "v165", "v166", "v167", "v168", "v169", "v170", "v171", "v172", "v173", "v174", "v175", "v176", "v177", "v178", "v179", "v180", "v181", "v182", "v183", "v184", "v185", "v186", "v187", "v188", "v189", "v190", "v191", "v192", "v193", "v194", "v195", "v196", "v197", "v198", "v199", "v236", "v237", "v248", "v249", "m0", "scc", "memory");
        if (wave < 4) {
            POLL_W
        }
    }
    const unsigned long long mrow = 0x0001000100010001ull * ((1u << (cnt - 16)) - 1u);
#define KARGS                                                                                      \
    unsigned long long xb, wb, ob;                                                                 \
    asm volatile("s_load_dwordx2 %0, %3, 0x0\n\ts_load_dwordx2 %1, %3, 0x20\n\ts_load_dwordx2 %2, %3, 0x28\n\ts_waitcnt lgkmcnt(0)" \
                 : "=&s"(xb), "=&s"(wb), "=&s"(ob) : "s"(__builtin_amdgcn_kernarg_segment_ptr()) : "memory"); \
    xb -= XBIAS;                                                                                   \
    const u32x4 wr = {(unsigned)wb, (unsigned)(wb >> 32) & 0xffffu, (unsigned)WS_BYTES, 0x00020000u}; \
    const u32x4 orr = {(unsigned)ob, (unsigned)(ob >> 32) & 0xffffu, (unsigned)(IMG * IMG * CH * 2), 0x00020000u}; \
    const u32x4 xr = {(unsigned)xb, (unsigned)(xb >> 32) & 0xffffu, (unsigned)(IMG * IMG * CH * 2 + XBIAS), 0x00020000u};
    const int tk_ = (xi + 4) / 6;
    const unsigned touch = wave * 36864 + ((xi + 4 - 6 * tk_) * 6 + tk_) * 1024;
#define B_SCALARS \
            CONV_CONSTS(0) \
            const unsigned aCA_ = ldsb + CT_OFF + lane * 16; \
            KARGS \
            unsigned s45 = OOB, s46 = (en0 & 0x7ffffffu) + 96 * w, s47 = (en1 & 0x7ffffffu) + 96 * w; \
            unsigned s48 = ldsb + X_OFF, s67 = ldsb + X1_OFF, s76 = ldsb + WL_OFF + 8, s70 = 0; \
            unsigned s56 = __builtin_amdgcn_readfirstlane((en0 >> 30) * 4 + (en1 >> 30)); \
            unsigned s69 = 0x401fffffu | (en1 & 0x38000000u), s79 = (en1 & 0x08000000u) ? OOB : s47, s59 = ldsb + LX_OFF(1) + 32 * w; \
            unsigned long long xp = 0, sp = 0; \
            _Pragma("unroll") for (int v = 0; v < 4; ++v) { \
                const unsigned r = (CROW >> (8 * v)) & 0xFF; \
                xp |= (unsigned long long)XPAT(r) << (16 * v); \
                sp |= (unsigned long long)SPAT(r) << (16 * v); \
            }
#define B_SIO "+{s45}"(s45), "+{s46}"(s46), "+{s47}"(s47), "+{s48}"(s48), "+{s67}"(s67), "+{s76}"(s76), "+{s56}"(s56), "+{s70}"(s70), "+{s69}"(s69), "+{s79}"(s79), "+{s59}"(s59)
#define B_SIN "{v[238:241]}"(sel4), "{s[40:43]}"(orr), "{s[72:75]}"(xr), "{s39}"(0x401fffffu), "{s[28:31]}"(wr), "{s33}"(wave * 36864), "{s78}"(96 * w), \
              "{s27}"(96 * w - 384), "{s[50:51]}"(xp), "{s[52:53]}"(sp), "{s54}"(CLANE), "{s55}"(CROW & 0x0F0F0F0Fu), "{s[34:35]}"(0x0001000100010001ull), \
              "{s[60:61]}"(0x2222222222222222ull), "{s[62:63]}"(0x4444444444444444ull), "{s[64:65]}"(0x8888888888888888ull), \
              "{s66}"(ldsb + w * 8192), "{s71}"((ldsb + X_OFF) ^ (ldsb + X1_OFF))
#define B_SCLOB "s83", "s84", "s26", "s77", "s68", "s36", "s37", "s38", "s57", "s58", "s49", "vcc", "scc", "m0", "memory"

    if (wave < 4) {
        LANE_SETUP
        const int dW = X_OFF + l15 * 384 + (((2 * w + (g >> 1)) ^ sw) << 4) + 8 * (g & 1);
        const int vB = V_OFF + w * 4608 + 1024 + lane * 8, rsR = RS_BASE + l15 * 4;
        {
            const unsigned rsb = ldsb + RS_BASE;
            const unsigned pE_ = fE - X_OFF, aO_ = ldsb + dW + (O_OFF - X_OFF), sel_ = (l15 & 3) ? 0u : 1u, aVB_ = ldsb + vB;
            const unsigned pST_ = lane * 16, aPLw_ = rsb + lane * 2, aRSr_ = ldsb + rsR;
            unsigned aRSw_ = rsb + lane * 4;
            const unsigned g16_ = (l15 & 3) ? ldsb + AT_OFF + 18816 : g * 16, g8_ = (l15 & 3) ? ldsb + AT_OFF + 18816 : g * 8;
            const unsigned loVL_ = ((2 * w + (l15 >> 3)) << 4) + (l15 & 7) * 2, loOL_ = g * 128;
            const unsigned on = (l15 & 3) ? 0u : 0x3F803F80u;
            const u32x4 ones4 = {on, on, on, on};
            unsigned s44 = cnt, s48 = ldsb + X_OFF, s70 = 0;
            unsigned s72 = ldsb + KQL_OFF + w * 192, s73 = ldsb + OL_OFF, s74 = ldsb + KQL_OFF + 784, s77 = ldsb + OL_OFF, s76 = ldsb + KQL_OFF + 768 + w * 4;
            KARGS
            asm volatile(
            "s_nop 4\n\t"
            "buffer_load_dwordx4 v[140:143], v248, s[84:87], s88 offen sc1\n\t"
            "s_add_u32 s49, s46, 0\n\t"
            "buffer_load_dwordx4 v[0:3], v248, s[84:87], s49 offen sc1\n\t"
            "s_add_u32 s49, s46, 6144\n\t"
            "buffer_load_dwordx4 v[24:27], v248, s[84:87], s49 offen sc1\n\t"
            "s_add_u32 s49, s46, 12288\n\t"
            "buffer_load_dwordx4 v[48:51], v248, s[84:87], s49 offen sc1\n\t"
            "s_add_u32 s49, s46, 18432\n\t"
            "buffer_load_dwordx4 v[72:75], v248, s[84:87], s49 offen sc1\n\t"
            "s_add_u32 s49, s46, 24576\n\t"
            "buffer_load_dwordx4 v[96:99], v248, s[84:87], s49 offen sc1\n\t"
            "s_add_u32 s49, s46, 30720\n\t"
            "buffer_load_dwordx4 v[120:123], v248, s[84:87], s49 offen sc1\n\t"
            "s_add_u32 s49, s46, 1024\n\t"
            "buffer_load_dwordx4 v[4:7], v248, s[84:87], s49 offen sc1\n\t"
            "s_add_u32 s49, s46, 7168\n\t"
            "buffer_load_dwordx4 v[28:31], v248, s[84:87], s49 offen sc1\n\t"
            "s_add_u32 s49, s46, 13312\n\t"
            "buffer_load_dwordx4 v[52:55], v248, s[84:87], s49 offen sc1\n\t"
            "s_add_u32 s49, s46, 19456\n\t"
            "buffer_load_dwordx4 v[76:79], v248, s[84:87], s49 offen sc1\n\t"
            "s_add_u32 s49, s46, 25600\n\t"
            "buffer_load_dwordx4 v[100:103], v248, s[84:87], s49 offen sc1\n\t"
            "s_add_u32 s49, s46, 31744\n\t"
            "buffer_load_dwordx4 v[124:127], v248, s[84:87], s49 offen sc1\n\t"
            "s_add_u32 s49, s46, 2048\n\t"
            "buffer_load_dwordx4 v[8:11], v248, s[84:87], s49 offen sc1\n\t"
            "s_add_u32 s49, s46, 8192\n\t"
            "buffer_load_dwordx4 v[32:35], v248, s[84:87], s49 offen sc1\n\t"
            "s_add_u32 s49, s46, 14336\n\t"
            "buffer_load_dwordx4 v[56:59], v248, s[84:87], s49 offen sc1\n\t"
            "s_add_u32 s49, s46, 20480\n\t"
            "buffer_load_dwordx4 v[80:83], v248, s[84:87], s49 offen sc1\n\t"
            "s_add_u32 s49, s46, 26624\n\t"
            "buffer_load_dwordx4 v[104:107], v248, s[84:87], s49 offen sc1\n\t"
            "s_add_u32 s49, s46, 32768\n\t"
            "buffer_load_dwordx4 v[128:131], v248, s[84:87], s49 offen sc1\n\t"
            "s_add_u32 s49, s46, 3072\n\t"
            "buffer_load_dwordx4 v[12:15], v248, s[84:87], s49 offen sc1\n\t"
            "s_add_u32 s49, s46, 9216\n\t"
            "buffer_load_dwordx4 v[36:39], v248, s[84:87], s49 offen sc1\n\t"
            "s_add_u32 s49, s46, 15360\n\t"
            "buffer_load_dwordx4 v[60:63], v248, s[84:87], s49 offen sc1\n\t"
            "s_add_u32 s49, s46, 21504\n\t"
            "buffer_load_dwordx4 v[84:87], v248, s[84:87], s49 offen sc1\n\t"
            "s_add_u32 s49, s46, 27648\n\t"
            "buffer_load_dwordx4 v[108:111], v248, s[84:87], s49 offen sc1\n\t"
            "s_add_u32 s49, s46, 33792\n\t"
            "buffer_load_dwordx4 v[132:135], v248, s[84:87], s49 offen sc1\n\t"
            "s_add_u32 s49, s46, 4096\n\t"
            "buffer_load_dwordx4 v[16:19], v248, s[84:87], s49 offen sc1\n\t"
            "s_add_u32 s49, s46, 10240\n\t"
            "buffer_load_dwordx4 v[40:43], v248, s[84:87], s49 offen sc1\n\t"
            "s_add_u32 s49, s46, 16384\n\t"
            "buffer_load_dwordx4 v[64:67], v248, s[84:87], s49 offen sc1\n\t"
            "s_add_u32 s49, s46, 22528\n\t"
            "buffer_load_dwordx4 v[88:91], v248, s[84:87], s49 offen sc1\n\t"
            "s_add_u32 s49, s46, 28672\n\t"
            "buffer_load_dwordx4 v[112:115], v248, s[84:87], s49 offen sc1\n\t"
            "s_add_u32 s49, s46, 34816\n\t"
            "buffer_load_dwordx4 v[136:139], v248, s[84:87], s49 offen sc1\n\t"
            "s_add_u32 s49, s46, 5120\n\t"
            "buffer_load_dwordx4 v[20:23], v248, s[84:87], s49 offen sc1\n\t"
            "s_add_u32 s49, s46, 11264\n\t"
            "buffer_load_dwordx4 v[44:47], v248, s[84:87], s49 offen sc1\n\t"
            "s_add_u32 s49, s46, 17408\n\t"
            "buffer_load_dwordx4 v[68:71], v248, s[84:87], s49 offen sc1\n\t"
            "s_add_u32 s49, s46, 23552\n\t"
            "buffer_load_dwordx4 v[92:95], v248, s[84:87], s49 offen sc1\n\t"
            "s_add_u32 s49, s46, 29696\n\t"
            "buffer_load_dwordx4 v[116:119], v248, s[84:87], s49 offen sc1\n\t"
            "s_add_u32 s49, s46, 35840\n\t"
            "buffer_load_dwordx4 v[140:143], v248, s[84:87], s49 offen sc1\n\t"
            "s_waitcnt lgkmcnt(0)\n\t"
            "s_barrier\n\t"
            "s_setprio 2\n\t"
            "v_add_u32 v235, s71, v248\n\t"
            "v_add_u32 v232, s71, v244\n\t"
            "v_xor_b32 v233, 64, v232\n\t"
            "v_subrev_u32 v239, s75, v251\n\t"
            "v_mul_u32_u24 v239, 200, v239\n\t"
            "v_lshrrev_b32 v234, 3, v255\n\t"
            "v_add3_u32 v234, v234, v239, s72\n\t"
            "ds_read_b128 v[228:231], v235 offset:12288\n\t"
            "ds_read_b128 v[144:147], v232\n\t"
            "ds_read_b128 v[148:151], v233\n\t"
            "ds_read_b128 v[204:207], v232 offset:6144\n\t"
            "ds_read_b128 v[208:211], v233 offset:6144\n\t"
            "ds_read_b128 v[152:155], v232 offset:128\n\t"
            "ds_read_b128 v[156:159], v233 offset:128\n\t"
            "ds_read_b128 v[212:215], v232 offset:6272\n\t"
            "ds_read_b128 v[224:227], v233 offset:6272\n\t"
            "ds_read_b128 v[160:163], v232 offset:256\n\t"
            "ds_read_b128 v[164:167], v233 offset:256\n\t"
            "s_waitcnt vmcnt(24)\n\t"
            "s_waitcnt lgkmcnt(10)\n\t"
            "v_add_f32 v236, v228, v230\n\t"
            "v_add_f32 v237, v229, v231\n\t"
            "s_waitcnt lgkmcnt(8)\n\t"
            "v_mfma_f32_16x16x32_bf16 v[168:171], v[0:3], v[144:147], 0\n\t"
            "ds_read_b128 v[228:231], v235 offset:13312\n\t"
            "v_lshrrev_b32 v235, 4, v255\n\t"
            "v_mfma_f32_16x16x32_bf16 v[168:171], v[4:7], v[148:151], v[168:171]\n\t"
            "v_add3_u32 v235, v235, v239, s72\n\t"
            "v_mfma_f32_16x16x32_bf16 v[176:179], v[48:51], v[144:147], 0\n\t"
            "v_mfma_f32_16x16x32_bf16 v[176:179], v[52:55], v[148:151], v[176:179]\n\t"
            "v_mfma_f32_16x16x32_bf16 v[172:175], v[24:27], v[144:147], 0\n\t"
            "v_mfma_f32_16x16x32_bf16 v[172:175], v[28:31], v[148:151], v[172:175]\n\t"
            "v_mfma_f32_16x16x32_bf16 v[180:183], v[72:75], v[144:147], 0\n\t"
            "v_mfma_f32_16x16x32_bf16 v[180:183], v[76:79], v[148:151], v[180:183]\n\t"
            "v_mfma_f32_16x16x32_bf16 v[184:187], v[96:99], v[144:147], 0\n\t"
            "v_mfma_f32_16x16x32_bf16 v[184:187], v[100:103], v[148:151], v[184:187]\n\t"
            "v_mfma_f32_16x16x32_bf16 v[188:191], v[120:123], v[144:147], 0\n\t"
            "s_waitcnt lgkmcnt(0)\n\t"
            "v_add_f32 v228, v228, v230\n\t"
            "v_add_f32 v236, v236, v228\n\t"
            "v_mfma_f32_16x16x32_bf16 v[188:191], v[124:127], v[148:151], v[188:191]\n\t"
            "v_add_f32 v229, v229, v231\n\t"
            "v_mul_f32 v236, 0x3baaaaab, v236\n\t"
            "v_mfma_f32_16x16x32_bf16 v[192:195], v[0:3], v[204:207], 0\n\t"
            "v_add_f32 v237, v237, v229\n\t"
            "v_mul_f32_e64 v236, v236, -v236\n\t"
            "v_mfma_f32_16x16x32_bf16 v[192:195], v[4:7], v[208:211], v[192:195]\n\t"
            "v_fmac_f32 v236, 0x3baaaaab, v237\n\t"
            "v_max_f32 v236, 0, v236\n\t"
            "v_mfma_f32_16x16x32_bf16 v[196:199], v[24:27], v[204:207], 0\n\t"
            "v_add_f32 v236, 0x3727c5ac, v236\n\t"
            "v_rsq_f32 v236, v236\n\t"
            "v_mfma_f32_16x16x32_bf16 v[196:199], v[28:31], v[208:211], v[196:199]\n\t"
            "ds_write_b32 v249, v236\n\t"
            "v_mfma_f32_16x16x32_bf16 v[200:203], v[48:51], v[204:207], 0\n\t"
            "ds_read_b32 v236, v251\n\t"
            "ds_read_b32 v237, v251 offset:64\n\t"
            "v_mfma_f32_16x16x32_bf16 v[200:203], v[52:55], v[208:211], v[200:203]\n\t"
            "v_mfma_f32_16x16x32_bf16 v[216:219], v[72:75], v[204:207], 0\n\t"
            "v_mfma_f32_16x16x32_bf16 v[216:219], v[76:79], v[208:211], v[216:219]\n\t"
            "v_mfma_f32_16x16x32_bf16 v[220:223], v[96:99], v[204:207], 0\n\t"
            "v_mfma_f32_16x16x32_bf16 v[220:223], v[100:103], v[208:211], v[220:223]\n\t"
            "v_mfma_f32_16x16x32_bf16 v[228:231], v[120:123], v[204:207], 0\n\t"
            "ds_read_b128 v[204:207], v232 offset:6400\n\t"
            "v_add_u32 v232, s48, v244\n\t"
            "v_mfma_f32_16x16x32_bf16 v[228:231], v[124:127], v[208:211], v[228:231]\n\t"
            "ds_read_b128 v[208:211], v233 offset:6400\n\t"
            "s_waitcnt vmcnt(12)\n\t"
            "v_mfma_f32_16x16x32_bf16 v[168:171], v[8:11], v[152:155], v[168:171]\n\t"
            "v_xor_b32 v233, 64, v232\n\t"
            "v_mfma_f32_16x16x32_bf16 v[168:171], v[12:15], v[156:159], v[168:171]\n\t"
            "v_mfma_f32_16x16x32_bf16 v[176:179], v[56:59], v[152:155], v[176:179]\n\t"
            "v_mfma_f32_16x16x32_bf16 v[176:179], v[60:63], v[156:159], v[176:179]\n\t"
            "v_mfma_f32_16x16x32_bf16 v[172:175], v[32:35], v[152:155], v[172:175]\n\t"
            "v_mfma_f32_16x16x32_bf16 v[172:175], v[36:39], v[156:159], v[172:175]\n\t"
            "v_mfma_f32_16x16x32_bf16 v[180:183], v[80:83], v[152:155], v[180:183]\n\t"
            "v_mfma_f32_16x16x32_bf16 v[180:183], v[84:87], v[156:159], v[180:183]\n\t"
            "v_mfma_f32_16x16x32_bf16 v[184:187], v[104:107], v[152:155], v[184:187]\n\t"
            "v_mfma_f32_16x16x32_bf16 v[184:187], v[108:111], v[156:159], v[184:187]\n\t"
            "v_mfma_f32_16x16x32_bf16 v[188:191], v[128:131], v[152:155], v[188:191]\n\t"
            "v_mfma_f32_16x16x32_bf16 v[188:191], v[132:135], v[156:159], v[188:191]\n\t"
            "v_mfma_f32_16x16x32_bf16 v[192:195], v[8:11], v[212:215], v[192:195]\n\t"
            "v_mfma_f32_16x16x32_bf16 v[192:195], v[12:15], v[224:227], v[192:195]\n\t"
            "v_mfma_f32_16x16x32_bf16 v[196:199], v[32:35], v[212:215], v[196:199]\n\t"
            "v_mfma_f32_16x16x32_bf16 v[196:199], v[36:39], v[224:227], v[196:199]\n\t"
            "v_mfma_f32_16x16x32_bf16 v[200:203], v[56:59], v[212:215], v[200:203]\n\t"
            "v_mfma_f32_16x16x32_bf16 v[200:203], v[60:63], v[224:227], v[200:203]\n\t"
            "v_mfma_f32_16x16x32_bf16 v[216:219], v[80:83], v[212:215], v[216:219]\n\t"
            "v_mfma_f32_16x16x32_bf16 v[216:219], v[84:87], v[224:227], v[216:219]\n\t"
            "v_mfma_f32_16x16x32_bf16 v[220:223], v[104:107], v[212:215], v[220:223]\n\t"
            "v_mfma_f32_16x16x32_bf16 v[220:223], v[108:111], v[224:227], v[220:223]\n\t"
            "v_mfma_f32_16x16x32_bf16 v[228:231], v[128:131], v[212:215], v[228:231]\n\t"
            "ds_read_b128 v[212:215], v232 offset:256\n\t"
            "v_mfma_f32_16x16x32_bf16 v[228:231], v[132:135], v[224:227], v[228:231]\n\t"
            "ds_read_b128 v[224:227], v233 offset:256\n\t"
            "s_waitcnt vmcnt(0)\n\t"
            "v_mfma_f32_16x16x32_bf16 v[168:171], v[16:19], v[160:163], v[168:171]\n\t"
            "v_mfma_f32_16x16x32_bf16 v[168:171], v[20:23], v[164:167], v[168:171]\n\t"
            "v_mfma_f32_16x16x32_bf16 v[176:179], v[64:67], v[160:163], v[176:179]\n\t"
            "v_mfma_f32_16x16x32_bf16 v[176:179], v[68:71], v[164:167], v[176:179]\n\t"
            "v_mfma_f32_16x16x32_bf16 v[180:183], v[88:91], v[160:163], v[180:183]\n\t"
            "v_mfma_f32_16x16x32_bf16 v[180:183], v[92:95], v[164:167], v[180:183]\n\t"
            "v_mfma_f32_16x16x32_bf16 v[172:175], v[40:43], v[160:163], v[172:175]\n\t"
            "v_mfma_f32_16x16x32_bf16 v[172:175], v[44:47], v[164:167], v[172:175]\n\t"
            "s_waitcnt lgkmcnt(5)\n\t"
            "s_nop 0\n\t"
            "v_mul_f32 v168, v236, v168\n\t"
            "v_mfma_f32_16x16x32_bf16 v[184:187], v[112:115], v[160:163], v[184:187]\n\t"
            "v_mul_f32 v169, v236, v169\n\t"
            "v_mul_f32 v170, v236, v170\n\t"
            "v_mfma_f32_16x16x32_bf16 v[184:187], v[116:119], v[164:167], v[184:187]\n\t"
            "v_mul_f32 v176, v236, v176\n\t"
            "v_mul_f32 v177, v236, v177\n\t"
            "v_mfma_f32_16x16x32_bf16 v[188:191], v[136:139], v[160:163], v[188:191]\n\t"
            "v_mul_f32 v171, v236, v171\n\t"
            "v_mul_f32 v178, v236, v178\n\t"
            "v_mfma_f32_16x16x32_bf16 v[188:191], v[140:143], v[164:167], v[188:191]\n\t"
            "v_mul_f32 v179, v236, v179\n\t"
            "v_cvt_pk_bf16_f32 v168, v168, v169\n\t"
            "s_waitcnt lgkmcnt(2)\n\t"
            "v_mfma_f32_16x16x32_bf16 v[192:195], v[16:19], v[204:207], v[192:195]\n\t"
            "v_cvt_pk_bf16_f32 v176, v176, v177\n\t"
            "v_cvt_pk_bf16_f32 v180, v180, v181\n\t"
            "v_mfma_f32_16x16x32_bf16 v[192:195], v[20:23], v[208:211], v[192:195]\n\t"
            "v_mul_f32 v172, v236, v172\n\t"
            "v_mul_f32 v173, v236, v173\n\t"
            "v_mfma_f32_16x16x32_bf16 v[196:199], v[40:43], v[204:207], v[196:199]\n\t"
            "v_mul_f32 v174, v236, v174\n\t"
            "v_mul_f32 v175, v236, v175\n\t"
            "v_mfma_f32_16x16x32_bf16 v[196:199], v[44:47], v[208:211], v[196:199]\n\t"
            "v_cvt_pk_bf16_f32 v169, v170, v171\n\t"
            "v_cvt_pk_bf16_f32 v177, v178, v179\n\t"
            "v_mfma_f32_16x16x32_bf16 v[200:203], v[64:67], v[204:207], v[200:203]\n\t"
            "v_cvt_pk_bf16_f32 v181, v182, v183\n\t"
            "v_cvt_pk_bf16_f32 v170, v172, v173\n\t"
            "v_mfma_f32_16x16x32_bf16 v[200:203], v[68:71], v[208:211], v[200:203]\n\t"
            "v_cvt_pk_bf16_f32 v171, v174, v175\n\t"
            "v_cvt_pk_bf16_f32 v182, v184, v185\n\t"
            "v_mfma_f32_16x16x32_bf16 v[216:219], v[88:91], v[204:207], v[216:219]\n\t"
            "v_cvt_pk_bf16_f32 v183, v186, v187\n\t"
            "v_cvt_pk_bf16_f32 v178, v188, v189\n\t"
            "v_mfma_f32_16x16x32_bf16 v[216:219], v[92:95], v[208:211], v[216:219]\n\t"
            "v_cvt_pk_bf16_f32 v179, v190, v191\n\t"
            "v_mul_f32 v236, 0x3e553b94, v236\n\t"
            "v_mfma_f32_16x16x32_bf16 v[220:223], v[112:115], v[204:207], v[220:223]\n\t"
            "v_add_u32 v184, s76, v239\n\t"
            "v_add_u32 v185, s74, v239\n\t"
            "v_mfma_f32_16x16x32_bf16 v[220:223], v[116:119], v[208:211], v[220:223]\n\t"
            "s_mov_b64 exec, -1\n\t"
            "ds_write_b128 v234, v[168:171]\n\t"
            "ds_write_b128 v234, v[180:183] offset:64\n\t"
            "ds_write_b64 v235, v[176:177] offset:128\n\t"
            "ds_write_b64 v235, v[178:179] offset:160\n\t"
            "s_mov_b64 exec, -1\n\t"
            "v_mfma_f32_16x16x16_bf16 v[172:175], v[176:177], v[178:179], 0\n\t"
            "v_mul_f32 v192, v237, v192\n\t"
            "v_mul_f32 v193, v237, v193\n\t"
            "v_mul_f32 v200, v237, v200\n\t"
            "v_mfma_f32_16x16x32_bf16 v[228:231], v[136:139], v[204:207], v[228:231]\n\t"
            "ds_read_b128 v[204:207], v232\n\t"
            "v_mul_f32 v201, v237, v201\n\t"
            "v_mfma_f32_16x16x32_bf16 v[228:231], v[140:143], v[208:211], v[228:231]\n\t"
            "ds_read_b128 v[208:211], v233\n\t"
            "v_mul_f32 v194, v237, v194\n\t"
            "v_mfma_f32_16x16x32_bf16 v[172:175], v[168:171], v[180:183], v[172:175]\n\t"
            "v_mul_f32 v195, v237, v195\n\t"
            "v_mul_f32 v202, v237, v202\n\t"
            "v_mul_f32 v203, v237, v203\n\t"
            "v_cvt_pk_bf16_f32 v192, v192, v193\n\t"
            "v_cvt_pk_bf16_f32 v200, v200, v201\n\t"
            "v_cvt_pk_bf16_f32 v216, v216, v217\n\t"
            "v_mul_f32 v196, v237, v196\n\t"
            "v_mul_f32 v197, v237, v197\n\t"
            "v_mul_f32 v198, v237, v198\n\t"
            "v_mul_f32 v199, v237, v199\n\t"
            "v_cvt_pk_bf16_f32 v193, v194, v195\n\t"
            "v_cvt_pk_bf16_f32 v201, v202, v203\n\t"
            "v_cvt_pk_bf16_f32 v217, v218, v219\n\t"
            "v_cvt_pk_bf16_f32 v194, v196, v197\n\t"
            "v_cvt_pk_bf16_f32 v195, v198, v199\n\t"
            "v_cvt_pk_bf16_f32 v218, v220, v221\n\t"
            "v_cvt_pk_bf16_f32 v219, v222, v223\n\t"
            "v_cvt_pk_bf16_f32 v202, v228, v229\n\t"
            "v_cvt_pk_bf16_f32 v203, v230, v231\n\t"
            "v_mul_f32 v237, 0x3e553b94, v237\n\t"
            "v_add_u32 v220, s76, v239\n\t"
            "v_mfma_f32_16x16x16_bf16 v[196:199], v[200:201], v[202:203], 0\n\t"
            "s_and_b32 exec_lo, s56, 0x11111111\n\t"
            "s_and_b32 exec_hi, s57, 0x11111111\n\t"
            "ds_write_b32 v184, v172 offset:0\n\t"
            "s_and_b32 exec_lo, s56, 0x22222222\n\t"
            "s_and_b32 exec_hi, s57, 0x22222222\n\t"
            "ds_write_b32 v184, v173 offset:0\n\t"
            "s_and_b32 exec_lo, s56, 0x44444444\n\t"
            "s_and_b32 exec_hi, s57, 0x44444444\n\t"
            "ds_write_b32 v184, v174 offset:0\n\t"
            "s_and_b32 exec_lo, s56, 0x88888888\n\t"
            "s_and_b32 exec_hi, s57, 0x88888888\n\t"
            "ds_write_b32 v184, v175 offset:0\n\t"
            "s_mov_b64 exec, s[56:57]\n\t"
            "ds_write_b32 v185, v236 offset:0\n\t"
            "s_mov_b64 exec, -1\n\t"
            "s_waitcnt lgkmcnt(5)\n\t"
            "v_mfma_f32_16x16x32_bf16 v[144:147], v[0:3], v[204:207], 0\n\t"
            "v_add_u32 v221, s74, v239\n\t"
            "v_mfma_f32_16x16x32_bf16 v[144:147], v[4:7], v[208:211], v[144:147]\n\t"
            "s_mov_b64 exec, s[54:55]\n\t"
            "ds_write_b128 v234, v[192:195] offset:12800\n\t"
            "ds_write_b128 v234, v[216:219] offset:12864\n\t"
            "ds_write_b64 v235, v[200:201] offset:12928\n\t"
            "ds_write_b64 v235, v[202:203] offset:12960\n\t"
            "s_mov_b64 exec, -1\n\t"
            "v_mfma_f32_16x16x32_bf16 v[148:151], v[28:31], v[208:211], 0\n\t"
            "v_add_u32 v235, s48, v248\n\t"
            "v_mfma_f32_16x16x32_bf16 v[148:151], v[24:27], v[204:207], v[148:151]\n\t"
            "ds_read_b128 v[228:231], v235 offset:18816\n\t"
            "v_mfma_f32_16x16x32_bf16 v[152:155], v[48:51], v[204:207], 0\n\t"
            "v_mfma_f32_16x16x32_bf16 v[152:155], v[52:55], v[208:211], v[152:155]\n\t"
            "v_mfma_f32_16x16x32_bf16 v[156:159], v[76:79], v[208:211], 0\n\t"
            "v_mfma_f32_16x16x32_bf16 v[156:159], v[72:75], v[204:207], v[156:159]\n\t"
            "v_mfma_f32_16x16x32_bf16 v[160:163], v[96:99], v[204:207], 0\n\t"
            "v_mfma_f32_16x16x32_bf16 v[160:163], v[100:103], v[208:211], v[160:163]\n\t"
            "v_mfma_f32_16x16x32_bf16 v[164:167], v[124:127], v[208:211], 0\n\t"
            "ds_read_b128 v[208:211], v233 offset:128\n\t"
            "v_mfma_f32_16x16x32_bf16 v[164:167], v[120:123], v[204:207], v[164:167]\n\t"
            "ds_read_b128 v[204:207], v232 offset:128\n\t"
            "v_mfma_f32_16x16x32_bf16 v[196:199], v[192:195], v[216:219], v[196:199]\n\t"
            "s_waitcnt lgkmcnt(2)\n\t"
            "v_add_f32 v236, v228, v230\n\t"
            "s_nop 5\n\t"
            "s_and_b32 exec_lo, s78, 0x11111111\n\t"
            "s_and_b32 exec_hi, s79, 0x11111111\n\t"
            "ds_write_b32 v220, v196 offset:12800\n\t"
            "s_and_b32 exec_lo, s78, 0x22222222\n\t"
            "s_and_b32 exec_hi, s79, 0x22222222\n\t"
            "ds_write_b32 v220, v197 offset:12800\n\t"
            "s_and_b32 exec_lo, s78, 0x44444444\n\t"
            "s_and_b32 exec_hi, s79, 0x44444444\n\t"
            "ds_write_b32 v220, v198 offset:12800\n\t"
            "s_and_b32 exec_lo, s78, 0x88888888\n\t"
            "s_and_b32 exec_hi, s79, 0x88888888\n\t"
            "ds_write_b32 v220, v199 offset:12800\n\t"
            "s_mov_b64 exec, s[78:79]\n\t"
            "ds_write_b32 v221, v237 offset:12800\n\t"
            "s_mov_b64 exec, -1\n\t"
            "s_waitcnt lgkmcnt(5)\n\t"
            "v_mfma_f32_16x16x32_bf16 v[144:147], v[8:11], v[204:207], v[144:147]\n\t"
            "v_add_f32 v237, v229, v231\n\t"
            "v_mfma_f32_16x16x32_bf16 v[144:147], v[12:15], v[208:211], v[144:147]\n\t"
            "ds_read_b128 v[228:231], v235 offset:19840\n\t"
            "v_mfma_f32_16x16x32_bf16 v[148:151], v[36:39], v[208:211], v[148:151]\n\t"
            "v_mfma_f32_16x16x32_bf16 v[148:151], v[32:35], v[204:207], v[148:151]\n\t"
            "v_mfma_f32_16x16x32_bf16 v[152:155], v[56:59], v[204:207], v[152:155]\n\t"
            "v_mfma_f32_16x16x32_bf16 v[152:155], v[60:63], v[208:211], v[152:155]\n\t"
            "v_mfma_f32_16x16x32_bf16 v[156:159], v[84:87], v[208:211], v[156:159]\n\t"
            "v_mfma_f32_16x16x32_bf16 v[156:159], v[80:83], v[204:207], v[156:159]\n\t"
            "v_mfma_f32_16x16x32_bf16 v[160:163], v[104:107], v[204:207], v[160:163]\n\t"
            "v_mfma_f32_16x16x32_bf16 v[160:163], v[108:111], v[208:211], v[160:163]\n\t"
            "v_mfma_f32_16x16x32_bf16 v[164:167], v[132:135], v[208:211], v[164:167]\n\t"
            "ds_read_b128 v[208:211], v233 offset:6144\n\t"
            "v_mfma_f32_16x16x32_bf16 v[164:167], v[128:131], v[204:207], v[164:167]\n\t"
            "ds_read_b128 v[204:207], v232 offset:6144\n\t"
            "s_waitcnt lgkmcnt(2)\n\t"
            "v_add_f32 v228, v228, v230\n\t"
            "v_mfma_f32_16x16x32_bf16 v[144:147], v[16:19], v[212:215], v[144:147]\n\t"
            "v_add_f32 v236, v236, v228\n\t"
            "v_add_f32 v229, v229, v231\n\t"
            "v_mfma_f32_16x16x32_bf16 v[144:147], v[20:23], v[224:227], v[144:147]\n\t"
            "v_mul_f32 v236, 0x3baaaaab, v236\n\t"
            "v_add_f32 v237, v237, v229\n\t"
            "v_mfma_f32_16x16x32_bf16 v[148:151], v[44:47], v[224:227], v[148:151]\n\t"
            "v_mul_f32_e64 v236, v236, -v236\n\t"
            "v_fmac_f32 v236, 0x3baaaaab, v237\n\t"
            "v_mfma_f32_16x16x32_bf16 v[148:151], v[40:43], v[212:215], v[148:151]\n\t"
            "v_max_f32 v236, 0, v236\n\t"
            "v_add_f32 v236, 0x3727c5ac, v236\n\t"
            "v_mfma_f32_16x16x32_bf16 v[152:155], v[64:67], v[212:215], v[152:155]\n\t"
            "v_rsq_f32 v236, v236\n\t"
            "v_mfma_f32_16x16x32_bf16 v[152:155], v[68:71], v[224:227], v[152:155]\n\t"
            "ds_write_b32 v249, v236\n\t"
            "v_mfma_f32_16x16x32_bf16 v[156:159], v[92:95], v[224:227], v[156:159]\n\t"
            "ds_read_b32 v236, v251\n\t"
            "ds_read_b32 v237, v251 offset:64\n\t"
            "v_mfma_f32_16x16x32_bf16 v[156:159], v[88:91], v[212:215], v[156:159]\n\t"
            "ds_read_b32 v238, v251 offset:128\n\t"
            "v_mfma_f32_16x16x32_bf16 v[160:163], v[112:115], v[212:215], v[160:163]\n\t"
            "v_mfma_f32_16x16x32_bf16 v[160:163], v[116:119], v[224:227], v[160:163]\n\t"
            "v_mfma_f32_16x16x32_bf16 v[164:167], v[140:143], v[224:227], v[164:167]\n\t"
            "ds_read_b128 v[224:227], v233 offset:6272\n\t"
            "v_mfma_f32_16x16x32_bf16 v[164:167], v[136:139], v[212:215], v[164:167]\n\t"
            "ds_read_b128 v[212:215], v232 offset:6272\n\t"
            "s_waitcnt lgkmcnt(6)\n\t"
            "v_mfma_f32_16x16x32_bf16 v[168:171], v[0:3], v[204:207], 0\n\t"
            "v_cvt_pk_bf16_f32 v156, v156, v157\n\t"
            "v_cvt_pk_bf16_f32 v157, v158, v159\n\t"
            "v_mfma_f32_16x16x32_bf16 v[168:171], v[4:7], v[208:211], v[168:171]\n\t"
            "v_cvt_pk_bf16_f32 v158, v160, v161\n\t"
            "v_cvt_pk_bf16_f32 v159, v162, v163\n\t"
            "v_mfma_f32_16x16x32_bf16 v[172:175], v[28:31], v[208:211], 0\n\t"
            "v_mfma_f32_16x16x32_bf16 v[172:175], v[24:27], v[204:207], v[172:175]\n\t"
            "v_mfma_f32_16x16x32_bf16 v[176:179], v[48:51], v[204:207], 0\n\t"
            "s_waitcnt lgkmcnt(4)\n\t"
            "v_mul_f32 v144, v236, v144\n\t"
            "v_mul_f32 v145, v236, v145\n\t"
            "v_mfma_f32_16x16x32_bf16 v[176:179], v[52:55], v[208:211], v[176:179]\n\t"
            "v_mul_f32 v152, v236, v152\n\t"
            "v_mul_f32 v153, v236, v153\n\t"
            "v_mfma_f32_16x16x32_bf16 v[180:183], v[76:79], v[208:211], 0\n\t"
            "v_mul_f32 v146, v236, v146\n\t"
            "v_mul_f32 v147, v236, v147\n\t"
            "v_mfma_f32_16x16x32_bf16 v[180:183], v[72:75], v[204:207], v[180:183]\n\t"
            "v_mul_f32 v154, v236, v154\n\t"
            "v_mul_f32 v155, v236, v155\n\t"
            "v_mfma_f32_16x16x32_bf16 v[184:187], v[96:99], v[204:207], 0\n\t"
            "v_cvt_pk_bf16_f32 v144, v144, v145\n\t"
            "v_cvt_pk_bf16_f32 v152, v152, v153\n\t"
            "v_mfma_f32_16x16x32_bf16 v[184:187], v[100:103], v[208:211], v[184:187]\n\t"
            "v_mul_f32 v148, v236, v148\n\t"
            "v_mul_f32 v149, v236, v149\n\t"
            "v_mfma_f32_16x16x32_bf16 v[188:191], v[124:127], v[208:211], 0\n\t"
            "ds_read_b128 v[208:211], v233 offset:6400\n\t"
            "v_mul_f32 v150, v236, v150\n\t"
            "v_mfma_f32_16x16x32_bf16 v[188:191], v[120:123], v[204:207], v[188:191]\n\t"
            "ds_read_b128 v[204:207], v232 offset:6400\n\t"
            "v_mul_f32 v151, v236, v151\n\t"
            "s_waitcnt lgkmcnt(2)\n\t"
            "v_mfma_f32_16x16x32_bf16 v[168:171], v[8:11], v[212:215], v[168:171]\n\t"
            "v_cvt_pk_bf16_f32 v145, v146, v147\n\t"
            "v_cvt_pk_bf16_f32 v153, v154, v155\n\t"
            "v_mfma_f32_16x16x32_bf16 v[168:171], v[12:15], v[224:227], v[168:171]\n\t"
            "v_cvt_pk_bf16_f32 v146, v148, v149\n\t"
            "v_cvt_pk_bf16_f32 v147, v150, v151\n\t"
            "v_mfma_f32_16x16x32_bf16 v[172:175], v[36:39], v[224:227], v[172:175]\n\t"
            "v_cvt_pk_bf16_f32 v154, v164, v165\n\t"
            "v_cvt_pk_bf16_f32 v155, v166, v167\n\t"
            "v_mfma_f32_16x16x32_bf16 v[172:175], v[32:35], v[212:215], v[172:175]\n\t"
            "v_mul_f32 v236, 0x3e553b94, v236\n\t"
            "v_mfma_f32_16x16x32_bf16 v[176:179], v[56:59], v[212:215], v[176:179]\n\t"
            "v_mfma_f32_16x16x32_bf16 v[176:179], v[60:63], v[224:227], v[176:179]\n\t"
            "v_mfma_f32_16x16x32_bf16 v[180:183], v[84:87], v[224:227], v[180:183]\n\t"
            "v_mfma_f32_16x16x32_bf16 v[180:183], v[80:83], v[212:215], v[180:183]\n\t"
            "v_mfma_f32_16x16x32_bf16 v[184:187], v[104:107], v[212:215], v[184:187]\n\t"
            "v_mfma_f32_16x16x32_bf16 v[184:187], v[108:111], v[224:227], v[184:187]\n\t"
            "v_mfma_f32_16x16x32_bf16 v[188:191], v[132:135], v[224:227], v[188:191]\n\t"
            "ds_read_b128 v[224:227], v233 offset:12288\n\t"
            "v_mfma_f32_16x16x32_bf16 v[188:191], v[128:131], v[212:215], v[188:191]\n\t"
            "ds_read_b128 v[212:215], v232 offset:12288\n\t"
            "s_waitcnt lgkmcnt(2)\n\t"
            "v_mfma_f32_16x16x32_bf16 v[168:171], v[16:19], v[204:207], v[168:171]\n\t"
            "v_mfma_f32_16x16x32_bf16 v[168:171], v[20:23], v[208:211], v[168:171]\n\t"
            "v_mfma_f32_16x16x32_bf16 v[172:175], v[44:47], v[208:211], v[172:175]\n\t"
            "v_mfma_f32_16x16x32_bf16 v[172:175], v[40:43], v[204:207], v[172:175]\n\t"
            "v_mfma_f32_16x16x32_bf16 v[176:179], v[64:67], v[204:207], v[176:179]\n\t"
            "v_mfma_f32_16x16x32_bf16 v[176:179], v[68:71], v[208:211], v[176:179]\n\t"
            "v_mfma_f32_16x16x32_bf16 v[180:183], v[92:95], v[208:211], v[180:183]\n\t"
            "v_mfma_f32_16x16x32_bf16 v[180:183], v[88:91], v[204:207], v[180:183]\n\t"
            "v_mfma_f32_16x16x32_bf16 v[184:187], v[112:115], v[204:207], v[184:187]\n\t"
            "v_mfma_f32_16x16x32_bf16 v[184:187], v[116:119], v[208:211], v[184:187]\n\t"
            "v_mfma_f32_16x16x32_bf16 v[188:191], v[140:143], v[208:211], v[188:191]\n\t"
            "ds_read_b128 v[208:211], v233 offset:12416\n\t"
            "v_mfma_f32_16x16x32_bf16 v[188:191], v[136:139], v[204:207], v[188:191]\n\t"
            "ds_read_b128 v[204:207], v232 offset:12416\n\t"
            "s_waitcnt lgkmcnt(0)\n\t"
            "v_subrev_u32 v249, s75, v251\n\t"
            "v_add_u32 v249, 162816, v249\n\t"
            "s_nop 7\n\t"
            ".p2align 6\n\t"
            "s_nop 0\n\t"
            "s_nop 0\n\t"
            "s_nop 0\n\t"
            ".Lwa_loop%=:\n\t"
            "s_waitcnt lgkmcnt(2)\n\t"
            "v_mfma_f32_16x16x32_bf16 v[192:195], v[0:3], v[212:215], 0\n\t"
            "s_bfe_u32 s49, s70, 0x30001\n\t"
            "s_lshl_b32 s49, s49, 4\n\t"
            "s_or_b32 s73, s77, s49\n\t"
            "v_mfma_f32_16x16x32_bf16 v[192:195], v[4:7], v[224:227], v[192:195]\n\t"
            "v_mad_u32_u24 v220, v246, s72, v253\n\t"
            "v_mad_u32_u24 v216, v246, s72, v252\n\t"
            "v_mfma_f32_16x16x32_bf16 v[148:151], v[28:31], v[224:227], 0\n\t"
            "ds_read_b64 v[222:223], v220 offset:160\n\t"
            "ds_read_b64 v[220:221], v220 offset:128\n\t"
            "v_mfma_f32_16x16x32_bf16 v[148:151], v[24:27], v[212:215], v[148:151]\n\t"
            "v_mul_f32 v176, v237, v176\n\t"
            "v_mul_f32 v177, v237, v177\n\t"
            "v_mfma_f32_16x16x32_bf16 v[196:199], v[48:51], v[212:215], 0\n\t"
            "v_mul_f32 v178, v237, v178\n\t"
            "v_mul_f32 v179, v237, v179\n\t"
            "v_mfma_f32_16x16x32_bf16 v[196:199], v[52:55], v[224:227], v[196:199]\n\t"
            "v_cvt_pk_bf16_f32 v176, v176, v177\n\t"
            "v_mul_f32 v168, v237, v168\n\t"
            "v_mfma_f32_16x16x32_bf16 v[200:203], v[76:79], v[224:227], 0\n\t"
            "v_mul_f32 v169, v237, v169\n\t"
            "v_cvt_pk_bf16_f32 v177, v178, v179\n\t"
            "v_mfma_f32_16x16x32_bf16 v[200:203], v[72:75], v[212:215], v[200:203]\n\t"
            "v_mul_f32 v170, v237, v170\n\t"
            "v_mul_f32 v171, v237, v171\n\t"
            "v_mfma_f32_16x16x32_bf16 v[160:163], v[96:99], v[212:215], 0\n\t"
            "v_cvt_pk_bf16_f32 v168, v168, v169\n\t"
            "v_cvt_pk_bf16_f32 v180, v180, v181\n\t"
            "v_mfma_f32_16x16x32_bf16 v[160:163], v[100:103], v[224:227], v[160:163]\n\t"
            "v_cvt_pk_bf16_f32 v178, v188, v189\n\t"
            "v_cvt_pk_bf16_f32 v179, v190, v191\n\t"
            "v_mfma_f32_16x16x32_bf16 v[164:167], v[124:127], v[224:227], 0\n\t"
            "ds_read_b128 v[224:227], v233 offset:12544\n\t"
            "v_mul_f32 v172, v237, v172\n\t"
            "v_mfma_f32_16x16x32_bf16 v[164:167], v[120:123], v[212:215], v[164:167]\n\t"
            "ds_read_b128 v[212:215], v232 offset:12544\n\t"
            "v_mul_f32 v173, v237, v173\n\t"
            "s_waitcnt lgkmcnt(4)\n\t"
            "v_mfma_f32_16x16x32_bf16 v[192:195], v[8:11], v[204:207], v[192:195]\n\t"
            "v_mul_f32 v174, v237, v174\n\t"
            "v_mul_f32 v175, v237, v175\n\t"
            "v_mfma_f32_16x16x32_bf16 v[192:195], v[12:15], v[208:211], v[192:195]\n\t"
            "v_cvt_pk_bf16_f32 v169, v170, v171\n\t"
            "v_cvt_pk_bf16_f32 v181, v182, v183\n\t"
            "v_mfma_f32_16x16x32_bf16 v[196:199], v[56:59], v[204:207], v[196:199]\n\t"
            "v_cvt_pk_bf16_f32 v170, v172, v173\n\t"
            "v_cvt_pk_bf16_f32 v171, v174, v175\n\t"
            "v_mfma_f32_16x16x32_bf16 v[196:199], v[60:63], v[208:211], v[196:199]\n\t"
            "v_cvt_pk_bf16_f32 v182, v184, v185\n\t"
            "v_cvt_pk_bf16_f32 v183, v186, v187\n\t"
            "v_mfma_f32_16x16x32_bf16 v[160:163], v[104:107], v[204:207], v[160:163]\n\t"
            "v_mov_b32 v230, s76\n\t"
            "v_mov_b32 v239, s74\n\t"
            "v_mfma_f32_16x16x32_bf16 v[160:163], v[108:111], v[208:211], v[160:163]\n\t"
            "s_add_u32 s70, s70, 1\n\t"
            "s_add_u32 s72, s72, 800\n\t"
            "s_add_u32 s74, s74, 800\n\t"
            "s_add_u32 s76, s76, 800\n\t"
            "v_mfma_f32_16x16x32_bf16 v[148:151], v[36:39], v[208:211], v[148:151]\n\t"
            "s_add_u32 s77, s77, 384\n\t"
            "s_xor_b32 s48, s48, s47\n\t"
            "s_sub_u32 s44, s44, 1\n\t"
            "v_mfma_f32_16x16x32_bf16 v[148:151], v[32:35], v[204:207], v[148:151]\n\t"
            "ds_read_b32 v230, v230\n\t"
            "ds_read_b32 v239, v239\n\t"
            "v_mfma_f32_16x16x32_bf16 v[200:203], v[84:87], v[208:211], v[200:203]\n\t"
            "v_mul_f32 v237, 0x3e553b94, v237\n\t"
            "v_mfma_f32_16x16x32_bf16 v[200:203], v[80:83], v[204:207], v[200:203]\n\t"
            "v_mfma_f32_16x16x32_bf16 v[164:167], v[132:135], v[208:211], v[164:167]\n\t"
            "v_mfma_f32_16x16x32_bf16 v[164:167], v[128:131], v[204:207], v[164:167]\n\t"
            "s_waitcnt lgkmcnt(2)\n\t"
            "v_mfma_f32_16x16x32_bf16 v[192:195], v[16:19], v[212:215], v[192:195]\n\t"
            "v_mfma_f32_16x16x32_bf16 v[192:195], v[20:23], v[224:227], v[192:195]\n\t"
            "v_mfma_f32_16x16x32_bf16 v[196:199], v[64:67], v[212:215], v[196:199]\n\t"
            "v_mfma_f32_16x16x32_bf16 v[196:199], v[68:71], v[224:227], v[196:199]\n\t"
            "v_mfma_f32_16x16x32_bf16 v[160:163], v[112:115], v[212:215], v[160:163]\n\t"
            "v_mfma_f32_16x16x32_bf16 v[160:163], v[116:119], v[224:227], v[160:163]\n\t"
            "v_mfma_f32_16x16x32_bf16 v[148:151], v[44:47], v[224:227], v[148:151]\n\t"
            "v_mfma_f32_16x16x32_bf16 v[148:151], v[40:43], v[212:215], v[148:151]\n\t"
            "s_nop 1\n\t"
            "v_mul_f32 v192, v238, v192\n\t"
            "v_mfma_f32_16x16x32_bf16 v[200:203], v[92:95], v[224:227], v[200:203]\n\t"
            "v_mul_f32 v193, v238, v193\n\t"
            "v_mul_f32 v194, v238, v194\n\t"
            "v_mfma_f32_16x16x32_bf16 v[200:203], v[88:91], v[212:215], v[200:203]\n\t"
            "v_mul_f32 v195, v238, v195\n\t"
            "v_cvt_pk_bf16_f32 v192, v192, v193\n\t"
            "v_mfma_f32_16x16x32_bf16 v[164:167], v[140:143], v[224:227], v[164:167]\n\t"
            "ds_read_b128 v[224:227], v216 offset:64\n\t"
            "ds_read_b128 v[216:219], v216\n\t"
            "v_mfma_f32_16x16x32_bf16 v[164:167], v[136:139], v[212:215], v[164:167]\n\t"
            "v_mul_f32 v148, v238, v148\n\t"
            "v_mul_f32 v149, v238, v149\n\t"
            "v_mfma_f32_16x16x16_bf16 v[172:175], v[220:221], v[154:155], 0\n\t"
            "v_mul_f32 v150, v238, v150\n\t"
            "v_mul_f32 v151, v238, v151\n\t"
            "v_cvt_pk_bf16_f32 v193, v194, v195\n\t"
            "v_mfma_f32_16x16x16_bf16 v[184:187], v[152:153], v[178:179], 0\n\t"
            "v_mul_f32 v196, v238, v196\n\t"
            "v_mul_f32 v197, v238, v197\n\t"
            "v_cvt_pk_bf16_f32 v194, v148, v149\n\t"
            "v_mfma_f32_16x16x16_bf16 v[208:211], v[220:221], v[178:179], 0\n\t"
            "v_cvt_pk_bf16_f32 v195, v150, v151\n\t"
            "v_cvt_pk_bf16_f32 v200, v200, v201\n\t"
            "v_mul_f32 v198, v238, v198\n\t"
            "v_mfma_f32_16x16x16_bf16 v[148:151], v[152:153], v[154:155], 0\n\t"
            "v_mul_f32 v199, v238, v199\n\t"
            "v_cvt_pk_bf16_f32 v196, v196, v197\n\t"
            "v_cvt_pk_bf16_f32 v201, v202, v203\n\t"
            "v_mfma_f32_16x16x16_bf16 v[204:207], v[176:177], v[178:179], 0\n\t"
            "v_cvt_pk_bf16_f32 v197, v198, v199\n\t"
            "v_cvt_pk_bf16_f32 v202, v160, v161\n\t"
            "v_cvt_pk_bf16_f32 v203, v162, v163\n\t"
            "v_mfma_f32_16x16x16_bf16 v[160:163], v[176:177], v[154:155], 0\n\t"
            "v_cvt_pk_bf16_f32 v198, v164, v165\n\t"
            "v_cvt_pk_bf16_f32 v199, v166, v167\n\t"
            "s_setprio 0\n\t"
            "s_setprio 3\n\t"
            "v_mfma_f32_16x16x16_bf16 v[164:167], v[196:197], v[154:155], 0\n\t"
            "v_mul_f32 v238, 0x3e553b94, v238\n\t"
            "v_mfma_f32_16x16x16_bf16 v[188:191], v[196:197], v[178:179], 0\n\t"
            "v_mfma_f32_16x16x32_bf16 v[148:151], v[144:147], v[156:159], v[148:151]\n\t"
            "s_waitcnt lgkmcnt(0)\n\t"
            "v_mfma_f32_16x16x32_bf16 v[172:175], v[216:219], v[156:159], v[172:175]\n\t"
            "v_mfma_f32_16x16x32_bf16 v[184:187], v[144:147], v[180:183], v[184:187]\n\t"
            "v_mfma_f32_16x16x32_bf16 v[208:211], v[216:219], v[180:183], v[208:211]\n\t"
            "v_mfma_f32_16x16x32_bf16 v[160:163], v[168:171], v[156:159], v[160:163]\n\t"
            "v_mfma_f32_16x16x32_bf16 v[204:207], v[168:171], v[180:183], v[204:207]\n\t"
            "v_mfma_f32_16x16x32_bf16 v[164:167], v[192:195], v[156:159], v[164:167]\n\t"
            "v_mfma_f32_16x16x32_bf16 v[188:191], v[192:195], v[180:183], v[188:191]\n\t"
            "s_nop 1\n\t"
            "v_max3_f32 v228, v172, v148, v149\n\t"
            "v_mfma_f32_16x16x16_bf16 v[212:215], v[222:223], v[152:153], 0\n\t"
            "v_max3_f32 v228, v228, v150, v151\n\t"
            "v_max3_f32 v229, v208, v184, v185\n\t"
            "v_mfma_f32_16x16x16_bf16 v[156:159], v[222:223], v[196:197], 0\n\t"
            "v_max3_f32 v229, v229, v186, v187\n\t"
            "v_max3_f32 v228, v228, v160, v161\n\t"
            "v_max3_f32 v229, v229, v204, v205\n\t"
            "v_mfma_f32_16x16x32_bf16 v[212:215], v[224:227], v[144:147], v[212:215]\n\t"
            "v_max3_f32 v228, v228, v162, v163\n\t"
            "v_max3_f32 v229, v229, v206, v207\n\t"
            "v_mfma_f32_16x16x32_bf16 v[156:159], v[224:227], v[192:195], v[156:159]\n\t"
            "v_max3_f32 v228, v228, v164, v165\n\t"
            "v_max3_f32 v229, v229, v188, v189\n\t"
            "v_mfma_f32_16x16x16_bf16 v[180:183], v[152:153], v[198:199], 0\n\t"
            "v_max3_f32 v228, v228, v166, v167\n\t"
            "v_max3_f32 v229, v229, v190, v191\n\t"
            "s_nop 1\n\t"
            "v_permlane32_swap_b32 v228, v229\n\t"
            "v_max_f32 v228, v228, v229\n\t"
            "v_mov_b32 v231, v228\n\t"
            "v_mfma_f32_16x16x32_bf16 v[180:183], v[144:147], v[200:203], v[180:183]\n\t"
            "s_setprio 0\n\t"
            "v_permlane16_swap_b32 v228, v231\n\t"
            "v_max_f32 v228, v228, v231\n\t"
            "v_mov_b32 v229, v228\n\t"
            "s_nop 1\n\t"
            "v_permlane32_swap_b32 v228, v229\n\t"
            "v_mul_f32_e64 v228, -v228, v236\n\t"
            "v_fma_f32 v148, v148, v236, v228\n\t"
            "v_fma_f32 v149, v149, v236, v228\n\t"
            "v_exp_f32 v148, v148\n\t"
            "v_exp_f32 v149, v149\n\t"
            "v_fma_f32 v150, v150, v236, v228\n\t"
            "v_fma_f32 v151, v151, v236, v228\n\t"
            "v_exp_f32 v150, v150\n\t"
            "v_exp_f32 v151, v151\n\t"
            "v_fma_f32 v160, v160, v236, v228\n\t"
            "v_fma_f32 v161, v161, v236, v228\n\t"
            "v_fma_f32 v162, v162, v236, v228\n\t"
            "v_fma_f32 v163, v163, v236, v228\n\t"
            "v_exp_f32 v160, v160\n\t"
            "v_exp_f32 v161, v161\n\t"
            "v_exp_f32 v162, v162\n\t"
            "v_exp_f32 v163, v163\n\t"
            "v_cvt_pk_bf16_f32 v148, v148, v149\n\t"
            "v_cvt_pk_bf16_f32 v149, v150, v151\n\t"
            "v_cvt_pk_bf16_f32 v150, v160, v161\n\t"
            "v_cvt_pk_bf16_f32 v151, v162, v163\n\t"
            "s_setprio 3\n\t"
            "v_mfma_f32_16x16x16_bf16 v[160:163], v[222:223], v[176:177], 0\n\t"
            "s_setprio 0\n\t"
            "v_mul_f32_e64 v229, -v229, v237\n\t"
            "v_fma_f32 v188, v188, v237, v229\n\t"
            "v_fma_f32 v189, v189, v237, v229\n\t"
            "v_exp_f32 v188, v188\n\t"
            "v_exp_f32 v189, v189\n\t"
            "v_fma_f32 v190, v190, v237, v229\n\t"
            "s_setprio 3\n\t"
            "v_mfma_f32_16x16x32_bf16 v[160:163], v[224:227], v[168:171], v[160:163]\n\t"
            "s_setprio 0\n\t"
            "v_fma_f32 v191, v191, v237, v229\n\t"
            "v_fma_f32 v208, v208, v237, v229\n\t"
            "v_fma_f32 v184, v184, v237, v229\n\t"
            "v_fma_f32 v185, v185, v237, v229\n\t"
            "v_exp_f32 v190, v190\n\t"
            "v_exp_f32 v191, v191\n\t"
            "v_exp_f32 v208, v208\n\t"
            "v_exp_f32 v184, v184\n\t"
            "v_max3_f32 v234, v212, v160, v156\n\t"
            "v_max_f32 v234, v234, v230\n\t"
            "v_exp_f32 v185, v185\n\t"
            "v_fma_f32 v186, v186, v237, v229\n\t"
            "v_fma_f32 v187, v187, v237, v229\n\t"
            "v_fma_f32 v164, v164, v236, v228\n\t"
            "v_fma_f32 v165, v165, v236, v228\n\t"
            "v_exp_f32 v186, v186\n\t"
            "v_exp_f32 v187, v187\n\t"
            "v_fma_f32 v204, v204, v237, v229\n\t"
            "v_fma_f32 v205, v205, v237, v229\n\t"
            "v_fma_f32 v206, v206, v237, v229\n\t"
            "v_fma_f32 v207, v207, v237, v229\n\t"
            "v_cvt_pk_bf16_f32 v188, v188, v189\n\t"
            "v_exp_f32 v164, v164\n\t"
            "v_exp_f32 v165, v165\n\t"
            "v_fma_f32 v166, v166, v236, v228\n\t"
            "v_fma_f32 v167, v167, v236, v228\n\t"
            "v_fma_f32 v172, v172, v236, v228\n\t"
            "v_exp_f32 v204, v204\n\t"
            "v_exp_f32 v205, v205\n\t"
            "v_exp_f32 v206, v206\n\t"
            "v_exp_f32 v207, v207\n\t"
            "v_cvt_pk_bf16_f32 v189, v190, v191\n\t"
            "v_cvt_pk_bf16_f32 v208, v208, 0\n\t"
            "v_max_f32_dpp v234, v234, v234 row_ror:8 row_mask:0xf bank_mask:0xf\n\t"
            "v_exp_f32 v166, v166\n\t"
            "v_exp_f32 v167, v167\n\t"
            "v_exp_f32 v172, v172\n\t"
            "v_cvt_pk_bf16_f32 v184, v184, v185\n\t"
            "v_cndmask_b32_e64 v190, 0, v208, s[58:59]\n\t"
            "s_setprio 3\n\t"
            "v_mfma_f32_16x16x16_bf16 v[208:211], v[220:221], v[198:199], 0\n\t"
            "v_cvt_pk_bf16_f32 v185, v186, v187\n\t"
            "v_cvt_pk_bf16_f32 v164, v164, v165\n\t"
            "v_cvt_pk_bf16_f32 v186, v204, v205\n\t"
            "v_cvt_pk_bf16_f32 v187, v206, v207\n\t"
            "v_mfma_f32_16x16x16_bf16 v[204:207], v[176:177], v[198:199], 0\n\t"
            "v_max_f32_dpp v234, v234, v234 row_ror:4 row_mask:0xf bank_mask:0xf\n\t"
            "v_cvt_pk_bf16_f32 v165, v166, v167\n\t"
            "v_cvt_pk_bf16_f32 v172, v172, 0\n\t"
            "v_mfma_f32_16x16x32_bf16 v[208:211], v[216:219], v[200:203], v[208:211]\n\t"
            "v_cndmask_b32_e64 v166, 0, v172, s[58:59]\n\t"
            "v_max_f32_dpp v234, v234, v234 row_ror:2 row_mask:0xf bank_mask:0xf\n\t"
            "v_mfma_f32_16x16x16_bf16 v[172:175], v[196:197], v[198:199], 0\n\t"
            "v_cndmask_b32_e64 v235, v212, v160, s[60:61]\n\t"
            "v_cndmask_b32_e64 v235, v235, v156, s[62:63]\n\t"
            "v_max_f32_dpp v234, v234, v234 row_ror:1 row_mask:0xf bank_mask:0xf\n\t"
            "v_mfma_f32_16x16x32_bf16 v[204:207], v[168:171], v[200:203], v[204:207]\n\t"
            "v_cndmask_b32_e64 v235, v235, v230, s[64:65]\n\t"
            "v_sub_f32 v235, v235, v234\n\t"
            "v_mfma_f32_16x16x32_bf16 v[172:175], v[192:195], v[200:203], v[172:175]\n\t"
            "s_setprio 0\n\t"
            "v_mul_f32 v235, v239, v235\n\t"
            "v_exp_f32 v235, v235\n\t"
            "v_max3_f32 v230, v208, v180, v181\n\t"
            "v_cvt_pk_bf16_f32 v235, v235, 0\n\t"
            "v_cndmask_b32_e64 v235, 0, v235, s[66:67]\n\t"
            "v_max3_f32 v230, v230, v182, v183\n\t"
            "v_max3_f32 v230, v230, v204, v205\n\t"
            "v_max3_f32 v230, v230, v206, v207\n\t"
            "v_max3_f32 v230, v230, v172, v173\n\t"
            "v_max3_f32 v230, v230, v174, v175\n\t"
            "v_mov_b32 v231, v230\n\t"
            "v_mov_b32 v167, 0\n\t"
            "v_mov_b32 v191, 0\n\t"
            "v_permlane32_swap_b32 v230, v231\n\t"
            "v_max_f32 v230, v230, v231\n\t"
            "v_mov_b32 v231, v230\n\t"
            "ds_write_b16 v250, v235 offset:256\n\t"
            "s_nop 0\n\t"
            "v_permlane16_swap_b32 v230, v231\n\t"
            "v_max_f32 v230, v230, v231\n\t"
            "v_mul_f32_e64 v230, -v230, v238\n\t"
            "v_fma_f32 v180, v180, v238, v230\n\t"
            "v_fma_f32 v181, v181, v238, v230\n\t"
            "v_fma_f32 v172, v172, v238, v230\n\t"
            "v_fma_f32 v173, v173, v238, v230\n\t"
            "v_exp_f32 v180, v180\n\t"
            "v_exp_f32 v181, v181\n\t"
            "v_fma_f32 v182, v182, v238, v230\n\t"
            "v_fma_f32 v183, v183, v238, v230\n\t"
            "v_exp_f32 v172, v172\n\t"
            "v_exp_f32 v173, v173\n\t"
            "v_fma_f32 v174, v174, v238, v230\n\t"
            "v_fma_f32 v175, v175, v238, v230\n\t"
            "v_fma_f32 v208, v208, v238, v230\n\t"
            "v_exp_f32 v182, v182\n\t"
            "v_exp_f32 v183, v183\n\t"
            "v_fma_f32 v204, v204, v238, v230\n\t"
            "v_fma_f32 v205, v205, v238, v230\n\t"
            "v_fma_f32 v206, v206, v238, v230\n\t"
            "v_fma_f32 v207, v207, v238, v230\n\t"
            "v_exp_f32 v174, v174\n\t"
            "v_exp_f32 v175, v175\n\t"
            "v_exp_f32 v208, v208\n\t"
            "v_exp_f32 v204, v204\n\t"
            "v_exp_f32 v205, v205\n\t"
            "v_exp_f32 v206, v206\n\t"
            "v_exp_f32 v207, v207\n\t"
            "v_cvt_pk_bf16_f32 v180, v180, v181\n\t"
            "v_cvt_pk_bf16_f32 v172, v172, v173\n\t"
            "v_cvt_pk_bf16_f32 v181, v182, v183\n\t"
            "v_cvt_pk_bf16_f32 v173, v174, v175\n\t"
            "v_cvt_pk_bf16_f32 v208, v208, 0\n\t"
            "v_cvt_pk_bf16_f32 v182, v204, v205\n\t"
            "v_cvt_pk_bf16_f32 v183, v206, v207\n\t"
            "v_cndmask_b32_e64 v174, 0, v208, s[58:59]\n\t"
            "v_mov_b32 v175, 0\n\t"
            "s_barrier\n\t"
            "s_setprio 2\n\t"
            "v_mfma_f32_16x16x32_bf16 v[200:203], v[240:243], v[148:151], 0\n\t"
            "ds_read_b64 v[176:177], v247\n\t"
            "ds_read_b64 v[196:197], v247 offset:1536\n\t"
            "v_mfma_f32_16x16x32_bf16 v[200:203], v[240:243], v[164:167], v[200:203]\n\t"
            "ds_read_b64 v[220:221], v247 offset:3072\n\t"
            "v_xor_b32 v234, s73, v254\n\t"
            "v_add_u32 v235, s45, v248\n\t"
            "v_add_u32 v232, s48, v244\n\t"
            "v_xor_b32 v233, 64, v232\n\t"
            "v_mov_b32 v179, 0\n\t"
            "v_mov_b32 v199, 0\n\t"
            "v_mov_b32 v223, 0\n\t"
            "v_rcp_f32 v201, v200\n\t"
            "v_xor_b32 v249, 0x100, v249\n\t"
            "ds_read_b128 v[168:171], v235\n\t"
            "ds_read_u16 v178, v234\n\t"
            "ds_read_b128 v[192:195], v235 offset:1536\n\t"
            "ds_read_u16 v198, v234 offset:128\n\t"
            "ds_read_b128 v[216:219], v235 offset:3072\n\t"
            "ds_read_u16 v222, v234 offset:256\n\t"
            "ds_read_b128 v[204:207], v232\n\t"
            "ds_read_b128 v[208:211], v233\n\t"
            "ds_read_b32 v236, v249\n\t"
            "ds_read_b32 v237, v249 offset:64\n\t"
            "ds_read_b32 v238, v249 offset:128\n\t"
            "v_mad_u32_u24 v234, v246, s75, v253\n\t"
            "s_waitcnt lgkmcnt(9)\n\t"
            "v_mfma_f32_16x16x32_bf16 v[224:227], v[168:171], v[148:151], 0\n\t"
            "v_mfma_f32_16x16x32_bf16 v[224:227], v[176:179], v[164:167], v[224:227]\n\t"
            "s_waitcnt lgkmcnt(7)\n\t"
            "v_mfma_f32_16x16x32_bf16 v[212:215], v[192:195], v[148:151], 0\n\t"
            "v_mfma_f32_16x16x32_bf16 v[212:215], v[196:199], v[164:167], v[212:215]\n\t"
            "s_waitcnt lgkmcnt(5)\n\t"
            "v_mfma_f32_16x16x32_bf16 v[148:151], v[216:219], v[148:151], 0\n\t"
            "v_mfma_f32_16x16x32_bf16 v[148:151], v[220:223], v[164:167], v[148:151]\n\t"
            "s_waitcnt lgkmcnt(3)\n\t"
            "v_mfma_f32_16x16x32_bf16 v[144:147], v[0:3], v[204:207], 0\n\t"
            "v_mfma_f32_16x16x32_bf16 v[144:147], v[4:7], v[208:211], v[144:147]\n\t"
            "v_mul_f32 v224, v201, v224\n\t"
            "v_mfma_f32_16x16x32_bf16 v[152:155], v[48:51], v[204:207], 0\n\t"
            "v_mul_f32 v225, v201, v225\n\t"
            "v_mul_f32 v226, v201, v226\n\t"
            "v_mfma_f32_16x16x32_bf16 v[152:155], v[52:55], v[208:211], v[152:155]\n\t"
            "v_mul_f32 v212, v201, v212\n\t"
            "v_mul_f32 v213, v201, v213\n\t"
            "v_mfma_f32_16x16x32_bf16 v[156:159], v[76:79], v[208:211], 0\n\t"
            "v_mul_f32 v227, v201, v227\n\t"
            "v_cvt_pk_bf16_f32 v224, v224, v225\n\t"
            "v_mfma_f32_16x16x32_bf16 v[156:159], v[72:75], v[204:207], v[156:159]\n\t"
            "v_mul_f32 v214, v201, v214\n\t"
            "v_mul_f32 v215, v201, v215\n\t"
            "v_mfma_f32_16x16x32_bf16 v[160:163], v[96:99], v[204:207], 0\n\t"
            "v_cvt_pk_bf16_f32 v212, v212, v213\n\t"
            "v_cvt_pk_bf16_f32 v225, v226, v227\n\t"
            "v_mfma_f32_16x16x32_bf16 v[160:163], v[100:103], v[208:211], v[160:163]\n\t"
            "v_cvt_pk_bf16_f32 v213, v214, v215\n\t"
            "v_mul_f32 v148, v201, v148\n\t"
            "v_mfma_f32_16x16x32_bf16 v[164:167], v[124:127], v[208:211], 0\n\t"
            "v_mul_f32 v149, v201, v149\n\t"
            "v_mul_f32 v150, v201, v150\n\t"
            "v_mfma_f32_16x16x32_bf16 v[164:167], v[120:123], v[204:207], v[164:167]\n\t"
            "ds_write_b64 v245, v[224:225]\n\t"
            "v_mfma_f32_16x16x32_bf16 v[224:227], v[168:171], v[184:187], 0\n\t"
            "v_mul_f32 v151, v201, v151\n\t"
            "v_cvt_pk_bf16_f32 v148, v148, v149\n\t"
            "v_mfma_f32_16x16x32_bf16 v[224:227], v[176:179], v[188:191], v[224:227]\n\t"
            "ds_write_b64 v245, v[212:213] offset:128\n\t"
            "v_mfma_f32_16x16x32_bf16 v[200:203], v[240:243], v[184:187], 0\n\t"
            "v_cvt_pk_bf16_f32 v149, v150, v151\n\t"
            "v_mfma_f32_16x16x32_bf16 v[200:203], v[240:243], v[188:191], v[200:203]\n\t"
            "ds_write_b64 v245, v[148:149] offset:256\n\t"
            "v_mfma_f32_16x16x32_bf16 v[212:215], v[192:195], v[184:187], 0\n\t"
            "v_mfma_f32_16x16x32_bf16 v[212:215], v[196:199], v[188:191], v[212:215]\n\t"
            "v_mfma_f32_16x16x32_bf16 v[184:187], v[216:219], v[184:187], 0\n\t"
            "v_mfma_f32_16x16x32_bf16 v[184:187], v[220:223], v[188:191], v[184:187]\n\t"
            "v_mfma_f32_16x16x32_bf16 v[148:151], v[28:31], v[208:211], 0\n\t"
            "ds_read_b128 v[208:211], v233 offset:128\n\t"
            "v_mfma_f32_16x16x32_bf16 v[148:151], v[24:27], v[204:207], v[148:151]\n\t"
            "ds_read_b128 v[204:207], v232 offset:128\n\t"
            "v_rcp_f32 v201, v200\n\t"
            "v_mfma_f32_16x16x32_bf16 v[188:191], v[192:195], v[180:183], 0\n\t"
            "v_mul_f32 v224, v201, v224\n\t"
            "v_mul_f32 v225, v201, v225\n\t"
            "v_mfma_f32_16x16x32_bf16 v[188:191], v[196:199], v[172:175], v[188:191]\n\t"
            "v_mul_f32 v226, v201, v226\n\t"
            "v_mul_f32 v227, v201, v227\n\t"
            "v_mul_f32 v212, v201, v212\n\t"
            "v_mul_f32 v213, v201, v213\n\t"
            "v_mul_f32 v214, v201, v214\n\t"
            "v_mul_f32 v215, v201, v215\n\t"
            "v_mul_f32 v184, v201, v184\n\t"
            "v_mul_f32 v185, v201, v185\n\t"
            "v_mul_f32 v186, v201, v186\n\t"
            "v_mul_f32 v187, v201, v187\n\t"
            "v_mfma_f32_16x16x32_bf16 v[200:203], v[240:243], v[180:183], 0\n\t"
            "v_cvt_pk_bf16_f32 v224, v224, v225\n\t"
            "v_cvt_pk_bf16_f32 v225, v226, v227\n\t"
            "v_mfma_f32_16x16x32_bf16 v[200:203], v[240:243], v[172:175], v[200:203]\n\t"
            "ds_write_b64 v245, v[224:225] offset:6144\n\t"
            "v_mfma_f32_16x16x32_bf16 v[224:227], v[168:171], v[180:183], 0\n\t"
            "v_cvt_pk_bf16_f32 v184, v184, v185\n\t"
            "v_cvt_pk_bf16_f32 v185, v186, v187\n\t"
            "v_mfma_f32_16x16x32_bf16 v[224:227], v[176:179], v[172:175], v[224:227]\n\t"
            "ds_read_b64 v[186:187], v234 offset:288\n\t"
            "ds_write_b64 v245, v[184:185] offset:6400\n\t"
            "s_waitcnt lgkmcnt(3)\n\t"
            "v_mfma_f32_16x16x32_bf16 v[144:147], v[8:11], v[204:207], v[144:147]\n\t"
            "ds_read_b64 v[184:185], v234 offset:256\n\t"
            "v_rcp_f32 v201, v200\n\t"
            "v_mfma_f32_16x16x32_bf16 v[144:147], v[12:15], v[208:211], v[144:147]\n\t"
            "v_mul_f32 v188, v201, v188\n\t"
            "v_mul_f32 v189, v201, v189\n\t"
            "v_mfma_f32_16x16x32_bf16 v[148:151], v[36:39], v[208:211], v[148:151]\n\t"
            "v_mul_f32 v190, v201, v190\n\t"
            "v_mul_f32 v191, v201, v191\n\t"
            "v_mfma_f32_16x16x32_bf16 v[148:151], v[32:35], v[204:207], v[148:151]\n\t"
            "v_cvt_pk_bf16_f32 v188, v188, v189\n\t"
            "v_cvt_pk_bf16_f32 v189, v190, v191\n\t"
            "v_mfma_f32_16x16x32_bf16 v[152:155], v[56:59], v[204:207], v[152:155]\n\t"
            "ds_read_b64 v[190:191], v234 offset:352\n\t"
            "v_mul_f32 v224, v201, v224\n\t"
            "v_mfma_f32_16x16x32_bf16 v[152:155], v[60:63], v[208:211], v[152:155]\n\t"
            "ds_write_b64 v245, v[188:189] offset:12416\n\t"
            "v_mfma_f32_16x16x32_bf16 v[156:159], v[84:87], v[208:211], v[156:159]\n\t"
            "ds_read_b64 v[188:189], v234 offset:320\n\t"
            "v_mul_f32 v225, v201, v225\n\t"
            "v_mfma_f32_16x16x32_bf16 v[156:159], v[80:83], v[204:207], v[156:159]\n\t"
            "v_cvt_pk_bf16_f32 v212, v212, v213\n\t"
            "v_mul_f32 v226, v201, v226\n\t"
            "v_mfma_f32_16x16x32_bf16 v[160:163], v[104:107], v[204:207], v[160:163]\n\t"
            "v_mul_f32 v227, v201, v227\n\t"
            "v_cvt_pk_bf16_f32 v224, v224, v225\n\t"
            "v_mfma_f32_16x16x32_bf16 v[160:163], v[108:111], v[208:211], v[160:163]\n\t"
            "v_cvt_pk_bf16_f32 v213, v214, v215\n\t"
            "v_cvt_pk_bf16_f32 v225, v226, v227\n\t"
            "v_mfma_f32_16x16x32_bf16 v[164:167], v[132:135], v[208:211], v[164:167]\n\t"
            "ds_read_b128 v[208:211], v233 offset:6144\n\t"
            "v_mfma_f32_16x16x32_bf16 v[164:167], v[128:131], v[204:207], v[164:167]\n\t"
            "ds_read_b128 v[204:207], v232 offset:6144\n\t"
            "ds_write_b64 v245, v[212:213] offset:6272\n\t"
            "v_mfma_f32_16x16x32_bf16 v[180:183], v[216:219], v[180:183], 0\n\t"
            "ds_read_b128 v[212:215], v232 offset:256\n\t"
            "v_mfma_f32_16x16x32_bf16 v[180:183], v[220:223], v[172:175], v[180:183]\n\t"
            "ds_write_b64 v245, v[224:225] offset:12288\n\t"
            "ds_read_b128 v[224:227], v233 offset:256\n\t"
            "s_waitcnt lgkmcnt(6)\n\t"
            "v_mfma_f32_16x16x32_bf16 v[168:171], v[184:187], v[168:171], 0\n\t"
            "v_mfma_f32_16x16x32_bf16 v[168:171], v[188:191], v[176:179], v[168:171]\n\t"
            "v_mfma_f32_16x16x32_bf16 v[192:195], v[184:187], v[192:195], 0\n\t"
            "v_mfma_f32_16x16x32_bf16 v[192:195], v[188:191], v[196:199], v[192:195]\n\t"
            "s_nop 0\n\t"
            "v_mul_f32 v180, v201, v180\n\t"
            "v_mfma_f32_16x16x32_bf16 v[216:219], v[184:187], v[216:219], 0\n\t"
            "v_mul_f32 v181, v201, v181\n\t"
            "v_mul_f32 v182, v201, v182\n\t"
            "v_mfma_f32_16x16x32_bf16 v[216:219], v[188:191], v[220:223], v[216:219]\n\t"
            "v_mul_f32 v183, v201, v183\n\t"
            "v_cvt_pk_bf16_f32 v180, v180, v181\n\t"
            "v_mfma_f32_16x16x32_bf16 v[200:203], v[240:243], v[184:187], 0\n\t"
            "v_cvt_pk_bf16_f32 v181, v182, v183\n\t"
            "v_mfma_f32_16x16x32_bf16 v[200:203], v[240:243], v[188:191], v[200:203]\n\t"
            "ds_write_b64 v245, v[180:181] offset:12544\n\t"
            "s_waitcnt lgkmcnt(1)\n\t"
            "v_mfma_f32_16x16x32_bf16 v[144:147], v[16:19], v[212:215], v[144:147]\n\t"
            "v_mfma_f32_16x16x32_bf16 v[144:147], v[20:23], v[224:227], v[144:147]\n\t"
            "v_mfma_f32_16x16x32_bf16 v[148:151], v[44:47], v[224:227], v[148:151]\n\t"
            "v_mfma_f32_16x16x32_bf16 v[148:151], v[40:43], v[212:215], v[148:151]\n\t"
            "v_mfma_f32_16x16x32_bf16 v[152:155], v[64:67], v[212:215], v[152:155]\n\t"
            "s_nop 0\n\t"
            "v_cndmask_b32_e64 v203, v168, v192, s[60:61]\n\t"
            "v_mfma_f32_16x16x32_bf16 v[152:155], v[68:71], v[224:227], v[152:155]\n\t"
            "v_rcp_f32 v201, v200\n\t"
            "v_mfma_f32_16x16x32_bf16 v[156:159], v[92:95], v[224:227], v[156:159]\n\t"
            "v_cndmask_b32_e64 v203, v203, v216, s[62:63]\n\t"
            "v_xad_u32 v202, s73, v254, v255\n\t"
            "v_mfma_f32_16x16x32_bf16 v[156:159], v[88:91], v[212:215], v[156:159]\n\t"
            "v_mul_f32_dpp v203, v201, v203 quad_perm:[0,0,0,0] row_mask:0xf bank_mask:0xf\n\t"
            "v_cvt_pk_bf16_f32 v203, v203, 0\n\t"
            "v_mfma_f32_16x16x32_bf16 v[160:163], v[112:115], v[212:215], v[160:163]\n\t"
            "v_mul_f32 v144, v236, v144\n\t"
            "v_mul_f32 v145, v236, v145\n\t"
            "v_mfma_f32_16x16x32_bf16 v[160:163], v[116:119], v[224:227], v[160:163]\n\t"
            "s_mov_b64 exec, s[68:69]\n\t"
            "ds_write_b16 v202, v203\n\t"
            "s_mov_b64 exec, 1\n\t"
            "ds_add_u32 v253, v246 offset:7856\n\t"
            "s_mov_b64 exec, -1\n\t"
            "v_mfma_f32_16x16x32_bf16 v[164:167], v[140:143], v[224:227], v[164:167]\n\t"
            "ds_read_b128 v[224:227], v233 offset:6272\n\t"
            "v_mul_f32 v152, v236, v152\n\t"
            "v_mfma_f32_16x16x32_bf16 v[164:167], v[136:139], v[212:215], v[164:167]\n\t"
            "s_cmp_eq_u32 s44, 0\n\t"
            "s_cbranch_scc1 .Lwa_done%=\n\t"
            "ds_read_b128 v[212:215], v232 offset:6272\n\t"
            "v_mul_f32 v153, v236, v153\n\t"
            "v_mfma_f32_16x16x32_bf16 v[168:171], v[0:3], v[204:207], 0\n\t"
            "v_mul_f32 v146, v236, v146\n\t"
            "v_mul_f32 v147, v236, v147\n\t"
            "v_mfma_f32_16x16x32_bf16 v[168:171], v[4:7], v[208:211], v[168:171]\n\t"
            "v_mul_f32 v154, v236, v154\n\t"
            "v_mul_f32 v155, v236, v155\n\t"
            "v_mfma_f32_16x16x32_bf16 v[172:175], v[28:31], v[208:211], 0\n\t"
            "v_cvt_pk_bf16_f32 v144, v144, v145\n\t"
            "v_cvt_pk_bf16_f32 v152, v152, v153\n\t"
            "v_mfma_f32_16x16x32_bf16 v[172:175], v[24:27], v[204:207], v[172:175]\n\t"
            "v_cvt_pk_bf16_f32 v156, v156, v157\n\t"
            "v_mul_f32 v148, v236, v148\n\t"
            "v_mfma_f32_16x16x32_bf16 v[176:179], v[48:51], v[204:207], 0\n\t"
            "v_mul_f32 v149, v236, v149\n\t"
            "v_mul_f32 v150, v236, v150\n\t"
            "v_mfma_f32_16x16x32_bf16 v[176:179], v[52:55], v[208:211], v[176:179]\n\t"
            "v_mul_f32 v151, v236, v151\n\t"
            "v_cvt_pk_bf16_f32 v145, v146, v147\n\t"
            "v_mfma_f32_16x16x32_bf16 v[180:183], v[76:79], v[208:211], 0\n\t"
            "v_cvt_pk_bf16_f32 v153, v154, v155\n\t"
            "v_cvt_pk_bf16_f32 v157, v158, v159\n\t"
            "v_mfma_f32_16x16x32_bf16 v[180:183], v[72:75], v[204:207], v[180:183]\n\t"
            "v_cvt_pk_bf16_f32 v146, v148, v149\n\t"
            "v_cvt_pk_bf16_f32 v147, v150, v151\n\t"
            "v_mfma_f32_16x16x32_bf16 v[184:187], v[96:99], v[204:207], 0\n\t"
            "v_cvt_pk_bf16_f32 v158, v160, v161\n\t"
            "v_cvt_pk_bf16_f32 v159, v162, v163\n\t"
            "v_mfma_f32_16x16x32_bf16 v[184:187], v[100:103], v[208:211], v[184:187]\n\t"
            "v_cvt_pk_bf16_f32 v154, v164, v165\n\t"
            "v_cvt_pk_bf16_f32 v155, v166, v167\n\t"
            "v_mfma_f32_16x16x32_bf16 v[188:191], v[124:127], v[208:211], 0\n\t"
            "ds_read_b128 v[208:211], v233 offset:6400\n\t"
            "v_mul_f32 v236, 0x3e553b94, v236\n\t"
            "v_mfma_f32_16x16x32_bf16 v[188:191], v[120:123], v[204:207], v[188:191]\n\t"
            "ds_read_b128 v[204:207], v232 offset:6400\n\t"
            "s_waitcnt lgkmcnt(2)\n\t"
            "v_mfma_f32_16x16x32_bf16 v[168:171], v[8:11], v[212:215], v[168:171]\n\t"
            "v_mfma_f32_16x16x32_bf16 v[168:171], v[12:15], v[224:227], v[168:171]\n\t"
            "v_mfma_f32_16x16x32_bf16 v[172:175], v[36:39], v[224:227], v[172:175]\n\t"
            "v_mfma_f32_16x16x32_bf16 v[172:175], v[32:35], v[212:215], v[172:175]\n\t"
            "v_mfma_f32_16x16x32_bf16 v[176:179], v[56:59], v[212:215], v[176:179]\n\t"
            "v_mfma_f32_16x16x32_bf16 v[176:179], v[60:63], v[224:227], v[176:179]\n\t"
            "v_mfma_f32_16x16x32_bf16 v[180:183], v[84:87], v[224:227], v[180:183]\n\t"
            "v_mfma_f32_16x16x32_bf16 v[180:183], v[80:83], v[212:215], v[180:183]\n\t"
            "v_mfma_f32_16x16x32_bf16 v[184:187], v[104:107], v[212:215], v[184:187]\n\t"
            "v_mfma_f32_16x16x32_bf16 v[184:187], v[108:111], v[224:227], v[184:187]\n\t"
            "v_mfma_f32_16x16x32_bf16 v[188:191], v[132:135], v[224:227], v[188:191]\n\t"
            "ds_read_b128 v[224:227], v233 offset:12288\n\t"
            "v_mfma_f32_16x16x32_bf16 v[188:191], v[128:131], v[212:215], v[188:191]\n\t"
            "ds_read_b128 v[212:215], v232 offset:12288\n\t"
            "s_waitcnt lgkmcnt(2)\n\t"
            "v_mfma_f32_16x16x32_bf16 v[168:171], v[16:19], v[204:207], v[168:171]\n\t"
            "v_mfma_f32_16x16x32_bf16 v[168:171], v[20:23], v[208:211], v[168:171]\n\t"
            "v_mfma_f32_16x16x32_bf16 v[172:175], v[44:47], v[208:211], v[172:175]\n\t"
            "v_mfma_f32_16x16x32_bf16 v[172:175], v[40:43], v[204:207], v[172:175]\n\t"
            "v_mfma_f32_16x16x32_bf16 v[176:179], v[64:67], v[204:207], v[176:179]\n\t"
            "v_mfma_f32_16x16x32_bf16 v[176:179], v[68:71], v[208:211], v[176:179]\n\t"
            "v_mfma_f32_16x16x32_bf16 v[180:183], v[92:95], v[208:211], v[180:183]\n\t"
            "v_mfma_f32_16x16x32_bf16 v[180:183], v[88:91], v[204:207], v[180:183]\n\t"
            "v_mfma_f32_16x16x32_bf16 v[184:187], v[112:115], v[204:207], v[184:187]\n\t"
            "v_mfma_f32_16x16x32_bf16 v[184:187], v[116:119], v[208:211], v[184:187]\n\t"
            "v_mfma_f32_16x16x32_bf16 v[188:191], v[140:143], v[208:211], v[188:191]\n\t"
            "ds_read_b128 v[208:211], v233 offset:12416\n\t"
            "v_mfma_f32_16x16x32_bf16 v[188:191], v[136:139], v[204:207], v[188:191]\n\t"
            "ds_read_b128 v[204:207], v232 offset:12416\n\t"
            "s_branch .Lwa_loop%=\n\t"
            ".Lwa_done%=:\n\t"
            "s_waitcnt lgkmcnt(0)\n\t"
            "s_nop 7\n\t"
            "s_barrier\n\t"
            : "+{v249}"(aRSw_), "+{s44}"(s44), "+{s48}"(s48), "+{s70}"(s70), "+{s72}"(s72), "+{s73}"(s73), "+{s74}"(s74), "+{s76}"(s76), "+{s77}"(s77)
            : "{v244}"(pE_), "{v245}"(aO_), "{v246}"(sel_), "{v247}"(aVB_), "{v248}"(pST_), "{v250}"(aPLw_), "{v251}"(aRSr_), "{v252}"(g16_), "{v253}"(g8_), "{v254}"(loVL_), "{v255}"(loOL_), "{v[240:243]}"(ones4), "{s47}"((ldsb + X_OFF) ^ (ldsb + X1_OFF)),
              "{s[58:59]}"(0xFFFFull), "{s[60:61]}"(0xFFFF0000ull), "{s[62:63]}"(0xFFFF00000000ull), "{s[64:65]}"(0xFFFF000000000000ull),
              "{s[66:67]}"(0x1FFFFFFFFFFFFull), "{s75}"(rsb), "{s45}"(ldsb + V_OFF + w * 4608), "{s[68:69]}"(0xFFFFFFFFFFFFull),
              "{s[84:87]}"(wr), "{s46}"(wave * 36864), "{s88}"(touch), "{s71}"(ldsb + O_OFF), "{s[54:55]}"(mrow), "{s[56:57]}"(0xF0000F0000F0000Full),
              "{s[78:79]}"(mrow & 0xF0000F0000F0000Full)
            : "v144", "v145", "v146", "v147", "v148", "v149", "v150", "v151", "v152", "v153", "v154", "v155", "v156", "v157", "v158", "v159", "v160", "v161", "v162", "v163", "v164", "v165", "v166", "v167", "v168", "v169", "v170", "v171", "v172", "v173", "v174", "v175", "v176", "v177", "v178", "v179", "v180", "v181", "v182", "v183", "v184", "v185", "v186", "v187", "v188", "v189", "v190", "v191", "v192", "v193", "v194", "v195", "v196", "v197", "v198", "v199", "v200", "v201", "v202", "v203", "v204", "v205", "v206", "v207", "v208", "v209", "v210", "v211", "v212", "v213", "v214", "v215", "v216", "v217", "v218", "v219", "v220", "v221", "v222", "v223", "v224", "v225", "v226", "v227", "v228", "v229", "v230", "v231", "v232", "v233", "v234", "v235", "v236", "v237", "v238", "v239", "v0", "v1", "v2", "v3", "v4", "v5", "v6", "v7", "v8", "v9", "v10", "v11", "v12", "v13", "v14", "v15", "v16", "v17", "v18", "v19", "v20", "v21", "v22", "v23", "v24", "v25", "v26", "v27", "v28", "v29", "v30", "v31", "v32", "v33", "v34", "v35", "v36", "v37", "v38", "v39", "v40", "v41", "v42", "v43", "v44", "v45", "v46", "v47", "v48", "v49", "v50", "v51", "v52", "v53", "v54", "v55", "v56", "v57", "v58", "v59", "v60", "v61", "v62", "v63", "v64", "v65", "v66", "v67", "v68", "v69", "v70", "v71", "v72", "v73", "v74", "v75", "v76", "v77", "v78", "v79", "v80", "v81", "v82", "v83", "v84", "v85", "v86", "v87", "v88", "v89", "v90", "v91", "v92", "v93", "v94", "v95", "v96", "v97", "v98", "v99", "v100", "v101", "v102", "v103", "v104", "v105", "v106", "v107", "v108", "v109", "v110", "v111", "v112", "v113", "v114", "v115", "v116", "v117", "v118", "v119", "v120", "v121", "v122", "v123", "v124", "v125", "v126", "v127", "v128", "v129", "v130", "v131", "v132", "v133", "v134", "v135", "v136", "v137", "v138", "v139", "v140", "v141", "v142", "v143",  "m0", "s49", "vcc", "scc", "memory");

        }
        if (wave == 0 && blockIdx.x == 0) {
            int ln_;
            asm volatile("v_mbcnt_lo_u32_b32 %0, -1, 0\n\tv_mbcnt_hi_u32_b32 %0, -1, %0" : "=v"(ln_));
            KARGS
            unsigned long long tg;
            asm volatile("s_load_dwordx2 %0, %1, %2\n\ts_waitcnt lgkmcnt(0)" : "=s"(tg) : "s"(wb), "n"(EPO_OFF) : "memory");
            const unsigned lo = (unsigned)tg + 1u, hi = (unsigned)(tg >> 32);
            const auto wq = __builtin_amdgcn_make_buffer_rsrc((void*)wb, 0, WS_BYTES, 0x00020000);
            for (int it_ = 0; it_ < 100000; ++it_) {
                bool bad = false;
#pragma unroll
                for (int k = 0; k < 2; ++k) {
                    const u32x4 a_ = __builtin_amdgcn_raw_buffer_load_b128(wq, ARR_OFF + ln_ * 16, k * 1024, SC1);
                    bad |= a_[0] != lo || a_[1] != hi || a_[2] != lo || a_[3] != hi;
                }
                if (__builtin_amdgcn_ballot_w64(bad) == 0ull) break;
                __builtin_amdgcn_s_sleep(2);
            }
            if (ln_ == 0) __builtin_amdgcn_raw_buffer_store_b64((u32x2){lo, hi}, wq, EPO_OFF, 0, SC1);
        }
        asm volatile("wk_code_end:");
        __builtin_amdgcn_endpgm();
    } else if (wave < 6) {
        LANE_SETUP
        {
            const unsigned rsb = ldsb + RSX_OFF;
            const unsigned pE_ = fE - X_OFF;
            const unsigned aVAw_ = ldsb + V_OFF + w * 9216 + lane * 16, aVBw_ = ldsb + V_OFF + w * 9216 + 1024 + lane * 8;
            const unsigned pST_ = lane * 16;
            unsigned aRSw_ = rsb + 256 + lane * 4, aRSr_ = rsb + (lane & 48);
            B_SCALARS
            unsigned s44 = 4;
            asm volatile(
            "s_waitcnt lgkmcnt(0)\n\t"
            "s_mov_b32 s83, 0x10000\n\t"
            "s_mov_b32 s84, -47104\n\t"
            "s_nop 7\n\t"
            "s_setprio 3\n\t"
            "s_barrier\n\t"
            "buffer_load_dwordx4 v[140:143], v245, s[28:31], s82 offen sc1\n\t"
            "s_add_u32 s49, s33, 0\n\t"
            "buffer_load_dwordx4 v[0:3], v245, s[28:31], s49 offen sc1\n\t"
            "s_add_u32 s49, s33, 6144\n\t"
            "buffer_load_dwordx4 v[24:27], v245, s[28:31], s49 offen sc1\n\t"
            "s_add_u32 s49, s33, 12288\n\t"
            "buffer_load_dwordx4 v[48:51], v245, s[28:31], s49 offen sc1\n\t"
            "s_add_u32 s49, s33, 18432\n\t"
            "buffer_load_dwordx4 v[72:75], v245, s[28:31], s49 offen sc1\n\t"
            "s_add_u32 s49, s33, 24576\n\t"
            "buffer_load_dwordx4 v[96:99], v245, s[28:31], s49 offen sc1\n\t"
            "s_add_u32 s49, s33, 30720\n\t"
            "buffer_load_dwordx4 v[120:123], v245, s[28:31], s49 offen sc1\n\t"
            "s_add_u32 s49, s33, 1024\n\t"
            "buffer_load_dwordx4 v[4:7], v245, s[28:31], s49 offen sc1\n\t"
            "s_add_u32 s49, s33, 7168\n\t"
            "buffer_load_dwordx4 v[28:31], v245, s[28:31], s49 offen sc1\n\t"
            "s_add_u32 s49, s33, 13312\n\t"
            "buffer_load_dwordx4 v[52:55], v245, s[28:31], s49 offen sc1\n\t"
            "s_add_u32 s49, s33, 19456\n\t"
            "buffer_load_dwordx4 v[76:79], v245, s[28:31], s49 offen sc1\n\t"
            "s_add_u32 s49, s33, 25600\n\t"
            "buffer_load_dwordx4 v[100:103], v245, s[28:31], s49 offen sc1\n\t"
            "s_add_u32 s49, s33, 31744\n\t"
            "buffer_load_dwordx4 v[124:127], v245, s[28:31], s49 offen sc1\n\t"
            "s_add_u32 s49, s33, 2048\n\t"
            "buffer_load_dwordx4 v[8:11], v245, s[28:31], s49 offen sc1\n\t"
            "s_add_u32 s49, s33, 8192\n\t"
            "buffer_load_dwordx4 v[32:35], v245, s[28:31], s49 offen sc1\n\t"
            "s_add_u32 s49, s33, 14336\n\t"
            "buffer_load_dwordx4 v[56:59], v245, s[28:31], s49 offen sc1\n\t"
            "s_add_u32 s49, s33, 20480\n\t"
            "buffer_load_dwordx4 v[80:83], v245, s[28:31], s49 offen sc1\n\t"
            "s_add_u32 s49, s33, 26624\n\t"
            "buffer_load_dwordx4 v[104:107], v245, s[28:31], s49 offen sc1\n\t"
            "s_add_u32 s49, s33, 32768\n\t"
            "buffer_load_dwordx4 v[128:131], v245, s[28:31], s49 offen sc1\n\t"
            "s_add_u32 s49, s33, 3072\n\t"
            "buffer_load_dwordx4 v[12:15], v245, s[28:31], s49 offen sc1\n\t"
            "s_add_u32 s49, s33, 9216\n\t"
            "buffer_load_dwordx4 v[36:39], v245, s[28:31], s49 offen sc1\n\t"
            "s_add_u32 s49, s33, 15360\n\t"
            "buffer_load_dwordx4 v[60:63], v245, s[28:31], s49 offen sc1\n\t"
            "s_add_u32 s49, s33, 21504\n\t"
            "buffer_load_dwordx4 v[84:87], v245, s[28:31], s49 offen sc1\n\t"
            "s_add_u32 s49, s33, 27648\n\t"
            "buffer_load_dwordx4 v[108:111], v245, s[28:31], s49 offen sc1\n\t"
            "s_add_u32 s49, s33, 33792\n\t"
            "buffer_load_dwordx4 v[132:135], v245, s[28:31], s49 offen sc1\n\t"
            "s_add_u32 s49, s33, 4096\n\t"
            "buffer_load_dwordx4 v[16:19], v245, s[28:31], s49 offen sc1\n\t"
            "s_add_u32 s49, s33, 10240\n\t"
            "buffer_load_dwordx4 v[40:43], v245, s[28:31], s49 offen sc1\n\t"
            "s_add_u32 s49, s33, 16384\n\t"
            "buffer_load_dwordx4 v[64:67], v245, s[28:31], s49 offen sc1\n\t"
            "s_add_u32 s49, s33, 22528\n\t"
            "buffer_load_dwordx4 v[88:91], v245, s[28:31], s49 offen sc1\n\t"
            "s_add_u32 s49, s33, 28672\n\t"
            "buffer_load_dwordx4 v[112:115], v245, s[28:31], s49 offen sc1\n\t"
            "s_add_u32 s49, s33, 34816\n\t"
            "buffer_load_dwordx4 v[136:139], v245, s[28:31], s49 offen sc1\n\t"
            "s_add_u32 s49, s33, 5120\n\t"
            "buffer_load_dwordx4 v[20:23], v245, s[28:31], s49 offen sc1\n\t"
            "s_add_u32 s49, s33, 11264\n\t"
            "buffer_load_dwordx4 v[44:47], v245, s[28:31], s49 offen sc1\n\t"
            "s_add_u32 s49, s33, 17408\n\t"
            "buffer_load_dwordx4 v[68:71], v245, s[28:31], s49 offen sc1\n\t"
            "s_add_u32 s49, s33, 23552\n\t"
            "buffer_load_dwordx4 v[92:95], v245, s[28:31], s49 offen sc1\n\t"
            "s_add_u32 s49, s33, 29696\n\t"
            "buffer_load_dwordx4 v[116:119], v245, s[28:31], s49 offen sc1\n\t"
            "s_add_u32 s49, s33, 35840\n\t"
            "buffer_load_dwordx4 v[140:143], v245, s[28:31], s49 offen sc1\n\t"
            "ds_read_b32 v249, v252 offset:12\n\t"
            "s_waitcnt lgkmcnt(0)\n\t"
            "v_and_b32 v249, s69, v249\n\t"
            "s_mov_b32 exec_hi, 0x3fffff\n\t"
            "s_add_u32 m0, s66, 0\n\t"
            "s_add_u32 s49, s79, 0\n\t"
            "buffer_load_dwordx4 v249, s[72:75], s49 offen lds\n\t"
            "s_mov_b32 exec_hi, -1\n\t"
            "s_mov_b32 exec_hi, 0x3fffff\n\t"
            "s_add_u32 m0, s66, 872\n\t"
            "s_add_u32 s49, s47, 196224\n\t"
            "buffer_load_dwordx4 v249, s[72:75], s49 offen lds\n\t"
            "s_mov_b32 exec_hi, -1\n\t"
            "s_mov_b32 exec_hi, 0x3fffff\n\t"
            "s_add_u32 m0, s66, 1744\n\t"
            "s_add_u32 s49, s47, 392448\n\t"
            "buffer_load_dwordx4 v249, s[72:75], s49 offen lds\n\t"
            "s_mov_b32 exec_hi, -1\n\t"
            "s_mov_b32 exec_hi, 0x3fffff\n\t"
            "s_add_u32 m0, s66, 2616\n\t"
            "s_add_u32 s49, s47, 588672\n\t"
            "buffer_load_dwordx4 v249, s[72:75], s49 offen lds\n\t"
            "s_mov_b32 exec_hi, -1\n\t"
            "s_mov_b32 exec_hi, 0x3fffff\n\t"
            "s_add_u32 m0, s66, 3488\n\t"
            "s_add_u32 s49, s47, 784896\n\t"
            "buffer_load_dwordx4 v249, s[72:75], s49 offen lds\n\t"
            "s_mov_b32 exec_hi, -1\n\t"
            "s_mov_b32 exec_hi, 0x3fffff\n\t"
            "s_add_u32 m0, s66, 4360\n\t"
            "s_add_u32 s49, s47, 981120\n\t"
            "buffer_load_dwordx4 v249, s[72:75], s49 offen lds\n\t"
            "s_mov_b32 exec_hi, -1\n\t"
            "s_mov_b32 exec_hi, 0x3fffff\n\t"
            "s_add_u32 m0, s66, 5232\n\t"
            "s_add_u32 s49, s47, 1177344\n\t"
            "buffer_load_dwordx4 v249, s[72:75], s49 offen lds\n\t"
            "s_mov_b32 exec_hi, -1\n\t"
            "s_mov_b32 exec_hi, 0x3fffff\n\t"
            "s_add_u32 m0, s66, 6104\n\t"
            "s_add_u32 s49, s47, 1373568\n\t"
            "buffer_load_dwordx4 v249, s[72:75], s49 offen lds\n\t"
            "s_mov_b32 exec_hi, -1\n\t"
            "s_mov_b32 exec_hi, 0x3fffff\n\t"
            "s_add_u32 m0, s66, 6976\n\t"
            "s_add_u32 s49, s47, 1569792\n\t"
            "buffer_load_dwordx4 v249, s[72:75], s49 offen lds\n\t"
            "s_mov_b32 exec_hi, -1\n\t"
            "ds_read_b128 v[168:171], v254\n\t"
            "ds_read_b128 v[172:175], v254 offset:64\n\t"
            "v_add_u32 v180, s48, v245\n\t"
            "v_add_u32 v232, s48, v242\n\t"
            "v_xor_b32 v233, 64, v232\n\t"
            "v_xor_b32 v246, 0x100, v246\n\t"
            "ds_read_b128 v[176:179], v180 offset:18816\n\t"
            "ds_read_b128 v[180:183], v180 offset:19840\n\t"
            "ds_read_b128 v[192:195], v232\n\t"
            "ds_read_b128 v[196:199], v233\n\t"
            "ds_read_b128 v[224:227], v232 offset:128\n\t"
            "ds_read_b128 v[228:231], v233 offset:128\n\t"
            "s_waitcnt lgkmcnt(5)\n\t"
            "v_add_f32 v176, v176, v178\n\t"
            "s_waitcnt lgkmcnt(4)\n\t"
            "v_add_f32 v180, v180, v182\n\t"
            "v_add_f32 v176, v176, v180\n\t"
            "v_add_f32 v177, v177, v179\n\t"
            "v_add_f32 v181, v181, v183\n\t"
            "v_mul_f32 v176, 0x3baaaaab, v176\n\t"
            "v_add_f32 v177, v177, v181\n\t"
            "v_mul_f32_e64 v176, v176, -v176\n\t"
            "v_fmac_f32 v176, 0x3baaaaab, v177\n\t"
            "v_max_f32 v176, 0, v176\n\t"
            "v_add_f32 v176, 0x3727c5ac, v176\n\t"
            "v_rsq_f32 v176, v176\n\t"
            "s_nop 0\n\t"
            "ds_write_b32 v246, v176\n\t"
            "s_setprio 3\n\t"
            "s_waitcnt vmcnt(9)\n\t"
            "s_waitcnt lgkmcnt(3)\n\t"
            "v_mfma_f32_16x16x32_bf16 v[200:203], v[192:195], v[0:3], 0\n\t"
            "v_mfma_f32_16x16x32_bf16 v[200:203], v[196:199], v[4:7], v[200:203]\n\t"
            "v_mfma_f32_16x16x32_bf16 v[204:207], v[196:199], v[28:31], 0\n\t"
            "v_mfma_f32_16x16x32_bf16 v[204:207], v[192:195], v[24:27], v[204:207]\n\t"
            "v_mfma_f32_16x16x32_bf16 v[208:211], v[192:195], v[48:51], 0\n\t"
            "v_mfma_f32_16x16x32_bf16 v[208:211], v[196:199], v[52:55], v[208:211]\n\t"
            "v_mfma_f32_16x16x32_bf16 v[212:215], v[196:199], v[76:79], 0\n\t"
            "v_mfma_f32_16x16x32_bf16 v[212:215], v[192:195], v[72:75], v[212:215]\n\t"
            "v_mfma_f32_16x16x32_bf16 v[216:219], v[192:195], v[96:99], 0\n\t"
            "v_mfma_f32_16x16x32_bf16 v[216:219], v[196:199], v[100:103], v[216:219]\n\t"
            "v_mfma_f32_16x16x32_bf16 v[220:223], v[196:199], v[124:127], 0\n\t"
            "v_mfma_f32_16x16x32_bf16 v[220:223], v[192:195], v[120:123], v[220:223]\n\t"
            "s_waitcnt lgkmcnt(1)\n\t"
            "v_mfma_f32_16x16x32_bf16 v[200:203], v[224:227], v[8:11], v[200:203]\n\t"
            "v_mfma_f32_16x16x32_bf16 v[200:203], v[228:231], v[12:15], v[200:203]\n\t"
            "v_mfma_f32_16x16x32_bf16 v[204:207], v[228:231], v[36:39], v[204:207]\n\t"
            "v_mfma_f32_16x16x32_bf16 v[204:207], v[224:227], v[32:35], v[204:207]\n\t"
            "v_mfma_f32_16x16x32_bf16 v[208:211], v[224:227], v[56:59], v[208:211]\n\t"
            "v_mfma_f32_16x16x32_bf16 v[208:211], v[228:231], v[60:63], v[208:211]\n\t"
            "v_mfma_f32_16x16x32_bf16 v[212:215], v[228:231], v[84:87], v[212:215]\n\t"
            "v_mfma_f32_16x16x32_bf16 v[212:215], v[224:227], v[80:83], v[212:215]\n\t"
            "v_mfma_f32_16x16x32_bf16 v[216:219], v[224:227], v[104:107], v[216:219]\n\t"
            "s_waitcnt vmcnt(0)\n\t"
            "ds_read_b64 v[144:145], v253\n\t"
            "v_mfma_f32_16x16x32_bf16 v[216:219], v[228:231], v[108:111], v[216:219]\n\t"
            "ds_read_b64 v[146:147], v253 offset:96\n\t"
            "ds_read_b64 v[148:149], v253 offset:1744\n\t"
            "v_mfma_f32_16x16x32_bf16 v[220:223], v[228:231], v[132:135], v[220:223]\n\t"
            "ds_read_b128 v[228:231], v233 offset:256\n\t"
            "ds_read_b64 v[150:151], v253 offset:1840\n\t"
            "v_mfma_f32_16x16x32_bf16 v[220:223], v[224:227], v[128:131], v[220:223]\n\t"
            "ds_read_b128 v[224:227], v232 offset:256\n\t"
            "ds_read_b64 v[152:153], v253 offset:8\n\t"
            "ds_read_b64 v[154:155], v253 offset:104\n\t"
            "ds_read_b64 v[156:157], v253 offset:1752\n\t"
            "ds_read_b64 v[158:159], v253 offset:1848\n\t"
            "ds_read_b64 v[160:161], v253 offset:16\n\t"
            "ds_read_b64 v[162:163], v253 offset:112\n\t"
            "ds_read_b64 v[164:165], v253 offset:1760\n\t"
            "ds_read_b64 v[166:167], v253 offset:1856\n\t"
            "s_waitcnt lgkmcnt(0)\n\t"
            "s_nop 7\n\t"
            ".p2align 6\n\t"
            "s_nop 0\n\t"
            "s_nop 0\n\t"
            "s_nop 0\n\t"
            "s_nop 0\n\t"
            ".Lwc_loop%=:\n\t"
            "s_setprio 1\n\t"
            "s_setprio 3\n\t"
            "v_mfma_f32_16x16x32_bf16 v[200:203], v[224:227], v[16:19], v[200:203]\n\t"
            "v_mov_b64 v[176:177], 0\n\t"
            "v_mov_b64 v[178:179], 0\n\t"
            "v_mfma_f32_16x16x32_bf16 v[200:203], v[228:231], v[20:23], v[200:203]\n\t"
            "v_mov_b64 v[180:181], 0\n\t"
            "v_mov_b64 v[182:183], 0\n\t"
            "s_waitcnt lgkmcnt(0)\n\t"
            "v_smfmac_f32_16x16x64_bf16 v[176:179], v[168:171], v[144:151], v255\n\t"
            "ds_read_b128 v[168:171], v254 offset:128\n\t"
            "v_smfmac_f32_16x16x64_bf16 v[180:183], v[172:175], v[152:159], v255\n\t"
            "ds_read_b64 v[144:145], v253 offset:24\n\t"
            "v_mfma_f32_16x16x32_bf16 v[204:207], v[228:231], v[44:47], v[204:207]\n\t"
            "ds_read_b64 v[146:147], v253 offset:120\n\t"
            "ds_read_b64 v[148:149], v253 offset:1768\n\t"
            "v_mfma_f32_16x16x32_bf16 v[204:207], v[224:227], v[40:43], v[204:207]\n\t"
            "ds_read_b64 v[150:151], v253 offset:1864\n\t"
            "ds_read_b128 v[172:175], v254 offset:192\n\t"
            "v_mfma_f32_16x16x32_bf16 v[208:211], v[224:227], v[64:67], v[208:211]\n\t"
            "ds_read_b64 v[152:153], v253 offset:32\n\t"
            "ds_read_b64 v[154:155], v253 offset:128\n\t"
            "v_mfma_f32_16x16x32_bf16 v[208:211], v[228:231], v[68:71], v[208:211]\n\t"
            "ds_read_b64 v[156:157], v253 offset:1776\n\t"
            "ds_read_b64 v[158:159], v253 offset:1872\n\t"
            "v_mfma_f32_16x16x32_bf16 v[212:215], v[228:231], v[92:95], v[212:215]\n\t"
            "v_mov_b32 v249, s76\n\t"
            "v_mov_b64 v[184:185], 0\n\t"
            "v_mfma_f32_16x16x32_bf16 v[212:215], v[224:227], v[88:91], v[212:215]\n\t"
            "ds_read_b32 v249, v249\n\t"
            "v_mov_b64 v[186:187], 0\n\t"
            "v_mfma_f32_16x16x32_bf16 v[216:219], v[224:227], v[112:115], v[216:219]\n\t"
            "v_mov_b64 v[188:189], 0\n\t"
            "v_mov_b64 v[190:191], 0\n\t"
            "v_mfma_f32_16x16x32_bf16 v[216:219], v[228:231], v[116:119], v[216:219]\n\t"
            "v_cvt_pk_bf16_f32 v176, v176, v177\n\t"
            "v_add_u32 v236, s67, v250\n\t"
            "s_waitcnt lgkmcnt(10)\n\t"
            "v_smfmac_f32_16x16x64_bf16 v[184:187], v[168:171], v[160:167], v255\n\t"
            "ds_read_b128 v[168:171], v254 offset:256\n\t"
            "s_waitcnt lgkmcnt(6)\n\t"
            "v_smfmac_f32_16x16x64_bf16 v[188:191], v[172:175], v[144:151], v255\n\t"
            "ds_read_b64 v[160:161], v253 offset:40\n\t"
            "v_mfma_f32_16x16x32_bf16 v[220:223], v[228:231], v[140:143], v[220:223]\n\t"
            "ds_read_b64 v[162:163], v253 offset:136\n\t"
            "ds_read_b64 v[164:165], v253 offset:1784\n\t"
            "v_mfma_f32_16x16x32_bf16 v[220:223], v[224:227], v[136:139], v[220:223]\n\t"
            "s_setprio 1\n\t"
            "ds_read_b64 v[166:167], v253 offset:1880\n\t"
            "ds_read_b128 v[172:175], v254 offset:320\n\t"
            "ds_read_b64 v[144:145], v253 offset:48\n\t"
            "ds_read_b64 v[146:147], v253 offset:144\n\t"
            "ds_read_b64 v[148:149], v253 offset:1792\n\t"
            "ds_read_b64 v[150:151], v253 offset:1888\n\t"
            "s_waitcnt lgkmcnt(14)\n\t"
            "ds_read_b128 v[224:227], v232 offset:6272\n\t"
            "s_waitcnt lgkmcnt(14)\n\t"
            "ds_read_b128 v[228:231], v233 offset:6272\n\t"
            "s_waitcnt lgkmcnt(12)\n\t"
            "v_readfirstlane_b32 s26, v249\n\t"
            "v_cvt_pk_bf16_f32 v177, v178, v179\n\t"
            "v_cvt_pk_bf16_f32 v178, v180, v181\n\t"
            "s_and_b32 s68, s26, 0x7ffffff\n\t"
            "s_add_u32 s68, s68, s78\n\t"
            "s_and_b32 s69, s26, 0x38000000\n\t"
            "s_or_b32 s69, s69, s39\n\t"
            "s_bitcmp1_b32 s26, 27\n\t"
            "s_cselect_b32 s79, 2.0, s68\n\t"
            "s_setprio 3\n\t"
            "v_mfma_f32_4x4x4_16b_bf16 v[196:199], v[176:177], v[176:177], 0\n\t"
            "s_setprio 1\n\t"
            "ds_read_b32 v249, v252 offset:12\n\t"
            "s_and_b32 s57, s56, 3\n\t"
            "s_lshl_b32 s58, s57, 3\n\t"
            "s_lshr_b32 m0, s54, s58\n\t"
            "s_lshl_b32 s58, s57, 4\n\t"
            "s_lshr_b64 s[36:37], s[50:51], s58\n\t"
            "s_and_b32 s57, s36, 0xffff\n\t"
            "s_add_u32 s57, s57, s67\n\t"
            "v_writelane_b32 v236, s57, 63\n\t"
            "v_writelane_b32 v236, s59, m0\n\t"
            "v_cvt_pk_bf16_f32 v179, v182, v183\n\t"
            "v_mov_b64 v[180:181], 0\n\t"
            "v_mov_b64 v[182:183], 0\n\t"
            "s_setprio 3\n\t"
            "v_mfma_f32_16x16x32_bf16 v[192:195], v[238:241], v[176:179], 0\n\t"
            "s_lshr_b64 s[36:37], s[52:53], s58\n\t"
            "s_and_b32 s58, s36, 0xffff\n\t"
            "s_add_u32 s58, s58, s67\n\t"
            "v_mfma_f32_4x4x4_16b_bf16 v[196:199], v[178:179], v[178:179], v[196:199]\n\t"
            "ds_write_b128 v236, v[176:179]\n\t"
            "s_waitcnt lgkmcnt(8)\n\t"
            "v_smfmac_f32_16x16x64_bf16 v[180:183], v[172:175], v[160:167], v255\n\t"
            "s_setprio 1\n\t"
            "s_and_b32 s38, s52, 0xffff\n\t"
            "s_add_u32 s38, s38, s67\n\t"
            "ds_read_b128 v[172:175], v254 offset:448\n\t"
            "ds_read_b64 v[160:161], v253 offset:64\n\t"
            "ds_read_b64 v[162:163], v253 offset:160\n\t"
            "ds_read_b64 v[164:165], v253 offset:1808\n\t"
            "ds_read_b64 v[166:167], v253 offset:1904\n\t"
            "v_mov_b64 v[176:177], 0\n\t"
            "v_mov_b64 v[178:179], 0\n\t"
            "v_cvt_pk_bf16_f32 v184, v184, v185\n\t"
            "v_cvt_pk_bf16_f32 v185, v186, v187\n\t"
            "s_setprio 3\n\t"
            "v_smfmac_f32_16x16x64_bf16 v[176:179], v[168:171], v[152:159], v255\n\t"
            "ds_read_b128 v[168:171], v254 offset:384\n\t"
            "v_mfma_f32_4x4x4_16b_bf16 v[196:199], v[184:185], v[184:185], v[196:199]\n\t"
            "s_setprio 1\n\t"
            "ds_read_b64 v[152:153], v253 offset:56\n\t"
            "s_waitcnt lgkmcnt(14)\n\t"
            "ds_read_b64 v[154:155], v253 offset:152\n\t"
            "s_waitcnt lgkmcnt(14)\n\t"
            "ds_read_b64 v[156:157], v253 offset:1800\n\t"
            "s_waitcnt lgkmcnt(14)\n\t"
            "ds_read_b64 v[158:159], v253 offset:1896\n\t"
            "v_xor_b32 v237, 16, v236\n\t"
            "v_cvt_pk_bf16_f32 v186, v188, v189\n\t"
            "v_cvt_pk_bf16_f32 v187, v190, v191\n\t"
            "v_mov_b64 v[188:189], 0\n\t"
            "v_mov_b64 v[190:191], 0\n\t"
            "s_setprio 3\n\t"
            "v_mfma_f32_16x16x32_bf16 v[192:195], v[238:241], v[184:187], v[192:195]\n\t"
            "s_waitcnt lgkmcnt(14)\n\t"
            "ds_write_b128 v237, v[184:187]\n\t"
            "v_mfma_f32_4x4x4_16b_bf16 v[196:199], v[186:187], v[186:187], v[196:199]\n\t"
            "s_setprio 1\n\t"
            "v_mov_b64 v[184:185], 0\n\t"
            "v_mov_b64 v[186:187], 0\n\t"
            "v_cvt_pk_bf16_f32 v176, v176, v177\n\t"
            "v_cvt_pk_bf16_f32 v177, v178, v179\n\t"
            "v_cvt_pk_bf16_f32 v178, v180, v181\n\t"
            "v_cvt_pk_bf16_f32 v179, v182, v183\n\t"
            "s_setprio 3\n\t"
            "v_mfma_f32_4x4x4_16b_bf16 v[196:199], v[176:177], v[176:177], v[196:199]\n\t"
            "s_waitcnt lgkmcnt(14)\n\t"
            "ds_write_b128 v236, v[176:179] offset:128\n\t"
            "s_waitcnt lgkmcnt(6)\n\t"
            "v_smfmac_f32_16x16x64_bf16 v[184:187], v[168:171], v[144:151], v255\n\t"
            "ds_read_b128 v[168:171], v254 offset:512\n\t"
            "s_waitcnt lgkmcnt(3)\n\t"
            "v_smfmac_f32_16x16x64_bf16 v[188:191], v[172:175], v[152:159], v255\n\t"
            "ds_read_b64 v[144:145], v253 offset:72\n\t"
            "v_mfma_f32_16x16x32_bf16 v[192:195], v[238:241], v[176:179], v[192:195]\n\t"
            "ds_read_b64 v[146:147], v253 offset:168\n\t"
            "ds_read_b64 v[148:149], v253 offset:1816\n\t"
            "v_mfma_f32_4x4x4_16b_bf16 v[196:199], v[178:179], v[178:179], v[196:199]\n\t"
            "s_setprio 1\n\t"
            "ds_read_b64 v[150:151], v253 offset:1912\n\t"
            "ds_read_b128 v[172:175], v254 offset:576\n\t"
            "ds_read_b64 v[152:153], v253 offset:80\n\t"
            "ds_read_b64 v[154:155], v253 offset:176\n\t"
            "ds_read_b64 v[156:157], v253 offset:1824\n\t"
            "ds_read_b64 v[158:159], v253 offset:1920\n\t"
            "v_mov_b64 v[176:177], 0\n\t"
            "v_mov_b64 v[178:179], 0\n\t"
            "v_mov_b64 v[180:181], 0\n\t"
            "v_mov_b64 v[182:183], 0\n\t"
            "v_cvt_pk_bf16_f32 v184, v184, v185\n\t"
            "v_cvt_pk_bf16_f32 v185, v186, v187\n\t"
            "v_cvt_pk_bf16_f32 v186, v188, v189\n\t"
            "v_cvt_pk_bf16_f32 v187, v190, v191\n\t"
            "s_setprio 3\n\t"
            "v_mfma_f32_4x4x4_16b_bf16 v[196:199], v[184:185], v[184:185], v[196:199]\n\t"
            "ds_write_b128 v237, v[184:187] offset:128\n\t"
            "s_waitcnt lgkmcnt(10)\n\t"
            "v_smfmac_f32_16x16x64_bf16 v[176:179], v[168:171], v[160:167], v255\n\t"
            "ds_read_b128 v[168:171], v254 offset:640\n\t"
            "v_mfma_f32_16x16x32_bf16 v[192:195], v[238:241], v[184:187], v[192:195]\n\t"
            "ds_read_b64 v[160:161], v253 offset:88\n\t"
            "ds_read_b64 v[162:163], v253 offset:184\n\t"
            "s_waitcnt lgkmcnt(8)\n\t"
            "v_smfmac_f32_16x16x64_bf16 v[180:183], v[172:175], v[144:151], v255\n\t"
            "ds_read_b64 v[164:165], v253 offset:1832\n\t"
            "v_mfma_f32_4x4x4_16b_bf16 v[196:199], v[186:187], v[186:187], v[196:199]\n\t"
            "s_setprio 1\n\t"
            "ds_read_b64 v[166:167], v253 offset:1928\n\t"
            "ds_read_b128 v[172:175], v254 offset:704\n\t"
            "v_mov_b64 v[184:185], 0\n\t"
            "v_mov_b64 v[186:187], 0\n\t"
            "v_mov_b64 v[188:189], 0\n\t"
            "v_mov_b64 v[190:191], 0\n\t"
            "v_add_u32 v248, s67, v251\n\t"
            "v_writelane_b32 v248, s58, 63\n\t"
            "v_writelane_b32 v248, s38, m0\n\t"
            "v_and_b32 v249, s69, v249\n\t"
            "v_cvt_pk_bf16_f32 v176, v176, v177\n\t"
            "v_cvt_pk_bf16_f32 v177, v178, v179\n\t"
            "v_cvt_pk_bf16_f32 v178, v180, v181\n\t"
            "v_cvt_pk_bf16_f32 v179, v182, v183\n\t"
            "s_setprio 3\n\t"
            "v_mfma_f32_4x4x4_16b_bf16 v[196:199], v[176:177], v[176:177], v[196:199]\n\t"
            "ds_write_b128 v236, v[176:179] offset:256\n\t"
            "s_waitcnt lgkmcnt(6)\n\t"
            "v_smfmac_f32_16x16x64_bf16 v[184:187], v[168:171], v[152:159], v255\n\t"
            "v_mov_b32 v234, 0\n\t"
            "v_mfma_f32_4x4x4_16b_bf16 v[196:199], v[178:179], v[178:179], v[196:199]\n\t"
            "v_xor_b32 v246, 0x100, v246\n\t"
            "v_mfma_f32_16x16x32_bf16 v[192:195], v[238:241], v[176:179], v[192:195]\n\t"
            "s_waitcnt lgkmcnt(1)\n\t"
            "v_smfmac_f32_16x16x64_bf16 v[188:191], v[172:175], v[160:167], v255\n\t"
            "s_setprio 1\n\t"
            "s_mov_b32 exec_hi, 0x3fffff\n\t"
            "s_add_u32 m0, s66, 0\n\t"
            "s_add_u32 s49, s79, 0\n\t"
            "buffer_load_dwordx4 v249, s[72:75], s49 offen lds\n\t"
            "s_mov_b32 exec_hi, -1\n\t"
            "s_mov_b32 exec_hi, 0x3fffff\n\t"
            "s_add_u32 m0, s66, 872\n\t"
            "s_add_u32 s49, s68, 196224\n\t"
            "buffer_load_dwordx4 v249, s[72:75], s49 offen lds\n\t"
            "s_mov_b32 exec_hi, -1\n\t"
            "s_mov_b32 exec_hi, 0x3fffff\n\t"
            "s_add_u32 m0, s66, 1744\n\t"
            "s_add_u32 s49, s68, 392448\n\t"
            "buffer_load_dwordx4 v249, s[72:75], s49 offen lds\n\t"
            "s_mov_b32 exec_hi, -1\n\t"
            "s_mov_b32 exec_hi, 0x3fffff\n\t"
            "s_add_u32 m0, s66, 2616\n\t"
            "s_add_u32 s49, s68, 588672\n\t"
            "buffer_load_dwordx4 v249, s[72:75], s49 offen lds\n\t"
            "s_mov_b32 exec_hi, -1\n\t"
            "s_mov_b32 exec_hi, 0x3fffff\n\t"
            "s_add_u32 m0, s66, 3488\n\t"
            "s_add_u32 s49, s68, 784896\n\t"
            "buffer_load_dwordx4 v249, s[72:75], s49 offen lds\n\t"
            "s_mov_b32 exec_hi, -1\n\t"
            "v_cvt_pk_bf16_f32 v184, v184, v185\n\t"
            "v_cvt_pk_bf16_f32 v185, v186, v187\n\t"
            "v_cvt_pk_bf16_f32 v186, v188, v189\n\t"
            "v_cvt_pk_bf16_f32 v187, v190, v191\n\t"
            "s_setprio 3\n\t"
            "v_mfma_f32_4x4x4_16b_bf16 v[196:199], v[184:185], v[184:185], v[196:199]\n\t"
            "s_mov_b32 exec_hi, 0x3fffff\n\t"
            "s_add_u32 m0, s66, 4360\n\t"
            "s_add_u32 s49, s68, 981120\n\t"
            "buffer_load_dwordx4 v249, s[72:75], s49 offen lds\n\t"
            "s_mov_b32 exec_hi, -1\n\t"
            "v_mfma_f32_4x4x4_16b_bf16 v[196:199], v[186:187], v[186:187], v[196:199]\n\t"
            "s_mov_b32 exec_hi, 0x3fffff\n\t"
            "s_add_u32 m0, s66, 5232\n\t"
            "s_add_u32 s49, s68, 1177344\n\t"
            "buffer_load_dwordx4 v249, s[72:75], s49 offen lds\n\t"
            "s_mov_b32 exec_hi, -1\n\t"
            "v_mfma_f32_16x16x32_bf16 v[192:195], v[238:241], v[184:187], v[192:195]\n\t"
            "s_setprio 1\n\t"
            "s_mov_b32 exec_hi, 0x3fffff\n\t"
            "s_add_u32 m0, s66, 6104\n\t"
            "s_add_u32 s49, s68, 1373568\n\t"
            "buffer_load_dwordx4 v249, s[72:75], s49 offen lds\n\t"
            "s_mov_b32 exec_hi, -1\n\t"
            "s_mov_b32 exec_hi, 0x3fffff\n\t"
            "s_add_u32 m0, s66, 6976\n\t"
            "s_add_u32 s49, s68, 1569792\n\t"
            "buffer_load_dwordx4 v249, s[72:75], s49 offen lds\n\t"
            "s_mov_b32 exec_hi, -1\n\t"
            "v_cndmask_b32_e64 v193, v196, v197, s[60:61]\n\t"
            "v_cndmask_b32_e64 v193, v193, v198, s[62:63]\n\t"
            "v_cndmask_b32_e64 v193, v193, v199, s[64:65]\n\t"
            "s_setprio 1\n\t"
            "ds_read_b128 v[196:199], v233 offset:6144\n\t"
            "ds_write_b64 v248, v[192:193] offset:18816\n\t"
            "ds_read_b128 v[192:195], v232 offset:6144\n\t"
            "ds_read_b64 v[248:249], v247 offset:8\n\t"
            "ds_write_b128 v237, v[184:187] offset:256\n\t"
            "ds_read_b64 v[236:237], v247\n\t"
            "s_mov_b64 exec, 1\n\t"
            "ds_inc_u32 v255, v238 offset:7860\n\t"
            "s_mov_b64 exec, -1\n\t"
            "s_waitcnt lgkmcnt(4)\n\t"
            "v_mfma_f32_16x16x32_bf16 v[144:147], v[192:195], v[0:3], 0\n\t"
            "s_waitcnt lgkmcnt(3)\n\t"
            "v_mul_f32 v202, v248, v202\n\t"
            "v_mul_f32 v203, v249, v203\n\t"
            "v_mfma_f32_16x16x32_bf16 v[144:147], v[196:199], v[4:7], v[144:147]\n\t"
            "v_mul_f32 v206, v248, v206\n\t"
            "s_waitcnt lgkmcnt(1)\n\t"
            "v_mul_f32 v200, v236, v200\n\t"
            "v_mfma_f32_16x16x32_bf16 v[148:151], v[196:199], v[28:31], 0\n\t"
            "v_mul_f32 v201, v237, v201\n\t"
            "v_mul_f32 v204, v236, v204\n\t"
            "v_mfma_f32_16x16x32_bf16 v[148:151], v[192:195], v[24:27], v[148:151]\n\t"
            "v_mul_f32 v205, v237, v205\n\t"
            "v_mul_f32 v208, v236, v208\n\t"
            "v_mfma_f32_16x16x32_bf16 v[168:171], v[192:195], v[48:51], 0\n\t"
            "v_mul_f32 v209, v237, v209\n\t"
            "v_mul_f32 v212, v236, v212\n\t"
            "v_mfma_f32_16x16x32_bf16 v[168:171], v[196:199], v[52:55], v[168:171]\n\t"
            "v_mul_f32 v213, v237, v213\n\t"
            "v_mul_f32 v216, v236, v216\n\t"
            "v_mfma_f32_16x16x32_bf16 v[172:175], v[196:199], v[76:79], 0\n\t"
            "v_mul_f32 v217, v237, v217\n\t"
            "v_mul_f32 v220, v236, v220\n\t"
            "v_mfma_f32_16x16x32_bf16 v[172:175], v[192:195], v[72:75], v[172:175]\n\t"
            "v_mul_f32 v221, v237, v221\n\t"
            "v_mul_f32 v207, v249, v207\n\t"
            "v_mfma_f32_16x16x32_bf16 v[152:155], v[192:195], v[96:99], 0\n\t"
            "ds_read_b64 v[236:237], v247 offset:64\n\t"
            "v_mul_f32 v210, v248, v210\n\t"
            "v_mfma_f32_16x16x32_bf16 v[152:155], v[196:199], v[100:103], v[152:155]\n\t"
            "v_mul_f32 v211, v249, v211\n\t"
            "v_mul_f32 v214, v248, v214\n\t"
            "v_mfma_f32_16x16x32_bf16 v[156:159], v[196:199], v[124:127], 0\n\t"
            "ds_read_b128 v[196:199], v233 offset:6400\n\t"
            "v_mul_f32 v215, v249, v215\n\t"
            "v_mfma_f32_16x16x32_bf16 v[156:159], v[192:195], v[120:123], v[156:159]\n\t"
            "ds_read_b128 v[192:195], v232 offset:6400\n\t"
            "v_mul_f32 v218, v248, v218\n\t"
            "v_mfma_f32_16x16x32_bf16 v[144:147], v[224:227], v[8:11], v[144:147]\n\t"
            "v_mul_f32 v219, v249, v219\n\t"
            "v_mul_f32 v222, v248, v222\n\t"
            "v_mfma_f32_16x16x32_bf16 v[144:147], v[228:231], v[12:15], v[144:147]\n\t"
            "v_mul_f32 v223, v249, v223\n\t"
            "v_cvt_pk_bf16_f32 v212, v212, v213\n\t"
            "v_mfma_f32_16x16x32_bf16 v[148:151], v[228:231], v[36:39], v[148:151]\n\t"
            "ds_read_b64 v[248:249], v247 offset:72\n\t"
            "v_cvt_pk_bf16_f32 v208, v208, v209\n\t"
            "v_mfma_f32_16x16x32_bf16 v[148:151], v[224:227], v[32:35], v[148:151]\n\t"
            "v_cvt_pk_bf16_f32 v213, v214, v215\n\t"
            "v_cvt_pk_bf16_f32 v209, v210, v211\n\t"
            "v_mfma_f32_16x16x32_bf16 v[168:171], v[224:227], v[56:59], v[168:171]\n\t"
            "v_cvt_pk_bf16_f32 v200, v200, v201\n\t"
            "v_cvt_pk_bf16_f32 v204, v204, v205\n\t"
            "v_mfma_f32_16x16x32_bf16 v[168:171], v[228:231], v[60:63], v[168:171]\n\t"
            "v_cvt_pk_bf16_f32 v216, v216, v217\n\t"
            "v_cvt_pk_bf16_f32 v220, v220, v221\n\t"
            "v_mfma_f32_16x16x32_bf16 v[172:175], v[228:231], v[84:87], v[172:175]\n\t"
            "v_cvt_pk_bf16_f32 v201, v202, v203\n\t"
            "v_cvt_pk_bf16_f32 v205, v206, v207\n\t"
            "v_mfma_f32_16x16x32_bf16 v[172:175], v[224:227], v[80:83], v[172:175]\n\t"
            "v_cvt_pk_bf16_f32 v217, v218, v219\n\t"
            "v_cvt_pk_bf16_f32 v221, v222, v223\n\t"
            "v_mfma_f32_16x16x32_bf16 v[152:155], v[224:227], v[104:107], v[152:155]\n\t"
            "v_mfma_f32_16x16x32_bf16 v[152:155], v[228:231], v[108:111], v[152:155]\n\t"
            "v_mfma_f32_16x16x32_bf16 v[156:159], v[228:231], v[132:135], v[156:159]\n\t"
            "ds_read_b128 v[228:231], v233 offset:12288\n\t"
            "v_mfma_f32_16x16x32_bf16 v[156:159], v[224:227], v[128:131], v[156:159]\n\t"
            "ds_read_b128 v[224:227], v232 offset:12288\n\t"
            "s_waitcnt lgkmcnt(3)\n\t"
            "v_mfma_f32_16x16x32_bf16 v[144:147], v[192:195], v[16:19], v[144:147]\n\t"
            "v_mfma_f32_16x16x32_bf16 v[144:147], v[196:199], v[20:23], v[144:147]\n\t"
            "v_mfma_f32_16x16x32_bf16 v[148:151], v[196:199], v[44:47], v[148:151]\n\t"
            "v_mfma_f32_16x16x32_bf16 v[148:151], v[192:195], v[40:43], v[148:151]\n\t"
            "v_mfma_f32_16x16x32_bf16 v[168:171], v[192:195], v[64:67], v[168:171]\n\t"
            "v_mfma_f32_16x16x32_bf16 v[168:171], v[196:199], v[68:71], v[168:171]\n\t"
            "ds_read_b64 v[234:235], v234 offset:7856\n\t"
            "v_mfma_f32_16x16x32_bf16 v[172:175], v[196:199], v[92:95], v[172:175]\n\t"
            "s_nop 1\n\t"
            "v_mul_f32 v144, v236, v144\n\t"
            "v_mfma_f32_16x16x32_bf16 v[172:175], v[192:195], v[88:91], v[172:175]\n\t"
            "v_mul_f32 v145, v237, v145\n\t"
            "s_waitcnt lgkmcnt(3)\n\t"
            "v_mul_f32 v146, v248, v146\n\t"
            "v_mfma_f32_16x16x32_bf16 v[152:155], v[192:195], v[112:115], v[152:155]\n\t"
            "v_mul_f32 v168, v236, v168\n\t"
            "v_mul_f32 v169, v237, v169\n\t"
            "v_mfma_f32_16x16x32_bf16 v[152:155], v[196:199], v[116:119], v[152:155]\n\t"
            "v_mul_f32 v170, v248, v170\n\t"
            "v_mul_f32 v171, v249, v171\n\t"
            "v_mfma_f32_16x16x32_bf16 v[156:159], v[196:199], v[140:143], v[156:159]\n\t"
            "ds_read_b128 v[196:199], v233 offset:12416\n\t"
            "v_mul_f32 v172, v236, v172\n\t"
            "v_mfma_f32_16x16x32_bf16 v[156:159], v[192:195], v[136:139], v[156:159]\n\t"
            "ds_read_b128 v[192:195], v232 offset:12416\n\t"
            "v_mul_f32 v173, v237, v173\n\t"
            "s_waitcnt lgkmcnt(3)\n\t"
            "v_mfma_f32_16x16x32_bf16 v[160:163], v[224:227], v[0:3], 0\n\t"
            "v_cvt_pk_bf16_f32 v214, v172, v173\n\t"
            "v_cvt_pk_bf16_f32 v210, v168, v169\n\t"
            "v_mfma_f32_16x16x32_bf16 v[160:163], v[228:231], v[4:7], v[160:163]\n\t"
            "s_waitcnt lgkmcnt(2)\n\t"
            "v_readfirstlane_b32 s77, v234\n\t"
            "v_readfirstlane_b32 s38, v235\n\t"
            "s_add_u32 s77, s77, s38\n\t"
            "s_cmp_ge_u32 s77, s44\n\t"
            "s_cbranch_scc0 .Lwc_spin%=\n\t"
            ".Lwc_go%=:\n\t"
            "v_mfma_f32_16x16x32_bf16 v[164:167], v[228:231], v[28:31], 0\n\t"
            "s_add_u32 s59, s59, s84\n\t"
            "s_movk_i32 s84, 800\n\t"
            "v_cvt_pk_bf16_f32 v211, v170, v171\n\t"
            "v_mfma_f32_16x16x32_bf16 v[164:167], v[224:227], v[24:27], v[164:167]\n\t"
            "s_add_u32 s45, s46, s27\n\t"
            "s_mov_b32 s46, s47\n\t"
            "s_mov_b32 s47, s68\n\t"
            "v_mfma_f32_16x16x32_bf16 v[176:179], v[224:227], v[48:51], 0\n\t"
            "v_mul_f32 v174, v248, v174\n\t"
            "v_mul_f32 v175, v249, v175\n\t"
            "v_mfma_f32_16x16x32_bf16 v[176:179], v[228:231], v[52:55], v[176:179]\n\t"
            "v_add_u32 v172, s67, v245\n\t"
            "v_cvt_pk_bf16_f32 v215, v174, v175\n\t"
            "v_mfma_f32_16x16x32_bf16 v[180:183], v[228:231], v[76:79], 0\n\t"
            "ds_read_b128 v[168:171], v172 offset:18816\n\t"
            "ds_read_b128 v[172:175], v172 offset:19840\n\t"
            "v_mfma_f32_16x16x32_bf16 v[180:183], v[224:227], v[72:75], v[180:183]\n\t"
            "v_mul_f32 v148, v236, v148\n\t"
            "v_mul_f32 v149, v237, v149\n\t"
            "v_mfma_f32_16x16x32_bf16 v[184:187], v[224:227], v[96:99], 0\n\t"
            "v_mul_f32 v152, v236, v152\n\t"
            "v_mul_f32 v153, v237, v153\n\t"
            "v_mfma_f32_16x16x32_bf16 v[184:187], v[228:231], v[100:103], v[184:187]\n\t"
            "v_mul_f32 v156, v236, v156\n\t"
            "v_mul_f32 v157, v237, v157\n\t"
            "v_mfma_f32_16x16x32_bf16 v[188:191], v[228:231], v[124:127], 0\n\t"
            "ds_read_b128 v[228:231], v233 offset:12544\n\t"
            "ds_read_b64 v[236:237], v247 offset:128\n\t"
            "v_mfma_f32_16x16x32_bf16 v[188:191], v[224:227], v[120:123], v[188:191]\n\t"
            "ds_read_b128 v[224:227], v232 offset:12544\n\t"
            "v_mul_f32 v147, v249, v147\n\t"
            "s_waitcnt lgkmcnt(5)\n\t"
            "v_mfma_f32_16x16x32_bf16 v[160:163], v[192:195], v[8:11], v[160:163]\n\t"
            "v_mul_f32 v150, v248, v150\n\t"
            "v_mul_f32 v151, v249, v151\n\t"
            "v_mfma_f32_16x16x32_bf16 v[160:163], v[196:199], v[12:15], v[160:163]\n\t"
            "v_mul_f32 v154, v248, v154\n\t"
            "v_mul_f32 v155, v249, v155\n\t"
            "v_mfma_f32_16x16x32_bf16 v[164:167], v[196:199], v[36:39], v[164:167]\n\t"
            "v_mul_f32 v158, v248, v158\n\t"
            "v_mul_f32 v159, v249, v159\n\t"
            "v_mfma_f32_16x16x32_bf16 v[164:167], v[192:195], v[32:35], v[164:167]\n\t"
            "ds_read_b64 v[248:249], v247 offset:136\n\t"
            "s_waitcnt lgkmcnt(5)\n\t"
            "v_add_f32 v168, v168, v170\n\t"
            "v_mfma_f32_16x16x32_bf16 v[176:179], v[192:195], v[56:59], v[176:179]\n\t"
            "s_waitcnt lgkmcnt(4)\n\t"
            "v_add_f32 v172, v172, v174\n\t"
            "v_add_f32 v168, v168, v172\n\t"
            "v_mfma_f32_16x16x32_bf16 v[176:179], v[196:199], v[60:63], v[176:179]\n\t"
            "v_add_f32 v169, v169, v171\n\t"
            "v_add_f32 v173, v173, v175\n\t"
            "v_mfma_f32_16x16x32_bf16 v[180:183], v[196:199], v[84:87], v[180:183]\n\t"
            "v_mul_f32 v168, 0x3baaaaab, v168\n\t"
            "v_add_f32 v169, v169, v173\n\t"
            "v_mfma_f32_16x16x32_bf16 v[180:183], v[192:195], v[80:83], v[180:183]\n\t"
            "ds_read_b128 v[172:175], v254 offset:64\n\t"
            "v_mul_f32_e64 v168, v168, -v168\n\t"
            "v_mfma_f32_16x16x32_bf16 v[184:187], v[192:195], v[104:107], v[184:187]\n\t"
            "v_fmac_f32 v168, 0x3baaaaab, v169\n\t"
            "v_max_f32 v168, 0, v168\n\t"
            "v_mfma_f32_16x16x32_bf16 v[184:187], v[196:199], v[108:111], v[184:187]\n\t"
            "v_cvt_pk_bf16_f32 v202, v144, v145\n\t"
            "v_cvt_pk_bf16_f32 v203, v146, v147\n\t"
            "v_mfma_f32_16x16x32_bf16 v[188:191], v[196:199], v[132:135], v[188:191]\n\t"
            "v_cvt_pk_bf16_f32 v206, v148, v149\n\t"
            "v_cvt_pk_bf16_f32 v207, v150, v151\n\t"
            "v_mfma_f32_16x16x32_bf16 v[188:191], v[192:195], v[128:131], v[188:191]\n\t"
            "v_cvt_pk_bf16_f32 v218, v152, v153\n\t"
            "v_cvt_pk_bf16_f32 v219, v154, v155\n\t"
            "s_waitcnt lgkmcnt(2)\n\t"
            "v_mfma_f32_16x16x32_bf16 v[160:163], v[224:227], v[16:19], v[160:163]\n\t"
            "v_cvt_pk_bf16_f32 v222, v156, v157\n\t"
            "v_cvt_pk_bf16_f32 v223, v158, v159\n\t"
            "v_mfma_f32_16x16x32_bf16 v[160:163], v[228:231], v[20:23], v[160:163]\n\t"
            "v_add_f32 v168, 0x3727c5ac, v168\n\t"
            "v_rsq_f32 v168, v168\n\t"
            "v_mfma_f32_16x16x32_bf16 v[164:167], v[228:231], v[44:47], v[164:167]\n\t"
            "v_add_u32 v232, s67, v242\n\t"
            "v_xor_b32 v233, 64, v232\n\t"
            "v_mfma_f32_16x16x32_bf16 v[164:167], v[224:227], v[40:43], v[164:167]\n\t"
            "s_xor_b32 s48, s48, s71\n\t"
            "s_xor_b32 s67, s67, s71\n\t"
            "s_add_u32 s76, s76, 4\n\t"
            "s_add_u32 s44, s44, 8\n\t"
            "v_mfma_f32_16x16x32_bf16 v[176:179], v[224:227], v[64:67], v[176:179]\n\t"
            "s_lshl_b32 s56, s56, 2\n\t"
            "s_lshr_b32 s49, s26, 30\n\t"
            "s_or_b32 s56, s56, s49\n\t"
            "v_mfma_f32_16x16x32_bf16 v[176:179], v[228:231], v[68:71], v[176:179]\n\t"
            "ds_read_b128 v[192:195], v232\n\t"
            "ds_read_b128 v[196:199], v233\n\t"
            "v_mfma_f32_16x16x32_bf16 v[180:183], v[228:231], v[92:95], v[180:183]\n\t"
            "v_mul_f32 v160, v236, v160\n\t"
            "v_mul_f32 v161, v237, v161\n\t"
            "v_mfma_f32_16x16x32_bf16 v[180:183], v[224:227], v[88:91], v[180:183]\n\t"
            "v_mul_f32 v164, v236, v164\n\t"
            "v_mul_f32 v165, v237, v165\n\t"
            "v_mfma_f32_16x16x32_bf16 v[184:187], v[224:227], v[112:115], v[184:187]\n\t"
            "v_mul_f32 v176, v236, v176\n\t"
            "v_mul_f32 v177, v237, v177\n\t"
            "v_mfma_f32_16x16x32_bf16 v[184:187], v[228:231], v[116:119], v[184:187]\n\t"
            "ds_write_b128 v243, v[200:203]\n\t"
            "v_mfma_f32_16x16x32_bf16 v[188:191], v[228:231], v[140:143], v[188:191]\n\t"
            "ds_read_b128 v[228:231], v233 offset:128\n\t"
            "v_mul_f32 v180, v236, v180\n\t"
            "v_mfma_f32_16x16x32_bf16 v[188:191], v[224:227], v[136:139], v[188:191]\n\t"
            "ds_read_b128 v[224:227], v232 offset:128\n\t"
            "v_mul_f32 v181, v237, v181\n\t"
            "s_waitcnt lgkmcnt(3)\n\t"
            "v_mfma_f32_16x16x32_bf16 v[200:203], v[192:195], v[0:3], 0\n\t"
            "v_mul_f32 v184, v236, v184\n\t"
            "v_mul_f32 v185, v237, v185\n\t"
            "v_mfma_f32_16x16x32_bf16 v[200:203], v[196:199], v[4:7], v[200:203]\n\t"
            "ds_write_b128 v243, v[204:207] offset:1536\n\t"
            "v_mfma_f32_16x16x32_bf16 v[204:207], v[196:199], v[28:31], 0\n\t"
            "v_mul_f32 v188, v236, v188\n\t"
            "v_mul_f32 v189, v237, v189\n\t"
            "v_mfma_f32_16x16x32_bf16 v[204:207], v[192:195], v[24:27], v[204:207]\n\t"
            "ds_write_b128 v243, v[208:211] offset:3072\n\t"
            "v_mfma_f32_16x16x32_bf16 v[208:211], v[192:195], v[48:51], 0\n\t"
            "v_mul_f32 v162, v248, v162\n\t"
            "v_mul_f32 v163, v249, v163\n\t"
            "v_mfma_f32_16x16x32_bf16 v[208:211], v[196:199], v[52:55], v[208:211]\n\t"
            "ds_write_b128 v243, v[212:215] offset:4608\n\t"
            "v_mfma_f32_16x16x32_bf16 v[212:215], v[196:199], v[76:79], 0\n\t"
            "v_cvt_pk_bf16_f32 v160, v160, v161\n\t"
            "v_mul_f32 v166, v248, v166\n\t"
            "v_mfma_f32_16x16x32_bf16 v[212:215], v[192:195], v[72:75], v[212:215]\n\t"
            "ds_write_b128 v243, v[216:219] offset:6144\n\t"
            "v_mfma_f32_16x16x32_bf16 v[216:219], v[192:195], v[96:99], 0\n\t"
            "v_mul_f32 v167, v249, v167\n\t"
            "v_cvt_pk_bf16_f32 v164, v164, v165\n\t"
            "v_mfma_f32_16x16x32_bf16 v[216:219], v[196:199], v[100:103], v[216:219]\n\t"
            "ds_write_b128 v243, v[220:223] offset:7680\n\t"
            "v_mfma_f32_16x16x32_bf16 v[220:223], v[196:199], v[124:127], 0\n\t"
            "v_mul_f32 v178, v248, v178\n\t"
            "v_mul_f32 v179, v249, v179\n\t"
            "v_mfma_f32_16x16x32_bf16 v[220:223], v[192:195], v[120:123], v[220:223]\n\t"
            "v_cvt_pk_bf16_f32 v176, v176, v177\n\t"
            "v_mul_f32 v182, v248, v182\n\t"
            "s_waitcnt lgkmcnt(5)\n\t"
            "v_mfma_f32_16x16x32_bf16 v[200:203], v[224:227], v[8:11], v[200:203]\n\t"
            "s_waitcnt vmcnt(0)\n\t"
            "ds_read_b64 v[144:145], v253\n\t"
            "ds_read_b64 v[146:147], v253 offset:96\n\t"
            "v_mfma_f32_16x16x32_bf16 v[200:203], v[228:231], v[12:15], v[200:203]\n\t"
            "ds_read_b64 v[148:149], v253 offset:1744\n\t"
            "ds_read_b64 v[150:151], v253 offset:1840\n\t"
            "v_mfma_f32_16x16x32_bf16 v[204:207], v[228:231], v[36:39], v[204:207]\n\t"
            "ds_read_b64 v[152:153], v253 offset:8\n\t"
            "ds_read_b64 v[154:155], v253 offset:104\n\t"
            "v_mfma_f32_16x16x32_bf16 v[204:207], v[224:227], v[32:35], v[204:207]\n\t"
            "ds_read_b64 v[156:157], v253 offset:1752\n\t"
            "ds_read_b64 v[158:159], v253 offset:1848\n\t"
            "v_mfma_f32_16x16x32_bf16 v[208:211], v[224:227], v[56:59], v[208:211]\n\t"
            "v_mul_f32 v183, v249, v183\n\t"
            "v_cvt_pk_bf16_f32 v180, v180, v181\n\t"
            "v_mfma_f32_16x16x32_bf16 v[208:211], v[228:231], v[60:63], v[208:211]\n\t"
            "v_mul_f32 v186, v248, v186\n\t"
            "v_mul_f32 v187, v249, v187\n\t"
            "v_mfma_f32_16x16x32_bf16 v[212:215], v[228:231], v[84:87], v[212:215]\n\t"
            "v_cvt_pk_bf16_f32 v184, v184, v185\n\t"
            "v_mul_f32 v190, v248, v190\n\t"
            "v_mfma_f32_16x16x32_bf16 v[212:215], v[224:227], v[80:83], v[212:215]\n\t"
            "v_mul_f32 v191, v249, v191\n\t"
            "v_cvt_pk_bf16_f32 v188, v188, v189\n\t"
            "v_mfma_f32_16x16x32_bf16 v[216:219], v[224:227], v[104:107], v[216:219]\n\t"
            "v_cvt_pk_bf16_f32 v161, v162, v163\n\t"
            "v_cvt_pk_bf16_f32 v165, v166, v167\n\t"
            "v_mfma_f32_16x16x32_bf16 v[216:219], v[228:231], v[108:111], v[216:219]\n\t"
            "ds_read_b64 v[162:163], v253 offset:112\n\t"
            "ds_read_b64 v[166:167], v253 offset:1856\n\t"
            "v_mfma_f32_16x16x32_bf16 v[220:223], v[228:231], v[132:135], v[220:223]\n\t"
            "s_waitcnt lgkmcnt(14)\n\t"
            "ds_read_b128 v[228:231], v233 offset:256\n\t"
            "v_cvt_pk_bf16_f32 v177, v178, v179\n\t"
            "v_mfma_f32_16x16x32_bf16 v[220:223], v[224:227], v[128:131], v[220:223]\n\t"
            "s_waitcnt lgkmcnt(14)\n\t"
            "ds_read_b128 v[224:227], v232 offset:256\n\t"
            "v_cvt_pk_bf16_f32 v181, v182, v183\n\t"
            "v_cvt_pk_bf16_f32 v185, v186, v187\n\t"
            "v_cvt_pk_bf16_f32 v189, v190, v191\n\t"
            "v_xor_b32 v247, 0x100, v247\n\t"
            "s_waitcnt lgkmcnt(14)\n\t"
            "ds_write_b64 v244, v[160:161]\n\t"
            "s_waitcnt lgkmcnt(14)\n\t"
            "ds_read_b64 v[160:161], v253 offset:16\n\t"
            "s_waitcnt lgkmcnt(14)\n\t"
            "ds_write_b64 v244, v[164:165] offset:1536\n\t"
            "s_waitcnt lgkmcnt(14)\n\t"
            "ds_read_b64 v[164:165], v253 offset:1760\n\t"
            "s_waitcnt lgkmcnt(14)\n\t"
            "ds_write_b64 v244, v[176:177] offset:3072\n\t"
            "s_waitcnt lgkmcnt(14)\n\t"
            "ds_write_b64 v244, v[180:181] offset:4608\n\t"
            "s_waitcnt lgkmcnt(14)\n\t"
            "ds_write_b64 v244, v[184:185] offset:6144\n\t"
            "s_waitcnt lgkmcnt(14)\n\t"
            "ds_write_b64 v244, v[188:189] offset:7680\n\t"
            "s_waitcnt lgkmcnt(14)\n\t"
            "ds_write_b32 v246, v168\n\t"
            "s_waitcnt lgkmcnt(14)\n\t"
            "ds_read_b128 v[168:171], v254\n\t"
            "s_waitcnt lgkmcnt(1)\n\t"
            "s_barrier\n\t"
            "s_cmp_lt_u32 s46, 0x7000000\n\t"
            "s_cbranch_scc1 .Lwc_loop%=\n\t"
            "s_branch .Lwc_end%=\n\t"
            ".Lwc_spin%=:\n\t"
            "s_cmp_eq_u32 s83, 0\n\t"
            "s_cbranch_scc1 .Lwc_go%=\n\t"
            "s_sub_u32 s83, s83, 1\n\t"
            "s_sleep 2\n\t"
            "v_mov_b32 v234, 0\n\t"
            "ds_read_b64 v[234:235], v234 offset:7856\n\t"
            "s_waitcnt lgkmcnt(0)\n\t"
            "v_readfirstlane_b32 s77, v234\n\t"
            "v_readfirstlane_b32 s38, v235\n\t"
            "s_add_u32 s77, s77, s38\n\t"
            "s_cmp_ge_u32 s77, s44\n\t"
            "s_cbranch_scc0 .Lwc_spin%=\n\t"
            "s_branch .Lwc_go%=\n\t"
            ".Lwc_end%=:\n\t"
            "s_waitcnt lgkmcnt(0)\n\t"
            "s_nop 7\n\t"
            "s_barrier\n\t"
            : "+{v246}"(aRSw_), "+{v247}"(aRSr_), "+{s44}"(s44), B_SIO
            : "{v242}"(pE_), "{v243}"(aVAw_), "{v244}"(aVBw_), "{v245}"(pST_), "{v250}"(pXw_), "{v251}"(pSTw_), "{v252}"(aCA_), "{v253}"(aB_), "{v254}"(aA_), "{v255}"(idx_), B_SIN, "{s82}"(touch)
            : "v144", "v145", "v146", "v147", "v148", "v149", "v150", "v151", "v152", "v153", "v154", "v155", "v156", "v157", "v158", "v159", "v160", "v161", "v162", "v163", "v164", "v165", "v166", "v167", "v168", "v169", "v170", "v171", "v172", "v173", "v174", "v175", "v176", "v177", "v178", "v179", "v180", "v181", "v182", "v183", "v184", "v185", "v186", "v187", "v188", "v189", "v190", "v191", "v192", "v193", "v194", "v195", "v196", "v197", "v198", "v199", "v236", "v237", "v248", "v249", "v0", "v1", "v2", "v3", "v4", "v5", "v6", "v7", "v8", "v9", "v10", "v11", "v12", "v13", "v14", "v15", "v16", "v17", "v18", "v19", "v20", "v21", "v22", "v23", "v24", "v25", "v26", "v27", "v28", "v29", "v30", "v31", "v32", "v33", "v34", "v35", "v36", "v37", "v38", "v39", "v40", "v41", "v42", "v43", "v44", "v45", "v46", "v47", "v48", "v49", "v50", "v51", "v52", "v53", "v54", "v55", "v56", "v57", "v58", "v59", "v60", "v61", "v62", "v63", "v64", "v65", "v66", "v67", "v68", "v69", "v70", "v71", "v72", "v73", "v74", "v75", "v76", "v77", "v78", "v79", "v80", "v81", "v82", "v83", "v84", "v85", "v86", "v87", "v88", "v89", "v90", "v91", "v92", "v93", "v94", "v95", "v96", "v97", "v98", "v99", "v100", "v101", "v102", "v103", "v104", "v105", "v106", "v107", "v108", "v109", "v110", "v111", "v112", "v113", "v114", "v115", "v116", "v117", "v118", "v119", "v120", "v121", "v122", "v123", "v124", "v125", "v126", "v127", "v128", "v129", "v130", "v131", "v132", "v133", "v134", "v135", "v136", "v137", "v138", "v139", "v140", "v141", "v142", "v143", "v200", "v201", "v202", "v203", "v204", "v205", "v206", "v207", "v208", "v209", "v210", "v211", "v212", "v213", "v214", "v215", "v216", "v217", "v218", "v219", "v220", "v221", "v222", "v223", "v224", "v225", "v226", "v227", "v228", "v229", "v230", "v231", "v232", "v233", "v234", "v235", B_SCLOB);
        }
        __builtin_amdgcn_endpgm();
    } else {
        LANE_SETUP
        unsigned t45, t56;
        u32x4 xl[9];
        {
            const unsigned oE_ = ldsb + fE + (O_OFF - X_OFF), oO_ = oE_ ^ 64;
            unsigned l16_ = lane * 16;
            unsigned pr[3];
#pragma unroll
            for (int k = 0; k < 3; ++k) {
                const int c8 = 12 * (w - 2) + 4 * k + g;
                const int ws = c8 / 6, r6 = c8 - 6 * ws;
                pr[k] = l15 * 384 + (r6 >> 1) * 128 + (((2 * ws + (r6 & 1)) ^ sw) << 4);
            }
            const unsigned pR0_ = pr[0], pR1_ = pr[1], pR2_ = pr[2];
            B_SCALARS
            asm volatile(
            "s_waitcnt lgkmcnt(0)\n\t"
            "s_mov_b32 s83, 0x10000\n\t"
            "s_mov_b32 s84, -47104\n\t"
            "s_nop 7\n\t"
            "s_barrier\n\t"
            "ds_read_b64 v[236:237], v252 offset:2560\n\t"
            "s_waitcnt lgkmcnt(0)\n\t"
            "s_mov_b64 exec, 1\n\t"
            "buffer_store_dwordx2 v[236:237], off, s[28:31], s44 sc1\n\t"
            "s_mov_b64 exec, -1\n\t"
            "s_sleep 30\n\t"
            "ds_read_b32 v249, v252 offset:12\n\t"
            "s_waitcnt lgkmcnt(0)\n\t"
            "v_and_b32 v249, s69, v249\n\t"
            "s_mov_b32 exec_hi, 0x3fffff\n\t"
            "s_add_u32 m0, s66, 0\n\t"
            "s_add_u32 s49, s79, 0\n\t"
            "buffer_load_dwordx4 v249, s[72:75], s49 offen lds\n\t"
            "s_mov_b32 exec_hi, -1\n\t"
            "s_mov_b32 exec_hi, 0x3fffff\n\t"
            "s_add_u32 m0, s66, 872\n\t"
            "s_add_u32 s49, s47, 196224\n\t"
            "buffer_load_dwordx4 v249, s[72:75], s49 offen lds\n\t"
            "s_mov_b32 exec_hi, -1\n\t"
            "s_mov_b32 exec_hi, 0x3fffff\n\t"
            "s_add_u32 m0, s66, 1744\n\t"
            "s_add_u32 s49, s47, 392448\n\t"
            "buffer_load_dwordx4 v249, s[72:75], s49 offen lds\n\t"
            "s_mov_b32 exec_hi, -1\n\t"
            "s_mov_b32 exec_hi, 0x3fffff\n\t"
            "s_add_u32 m0, s66, 2616\n\t"
            "s_add_u32 s49, s47, 588672\n\t"
            "buffer_load_dwordx4 v249, s[72:75], s49 offen lds\n\t"
            "s_mov_b32 exec_hi, -1\n\t"
            "s_mov_b32 exec_hi, 0x3fffff\n\t"
            "s_add_u32 m0, s66, 3488\n\t"
            "s_add_u32 s49, s47, 784896\n\t"
            "buffer_load_dwordx4 v249, s[72:75], s49 offen lds\n\t"
            "s_mov_b32 exec_hi, -1\n\t"
            "s_mov_b32 exec_hi, 0x3fffff\n\t"
            "s_add_u32 m0, s66, 4360\n\t"
            "s_add_u32 s49, s47, 981120\n\t"
            "buffer_load_dwordx4 v249, s[72:75], s49 offen lds\n\t"
            "s_mov_b32 exec_hi, -1\n\t"
            "s_mov_b32 exec_hi, 0x3fffff\n\t"
            "s_add_u32 m0, s66, 5232\n\t"
            "s_add_u32 s49, s47, 1177344\n\t"
            "buffer_load_dwordx4 v249, s[72:75], s49 offen lds\n\t"
            "s_mov_b32 exec_hi, -1\n\t"
            "s_mov_b32 exec_hi, 0x3fffff\n\t"
            "s_add_u32 m0, s66, 6104\n\t"
            "s_add_u32 s49, s47, 1373568\n\t"
            "buffer_load_dwordx4 v249, s[72:75], s49 offen lds\n\t"
            "s_mov_b32 exec_hi, -1\n\t"
            "s_mov_b32 exec_hi, 0x3fffff\n\t"
            "s_add_u32 m0, s66, 6976\n\t"
            "s_add_u32 s49, s47, 1569792\n\t"
            "buffer_load_dwordx4 v249, s[72:75], s49 offen lds\n\t"
            "s_mov_b32 exec_hi, -1\n\t"
            "s_add_u32 s49, s33, -73728\n\t"
            "buffer_load_dwordx4 v[0:3], v247, s[28:31], s49 offen sc1\n\t"
            "s_add_u32 s49, s33, -67584\n\t"
            "buffer_load_dwordx4 v[24:27], v247, s[28:31], s49 offen sc1\n\t"
            "s_add_u32 s49, s33, -61440\n\t"
            "buffer_load_dwordx4 v[48:51], v247, s[28:31], s49 offen sc1\n\t"
            "s_add_u32 s49, s33, -55296\n\t"
            "buffer_load_dwordx4 v[72:75], v247, s[28:31], s49 offen sc1\n\t"
            "s_add_u32 s49, s33, -49152\n\t"
            "buffer_load_dwordx4 v[96:99], v247, s[28:31], s49 offen sc1\n\t"
            "s_add_u32 s49, s33, -43008\n\t"
            "buffer_load_dwordx4 v[120:123], v247, s[28:31], s49 offen sc1\n\t"
            "s_add_u32 s49, s33, -72704\n\t"
            "buffer_load_dwordx4 v[4:7], v247, s[28:31], s49 offen sc1\n\t"
            "s_add_u32 s49, s33, -66560\n\t"
            "buffer_load_dwordx4 v[28:31], v247, s[28:31], s49 offen sc1\n\t"
            "s_add_u32 s49, s33, -60416\n\t"
            "buffer_load_dwordx4 v[52:55], v247, s[28:31], s49 offen sc1\n\t"
            "s_add_u32 s49, s33, -54272\n\t"
            "buffer_load_dwordx4 v[76:79], v247, s[28:31], s49 offen sc1\n\t"
            "s_add_u32 s49, s33, -48128\n\t"
            "buffer_load_dwordx4 v[100:103], v247, s[28:31], s49 offen sc1\n\t"
            "s_add_u32 s49, s33, -41984\n\t"
            "buffer_load_dwordx4 v[124:127], v247, s[28:31], s49 offen sc1\n\t"
            "s_add_u32 s49, s33, -71680\n\t"
            "buffer_load_dwordx4 v[8:11], v247, s[28:31], s49 offen sc1\n\t"
            "s_add_u32 s49, s33, -65536\n\t"
            "buffer_load_dwordx4 v[32:35], v247, s[28:31], s49 offen sc1\n\t"
            "s_add_u32 s49, s33, -59392\n\t"
            "buffer_load_dwordx4 v[56:59], v247, s[28:31], s49 offen sc1\n\t"
            "s_add_u32 s49, s33, -53248\n\t"
            "buffer_load_dwordx4 v[80:83], v247, s[28:31], s49 offen sc1\n\t"
            "s_add_u32 s49, s33, -47104\n\t"
            "buffer_load_dwordx4 v[104:107], v247, s[28:31], s49 offen sc1\n\t"
            "s_add_u32 s49, s33, -40960\n\t"
            "buffer_load_dwordx4 v[128:131], v247, s[28:31], s49 offen sc1\n\t"
            "s_add_u32 s49, s33, -70656\n\t"
            "buffer_load_dwordx4 v[12:15], v247, s[28:31], s49 offen sc1\n\t"
            "s_add_u32 s49, s33, -64512\n\t"
            "buffer_load_dwordx4 v[36:39], v247, s[28:31], s49 offen sc1\n\t"
            "s_add_u32 s49, s33, -58368\n\t"
            "buffer_load_dwordx4 v[60:63], v247, s[28:31], s49 offen sc1\n\t"
            "s_add_u32 s49, s33, -52224\n\t"
            "buffer_load_dwordx4 v[84:87], v247, s[28:31], s49 offen sc1\n\t"
            "s_add_u32 s49, s33, -46080\n\t"
            "buffer_load_dwordx4 v[108:111], v247, s[28:31], s49 offen sc1\n\t"
            "s_add_u32 s49, s33, -39936\n\t"
            "buffer_load_dwordx4 v[132:135], v247, s[28:31], s49 offen sc1\n\t"
            "s_add_u32 s49, s33, -69632\n\t"
            "buffer_load_dwordx4 v[16:19], v247, s[28:31], s49 offen sc1\n\t"
            "s_add_u32 s49, s33, -63488\n\t"
            "buffer_load_dwordx4 v[40:43], v247, s[28:31], s49 offen sc1\n\t"
            "s_add_u32 s49, s33, -57344\n\t"
            "buffer_load_dwordx4 v[64:67], v247, s[28:31], s49 offen sc1\n\t"
            "s_add_u32 s49, s33, -51200\n\t"
            "buffer_load_dwordx4 v[88:91], v247, s[28:31], s49 offen sc1\n\t"
            "s_add_u32 s49, s33, -45056\n\t"
            "buffer_load_dwordx4 v[112:115], v247, s[28:31], s49 offen sc1\n\t"
            "s_add_u32 s49, s33, -38912\n\t"
            "buffer_load_dwordx4 v[136:139], v247, s[28:31], s49 offen sc1\n\t"
            "s_add_u32 s49, s33, -68608\n\t"
            "buffer_load_dwordx4 v[20:23], v247, s[28:31], s49 offen sc1\n\t"
            "s_add_u32 s49, s33, -62464\n\t"
            "buffer_load_dwordx4 v[44:47], v247, s[28:31], s49 offen sc1\n\t"
            "s_add_u32 s49, s33, -56320\n\t"
            "buffer_load_dwordx4 v[68:71], v247, s[28:31], s49 offen sc1\n\t"
            "s_add_u32 s49, s33, -50176\n\t"
            "buffer_load_dwordx4 v[92:95], v247, s[28:31], s49 offen sc1\n\t"
            "s_add_u32 s49, s33, -44032\n\t"
            "buffer_load_dwordx4 v[116:119], v247, s[28:31], s49 offen sc1\n\t"
            "s_add_u32 s49, s33, -37888\n\t"
            "buffer_load_dwordx4 v[140:143], v247, s[28:31], s49 offen sc1\n\t"
            "s_setprio 3\n\t"
            "ds_read_b128 v[168:171], v254\n\t"
            "ds_read_b128 v[172:175], v254 offset:64\n\t"
            "ds_read_b128 v[220:223], v229 offset:12288\n\t"
            "ds_read_b128 v[224:227], v229 offset:13312\n\t"
            "ds_read_b128 v[200:203], v232\n\t"
            "ds_read_b128 v[208:211], v232 offset:128\n\t"
            "ds_read_b128 v[216:219], v232 offset:256\n\t"
            "s_waitcnt vmcnt(36)\n\t"
            "ds_read_b64 v[144:145], v253\n\t"
            "ds_read_b64 v[146:147], v253 offset:96\n\t"
            "ds_read_b64 v[148:149], v253 offset:1744\n\t"
            "ds_read_b64 v[150:151], v253 offset:1840\n\t"
            "ds_read_b64 v[152:153], v253 offset:8\n\t"
            "ds_read_b64 v[154:155], v253 offset:104\n\t"
            "ds_read_b64 v[156:157], v253 offset:1752\n\t"
            "ds_read_b64 v[158:159], v253 offset:1848\n\t"
            "s_waitcnt lgkmcnt(14)\n\t"
            "ds_read_b64 v[160:161], v253 offset:16\n\t"
            "s_waitcnt lgkmcnt(14)\n\t"
            "ds_read_b64 v[162:163], v253 offset:112\n\t"
            "s_waitcnt lgkmcnt(14)\n\t"
            "ds_read_b64 v[164:165], v253 offset:1760\n\t"
            "s_waitcnt lgkmcnt(14)\n\t"
            "ds_read_b64 v[166:167], v253 offset:1856\n\t"
            "v_mov_b64 v[176:177], 0\n\t"
            "v_mov_b64 v[178:179], 0\n\t"
            "v_mov_b64 v[180:181], 0\n\t"
            "v_mov_b64 v[182:183], 0\n\t"
            "v_mov_b32 v249, s76\n\t"
            "v_mov_b64 v[184:185], 0\n\t"
            "v_mov_b64 v[186:187], 0\n\t"
            "v_mov_b64 v[188:189], 0\n\t"
            "v_mov_b64 v[190:191], 0\n\t"
            "v_add_u32 v236, s67, v250\n\t"
            "v_add_u32 v248, s67, v251\n\t"
            "v_xor_b32 v235, 64, v232\n\t"
            "s_nop 0\n\t"
            "s_waitcnt lgkmcnt(14)\n\t"
            "ds_read_b32 v249, v249\n\t"
            "s_waitcnt lgkmcnt(14)\n\t"
            "ds_read_b128 v[204:207], v235\n\t"
            "s_waitcnt lgkmcnt(14)\n\t"
            "ds_read_b128 v[212:215], v235 offset:128\n\t"
            "v_add_f32 v220, v220, v222\n\t"
            "v_add_f32 v224, v224, v226\n\t"
            "v_add_f32 v220, v220, v224\n\t"
            "v_add_f32 v221, v221, v223\n\t"
            "v_add_f32 v225, v225, v227\n\t"
            "v_mul_f32 v220, 0x3baaaaab, v220\n\t"
            "v_add_f32 v221, v221, v225\n\t"
            "v_mul_f32_e64 v220, v220, -v220\n\t"
            "v_fmac_f32 v220, 0x3baaaaab, v221\n\t"
            "s_waitcnt lgkmcnt(11)\n\t"
            "v_smfmac_f32_16x16x64_bf16 v[176:179], v[168:171], v[144:151], v255\n\t"
            "ds_read_b128 v[168:171], v254 offset:128\n\t"
            "s_waitcnt lgkmcnt(8)\n\t"
            "v_smfmac_f32_16x16x64_bf16 v[180:183], v[172:175], v[152:159], v255\n\t"
            "ds_read_b64 v[144:145], v253 offset:24\n\t"
            "ds_read_b64 v[146:147], v253 offset:120\n\t"
            "ds_read_b64 v[148:149], v253 offset:1768\n\t"
            "ds_read_b64 v[150:151], v253 offset:1864\n\t"
            "ds_read_b128 v[172:175], v254 offset:192\n\t"
            "ds_read_b64 v[152:153], v253 offset:32\n\t"
            "ds_read_b64 v[154:155], v253 offset:128\n\t"
            "s_waitcnt lgkmcnt(14)\n\t"
            "ds_read_b64 v[156:157], v253 offset:1776\n\t"
            "s_waitcnt lgkmcnt(14)\n\t"
            "ds_read_b64 v[158:159], v253 offset:1872\n\t"
            "s_waitcnt lgkmcnt(14)\n\t"
            "ds_read_b128 v[224:227], v232 offset:6144\n\t"
            "v_cvt_pk_bf16_f32 v176, v176, v177\n\t"
            "v_cvt_pk_bf16_f32 v177, v178, v179\n\t"
            "v_cvt_pk_bf16_f32 v178, v180, v181\n\t"
            "v_cvt_pk_bf16_f32 v179, v182, v183\n\t"
            "v_mfma_f32_4x4x4_16b_bf16 v[196:199], v[176:177], v[176:177], 0\n\t"
            "v_mov_b64 v[180:181], 0\n\t"
            "v_mfma_f32_16x16x32_bf16 v[192:195], v[238:241], v[176:179], 0\n\t"
            "s_waitcnt lgkmcnt(13)\n\t"
            "v_readfirstlane_b32 s26, v249\n\t"
            "v_mov_b64 v[182:183], 0\n\t"
            "v_mfma_f32_4x4x4_16b_bf16 v[196:199], v[178:179], v[178:179], v[196:199]\n\t"
            "ds_read_b32 v249, v252 offset:12\n\t"
            "s_and_b32 s68, s26, 0x7ffffff\n\t"
            "s_add_u32 s68, s68, s78\n\t"
            "s_and_b32 s69, s26, 0x38000000\n\t"
            "s_or_b32 s69, s69, s39\n\t"
            "s_bitcmp1_b32 s26, 27\n\t"
            "s_cselect_b32 s79, 2.0, s68\n\t"
            "s_and_b32 s57, s56, 3\n\t"
            "s_lshl_b32 s58, s57, 3\n\t"
            "s_lshr_b32 m0, s54, s58\n\t"
            "s_lshl_b32 s58, s57, 4\n\t"
            "s_lshr_b64 s[36:37], s[50:51], s58\n\t"
            "s_and_b32 s57, s36, 0xffff\n\t"
            "s_add_u32 s57, s57, s67\n\t"
            "v_writelane_b32 v236, s57, 63\n\t"
            "v_writelane_b32 v236, s59, m0\n\t"
            "v_xor_b32 v237, 16, v236\n\t"
            "s_lshr_b64 s[36:37], s[52:53], s58\n\t"
            "s_and_b32 s58, s36, 0xffff\n\t"
            "s_add_u32 s58, s58, s67\n\t"
            "s_waitcnt lgkmcnt(11)\n\t"
            "v_smfmac_f32_16x16x64_bf16 v[184:187], v[168:171], v[160:167], v255\n\t"
            "s_and_b32 s38, s52, 0xffff\n\t"
            "s_add_u32 s38, s38, s67\n\t"
            "ds_read_b128 v[168:171], v254 offset:256\n\t"
            "ds_read_b64 v[160:161], v253 offset:40\n\t"
            "ds_read_b64 v[162:163], v253 offset:136\n\t"
            "ds_read_b64 v[164:165], v253 offset:1784\n\t"
            "s_waitcnt lgkmcnt(14)\n\t"
            "ds_read_b64 v[166:167], v253 offset:1880\n\t"
            "s_waitcnt lgkmcnt(11)\n\t"
            "v_smfmac_f32_16x16x64_bf16 v[188:191], v[172:175], v[144:151], v255\n\t"
            "ds_read_b128 v[172:175], v254 offset:320\n\t"
            "ds_read_b64 v[144:145], v253 offset:48\n\t"
            "ds_read_b64 v[146:147], v253 offset:144\n\t"
            "ds_read_b64 v[148:149], v253 offset:1792\n\t"
            "s_waitcnt lgkmcnt(14)\n\t"
            "ds_read_b64 v[150:151], v253 offset:1888\n\t"
            "s_waitcnt lgkmcnt(14)\n\t"
            "ds_write_b128 v236, v[176:179]\n\t"
            "v_mov_b64 v[176:177], 0\n\t"
            "v_mov_b64 v[178:179], 0\n\t"
            "v_cvt_pk_bf16_f32 v184, v184, v185\n\t"
            "v_cvt_pk_bf16_f32 v185, v186, v187\n\t"
            "v_cvt_pk_bf16_f32 v186, v188, v189\n\t"
            "v_cvt_pk_bf16_f32 v187, v190, v191\n\t"
            "v_mfma_f32_4x4x4_16b_bf16 v[196:199], v[184:185], v[184:185], v[196:199]\n\t"
            "s_waitcnt lgkmcnt(14)\n\t"
            "ds_write_b128 v237, v[184:187]\n\t"
            "v_mfma_f32_16x16x32_bf16 v[192:195], v[238:241], v[184:187], v[192:195]\n\t"
            "v_mov_b64 v[184:185], 0\n\t"
            "v_mov_b64 v[188:189], 0\n\t"
            "v_mfma_f32_4x4x4_16b_bf16 v[196:199], v[186:187], v[186:187], v[196:199]\n\t"
            "v_mov_b64 v[186:187], 0\n\t"
            "v_mov_b64 v[190:191], 0\n\t"
            "v_writelane_b32 v248, s58, 63\n\t"
            "v_writelane_b32 v248, s38, m0\n\t"
            "s_waitcnt lgkmcnt(12)\n\t"
            "v_and_b32 v249, s69, v249\n\t"
            "v_max_f32 v220, 0, v220\n\t"
            "v_add_f32 v220, 0x3727c5ac, v220\n\t"
            "v_rsq_f32 v220, v220\n\t"
            "s_waitcnt lgkmcnt(11)\n\t"
            "v_smfmac_f32_16x16x64_bf16 v[176:179], v[168:171], v[152:159], v255\n\t"
            "ds_read_b128 v[168:171], v254 offset:384\n\t"
            "ds_read_b64 v[152:153], v253 offset:56\n\t"
            "ds_read_b64 v[154:155], v253 offset:152\n\t"
            "ds_read_b64 v[156:157], v253 offset:1800\n\t"
            "s_waitcnt lgkmcnt(14)\n\t"
            "ds_read_b64 v[158:159], v253 offset:1896\n\t"
            "s_waitcnt lgkmcnt(11)\n\t"
            "v_smfmac_f32_16x16x64_bf16 v[180:183], v[172:175], v[160:167], v255\n\t"
            "ds_read_b128 v[172:175], v254 offset:448\n\t"
            "ds_read_b64 v[160:161], v253 offset:64\n\t"
            "ds_read_b64 v[162:163], v253 offset:160\n\t"
            "ds_read_b64 v[164:165], v253 offset:1808\n\t"
            "s_waitcnt lgkmcnt(14)\n\t"
            "ds_read_b64 v[166:167], v253 offset:1904\n\t"
            "v_cvt_pk_bf16_f32 v176, v176, v177\n\t"
            "v_cvt_pk_bf16_f32 v177, v178, v179\n\t"
            "v_cvt_pk_bf16_f32 v178, v180, v181\n\t"
            "v_cvt_pk_bf16_f32 v179, v182, v183\n\t"
            "v_mfma_f32_4x4x4_16b_bf16 v[196:199], v[176:177], v[176:177], v[196:199]\n\t"
            "s_waitcnt lgkmcnt(14)\n\t"
            "ds_write_b128 v236, v[176:179] offset:128\n\t"
            "v_mfma_f32_16x16x32_bf16 v[192:195], v[238:241], v[176:179], v[192:195]\n\t"
            "v_mov_b64 v[176:177], 0\n\t"
            "v_mov_b64 v[180:181], 0\n\t"
            "v_mfma_f32_4x4x4_16b_bf16 v[196:199], v[178:179], v[178:179], v[196:199]\n\t"
            "v_mov_b64 v[178:179], 0\n\t"
            "v_mov_b64 v[182:183], 0\n\t"
            "s_waitcnt lgkmcnt(14)\n\t"
            "ds_write_b32 v228, v220\n\t"
            "s_waitcnt lgkmcnt(14)\n\t"
            "ds_read_b128 v[220:223], v235 offset:256\n\t"
            "s_waitcnt lgkmcnt(14)\n\t"
            "ds_read_b32 v230, v233\n\t"
            "s_waitcnt lgkmcnt(14)\n\t"
            "ds_read_b32 v231, v233 offset:64\n\t"
            "v_xor_b32 v228, 32, v234\n\t"
            "s_waitcnt lgkmcnt(14)\n\t"
            "v_smfmac_f32_16x16x64_bf16 v[184:187], v[168:171], v[144:151], v255\n\t"
            "ds_read_b128 v[168:171], v254 offset:512\n\t"
            "s_waitcnt lgkmcnt(14)\n\t"
            "ds_read_b64 v[144:145], v253 offset:72\n\t"
            "s_waitcnt lgkmcnt(14)\n\t"
            "ds_read_b64 v[146:147], v253 offset:168\n\t"
            "s_waitcnt lgkmcnt(14)\n\t"
            "ds_read_b64 v[148:149], v253 offset:1816\n\t"
            "s_waitcnt lgkmcnt(14)\n\t"
            "ds_read_b64 v[150:151], v253 offset:1912\n\t"
            "s_waitcnt lgkmcnt(14)\n\t"
            "v_smfmac_f32_16x16x64_bf16 v[188:191], v[172:175], v[152:159], v255\n\t"
            "ds_read_b128 v[172:175], v254 offset:576\n\t"
            "s_waitcnt lgkmcnt(14)\n\t"
            "ds_read_b64 v[152:153], v253 offset:80\n\t"
            "s_waitcnt lgkmcnt(14)\n\t"
            "ds_read_b64 v[154:155], v253 offset:176\n\t"
            "s_waitcnt lgkmcnt(14)\n\t"
            "ds_read_b64 v[156:157], v253 offset:1824\n\t"
            "s_waitcnt lgkmcnt(14)\n\t"
            "ds_read_b64 v[158:159], v253 offset:1920\n\t"
            "v_cvt_pk_bf16_f32 v184, v184, v185\n\t"
            "v_cvt_pk_bf16_f32 v185, v186, v187\n\t"
            "v_cvt_pk_bf16_f32 v186, v188, v189\n\t"
            "v_cvt_pk_bf16_f32 v187, v190, v191\n\t"
            "v_mfma_f32_4x4x4_16b_bf16 v[196:199], v[184:185], v[184:185], v[196:199]\n\t"
            "s_waitcnt lgkmcnt(14)\n\t"
            "ds_write_b128 v237, v[184:187] offset:128\n\t"
            "v_mfma_f32_16x16x32_bf16 v[192:195], v[238:241], v[184:187], v[192:195]\n\t"
            "v_mov_b64 v[184:185], 0\n\t"
            "v_mov_b64 v[188:189], 0\n\t"
            "v_mfma_f32_4x4x4_16b_bf16 v[196:199], v[186:187], v[186:187], v[196:199]\n\t"
            "v_mov_b64 v[186:187], 0\n\t"
            "v_mov_b64 v[190:191], 0\n\t"
            "s_waitcnt lgkmcnt(10)\n\t"
            "v_smfmac_f32_16x16x64_bf16 v[176:179], v[168:171], v[160:167], v255\n\t"
            "ds_read_b128 v[168:171], v254 offset:640\n\t"
            "ds_read_b64 v[160:161], v253 offset:88\n\t"
            "ds_read_b64 v[162:163], v253 offset:184\n\t"
            "ds_read_b64 v[164:165], v253 offset:1832\n\t"
            "ds_read_b64 v[166:167], v253 offset:1928\n\t"
            "s_waitcnt lgkmcnt(10)\n\t"
            "v_smfmac_f32_16x16x64_bf16 v[180:183], v[172:175], v[144:151], v255\n\t"
            "ds_read_b128 v[172:175], v254 offset:704\n\t"
            "v_cvt_pk_bf16_f32 v176, v176, v177\n\t"
            "v_cvt_pk_bf16_f32 v177, v178, v179\n\t"
            "s_nop 1\n\t"
            "v_mfma_f32_4x4x4_16b_bf16 v[196:199], v[176:177], v[176:177], v[196:199]\n\t"
            "s_nop 1\n\t"
            "v_cvt_pk_bf16_f32 v178, v180, v181\n\t"
            "v_cvt_pk_bf16_f32 v179, v182, v183\n\t"
            "s_nop 1\n\t"
            "v_mfma_f32_4x4x4_16b_bf16 v[196:199], v[178:179], v[178:179], v[196:199]\n\t"
            "ds_write_b128 v236, v[176:179] offset:256\n\t"
            "v_mfma_f32_16x16x32_bf16 v[192:195], v[238:241], v[176:179], v[192:195]\n\t"
            "v_add_u32 v236, s48, v244\n\t"
            "s_waitcnt lgkmcnt(6)\n\t"
            "v_smfmac_f32_16x16x64_bf16 v[184:187], v[168:171], v[152:159], v255\n\t"
            "s_waitcnt lgkmcnt(1)\n\t"
            "v_smfmac_f32_16x16x64_bf16 v[188:191], v[172:175], v[160:167], v255\n\t"
            "s_mov_b32 exec_hi, 0x3fffff\n\t"
            "s_add_u32 m0, s66, 0\n\t"
            "s_add_u32 s49, s79, 0\n\t"
            "buffer_load_dwordx4 v249, s[72:75], s49 offen lds\n\t"
            "s_mov_b32 exec_hi, -1\n\t"
            "s_mov_b32 exec_hi, 0x3fffff\n\t"
            "s_add_u32 m0, s66, 872\n\t"
            "s_add_u32 s49, s68, 196224\n\t"
            "buffer_load_dwordx4 v249, s[72:75], s49 offen lds\n\t"
            "s_mov_b32 exec_hi, -1\n\t"
            "s_mov_b32 exec_hi, 0x3fffff\n\t"
            "s_add_u32 m0, s66, 1744\n\t"
            "s_add_u32 s49, s68, 392448\n\t"
            "buffer_load_dwordx4 v249, s[72:75], s49 offen lds\n\t"
            "s_mov_b32 exec_hi, -1\n\t"
            "s_mov_b32 exec_hi, 0x3fffff\n\t"
            "s_add_u32 m0, s66, 2616\n\t"
            "s_add_u32 s49, s68, 588672\n\t"
            "buffer_load_dwordx4 v249, s[72:75], s49 offen lds\n\t"
            "s_mov_b32 exec_hi, -1\n\t"
            "s_mov_b32 exec_hi, 0x3fffff\n\t"
            "s_add_u32 m0, s66, 3488\n\t"
            "s_add_u32 s49, s68, 784896\n\t"
            "buffer_load_dwordx4 v249, s[72:75], s49 offen lds\n\t"
            "s_mov_b32 exec_hi, -1\n\t"
            "s_mov_b32 exec_hi, 0x3fffff\n\t"
            "s_add_u32 m0, s66, 4360\n\t"
            "s_add_u32 s49, s68, 981120\n\t"
            "buffer_load_dwordx4 v249, s[72:75], s49 offen lds\n\t"
            "s_mov_b32 exec_hi, -1\n\t"
            "s_mov_b32 exec_hi, 0x3fffff\n\t"
            "s_add_u32 m0, s66, 5232\n\t"
            "s_add_u32 s49, s68, 1177344\n\t"
            "buffer_load_dwordx4 v249, s[72:75], s49 offen lds\n\t"
            "s_mov_b32 exec_hi, -1\n\t"
            "v_cvt_pk_bf16_f32 v184, v184, v185\n\t"
            "v_cvt_pk_bf16_f32 v185, v186, v187\n\t"
            "v_cvt_pk_bf16_f32 v186, v188, v189\n\t"
            "v_cvt_pk_bf16_f32 v187, v190, v191\n\t"
            "v_mfma_f32_4x4x4_16b_bf16 v[196:199], v[184:185], v[184:185], v[196:199]\n\t"
            "s_mov_b32 exec_hi, 0x3fffff\n\t"
            "s_add_u32 m0, s66, 6104\n\t"
            "s_add_u32 s49, s68, 1373568\n\t"
            "buffer_load_dwordx4 v249, s[72:75], s49 offen lds\n\t"
            "s_mov_b32 exec_hi, -1\n\t"
            "v_mfma_f32_4x4x4_16b_bf16 v[196:199], v[186:187], v[186:187], v[196:199]\n\t"
            "s_mov_b32 exec_hi, 0x3fffff\n\t"
            "s_add_u32 m0, s66, 6976\n\t"
            "s_add_u32 s49, s68, 1569792\n\t"
            "buffer_load_dwordx4 v249, s[72:75], s49 offen lds\n\t"
            "s_mov_b32 exec_hi, -1\n\t"
            "v_mfma_f32_16x16x32_bf16 v[192:195], v[238:241], v[184:187], v[192:195]\n\t"
            "s_waitcnt vmcnt(9)\n\t"
            "ds_write_b128 v237, v[184:187] offset:256\n\t"
            "v_mfma_f32_16x16x32_bf16 v[144:147], v[0:3], v[200:203], 0\n\t"
            "v_add_u32 v237, s48, v245\n\t"
            "v_mfma_f32_16x16x32_bf16 v[144:147], v[4:7], v[204:207], v[144:147]\n\t"
            "v_mfma_f32_16x16x32_bf16 v[148:151], v[24:27], v[200:203], 0\n\t"
            "s_nop 1\n\t"
            "v_cndmask_b32_e64 v193, v196, v197, s[60:61]\n\t"
            "v_mfma_f32_16x16x32_bf16 v[148:151], v[28:31], v[204:207], v[148:151]\n\t"
            "v_cndmask_b32_e64 v193, v193, v198, s[62:63]\n\t"
            "v_cndmask_b32_e64 v193, v193, v199, s[64:65]\n\t"
            "v_mfma_f32_16x16x32_bf16 v[168:171], v[48:51], v[200:203], 0\n\t"
            "ds_read_b128 v[196:199], v232 offset:6272\n\t"
            "v_mfma_f32_16x16x32_bf16 v[168:171], v[52:55], v[204:207], v[168:171]\n\t"
            "ds_write_b64 v248, v[192:193] offset:18816\n\t"
            "v_mfma_f32_16x16x32_bf16 v[172:175], v[72:75], v[200:203], 0\n\t"
            "ds_read_b128 v[192:195], v235 offset:6144\n\t"
            "v_add_u32 v248, s48, v246\n\t"
            "v_mfma_f32_16x16x32_bf16 v[172:175], v[76:79], v[204:207], v[172:175]\n\t"
            "s_mov_b64 exec, 1\n\t"
            "ds_inc_u32 v255, v238 offset:7860\n\t"
            "s_mov_b64 exec, -1\n\t"
            "v_mfma_f32_16x16x32_bf16 v[152:155], v[96:99], v[200:203], 0\n\t"
            "v_mfma_f32_16x16x32_bf16 v[152:155], v[100:103], v[204:207], v[152:155]\n\t"
            "v_mfma_f32_16x16x32_bf16 v[156:159], v[120:123], v[200:203], 0\n\t"
            "ds_read_b128 v[200:203], v235 offset:6272\n\t"
            "v_mfma_f32_16x16x32_bf16 v[156:159], v[124:127], v[204:207], v[156:159]\n\t"
            "ds_read_b128 v[204:207], v232 offset:6400\n\t"
            "v_mfma_f32_16x16x32_bf16 v[144:147], v[8:11], v[208:211], v[144:147]\n\t"
            "v_mfma_f32_16x16x32_bf16 v[144:147], v[12:15], v[212:215], v[144:147]\n\t"
            "v_mfma_f32_16x16x32_bf16 v[148:151], v[32:35], v[208:211], v[148:151]\n\t"
            "v_mfma_f32_16x16x32_bf16 v[148:151], v[36:39], v[212:215], v[148:151]\n\t"
            "v_mfma_f32_16x16x32_bf16 v[168:171], v[56:59], v[208:211], v[168:171]\n\t"
            "v_mfma_f32_16x16x32_bf16 v[168:171], v[60:63], v[212:215], v[168:171]\n\t"
            "v_mfma_f32_16x16x32_bf16 v[172:175], v[80:83], v[208:211], v[172:175]\n\t"
            "v_mfma_f32_16x16x32_bf16 v[172:175], v[84:87], v[212:215], v[172:175]\n\t"
            "v_mfma_f32_16x16x32_bf16 v[152:155], v[104:107], v[208:211], v[152:155]\n\t"
            "v_mfma_f32_16x16x32_bf16 v[152:155], v[108:111], v[212:215], v[152:155]\n\t"
            "v_mfma_f32_16x16x32_bf16 v[156:159], v[128:131], v[208:211], v[156:159]\n\t"
            "ds_read_b128 v[208:211], v235 offset:6400\n\t"
            "v_mfma_f32_16x16x32_bf16 v[156:159], v[132:135], v[212:215], v[156:159]\n\t"
            "ds_read_b128 v[212:215], v236 offset:6144\n\t"
            "v_mfma_f32_16x16x32_bf16 v[144:147], v[16:19], v[216:219], v[144:147]\n\t"
            "v_mfma_f32_16x16x32_bf16 v[144:147], v[20:23], v[220:223], v[144:147]\n\t"
            "v_mfma_f32_16x16x32_bf16 v[148:151], v[40:43], v[216:219], v[148:151]\n\t"
            "v_mfma_f32_16x16x32_bf16 v[148:151], v[44:47], v[220:223], v[148:151]\n\t"
            "v_mfma_f32_16x16x32_bf16 v[168:171], v[64:67], v[216:219], v[168:171]\n\t"
            "v_mfma_f32_16x16x32_bf16 v[168:171], v[68:71], v[220:223], v[168:171]\n\t"
            "v_mfma_f32_16x16x32_bf16 v[172:175], v[88:91], v[216:219], v[172:175]\n\t"
            "v_mfma_f32_16x16x32_bf16 v[172:175], v[92:95], v[220:223], v[172:175]\n\t"
            "s_nop 1\n\t"
            "v_mul_f32 v144, v230, v144\n\t"
            "v_mfma_f32_16x16x32_bf16 v[152:155], v[112:115], v[216:219], v[152:155]\n\t"
            "v_mul_f32 v145, v230, v145\n\t"
            "v_mul_f32 v146, v230, v146\n\t"
            "v_mfma_f32_16x16x32_bf16 v[152:155], v[116:119], v[220:223], v[152:155]\n\t"
            "v_mul_f32 v148, v230, v148\n\t"
            "v_mul_f32 v149, v230, v149\n\t"
            "v_mfma_f32_16x16x32_bf16 v[156:159], v[136:139], v[216:219], v[156:159]\n\t"
            "ds_read_b128 v[216:219], v237 offset:6144\n\t"
            "v_mul_f32 v168, v230, v168\n\t"
            "v_mfma_f32_16x16x32_bf16 v[156:159], v[140:143], v[220:223], v[156:159]\n\t"
            "ds_read_b128 v[220:223], v248 offset:6144\n\t"
            "v_mul_f32 v169, v230, v169\n\t"
            "s_waitcnt lgkmcnt(7)\n\t"
            "v_mfma_f32_16x16x32_bf16 v[160:163], v[0:3], v[224:227], 0\n\t"
            "v_mul_f32 v172, v230, v172\n\t"
            "v_mul_f32 v173, v230, v173\n\t"
            "v_mfma_f32_16x16x32_bf16 v[160:163], v[4:7], v[192:195], v[160:163]\n\t"
            "v_mul_f32 v152, v230, v152\n\t"
            "v_mul_f32 v153, v230, v153\n\t"
            "v_mfma_f32_16x16x32_bf16 v[164:167], v[24:27], v[224:227], 0\n\t"
            "v_mul_f32 v156, v230, v156\n\t"
            "v_mul_f32 v157, v230, v157\n\t"
            "v_mfma_f32_16x16x32_bf16 v[164:167], v[28:31], v[192:195], v[164:167]\n\t"
            "v_mul_f32 v147, v230, v147\n\t"
            "v_cvt_pk_bf16_f32 v144, v144, v145\n\t"
            "v_mfma_f32_16x16x32_bf16 v[176:179], v[48:51], v[224:227], 0\n\t"
            "v_mul_f32 v150, v230, v150\n\t"
            "v_mul_f32 v151, v230, v151\n\t"
            "v_mfma_f32_16x16x32_bf16 v[176:179], v[52:55], v[192:195], v[176:179]\n\t"
            "v_cvt_pk_bf16_f32 v148, v148, v149\n\t"
            "v_mul_f32 v170, v230, v170\n\t"
            "v_mfma_f32_16x16x32_bf16 v[180:183], v[72:75], v[224:227], 0\n\t"
            "v_mul_f32 v171, v230, v171\n\t"
            "v_cvt_pk_bf16_f32 v168, v168, v169\n\t"
            "v_mfma_f32_16x16x32_bf16 v[180:183], v[76:79], v[192:195], v[180:183]\n\t"
            "v_mul_f32 v174, v230, v174\n\t"
            "v_mul_f32 v175, v230, v175\n\t"
            "v_mfma_f32_16x16x32_bf16 v[184:187], v[96:99], v[224:227], 0\n\t"
            "v_cvt_pk_bf16_f32 v172, v172, v173\n\t"
            "v_mul_f32 v154, v230, v154\n\t"
            "v_mfma_f32_16x16x32_bf16 v[184:187], v[100:103], v[192:195], v[184:187]\n\t"
            "v_mul_f32 v155, v230, v155\n\t"
            "v_cvt_pk_bf16_f32 v152, v152, v153\n\t"
            "v_mfma_f32_16x16x32_bf16 v[188:191], v[120:123], v[224:227], 0\n\t"
            "ds_read_b128 v[224:227], v236 offset:12288\n\t"
            "v_mul_f32 v158, v230, v158\n\t"
            "v_mfma_f32_16x16x32_bf16 v[188:191], v[124:127], v[192:195], v[188:191]\n\t"
            "v_mul_f32 v159, v230, v159\n\t"
            "v_cvt_pk_bf16_f32 v156, v156, v157\n\t"
            "s_waitcnt lgkmcnt(6)\n\t"
            "v_mfma_f32_16x16x32_bf16 v[160:163], v[8:11], v[196:199], v[160:163]\n\t"
            "v_cvt_pk_bf16_f32 v145, v146, v147\n\t"
            "v_cvt_pk_bf16_f32 v149, v150, v151\n\t"
            "v_mfma_f32_16x16x32_bf16 v[160:163], v[12:15], v[200:203], v[160:163]\n\t"
            "v_cvt_pk_bf16_f32 v169, v170, v171\n\t"
            "v_cvt_pk_bf16_f32 v173, v174, v175\n\t"
            "v_mfma_f32_16x16x32_bf16 v[164:167], v[32:35], v[196:199], v[164:167]\n\t"
            "v_cvt_pk_bf16_f32 v153, v154, v155\n\t"
            "v_cvt_pk_bf16_f32 v157, v158, v159\n\t"
            "v_mfma_f32_16x16x32_bf16 v[164:167], v[36:39], v[200:203], v[164:167]\n\t"
            "s_mov_b64 exec, -1\n\t"
            "ds_write_b64 v234, v[144:145]\n\t"
            "ds_write_b64 v234, v[148:149] offset:128\n\t"
            "ds_write_b64 v234, v[168:169] offset:256\n\t"
            "ds_write_b64 v228, v[172:173]\n\t"
            "ds_write_b64 v228, v[152:153] offset:128\n\t"
            "ds_write_b64 v228, v[156:157] offset:256\n\t"
            "s_mov_b64 exec, -1\n\t"
            "v_mfma_f32_16x16x32_bf16 v[176:179], v[56:59], v[196:199], v[176:179]\n\t"
            "v_mfma_f32_16x16x32_bf16 v[176:179], v[60:63], v[200:203], v[176:179]\n\t"
            "s_add_u32 s49, s33, 0\n\t"
            "buffer_load_dwordx4 v[0:3], v247, s[28:31], s49 offen sc1\n\t"
            "v_mfma_f32_16x16x32_bf16 v[180:183], v[80:83], v[196:199], v[180:183]\n\t"
            "v_mfma_f32_16x16x32_bf16 v[180:183], v[84:87], v[200:203], v[180:183]\n\t"
            "s_add_u32 s49, s33, 6144\n\t"
            "buffer_load_dwordx4 v[24:27], v247, s[28:31], s49 offen sc1\n\t"
            "v_mfma_f32_16x16x32_bf16 v[184:187], v[104:107], v[196:199], v[184:187]\n\t"
            "v_mfma_f32_16x16x32_bf16 v[184:187], v[108:111], v[200:203], v[184:187]\n\t"
            "s_add_u32 s49, s33, 12288\n\t"
            "buffer_load_dwordx4 v[48:51], v247, s[28:31], s49 offen sc1\n\t"
            "v_mfma_f32_16x16x32_bf16 v[188:191], v[128:131], v[196:199], v[188:191]\n\t"
            "v_mfma_f32_16x16x32_bf16 v[188:191], v[132:135], v[200:203], v[188:191]\n\t"
            "ds_read_b128 v[200:203], v236\n\t"
            "s_add_u32 s49, s33, 18432\n\t"
            "buffer_load_dwordx4 v[72:75], v247, s[28:31], s49 offen sc1\n\t"
            "s_waitcnt lgkmcnt(11)\n\t"
            "v_mfma_f32_16x16x32_bf16 v[160:163], v[16:19], v[204:207], v[160:163]\n\t"
            "v_mfma_f32_16x16x32_bf16 v[160:163], v[20:23], v[208:211], v[160:163]\n\t"
            "s_add_u32 s49, s33, 24576\n\t"
            "buffer_load_dwordx4 v[96:99], v247, s[28:31], s49 offen sc1\n\t"
            "v_mfma_f32_16x16x32_bf16 v[164:167], v[40:43], v[204:207], v[164:167]\n\t"
            "v_mfma_f32_16x16x32_bf16 v[164:167], v[44:47], v[208:211], v[164:167]\n\t"
            "s_add_u32 s49, s33, 30720\n\t"
            "buffer_load_dwordx4 v[120:123], v247, s[28:31], s49 offen sc1\n\t"
            "v_mfma_f32_16x16x32_bf16 v[176:179], v[64:67], v[204:207], v[176:179]\n\t"
            "s_nop 0\n\t"
            "v_mul_f32 v160, v231, v160\n\t"
            "v_mul_f32 v161, v231, v161\n\t"
            "v_mfma_f32_16x16x32_bf16 v[176:179], v[68:71], v[208:211], v[176:179]\n\t"
            "v_mul_f32 v162, v231, v162\n\t"
            "v_mul_f32 v163, v231, v163\n\t"
            "v_mfma_f32_16x16x32_bf16 v[180:183], v[88:91], v[204:207], v[180:183]\n\t"
            "v_mul_f32 v164, v231, v164\n\t"
            "v_mul_f32 v165, v231, v165\n\t"
            "v_mfma_f32_16x16x32_bf16 v[180:183], v[92:95], v[208:211], v[180:183]\n\t"
            "v_cvt_pk_bf16_f32 v160, v160, v161\n\t"
            "v_mul_f32 v166, v231, v166\n\t"
            "v_mfma_f32_16x16x32_bf16 v[184:187], v[112:115], v[204:207], v[184:187]\n\t"
            "v_mul_f32 v176, v231, v176\n\t"
            "v_mul_f32 v177, v231, v177\n\t"
            "v_mfma_f32_16x16x32_bf16 v[184:187], v[116:119], v[208:211], v[184:187]\n\t"
            "v_mul_f32 v167, v231, v167\n\t"
            "v_cvt_pk_bf16_f32 v164, v164, v165\n\t"
            "v_mfma_f32_16x16x32_bf16 v[188:191], v[136:139], v[204:207], v[188:191]\n\t"
            "ds_read_b128 v[204:207], v237\n\t"
            "v_mul_f32 v180, v231, v180\n\t"
            "v_mfma_f32_16x16x32_bf16 v[188:191], v[140:143], v[208:211], v[188:191]\n\t"
            "ds_read_b128 v[208:211], v248\n\t"
            "v_mul_f32 v181, v231, v181\n\t"
            "v_mul_f32 v184, v231, v184\n\t"
            "v_mul_f32 v185, v231, v185\n\t"
            "v_mul_f32 v178, v231, v178\n\t"
            "v_mul_f32 v179, v231, v179\n\t"
            "v_cvt_pk_bf16_f32 v176, v176, v177\n\t"
            "v_mul_f32 v182, v231, v182\n\t"
            "v_mul_f32 v188, v231, v188\n\t"
            "v_mul_f32 v189, v231, v189\n\t"
            "v_mul_f32 v183, v231, v183\n\t"
            "v_cvt_pk_bf16_f32 v180, v180, v181\n\t"
            "v_mul_f32 v186, v231, v186\n\t"
            "v_mul_f32 v187, v231, v187\n\t"
            "v_cvt_pk_bf16_f32 v184, v184, v185\n\t"
            "v_mul_f32 v190, v231, v190\n\t"
            "v_mul_f32 v191, v231, v191\n\t"
            "v_cvt_pk_bf16_f32 v188, v188, v189\n\t"
            "v_cvt_pk_bf16_f32 v161, v162, v163\n\t"
            "v_cvt_pk_bf16_f32 v165, v166, v167\n\t"
            "v_cvt_pk_bf16_f32 v177, v178, v179\n\t"
            "v_cvt_pk_bf16_f32 v181, v182, v183\n\t"
            "v_cvt_pk_bf16_f32 v185, v186, v187\n\t"
            "v_cvt_pk_bf16_f32 v189, v190, v191\n\t"
            "s_waitcnt lgkmcnt(9)\n\t"
            "s_mov_b64 exec, s[80:81]\n\t"
            "ds_write_b64 v234, v[160:161] offset:6144\n\t"
            "ds_write_b64 v234, v[164:165] offset:6272\n\t"
            "ds_write_b64 v234, v[176:177] offset:6400\n\t"
            "ds_write_b64 v228, v[180:181] offset:6144\n\t"
            "ds_write_b64 v228, v[184:185] offset:6272\n\t"
            "ds_write_b64 v228, v[188:189] offset:6400\n\t"
            "s_mov_b64 exec, -1\n\t"
            "s_waitcnt lgkmcnt(14)\n\t"
            "ds_read_b128 v[228:231], v237 offset:12288\n\t"
            "s_waitcnt lgkmcnt(14)\n\t"
            "ds_read_b128 v[232:235], v248 offset:12288\n\t"
            "s_add_u32 s49, s33, 1024\n\t"
            "buffer_load_dwordx4 v[4:7], v247, s[28:31], s49 offen sc1\n\t"
            "s_add_u32 s49, s33, 7168\n\t"
            "buffer_load_dwordx4 v[28:31], v247, s[28:31], s49 offen sc1\n\t"
            "s_waitcnt lgkmcnt(0)\n\t"
            "s_barrier\n\t"
            "s_setprio 3\n\t"
            "s_add_u32 s49, s33, 13312\n\t"
            "buffer_load_dwordx4 v[52:55], v247, s[28:31], s49 offen sc1\n\t"
            "s_add_u32 s49, s33, 19456\n\t"
            "buffer_load_dwordx4 v[76:79], v247, s[28:31], s49 offen sc1\n\t"
            "s_add_u32 s49, s33, 25600\n\t"
            "buffer_load_dwordx4 v[100:103], v247, s[28:31], s49 offen sc1\n\t"
            "s_add_u32 s49, s33, 31744\n\t"
            "buffer_load_dwordx4 v[124:127], v247, s[28:31], s49 offen sc1\n\t"
            "s_add_u32 s49, s33, 2048\n\t"
            "buffer_load_dwordx4 v[8:11], v247, s[28:31], s49 offen sc1\n\t"
            "s_add_u32 s49, s33, 8192\n\t"
            "buffer_load_dwordx4 v[32:35], v247, s[28:31], s49 offen sc1\n\t"
            "s_add_u32 s49, s33, 14336\n\t"
            "buffer_load_dwordx4 v[56:59], v247, s[28:31], s49 offen sc1\n\t"
            "s_add_u32 s49, s33, 20480\n\t"
            "buffer_load_dwordx4 v[80:83], v247, s[28:31], s49 offen sc1\n\t"
            "s_add_u32 s49, s33, 26624\n\t"
            "buffer_load_dwordx4 v[104:107], v247, s[28:31], s49 offen sc1\n\t"
            "s_add_u32 s49, s33, 32768\n\t"
            "buffer_load_dwordx4 v[128:131], v247, s[28:31], s49 offen sc1\n\t"
            "s_add_u32 s49, s33, 3072\n\t"
            "buffer_load_dwordx4 v[12:15], v247, s[28:31], s49 offen sc1\n\t"
            "s_add_u32 s49, s33, 9216\n\t"
            "buffer_load_dwordx4 v[36:39], v247, s[28:31], s49 offen sc1\n\t"
            "s_add_u32 s49, s33, 15360\n\t"
            "buffer_load_dwordx4 v[60:63], v247, s[28:31], s49 offen sc1\n\t"
            "s_add_u32 s49, s33, 21504\n\t"
            "buffer_load_dwordx4 v[84:87], v247, s[28:31], s49 offen sc1\n\t"
            "s_add_u32 s49, s33, 27648\n\t"
            "buffer_load_dwordx4 v[108:111], v247, s[28:31], s49 offen sc1\n\t"
            "s_add_u32 s49, s33, 33792\n\t"
            "buffer_load_dwordx4 v[132:135], v247, s[28:31], s49 offen sc1\n\t"
            "s_add_u32 s49, s33, 4096\n\t"
            "buffer_load_dwordx4 v[16:19], v247, s[28:31], s49 offen sc1\n\t"
            "s_add_u32 s49, s33, 10240\n\t"
            "buffer_load_dwordx4 v[40:43], v247, s[28:31], s49 offen sc1\n\t"
            "s_add_u32 s49, s33, 16384\n\t"
            "buffer_load_dwordx4 v[64:67], v247, s[28:31], s49 offen sc1\n\t"
            "s_add_u32 s49, s33, 22528\n\t"
            "buffer_load_dwordx4 v[88:91], v247, s[28:31], s49 offen sc1\n\t"
            "s_add_u32 s49, s33, 28672\n\t"
            "buffer_load_dwordx4 v[112:115], v247, s[28:31], s49 offen sc1\n\t"
            "s_add_u32 s49, s33, 34816\n\t"
            "buffer_load_dwordx4 v[136:139], v247, s[28:31], s49 offen sc1\n\t"
            "s_add_u32 s49, s33, 5120\n\t"
            "buffer_load_dwordx4 v[20:23], v247, s[28:31], s49 offen sc1\n\t"
            "s_add_u32 s49, s33, 11264\n\t"
            "buffer_load_dwordx4 v[44:47], v247, s[28:31], s49 offen sc1\n\t"
            "s_add_u32 s49, s33, 17408\n\t"
            "buffer_load_dwordx4 v[68:71], v247, s[28:31], s49 offen sc1\n\t"
            "s_add_u32 s49, s33, 23552\n\t"
            "buffer_load_dwordx4 v[92:95], v247, s[28:31], s49 offen sc1\n\t"
            "s_add_u32 s49, s33, 29696\n\t"
            "buffer_load_dwordx4 v[116:119], v247, s[28:31], s49 offen sc1\n\t"
            "s_add_u32 s49, s33, 35840\n\t"
            "buffer_load_dwordx4 v[140:143], v247, s[28:31], s49 offen sc1\n\t"
            "s_add_u32 s59, s59, s84\n\t"
            "s_movk_i32 s84, 800\n\t"
            "s_add_u32 s45, s46, s27\n\t"
            "s_mov_b32 s46, s47\n\t"
            "s_mov_b32 s47, s68\n\t"
            "s_xor_b32 s48, s48, s71\n\t"
            "s_xor_b32 s67, s67, s71\n\t"
            "s_add_u32 s76, s76, 4\n\t"
            "s_add_u32 s70, s70, 4\n\t"
            "s_lshl_b32 s56, s56, 2\n\t"
            "s_lshr_b32 s49, s26, 30\n\t"
            "s_or_b32 s56, s56, s49\n\t"
            "s_waitcnt vmcnt(0)\n\t"
            "s_nop 7\n\t"
            ".p2align 6\n\t"
            "s_nop 0\n\t"
            "s_nop 0\n\t"
            "s_nop 0\n\t"
            "s_nop 0\n\t"
            "s_nop 0\n\t"
            "s_nop 0\n\t"
            "s_nop 0\n\t"
            ".Lwb_loop%=:\n\t"
            "s_setprio 1\n\t"
            "ds_read_b128 v[168:171], v254\n\t"
            "ds_read_b128 v[172:175], v254 offset:64\n\t"
            "s_waitcnt vmcnt(9)\n\t"
            "ds_read_b64 v[144:145], v253\n\t"
            "ds_read_b64 v[146:147], v253 offset:96\n\t"
            "ds_read_b64 v[148:149], v253 offset:1744\n\t"
            "ds_read_b64 v[150:151], v253 offset:1840\n\t"
            "ds_read_b64 v[152:153], v253 offset:8\n\t"
            "ds_read_b64 v[154:155], v253 offset:104\n\t"
            "ds_read_b64 v[156:157], v253 offset:1752\n\t"
            "ds_read_b64 v[158:159], v253 offset:1848\n\t"
            "ds_read_b64 v[160:161], v253 offset:16\n\t"
            "ds_read_b64 v[162:163], v253 offset:112\n\t"
            "ds_read_b64 v[164:165], v253 offset:1760\n\t"
            "ds_read_b64 v[166:167], v253 offset:1856\n\t"
            "v_mov_b64 v[176:177], 0\n\t"
            "v_mov_b64 v[178:179], 0\n\t"
            "v_mov_b64 v[180:181], 0\n\t"
            "v_mov_b64 v[182:183], 0\n\t"
            "v_mov_b32 v249, s76\n\t"
            "v_mov_b64 v[184:185], 0\n\t"
            "v_mov_b64 v[186:187], 0\n\t"
            "v_mov_b64 v[188:189], 0\n\t"
            "v_mov_b64 v[190:191], 0\n\t"
            "v_add_u32 v236, s67, v250\n\t"
            "v_add_u32 v248, s67, v251\n\t"
            "v_mov_b32 v247, 0\n\t"
            "ds_read_b32 v249, v249\n\t"
            "s_setprio 3\n\t"
            "s_waitcnt lgkmcnt(9)\n\t"
            "v_smfmac_f32_16x16x64_bf16 v[176:179], v[168:171], v[144:151], v255\n\t"
            "ds_read_b128 v[168:171], v254 offset:128\n\t"
            "s_waitcnt lgkmcnt(6)\n\t"
            "v_smfmac_f32_16x16x64_bf16 v[180:183], v[172:175], v[152:159], v255\n\t"
            "s_setprio 1\n\t"
            "ds_read_b64 v[144:145], v253 offset:24\n\t"
            "ds_read_b64 v[146:147], v253 offset:120\n\t"
            "ds_read_b64 v[148:149], v253 offset:1768\n\t"
            "ds_read_b64 v[150:151], v253 offset:1864\n\t"
            "ds_read_b128 v[172:175], v254 offset:192\n\t"
            "ds_read_b64 v[152:153], v253 offset:32\n\t"
            "ds_read_b64 v[154:155], v253 offset:128\n\t"
            "ds_read_b64 v[156:157], v253 offset:1776\n\t"
            "ds_read_b64 v[158:159], v253 offset:1872\n\t"
            "v_cvt_pk_bf16_f32 v176, v176, v177\n\t"
            "v_cvt_pk_bf16_f32 v177, v178, v179\n\t"
            "v_cvt_pk_bf16_f32 v178, v180, v181\n\t"
            "v_cvt_pk_bf16_f32 v179, v182, v183\n\t"
            "s_setprio 3\n\t"
            "v_mfma_f32_4x4x4_16b_bf16 v[196:199], v[176:177], v[176:177], 0\n\t"
            "v_mov_b64 v[180:181], 0\n\t"
            "v_mfma_f32_16x16x32_bf16 v[192:195], v[238:241], v[176:179], 0\n\t"
            "s_waitcnt lgkmcnt(10)\n\t"
            "v_readfirstlane_b32 s26, v249\n\t"
            "v_mov_b64 v[182:183], 0\n\t"
            "v_mfma_f32_4x4x4_16b_bf16 v[196:199], v[178:179], v[178:179], v[196:199]\n\t"
            "ds_read_b32 v249, v252 offset:12\n\t"
            "s_and_b32 s68, s26, 0x7ffffff\n\t"
            "s_add_u32 s68, s68, s78\n\t"
            "s_and_b32 s69, s26, 0x38000000\n\t"
            "s_or_b32 s69, s69, s39\n\t"
            "s_bitcmp1_b32 s26, 27\n\t"
            "s_cselect_b32 s79, 2.0, s68\n\t"
            "s_and_b32 s57, s56, 3\n\t"
            "s_lshl_b32 s58, s57, 3\n\t"
            "s_lshr_b32 m0, s54, s58\n\t"
            "s_lshl_b32 s58, s57, 4\n\t"
            "s_lshr_b64 s[36:37], s[50:51], s58\n\t"
            "s_and_b32 s57, s36, 0xffff\n\t"
            "s_add_u32 s57, s57, s67\n\t"
            "v_writelane_b32 v236, s57, 63\n\t"
            "v_writelane_b32 v236, s59, m0\n\t"
            "v_xor_b32 v237, 16, v236\n\t"
            "s_lshr_b64 s[36:37], s[52:53], s58\n\t"
            "s_and_b32 s58, s36, 0xffff\n\t"
            "s_add_u32 s58, s58, s67\n\t"
            "ds_write_b128 v236, v[176:179]\n\t"
            "s_waitcnt lgkmcnt(11)\n\t"
            "v_smfmac_f32_16x16x64_bf16 v[184:187], v[168:171], v[160:167], v255\n\t"
            "s_and_b32 s38, s52, 0xffff\n\t"
            "s_add_u32 s38, s38, s67\n\t"
            "ds_read_b128 v[168:171], v254 offset:256\n\t"
            "s_waitcnt lgkmcnt(7)\n\t"
            "v_smfmac_f32_16x16x64_bf16 v[188:191], v[172:175], v[144:151], v255\n\t"
            "s_setprio 1\n\t"
            "ds_read_b64 v[160:161], v253 offset:40\n\t"
            "ds_read_b64 v[162:163], v253 offset:136\n\t"
            "ds_read_b64 v[164:165], v253 offset:1784\n\t"
            "ds_read_b64 v[166:167], v253 offset:1880\n\t"
            "ds_read_b128 v[172:175], v254 offset:320\n\t"
            "ds_read_b64 v[144:145], v253 offset:48\n\t"
            "ds_read_b64 v[146:147], v253 offset:144\n\t"
            "ds_read_b64 v[148:149], v253 offset:1792\n\t"
            "s_waitcnt lgkmcnt(14)\n\t"
            "ds_read_b64 v[150:151], v253 offset:1888\n\t"
            "v_mov_b64 v[176:177], 0\n\t"
            "v_mov_b64 v[178:179], 0\n\t"
            "v_cvt_pk_bf16_f32 v184, v184, v185\n\t"
            "v_cvt_pk_bf16_f32 v185, v186, v187\n\t"
            "v_cvt_pk_bf16_f32 v186, v188, v189\n\t"
            "v_cvt_pk_bf16_f32 v187, v190, v191\n\t"
            "s_setprio 3\n\t"
            "v_mfma_f32_4x4x4_16b_bf16 v[196:199], v[184:185], v[184:185], v[196:199]\n\t"
            "s_waitcnt lgkmcnt(14)\n\t"
            "ds_write_b128 v237, v[184:187]\n\t"
            "v_mfma_f32_16x16x32_bf16 v[192:195], v[238:241], v[184:187], v[192:195]\n\t"
            "v_mov_b64 v[184:185], 0\n\t"
            "v_mov_b64 v[188:189], 0\n\t"
            "v_mfma_f32_4x4x4_16b_bf16 v[196:199], v[186:187], v[186:187], v[196:199]\n\t"
            "v_mov_b64 v[186:187], 0\n\t"
            "v_mov_b64 v[190:191], 0\n\t"
            "v_writelane_b32 v248, s58, 63\n\t"
            "v_writelane_b32 v248, s38, m0\n\t"
            "s_waitcnt lgkmcnt(12)\n\t"
            "v_and_b32 v249, s69, v249\n\t"
            "s_waitcnt lgkmcnt(10)\n\t"
            "v_smfmac_f32_16x16x64_bf16 v[176:179], v[168:171], v[152:159], v255\n\t"
            "ds_read_b128 v[168:171], v254 offset:384\n\t"
            "ds_read_b64 v[152:153], v253 offset:56\n\t"
            "ds_read_b64 v[154:155], v253 offset:152\n\t"
            "ds_read_b64 v[156:157], v253 offset:1800\n\t"
            "ds_read_b64 v[158:159], v253 offset:1896\n\t"
            "s_waitcnt lgkmcnt(10)\n\t"
            "v_smfmac_f32_16x16x64_bf16 v[180:183], v[172:175], v[160:167], v255\n\t"
            "s_setprio 1\n\t"
            "ds_read_b128 v[172:175], v254 offset:448\n\t"
            "ds_read_b64 v[160:161], v253 offset:64\n\t"
            "ds_read_b64 v[162:163], v253 offset:160\n\t"
            "ds_read_b64 v[164:165], v253 offset:1808\n\t"
            "ds_read_b64 v[166:167], v253 offset:1904\n\t"
            "v_cvt_pk_bf16_f32 v176, v176, v177\n\t"
            "v_cvt_pk_bf16_f32 v177, v178, v179\n\t"
            "v_cvt_pk_bf16_f32 v178, v180, v181\n\t"
            "v_cvt_pk_bf16_f32 v179, v182, v183\n\t"
            "s_setprio 3\n\t"
            "v_mfma_f32_4x4x4_16b_bf16 v[196:199], v[176:177], v[176:177], v[196:199]\n\t"
            "s_waitcnt lgkmcnt(14)\n\t"
            "ds_write_b128 v236, v[176:179] offset:128\n\t"
            "v_mfma_f32_16x16x32_bf16 v[192:195], v[238:241], v[176:179], v[192:195]\n\t"
            "v_mov_b64 v[176:177], 0\n\t"
            "v_mov_b64 v[180:181], 0\n\t"
            "v_mfma_f32_4x4x4_16b_bf16 v[196:199], v[178:179], v[178:179], v[196:199]\n\t"
            "v_mov_b64 v[178:179], 0\n\t"
            "v_mov_b64 v[182:183], 0\n\t"
            "s_waitcnt lgkmcnt(10)\n\t"
            "v_smfmac_f32_16x16x64_bf16 v[184:187], v[168:171], v[144:151], v255\n\t"
            "ds_read_b128 v[168:171], v254 offset:512\n\t"
            "ds_read_b64 v[144:145], v253 offset:72\n\t"
            "ds_read_b64 v[146:147], v253 offset:168\n\t"
            "ds_read_b64 v[148:149], v253 offset:1816\n\t"
            "ds_read_b64 v[150:151], v253 offset:1912\n\t"
            "s_waitcnt lgkmcnt(10)\n\t"
            "v_smfmac_f32_16x16x64_bf16 v[188:191], v[172:175], v[152:159], v255\n\t"
            "s_setprio 1\n\t"
            "ds_read_b128 v[172:175], v254 offset:576\n\t"
            "ds_read_b64 v[152:153], v253 offset:80\n\t"
            "ds_read_b64 v[154:155], v253 offset:176\n\t"
            "ds_read_b64 v[156:157], v253 offset:1824\n\t"
            "ds_read_b64 v[158:159], v253 offset:1920\n\t"
            "v_cvt_pk_bf16_f32 v184, v184, v185\n\t"
            "v_cvt_pk_bf16_f32 v185, v186, v187\n\t"
            "v_cvt_pk_bf16_f32 v186, v188, v189\n\t"
            "v_cvt_pk_bf16_f32 v187, v190, v191\n\t"
            "s_setprio 3\n\t"
            "v_mfma_f32_4x4x4_16b_bf16 v[196:199], v[184:185], v[184:185], v[196:199]\n\t"
            "s_waitcnt lgkmcnt(14)\n\t"
            "ds_write_b128 v237, v[184:187] offset:128\n\t"
            "v_mfma_f32_16x16x32_bf16 v[192:195], v[238:241], v[184:187], v[192:195]\n\t"
            "v_mov_b64 v[184:185], 0\n\t"
            "v_mov_b64 v[188:189], 0\n\t"
            "v_mfma_f32_4x4x4_16b_bf16 v[196:199], v[186:187], v[186:187], v[196:199]\n\t"
            "v_mov_b64 v[186:187], 0\n\t"
            "v_mov_b64 v[190:191], 0\n\t"
            "s_waitcnt lgkmcnt(10)\n\t"
            "v_smfmac_f32_16x16x64_bf16 v[176:179], v[168:171], v[160:167], v255\n\t"
            "ds_read_b128 v[168:171], v254 offset:640\n\t"
            "ds_read_b64 v[160:161], v253 offset:88\n\t"
            "ds_read_b64 v[162:163], v253 offset:184\n\t"
            "ds_read_b64 v[164:165], v253 offset:1832\n\t"
            "ds_read_b64 v[166:167], v253 offset:1928\n\t"
            "s_waitcnt lgkmcnt(10)\n\t"
            "v_smfmac_f32_16x16x64_bf16 v[180:183], v[172:175], v[144:151], v255\n\t"
            "ds_read_b128 v[172:175], v254 offset:704\n\t"
            "ds_read_b32 v247, v247 offset:7856\n\t"
            "v_cvt_pk_bf16_f32 v176, v176, v177\n\t"
            "v_cvt_pk_bf16_f32 v177, v178, v179\n\t"
            "v_lshlrev_b32 v144, 16, v212\n\t"
            "v_and_b32 v145, 0xffff0000, v212\n\t"
            "v_mfma_f32_4x4x4_16b_bf16 v[196:199], v[176:177], v[176:177], v[196:199]\n\t"
            "v_lshlrev_b32 v146, 16, v213\n\t"
            "v_cvt_pk_bf16_f32 v178, v180, v181\n\t"
            "v_cvt_pk_bf16_f32 v179, v182, v183\n\t"
            "v_lshlrev_b32 v180, 16, v210\n\t"
            "v_and_b32 v181, 0xffff0000, v210\n\t"
            "v_mfma_f32_4x4x4_16b_bf16 v[196:199], v[178:179], v[178:179], v[196:199]\n\t"
            "ds_write_b128 v236, v[176:179] offset:256\n\t"
            "v_mfma_f32_16x16x32_bf16 v[192:195], v[238:241], v[176:179], v[192:195]\n\t"
            "s_setprio 1\n\t"
            "v_lshlrev_b32 v176, 16, v208\n\t"
            "v_and_b32 v177, 0xffff0000, v208\n\t"
            "v_lshlrev_b32 v178, 16, v209\n\t"
            "v_and_b32 v179, 0xffff0000, v209\n\t"
            "v_lshlrev_b32 v182, 16, v211\n\t"
            "v_and_b32 v183, 0xffff0000, v211\n\t"
            "v_and_b32 v147, 0xffff0000, v213\n\t"
            "v_lshlrev_b32 v148, 16, v214\n\t"
            "v_and_b32 v149, 0xffff0000, v214\n\t"
            "v_lshlrev_b32 v150, 16, v215\n\t"
            "v_and_b32 v151, 0xffff0000, v215\n\t"
            "v_add_u32 v236, s48, v244\n\t"
            "ds_read_b128 v[212:215], v236 offset:6144\n\t"
            "s_setprio 3\n\t"
            "s_waitcnt lgkmcnt(8)\n\t"
            "v_smfmac_f32_16x16x64_bf16 v[184:187], v[168:171], v[152:159], v255\n\t"
            "v_lshlrev_b32 v152, 16, v200\n\t"
            "v_and_b32 v153, 0xffff0000, v200\n\t"
            "v_lshlrev_b32 v154, 16, v201\n\t"
            "v_and_b32 v155, 0xffff0000, v201\n\t"
            "v_lshlrev_b32 v156, 16, v202\n\t"
            "s_waitcnt lgkmcnt(3)\n\t"
            "v_smfmac_f32_16x16x64_bf16 v[188:191], v[172:175], v[160:167], v255\n\t"
            "s_setprio 1\n\t"
            "s_mov_b32 exec_hi, 0x3fffff\n\t"
            "s_add_u32 m0, s66, 0\n\t"
            "s_add_u32 s49, s79, 0\n\t"
            "buffer_load_dwordx4 v249, s[72:75], s49 offen lds\n\t"
            "s_mov_b32 exec_hi, -1\n\t"
            "s_mov_b32 exec_hi, 0x3fffff\n\t"
            "s_add_u32 m0, s66, 872\n\t"
            "s_add_u32 s49, s68, 196224\n\t"
            "buffer_load_dwordx4 v249, s[72:75], s49 offen lds\n\t"
            "s_mov_b32 exec_hi, -1\n\t"
            "s_mov_b32 exec_hi, 0x3fffff\n\t"
            "s_add_u32 m0, s66, 1744\n\t"
            "s_add_u32 s49, s68, 392448\n\t"
            "buffer_load_dwordx4 v249, s[72:75], s49 offen lds\n\t"
            "s_mov_b32 exec_hi, -1\n\t"
            "s_mov_b32 exec_hi, 0x3fffff\n\t"
            "s_add_u32 m0, s66, 2616\n\t"
            "s_add_u32 s49, s68, 588672\n\t"
            "buffer_load_dwordx4 v249, s[72:75], s49 offen lds\n\t"
            "s_mov_b32 exec_hi, -1\n\t"
            "s_mov_b32 exec_hi, 0x3fffff\n\t"
            "s_add_u32 m0, s66, 3488\n\t"
            "s_add_u32 s49, s68, 784896\n\t"
            "buffer_load_dwordx4 v249, s[72:75], s49 offen lds\n\t"
            "s_mov_b32 exec_hi, -1\n\t"
            "s_mov_b32 exec_hi, 0x3fffff\n\t"
            "s_add_u32 m0, s66, 4360\n\t"
            "s_add_u32 s49, s68, 981120\n\t"
            "buffer_load_dwordx4 v249, s[72:75], s49 offen lds\n\t"
            "s_mov_b32 exec_hi, -1\n\t"
            "s_mov_b32 exec_hi, 0x3fffff\n\t"
            "s_add_u32 m0, s66, 5232\n\t"
            "s_add_u32 s49, s68, 1177344\n\t"
            "buffer_load_dwordx4 v249, s[72:75], s49 offen lds\n\t"
            "s_mov_b32 exec_hi, -1\n\t"
            "s_mov_b32 exec_hi, 0x3fffff\n\t"
            "s_add_u32 m0, s66, 6104\n\t"
            "s_add_u32 s49, s68, 1373568\n\t"
            "buffer_load_dwordx4 v249, s[72:75], s49 offen lds\n\t"
            "s_mov_b32 exec_hi, -1\n\t"
            "s_mov_b32 exec_hi, 0x3fffff\n\t"
            "s_add_u32 m0, s66, 6976\n\t"
            "s_add_u32 s49, s68, 1569792\n\t"
            "buffer_load_dwordx4 v249, s[72:75], s49 offen lds\n\t"
            "s_mov_b32 exec_hi, -1\n\t"
            "v_cvt_pk_bf16_f32 v184, v184, v185\n\t"
            "v_cvt_pk_bf16_f32 v185, v186, v187\n\t"
            "v_cvt_pk_bf16_f32 v186, v188, v189\n\t"
            "v_cvt_pk_bf16_f32 v187, v190, v191\n\t"
            "s_setprio 3\n\t"
            "v_mfma_f32_4x4x4_16b_bf16 v[196:199], v[184:185], v[184:185], v[196:199]\n\t"
            "ds_write_b128 v237, v[184:187] offset:256\n\t"
            "v_mfma_f32_16x16x32_bf16 v[192:195], v[238:241], v[184:187], v[192:195]\n\t"
            "s_waitcnt lgkmcnt(3)\n\t"
            "v_readfirstlane_b32 s77, v247\n\t"
            "s_cmp_ge_u32 s77, s70\n\t"
            "s_cbranch_scc0 .Lwb_spin%=\n\t"
            ".Lwb_go%=:\n\t"
            "v_mfma_f32_4x4x4_16b_bf16 v[196:199], v[186:187], v[186:187], v[196:199]\n\t"
            "s_setprio 1\n\t"
            "ds_read_b128 v[184:187], v242\n\t"
            "ds_read_b128 v[188:191], v243\n\t"
            "ds_read_b128 v[172:175], v243 offset:128\n\t"
            "v_and_b32 v157, 0xffff0000, v202\n\t"
            "v_lshlrev_b32 v158, 16, v203\n\t"
            "v_and_b32 v159, 0xffff0000, v203\n\t"
            "v_lshlrev_b32 v160, 16, v204\n\t"
            "v_and_b32 v161, 0xffff0000, v204\n\t"
            "v_lshlrev_b32 v162, 16, v205\n\t"
            "v_cndmask_b32_e64 v193, v196, v197, s[60:61]\n\t"
            "v_cndmask_b32_e64 v193, v193, v198, s[62:63]\n\t"
            "v_cndmask_b32_e64 v193, v193, v199, s[64:65]\n\t"
            "v_and_b32 v163, 0xffff0000, v205\n\t"
            "v_lshlrev_b32 v164, 16, v206\n\t"
            "v_and_b32 v165, 0xffff0000, v206\n\t"
            "v_lshlrev_b32 v166, 16, v207\n\t"
            "v_and_b32 v167, 0xffff0000, v207\n\t"
            "v_lshlrev_b32 v168, 16, v216\n\t"
            "v_and_b32 v169, 0xffff0000, v216\n\t"
            "v_lshlrev_b32 v170, 16, v217\n\t"
            "v_and_b32 v171, 0xffff0000, v217\n\t"
            "s_bfe_u32 s57, s56, 0x20004\n\t"
            "s_lshl_b32 s58, s57, 3\n\t"
            "s_lshr_b32 s58, s55, s58\n\t"
            "s_lshl_b64 s[36:37], s[34:35], s58\n\t"
            "ds_read_b128 v[196:199], v252\n\t"
            "ds_read_b128 v[200:203], v236\n\t"
            "ds_write_b64 v248, v[192:193] offset:18816\n\t"
            "ds_read_b128 v[192:195], v242 offset:128\n\t"
            "s_bitcmp1_b32 s57, 1\n\t"
            "s_cselect_b32 s58, 1177344, 0\n\t"
            "s_bitcmp1_b32 s57, 0\n\t"
            "s_cselect_b32 s57, 2304, 0\n\t"
            "s_add_u32 s57, s57, s58\n\t"
            "s_mov_b64 exec, 1\n\t"
            "ds_inc_u32 v255, v238 offset:7860\n\t"
            "s_mov_b64 exec, -1\n\t"
            "v_add_u32 v237, s48, v245\n\t"
            "v_add_u32 v248, s48, v246\n\t"
            "s_add_u32 s59, s59, s84\n\t"
            "s_movk_i32 s84, 800\n\t"
            "ds_read_b128 v[204:207], v237\n\t"
            "s_setprio 3\n\t"
            "s_waitcnt lgkmcnt(7)\n\t"
            "v_mfma_f32_16x16x32_bf16 v[152:155], v[0:3], v[184:187], v[152:155]\n\t"
            "s_setprio 1\n\t"
            "s_setprio 1\n\t"
            "ds_read_b128 v[208:211], v248\n\t"
            "v_mfma_f32_16x16x32_bf16 v[152:155], v[4:7], v[188:191], v[152:155]\n\t"
            "v_mfma_f32_16x16x32_bf16 v[156:159], v[28:31], v[188:191], v[156:159]\n\t"
            "v_mfma_f32_16x16x32_bf16 v[156:159], v[24:27], v[184:187], v[156:159]\n\t"
            "v_mfma_f32_16x16x32_bf16 v[160:163], v[48:51], v[184:187], v[160:163]\n\t"
            "v_mfma_f32_16x16x32_bf16 v[160:163], v[52:55], v[188:191], v[160:163]\n\t"
            "v_mfma_f32_16x16x32_bf16 v[164:167], v[76:79], v[188:191], v[164:167]\n\t"
            "s_waitcnt lgkmcnt(6)\n\t"
            "s_mov_b64 exec, s[36:37]\n\t"
            "v_add_u32 v196, s57, v196\n\t"
            "s_mov_b64 exec, -1\n\t"
            "v_mfma_f32_16x16x32_bf16 v[164:167], v[72:75], v[184:187], v[164:167]\n\t"
            "v_mfma_f32_16x16x32_bf16 v[176:179], v[96:99], v[184:187], v[176:179]\n\t"
            "v_mfma_f32_16x16x32_bf16 v[176:179], v[100:103], v[188:191], v[176:179]\n\t"
            "v_mfma_f32_16x16x32_bf16 v[180:183], v[124:127], v[188:191], v[180:183]\n\t"
            "ds_read_b128 v[188:191], v243 offset:256\n\t"
            "v_mfma_f32_16x16x32_bf16 v[180:183], v[120:123], v[184:187], v[180:183]\n\t"
            "ds_read_b128 v[184:187], v242 offset:256\n\t"
            "s_waitcnt lgkmcnt(5)\n\t"
            "v_mfma_f32_16x16x32_bf16 v[152:155], v[8:11], v[192:195], v[152:155]\n\t"
            "v_mfma_f32_16x16x32_bf16 v[152:155], v[12:15], v[172:175], v[152:155]\n\t"
            "v_mfma_f32_16x16x32_bf16 v[156:159], v[36:39], v[172:175], v[156:159]\n\t"
            "v_mfma_f32_16x16x32_bf16 v[156:159], v[32:35], v[192:195], v[156:159]\n\t"
            "v_mfma_f32_16x16x32_bf16 v[160:163], v[56:59], v[192:195], v[160:163]\n\t"
            "v_mfma_f32_16x16x32_bf16 v[160:163], v[60:63], v[172:175], v[160:163]\n\t"
            "v_mfma_f32_16x16x32_bf16 v[164:167], v[84:87], v[172:175], v[164:167]\n\t"
            "v_mfma_f32_16x16x32_bf16 v[164:167], v[80:83], v[192:195], v[164:167]\n\t"
            "v_mfma_f32_16x16x32_bf16 v[176:179], v[104:107], v[192:195], v[176:179]\n\t"
            "v_mfma_f32_16x16x32_bf16 v[176:179], v[108:111], v[172:175], v[176:179]\n\t"
            "v_mfma_f32_16x16x32_bf16 v[180:183], v[132:135], v[172:175], v[180:183]\n\t"
            "ds_read_b128 v[172:175], v243 offset:6144\n\t"
            "v_mfma_f32_16x16x32_bf16 v[180:183], v[128:131], v[192:195], v[180:183]\n\t"
            "ds_read_b128 v[192:195], v242 offset:6144\n\t"
            "s_waitcnt lgkmcnt(2)\n\t"
            "v_mfma_f32_16x16x32_bf16 v[152:155], v[16:19], v[184:187], v[152:155]\n\t"
            "v_mfma_f32_16x16x32_bf16 v[152:155], v[20:23], v[188:191], v[152:155]\n\t"
            "v_mfma_f32_16x16x32_bf16 v[156:159], v[44:47], v[188:191], v[156:159]\n\t"
            "v_mfma_f32_16x16x32_bf16 v[156:159], v[40:43], v[184:187], v[156:159]\n\t"
            "v_mfma_f32_16x16x32_bf16 v[160:163], v[64:67], v[184:187], v[160:163]\n\t"
            "v_mfma_f32_16x16x32_bf16 v[160:163], v[68:71], v[188:191], v[160:163]\n\t"
            "v_mfma_f32_16x16x32_bf16 v[164:167], v[92:95], v[188:191], v[164:167]\n\t"
            "v_mfma_f32_16x16x32_bf16 v[164:167], v[88:91], v[184:187], v[164:167]\n\t"
            "s_nop 1\n\t"
            "v_cvt_pk_bf16_f32 v152, v152, v153\n\t"
            "v_mfma_f32_16x16x32_bf16 v[176:179], v[112:115], v[184:187], v[176:179]\n\t"
            "v_cvt_pk_bf16_f32 v153, v154, v155\n\t"
            "v_mfma_f32_16x16x32_bf16 v[176:179], v[116:119], v[188:191], v[176:179]\n\t"
            "v_cvt_pk_bf16_f32 v154, v156, v157\n\t"
            "v_cvt_pk_bf16_f32 v155, v158, v159\n\t"
            "v_mfma_f32_16x16x32_bf16 v[180:183], v[140:143], v[188:191], v[180:183]\n\t"
            "ds_read_b128 v[188:191], v243 offset:6272\n\t"
            "v_cvt_pk_bf16_f32 v160, v160, v161\n\t"
            "v_mfma_f32_16x16x32_bf16 v[180:183], v[136:139], v[184:187], v[180:183]\n\t"
            "ds_read_b128 v[184:187], v242 offset:6272\n\t"
            "v_cvt_pk_bf16_f32 v161, v162, v163\n\t"
            "s_waitcnt lgkmcnt(2)\n\t"
            "v_mfma_f32_16x16x32_bf16 v[144:147], v[0:3], v[192:195], v[144:147]\n\t"
            "v_cvt_pk_bf16_f32 v176, v176, v177\n\t"
            "v_cvt_pk_bf16_f32 v177, v178, v179\n\t"
            "v_mfma_f32_16x16x32_bf16 v[144:147], v[4:7], v[172:175], v[144:147]\n\t"
            "v_cvt_pk_bf16_f32 v162, v164, v165\n\t"
            "v_cvt_pk_bf16_f32 v163, v166, v167\n\t"
            "v_mfma_f32_16x16x32_bf16 v[148:151], v[28:31], v[172:175], v[148:151]\n\t"
            "v_cvt_pk_bf16_f32 v178, v180, v181\n\t"
            "v_cvt_pk_bf16_f32 v179, v182, v183\n\t"
            "v_mfma_f32_16x16x32_bf16 v[148:151], v[24:27], v[192:195], v[148:151]\n\t"
            "v_lshlrev_b32 v156, 16, v218\n\t"
            "v_and_b32 v157, 0xffff0000, v218\n\t"
            "v_mfma_f32_16x16x32_bf16 v[168:171], v[48:51], v[192:195], v[168:171]\n\t"
            "v_lshlrev_b32 v158, 16, v219\n\t"
            "v_and_b32 v159, 0xffff0000, v219\n\t"
            "v_mfma_f32_16x16x32_bf16 v[168:171], v[52:55], v[172:175], v[168:171]\n\t"
            "ds_read_b128 v[216:219], v237 offset:6144\n\t"
            "v_lshlrev_b32 v164, 16, v220\n\t"
            "v_mfma_f32_16x16x32_bf16 v[156:159], v[76:79], v[172:175], v[156:159]\n\t"
            "v_and_b32 v165, 0xffff0000, v220\n\t"
            "v_lshlrev_b32 v166, 16, v221\n\t"
            "v_mfma_f32_16x16x32_bf16 v[156:159], v[72:75], v[192:195], v[156:159]\n\t"
            "v_and_b32 v167, 0xffff0000, v221\n\t"
            "v_lshlrev_b32 v180, 16, v222\n\t"
            "v_and_b32 v181, 0xffff0000, v222\n\t"
            "v_mfma_f32_16x16x32_bf16 v[164:167], v[96:99], v[192:195], v[164:167]\n\t"
            "v_lshlrev_b32 v182, 16, v223\n\t"
            "v_and_b32 v183, 0xffff0000, v223\n\t"
            "v_mfma_f32_16x16x32_bf16 v[164:167], v[100:103], v[172:175], v[164:167]\n\t"
            "ds_read_b128 v[220:223], v248 offset:6144\n\t"
            "buffer_store_dwordx4 v[152:155], v196, s[40:43], s45 offen sc1\n\t"
            "v_mfma_f32_16x16x32_bf16 v[180:183], v[124:127], v[172:175], v[180:183]\n\t"
            "ds_read_b128 v[172:175], v243 offset:6400\n\t"
            "v_lshlrev_b32 v152, 16, v224\n\t"
            "v_mfma_f32_16x16x32_bf16 v[180:183], v[120:123], v[192:195], v[180:183]\n\t"
            "ds_read_b128 v[192:195], v242 offset:6400\n\t"
            "buffer_store_dwordx4 v[160:163], v196, s[40:43], s45 offen offset:64 sc1\n\t"
            "s_waitcnt lgkmcnt(4)\n\t"
            "v_mfma_f32_16x16x32_bf16 v[144:147], v[8:11], v[184:187], v[144:147]\n\t"
            "v_and_b32 v153, 0xffff0000, v224\n\t"
            "v_lshlrev_b32 v154, 16, v225\n\t"
            "v_mfma_f32_16x16x32_bf16 v[144:147], v[12:15], v[188:191], v[144:147]\n\t"
            "buffer_store_dwordx4 v[176:179], v196, s[40:43], s45 offen offset:128 sc1\n\t"
            "v_mfma_f32_16x16x32_bf16 v[148:151], v[36:39], v[188:191], v[148:151]\n\t"
            "v_and_b32 v155, 0xffff0000, v225\n\t"
            "v_lshlrev_b32 v160, 16, v226\n\t"
            "v_mfma_f32_16x16x32_bf16 v[148:151], v[32:35], v[184:187], v[148:151]\n\t"
            "v_and_b32 v161, 0xffff0000, v226\n\t"
            "v_lshlrev_b32 v162, 16, v227\n\t"
            "v_mfma_f32_16x16x32_bf16 v[168:171], v[56:59], v[184:187], v[168:171]\n\t"
            "v_and_b32 v163, 0xffff0000, v227\n\t"
            "v_lshlrev_b32 v176, 16, v228\n\t"
            "v_mfma_f32_16x16x32_bf16 v[168:171], v[60:63], v[188:191], v[168:171]\n\t"
            "ds_read_b128 v[224:227], v236 offset:12288\n\t"
            "v_and_b32 v177, 0xffff0000, v228\n\t"
            "v_mfma_f32_16x16x32_bf16 v[156:159], v[84:87], v[188:191], v[156:159]\n\t"
            "v_lshlrev_b32 v178, 16, v229\n\t"
            "v_and_b32 v179, 0xffff0000, v229\n\t"
            "v_mfma_f32_16x16x32_bf16 v[156:159], v[80:83], v[184:187], v[156:159]\n\t"
            "v_mfma_f32_16x16x32_bf16 v[164:167], v[104:107], v[184:187], v[164:167]\n\t"
            "v_mfma_f32_16x16x32_bf16 v[164:167], v[108:111], v[188:191], v[164:167]\n\t"
            "v_mfma_f32_16x16x32_bf16 v[180:183], v[132:135], v[188:191], v[180:183]\n\t"
            "ds_read_b128 v[188:191], v243 offset:12288\n\t"
            "v_mfma_f32_16x16x32_bf16 v[180:183], v[128:131], v[184:187], v[180:183]\n\t"
            "ds_read_b128 v[184:187], v242 offset:12288\n\t"
            "s_waitcnt lgkmcnt(3)\n\t"
            "v_mfma_f32_16x16x32_bf16 v[144:147], v[16:19], v[192:195], v[144:147]\n\t"
            "v_mfma_f32_16x16x32_bf16 v[144:147], v[20:23], v[172:175], v[144:147]\n\t"
            "v_mfma_f32_16x16x32_bf16 v[168:171], v[64:67], v[192:195], v[168:171]\n\t"
            "v_mfma_f32_16x16x32_bf16 v[168:171], v[68:71], v[172:175], v[168:171]\n\t"
            "v_mfma_f32_16x16x32_bf16 v[164:167], v[112:115], v[192:195], v[164:167]\n\t"
            "v_mfma_f32_16x16x32_bf16 v[164:167], v[116:119], v[172:175], v[164:167]\n\t"
            "v_mfma_f32_16x16x32_bf16 v[148:151], v[44:47], v[172:175], v[148:151]\n\t"
            "v_mfma_f32_16x16x32_bf16 v[148:151], v[40:43], v[192:195], v[148:151]\n\t"
            "s_nop 1\n\t"
            "v_cvt_pk_bf16_f32 v144, v144, v145\n\t"
            "v_mfma_f32_16x16x32_bf16 v[156:159], v[92:95], v[172:175], v[156:159]\n\t"
            "v_cvt_pk_bf16_f32 v145, v146, v147\n\t"
            "v_mfma_f32_16x16x32_bf16 v[156:159], v[88:91], v[192:195], v[156:159]\n\t"
            "v_cvt_pk_bf16_f32 v168, v168, v169\n\t"
            "v_cvt_pk_bf16_f32 v169, v170, v171\n\t"
            "v_mfma_f32_16x16x32_bf16 v[180:183], v[140:143], v[172:175], v[180:183]\n\t"
            "ds_read_b128 v[172:175], v243 offset:12416\n\t"
            "v_cvt_pk_bf16_f32 v164, v164, v165\n\t"
            "v_mfma_f32_16x16x32_bf16 v[180:183], v[136:139], v[192:195], v[180:183]\n\t"
            "ds_read_b128 v[192:195], v242 offset:12416\n\t"
            "v_cvt_pk_bf16_f32 v165, v166, v167\n\t"
            "s_waitcnt lgkmcnt(2)\n\t"
            "v_mfma_f32_16x16x32_bf16 v[152:155], v[0:3], v[184:187], v[152:155]\n\t"
            "v_cvt_pk_bf16_f32 v146, v148, v149\n\t"
            "v_cvt_pk_bf16_f32 v147, v150, v151\n\t"
            "v_mfma_f32_16x16x32_bf16 v[152:155], v[4:7], v[188:191], v[152:155]\n\t"
            "v_cvt_pk_bf16_f32 v170, v156, v157\n\t"
            "v_cvt_pk_bf16_f32 v171, v158, v159\n\t"
            "v_mfma_f32_16x16x32_bf16 v[160:163], v[28:31], v[188:191], v[160:163]\n\t"
            "v_cvt_pk_bf16_f32 v166, v180, v181\n\t"
            "v_cvt_pk_bf16_f32 v167, v182, v183\n\t"
            "v_mfma_f32_16x16x32_bf16 v[160:163], v[24:27], v[184:187], v[160:163]\n\t"
            "v_lshlrev_b32 v148, 16, v230\n\t"
            "v_and_b32 v149, 0xffff0000, v230\n\t"
            "v_mfma_f32_16x16x32_bf16 v[176:179], v[48:51], v[184:187], v[176:179]\n\t"
            "v_lshlrev_b32 v150, 16, v231\n\t"
            "v_and_b32 v151, 0xffff0000, v231\n\t"
            "v_mfma_f32_16x16x32_bf16 v[176:179], v[52:55], v[188:191], v[176:179]\n\t"
            "ds_read_b128 v[228:231], v237 offset:12288\n\t"
            "v_lshlrev_b32 v156, 16, v232\n\t"
            "v_mfma_f32_16x16x32_bf16 v[148:151], v[76:79], v[188:191], v[148:151]\n\t"
            "v_and_b32 v157, 0xffff0000, v232\n\t"
            "v_lshlrev_b32 v158, 16, v233\n\t"
            "v_mfma_f32_16x16x32_bf16 v[148:151], v[72:75], v[184:187], v[148:151]\n\t"
            "v_and_b32 v159, 0xffff0000, v233\n\t"
            "v_lshlrev_b32 v180, 16, v234\n\t"
            "v_and_b32 v181, 0xffff0000, v234\n\t"
            "v_mfma_f32_16x16x32_bf16 v[156:159], v[96:99], v[184:187], v[156:159]\n\t"
            "v_lshlrev_b32 v182, 16, v235\n\t"
            "v_and_b32 v183, 0xffff0000, v235\n\t"
            "v_mfma_f32_16x16x32_bf16 v[156:159], v[100:103], v[188:191], v[156:159]\n\t"
            "ds_read_b128 v[232:235], v248 offset:12288\n\t"
            "buffer_store_dwordx4 v[144:147], v197, s[40:43], s45 offen sc1\n\t"
            "v_mfma_f32_16x16x32_bf16 v[180:183], v[124:127], v[188:191], v[180:183]\n\t"
            "ds_read_b128 v[188:191], v243 offset:12544\n\t"
            "v_mfma_f32_16x16x32_bf16 v[180:183], v[120:123], v[184:187], v[180:183]\n\t"
            "ds_read_b128 v[184:187], v242 offset:12544\n\t"
            "buffer_store_dwordx4 v[168:171], v197, s[40:43], s45 offen offset:64 sc1\n\t"
            "s_waitcnt lgkmcnt(4)\n\t"
            "v_mfma_f32_16x16x32_bf16 v[152:155], v[8:11], v[192:195], v[152:155]\n\t"
            "v_mfma_f32_16x16x32_bf16 v[152:155], v[12:15], v[172:175], v[152:155]\n\t"
            "buffer_store_dwordx4 v[164:167], v197, s[40:43], s45 offen offset:128 sc1\n\t"
            "v_mfma_f32_16x16x32_bf16 v[160:163], v[36:39], v[172:175], v[160:163]\n\t"
            "v_mfma_f32_16x16x32_bf16 v[160:163], v[32:35], v[192:195], v[160:163]\n\t"
            "s_waitcnt lgkmcnt(0)\n\t"
            "s_barrier\n\t"
            "s_setprio 3\n\t"
            "v_mfma_f32_16x16x32_bf16 v[176:179], v[56:59], v[192:195], v[176:179]\n\t"
            "v_mfma_f32_16x16x32_bf16 v[176:179], v[60:63], v[172:175], v[176:179]\n\t"
            "v_mfma_f32_16x16x32_bf16 v[148:151], v[84:87], v[172:175], v[148:151]\n\t"
            "v_mfma_f32_16x16x32_bf16 v[148:151], v[80:83], v[192:195], v[148:151]\n\t"
            "v_mfma_f32_16x16x32_bf16 v[156:159], v[104:107], v[192:195], v[156:159]\n\t"
            "v_mfma_f32_16x16x32_bf16 v[156:159], v[108:111], v[172:175], v[156:159]\n\t"
            "v_mfma_f32_16x16x32_bf16 v[152:155], v[16:19], v[184:187], v[152:155]\n\t"
            "v_mfma_f32_16x16x32_bf16 v[152:155], v[20:23], v[188:191], v[152:155]\n\t"
            "v_mfma_f32_16x16x32_bf16 v[160:163], v[44:47], v[188:191], v[160:163]\n\t"
            "v_mfma_f32_16x16x32_bf16 v[160:163], v[40:43], v[184:187], v[160:163]\n\t"
            "v_mfma_f32_16x16x32_bf16 v[180:183], v[132:135], v[172:175], v[180:183]\n\t"
            "v_mfma_f32_16x16x32_bf16 v[180:183], v[128:131], v[192:195], v[180:183]\n\t"
            "v_mfma_f32_16x16x32_bf16 v[176:179], v[64:67], v[184:187], v[176:179]\n\t"
            "v_mfma_f32_16x16x32_bf16 v[176:179], v[68:71], v[188:191], v[176:179]\n\t"
            "s_nop 1\n\t"
            "v_cvt_pk_bf16_f32 v152, v152, v153\n\t"
            "v_mfma_f32_16x16x32_bf16 v[148:151], v[92:95], v[188:191], v[148:151]\n\t"
            "v_cvt_pk_bf16_f32 v153, v154, v155\n\t"
            "v_mfma_f32_16x16x32_bf16 v[148:151], v[88:91], v[184:187], v[148:151]\n\t"
            "v_cvt_pk_bf16_f32 v154, v160, v161\n\t"
            "v_cvt_pk_bf16_f32 v155, v162, v163\n\t"
            "v_mfma_f32_16x16x32_bf16 v[156:159], v[112:115], v[184:187], v[156:159]\n\t"
            "v_cvt_pk_bf16_f32 v176, v176, v177\n\t"
            "v_cvt_pk_bf16_f32 v177, v178, v179\n\t"
            "v_mfma_f32_16x16x32_bf16 v[156:159], v[116:119], v[188:191], v[156:159]\n\t"
            "buffer_store_dwordx4 v[152:155], v198, s[40:43], s45 offen sc1\n\t"
            "v_mfma_f32_16x16x32_bf16 v[180:183], v[140:143], v[188:191], v[180:183]\n\t"
            "v_cvt_pk_bf16_f32 v178, v148, v149\n\t"
            "v_cvt_pk_bf16_f32 v179, v150, v151\n\t"
            "v_mfma_f32_16x16x32_bf16 v[180:183], v[136:139], v[184:187], v[180:183]\n\t"
            "buffer_store_dwordx4 v[176:179], v198, s[40:43], s45 offen offset:64 sc1\n\t"
            "s_nop 1\n\t"
            "v_cvt_pk_bf16_f32 v156, v156, v157\n\t"
            "v_cvt_pk_bf16_f32 v157, v158, v159\n\t"
            "s_nop 2\n\t"
            "v_cvt_pk_bf16_f32 v158, v180, v181\n\t"
            "v_cvt_pk_bf16_f32 v159, v182, v183\n\t"
            "buffer_store_dwordx4 v[156:159], v198, s[40:43], s45 offen offset:128 sc1\n\t"
            "s_add_u32 s45, s46, s27\n\t"
            "s_mov_b32 s46, s47\n\t"
            "s_mov_b32 s47, s68\n\t"
            "s_xor_b32 s48, s48, s71\n\t"
            "s_xor_b32 s67, s67, s71\n\t"
            "s_add_u32 s76, s76, 4\n\t"
            "s_add_u32 s70, s70, 4\n\t"
            "s_lshl_b32 s56, s56, 2\n\t"
            "s_lshr_b32 s49, s26, 30\n\t"
            "s_or_b32 s56, s56, s49\n\t"
            "s_cmp_lt_u32 s46, 0x7000000\n\t"
            "s_cbranch_scc1 .Lwb_loop%=\n\t"
            "s_branch .Lwb_end%=\n\t"
            ".Lwb_spin%=:\n\t"
            "s_cmp_eq_u32 s83, 0\n\t"
            "s_cbranch_scc1 .Lwb_go%=\n\t"
            "s_sub_u32 s83, s83, 1\n\t"
            "s_sleep 2\n\t"
            "v_mov_b32 v247, 0\n\t"
            "ds_read_b32 v247, v247 offset:7856\n\t"
            "s_waitcnt lgkmcnt(0)\n\t"
            "v_readfirstlane_b32 s77, v247\n\t"
            "s_cmp_ge_u32 s77, s70\n\t"
            "s_cbranch_scc0 .Lwb_spin%=\n\t"
            "s_branch .Lwb_go%=\n\t"
            ".Lwb_end%=:\n\t"
            "s_waitcnt lgkmcnt(0)\n\t"
            "s_nop 7\n\t"
            : "={v[0:3]}"(W[0]), "={v[4:7]}"(W[1]), "={v[8:11]}"(W[2]), "={v[12:15]}"(W[3]), "={v[16:19]}"(W[4]), "={v[20:23]}"(W[5]), "={v[24:27]}"(W[6]), "={v[28:31]}"(W[7]), "={v[32:35]}"(W[8]), "={v[36:39]}"(W[9]), "={v[40:43]}"(W[10]), "={v[44:47]}"(W[11]), "={v[48:51]}"(W[12]), "={v[52:55]}"(W[13]), "={v[56:59]}"(W[14]), "={v[60:63]}"(W[15]), "={v[64:67]}"(W[16]), "={v[68:71]}"(W[17]), "={v[72:75]}"(W[18]), "={v[76:79]}"(W[19]), "={v[80:83]}"(W[20]), "={v[84:87]}"(W[21]), "={v[88:91]}"(W[22]), "={v[92:95]}"(W[23]), "={v[96:99]}"(W[24]), "={v[100:103]}"(W[25]), "={v[104:107]}"(W[26]), "={v[108:111]}"(W[27]), "={v[112:115]}"(W[28]), "={v[116:119]}"(W[29]), "={v[120:123]}"(W[30]), "={v[124:127]}"(W[31]), "={v[128:131]}"(W[32]), "={v[132:135]}"(W[33]), "={v[136:139]}"(W[34]), "={v[140:143]}"(W[35]), "={v[200:203]}"(xl[0]), "={v[204:207]}"(xl[1]), "={v[208:211]}"(xl[2]), "={v[212:215]}"(xl[3]), "={v[216:219]}"(xl[4]), "={v[220:223]}"(xl[5]), "={v[224:227]}"(xl[6]), "={v[228:231]}"(xl[7]), "={v[232:235]}"(xl[8]), B_SIO, "+{v247}"(l16_)
            : "{v242}"(oE_), "{v243}"(oO_), "{v244}"(pR0_), "{v245}"(pR1_), "{v246}"(pR2_), "{v250}"(pXw_), "{v251}"(pSTw_), "{v252}"(aCA_), "{v253}"(aB_), "{v254}"(aA_), "{v255}"(idx_), B_SIN, "{s44}"(ARR_OFF + blockIdx.x * 8), "{s[80:81]}"(mrow),
              "{v228}"(ldsb + PF_OFF + 1024 + lane * 4), "{v229}"(ldsb + O_OFF + lane * 16), "{v232}"(oE_), "{v233}"(ldsb + PF_OFF + 1024 + l15 * 4),
              "{v234}"(ldsb + OL_OFF + l15 * 384 + (((4 * (w - 2) + (g >> 1)) ^ sw) << 4) + 8 * (g & 1))
            : "v144", "v145", "v146", "v147", "v148", "v149", "v150", "v151", "v152", "v153", "v154", "v155", "v156", "v157", "v158", "v159", "v160", "v161", "v162", "v163", "v164", "v165", "v166", "v167", "v168", "v169", "v170", "v171", "v172", "v173", "v174", "v175", "v176", "v177", "v178", "v179", "v180", "v181", "v182", "v183", "v184", "v185", "v186", "v187", "v188", "v189", "v190", "v191", "v192", "v193", "v194", "v195", "v196", "v197", "v198", "v199", "v236", "v237", "v248", "v249", B_SCLOB);
            t45 = s45; t56 = s56;
        }
        {
            int ln_;
            asm volatile("v_mbcnt_lo_u32_b32 %0, -1, 0\n\tv_mbcnt_hi_u32_b32 %0, -1, %0" : "=v"(ln_));
            const int l15_ = ln_ & 15;
            const int g_ = ln_ >> 4;
            KARGS
            const auto orsrc = __builtin_amdgcn_make_buffer_rsrc((void*)ob, 0, IMG * IMG * CH * 2, 0x00020000);
#pragma unroll
            for (int lt = 0; lt < 2; ++lt) {
                int i = 16 * lt + l15_;
                const unsigned en = *(const unsigned*)(smem + WL_OFF + i * 4);
                const bool ok = (en & 0x7ffffffu) != 0x7ffffffu;
                i = ok ? i : 0;
                const int lE = OL_OFF + i * 384 + ((g_ ^ ((i >> 1) & 7)) << 4);
                const unsigned o = ok ? (en & 0x7ffffffu) + LPOS(en >> 31) * ROWBYTES + LPOS(en >> 30) * (CH * 2) + 192 * (w - 2) + 16 * g_ : OOB;
                u32x4 rl[3];
#pragma unroll
                for (int k = 0; k < 3; ++k) {
                    const int c8 = 12 * (w - 2) + 4 * k + g_, ws = (c8 * 43) >> 8, r6 = c8 - 6 * ws;
                    rl[k] = *(const u32x4*)(smem + LX_OFF(i) + (r6 >> 1) * 128 + ((2 * ws + (r6 & 1)) << 4));
                }
                if (lt == 1) {
                    const unsigned oE_ = ldsb + O_OFF + l15_ * 384 + ((g_ ^ (l15_ >> 1)) << 4), oO_ = oE_ ^ 64, aCA_ = ldsb + CT_OFF + ln_ * 16;
                    asm volatile(
            "s_waitcnt lgkmcnt(0)\n\t"
            "s_barrier\n\t"
            "s_nop 7\n\t"
            "ds_read_b128 v[184:187], v242\n\t"
            "ds_read_b128 v[188:191], v243\n\t"
            "ds_read_b128 v[192:195], v242 offset:128\n\t"
            "ds_read_b128 v[172:175], v243 offset:128\n\t"
            "ds_read_b128 v[196:199], v252\n\t"
            "v_lshlrev_b32 v152, 16, v200\n\t"
            "v_and_b32 v153, 0xffff0000, v200\n\t"
            "v_lshlrev_b32 v154, 16, v201\n\t"
            "v_and_b32 v155, 0xffff0000, v201\n\t"
            "v_lshlrev_b32 v156, 16, v202\n\t"
            "v_and_b32 v157, 0xffff0000, v202\n\t"
            "v_lshlrev_b32 v158, 16, v203\n\t"
            "v_and_b32 v159, 0xffff0000, v203\n\t"
            "v_lshlrev_b32 v160, 16, v204\n\t"
            "v_and_b32 v161, 0xffff0000, v204\n\t"
            "v_lshlrev_b32 v162, 16, v205\n\t"
            "v_and_b32 v163, 0xffff0000, v205\n\t"
            "v_lshlrev_b32 v164, 16, v206\n\t"
            "v_and_b32 v165, 0xffff0000, v206\n\t"
            "v_lshlrev_b32 v166, 16, v207\n\t"
            "v_and_b32 v167, 0xffff0000, v207\n\t"
            "v_lshlrev_b32 v176, 16, v208\n\t"
            "v_and_b32 v177, 0xffff0000, v208\n\t"
            "v_lshlrev_b32 v178, 16, v209\n\t"
            "v_and_b32 v179, 0xffff0000, v209\n\t"
            "v_lshlrev_b32 v180, 16, v210\n\t"
            "v_and_b32 v181, 0xffff0000, v210\n\t"
            "v_lshlrev_b32 v182, 16, v211\n\t"
            "v_and_b32 v183, 0xffff0000, v211\n\t"
            "v_lshlrev_b32 v144, 16, v212\n\t"
            "v_and_b32 v145, 0xffff0000, v212\n\t"
            "v_lshlrev_b32 v146, 16, v213\n\t"
            "v_and_b32 v147, 0xffff0000, v213\n\t"
            "v_lshlrev_b32 v148, 16, v214\n\t"
            "v_and_b32 v149, 0xffff0000, v214\n\t"
            "v_lshlrev_b32 v150, 16, v215\n\t"
            "v_and_b32 v151, 0xffff0000, v215\n\t"
            "v_lshlrev_b32 v168, 16, v216\n\t"
            "v_and_b32 v169, 0xffff0000, v216\n\t"
            "v_lshlrev_b32 v170, 16, v217\n\t"
            "v_and_b32 v171, 0xffff0000, v217\n\t"
            "s_bfe_u32 s57, s56, 0x20004\n\t"
            "s_lshl_b32 s58, s57, 3\n\t"
            "s_lshr_b32 s58, s55, s58\n\t"
            "s_lshl_b64 s[36:37], s[34:35], s58\n\t"
            "s_bitcmp1_b32 s57, 1\n\t"
            "s_cselect_b32 s58, 1177344, 0\n\t"
            "s_bitcmp1_b32 s57, 0\n\t"
            "s_cselect_b32 s57, 2304, 0\n\t"
            "s_add_u32 s57, s57, s58\n\t"
            "s_waitcnt lgkmcnt(3)\n\t"
            "v_mfma_f32_16x16x32_bf16 v[152:155], v[0:3], v[184:187], v[152:155]\n\t"
            "s_waitcnt lgkmcnt(0)\n\t"
            "s_mov_b64 exec, s[36:37]\n\t"
            "v_add_u32 v196, s57, v196\n\t"
            "s_mov_b64 exec, -1\n\t"
            "v_mfma_f32_16x16x32_bf16 v[152:155], v[4:7], v[188:191], v[152:155]\n\t"
            "v_mfma_f32_16x16x32_bf16 v[156:159], v[28:31], v[188:191], v[156:159]\n\t"
            "v_mfma_f32_16x16x32_bf16 v[156:159], v[24:27], v[184:187], v[156:159]\n\t"
            "v_mfma_f32_16x16x32_bf16 v[160:163], v[48:51], v[184:187], v[160:163]\n\t"
            "v_mfma_f32_16x16x32_bf16 v[160:163], v[52:55], v[188:191], v[160:163]\n\t"
            "v_mfma_f32_16x16x32_bf16 v[164:167], v[76:79], v[188:191], v[164:167]\n\t"
            "v_mfma_f32_16x16x32_bf16 v[164:167], v[72:75], v[184:187], v[164:167]\n\t"
            "v_mfma_f32_16x16x32_bf16 v[176:179], v[96:99], v[184:187], v[176:179]\n\t"
            "v_mfma_f32_16x16x32_bf16 v[176:179], v[100:103], v[188:191], v[176:179]\n\t"
            "v_mfma_f32_16x16x32_bf16 v[180:183], v[124:127], v[188:191], v[180:183]\n\t"
            "ds_read_b128 v[188:191], v243 offset:256\n\t"
            "v_mfma_f32_16x16x32_bf16 v[180:183], v[120:123], v[184:187], v[180:183]\n\t"
            "ds_read_b128 v[184:187], v242 offset:256\n\t"
            "v_mfma_f32_16x16x32_bf16 v[152:155], v[8:11], v[192:195], v[152:155]\n\t"
            "v_mfma_f32_16x16x32_bf16 v[152:155], v[12:15], v[172:175], v[152:155]\n\t"
            "v_mfma_f32_16x16x32_bf16 v[156:159], v[36:39], v[172:175], v[156:159]\n\t"
            "v_mfma_f32_16x16x32_bf16 v[156:159], v[32:35], v[192:195], v[156:159]\n\t"
            "v_mfma_f32_16x16x32_bf16 v[160:163], v[56:59], v[192:195], v[160:163]\n\t"
            "v_mfma_f32_16x16x32_bf16 v[160:163], v[60:63], v[172:175], v[160:163]\n\t"
            "v_mfma_f32_16x16x32_bf16 v[164:167], v[84:87], v[172:175], v[164:167]\n\t"
            "v_mfma_f32_16x16x32_bf16 v[164:167], v[80:83], v[192:195], v[164:167]\n\t"
            "v_mfma_f32_16x16x32_bf16 v[176:179], v[104:107], v[192:195], v[176:179]\n\t"
            "v_mfma_f32_16x16x32_bf16 v[176:179], v[108:111], v[172:175], v[176:179]\n\t"
            "v_mfma_f32_16x16x32_bf16 v[180:183], v[132:135], v[172:175], v[180:183]\n\t"
            "ds_read_b128 v[172:175], v243 offset:6144\n\t"
            "v_mfma_f32_16x16x32_bf16 v[180:183], v[128:131], v[192:195], v[180:183]\n\t"
            "ds_read_b128 v[192:195], v242 offset:6144\n\t"
            "s_waitcnt lgkmcnt(2)\n\t"
            "v_mfma_f32_16x16x32_bf16 v[152:155], v[16:19], v[184:187], v[152:155]\n\t"
            "v_mfma_f32_16x16x32_bf16 v[152:155], v[20:23], v[188:191], v[152:155]\n\t"
            "v_mfma_f32_16x16x32_bf16 v[156:159], v[44:47], v[188:191], v[156:159]\n\t"
            "v_mfma_f32_16x16x32_bf16 v[156:159], v[40:43], v[184:187], v[156:159]\n\t"
            "v_mfma_f32_16x16x32_bf16 v[160:163], v[64:67], v[184:187], v[160:163]\n\t"
            "v_mfma_f32_16x16x32_bf16 v[160:163], v[68:71], v[188:191], v[160:163]\n\t"
            "v_mfma_f32_16x16x32_bf16 v[164:167], v[92:95], v[188:191], v[164:167]\n\t"
            "v_mfma_f32_16x16x32_bf16 v[164:167], v[88:91], v[184:187], v[164:167]\n\t"
            "s_nop 1\n\t"
            "v_cvt_pk_bf16_f32 v152, v152, v153\n\t"
            "v_mfma_f32_16x16x32_bf16 v[176:179], v[112:115], v[184:187], v[176:179]\n\t"
            "v_cvt_pk_bf16_f32 v153, v154, v155\n\t"
            "v_mfma_f32_16x16x32_bf16 v[176:179], v[116:119], v[188:191], v[176:179]\n\t"
            "v_cvt_pk_bf16_f32 v154, v156, v157\n\t"
            "v_cvt_pk_bf16_f32 v155, v158, v159\n\t"
            "v_mfma_f32_16x16x32_bf16 v[180:183], v[140:143], v[188:191], v[180:183]\n\t"
            "ds_read_b128 v[188:191], v243 offset:6272\n\t"
            "v_cvt_pk_bf16_f32 v160, v160, v161\n\t"
            "v_mfma_f32_16x16x32_bf16 v[180:183], v[136:139], v[184:187], v[180:183]\n\t"
            "ds_read_b128 v[184:187], v242 offset:6272\n\t"
            "v_cvt_pk_bf16_f32 v161, v162, v163\n\t"
            "s_waitcnt lgkmcnt(2)\n\t"
            "v_mfma_f32_16x16x32_bf16 v[144:147], v[0:3], v[192:195], v[144:147]\n\t"
            "v_cvt_pk_bf16_f32 v176, v176, v177\n\t"
            "v_cvt_pk_bf16_f32 v177, v178, v179\n\t"
            "v_mfma_f32_16x16x32_bf16 v[144:147], v[4:7], v[172:175], v[144:147]\n\t"
            "v_cvt_pk_bf16_f32 v162, v164, v165\n\t"
            "v_cvt_pk_bf16_f32 v163, v166, v167\n\t"
            "v_mfma_f32_16x16x32_bf16 v[148:151], v[28:31], v[172:175], v[148:151]\n\t"
            "v_cvt_pk_bf16_f32 v178, v180, v181\n\t"
            "v_cvt_pk_bf16_f32 v179, v182, v183\n\t"
            "v_mfma_f32_16x16x32_bf16 v[148:151], v[24:27], v[192:195], v[148:151]\n\t"
            "v_lshlrev_b32 v156, 16, v218\n\t"
            "v_and_b32 v157, 0xffff0000, v218\n\t"
            "v_mfma_f32_16x16x32_bf16 v[168:171], v[48:51], v[192:195], v[168:171]\n\t"
            "v_lshlrev_b32 v158, 16, v219\n\t"
            "v_and_b32 v159, 0xffff0000, v219\n\t"
            "v_mfma_f32_16x16x32_bf16 v[168:171], v[52:55], v[172:175], v[168:171]\n\t"
            "v_lshlrev_b32 v164, 16, v220\n\t"
            "v_and_b32 v165, 0xffff0000, v220\n\t"
            "v_mfma_f32_16x16x32_bf16 v[156:159], v[76:79], v[172:175], v[156:159]\n\t"
            "v_lshlrev_b32 v166, 16, v221\n\t"
            "v_and_b32 v167, 0xffff0000, v221\n\t"
            "v_mfma_f32_16x16x32_bf16 v[156:159], v[72:75], v[192:195], v[156:159]\n\t"
            "v_lshlrev_b32 v180, 16, v222\n\t"
            "v_and_b32 v181, 0xffff0000, v222\n\t"
            "v_mfma_f32_16x16x32_bf16 v[164:167], v[96:99], v[192:195], v[164:167]\n\t"
            "v_lshlrev_b32 v182, 16, v223\n\t"
            "v_and_b32 v183, 0xffff0000, v223\n\t"
            "v_mfma_f32_16x16x32_bf16 v[164:167], v[100:103], v[172:175], v[164:167]\n\t"
            "buffer_store_dwordx4 v[152:155], v196, s[40:43], s45 offen sc1\n\t"
            "v_mfma_f32_16x16x32_bf16 v[180:183], v[124:127], v[172:175], v[180:183]\n\t"
            "ds_read_b128 v[172:175], v243 offset:6400\n\t"
            "v_lshlrev_b32 v152, 16, v224\n\t"
            "v_mfma_f32_16x16x32_bf16 v[180:183], v[120:123], v[192:195], v[180:183]\n\t"
            "ds_read_b128 v[192:195], v242 offset:6400\n\t"
            "buffer_store_dwordx4 v[160:163], v196, s[40:43], s45 offen offset:64 sc1\n\t"
            "s_waitcnt lgkmcnt(2)\n\t"
            "v_mfma_f32_16x16x32_bf16 v[144:147], v[8:11], v[184:187], v[144:147]\n\t"
            "v_and_b32 v153, 0xffff0000, v224\n\t"
            "v_lshlrev_b32 v154, 16, v225\n\t"
            "v_mfma_f32_16x16x32_bf16 v[144:147], v[12:15], v[188:191], v[144:147]\n\t"
            "buffer_store_dwordx4 v[176:179], v196, s[40:43], s45 offen offset:128 sc1\n\t"
            "v_mfma_f32_16x16x32_bf16 v[148:151], v[36:39], v[188:191], v[148:151]\n\t"
            "v_and_b32 v155, 0xffff0000, v225\n\t"
            "v_lshlrev_b32 v160, 16, v226\n\t"
            "v_mfma_f32_16x16x32_bf16 v[148:151], v[32:35], v[184:187], v[148:151]\n\t"
            "v_and_b32 v161, 0xffff0000, v226\n\t"
            "v_lshlrev_b32 v162, 16, v227\n\t"
            "v_mfma_f32_16x16x32_bf16 v[168:171], v[56:59], v[184:187], v[168:171]\n\t"
            "v_and_b32 v163, 0xffff0000, v227\n\t"
            "v_lshlrev_b32 v176, 16, v228\n\t"
            "v_mfma_f32_16x16x32_bf16 v[168:171], v[60:63], v[188:191], v[168:171]\n\t"
            "v_and_b32 v177, 0xffff0000, v228\n\t"
            "v_lshlrev_b32 v178, 16, v229\n\t"
            "v_mfma_f32_16x16x32_bf16 v[156:159], v[84:87], v[188:191], v[156:159]\n\t"
            "v_and_b32 v179, 0xffff0000, v229\n\t"
            "v_mfma_f32_16x16x32_bf16 v[156:159], v[80:83], v[184:187], v[156:159]\n\t"
            "v_mfma_f32_16x16x32_bf16 v[164:167], v[104:107], v[184:187], v[164:167]\n\t"
            "v_mfma_f32_16x16x32_bf16 v[164:167], v[108:111], v[188:191], v[164:167]\n\t"
            "v_mfma_f32_16x16x32_bf16 v[180:183], v[132:135], v[188:191], v[180:183]\n\t"
            "ds_read_b128 v[188:191], v243 offset:12288\n\t"
            "v_mfma_f32_16x16x32_bf16 v[180:183], v[128:131], v[184:187], v[180:183]\n\t"
            "ds_read_b128 v[184:187], v242 offset:12288\n\t"
            "s_waitcnt lgkmcnt(2)\n\t"
            "v_mfma_f32_16x16x32_bf16 v[144:147], v[16:19], v[192:195], v[144:147]\n\t"
            "v_mfma_f32_16x16x32_bf16 v[144:147], v[20:23], v[172:175], v[144:147]\n\t"
            "v_mfma_f32_16x16x32_bf16 v[168:171], v[64:67], v[192:195], v[168:171]\n\t"
            "v_mfma_f32_16x16x32_bf16 v[168:171], v[68:71], v[172:175], v[168:171]\n\t"
            "v_mfma_f32_16x16x32_bf16 v[164:167], v[112:115], v[192:195], v[164:167]\n\t"
            "v_mfma_f32_16x16x32_bf16 v[164:167], v[116:119], v[172:175], v[164:167]\n\t"
            "v_mfma_f32_16x16x32_bf16 v[148:151], v[44:47], v[172:175], v[148:151]\n\t"
            "v_mfma_f32_16x16x32_bf16 v[148:151], v[40:43], v[192:195], v[148:151]\n\t"
            "s_nop 1\n\t"
            "v_cvt_pk_bf16_f32 v144, v144, v145\n\t"
            "v_mfma_f32_16x16x32_bf16 v[156:159], v[92:95], v[172:175], v[156:159]\n\t"
            "v_cvt_pk_bf16_f32 v145, v146, v147\n\t"
            "v_mfma_f32_16x16x32_bf16 v[156:159], v[88:91], v[192:195], v[156:159]\n\t"
            "v_cvt_pk_bf16_f32 v168, v168, v169\n\t"
            "v_cvt_pk_bf16_f32 v169, v170, v171\n\t"
            "v_mfma_f32_16x16x32_bf16 v[180:183], v[140:143], v[172:175], v[180:183]\n\t"
            "ds_read_b128 v[172:175], v243 offset:12416\n\t"
            "v_cvt_pk_bf16_f32 v164, v164, v165\n\t"
            "v_mfma_f32_16x16x32_bf16 v[180:183], v[136:139], v[192:195], v[180:183]\n\t"
            "ds_read_b128 v[192:195], v242 offset:12416\n\t"
            "v_cvt_pk_bf16_f32 v165, v166, v167\n\t"
            "s_waitcnt lgkmcnt(2)\n\t"
            "v_mfma_f32_16x16x32_bf16 v[152:155], v[0:3], v[184:187], v[152:155]\n\t"
            "v_cvt_pk_bf16_f32 v146, v148, v149\n\t"
            "v_cvt_pk_bf16_f32 v147, v150, v151\n\t"
            "v_mfma_f32_16x16x32_bf16 v[152:155], v[4:7], v[188:191], v[152:155]\n\t"
            "v_cvt_pk_bf16_f32 v170, v156, v157\n\t"
            "v_cvt_pk_bf16_f32 v171, v158, v159\n\t"
            "v_mfma_f32_16x16x32_bf16 v[160:163], v[28:31], v[188:191], v[160:163]\n\t"
            "v_cvt_pk_bf16_f32 v166, v180, v181\n\t"
            "v_cvt_pk_bf16_f32 v167, v182, v183\n\t"
            "v_mfma_f32_16x16x32_bf16 v[160:163], v[24:27], v[184:187], v[160:163]\n\t"
            "v_lshlrev_b32 v148, 16, v230\n\t"
            "v_and_b32 v149, 0xffff0000, v230\n\t"
            "v_mfma_f32_16x16x32_bf16 v[176:179], v[48:51], v[184:187], v[176:179]\n\t"
            "v_lshlrev_b32 v150, 16, v231\n\t"
            "v_and_b32 v151, 0xffff0000, v231\n\t"
            "v_mfma_f32_16x16x32_bf16 v[176:179], v[52:55], v[188:191], v[176:179]\n\t"
            "v_lshlrev_b32 v156, 16, v232\n\t"
            "v_and_b32 v157, 0xffff0000, v232\n\t"
            "v_mfma_f32_16x16x32_bf16 v[148:151], v[76:79], v[188:191], v[148:151]\n\t"
            "v_lshlrev_b32 v158, 16, v233\n\t"
            "v_and_b32 v159, 0xffff0000, v233\n\t"
            "v_mfma_f32_16x16x32_bf16 v[148:151], v[72:75], v[184:187], v[148:151]\n\t"
            "v_lshlrev_b32 v180, 16, v234\n\t"
            "v_and_b32 v181, 0xffff0000, v234\n\t"
            "v_mfma_f32_16x16x32_bf16 v[156:159], v[96:99], v[184:187], v[156:159]\n\t"
            "v_lshlrev_b32 v182, 16, v235\n\t"
            "v_and_b32 v183, 0xffff0000, v235\n\t"
            "v_mfma_f32_16x16x32_bf16 v[156:159], v[100:103], v[188:191], v[156:159]\n\t"
            "buffer_store_dwordx4 v[144:147], v197, s[40:43], s45 offen sc1\n\t"
            "v_mfma_f32_16x16x32_bf16 v[180:183], v[124:127], v[188:191], v[180:183]\n\t"
            "ds_read_b128 v[188:191], v243 offset:12544\n\t"
            "v_mfma_f32_16x16x32_bf16 v[180:183], v[120:123], v[184:187], v[180:183]\n\t"
            "ds_read_b128 v[184:187], v242 offset:12544\n\t"
            "buffer_store_dwordx4 v[168:171], v197, s[40:43], s45 offen offset:64 sc1\n\t"
            "s_waitcnt lgkmcnt(2)\n\t"
            "v_mfma_f32_16x16x32_bf16 v[152:155], v[8:11], v[192:195], v[152:155]\n\t"
            "v_mfma_f32_16x16x32_bf16 v[152:155], v[12:15], v[172:175], v[152:155]\n\t"
            "buffer_store_dwordx4 v[164:167], v197, s[40:43], s45 offen offset:128 sc1\n\t"
            "v_mfma_f32_16x16x32_bf16 v[160:163], v[36:39], v[172:175], v[160:163]\n\t"
            "v_mfma_f32_16x16x32_bf16 v[160:163], v[32:35], v[192:195], v[160:163]\n\t"
            "v_mfma_f32_16x16x32_bf16 v[176:179], v[56:59], v[192:195], v[176:179]\n\t"
            "v_mfma_f32_16x16x32_bf16 v[176:179], v[60:63], v[172:175], v[176:179]\n\t"
            "v_mfma_f32_16x16x32_bf16 v[148:151], v[84:87], v[172:175], v[148:151]\n\t"
            "v_mfma_f32_16x16x32_bf16 v[148:151], v[80:83], v[192:195], v[148:151]\n\t"
            "v_mfma_f32_16x16x32_bf16 v[156:159], v[104:107], v[192:195], v[156:159]\n\t"
            "v_mfma_f32_16x16x32_bf16 v[156:159], v[108:111], v[172:175], v[156:159]\n\t"
            "s_waitcnt lgkmcnt(0)\n\t"
            "v_mfma_f32_16x16x32_bf16 v[152:155], v[16:19], v[184:187], v[152:155]\n\t"
            "v_mfma_f32_16x16x32_bf16 v[152:155], v[20:23], v[188:191], v[152:155]\n\t"
            "v_mfma_f32_16x16x32_bf16 v[160:163], v[44:47], v[188:191], v[160:163]\n\t"
            "v_mfma_f32_16x16x32_bf16 v[160:163], v[40:43], v[184:187], v[160:163]\n\t"
            "v_mfma_f32_16x16x32_bf16 v[180:183], v[132:135], v[172:175], v[180:183]\n\t"
            "v_mfma_f32_16x16x32_bf16 v[180:183], v[128:131], v[192:195], v[180:183]\n\t"
            "v_mfma_f32_16x16x32_bf16 v[176:179], v[64:67], v[184:187], v[176:179]\n\t"
            "v_mfma_f32_16x16x32_bf16 v[176:179], v[68:71], v[188:191], v[176:179]\n\t"
            "s_nop 1\n\t"
            "v_cvt_pk_bf16_f32 v152, v152, v153\n\t"
            "v_mfma_f32_16x16x32_bf16 v[148:151], v[92:95], v[188:191], v[148:151]\n\t"
            "v_cvt_pk_bf16_f32 v153, v154, v155\n\t"
            "v_mfma_f32_16x16x32_bf16 v[148:151], v[88:91], v[184:187], v[148:151]\n\t"
            "v_cvt_pk_bf16_f32 v154, v160, v161\n\t"
            "v_cvt_pk_bf16_f32 v155, v162, v163\n\t"
            "v_mfma_f32_16x16x32_bf16 v[156:159], v[112:115], v[184:187], v[156:159]\n\t"
            "v_cvt_pk_bf16_f32 v176, v176, v177\n\t"
            "v_cvt_pk_bf16_f32 v177, v178, v179\n\t"
            "v_mfma_f32_16x16x32_bf16 v[156:159], v[116:119], v[188:191], v[156:159]\n\t"
            "buffer_store_dwordx4 v[152:155], v198, s[40:43], s45 offen sc1\n\t"
            "v_mfma_f32_16x16x32_bf16 v[180:183], v[140:143], v[188:191], v[180:183]\n\t"
            "v_cvt_pk_bf16_f32 v178, v148, v149\n\t"
            "v_cvt_pk_bf16_f32 v179, v150, v151\n\t"
            "v_mfma_f32_16x16x32_bf16 v[180:183], v[136:139], v[184:187], v[180:183]\n\t"
            "buffer_store_dwordx4 v[176:179], v198, s[40:43], s45 offen offset:64 sc1\n\t"
            "s_nop 1\n\t"
            "v_cvt_pk_bf16_f32 v156, v156, v157\n\t"
            "v_cvt_pk_bf16_f32 v157, v158, v159\n\t"
            "s_nop 2\n\t"
            "v_cvt_pk_bf16_f32 v158, v180, v181\n\t"
            "v_cvt_pk_bf16_f32 v159, v182, v183\n\t"
            "buffer_store_dwordx4 v[156:159], v198, s[40:43], s45 offen offset:128 sc1\n\t"
            "s_nop 7\n\t"
                    :: "{v[0:3]}"(W[0]), "{v[4:7]}"(W[1]), "{v[8:11]}"(W[2]), "{v[12:15]}"(W[3]), "{v[16:19]}"(W[4]), "{v[20:23]}"(W[5]), "{v[24:27]}"(W[6]), "{v[28:31]}"(W[7]), "{v[32:35]}"(W[8]), "{v[36:39]}"(W[9]), "{v[40:43]}"(W[10]), "{v[44:47]}"(W[11]), "{v[48:51]}"(W[12]), "{v[52:55]}"(W[13]), "{v[56:59]}"(W[14]), "{v[60:63]}"(W[15]), "{v[64:67]}"(W[16]), "{v[68:71]}"(W[17]), "{v[72:75]}"(W[18]), "{v[76:79]}"(W[19]), "{v[80:83]}"(W[20]), "{v[84:87]}"(W[21]), "{v[88:91]}"(W[22]), "{v[92:95]}"(W[23]), "{v[96:99]}"(W[24]), "{v[100:103]}"(W[25]), "{v[104:107]}"(W[26]), "{v[108:111]}"(W[27]), "{v[112:115]}"(W[28]), "{v[116:119]}"(W[29]), "{v[120:123]}"(W[30]), "{v[124:127]}"(W[31]), "{v[128:131]}"(W[32]), "{v[132:135]}"(W[33]), "{v[136:139]}"(W[34]), "{v[140:143]}"(W[35]), "{v[200:203]}"(xl[0]), "{v[204:207]}"(xl[1]), "{v[208:211]}"(xl[2]), "{v[212:215]}"(xl[3]), "{v[216:219]}"(xl[4]), "{v[220:223]}"(xl[5]), "{v[224:227]}"(xl[6]), "{v[228:231]}"(xl[7]), "{v[232:235]}"(xl[8]), "{v242}"(oE_), "{v243}"(oO_), "{v252}"(aCA_),
                       "{s[40:43]}"(orr), "{s45}"(t45), "{s56}"(t56), "{s55}"(CROW & 0x0F0F0F0Fu), "{s[34:35]}"(0x0001000100010001ull)
                    : "v144", "v145", "v146", "v147", "v148", "v149", "v150", "v151", "v152", "v153", "v154", "v155", "v156", "v157", "v158", "v159", "v160", "v161", "v162", "v163", "v164", "v165", "v166", "v167", "v168", "v169", "v170", "v171", "v172", "v173", "v174", "v175", "v176", "v177", "v178", "v179", "v180", "v181", "v182", "v183", "v184", "v185", "v186", "v187", "v188", "v189", "v190", "v191", "v192", "v193", "v194", "v195", "v196", "v197", "v198", "v199", "s36", "s37", "s57", "s58", "scc", "memory");
                }
                f32x4 acc[6];
#pragma unroll
                for (int ks = 0; ks < 6; ++ks) {
                    const bf16x8 of = *(const bf16x8*)(smem + ((ks & 1) ? (lE ^ 64) : lE) + (ks >> 1) * 128);
#pragma unroll
                    for (int j = 0; j < 6; ++j) acc[j] = MFMA32(W[j * 6 + ks], of, ks == 0 ? ZERO4 : acc[j]);
                }
#pragma unroll
                for (int k = 0; k < 3; ++k) {
                    const f32x4 a = acc[2 * k], b = acc[2 * k + 1];
                    const u32x4 ov = {pk2(a[0] + bflo(rl[k][0]), a[1] + bfhi(rl[k][0])), pk2(a[2] + bflo(rl[k][1]), a[3] + bfhi(rl[k][1])),
                                      pk2(b[0] + bflo(rl[k][2]), b[1] + bfhi(rl[k][2])), pk2(b[2] + bflo(rl[k][3]), b[3] + bfhi(rl[k][3]))};
                    __builtin_amdgcn_raw_buffer_store_b128(ov, orsrc, o + 64 * k, 0, SC1);
                }
                SB();
            }
        }
        __builtin_amdgcn_endpgm();
    }
}

extern "C" void kernel_launch(void* const* d_in, const int* in_sizes, int n_in,
                              void* d_out, int out_size, void* d_ws, size_t ws_size,
                              hipStream_t stream) {
    const unsigned short* x = (const unsigned short*)d_in[0];
    const unsigned short* dw = (const unsigned short*)d_in[1];
    const unsigned short* wqkv = (const unsigned short*)d_in[2];
    const unsigned short* wo = (const unsigned short*)d_in[3];
    unsigned short* out = (unsigned short*)d_out;
    u32x4* wf = (u32x4*)d_ws;
    win_attn_kernel<<<GRID, 512, 0, stream>>>(x, dw, wqkv, wo, wf, out);
}
```

```cpp
#include <hip/hip_runtime.h>
#include <hip/hip_bf16.h>
#include <stdint.h>

typedef short bf16x8 __attribute__((ext_vector_type(8)));
typedef float f32x4 __attribute__((ext_vector_type(4)));
typedef float f32x2 __attribute__((ext_vector_type(2)));
typedef unsigned u32x2 __attribute__((ext_vector_type(2)));
typedef unsigned u32x4 __attribute__((ext_vector_type(4)));

constexpr int IMG = 511;
constexpr int CH = 192;
constexpr int NWX = 73;
constexpr int NWIN = NWX * NWX;
constexpr int GRID = 256;
constexpr int ROWBYTES = IMG * CH * 2;
constexpr int XBIAS = (IMG + 1) * CH * 2;
constexpr unsigned OOB = 0x40000000u;

constexpr int FLAG_OFF = 7856;
constexpr int X_OFF = 32768;
constexpr int X1_OFF = 53632;
constexpr int O_OFF = 74496;
constexpr int V_OFF = 92928;
constexpr int KQL_OFF = 111360;
constexpr int OL_OFF = 128256;
constexpr int AT_OFF = 137472;
constexpr int CT_OFF = AT_OFF + 19584;
constexpr int RS_OFF = CT_OFF + 4096;
constexpr int RSX_OFF = 162816;
constexpr int WL_OFF = RS_OFF + 2304 + 64;
constexpr int LDS_BYTES = WL_OFF + 128;
#define RS_BASE (RS_OFF + wave * 384)
constexpr int STL_OFF = O_OFF + 12288;
constexpr int DW_OFF = O_OFF + 8448;
#define LX_OFF(i) ((i) < 2 ? CT_OFF + 1024 + (i) * 384 : KQL_OFF + ((i) - 2) * 800)
constexpr int PF_OFF = CT_OFF + 2048;
constexpr int PSLEEP = 4;
#define LB_OFF (w < 3 ? V_OFF + w * 13952 : X1_OFF)
#define LPOS(wc) (((wc) & 1) ? 0 : 6)
constexpr unsigned CROW = 0x060a0230u;
constexpr unsigned CLANE = 0x00132C3Fu;

constexpr unsigned long long TOKMAP[6] = {0x20001c190f2a2202ull, 0x052517121f061a13ull, 0x181e08151d0b260cull, 0x1b2b2903102d0901ull, 0x04162e0e2824142cull, 0x110a072127232f0dull};
constexpr unsigned long long ROWMAP[8] = {0x08321b240f1c0006ull, 0x23310e2a09170d14ull, 0x210c2f030a273418ull, 0x1d2b11221f043739ull, 0x3234363a103e192dull, 0x291a20022c3f1605ull, 0x303a3c2628122e15ull, 0x39253d1e01070b13ull};

constexpr int WF_BYTES = 8 * 36 * 1024;
constexpr int FLG_OFF = WF_BYTES;
constexpr int ARR_OFF = FLG_OFF + 4096;
constexpr int EPO_OFF = ARR_OFF + 2048;
constexpr int WS_BYTES = EPO_OFF + 128;
constexpr int SC1 = 16;

#define MFMA32(a, b, c) __builtin_amdgcn_mfma_f32_16x16x32_bf16((a), (b), (c), 0, 0, 0)
#define BC8(v) __builtin_bit_cast(bf16x8, (v))
#define BAR() asm volatile("s_waitcnt lgkmcnt(0)\n\ts_barrier" ::: "memory")
#define SB() __builtin_amdgcn_sched_barrier(0)
#define OPQ(x) asm volatile("" : "+v"(x))
#define ZERO4 ((f32x4){0.f, 0.f, 0.f, 0.f})

typedef __bf16 nbf16x2 __attribute__((ext_vector_type(2)));
__device__ __forceinline__ unsigned pk2(float a, float b) {
    f32x2 v = {a, b};
    nbf16x2 r = __builtin_convertvector(v, nbf16x2);
    return __builtin_bit_cast(unsigned, r);
}
__device__ __forceinline__ float bflo(unsigned u) { return __uint_as_float(u << 16); }
__device__ __forceinline__ float bfhi(unsigned u) { return __uint_as_float(u & 0xffff0000u); }

__device__ __forceinline__ float bf2f(unsigned short v) { return __uint_as_float((unsigned)v << 16); }
__device__ __forceinline__ unsigned byte_of(unsigned long long a, unsigned long long b, int i) {
    return (unsigned)(((i & 8) ? b : a) >> (8 * (i & 7))) & 0xFFu;
}
#define M24(a, b) ((unsigned)__mul24((int)(a), (int)(b)))
__device__ __forceinline__ void seq_to_win(int seq, int& wy, int& wx) {
    const bool last = seq >= 5110;
    const int s = last ? 35 : M24(seq, 28729) >> 22, r = seq - M24(146, s);
    const int kx = last ? M24(r, 171) >> 10 : r >> 2, pos = r - M24(last ? 6 : 4, kx);
    const bool low = last && pos >= (kx == 36 ? 2 : 4);
    wy = low ? 72 : 2 * s + (pos & 1);
    wx = 2 * kx + (low ? (kx == 36 ? 0 : pos - 4) : pos >> 1);
}
__device__ __forceinline__ int win_to_seq(int wy, int wx) {
    const int kx = wx >> 1;
    return M24(146, wy == 72 ? 35 : wy >> 1) + M24(wy >= 70 ? 6 : 4, kx) + (wy == 72 ? (kx == 36 ? 2 : 4 + (wx & 1)) : 2 * (wx & 1) + (wy & 1));
}
__device__ __forceinline__ int block_of(int wy, int wx) { return wy >= 70 ? 1295 + (wx & ~1) + (wy == 72) : M24(37, wy >> 1) + (wx >> 1); }
__device__ __forceinline__ void block_pos(int b, int& ky, int& kx) {
    const int s = M24(b, 1772) >> 16, l = b - 1295;
    kx = l >= 0 ? l >> 1 : b - M24(37, s);
    ky = l >= 0 ? 70 + 2 * (l & 1) : 2 * s;
}
__device__ __forceinline__ unsigned win_org(int wy, int wx) { return M24(wy, 7 * ROWBYTES) + M24(wx, 7 * CH * 2); }
__device__ __forceinline__ unsigned wl_entry(int wy, int wx) {
    return win_org(wy, wx) | (wy == 0 ? 0x08000000u : 0u) | (wx == 0 ? 0x10000000u : 0u) |
           (wx == NWX - 1 ? 0x20000000u : 0u) | ((unsigned)(wx & 1) << 30) | ((unsigned)(wy & 1) << 31);
}
__device__ __forceinline__ unsigned dma_off(int lane) {
    const int col = (lane * 43) >> 8;
    return (unsigned)(col * (CH * 2 - 96) + 16 * lane) | (col == 0 ? 0x10000000u : 0u) | (col == 8 ? 0x20000000u : 0u);
}

#if defined(__HIP_DEVICE_COMPILE__)
#define NOPK __attribute__((target("no-packed-fp32-ops")))
#else
#define NOPK
#endif
__global__ __launch_bounds__(512, 2) NOPK void win_attn_kernel(const unsigned short* __restrict__ x,
                                                          const unsigned short* __restrict__ dw, const unsigned short* __restrict__ wqkv,
                                                          const unsigned short* __restrict__ wo, u32x4* wf,
                                                          unsigned short* __restrict__ out) {
    __shared__ __attribute__((aligned(16))) char smem[LDS_BYTES];

    asm volatile("wk_code_begin:");
    const int tid = threadIdx.x;
    const int wave = __builtin_amdgcn_readfirstlane(tid >> 6);
    const int w = wave & 3;
    const unsigned ldsb = (unsigned)(size_t)smem;
    {
        unsigned long long pc;
        unsigned len, rel;
        asm volatile("s_getpc_b64 %0\n.Lwk_here%=:\n\ts_mov_b32 %1, wk_code_end-wk_code_begin\n\ts_mov_b32 %2, .Lwk_here%=-wk_code_begin" : "=s"(pc), "=s"(len), "=s"(rel));
        pc -= rel;
        const u32x4 cr = {(unsigned)pc, (unsigned)(pc >> 32) & 0xffffu, len, 0x00020000u};
        asm volatile("s_mov_b32 m0, %0\n\ts_nop 0\n\tbuffer_load_dwordx4 %1, %2, %3 offen lds"
                     :: "s"(ldsb + X1_OFF + 14336 + w * 1024), "v"((tid & 63) * 16), "s"(cr), "s"((wave * 32 + (blockIdx.x >> 3)) * 1024) : "m0", "memory");
    }
    *(unsigned*)(smem + PF_OFF + tid * 4) = 0x9E3779B9u * (tid + 1);
    *(unsigned*)(smem + (tid < 192 ? AT_OFF + 18816 + tid * 4 : FLAG_OFF + (tid & 3) * 4)) = 0u;
    BAR();
    const unsigned flg = FLG_OFF + (blockIdx.x & 7) * 512;
    asm volatile("" :: "s"(x), "s"(dw), "s"(wqkv), "s"(wo), "s"(wf));
    const auto wrsrc = __builtin_amdgcn_make_buffer_rsrc((void*)wf, 0, WS_BYTES, 0x00020000);
    unsigned tag_lo = 0, tag_hi = 0;
#define TAG_READY                                                                                  \
    {                                                                                              \
        unsigned long long tag_;                                                                   \
        asm volatile("s_load_dwordx2 %0, %1, %2\n\ts_waitcnt lgkmcnt(0)" : "=s"(tag_) : "s"(wf), "n"(EPO_OFF) : "memory"); \
        tag_lo = (unsigned)tag_ + 1u; tag_hi = (unsigned)(tag_ >> 32);                             \
    }
    const int pb = blockIdx.x;
    if (pb < 36) {
        const int lane = tid & 63, n = lane & 15, g = lane >> 4;
        const bool isO = wave >= 6;
        const int fr = isO ? 216 + 2 * pb + (wave - 6) : 6 * pb + wave;
        const int tile = fr / 6, ks = fr - 6 * tile, tw = tile / 6, mdt = tile - 6 * tw;
        const int col = isO ? 96 * (tw - 6) + 32 * (mdt >> 1) + 8 * (n >> 2) + 4 * (mdt & 1) + (n & 3)
                            : (tw < 4 ? (mdt < 3 ? 192 : -48) + 48 * tw : 96 * tw) + 16 * mdt + n;
        const int rb = isO ? 384 : 1152;
        const auto rr = __builtin_amdgcn_make_buffer_rsrc((void*)(isO ? wo : wqkv), 0, 192 * rb, 0x00020000);
        const int vo = (48 * (g >> 1) + 8 * (g & 1)) * rb + 2 * col;
        float v[8];
#pragma unroll
        for (int j = 0; j < 8; ++j) v[j] = bf2f(__builtin_amdgcn_raw_buffer_load_b16(rr, vo, (96 * (ks & 1) + 16 * (ks >> 1) + j) * rb, 0));
        TAG_READY
        float* red = (float*)smem;
        red[n * 36 + wave * 4 + g] = ((v[0] + v[1]) + (v[2] + v[3])) + ((v[4] + v[5]) + (v[6] + v[7]));
        BAR();
        float mean = 0.f;
#pragma unroll
        for (int i = 0; i < 6; ++i) {
            const f32x4 p = *(const f32x4*)(red + n * 36 + 4 * i);
            mean += (p[0] + p[1]) + (p[2] + p[3]);
        }
        mean = isO ? 0.f : mean * (1.f / 192.f);
        u32x4 o;
#pragma unroll
        for (int j = 0; j < 4; ++j) o[j] = pk2(v[2 * j] - mean, v[2 * j + 1] - mean);
        __builtin_amdgcn_raw_buffer_store_b128(o, wrsrc, lane * 16, fr * 1024, SC1);
        asm volatile("s_waitcnt vmcnt(0)" ::: "memory");
        BAR();
        if (tid < 8) __builtin_amdgcn_raw_buffer_store_b64((u32x2){tag_lo, tag_hi}, wrsrc, FLG_OFF + tid * 512 + pb * 8, 0, SC1);
    }
    const int xi = blockIdx.x >> 3, nshort = (int)(0x0506060606060606ull >> (8 * (blockIdx.x & 7))) & 63;
    const int start = 672 * (blockIdx.x & 7) - ((int)(0x2a241e18120c0600ull >> (8 * (blockIdx.x & 7))) & 63) + 21 * xi - (xi < nshort ? xi : nshort);
    const int cnt = xi < nshort ? 20 : 21;

    const auto xrsrc = __builtin_amdgcn_make_buffer_rsrc((void*)((const char*)x - XBIAS), 0, IMG * IMG * CH * 2 + XBIAS, 0x00020000);
#define POLL_ISSUE                                                                                  \
    asm volatile("s_mov_b32 m0, %0\n\ts_mov_b64 exec, 0x3ffff\n\tbuffer_load_dwordx4 %1, %2, %3 offen sc1 lds\n\ts_mov_b64 exec, -1" \
                 :: "s"(ldsb + PF_OFF), "v"(lane * 16), "s"(wrsrc), "s"(flg) : "m0", "memory");
#define POLL_W                                                                                      \
    {                                                                                               \
        const int fi_ = lane < 36 ? lane : 0;                                                       \
        for (int it_ = 0; it_ < 100000; ++it_) {                                                    \
            u32x2 f_;                                                                               \
            if (it_ < 48) asm volatile("ds_read_b64 %0, %1\n\ts_waitcnt lgkmcnt(0)" : "=v"(f_) : "v"(ldsb + PF_OFF + fi_ * 8) : "memory"); \
            else f_ = __builtin_amdgcn_raw_buffer_load_b64(wrsrc, fi_ * 8, flg, SC1);        \
            if (__builtin_amdgcn_ballot_w64(f_[0] != tag_lo || f_[1] != tag_hi) == 0ull) break;     \
            if (it_ < 47 && wave == 0) POLL_ISSUE                                                              \
            __builtin_amdgcn_s_sleep(PSLEEP);                                                       \
        }                                                                                           \
        *(u32x2*)(smem + PF_OFF + 512) = (u32x2){tag_lo, tag_hi};                                   \
        __builtin_amdgcn_fence(__ATOMIC_ACQUIRE, "wavefront");                                      \
        SB();                                                                                       \
    }
#define LANE_SETUP                                                                                 \
    int lane;                                                                                      \
    asm volatile("v_mbcnt_lo_u32_b32 %0, -1, 0\n\tv_mbcnt_hi_u32_b32 %0, -1, %0" : "=v"(lane));    \
    __builtin_assume(lane >= 0 && lane < 64);                                                      \
    const int g = lane >> 4, l15 = lane & 15, sw = l15 >> 1;                                       \
    int fE = X_OFF + l15 * 384 + ((g ^ sw) << 4);                                                  \
    OPQ(fE);

#define DMA_SLICE(en)                                                                              \
        {                                                                                          \
            const unsigned vo_ = dma_off(lane) & (0x401fffffu | ((en) & 0x38000000u));             \
            const unsigned so_ = ((en) & 0x7ffffffu) + 96 * w;                                     \
            _Pragma("unroll") for (int r = 0; r < 9; ++r) {                                        \
                unsigned m0_ = ldsb + w * 8192 + r * 872;                                          \
                unsigned sr_ = (r == 0 && ((en) & 0x08000000u)) ? OOB : so_ + r * ROWBYTES;        \
                asm volatile("s_mov_b32 m0, %0\n\ts_mov_b32 exec_hi, 0x3fffff\n\tbuffer_load_dwordx4 %1, %2, %3 offen lds\n\ts_mov_b32 exec_hi, -1" \
                             :: "s"(m0_), "v"(vo_), "s"(xrsrc), "s"(sr_) : "m0", "memory");        \
            }                                                                                      \
        }
#define XPAT(r) (((r) < 48 ? (r) : 48) * 384 + (((2 * w) ^ (((r) >> 1) & 7)) << 4))
#define SPAT(r) ((w >> 1) * 1024 + (w & 1) * 8 + (r) * 16)
#define CONV_CONSTS(var) CONV_ROWS(var) CONV_OPS
#define CONV_ROWS(var) \
            const unsigned long long rlo = g == 0 ? ROWMAP[0] : (g == 1 ? ROWMAP[2] : (g == 2 ? ROWMAP[4] : ROWMAP[6])); \
            const unsigned long long rhi = g == 0 ? ROWMAP[1] : (g == 1 ? ROWMAP[3] : (g == 2 ? ROWMAP[5] : ROWMAP[7])); \
            int xrow = byte_of(rlo, rhi, l15); \
            if (lane == (int)((CLANE >> (8 * (var))) & 0xFF)) xrow = 48; \
            if (lane == 63) xrow = (CROW >> (8 * (var))) & 0xFF; \
            const unsigned pXw_ = XPAT(xrow); \
            const unsigned pSTw_ = (w >> 1) * 1024 + (w & 1) * 8 + xrow * 16; \
            const int oy = (0x5320 >> (4 * (l15 >> 2))) & 15, ox = (0x5320 >> (4 * (l15 & 3))) & 15; \
            const unsigned aB_ = ldsb + w * 8192 + (oy + (g >> 1)) * 872 + (ox + 2 * (g & 1)) * 96;
#define CONV_OPS \
            const int dyi = g - (l15 >> 3); \
            const unsigned aA_ = ldsb + AT_OFF + (l15 & 3) * 16 + \
                                 ((dyi < 0 || dyi > 2) ? 18816 : (dyi == 0 ? 0 : (dyi == 1 ? 6208 : 12544)) + ((l15 >> 2) & 1) * 3072 + w * 768); \
            const unsigned idx_ = (l15 & 3) * 0x5555u; \
            const unsigned se = (l15 == 4 * g) ? 0x3F803F80u : 0u; \
            const u32x4 sel4 = {se, se, se, se};

    unsigned en0, en1;
    {
        LANE_SETUP
        const int px = (lane * 43) >> 8;

        int wy, wx;
        seq_to_win(start + (lane < cnt ? lane : cnt - 1), wy, wx);
        const unsigned en = wl_entry(wy, wx);
        en0 = __builtin_amdgcn_readlane(en, 0);
        en1 = __builtin_amdgcn_readlane(en, 1);
        unsigned cB, cX, cS;
        if (wave >= 4) {
            const int bk = block_of(wy, wx);
            const int key0 = __builtin_amdgcn_readlane(bk, 0), nblk = __builtin_amdgcn_readlane(bk, 63) - key0 + 1;
            const int b = l15 < nblk ? l15 : 0;
            int ky, kx;
            block_pos(key0 + b, ky, kx);
            const unsigned org = l15 < nblk ? (win_org(ky, 2 * kx) + (5 * ROWBYTES + 5 * CH * 2 + XBIAS)) | (kx == 36 ? OOB : 0u) : OOB + 0x8000000u;
            const unsigned lo = (px & 3) * (CH * 2) + (lane - 6 * px) * 16, keep = (px & 2) ? ~0u : ~OOB;
#pragma unroll
            for (int p = 0; p < 4; ++p) {
                if (p < 3 || nblk > 6) {
                    const unsigned oa = __builtin_amdgcn_readlane(org, 2 * p), ob = __builtin_amdgcn_readlane(org, 2 * p + 1);
                    const unsigned vo_ = ((lane < 24 ? oa : ob) & keep) + lo;
#pragma unroll
                    for (int r = 0; r < 4; ++r) {
                        unsigned m0_ = ldsb + LB_OFF + (4 * p + r) * 872;
                        asm volatile("s_mov_b32 m0, %0\n\ts_mov_b32 exec_hi, 0xffff\n\tbuffer_load_dwordx4 %1, %2, %3 offen lds\n\ts_mov_b32 exec_hi, -1"
                                     :: "s"(m0_), "v"(vo_), "s"(xrsrc), "s"(96 * w + r * ROWBYTES) : "m0", "memory");
                    }
                }
            }
            SB();
            const int ly = ky + (g >> 1), lx = 2 * kx + (g & 1), li = win_to_seq(ly, lx) - start;
            const bool ok = ly < NWX && lx < NWX && li >= 0 && li < cnt && l15 < nblk;
            const int row = ok ? li : 47;
            cB = ldsb + LB_OFF + (4 * (b >> 1) + (g >> 1)) * 872 + (b & 1) * 384 + (g & 1) * 192;
            cX = XPAT(row);
            cS = SPAT(row) + (unsigned)(STL_OFF - O_OFF - 18816);
            for (int it_ = 0; it_ < 100000; ++it_) {
                unsigned n_;
                asm volatile("ds_read_b32 %0, %1\n\ts_waitcnt lgkmcnt(0)" : "=v"(n_) : "v"(ldsb + FLAG_OFF + 8) : "memory");
                if (__builtin_amdgcn_readfirstlane(n_) >= 4) break;
                __builtin_amdgcn_s_sleep(1);
            }
        } else {
            {
                const auto dr = __builtin_amdgcn_make_buffer_rsrc((void*)dw, 0, 9 * 384, 0x00020000);
                unsigned m0_ = ldsb + DW_OFF + w * 864, vo_ = px * 288 + lane * 16;
                asm volatile("s_mov_b32 m0, %0\n\ts_mov_b32 exec_hi, 0x3fffff\n\tbuffer_load_dwordx4 %1, %2, %3 offen lds\n\ts_mov_b32 exec_hi, -1"
                             :: "s"(m0_), "v"(vo_), "s"(dr), "s"(96 * w) : "m0", "memory");
            }
            DMA_SLICE(en0)
            SB();
            if (wave == 2) {
                u32x4 so;
#pragma unroll
                for (int e = 0; e < 3; ++e) {
                    const int tk = byte_of(TOKMAP[2 * e], TOKMAP[2 * e + 1], l15);
                    const int ty = (tk * 37) >> 8, tx = tk - 7 * ty;
                    so[e] = (unsigned)(ty * ROWBYTES + tx * (CH * 2)) + 16 * g;
                }
                so[3] = dma_off(lane);
                *(u32x4*)(smem + CT_OFF + lane * 16) = so;
            }
            if (wave == 1 && lane < 32) *(unsigned*)(smem + WL_OFF + lane * 4) = lane < cnt ? en : 0x7ffffffu;
            CONV_ROWS(en0 >> 30)
            cB = aB_;
            cX = lane == (int)((CLANE >> (8 * (en0 >> 30))) & 0xFF) ? LX_OFF(0) - X_OFF + 32 * w : pXw_;
            cS = pSTw_;
            TAG_READY
            asm volatile("s_waitcnt vmcnt(9)" ::: "memory");
            if (lane < 48) {
                unsigned short dv[9];
#pragma unroll
                for (int i = 0; i < 9; ++i) dv[i] = *(const unsigned short*)(smem + DW_OFF + w * 864 + i * 96 + lane * 2);
#pragma unroll
                for (int dy = 0; dy < 3; ++dy) {
                    const unsigned a = dv[3 * dy], b2 = dv[3 * dy + 1] | (dy == 1 ? 0x3F800000u : 0u), c = dv[3 * dy + 2];
                    char* p = smem + AT_OFF + (dy == 0 ? 0 : (dy == 1 ? 6208 : 12544)) + w * 768 + lane * 16;
                    *(u32x4*)p = (u32x4){a, b2, c, 0u};
                    *(u32x4*)(p + 3072) = (u32x4){0u, a, b2, c};
                }
            }
            asm volatile("s_mov_b64 exec, 1\n\tds_add_u32 %0, %1\n\ts_mov_b64 exec, -1" :: "v"(ldsb + FLAG_OFF + 8), "v"(1) : "memory");
        }
        CONV_OPS
        asm volatile("s_waitcnt vmcnt(0) lgkmcnt(0)" ::: "memory");
        if (wave == 0) POLL_ISSUE
        asm volatile(
            "s_mov_b32 m0, s73\n\t"
            "s_cmp_eq_u32 s73, 0\n\t"
            "s_nop 7\n\t"
            "ds_read_b128 v[168:171], v254\n\t"
            "ds_read_b64 v[144:145], v253\n\t"
            "ds_read_b64 v[146:147], v253 offset:96\n\t"
            "ds_read_b64 v[148:149], v253 offset:1744\n\t"
            "ds_read_b64 v[150:151], v253 offset:1840\n\t"
            "ds_read_b128 v[172:175], v254 offset:64\n\t"
            "ds_read_b64 v[152:153], v253 offset:8\n\t"
            "ds_read_b64 v[154:155], v253 offset:104\n\t"
            "ds_read_b64 v[156:157], v253 offset:1752\n\t"
            "ds_read_b64 v[158:159], v253 offset:1848\n\t"
            "ds_read_b64 v[160:161], v253 offset:16\n\t"
            "ds_read_b64 v[162:163], v253 offset:112\n\t"
            "ds_read_b64 v[164:165], v253 offset:1760\n\t"
            "ds_read_b64 v[166:167], v253 offset:1856\n\t"
            "v_mov_b64 v[176:177], 0\n\t"
            "v_mov_b64 v[178:179], 0\n\t"
            "v_mov_b64 v[180:181], 0\n\t"
            "v_mov_b64 v[182:183], 0\n\t"
            "v_mov_b64 v[184:185], 0\n\t"
            "v_mov_b64 v[186:187], 0\n\t"
            "v_mov_b64 v[188:189], 0\n\t"
            "v_mov_b64 v[190:191], 0\n\t"
            "v_add_u32 v236, s67, v250\n\t"
            "v_xor_b32 v237, 16, v236\n\t"
            "v_add_u32 v248, s67, v251\n\t"
            "s_waitcnt lgkmcnt(9)\n\t"
            "v_smfmac_f32_16x16x64_bf16 v[176:179], v[168:171], v[144:151], v255\n\t"
            "ds_read_b128 v[168:171], v254 offset:128\n\t"
            "ds_read_b64 v[144:145], v253 offset:24\n\t"
            "s_waitcnt lgkmcnt(6)\n\t"
            "v_smfmac_f32_16x16x64_bf16 v[180:183], v[172:175], v[152:159], v255\n\t"
            "ds_read_b64 v[146:147], v253 offset:120\n\t"
            "ds_read_b64 v[148:149], v253 offset:1768\n\t"
            "ds_read_b64 v[150:151], v253 offset:1864\n\t"
            "ds_read_b128 v[172:175], v254 offset:192\n\t"
            "ds_read_b64 v[152:153], v253 offset:32\n\t"
            "ds_read_b64 v[154:155], v253 offset:128\n\t"
            "ds_read_b64 v[156:157], v253 offset:1776\n\t"
            "ds_read_b64 v[158:159], v253 offset:1872\n\t"
            "v_cvt_pk_bf16_f32 v176, v176, v177\n\t"
            "v_cvt_pk_bf16_f32 v177, v178, v179\n\t"
            "v_cvt_pk_bf16_f32 v178, v180, v181\n\t"
            "v_cvt_pk_bf16_f32 v179, v182, v183\n\t"
            "v_mfma_f32_4x4x4_16b_bf16 v[196:199], v[176:177], v[176:177], 0\n\t"
            "ds_write_b128 v236, v[176:179]\n\t"
            "v_mfma_f32_16x16x32_bf16 v[192:195], v[238:241], v[176:179], 0\n\t"
            "v_mov_b64 v[176:177], 0\n\t"
            "v_mov_b64 v[180:181], 0\n\t"
            "v_mfma_f32_4x4x4_16b_bf16 v[196:199], v[178:179], v[178:179], v[196:199]\n\t"
            "v_mov_b64 v[178:179], 0\n\t"
            "v_mov_b64 v[182:183], 0\n\t"
            "s_waitcnt lgkmcnt(10)\n\t"
            "v_smfmac_f32_16x16x64_bf16 v[184:187], v[168:171], v[160:167], v255\n\t"
            "ds_read_b128 v[168:171], v254 offset:256\n\t"
            "ds_read_b64 v[160:161], v253 offset:40\n\t"
            "ds_read_b64 v[162:163], v253 offset:136\n\t"
            "ds_read_b64 v[164:165], v253 offset:1784\n\t"
            "ds_read_b64 v[166:167], v253 offset:1880\n\t"
            "s_waitcnt lgkmcnt(10)\n\t"
            "v_smfmac_f32_16x16x64_bf16 v[188:191], v[172:175], v[144:151], v255\n\t"
            "ds_read_b128 v[172:175], v254 offset:320\n\t"
            "ds_read_b64 v[144:145], v253 offset:48\n\t"
            "ds_read_b64 v[146:147], v253 offset:144\n\t"
            "s_cbranch_scc1 .Lwk_np1%=\n\t"
            "s_mov_b64 exec, 0x3ffff\n\t"
            "buffer_load_dwordx4 v252, s[68:71], s72 offen sc1 lds\n\t"
            "s_mov_b64 exec, -1\n\t"
            ".Lwk_np1%=:\n\t"
            "ds_read_b64 v[148:149], v253 offset:1792\n\t"
            "ds_read_b64 v[150:151], v253 offset:1888\n\t"
            "v_cvt_pk_bf16_f32 v184, v184, v185\n\t"
            "v_cvt_pk_bf16_f32 v185, v186, v187\n\t"
            "s_nop 0\n\t"
            "v_cvt_pk_bf16_f32 v186, v188, v189\n\t"
            "v_cvt_pk_bf16_f32 v187, v190, v191\n\t"
            "v_mfma_f32_4x4x4_16b_bf16 v[196:199], v[184:185], v[184:185], v[196:199]\n\t"
            "s_waitcnt lgkmcnt(14)\n\t"
            "ds_write_b128 v237, v[184:187]\n\t"
            "v_mfma_f32_16x16x32_bf16 v[192:195], v[238:241], v[184:187], v[192:195]\n\t"
            "v_mov_b64 v[184:185], 0\n\t"
            "v_mov_b64 v[188:189], 0\n\t"
            "v_mfma_f32_4x4x4_16b_bf16 v[196:199], v[186:187], v[186:187], v[196:199]\n\t"
            "v_mov_b64 v[186:187], 0\n\t"
            "v_mov_b64 v[190:191], 0\n\t"
            "s_waitcnt lgkmcnt(10)\n\t"
            "v_smfmac_f32_16x16x64_bf16 v[176:179], v[168:171], v[152:159], v255\n\t"
            "ds_read_b128 v[168:171], v254 offset:384\n\t"
            "ds_read_b64 v[152:153], v253 offset:56\n\t"
            "ds_read_b64 v[154:155], v253 offset:152\n\t"
            "ds_read_b64 v[156:157], v253 offset:1800\n\t"
            "ds_read_b64 v[158:159], v253 offset:1896\n\t"
            "s_waitcnt lgkmcnt(10)\n\t"
            "v_smfmac_f32_16x16x64_bf16 v[180:183], v[172:175], v[160:167], v255\n\t"
            "ds_read_b128 v[172:175], v254 offset:448\n\t"
            "ds_read_b64 v[160:161], v253 offset:64\n\t"
            "ds_read_b64 v[162:163], v253 offset:160\n\t"
            "ds_read_b64 v[164:165], v253 offset:1808\n\t"
            "ds_read_b64 v[166:167], v253 offset:1904\n\t"
            "v_cvt_pk_bf16_f32 v176, v176, v177\n\t"
            "v_cvt_pk_bf16_f32 v177, v178, v179\n\t"
            "s_nop 0\n\t"
            "v_cvt_pk_bf16_f32 v178, v180, v181\n\t"
            "v_cvt_pk_bf16_f32 v179, v182, v183\n\t"
            "v_mfma_f32_4x4x4_16b_bf16 v[196:199], v[176:177], v[176:177], v[196:199]\n\t"
            "s_waitcnt lgkmcnt(14)\n\t"
            "ds_write_b128 v236, v[176:179] offset:128\n\t"
            "v_mfma_f32_16x16x32_bf16 v[192:195], v[238:241], v[176:179], v[192:195]\n\t"
            "v_mov_b64 v[176:177], 0\n\t"
            "v_mov_b64 v[180:181], 0\n\t"
            "v_mfma_f32_4x4x4_16b_bf16 v[196:199], v[178:179], v[178:179], v[196:199]\n\t"
            "v_mov_b64 v[178:179], 0\n\t"
            "v_mov_b64 v[182:183], 0\n\t"
            "s_waitcnt lgkmcnt(10)\n\t"
            "v_smfmac_f32_16x16x64_bf16 v[184:187], v[168:171], v[144:151], v255\n\t"
            "ds_read_b128 v[168:171], v254 offset:512\n\t"
            "ds_read_b64 v[144:145], v253 offset:72\n\t"
            "ds_read_b64 v[146:147], v253 offset:168\n\t"
            "ds_read_b64 v[148:149], v253 offset:1816\n\t"
            "ds_read_b64 v[150:151], v253 offset:1912\n\t"
            "s_waitcnt lgkmcnt(10)\n\t"
            "v_smfmac_f32_16x16x64_bf16 v[188:191], v[172:175], v[152:159], v255\n\t"
            "ds_read_b128 v[172:175], v254 offset:576\n\t"
            "ds_read_b64 v[152:153], v253 offset:80\n\t"
            "ds_read_b64 v[154:155], v253 offset:176\n\t"
            "ds_read_b64 v[156:157], v253 offset:1824\n\t"
            "ds_read_b64 v[158:159], v253 offset:1920\n\t"
            "v_cvt_pk_bf16_f32 v184, v184, v185\n\t"
            "v_cvt_pk_bf16_f32 v185, v186, v187\n\t"
            "s_cbranch_scc1 .Lwk_np0%=\n\t"
            "s_mov_b64 exec, 0x3ffff\n\t"
            "buffer_load_dwordx4 v252, s[68:71], s72 offen sc1 lds\n\t"
            "s_mov_b64 exec, -1\n\t"
            ".Lwk_np0%=:\n\t"
            "s_nop 0\n\t"
            "v_cvt_pk_bf16_f32 v186, v188, v189\n\t"
            "v_cvt_pk_bf16_f32 v187, v190, v191\n\t"
            "v_mfma_f32_4x4x4_16b_bf16 v[196:199], v[184:185], v[184:185], v[196:199]\n\t"
            "s_waitcnt lgkmcnt(14)\n\t"
            "ds_write_b128 v237, v[184:187] offset:128\n\t"
            "v_mfma_f32_16x16x32_bf16 v[192:195], v[238:241], v[184:187], v[192:195]\n\t"
            "v_mov_b64 v[184:185], 0\n\t"
            "v_mov_b64 v[188:189], 0\n\t"
            "v_mfma_f32_4x4x4_16b_bf16 v[196:199], v[186:187], v[186:187], v[196:199]\n\t"
            "v_mov_b64 v[186:187], 0\n\t"
            "v_mov_b64 v[190:191], 0\n\t"
            "s_waitcnt lgkmcnt(10)\n\t"
            "v_smfmac_f32_16x16x64_bf16 v[176:179], v[168:171], v[160:167], v255\n\t"
            "ds_read_b128 v[168:171], v254 offset:640\n\t"
            "ds_read_b64 v[160:161], v253 offset:88\n\t"
            "ds_read_b64 v[162:163], v253 offset:184\n\t"
            "ds_read_b64 v[164:165], v253 offset:1832\n\t"
            "ds_read_b64 v[166:167], v253 offset:1928\n\t"
            "s_waitcnt lgkmcnt(10)\n\t"
            "v_smfmac_f32_16x16x64_bf16 v[180:183], v[172:175], v[144:151], v255\n\t"
            "ds_read_b128 v[172:175], v254 offset:704\n\t"
            "v_cvt_pk_bf16_f32 v176, v176, v177\n\t"
            "v_cvt_pk_bf16_f32 v177, v178, v179\n\t"
            "s_nop 1\n\t"
            "v_mfma_f32_4x4x4_16b_bf16 v[196:199], v[176:177], v[176:177], v[196:199]\n\t"
            "s_nop 1\n\t"
            "v_cvt_pk_bf16_f32 v178, v180, v181\n\t"
            "v_cvt_pk_bf16_f32 v179, v182, v183\n\t"
            "s_nop 1\n\t"
            "v_mfma_f32_4x4x4_16b_bf16 v[196:199], v[178:179], v[178:179], v[196:199]\n\t"
            "ds_write_b128 v236, v[176:179] offset:256\n\t"
            "v_mfma_f32_16x16x32_bf16 v[192:195], v[238:241], v[176:179], v[192:195]\n\t"
            "s_waitcnt lgkmcnt(6)\n\t"
            "v_smfmac_f32_16x16x64_bf16 v[184:187], v[168:171], v[152:159], v255\n\t"
            "s_waitcnt lgkmcnt(1)\n\t"
            "v_smfmac_f32_16x16x64_bf16 v[188:191], v[172:175], v[160:167], v255\n\t"
            "s_nop 5\n\t"
            "v_cvt_pk_bf16_f32 v184, v184, v185\n\t"
            "v_cvt_pk_bf16_f32 v185, v186, v187\n\t"
            "s_nop 1\n\t"
            "v_mfma_f32_4x4x4_16b_bf16 v[196:199], v[184:185], v[184:185], v[196:199]\n\t"
            "v_cvt_pk_bf16_f32 v186, v188, v189\n\t"
            "v_cvt_pk_bf16_f32 v187, v190, v191\n\t"
            "s_nop 1\n\t"
            "v_mfma_f32_4x4x4_16b_bf16 v[196:199], v[186:187], v[186:187], v[196:199]\n\t"
            "v_mfma_f32_16x16x32_bf16 v[192:195], v[238:241], v[184:187], v[192:195]\n\t"
            "ds_write_b128 v237, v[184:187] offset:256\n\t"
            "s_nop 6\n\t"
            "v_cndmask_b32_e64 v193, v196, v197, s[60:61]\n\t"
            "v_cndmask_b32_e64 v193, v193, v198, s[62:63]\n\t"
            "v_cndmask_b32_e64 v193, v193, v199, s[64:65]\n\t"
            "ds_write_b64 v248, v[192:193] offset:18816\n\t"
            "s_waitcnt lgkmcnt(0)\n\t"
            "s_nop 7\n\t"
            :
            : "{v250}"(cX), "{v251}"(cS), "{v253}"(cB), "{v254}"(aA_), "{v255}"(idx_), "{v[238:241]}"(sel4), "{s67}"(ldsb + (wave < 4 ? X_OFF : O_OFF)),
              "{v252}"(lane * 16), "{s[68:71]}"(wrsrc), "{s72}"(flg), "{s73}"(wave == 0 ? ldsb + PF_OFF : 0u),
              "{s[60:61]}"(0x2222222222222222ull), "{s[62:63]}"(0x4444444444444444ull), "{s[64:65]}"(0x8888888888888888ull)
            : "v144", "v145", "v146", "v147", "v148", "v149", "v150", "v151", "v152", "v153", "v154", "v155", "v156", "v157", "v158", "v159", "v160", "v161", "v162", "v163", "v164", "v165", "v166", "v167", "v168", "v169", "v170", "v171", "v172", "v173", "v174", "v175", "v176", "v177", "v178", "v179", "v180", "v181", "v182", "v183", "v184", "v185", "v186", "v187", "v188", "v189", "v190", "v191", "v192", "v193", "v194", "v195", "v196", "v197", "v198", "v199", "v236", "v237", "v248", "v249", "m0", "scc", "memory");
        if (wave < 4) {
            POLL_W
        }
    }
    const unsigned long long mrow = 0x0001000100010001ull * ((1u << (cnt - 16)) - 1u);
#define KARGS                                                                                      \
    unsigned long long xb, wb, ob;                                                                 \
    asm volatile("s_load_dwordx2 %0, %3, 0x0\n\ts_load_dwordx2 %1, %3, 0x20\n\ts_load_dwordx2 %2, %3, 0x28\n\ts_waitcnt lgkmcnt(0)" \
                 : "=&s"(xb), "=&s"(wb), "=&s"(ob) : "s"(__builtin_amdgcn_kernarg_segment_ptr()) : "memory"); \
    xb -= XBIAS;                                                                                   \
    const u32x4 wr = {(unsigned)wb, (unsigned)(wb >> 32) & 0xffffu, (unsigned)WS_BYTES, 0x00020000u}; \
    const u32x4 orr = {(unsigned)ob, (unsigned)(ob >> 32) & 0xffffu, (unsigned)(IMG * IMG * CH * 2), 0x00020000u}; \
    const u32x4 xr = {(unsigned)xb, (unsigned)(xb >> 32) & 0xffffu, (unsigned)(IMG * IMG * CH * 2 + XBIAS), 0x00020000u};
    const int tk_ = (xi + 4) / 6;
    const unsigned touch = wave * 36864 + ((xi + 4 - 6 * tk_) * 6 + tk_) * 1024;
#define B_SCALARS \
            CONV_CONSTS(0) \
            const unsigned aCA_ = ldsb + CT_OFF + lane * 16; \
            KARGS \
            unsigned s45 = OOB, s46 = (en0 & 0x7ffffffu) + 96 * w, s47 = (en1 & 0x7ffffffu) + 96 * w; \
            unsigned s48 = ldsb + X_OFF, s67 = ldsb + X1_OFF, s76 = ldsb + WL_OFF + 8, s70 = 0; \
            unsigned s56 = __builtin_amdgcn_readfirstlane((en0 >> 30) * 4 + (en1 >> 30)); \
            unsigned s69 = 0x401fffffu | (en1 & 0x38000000u), s79 = (en1 & 0x08000000u) ? OOB : s47, s59 = ldsb + LX_OFF(1) + 32 * w; \
            unsigned long long wbase = wb + 6 * 36864; \
            unsigned wlen = 2 * 36864; \
            if (wave >= 6) { \
                unsigned rel; \
                asm volatile("s_getpc_b64 %0\n.Lwk_h2%=:\n\ts_mov_b32 %1, wk_code_end-wk_code_begin\n\ts_mov_b32 %2, .Lwk_h2%=-wk_code_begin" : "=s"(wbase), "=s"(wlen), "=s"(rel)); \
                wbase -= rel; \
            } \
            const unsigned wo_ = xi * 3456 + 96 + 96 * (w & 1); \
            wbase = (wbase + wo_) & 0xffffffffffffull; \
            wlen = __builtin_amdgcn_readfirstlane(wlen > wo_ ? wlen - wo_ : 0u); \
            unsigned long long xp = 0, sp = 0; \
            _Pragma("unroll") for (int v = 0; v < 4; ++v) { \
                const unsigned r = (CROW >> (8 * v)) & 0xFF; \
                xp |= (unsigned long long)XPAT(r) << (16 * v); \
                sp |= (unsigned long long)SPAT(r) << (16 * v); \
            }
#define B_SIO "+{s45}"(s45), "+{s46}"(s46), "+{s47}"(s47), "+{s48}"(s48), "+{s67}"(s67), "+{s76}"(s76), "+{s56}"(s56), "+{s70}"(s70), "+{s69}"(s69), "+{s79}"(s79), "+{s59}"(s59)
#define B_SIN "{v[238:241]}"(sel4), "{s[40:43]}"(orr), "{s[72:75]}"(xr), "{s39}"(0x401fffffu), "{s[28:31]}"(wr), "{s33}"(wave * 36864), "{s78}"(96 * w), \
              "{s27}"(96 * w - 384), "{s[50:51]}"(xp), "{s[52:53]}"(sp), "{s54}"(CLANE), "{s55}"(CROW & 0x0F0F0F0Fu), "{s[34:35]}"(0x0001000100010001ull), \
              "{s[60:61]}"(0x2222222222222222ull), "{s[62:63]}"(0x4444444444444444ull), "{s[64:65]}"(0x8888888888888888ull), \
              "{s66}"(ldsb + w * 8192), "{s71}"((ldsb + X_OFF) ^ (ldsb + X1_OFF)), "{s[86:87]}"(wbase), "{s85}"(wlen)
#define B_SCLOB "s83", "s84", "s88", "s89", "s90", "s91", "s26", "s77", "s68", "s36", "s37", "s38", "s57", "s58", "s49", "vcc", "scc", "m0", "memory"

    if (wave < 4) {
        LANE_SETUP
        const int dW = X_OFF + l15 * 384 + (((2 * w + (g >> 1)) ^ sw) << 4) + 8 * (g & 1);
        const int vB = V_OFF + w * 4608 + 1024 + lane * 8, rsR = RS_BASE + l15 * 4;
        {
            const unsigned rsb = ldsb + RS_BASE;
            const unsigned pE_ = fE - X_OFF, aO_ = ldsb + dW + (O_OFF - X_OFF), sel_ = (l15 & 3) ? 0u : 1u, aVB_ = ldsb + vB;
            const unsigned pST_ = lane * 16, aPLw_ = rsb + lane * 2, aRSr_ = ldsb + rsR;
            unsigned aRSw_ = rsb + lane * 4;
            const unsigned g16_ = (l15 & 3) ? ldsb + AT_OFF + 18816 : g * 16, g8_ = (l15 & 3) ? ldsb + AT_OFF + 18816 : g * 8;
            const unsigned loVL_ = ((2 * w + (l15 >> 3)) << 4) + (l15 & 7) * 2, loOL_ = g * 128;
            const unsigned on = (l15 & 3) ? 0u : 0x3F803F80u;
            const u32x4 ones4 = {on, on, on, on};
            unsigned s44 = cnt, s48 = ldsb + X_OFF, s70 = 0;
            unsigned s72 = ldsb + KQL_OFF + w * 192, s73 = ldsb + OL_OFF, s74 = ldsb + KQL_OFF + 784, s77 = ldsb + OL_OFF, s76 = ldsb + KQL_OFF + 768 + w * 4;
            KARGS
            asm volatile(
            "s_nop 4\n\t"
            "buffer_load_dwordx4 v[140:143], v248, s[84:87], s88 offen sc1\n\t"
            "s_add_u32 s49, s46, 0\n\t"
            "buffer_load_dwordx4 v[0:3], v248, s[84:87], s49 offen sc1\n\t"
            "s_add_u32 s49, s46, 6144\n\t"
            "buffer_load_dwordx4 v[24:27], v248, s[84:87], s49 offen sc1\n\t"
            "s_add_u32 s49, s46, 12288\n\t"
            "buffer_load_dwordx4 v[48:51], v248, s[84:87], s49 offen sc1\n\t"
            "s_add_u32 s49, s46, 18432\n\t"
            "buffer_load_dwordx4 v[72:75], v248, s[84:87], s49 offen sc1\n\t"
            "s_add_u32 s49, s46, 24576\n\t"
            "buffer_load_dwordx4 v[96:99], v248, s[84:87], s49 offen sc1\n\t"
            "s_add_u32 s49, s46, 30720\n\t"
            "buffer_load_dwordx4 v[120:123], v248, s[84:87], s49 offen sc1\n\t"
            "s_add_u32 s49, s46, 1024\n\t"
            "buffer_load_dwordx4 v[4:7], v248, s[84:87], s49 offen sc1\n\t"
            "s_add_u32 s49, s46, 7168\n\t"
            "buffer_load_dwordx4 v[28:31], v248, s[84:87], s49 offen sc1\n\t"
            "s_add_u32 s49, s46, 13312\n\t"
            "buffer_load_dwordx4 v[52:55], v248, s[84:87], s49 offen sc1\n\t"
            "s_add_u32 s49, s46, 19456\n\t"
            "buffer_load_dwordx4 v[76:79], v248, s[84:87], s49 offen sc1\n\t"
            "s_add_u32 s49, s46, 25600\n\t"
            "buffer_load_dwordx4 v[100:103], v248, s[84:87], s49 offen sc1\n\t"
            "s_add_u32 s49, s46, 31744\n\t"
            "buffer_load_dwordx4 v[124:127], v248, s[84:87], s49 offen sc1\n\t"
            "s_add_u32 s49, s46, 2048\n\t"
            "buffer_load_dwordx4 v[8:11], v248, s[84:87], s49 offen sc1\n\t"
            "s_add_u32 s49, s46, 8192\n\t"
            "buffer_load_dwordx4 v[32:35], v248, s[84:87], s49 offen sc1\n\t"
            "s_add_u32 s49, s46, 14336\n\t"
            "buffer_load_dwordx4 v[56:59], v248, s[84:87], s49 offen sc1\n\t"
            "s_add_u32 s49, s46, 20480\n\t"
            "buffer_load_dwordx4 v[80:83], v248, s[84:87], s49 offen sc1\n\t"
            "s_add_u32 s49, s46, 26624\n\t"
            "buffer_load_dwordx4 v[104:107], v248, s[84:87], s49 offen sc1\n\t"
            "s_add_u32 s49, s46, 32768\n\t"
            "buffer_load_dwordx4 v[128:131], v248, s[84:87], s49 offen sc1\n\t"
            "s_add_u32 s49, s46, 3072\n\t"
            "buffer_load_dwordx4 v[12:15], v248, s[84:87], s49 offen sc1\n\t"
            "s_add_u32 s49, s46, 9216\n\t"
            "buffer_load_dwordx4 v[36:39], v248, s[84:87], s49 offen sc1\n\t"
            "s_add_u32 s49, s46, 15360\n\t"
            "buffer_load_dwordx4 v[60:63], v248, s[84:87], s49 offen sc1\n\t"
            "s_add_u32 s49, s46, 21504\n\t"
            "buffer_load_dwordx4 v[84:87], v248, s[84:87], s49 offen sc1\n\t"
            "s_add_u32 s49, s46, 27648\n\t"
            "buffer_load_dwordx4 v[108:111], v248, s[84:87], s49 offen sc1\n\t"
            "s_add_u32 s49, s46, 33792\n\t"
            "buffer_load_dwordx4 v[132:135], v248, s[84:87], s49 offen sc1\n\t"
            "s_add_u32 s49, s46, 4096\n\t"
            "buffer_load_dwordx4 v[16:19], v248, s[84:87], s49 offen sc1\n\t"
            "s_add_u32 s49, s46, 10240\n\t"
            "buffer_load_dwordx4 v[40:43], v248, s[84:87], s49 offen sc1\n\t"
            "s_add_u32 s49, s46, 16384\n\t"
            "buffer_load_dwordx4 v[64:67], v248, s[84:87], s49 offen sc1\n\t"
            "s_add_u32 s49, s46, 22528\n\t"
            "buffer_load_dwordx4 v[88:91], v248, s[84:87], s49 offen sc1\n\t"
            "s_add_u32 s49, s46, 28672\n\t"
            "buffer_load_dwordx4 v[112:115], v248, s[84:87], s49 offen sc1\n\t"
            "s_add_u32 s49, s46, 34816\n\t"
            "buffer_load_dwordx4 v[136:139], v248, s[84:87], s49 offen sc1\n\t"
            "s_add_u32 s49, s46, 5120\n\t"
            "buffer_load_dwordx4 v[20:23], v248, s[84:87], s49 offen sc1\n\t"
            "s_add_u32 s49, s46, 11264\n\t"
            "buffer_load_dwordx4 v[44:47], v248, s[84:87], s49 offen sc1\n\t"
            "s_add_u32 s49, s46, 17408\n\t"
            "buffer_load_dwordx4 v[68:71], v248, s[84:87], s49 offen sc1\n\t"
            "s_add_u32 s49, s46, 23552\n\t"
            "buffer_load_dwordx4 v[92:95], v248, s[84:87], s49 offen sc1\n\t"
            "s_add_u32 s49, s46, 29696\n\t"
            "buffer_load_dwordx4 v[116:119], v248, s[84:87], s49 offen sc1\n\t"
            "s_add_u32 s49, s46, 35840\n\t"
            "buffer_load_dwordx4 v[140:143], v248, s[84:87], s49 offen sc1\n\t"
            "s_waitcnt lgkmcnt(0)\n\t"
            "s_barrier\n\t"
            "s_setprio 2\n\t"
            "v_add_u32 v235, s71, v248\n\t"
            "v_add_u32 v232, s71, v244\n\t"
            "v_xor_b32 v233, 64, v232\n\t"
            "v_subrev_u32 v239, s75, v251\n\t"
            "v_mul_u32_u24 v239, 200, v239\n\t"
            "v_lshrrev_b32 v234, 3, v255\n\t"
            "v_add3_u32 v234, v234, v239, s72\n\t"
            "ds_read_b128 v[228:231], v235 offset:12288\n\t"
            "ds_read_b128 v[144:147], v232\n\t"
            "ds_read_b128 v[148:151], v233\n\t"
            "ds_read_b128 v[204:207], v232 offset:6144\n\t"
            "ds_read_b128 v[208:211], v233 offset:6144\n\t"
            "ds_read_b128 v[152:155], v232 offset:128\n\t"
            "ds_read_b128 v[156:159], v233 offset:128\n\t"
            "ds_read_b128 v[212:215], v232 offset:6272\n\t"
            "ds_read_b128 v[224:227], v233 offset:6272\n\t"
            "ds_read_b128 v[160:163], v232 offset:256\n\t"
            "ds_read_b128 v[164:167], v233 offset:256\n\t"
            "s_waitcnt vmcnt(24)\n\t"
            "s_waitcnt lgkmcnt(10)\n\t"
            "v_add_f32 v236, v228, v230\n\t"
            "v_add_f32 v237, v229, v231\n\t"
            "s_waitcnt lgkmcnt(8)\n\t"
            "v_mfma_f32_16x16x32_bf16 v[168:171], v[0:3], v[144:147], 0\n\t"
            "ds_read_b128 v[228:231], v235 offset:13312\n\t"
            "v_lshrrev_b32 v235, 4, v255\n\t"
            "v_mfma_f32_16x16x32_bf16 v[168:171], v[4:7], v[148:151], v[168:171]\n\t"
            "v_add3_u32 v235, v235, v239, s72\n\t"
            "v_mfma_f32_16x16x32_bf16 v[176:179], v[48:51], v[144:147], 0\n\t"
            "v_mfma_f32_16x16x32_bf16 v[176:179], v[52:55], v[148:151], v[176:179]\n\t"
            "v_mfma_f32_16x16x32_bf16 v[172:175], v[24:27], v[144:147], 0\n\t"
            "v_mfma_f32_16x16x32_bf16 v[172:175], v[28:31], v[148:151], v[172:175]\n\t"
            "v_mfma_f32_16x16x32_bf16 v[180:183], v[72:75], v[144:147], 0\n\t"
            "v_mfma_f32_16x16x32_bf16 v[180:183], v[76:79], v[148:151], v[180:183]\n\t"
            "v_mfma_f32_16x16x32_bf16 v[184:187], v[96:99], v[144:147], 0\n\t"
            "v_mfma_f32_16x16x32_bf16 v[184:187], v[100:103], v[148:151], v[184:187]\n\t"
            "v_mfma_f32_16x16x32_bf16 v[188:191], v[120:123], v[144:147], 0\n\t"
            "s_waitcnt lgkmcnt(0)\n\t"
            "v_add_f32 v228, v228, v230\n\t"
            "v_add_f32 v236, v236, v228\n\t"
            "v_mfma_f32_16x16x32_bf16 v[188:191], v[124:127], v[148:151], v[188:191]\n\t"
            "v_add_f32 v229, v229, v231\n\t"
            "v_mul_f32 v236, 0x3baaaaab, v236\n\t"
            "v_mfma_f32_16x16x32_bf16 v[192:195], v[0:3], v[204:207], 0\n\t"
            "v_add_f32 v237, v237, v229\n\t"
            "v_mul_f32_e64 v236, v236, -v236\n\t"
            "v_mfma_f32_16x16x32_bf16 v[192:195], v[4:7], v[208:211], v[192:195]\n\t"
            "v_fmac_f32 v236, 0x3baaaaab, v237\n\t"
            "v_max_f32 v236, 0, v236\n\t"
            "v_mfma_f32_16x16x32_bf16 v[196:199], v[24:27], v[204:207], 0\n\t"
            "v_add_f32 v236, 0x3727c5ac, v236\n\t"
            "v_rsq_f32 v236, v236\n\t"
            "v_mfma_f32_16x16x32_bf16 v[196:199], v[28:31], v[208:211], v[196:199]\n\t"
            "ds_write_b32 v249, v236\n\t"
            "v_mfma_f32_16x16x32_bf16 v[200:203], v[48:51], v[204:207], 0\n\t"
            "ds_read_b32 v236, v251\n\t"
            "ds_read_b32 v237, v251 offset:64\n\t"
            "v_mfma_f32_16x16x32_bf16 v[200:203], v[52:55], v[208:211], v[200:203]\n\t"
            "v_mfma_f32_16x16x32_bf16 v[216:219], v[72:75], v[204:207], 0\n\t"
            "v_mfma_f32_16x16x32_bf16 v[216:219], v[76:79], v[208:211], v[216:219]\n\t"
            "v_mfma_f32_16x16x32_bf16 v[220:223], v[96:99], v[204:207], 0\n\t"
            "v_mfma_f32_16x16x32_bf16 v[220:223], v[100:103], v[208:211], v[220:223]\n\t"
            "v_mfma_f32_16x16x32_bf16 v[228:231], v[120:123], v[204:207], 0\n\t"
            "ds_read_b128 v[204:207], v232 offset:6400\n\t"
            "v_add_u32 v232, s48, v244\n\t"
            "v_mfma_f32_16x16x32_bf16 v[228:231], v[124:127], v[208:211], v[228:231]\n\t"
            "ds_read_b128 v[208:211], v233 offset:6400\n\t"
            "s_waitcnt vmcnt(12)\n\t"
            "v_mfma_f32_16x16x32_bf16 v[168:171], v[8:11], v[152:155], v[168:171]\n\t"
            "v_xor_b32 v233, 64, v232\n\t"
            "v_mfma_f32_16x16x32_bf16 v[168:171], v[12:15], v[156:159], v[168:171]\n\t"
            "v_mfma_f32_16x16x32_bf16 v[176:179], v[56:59], v[152:155], v[176:179]\n\t"
            "v_mfma_f32_16x16x32_bf16 v[176:179], v[60:63], v[156:159], v[176:179]\n\t"
            "v_mfma_f32_16x16x32_bf16 v[172:175], v[32:35], v[152:155], v[172:175]\n\t"
            "v_mfma_f32_16x16x32_bf16 v[172:175], v[36:39], v[156:159], v[172:175]\n\t"
            "v_mfma_f32_16x16x32_bf16 v[180:183], v[80:83], v[152:155], v[180:183]\n\t"
            "v_mfma_f32_16x16x32_bf16 v[180:183], v[84:87], v[156:159], v[180:183]\n\t"
            "v_mfma_f32_16x16x32_bf16 v[184:187], v[104:107], v[152:155], v[184:187]\n\t"
            "v_mfma_f32_16x16x32_bf16 v[184:187], v[108:111], v[156:159], v[184:187]\n\t"
            "v_mfma_f32_16x16x32_bf16 v[188:191], v[128:131], v[152:155], v[188:191]\n\t"
            "v_mfma_f32_16x16x32_bf16 v[188:191], v[132:135], v[156:159], v[188:191]\n\t"
            "v_mfma_f32_16x16x32_bf16 v[192:195], v[8:11], v[212:215], v[192:195]\n\t"
            "v_mfma_f32_16x16x32_bf16 v[192:195], v[12:15], v[224:227], v[192:195]\n\t"
            "v_mfma_f32_16x16x32_bf16 v[196:199], v[32:35], v[212:215], v[196:199]\n\t"
            "v_mfma_f32_16x16x32_bf16 v[196:199], v[36:39], v[224:227], v[196:199]\n\t"
            "v_mfma_f32_16x16x32_bf16 v[200:203], v[56:59], v[212:215], v[200:203]\n\t"
            "v_mfma_f32_16x16x32_bf16 v[200:203], v[60:63], v[224:227], v[200:203]\n\t"
            "v_mfma_f32_16x16x32_bf16 v[216:219], v[80:83], v[212:215], v[216:219]\n\t"
            "v_mfma_f32_16x16x32_bf16 v[216:219], v[84:87], v[224:227], v[216:219]\n\t"
            "v_mfma_f32_16x16x32_bf16 v[220:223], v[104:107], v[212:215], v[220:223]\n\t"
            "v_mfma_f32_16x16x32_bf16 v[220:223], v[108:111], v[224:227], v[220:223]\n\t"
            "v_mfma_f32_16x16x32_bf16 v[228:231], v[128:131], v[212:215], v[228:231]\n\t"
            "ds_read_b128 v[212:215], v232 offset:256\n\t"
            "v_mfma_f32_16x16x32_bf16 v[228:231], v[132:135], v[224:227], v[228:231]\n\t"
            "ds_read_b128 v[224:227], v233 offset:256\n\t"
            "s_waitcnt vmcnt(0)\n\t"
            "v_mfma_f32_16x16x32_bf16 v[168:171], v[16:19], v[160:163], v[168:171]\n\t"
            "v_mfma_f32_16x16x32_bf16 v[168:171], v[20:23], v[164:167], v[168:171]\n\t"
            "v_mfma_f32_16x16x32_bf16 v[176:179], v[64:67], v[160:163], v[176:179]\n\t"
            "v_mfma_f32_16x16x32_bf16 v[176:179], v[68:71], v[164:167], v[176:179]\n\t"
            "v_mfma_f32_16x16x32_bf16 v[180:183], v[88:91], v[160:163], v[180:183]\n\t"
            "v_mfma_f32_16x16x32_bf16 v[180:183], v[92:95], v[164:167], v[180:183]\n\t"
            "v_mfma_f32_16x16x32_bf16 v[172:175], v[40:43], v[160:163], v[172:175]\n\t"
            "v_mfma_f32_16x16x32_bf16 v[172:175], v[44:47], v[164:167], v[172:175]\n\t"
            "s_waitcnt lgkmcnt(5)\n\t"
            "s_nop 0\n\t"
            "v_mul_f32 v168, v236, v168\n\t"
            "v_mfma_f32_16x16x32_bf16 v[184:187], v[112:115], v[160:163], v[184:187]\n\t"
            "v_mul_f32 v169, v236, v169\n\t"
            "v_mul_f32 v170, v236, v170\n\t"
            "v_mfma_f32_16x16x32_bf16 v[184:187], v[116:119], v[164:167], v[184:187]\n\t"
            "v_mul_f32 v176, v236, v176\n\t"
            "v_mul_f32 v177, v236, v177\n\t"
            "v_mfma_f32_16x16x32_bf16 v[188:191], v[136:139], v[160:163], v[188:191]\n\t"
            "v_mul_f32 v171, v236, v171\n\t"
            "v_mul_f32 v178, v236, v178\n\t"
            "v_mfma_f32_16x16x32_bf16 v[188:191], v[140:143], v[164:167], v[188:191]\n\t"
            "v_mul_f32 v179, v236, v179\n\t"
            "v_cvt_pk_bf16_f32 v168, v168, v169\n\t"
            "s_waitcnt lgkmcnt(2)\n\t"
            "v_mfma_f32_16x16x32_bf16 v[192:195], v[16:19], v[204:207], v[192:195]\n\t"
            "v_cvt_pk_bf16_f32 v176, v176, v177\n\t"
            "v_cvt_pk_bf16_f32 v180, v180, v181\n\t"
            "v_mfma_f32_16x16x32_bf16 v[192:195], v[20:23], v[208:211], v[192:195]\n\t"
            "v_mul_f32 v172, v236, v172\n\t"
            "v_mul_f32 v173, v236, v173\n\t"
            "v_mfma_f32_16x16x32_bf16 v[196:199], v[40:43], v[204:207], v[196:199]\n\t"
            "v_mul_f32 v174, v236, v174\n\t"
            "v_mul_f32 v175, v236, v175\n\t"
            "v_mfma_f32_16x16x32_bf16 v[196:199], v[44:47], v[208:211], v[196:199]\n\t"
            "v_cvt_pk_bf16_f32 v169, v170, v171\n\t"
            "v_cvt_pk_bf16_f32 v177, v178, v179\n\t"
            "v_mfma_f32_16x16x32_bf16 v[200:203], v[64:67], v[204:207], v[200:203]\n\t"
            "v_cvt_pk_bf16_f32 v181, v182, v183\n\t"
            "v_cvt_pk_bf16_f32 v170, v172, v173\n\t"
            "v_mfma_f32_16x16x32_bf16 v[200:203], v[68:71], v[208:211], v[200:203]\n\t"
            "v_cvt_pk_bf16_f32 v171, v174, v175\n\t"
            "v_cvt_pk_bf16_f32 v182, v184, v185\n\t"
            "v_mfma_f32_16x16x32_bf16 v[216:219], v[88:91], v[204:207], v[216:219]\n\t"
            "v_cvt_pk_bf16_f32 v183, v186, v187\n\t"
            "v_cvt_pk_bf16_f32 v178, v188, v189\n\t"
            "v_mfma_f32_16x16x32_bf16 v[216:219], v[92:95], v[208:211], v[216:219]\n\t"
            "v_cvt_pk_bf16_f32 v179, v190, v191\n\t"
            "v_mul_f32 v236, 0x3e553b94, v236\n\t"
            "v_mfma_f32_16x16x32_bf16 v[220:223], v[112:115], v[204:207], v[220:223]\n\t"
            "v_add_u32 v184, s76, v239\n\t"
            "v_add_u32 v185, s74, v239\n\t"
            "v_mfma_f32_16x16x32_bf16 v[220:223], v[116:119], v[208:211], v[220:223]\n\t"
            "s_mov_b64 exec, -1\n\t"
            "ds_write_b128 v234, v[168:171]\n\t"
            "ds_write_b128 v234, v[180:183] offset:64\n\t"
            "ds_write_b64 v235, v[176:177] offset:128\n\t"
            "ds_write_b64 v235, v[178:179] offset:160\n\t"
            "s_mov_b64 exec, -1\n\t"
            "v_mfma_f32_16x16x16_bf16 v[172:175], v[176:177], v[178:179], 0\n\t"
            "v_mul_f32 v192, v237, v192\n\t"
            "v_mul_f32 v193, v237, v193\n\t"
            "v_mul_f32 v200, v237, v200\n\t"
            "v_mfma_f32_16x16x32_bf16 v[228:231], v[136:139], v[204:207], v[228:231]\n\t"
            "ds_read_b128 v[204:207], v232\n\t"
            "v_mul_f32 v201, v237, v201\n\t"
            "v_mfma_f32_16x16x32_bf16 v[228:231], v[140:143], v[208:211], v[228:231]\n\t"
            "ds_read_b128 v[208:211], v233\n\t"
            "v_mul_f32 v194, v237, v194\n\t"
            "v_mfma_f32_16x16x32_bf16 v[172:175], v[168:171], v[180:183], v[172:175]\n\t"
            "v_mul_f32 v195, v237, v195\n\t"
            "v_mul_f32 v202, v237, v202\n\t"
            "v_mul_f32 v203, v237, v203\n\t"
            "v_cvt_pk_bf16_f32 v192, v192, v193\n\t"
            "v_cvt_pk_bf16_f32 v200, v200, v201\n\t"
            "v_cvt_pk_bf16_f32 v216, v216, v217\n\t"
            "v_mul_f32 v196, v237, v196\n\t"
            "v_mul_f32 v197, v237, v197\n\t"
            "v_mul_f32 v198, v237, v198\n\t"
            "v_mul_f32 v199, v237, v199\n\t"
            "v_cvt_pk_bf16_f32 v193, v194, v195\n\t"
            "v_cvt_pk_bf16_f32 v201, v202, v203\n\t"
            "v_cvt_pk_bf16_f32 v217, v218, v219\n\t"
            "v_cvt_pk_bf16_f32 v194, v196, v197\n\t"
            "v_cvt_pk_bf16_f32 v195, v198, v199\n\t"
            "v_cvt_pk_bf16_f32 v218, v220, v221\n\t"
            "v_cvt_pk_bf16_f32 v219, v222, v223\n\t"
            "v_cvt_pk_bf16_f32 v202, v228, v229\n\t"
            "v_cvt_pk_bf16_f32 v203, v230, v231\n\t"
            "v_mul_f32 v237, 0x3e553b94, v237\n\t"
            "v_add_u32 v220, s76, v239\n\t"
            "v_mfma_f32_16x16x16_bf16 v[196:199], v[200:201], v[202:203], 0\n\t"
            "s_and_b32 exec_lo, s56, 0x11111111\n\t"
            "s_and_b32 exec_hi, s57, 0x11111111\n\t"
            "ds_write_b32 v184, v172 offset:0\n\t"
            "s_and_b32 exec_lo, s56, 0x22222222\n\t"
            "s_and_b32 exec_hi, s57, 0x22222222\n\t"
            "ds_write_b32 v184, v173 offset:0\n\t"
            "s_and_b32 exec_lo, s56, 0x44444444\n\t"
            "s_and_b32 exec_hi, s57, 0x44444444\n\t"
            "ds_write_b32 v184, v174 offset:0\n\t"
            "s_and_b32 exec_lo, s56, 0x88888888\n\t"
            "s_and_b32 exec_hi, s57, 0x88888888\n\t"
            "ds_write_b32 v184, v175 offset:0\n\t"
            "s_mov_b64 exec, s[56:57]\n\t"
            "ds_write_b32 v185, v236 offset:0\n\t"
            "s_mov_b64 exec, -1\n\t"
            "s_waitcnt lgkmcnt(5)\n\t"
            "v_mfma_f32_16x16x32_bf16 v[144:147], v[0:3], v[204:207], 0\n\t"
            "v_add_u32 v221, s74, v239\n\t"
            "v_mfma_f32_16x16x32_bf16 v[144:147], v[4:7], v[208:211], v[144:147]\n\t"
            "s_mov_b64 exec, s[54:55]\n\t"
            "ds_write_b128 v234, v[192:195] offset:12800\n\t"
            "ds_write_b128 v234, v[216:219] offset:12864\n\t"
            "ds_write_b64 v235, v[200:201] offset:12928\n\t"
            "ds_write_b64 v235, v[202:203] offset:12960\n\t"
            "s_mov_b64 exec, -1\n\t"
            "v_mfma_f32_16x16x32_bf16 v[148:151], v[28:31], v[208:211], 0\n\t"
            "v_add_u32 v235, s48, v248\n\t"
            "v_mfma_f32_16x16x32_bf16 v[148:151], v[24:27], v[204:207], v[148:151]\n\t"
            "ds_read_b128 v[228:231], v235 offset:18816\n\t"
            "v_mfma_f32_16x16x32_bf16 v[152:155], v[48:51], v[204:207], 0\n\t"
            "v_mfma_f32_16x16x32_bf16 v[152:155], v[52:55], v[208:211], v[152:155]\n\t"
            "v_mfma_f32_16x16x32_bf16 v[156:159], v[76:79], v[208:211], 0\n\t"
            "v_mfma_f32_16x16x32_bf16 v[156:159], v[72:75], v[204:207], v[156:159]\n\t"
            "v_mfma_f32_16x16x32_bf16 v[160:163], v[96:99], v[204:207], 0\n\t"
            "v_mfma_f32_16x16x32_bf16 v[160:163], v[100:103], v[208:211], v[160:163]\n\t"
            "v_mfma_f32_16x16x32_bf16 v[164:167], v[124:127], v[208:211], 0\n\t"
            "ds_read_b128 v[208:211], v233 offset:128\n\t"
            "v_mfma_f32_16x16x32_bf16 v[164:167], v[120:123], v[204:207], v[164:167]\n\t"
            "ds_read_b128 v[204:207], v232 offset:128\n\t"
            "v_mfma_f32_16x16x32_bf16 v[196:199], v[192:195], v[216:219], v[196:199]\n\t"
            "s_waitcnt lgkmcnt(2)\n\t"
            "v_add_f32 v236, v228, v230\n\t"
            "s_nop 5\n\t"
            "s_and_b32 exec_lo, s78, 0x11111111\n\t"
            "s_and_b32 exec_hi, s79, 0x11111111\n\t"
            "ds_write_b32 v220, v196 offset:12800\n\t"
            "s_and_b32 exec_lo, s78, 0x22222222\n\t"
            "s_and_b32 exec_hi, s79, 0x22222222\n\t"
            "ds_write_b32 v220, v197 offset:12800\n\t"
            "s_and_b32 exec_lo, s78, 0x44444444\n\t"
            "s_and_b32 exec_hi, s79, 0x44444444\n\t"
            "ds_write_b32 v220, v198 offset:12800\n\t"
            "s_and_b32 exec_lo, s78, 0x88888888\n\t"
            "s_and_b32 exec_hi, s79, 0x88888888\n\t"
            "ds_write_b32 v220, v199 offset:12800\n\t"
            "s_mov_b64 exec, s[78:79]\n\t"
            "ds_write_b32 v221, v237 offset:12800\n\t"
            "s_mov_b64 exec, -1\n\t"
            "s_waitcnt lgkmcnt(5)\n\t"
            "v_mfma_f32_16x16x32_bf16 v[144:147], v[8:11], v[204:207], v[144:147]\n\t"
            "v_add_f32 v237, v229, v231\n\t"
            "v_mfma_f32_16x16x32_bf16 v[144:147], v[12:15], v[208:211], v[144:147]\n\t"
            "ds_read_b128 v[228:231], v235 offset:19840\n\t"
            "v_mfma_f32_16x16x32_bf16 v[148:151], v[36:39], v[208:211], v[148:151]\n\t"
            "v_mfma_f32_16x16x32_bf16 v[148:151], v[32:35], v[204:207], v[148:151]\n\t"
            "v_mfma_f32_16x16x32_bf16 v[152:155], v[56:59], v[204:207], v[152:155]\n\t"
            "v_mfma_f32_16x16x32_bf16 v[152:155], v[60:63], v[208:211], v[152:155]\n\t"
            "v_mfma_f32_16x16x32_bf16 v[156:159], v[84:87], v[208:211], v[156:159]\n\t"
            "v_mfma_f32_16x16x32_bf16 v[156:159], v[80:83], v[204:207], v[156:159]\n\t"
            "v_mfma_f32_16x16x32_bf16 v[160:163], v[104:107], v[204:207], v[160:163]\n\t"
            "v_mfma_f32_16x16x32_bf16 v[160:163], v[108:111], v[208:211], v[160:163]\n\t"
            "v_mfma_f32_16x16x32_bf16 v[164:167], v[132:135], v[208:211], v[164:167]\n\t"
            "ds_read_b128 v[208:211], v233 offset:6144\n\t"
            "v_mfma_f32_16x16x32_bf16 v[164:167], v[128:131], v[204:207], v[164:167]\n\t"
            "ds_read_b128 v[204:207], v232 offset:6144\n\t"
            "s_waitcnt lgkmcnt(2)\n\t"
            "v_add_f32 v228, v228, v230\n\t"
            "v_mfma_f32_16x16x32_bf16 v[144:147], v[16:19], v[212:215], v[144:147]\n\t"
            "v_add_f32 v236, v236, v228\n\t"
            "v_add_f32 v229, v229, v231\n\t"
            "v_mfma_f32_16x16x32_bf16 v[144:147], v[20:23], v[224:227], v[144:147]\n\t"
            "v_mul_f32 v236, 0x3baaaaab, v236\n\t"
            "v_add_f32 v237, v237, v229\n\t"
            "v_mfma_f32_16x16x32_bf16 v[148:151], v[44:47], v[224:227], v[148:151]\n\t"
            "v_mul_f32_e64 v236, v236, -v236\n\t"
            "v_fmac_f32 v236, 0x3baaaaab, v237\n\t"
            "v_mfma_f32_16x16x32_bf16 v[148:151], v[40:43], v[212:215], v[148:151]\n\t"
            "v_max_f32 v236, 0, v236\n\t"
            "v_add_f32 v236, 0x3727c5ac, v236\n\t"
            "v_mfma_f32_16x16x32_bf16 v[152:155], v[64:67], v[212:215], v[152:155]\n\t"
            "v_rsq_f32 v236, v236\n\t"
            "v_mfma_f32_16x16x32_bf16 v[152:155], v[68:71], v[224:227], v[152:155]\n\t"
            "ds_write_b32 v249, v236\n\t"
            "v_mfma_f32_16x16x32_bf16 v[156:159], v[92:95], v[224:227], v[156:159]\n\t"
            "ds_read_b32 v236, v251\n\t"
            "ds_read_b32 v237, v251 offset:64\n\t"
            "v_mfma_f32_16x16x32_bf16 v[156:159], v[88:91], v[212:215], v[156:159]\n\t"
            "ds_read_b32 v238, v251 offset:128\n\t"
            "v_mfma_f32_16x16x32_bf16 v[160:163], v[112:115], v[212:215], v[160:163]\n\t"
            "v_mfma_f32_16x16x32_bf16 v[160:163], v[116:119], v[224:227], v[160:163]\n\t"
            "v_mfma_f32_16x16x32_bf16 v[164:167], v[140:143], v[224:227], v[164:167]\n\t"
            "ds_read_b128 v[224:227], v233 offset:6272\n\t"
            "v_mfma_f32_16x16x32_bf16 v[164:167], v[136:139], v[212:215], v[164:167]\n\t"
            "ds_read_b128 v[212:215], v232 offset:6272\n\t"
            "s_waitcnt lgkmcnt(6)\n\t"
            "v_mfma_f32_16x16x32_bf16 v[168:171], v[0:3], v[204:207], 0\n\t"
            "v_cvt_pk_bf16_f32 v156, v156, v157\n\t"
            "v_cvt_pk_bf16_f32 v157, v158, v159\n\t"
            "v_mfma_f32_16x16x32_bf16 v[168:171], v[4:7], v[208:211], v[168:171]\n\t"
            "v_cvt_pk_bf16_f32 v158, v160, v161\n\t"
            "v_cvt_pk_bf16_f32 v159, v162, v163\n\t"
            "v_mfma_f32_16x16x32_bf16 v[172:175], v[28:31], v[208:211], 0\n\t"
            "v_mfma_f32_16x16x32_bf16 v[172:175], v[24:27], v[204:207], v[172:175]\n\t"
            "v_mfma_f32_16x16x32_bf16 v[176:179], v[48:51], v[204:207], 0\n\t"
            "s_waitcnt lgkmcnt(4)\n\t"
            "v_mul_f32 v144, v236, v144\n\t"
            "v_mul_f32 v145, v236, v145\n\t"
            "v_mfma_f32_16x16x32_bf16 v[176:179], v[52:55], v[208:211], v[176:179]\n\t"
            "v_mul_f32 v152, v236, v152\n\t"
            "v_mul_f32 v153, v236, v153\n\t"
            "v_mfma_f32_16x16x32_bf16 v[180:183], v[76:79], v[208:211], 0\n\t"
            "v_mul_f32 v146, v236, v146\n\t"
            "v_mul_f32 v147, v236, v147\n\t"
            "v_mfma_f32_16x16x32_bf16 v[180:183], v[72:75], v[204:207], v[180:183]\n\t"
            "v_mul_f32 v154, v236, v154\n\t"
            "v_mul_f32 v155, v236, v155\n\t"
            "v_mfma_f32_16x16x32_bf16 v[184:187], v[96:99], v[204:207], 0\n\t"
            "v_cvt_pk_bf16_f32 v144, v144, v145\n\t"
            "v_cvt_pk_bf16_f32 v152, v152, v153\n\t"
            "v_mfma_f32_16x16x32_bf16 v[184:187], v[100:103], v[208:211], v[184:187]\n\t"
            "v_mul_f32 v148, v236, v148\n\t"
            "v_mul_f32 v149, v236, v149\n\t"
            "v_mfma_f32_16x16x32_bf16 v[188:191], v[124:127], v[208:211], 0\n\t"
            "ds_read_b128 v[208:211], v233 offset:6400\n\t"
            "v_mul_f32 v150, v236, v150\n\t"
            "v_mfma_f32_16x16x32_bf16 v[188:191], v[120:123], v[204:207], v[188:191]\n\t"
            "ds_read_b128 v[204:207], v232 offset:6400\n\t"
            "v_mul_f32 v151, v236, v151\n\t"
            "s_waitcnt lgkmcnt(2)\n\t"
            "v_mfma_f32_16x16x32_bf16 v[168:171], v[8:11], v[212:215], v[168:171]\n\t"
            "v_cvt_pk_bf16_f32 v145, v146, v147\n\t"
            "v_cvt_pk_bf16_f32 v153, v154, v155\n\t"
            "v_mfma_f32_16x16x32_bf16 v[168:171], v[12:15], v[224:227], v[168:171]\n\t"
            "v_cvt_pk_bf16_f32 v146, v148, v149\n\t"
            "v_cvt_pk_bf16_f32 v147, v150, v151\n\t"
            "v_mfma_f32_16x16x32_bf16 v[172:175], v[36:39], v[224:227], v[172:175]\n\t"
            "v_cvt_pk_bf16_f32 v154, v164, v165\n\t"
            "v_cvt_pk_bf16_f32 v155, v166, v167\n\t"
            "v_mfma_f32_16x16x32_bf16 v[172:175], v[32:35], v[212:215], v[172:175]\n\t"
            "v_mul_f32 v236, 0x3e553b94, v236\n\t"
            "v_mfma_f32_16x16x32_bf16 v[176:179], v[56:59], v[212:215], v[176:179]\n\t"
            "v_mfma_f32_16x16x32_bf16 v[176:179], v[60:63], v[224:227], v[176:179]\n\t"
            "v_mfma_f32_16x16x32_bf16 v[180:183], v[84:87], v[224:227], v[180:183]\n\t"
            "v_mfma_f32_16x16x32_bf16 v[180:183], v[80:83], v[212:215], v[180:183]\n\t"
            "v_mfma_f32_16x16x32_bf16 v[184:187], v[104:107], v[212:215], v[184:187]\n\t"
            "v_mfma_f32_16x16x32_bf16 v[184:187], v[108:111], v[224:227], v[184:187]\n\t"
            "v_mfma_f32_16x16x32_bf16 v[188:191], v[132:135], v[224:227], v[188:191]\n\t"
            "ds_read_b128 v[224:227], v233 offset:12288\n\t"
            "v_mfma_f32_16x16x32_bf16 v[188:191], v[128:131], v[212:215], v[188:191]\n\t"
            "ds_read_b128 v[212:215], v232 offset:12288\n\t"
            "s_waitcnt lgkmcnt(2)\n\t"
            "v_mfma_f32_16x16x32_bf16 v[168:171], v[16:19], v[204:207], v[168:171]\n\t"
            "v_mfma_f32_16x16x32_bf16 v[168:171], v[20:23], v[208:211], v[168:171]\n\t"
            "v_mfma_f32_16x16x32_bf16 v[172:175], v[44:47], v[208:211], v[172:175]\n\t"
            "v_mfma_f32_16x16x32_bf16 v[172:175], v[40:43], v[204:207], v[172:175]\n\t"
            "v_mfma_f32_16x16x32_bf16 v[176:179], v[64:67], v[204:207], v[176:179]\n\t"
            "v_mfma_f32_16x16x32_bf16 v[176:179], v[68:71], v[208:211], v[176:179]\n\t"
            "v_mfma_f32_16x16x32_bf16 v[180:183], v[92:95], v[208:211], v[180:183]\n\t"
            "v_mfma_f32_16x16x32_bf16 v[180:183], v[88:91], v[204:207], v[180:183]\n\t"
            "v_mfma_f32_16x16x32_bf16 v[184:187], v[112:115], v[204:207], v[184:187]\n\t"
            "v_mfma_f32_16x16x32_bf16 v[184:187], v[116:119], v[208:211], v[184:187]\n\t"
            "v_mfma_f32_16x16x32_bf16 v[188:191], v[140:143], v[208:211], v[188:191]\n\t"
            "ds_read_b128 v[208:211], v233 offset:12416\n\t"
            "v_mfma_f32_16x16x32_bf16 v[188:191], v[136:139], v[204:207], v[188:191]\n\t"
            "ds_read_b128 v[204:207], v232 offset:12416\n\t"
            "s_waitcnt lgkmcnt(0)\n\t"
            "v_subrev_u32 v249, s75, v251\n\t"
            "v_add_u32 v249, 162816, v249\n\t"
            "s_nop 7\n\t"
            ".p2align 6\n\t"
            "s_nop 0\n\t"
            "s_nop 0\n\t"
            "s_nop 0\n\t"
            ".Lwa_loop%=:\n\t"
            "s_waitcnt lgkmcnt(2)\n\t"
            "v_mfma_f32_16x16x32_bf16 v[192:195], v[0:3], v[212:215], 0\n\t"
            "s_bfe_u32 s49, s70, 0x30001\n\t"
            "s_lshl_b32 s49, s49, 4\n\t"
            "s_or_b32 s73, s77, s49\n\t"
            "v_mfma_f32_16x16x32_bf16 v[192:195], v[4:7], v[224:227], v[192:195]\n\t"
            "v_mad_u32_u24 v220, v246, s72, v253\n\t"
            "v_mad_u32_u24 v216, v246, s72, v252\n\t"
            "v_mfma_f32_16x16x32_bf16 v[148:151], v[28:31], v[224:227], 0\n\t"
            "ds_read_b64 v[222:223], v220 offset:160\n\t"
            "ds_read_b64 v[220:221], v220 offset:128\n\t"
            "v_mfma_f32_16x16x32_bf16 v[148:151], v[24:27], v[212:215], v[148:151]\n\t"
            "v_mul_f32 v176, v237, v176\n\t"
            "v_mul_f32 v177, v237, v177\n\t"
            "v_mfma_f32_16x16x32_bf16 v[196:199], v[48:51], v[212:215], 0\n\t"
            "v_mul_f32 v178, v237, v178\n\t"
            "v_mul_f32 v179, v237, v179\n\t"
            "v_mfma_f32_16x16x32_bf16 v[196:199], v[52:55], v[224:227], v[196:199]\n\t"
            "v_cvt_pk_bf16_f32 v176, v176, v177\n\t"
            "v_mul_f32 v168, v237, v168\n\t"
            "v_mfma_f32_16x16x32_bf16 v[200:203], v[76:79], v[224:227], 0\n\t"
            "v_mul_f32 v169, v237, v169\n\t"
            "v_cvt_pk_bf16_f32 v177, v178, v179\n\t"
            "v_mfma_f32_16x16x32_bf16 v[200:203], v[72:75], v[212:215], v[200:203]\n\t"
            "v_mul_f32 v170, v237, v170\n\t"
            "v_mul_f32 v171, v237, v171\n\t"
            "v_mfma_f32_16x16x32_bf16 v[160:163], v[96:99], v[212:215], 0\n\t"
            "v_cvt_pk_bf16_f32 v168, v168, v169\n\t"
            "v_cvt_pk_bf16_f32 v180, v180, v181\n\t"
            "v_mfma_f32_16x16x32_bf16 v[160:163], v[100:103], v[224:227], v[160:163]\n\t"
            "v_cvt_pk_bf16_f32 v178, v188, v189\n\t"
            "v_cvt_pk_bf16_f32 v179, v190, v191\n\t"
            "v_mfma_f32_16x16x32_bf16 v[164:167], v[124:127], v[224:227], 0\n\t"
            "ds_read_b128 v[224:227], v233 offset:12544\n\t"
            "v_mul_f32 v172, v237, v172\n\t"
            "v_mfma_f32_16x16x32_bf16 v[164:167], v[120:123], v[212:215], v[164:167]\n\t"
            "ds_read_b128 v[212:215], v232 offset:12544\n\t"
            "v_mul_f32 v173, v237, v173\n\t"
            "s_waitcnt lgkmcnt(4)\n\t"
            "v_mfma_f32_16x16x32_bf16 v[192:195], v[8:11], v[204:207], v[192:195]\n\t"
            "v_mul_f32 v174, v237, v174\n\t"
            "v_mul_f32 v175, v237, v175\n\t"
            "v_mfma_f32_16x16x32_bf16 v[192:195], v[12:15], v[208:211], v[192:195]\n\t"
            "v_cvt_pk_bf16_f32 v169, v170, v171\n\t"
            "v_cvt_pk_bf16_f32 v181, v182, v183\n\t"
            "v_mfma_f32_16x16x32_bf16 v[196:199], v[56:59], v[204:207], v[196:199]\n\t"
            "v_cvt_pk_bf16_f32 v170, v172, v173\n\t"
            "v_cvt_pk_bf16_f32 v171, v174, v175\n\t"
            "v_mfma_f32_16x16x32_bf16 v[196:199], v[60:63], v[208:211], v[196:199]\n\t"
            "v_cvt_pk_bf16_f32 v182, v184, v185\n\t"
            "v_cvt_pk_bf16_f32 v183, v186, v187\n\t"
            "v_mfma_f32_16x16x32_bf16 v[160:163], v[104:107], v[204:207], v[160:163]\n\t"
            "v_mov_b32 v230, s76\n\t"
            "v_mov_b32 v239, s74\n\t"
            "v_mfma_f32_16x16x32_bf16 v[160:163], v[108:111], v[208:211], v[160:163]\n\t"
            "s_add_u32 s70, s70, 1\n\t"
            "s_add_u32 s72, s72, 800\n\t"
            "s_add_u32 s74, s74, 800\n\t"
            "s_add_u32 s76, s76, 800\n\t"
            "v_mfma_f32_16x16x32_bf16 v[148:151], v[36:39], v[208:211], v[148:151]\n\t"
            "s_add_u32 s77, s77, 384\n\t"
            "s_xor_b32 s48, s48, s47\n\t"
            "s_sub_u32 s44, s44, 1\n\t"
            "v_mfma_f32_16x16x32_bf16 v[148:151], v[32:35], v[204:207], v[148:151]\n\t"
            "ds_read_b32 v230, v230\n\t"
            "ds_read_b32 v239, v239\n\t"
            "v_mfma_f32_16x16x32_bf16 v[200:203], v[84:87], v[208:211], v[200:203]\n\t"
            "v_mul_f32 v237, 0x3e553b94, v237\n\t"
            "v_mfma_f32_16x16x32_bf16 v[200:203], v[80:83], v[204:207], v[200:203]\n\t"
            "v_mfma_f32_16x16x32_bf16 v[164:167], v[132:135], v[208:211], v[164:167]\n\t"
            "v_mfma_f32_16x16x32_bf16 v[164:167], v[128:131], v[204:207], v[164:167]\n\t"
            "s_waitcnt lgkmcnt(2)\n\t"
            "v_mfma_f32_16x16x32_bf16 v[192:195], v[16:19], v[212:215], v[192:195]\n\t"
            "v_mfma_f32_16x16x32_bf16 v[192:195], v[20:23], v[224:227], v[192:195]\n\t"
            "v_mfma_f32_16x16x32_bf16 v[196:199], v[64:67], v[212:215], v[196:199]\n\t"
            "v_mfma_f32_16x16x32_bf16 v[196:199], v[68:71], v[224:227], v[196:199]\n\t"
            "v_mfma_f32_16x16x32_bf16 v[160:163], v[112:115], v[212:215], v[160:163]\n\t"
            "v_mfma_f32_16x16x32_bf16 v[160:163], v[116:119], v[224:227], v[160:163]\n\t"
            "v_mfma_f32_16x16x32_bf16 v[148:151], v[44:47], v[224:227], v[148:151]\n\t"
            "v_mfma_f32_16x16x32_bf16 v[148:151], v[40:43], v[212:215], v[148:151]\n\t"
            "s_nop 1\n\t"
            "v_mul_f32 v192, v238, v192\n\t"
            "v_mfma_f32_16x16x32_bf16 v[200:203], v[92:95], v[224:227], v[200:203]\n\t"
            "v_mul_f32 v193, v238, v193\n\t"
            "v_mul_f32 v194, v238, v194\n\t"
            "v_mfma_f32_16x16x32_bf16 v[200:203], v[88:91], v[212:215], v[200:203]\n\t"
            "v_mul_f32 v195, v238, v195\n\t"
            "v_cvt_pk_bf16_f32 v192, v192, v193\n\t"
            "v_mfma_f32_16x16x32_bf16 v[164:167], v[140:143], v[224:227], v[164:167]\n\t"
            "ds_read_b128 v[224:227], v216 offset:64\n\t"
            "ds_read_b128 v[216:219], v216\n\t"
            "v_mfma_f32_16x16x32_bf16 v[164:167], v[136:139], v[212:215], v[164:167]\n\t"
            "v_mul_f32 v148, v238, v148\n\t"
            "v_mul_f32 v149, v238, v149\n\t"
            "v_mfma_f32_16x16x16_bf16 v[172:175], v[220:221], v[154:155], 0\n\t"
            "v_mul_f32 v150, v238, v150\n\t"
            "v_mul_f32 v151, v238, v151\n\t"
            "v_cvt_pk_bf16_f32 v193, v194, v195\n\t"
            "v_mfma_f32_16x16x16_bf16 v[184:187], v[152:153], v[178:179], 0\n\t"
            "v_mul_f32 v196, v238, v196\n\t"
            "v_mul_f32 v197, v238, v197\n\t"
            "v_cvt_pk_bf16_f32 v194, v148, v149\n\t"
            "v_mfma_f32_16x16x16_bf16 v[208:211], v[220:221], v[178:179], 0\n\t"
            "v_cvt_pk_bf16_f32 v195, v150, v151\n\t"
            "v_cvt_pk_bf16_f32 v200, v200, v201\n\t"
            "v_mul_f32 v198, v238, v198\n\t"
            "v_mfma_f32_16x16x16_bf16 v[148:151], v[152:153], v[154:155], 0\n\t"
            "v_mul_f32 v199, v238, v199\n\t"
            "v_cvt_pk_bf16_f32 v196, v196, v197\n\t"
            "v_cvt_pk_bf16_f32 v201, v202, v203\n\t"
            "v_mfma_f32_16x16x16_bf16 v[204:207], v[176:177], v[178:179], 0\n\t"
            "v_cvt_pk_bf16_f32 v197, v198, v199\n\t"
            "v_cvt_pk_bf16_f32 v202, v160, v161\n\t"
            "v_cvt_pk_bf16_f32 v203, v162, v163\n\t"
            "v_mfma_f32_16x16x16_bf16 v[160:163], v[176:177], v[154:155], 0\n\t"
            "v_cvt_pk_bf16_f32 v198, v164, v165\n\t"
            "v_cvt_pk_bf16_f32 v199, v166, v167\n\t"
            "s_setprio 0\n\t"
            "s_setprio 3\n\t"
            "v_mfma_f32_16x16x16_bf16 v[164:167], v[196:197], v[154:155], 0\n\t"
            "v_mul_f32 v238, 0x3e553b94, v238\n\t"
            "v_mfma_f32_16x16x16_bf16 v[188:191], v[196:197], v[178:179], 0\n\t"
            "v_mfma_f32_16x16x32_bf16 v[148:151], v[144:147], v[156:159], v[148:151]\n\t"
            "s_waitcnt lgkmcnt(0)\n\t"
            "v_mfma_f32_16x16x32_bf16 v[172:175], v[216:219], v[156:159], v[172:175]\n\t"
            "v_mfma_f32_16x16x32_bf16 v[184:187], v[144:147], v[180:183], v[184:187]\n\t"
            "v_mfma_f32_16x16x32_bf16 v[208:211], v[216:219], v[180:183], v[208:211]\n\t"
            "v_mfma_f32_16x16x32_bf16 v[160:163], v[168:171], v[156:159], v[160:163]\n\t"
            "v_mfma_f32_16x16x32_bf16 v[204:207], v[168:171], v[180:183], v[204:207]\n\t"
            "v_mfma_f32_16x16x32_bf16 v[164:167], v[192:195], v[156:159], v[164:167]\n\t"
            "v_mfma_f32_16x16x32_bf16 v[188:191], v[192:195], v[180:183], v[188:191]\n\t"
            "s_nop 1\n\t"
            "v_max3_f32 v228, v172, v148, v149\n\t"
            "v_mfma_f32_16x16x16_bf16 v[212:215], v[222:223], v[152:153], 0\n\t"
            "v_max3_f32 v228, v228, v150, v151\n\t"
            "v_max3_f32 v229, v208, v184, v185\n\t"
            "v_mfma_f32_16x16x16_bf16 v[156:159], v[222:223], v[196:197], 0\n\t"
            "v_max3_f32 v229, v229, v186, v187\n\t"
            "v_max3_f32 v228, v228, v160, v161\n\t"
            "v_max3_f32 v229, v229, v204, v205\n\t"
            "v_mfma_f32_16x16x32_bf16 v[212:215], v[224:227], v[144:147], v[212:215]\n\t"
            "v_max3_f32 v228, v228, v162, v163\n\t"
            "v_max3_f32 v229, v229, v206, v207\n\t"
            "v_mfma_f32_16x16x32_bf16 v[156:159], v[224:227], v[192:195], v[156:159]\n\t"
            "v_max3_f32 v228, v228, v164, v165\n\t"
            "v_max3_f32 v229, v229, v188, v189\n\t"
            "v_mfma_f32_16x16x16_bf16 v[180:183], v[152:153], v[198:199], 0\n\t"
            "v_max3_f32 v228, v228, v166, v167\n\t"
            "v_max3_f32 v229, v229, v190, v191\n\t"
            "s_nop 1\n\t"
            "v_permlane32_swap_b32 v228, v229\n\t"
            "v_max_f32 v228, v228, v229\n\t"
            "v_mov_b32 v231, v228\n\t"
            "v_mfma_f32_16x16x32_bf16 v[180:183], v[144:147], v[200:203], v[180:183]\n\t"
            "s_setprio 0\n\t"
            "v_permlane16_swap_b32 v228, v231\n\t"
            "v_max_f32 v228, v228, v231\n\t"
            "v_mov_b32 v229, v228\n\t"
            "s_nop 1\n\t"
            "v_permlane32_swap_b32 v228, v229\n\t"
            "v_mul_f32_e64 v228, -v228, v236\n\t"
            "v_fma_f32 v148, v148, v236, v228\n\t"
            "v_fma_f32 v149, v149, v236, v228\n\t"
            "v_exp_f32 v148, v148\n\t"
            "v_exp_f32 v149, v149\n\t"
            "v_fma_f32 v150, v150, v236, v228\n\t"
            "v_fma_f32 v151, v151, v236, v228\n\t"
            "v_exp_f32 v150, v150\n\t"
            "v_exp_f32 v151, v151\n\t"
            "v_fma_f32 v160, v160, v236, v228\n\t"
            "v_fma_f32 v161, v161, v236, v228\n\t"
            "v_fma_f32 v162, v162, v236, v228\n\t"
            "v_fma_f32 v163, v163, v236, v228\n\t"
            "v_exp_f32 v160, v160\n\t"
            "v_exp_f32 v161, v161\n\t"
            "v_exp_f32 v162, v162\n\t"
            "v_exp_f32 v163, v163\n\t"
            "v_cvt_pk_bf16_f32 v148, v148, v149\n\t"
            "v_cvt_pk_bf16_f32 v149, v150, v151\n\t"
            "v_cvt_pk_bf16_f32 v150, v160, v161\n\t"
            "v_cvt_pk_bf16_f32 v151, v162, v163\n\t"
            "s_setprio 3\n\t"
            "v_mfma_f32_16x16x16_bf16 v[160:163], v[222:223], v[176:177], 0\n\t"
            "s_setprio 0\n\t"
            "v_mul_f32_e64 v229, -v229, v237\n\t"
            "v_fma_f32 v188, v188, v237, v229\n\t"
            "v_fma_f32 v189, v189, v237, v229\n\t"
            "v_exp_f32 v188, v188\n\t"
            "v_exp_f32 v189, v189\n\t"
            "v_fma_f32 v190, v190, v237, v229\n\t"
            "s_setprio 3\n\t"
            "v_mfma_f32_16x16x32_bf16 v[160:163], v[224:227], v[168:171], v[160:163]\n\t"
            "s_setprio 0\n\t"
            "v_fma_f32 v191, v191, v237, v229\n\t"
            "v_fma_f32 v208, v208, v237, v229\n\t"
            "v_fma_f32 v184, v184, v237, v229\n\t"
            "v_fma_f32 v185, v185, v237, v229\n\t"
            "v_exp_f32 v190, v190\n\t"
            "v_exp_f32 v191, v191\n\t"
            "v_exp_f32 v208, v208\n\t"
            "v_exp_f32 v184, v184\n\t"
            "v_max3_f32 v234, v212, v160, v156\n\t"
            "v_max_f32 v234, v234, v230\n\t"
            "v_exp_f32 v185, v185\n\t"
            "v_fma_f32 v186, v186, v237, v229\n\t"
            "v_fma_f32 v187, v187, v237, v229\n\t"
            "v_fma_f32 v164, v164, v236, v228\n\t"
            "v_fma_f32 v165, v165, v236, v228\n\t"
            "v_exp_f32 v186, v186\n\t"
            "v_exp_f32 v187, v187\n\t"
            "v_fma_f32 v204, v204, v237, v229\n\t"
            "v_fma_f32 v205, v205, v237, v229\n\t"
            "v_fma_f32 v206, v206, v237, v229\n\t"
            "v_fma_f32 v207, v207, v237, v229\n\t"
            "v_cvt_pk_bf16_f32 v188, v188, v189\n\t"
            "v_exp_f32 v164, v164\n\t"
            "v_exp_f32 v165, v165\n\t"
            "v_fma_f32 v166, v166, v236, v228\n\t"
            "v_fma_f32 v167, v167, v236, v228\n\t"
            "v_fma_f32 v172, v172, v236, v228\n\t"
            "v_exp_f32 v204, v204\n\t"
            "v_exp_f32 v205, v205\n\t"
            "v_exp_f32 v206, v206\n\t"
            "v_exp_f32 v207, v207\n\t"
            "v_cvt_pk_bf16_f32 v189, v190, v191\n\t"
            "v_cvt_pk_bf16_f32 v208, v208, 0\n\t"
            "v_max_f32_dpp v234, v234, v234 row_ror:8 row_mask:0xf bank_mask:0xf\n\t"
            "v_exp_f32 v166, v166\n\t"
            "v_exp_f32 v167, v167\n\t"
            "v_exp_f32 v172, v172\n\t"
            "v_cvt_pk_bf16_f32 v184, v184, v185\n\t"
            "v_cndmask_b32_e64 v190, 0, v208, s[58:59]\n\t"
            "s_setprio 3\n\t"
            "v_mfma_f32_16x16x16_bf16 v[208:211], v[220:221], v[198:199], 0\n\t"
            "v_cvt_pk_bf16_f32 v185, v186, v187\n\t"
            "v_cvt_pk_bf16_f32 v164, v164, v165\n\t"
            "v_cvt_pk_bf16_f32 v186, v204, v205\n\t"
            "v_cvt_pk_bf16_f32 v187, v206, v207\n\t"
            "v_mfma_f32_16x16x16_bf16 v[204:207], v[176:177], v[198:199], 0\n\t"
            "v_max_f32_dpp v234, v234, v234 row_ror:4 row_mask:0xf bank_mask:0xf\n\t"
            "v_cvt_pk_bf16_f32 v165, v166, v167\n\t"
            "v_cvt_pk_bf16_f32 v172, v172, 0\n\t"
            "v_mfma_f32_16x16x32_bf16 v[208:211], v[216:219], v[200:203], v[208:211]\n\t"
            "v_cndmask_b32_e64 v166, 0, v172, s[58:59]\n\t"
            "v_max_f32_dpp v234, v234, v234 row_ror:2 row_mask:0xf bank_mask:0xf\n\t"
            "v_mfma_f32_16x16x16_bf16 v[172:175], v[196:197], v[198:199], 0\n\t"
            "v_cndmask_b32_e64 v235, v212, v160, s[60:61]\n\t"
            "v_cndmask_b32_e64 v235, v235, v156, s[62:63]\n\t"
            "v_max_f32_dpp v234, v234, v234 row_ror:1 row_mask:0xf bank_mask:0xf\n\t"
            "v_mfma_f32_16x16x32_bf16 v[204:207], v[168:171], v[200:203], v[204:207]\n\t"
            "v_cndmask_b32_e64 v235, v235, v230, s[64:65]\n\t"
            "v_sub_f32 v235, v235, v234\n\t"
            "v_mfma_f32_16x16x32_bf16 v[172:175], v[192:195], v[200:203], v[172:175]\n\t"
            "s_setprio 0\n\t"
            "v_mul_f32 v235, v239, v235\n\t"
            "v_exp_f32 v235, v235\n\t"
            "v_max3_f32 v230, v208, v180, v181\n\t"
            "v_cvt_pk_bf16_f32 v235, v235, 0\n\t"
            "v_cndmask_b32_e64 v235, 0, v235, s[66:67]\n\t"
            "v_max3_f32 v230, v230, v182, v183\n\t"
            "v_max3_f32 v230, v230, v204, v205\n\t"
            "v_max3_f32 v230, v230, v206, v207\n\t"
            "v_max3_f32 v230, v230, v172, v173\n\t"
            "v_max3_f32 v230, v230, v174, v175\n\t"
            "v_mov_b32 v231, v230\n\t"
            "v_mov_b32 v167, 0\n\t"
            "v_mov_b32 v191, 0\n\t"
            "v_permlane32_swap_b32 v230, v231\n\t"
            "v_max_f32 v230, v230, v231\n\t"
            "v_mov_b32 v231, v230\n\t"
            "ds_write_b16 v250, v235 offset:256\n\t"
            "s_nop 0\n\t"
            "v_permlane16_swap_b32 v230, v231\n\t"
            "v_max_f32 v230, v230, v231\n\t"
            "v_mul_f32_e64 v230, -v230, v238\n\t"
            "v_fma_f32 v180, v180, v238, v230\n\t"
            "v_fma_f32 v181, v181, v238, v230\n\t"
            "v_fma_f32 v172, v172, v238, v230\n\t"
            "v_fma_f32 v173, v173, v238, v230\n\t"
            "v_exp_f32 v180, v180\n\t"
            "v_exp_f32 v181, v181\n\t"
            "v_fma_f32 v182, v182, v238, v230\n\t"
            "v_fma_f32 v183, v183, v238, v230\n\t"
            "v_exp_f32 v172, v172\n\t"
            "v_exp_f32 v173, v173\n\t"
            "v_fma_f32 v174, v174, v238, v230\n\t"
            "v_fma_f32 v175, v175, v238, v230\n\t"
            "v_fma_f32 v208, v208, v238, v230\n\t"
            "v_exp_f32 v182, v182\n\t"
            "v_exp_f32 v183, v183\n\t"
            "v_fma_f32 v204, v204, v238, v230\n\t"
            "v_fma_f32 v205, v205, v238, v230\n\t"
            "v_fma_f32 v206, v206, v238, v230\n\t"
            "v_fma_f32 v207, v207, v238, v230\n\t"
            "v_exp_f32 v174, v174\n\t"
            "v_exp_f32 v175, v175\n\t"
            "v_exp_f32 v208, v208\n\t"
            "v_exp_f32 v204, v204\n\t"
            "v_exp_f32 v205, v205\n\t"
            "v_exp_f32 v206, v206\n\t"
            "v_exp_f32 v207, v207\n\t"
            "v_cvt_pk_bf16_f32 v180, v180, v181\n\t"
            "v_cvt_pk_bf16_f32 v172, v172, v173\n\t"
            "v_cvt_pk_bf16_f32 v181, v182, v183\n\t"
            "v_cvt_pk_bf16_f32 v173, v174, v175\n\t"
            "v_cvt_pk_bf16_f32 v208, v208, 0\n\t"
            "v_cvt_pk_bf16_f32 v182, v204, v205\n\t"
            "v_cvt_pk_bf16_f32 v183, v206, v207\n\t"
            "v_cndmask_b32_e64 v174, 0, v208, s[58:59]\n\t"
            "v_mov_b32 v175, 0\n\t"
            "s_barrier\n\t"
            "s_setprio 2\n\t"
            "s_cmp_eq_u32 s44, 0\n\t"
            "s_cbranch_scc1 .Lwa_last%=\n\t"
            "v_mfma_f32_16x16x32_bf16 v[200:203], v[240:243], v[148:151], 0\n\t"
            "ds_read_b64 v[176:177], v247\n\t"
            "ds_read_b64 v[196:197], v247 offset:1536\n\t"
            "v_mfma_f32_16x16x32_bf16 v[200:203], v[240:243], v[164:167], v[200:203]\n\t"
            "ds_read_b64 v[220:221], v247 offset:3072\n\t"
            "v_xor_b32 v234, s73, v254\n\t"
            "v_add_u32 v235, s45, v248\n\t"
            "v_add_u32 v232, s48, v244\n\t"
            "v_xor_b32 v233, 64, v232\n\t"
            "v_mov_b32 v179, 0\n\t"
            "v_mov_b32 v199, 0\n\t"
            "v_mov_b32 v223, 0\n\t"
            "v_rcp_f32 v201, v200\n\t"
            "v_xor_b32 v249, 0x100, v249\n\t"
            "ds_read_b128 v[168:171], v235\n\t"
            "ds_read_u16 v178, v234\n\t"
            "ds_read_b128 v[192:195], v235 offset:1536\n\t"
            "ds_read_u16 v198, v234 offset:128\n\t"
            "ds_read_b128 v[216:219], v235 offset:3072\n\t"
            "ds_read_u16 v222, v234 offset:256\n\t"
            "ds_read_b128 v[204:207], v232\n\t"
            "ds_read_b128 v[208:211], v233\n\t"
            "ds_read_b32 v236, v249\n\t"
            "ds_read_b32 v237, v249 offset:64\n\t"
            "ds_read_b32 v238, v249 offset:128\n\t"
            "v_mad_u32_u24 v234, v246, s75, v253\n\t"
            "s_waitcnt lgkmcnt(9)\n\t"
            "v_mfma_f32_16x16x32_bf16 v[224:227], v[168:171], v[148:151], 0\n\t"
            "v_mfma_f32_16x16x32_bf16 v[224:227], v[176:179], v[164:167], v[224:227]\n\t"
            "s_waitcnt lgkmcnt(7)\n\t"
            "v_mfma_f32_16x16x32_bf16 v[212:215], v[192:195], v[148:151], 0\n\t"
            "v_mfma_f32_16x16x32_bf16 v[212:215], v[196:199], v[164:167], v[212:215]\n\t"
            "s_waitcnt lgkmcnt(5)\n\t"
            "v_mfma_f32_16x16x32_bf16 v[148:151], v[216:219], v[148:151], 0\n\t"
            "v_mfma_f32_16x16x32_bf16 v[148:151], v[220:223], v[164:167], v[148:151]\n\t"
            "s_waitcnt lgkmcnt(3)\n\t"
            "v_mfma_f32_16x16x32_bf16 v[144:147], v[0:3], v[204:207], 0\n\t"
            "v_mfma_f32_16x16x32_bf16 v[144:147], v[4:7], v[208:211], v[144:147]\n\t"
            "v_mul_f32 v224, v201, v224\n\t"
            "v_mfma_f32_16x16x32_bf16 v[152:155], v[48:51], v[204:207], 0\n\t"
            "v_mul_f32 v225, v201, v225\n\t"
            "v_mul_f32 v226, v201, v226\n\t"
            "v_mfma_f32_16x16x32_bf16 v[152:155], v[52:55], v[208:211], v[152:155]\n\t"
            "v_mul_f32 v212, v201, v212\n\t"
            "v_mul_f32 v213, v201, v213\n\t"
            "v_mfma_f32_16x16x32_bf16 v[156:159], v[76:79], v[208:211], 0\n\t"
            "v_mul_f32 v227, v201, v227\n\t"
            "v_cvt_pk_bf16_f32 v224, v224, v225\n\t"
            "v_mfma_f32_16x16x32_bf16 v[156:159], v[72:75], v[204:207], v[156:159]\n\t"
            "v_mul_f32 v214, v201, v214\n\t"
            "v_mul_f32 v215, v201, v215\n\t"
            "v_mfma_f32_16x16x32_bf16 v[160:163], v[96:99], v[204:207], 0\n\t"
            "v_cvt_pk_bf16_f32 v212, v212, v213\n\t"
            "v_cvt_pk_bf16_f32 v225, v226, v227\n\t"
            "v_mfma_f32_16x16x32_bf16 v[160:163], v[100:103], v[208:211], v[160:163]\n\t"
            "v_cvt_pk_bf16_f32 v213, v214, v215\n\t"
            "v_mul_f32 v148, v201, v148\n\t"
            "v_mfma_f32_16x16x32_bf16 v[164:167], v[124:127], v[208:211], 0\n\t"
            "v_mul_f32 v149, v201, v149\n\t"
            "v_mul_f32 v150, v201, v150\n\t"
            "v_mfma_f32_16x16x32_bf16 v[164:167], v[120:123], v[204:207], v[164:167]\n\t"
            "ds_write_b64 v245, v[224:225]\n\t"
            "v_mfma_f32_16x16x32_bf16 v[224:227], v[168:171], v[184:187], 0\n\t"
            "v_mul_f32 v151, v201, v151\n\t"
            "v_cvt_pk_bf16_f32 v148, v148, v149\n\t"
            "v_mfma_f32_16x16x32_bf16 v[224:227], v[176:179], v[188:191], v[224:227]\n\t"
            "ds_write_b64 v245, v[212:213] offset:128\n\t"
            "v_mfma_f32_16x16x32_bf16 v[200:203], v[240:243], v[184:187], 0\n\t"
            "v_cvt_pk_bf16_f32 v149, v150, v151\n\t"
            "v_mfma_f32_16x16x32_bf16 v[200:203], v[240:243], v[188:191], v[200:203]\n\t"
            "ds_write_b64 v245, v[148:149] offset:256\n\t"
            "v_mfma_f32_16x16x32_bf16 v[212:215], v[192:195], v[184:187], 0\n\t"
            "v_mfma_f32_16x16x32_bf16 v[212:215], v[196:199], v[188:191], v[212:215]\n\t"
            "v_mfma_f32_16x16x32_bf16 v[184:187], v[216:219], v[184:187], 0\n\t"
            "v_mfma_f32_16x16x32_bf16 v[184:187], v[220:223], v[188:191], v[184:187]\n\t"
            "v_mfma_f32_16x16x32_bf16 v[148:151], v[28:31], v[208:211], 0\n\t"
            "ds_read_b128 v[208:211], v233 offset:128\n\t"
            "v_mfma_f32_16x16x32_bf16 v[148:151], v[24:27], v[204:207], v[148:151]\n\t"
            "ds_read_b128 v[204:207], v232 offset:128\n\t"
            "v_rcp_f32 v201, v200\n\t"
            "v_mfma_f32_16x16x32_bf16 v[188:191], v[192:195], v[180:183], 0\n\t"
            "v_mul_f32 v224, v201, v224\n\t"
            "v_mul_f32 v225, v201, v225\n\t"
            "v_mfma_f32_16x16x32_bf16 v[188:191], v[196:199], v[172:175], v[188:191]\n\t"
            "v_mul_f32 v226, v201, v226\n\t"
            "v_mul_f32 v227, v201, v227\n\t"
            "v_mul_f32 v212, v201, v212\n\t"
            "v_mul_f32 v213, v201, v213\n\t"
            "v_mul_f32 v214, v201, v214\n\t"
            "v_mul_f32 v215, v201, v215\n\t"
            "v_mul_f32 v184, v201, v184\n\t"
            "v_mul_f32 v185, v201, v185\n\t"
            "v_mul_f32 v186, v201, v186\n\t"
            "v_mul_f32 v187, v201, v187\n\t"
            "v_mfma_f32_16x16x32_bf16 v[200:203], v[240:243], v[180:183], 0\n\t"
            "v_cvt_pk_bf16_f32 v224, v224, v225\n\t"
            "v_cvt_pk_bf16_f32 v225, v226, v227\n\t"
            "v_mfma_f32_16x16x32_bf16 v[200:203], v[240:243], v[172:175], v[200:203]\n\t"
            "ds_write_b64 v245, v[224:225] offset:6144\n\t"
            "v_mfma_f32_16x16x32_bf16 v[224:227], v[168:171], v[180:183], 0\n\t"
            "v_cvt_pk_bf16_f32 v184, v184, v185\n\t"
            "v_cvt_pk_bf16_f32 v185, v186, v187\n\t"
            "v_mfma_f32_16x16x32_bf16 v[224:227], v[176:179], v[172:175], v[224:227]\n\t"
            "ds_read_b64 v[186:187], v234 offset:288\n\t"
            "ds_write_b64 v245, v[184:185] offset:6400\n\t"
            "s_waitcnt lgkmcnt(3)\n\t"
            "v_mfma_f32_16x16x32_bf16 v[144:147], v[8:11], v[204:207], v[144:147]\n\t"
            "ds_read_b64 v[184:185], v234 offset:256\n\t"
            "v_rcp_f32 v201, v200\n\t"
            "v_mfma_f32_16x16x32_bf16 v[144:147], v[12:15], v[208:211], v[144:147]\n\t"
            "v_mul_f32 v188, v201, v188\n\t"
            "v_mul_f32 v189, v201, v189\n\t"
            "v_mfma_f32_16x16x32_bf16 v[148:151], v[36:39], v[208:211], v[148:151]\n\t"
            "v_mul_f32 v190, v201, v190\n\t"
            "v_mul_f32 v191, v201, v191\n\t"
            "v_mfma_f32_16x16x32_bf16 v[148:151], v[32:35], v[204:207], v[148:151]\n\t"
            "v_cvt_pk_bf16_f32 v188, v188, v189\n\t"
            "v_cvt_pk_bf16_f32 v189, v190, v191\n\t"
            "v_mfma_f32_16x16x32_bf16 v[152:155], v[56:59], v[204:207], v[152:155]\n\t"
            "ds_read_b64 v[190:191], v234 offset:352\n\t"
            "v_mul_f32 v224, v201, v224\n\t"
            "v_mfma_f32_16x16x32_bf16 v[152:155], v[60:63], v[208:211], v[152:155]\n\t"
            "ds_write_b64 v245, v[188:189] offset:12416\n\t"
            "v_mfma_f32_16x16x32_bf16 v[156:159], v[84:87], v[208:211], v[156:159]\n\t"
            "ds_read_b64 v[188:189], v234 offset:320\n\t"
            "v_mul_f32 v225, v201, v225\n\t"
            "v_mfma_f32_16x16x32_bf16 v[156:159], v[80:83], v[204:207], v[156:159]\n\t"
            "v_cvt_pk_bf16_f32 v212, v212, v213\n\t"
            "v_mul_f32 v226, v201, v226\n\t"
            "v_mfma_f32_16x16x32_bf16 v[160:163], v[104:107], v[204:207], v[160:163]\n\t"
            "v_mul_f32 v227, v201, v227\n\t"
            "v_cvt_pk_bf16_f32 v224, v224, v225\n\t"
            "v_mfma_f32_16x16x32_bf16 v[160:163], v[108:111], v[208:211], v[160:163]\n\t"
            "v_cvt_pk_bf16_f32 v213, v214, v215\n\t"
            "v_cvt_pk_bf16_f32 v225, v226, v227\n\t"
            "v_mfma_f32_16x16x32_bf16 v[164:167], v[132:135], v[208:211], v[164:167]\n\t"
            "ds_read_b128 v[208:211], v233 offset:6144\n\t"
            "v_mfma_f32_16x16x32_bf16 v[164:167], v[128:131], v[204:207], v[164:167]\n\t"
            "ds_read_b128 v[204:207], v232 offset:6144\n\t"
            "ds_write_b64 v245, v[212:213] offset:6272\n\t"
            "v_mfma_f32_16x16x32_bf16 v[180:183], v[216:219], v[180:183], 0\n\t"
            "ds_read_b128 v[212:215], v232 offset:256\n\t"
            "v_mfma_f32_16x16x32_bf16 v[180:183], v[220:223], v[172:175], v[180:183]\n\t"
            "ds_write_b64 v245, v[224:225] offset:12288\n\t"
            "ds_read_b128 v[224:227], v233 offset:256\n\t"
            "s_waitcnt lgkmcnt(6)\n\t"
            "v_mfma_f32_16x16x32_bf16 v[168:171], v[184:187], v[168:171], 0\n\t"
            "v_mfma_f32_16x16x32_bf16 v[168:171], v[188:191], v[176:179], v[168:171]\n\t"
            "v_mfma_f32_16x16x32_bf16 v[192:195], v[184:187], v[192:195], 0\n\t"
            "v_mfma_f32_16x16x32_bf16 v[192:195], v[188:191], v[196:199], v[192:195]\n\t"
            "s_nop 0\n\t"
            "v_mul_f32 v180, v201, v180\n\t"
            "v_mfma_f32_16x16x32_bf16 v[216:219], v[184:187], v[216:219], 0\n\t"
            "v_mul_f32 v181, v201, v181\n\t"
            "v_mul_f32 v182, v201, v182\n\t"
            "v_mfma_f32_16x16x32_bf16 v[216:219], v[188:191], v[220:223], v[216:219]\n\t"
            "v_mul_f32 v183, v201, v183\n\t"
            "v_cvt_pk_bf16_f32 v180, v180, v181\n\t"
            "v_mfma_f32_16x16x32_bf16 v[200:203], v[240:243], v[184:187], 0\n\t"
            "v_cvt_pk_bf16_f32 v181, v182, v183\n\t"
            "v_mfma_f32_16x16x32_bf16 v[200:203], v[240:243], v[188:191], v[200:203]\n\t"
            "ds_write_b64 v245, v[180:181] offset:12544\n\t"
            "s_waitcnt lgkmcnt(1)\n\t"
            "v_mfma_f32_16x16x32_bf16 v[144:147], v[16:19], v[212:215], v[144:147]\n\t"
            "v_mfma_f32_16x16x32_bf16 v[144:147], v[20:23], v[224:227], v[144:147]\n\t"
            "v_mfma_f32_16x16x32_bf16 v[148:151], v[44:47], v[224:227], v[148:151]\n\t"
            "v_mfma_f32_16x16x32_bf16 v[148:151], v[40:43], v[212:215], v[148:151]\n\t"
            "v_mfma_f32_16x16x32_bf16 v[152:155], v[64:67], v[212:215], v[152:155]\n\t"
            "s_nop 0\n\t"
            "v_cndmask_b32_e64 v203, v168, v192, s[60:61]\n\t"
            "v_mfma_f32_16x16x32_bf16 v[152:155], v[68:71], v[224:227], v[152:155]\n\t"
            "v_rcp_f32 v201, v200\n\t"
            "v_mfma_f32_16x16x32_bf16 v[156:159], v[92:95], v[224:227], v[156:159]\n\t"
            "v_cndmask_b32_e64 v203, v203, v216, s[62:63]\n\t"
            "v_xad_u32 v202, s73, v254, v255\n\t"
            "v_mfma_f32_16x16x32_bf16 v[156:159], v[88:91], v[212:215], v[156:159]\n\t"
            "v_mul_f32_dpp v203, v201, v203 quad_perm:[0,0,0,0] row_mask:0xf bank_mask:0xf\n\t"
            "v_cvt_pk_bf16_f32 v203, v203, 0\n\t"
            "v_mfma_f32_16x16x32_bf16 v[160:163], v[112:115], v[212:215], v[160:163]\n\t"
            "v_mul_f32 v144, v236, v144\n\t"
            "v_mul_f32 v145, v236, v145\n\t"
            "v_mfma_f32_16x16x32_bf16 v[160:163], v[116:119], v[224:227], v[160:163]\n\t"
            "s_mov_b64 exec, s[68:69]\n\t"
            "ds_write_b16 v202, v203\n\t"
            "s_mov_b64 exec, 1\n\t"
            "ds_add_u32 v253, v246 offset:7856\n\t"
            "s_mov_b64 exec, -1\n\t"
            "v_mfma_f32_16x16x32_bf16 v[164:167], v[140:143], v[224:227], v[164:167]\n\t"
            "ds_read_b128 v[224:227], v233 offset:6272\n\t"
            "v_mul_f32 v152, v236, v152\n\t"
            "v_mfma_f32_16x16x32_bf16 v[164:167], v[136:139], v[212:215], v[164:167]\n\t"
            "ds_read_b128 v[212:215], v232 offset:6272\n\t"
            "v_mul_f32 v153, v236, v153\n\t"
            "v_mfma_f32_16x16x32_bf16 v[168:171], v[0:3], v[204:207], 0\n\t"
            "v_mul_f32 v146, v236, v146\n\t"
            "v_mul_f32 v147, v236, v147\n\t"
            "v_mfma_f32_16x16x32_bf16 v[168:171], v[4:7], v[208:211], v[168:171]\n\t"
            "v_mul_f32 v154, v236, v154\n\t"
            "v_mul_f32 v155, v236, v155\n\t"
            "v_mfma_f32_16x16x32_bf16 v[172:175], v[28:31], v[208:211], 0\n\t"
            "v_cvt_pk_bf16_f32 v144, v144, v145\n\t"
            "v_cvt_pk_bf16_f32 v152, v152, v153\n\t"
            "v_mfma_f32_16x16x32_bf16 v[172:175], v[24:27], v[204:207], v[172:175]\n\t"
            "v_cvt_pk_bf16_f32 v156, v156, v157\n\t"
            "v_mul_f32 v148, v236, v148\n\t"
            "v_mfma_f32_16x16x32_bf16 v[176:179], v[48:51], v[204:207], 0\n\t"
            "v_mul_f32 v149, v236, v149\n\t"
            "v_mul_f32 v150, v236, v150\n\t"
            "v_mfma_f32_16x16x32_bf16 v[176:179], v[52:55], v[208:211], v[176:179]\n\t"
            "v_mul_f32 v151, v236, v151\n\t"
            "v_cvt_pk_bf16_f32 v145, v146, v147\n\t"
            "v_mfma_f32_16x16x32_bf16 v[180:183], v[76:79], v[208:211], 0\n\t"
            "v_cvt_pk_bf16_f32 v153, v154, v155\n\t"
            "v_cvt_pk_bf16_f32 v157, v158, v159\n\t"
            "v_mfma_f32_16x16x32_bf16 v[180:183], v[72:75], v[204:207], v[180:183]\n\t"
            "v_cvt_pk_bf16_f32 v146, v148, v149\n\t"
            "v_cvt_pk_bf16_f32 v147, v150, v151\n\t"
            "v_mfma_f32_16x16x32_bf16 v[184:187], v[96:99], v[204:207], 0\n\t"
            "v_cvt_pk_bf16_f32 v158, v160, v161\n\t"
            "v_cvt_pk_bf16_f32 v159, v162, v163\n\t"
            "v_mfma_f32_16x16x32_bf16 v[184:187], v[100:103], v[208:211], v[184:187]\n\t"
            "v_cvt_pk_bf16_f32 v154, v164, v165\n\t"
            "v_cvt_pk_bf16_f32 v155, v166, v167\n\t"
            "v_mfma_f32_16x16x32_bf16 v[188:191], v[124:127], v[208:211], 0\n\t"
            "ds_read_b128 v[208:211], v233 offset:6400\n\t"
            "v_mul_f32 v236, 0x3e553b94, v236\n\t"
            "v_mfma_f32_16x16x32_bf16 v[188:191], v[120:123], v[204:207], v[188:191]\n\t"
            "ds_read_b128 v[204:207], v232 offset:6400\n\t"
            "s_waitcnt lgkmcnt(2)\n\t"
            "v_mfma_f32_16x16x32_bf16 v[168:171], v[8:11], v[212:215], v[168:171]\n\t"
            "v_mfma_f32_16x16x32_bf16 v[168:171], v[12:15], v[224:227], v[168:171]\n\t"
            "v_mfma_f32_16x16x32_bf16 v[172:175], v[36:39], v[224:227], v[172:175]\n\t"
            "v_mfma_f32_16x16x32_bf16 v[172:175], v[32:35], v[212:215], v[172:175]\n\t"
            "v_mfma_f32_16x16x32_bf16 v[176:179], v[56:59], v[212:215], v[176:179]\n\t"
            "v_mfma_f32_16x16x32_bf16 v[176:179], v[60:63], v[224:227], v[176:179]\n\t"
            "v_mfma_f32_16x16x32_bf16 v[180:183], v[84:87], v[224:227], v[180:183]\n\t"
            "v_mfma_f32_16x16x32_bf16 v[180:183], v[80:83], v[212:215], v[180:183]\n\t"
            "v_mfma_f32_16x16x32_bf16 v[184:187], v[104:107], v[212:215], v[184:187]\n\t"
            "v_mfma_f32_16x16x32_bf16 v[184:187], v[108:111], v[224:227], v[184:187]\n\t"
            "v_mfma_f32_16x16x32_bf16 v[188:191], v[132:135], v[224:227], v[188:191]\n\t"
            "ds_read_b128 v[224:227], v233 offset:12288\n\t"
            "v_mfma_f32_16x16x32_bf16 v[188:191], v[128:131], v[212:215], v[188:191]\n\t"
            "ds_read_b128 v[212:215], v232 offset:12288\n\t"
            "s_waitcnt lgkmcnt(2)\n\t"
            "v_mfma_f32_16x16x32_bf16 v[168:171], v[16:19], v[204:207], v[168:171]\n\t"
            "v_mfma_f32_16x16x32_bf16 v[168:171], v[20:23], v[208:211], v[168:171]\n\t"
            "v_mfma_f32_16x16x32_bf16 v[172:175], v[44:47], v[208:211], v[172:175]\n\t"
            "v_mfma_f32_16x16x32_bf16 v[172:175], v[40:43], v[204:207], v[172:175]\n\t"
            "v_mfma_f32_16x16x32_bf16 v[176:179], v[64:67], v[204:207], v[176:179]\n\t"
            "v_mfma_f32_16x16x32_bf16 v[176:179], v[68:71], v[208:211], v[176:179]\n\t"
            "v_mfma_f32_16x16x32_bf16 v[180:183], v[92:95], v[208:211], v[180:183]\n\t"
            "v_mfma_f32_16x16x32_bf16 v[180:183], v[88:91], v[204:207], v[180:183]\n\t"
            "v_mfma_f32_16x16x32_bf16 v[184:187], v[112:115], v[204:207], v[184:187]\n\t"
            "v_mfma_f32_16x16x32_bf16 v[184:187], v[116:119], v[208:211], v[184:187]\n\t"
            "v_mfma_f32_16x16x32_bf16 v[188:191], v[140:143], v[208:211], v[188:191]\n\t"
            "ds_read_b128 v[208:211], v233 offset:12416\n\t"
            "v_mfma_f32_16x16x32_bf16 v[188:191], v[136:139], v[204:207], v[188:191]\n\t"
            "ds_read_b128 v[204:207], v232 offset:12416\n\t"
            "s_branch .Lwa_loop%=\n\t"
            ".Lwa_last%=:\n\t"
            "s_setprio 2\n\t"
            "s_waitcnt lgkmcnt(0)\n\t"
            "s_nop 7\n\t"
            "v_mfma_f32_16x16x32_bf16 v[156:159], v[240:243], v[184:187], 0\n\t"
            "ds_read_b64 v[176:177], v247\n\t"
            "ds_read_b64 v[196:197], v247 offset:1536\n\t"
            "v_mfma_f32_16x16x32_bf16 v[156:159], v[240:243], v[188:191], v[156:159]\n\t"
            "ds_read_b64 v[220:221], v247 offset:3072\n\t"
            "v_xor_b32 v234, s73, v254\n\t"
            "v_mfma_f32_16x16x32_bf16 v[228:231], v[240:243], v[148:151], 0\n\t"
            "ds_read_u16 v178, v234\n\t"
            "ds_read_u16 v198, v234 offset:128\n\t"
            "v_mfma_f32_16x16x32_bf16 v[228:231], v[240:243], v[164:167], v[228:231]\n\t"
            "ds_read_u16 v222, v234 offset:256\n\t"
            "v_add_u32 v235, s45, v248\n\t"
            "v_mfma_f32_16x16x32_bf16 v[208:211], v[240:243], v[180:183], 0\n\t"
            "ds_read_b128 v[168:171], v235\n\t"
            "ds_read_b128 v[192:195], v235 offset:1536\n\t"
            "v_mfma_f32_16x16x32_bf16 v[208:211], v[240:243], v[172:175], v[208:211]\n\t"
            "ds_read_b128 v[216:219], v235 offset:3072\n\t"
            "v_mov_b32 v179, 0\n\t"
            "v_mov_b32 v199, 0\n\t"
            "v_mov_b32 v223, 0\n\t"
            "v_rcp_f32 v157, v156\n\t"
            "v_mad_u32_u24 v234, v246, s75, v253\n\t"
            "v_rcp_f32 v229, v228\n\t"
            "s_nop 0\n\t"
            "v_rcp_f32 v209, v208\n\t"
            "s_waitcnt lgkmcnt(2)\n\t"
            "v_mfma_f32_16x16x32_bf16 v[144:147], v[168:171], v[184:187], 0\n\t"
            "v_mfma_f32_16x16x32_bf16 v[144:147], v[176:179], v[188:191], v[144:147]\n\t"
            "s_waitcnt lgkmcnt(1)\n\t"
            "v_mfma_f32_16x16x32_bf16 v[152:155], v[192:195], v[184:187], 0\n\t"
            "v_mfma_f32_16x16x32_bf16 v[152:155], v[196:199], v[188:191], v[152:155]\n\t"
            "s_waitcnt lgkmcnt(0)\n\t"
            "v_mfma_f32_16x16x32_bf16 v[184:187], v[216:219], v[184:187], 0\n\t"
            "v_mfma_f32_16x16x32_bf16 v[184:187], v[220:223], v[188:191], v[184:187]\n\t"
            "ds_read_b64 v[188:189], v234 offset:320\n\t"
            "ds_read_b64 v[190:191], v234 offset:352\n\t"
            "v_mfma_f32_16x16x32_bf16 v[224:227], v[168:171], v[148:151], 0\n\t"
            "v_mul_f32 v144, v157, v144\n\t"
            "v_mul_f32 v145, v157, v145\n\t"
            "v_mfma_f32_16x16x32_bf16 v[224:227], v[176:179], v[164:167], v[224:227]\n\t"
            "v_mul_f32 v152, v157, v152\n\t"
            "v_mul_f32 v153, v157, v153\n\t"
            "v_mfma_f32_16x16x32_bf16 v[212:215], v[192:195], v[148:151], 0\n\t"
            "v_mul_f32 v184, v157, v184\n\t"
            "v_mul_f32 v185, v157, v185\n\t"
            "v_mfma_f32_16x16x32_bf16 v[212:215], v[196:199], v[164:167], v[212:215]\n\t"
            "v_mul_f32 v186, v157, v186\n\t"
            "v_mul_f32 v187, v157, v187\n\t"
            "v_mfma_f32_16x16x32_bf16 v[160:163], v[168:171], v[180:183], 0\n\t"
            "v_cvt_pk_bf16_f32 v184, v184, v185\n\t"
            "v_cvt_pk_bf16_f32 v185, v186, v187\n\t"
            "v_mfma_f32_16x16x32_bf16 v[160:163], v[176:179], v[172:175], v[160:163]\n\t"
            "ds_read_b64 v[186:187], v234 offset:288\n\t"
            "ds_write_b64 v245, v[184:185] offset:6400\n\t"
            "v_mfma_f32_16x16x32_bf16 v[204:207], v[192:195], v[180:183], 0\n\t"
            "ds_read_b64 v[184:185], v234 offset:256\n\t"
            "v_mul_f32 v224, v229, v224\n\t"
            "v_mfma_f32_16x16x32_bf16 v[204:207], v[196:199], v[172:175], v[204:207]\n\t"
            "v_mul_f32 v225, v229, v225\n\t"
            "v_mul_f32 v212, v229, v212\n\t"
            "v_mfma_f32_16x16x32_bf16 v[148:151], v[216:219], v[148:151], 0\n\t"
            "v_mul_f32 v213, v229, v213\n\t"
            "v_mul_f32 v160, v209, v160\n\t"
            "v_mfma_f32_16x16x32_bf16 v[148:151], v[220:223], v[164:167], v[148:151]\n\t"
            "v_mul_f32 v161, v209, v161\n\t"
            "v_mul_f32 v226, v229, v226\n\t"
            "v_mfma_f32_16x16x32_bf16 v[180:183], v[216:219], v[180:183], 0\n\t"
            "v_mul_f32 v204, v209, v204\n\t"
            "v_mul_f32 v205, v209, v205\n\t"
            "v_mfma_f32_16x16x32_bf16 v[180:183], v[220:223], v[172:175], v[180:183]\n\t"
            "v_mul_f32 v227, v229, v227\n\t"
            "v_cvt_pk_bf16_f32 v224, v224, v225\n\t"
            "v_mul_f32 v148, v229, v148\n\t"
            "v_mul_f32 v149, v229, v149\n\t"
            "v_mul_f32 v214, v229, v214\n\t"
            "v_mul_f32 v215, v229, v215\n\t"
            "v_cvt_pk_bf16_f32 v212, v212, v213\n\t"
            "v_mul_f32 v150, v229, v150\n\t"
            "v_mul_f32 v180, v209, v180\n\t"
            "v_mul_f32 v181, v209, v181\n\t"
            "v_mul_f32 v151, v229, v151\n\t"
            "v_cvt_pk_bf16_f32 v148, v148, v149\n\t"
            "v_mul_f32 v146, v157, v146\n\t"
            "v_mul_f32 v147, v157, v147\n\t"
            "v_cvt_pk_bf16_f32 v144, v144, v145\n\t"
            "v_mul_f32 v154, v157, v154\n\t"
            "v_mul_f32 v155, v157, v155\n\t"
            "s_waitcnt lgkmcnt(0)\n\t"
            "v_mfma_f32_16x16x32_bf16 v[200:203], v[240:243], v[184:187], 0\n\t"
            "v_cvt_pk_bf16_f32 v152, v152, v153\n\t"
            "v_mul_f32 v162, v209, v162\n\t"
            "v_mfma_f32_16x16x32_bf16 v[200:203], v[240:243], v[188:191], v[200:203]\n\t"
            "v_mul_f32 v163, v209, v163\n\t"
            "v_cvt_pk_bf16_f32 v160, v160, v161\n\t"
            "v_mfma_f32_16x16x32_bf16 v[168:171], v[184:187], v[168:171], 0\n\t"
            "v_mul_f32 v206, v209, v206\n\t"
            "v_mul_f32 v207, v209, v207\n\t"
            "v_mfma_f32_16x16x32_bf16 v[168:171], v[188:191], v[176:179], v[168:171]\n\t"
            "v_cvt_pk_bf16_f32 v204, v204, v205\n\t"
            "v_mul_f32 v182, v209, v182\n\t"
            "v_mfma_f32_16x16x32_bf16 v[192:195], v[184:187], v[192:195], 0\n\t"
            "v_rcp_f32 v201, v200\n\t"
            "v_mfma_f32_16x16x32_bf16 v[192:195], v[188:191], v[196:199], v[192:195]\n\t"
            "v_mul_f32 v183, v209, v183\n\t"
            "v_cvt_pk_bf16_f32 v180, v180, v181\n\t"
            "v_mfma_f32_16x16x32_bf16 v[216:219], v[184:187], v[216:219], 0\n\t"
            "v_cvt_pk_bf16_f32 v225, v226, v227\n\t"
            "v_cvt_pk_bf16_f32 v213, v214, v215\n\t"
            "v_mfma_f32_16x16x32_bf16 v[216:219], v[188:191], v[220:223], v[216:219]\n\t"
            "v_cvt_pk_bf16_f32 v149, v150, v151\n\t"
            "v_cvt_pk_bf16_f32 v145, v146, v147\n\t"
            "v_cndmask_b32_e64 v203, v168, v192, s[60:61]\n\t"
            "v_cvt_pk_bf16_f32 v153, v154, v155\n\t"
            "v_cvt_pk_bf16_f32 v161, v162, v163\n\t"
            "v_cvt_pk_bf16_f32 v205, v206, v207\n\t"
            "v_cvt_pk_bf16_f32 v181, v182, v183\n\t"
            "v_xad_u32 v202, s73, v254, v255\n\t"
            "v_cndmask_b32_e64 v203, v203, v216, s[62:63]\n\t"
            "ds_write_b64 v245, v[224:225]\n\t"
            "ds_write_b64 v245, v[212:213] offset:128\n\t"
            "ds_write_b64 v245, v[148:149] offset:256\n\t"
            "ds_write_b64 v245, v[144:145] offset:6144\n\t"
            "ds_write_b64 v245, v[152:153] offset:6272\n\t"
            "ds_write_b64 v245, v[160:161] offset:12288\n\t"
            "ds_write_b64 v245, v[204:205] offset:12416\n\t"
            "ds_write_b64 v245, v[180:181] offset:12544\n\t"
            "v_mul_f32_dpp v203, v201, v203 quad_perm:[0,0,0,0] row_mask:0xf bank_mask:0xf\n\t"
            "v_cvt_pk_bf16_f32 v203, v203, 0\n\t"
            "s_mov_b64 exec, s[68:69]\n\t"
            "ds_write_b16 v202, v203\n\t"
            "s_mov_b64 exec, 1\n\t"
            "ds_add_u32 v253, v246 offset:7856\n\t"
            "s_mov_b64 exec, -1\n\t"
            ".Lwa_done%=:\n\t"
            "s_waitcnt lgkmcnt(0)\n\t"
            "s_nop 7\n\t"
            : "+{v249}"(aRSw_), "+{s44}"(s44), "+{s48}"(s48), "+{s70}"(s70), "+{s72}"(s72), "+{s73}"(s73), "+{s74}"(s74), "+{s76}"(s76), "+{s77}"(s77)
            : "{v244}"(pE_), "{v245}"(aO_), "{v246}"(sel_), "{v247}"(aVB_), "{v248}"(pST_), "{v250}"(aPLw_), "{v251}"(aRSr_), "{v252}"(g16_), "{v253}"(g8_), "{v254}"(loVL_), "{v255}"(loOL_), "{v[240:243]}"(ones4), "{s47}"((ldsb + X_OFF) ^ (ldsb + X1_OFF)),
              "{s[58:59]}"(0xFFFFull), "{s[60:61]}"(0xFFFF0000ull), "{s[62:63]}"(0xFFFF00000000ull), "{s[64:65]}"(0xFFFF000000000000ull),
              "{s[66:67]}"(0x1FFFFFFFFFFFFull), "{s75}"(rsb), "{s45}"(ldsb + V_OFF + w * 4608), "{s[68:69]}"(0xFFFFFFFFFFFFull),
              "{s[84:87]}"(wr), "{s46}"(wave * 36864), "{s88}"(touch), "{s71}"(ldsb + O_OFF), "{s[54:55]}"(mrow), "{s[56:57]}"(0xF0000F0000F0000Full),
              "{s[78:79]}"(mrow & 0xF0000F0000F0000Full)
            : "v144", "v145", "v146", "v147", "v148", "v149", "v150", "v151", "v152", "v153", "v154", "v155", "v156", "v157", "v158", "v159", "v160", "v161", "v162", "v163", "v164", "v165", "v166", "v167", "v168", "v169", "v170", "v171", "v172", "v173", "v174", "v175", "v176", "v177", "v178", "v179", "v180", "v181", "v182", "v183", "v184", "v185", "v186", "v187", "v188", "v189", "v190", "v191", "v192", "v193", "v194", "v195", "v196", "v197", "v198", "v199", "v200", "v201", "v202", "v203", "v204", "v205", "v206", "v207", "v208", "v209", "v210", "v211", "v212", "v213", "v214", "v215", "v216", "v217", "v218", "v219", "v220", "v221", "v222", "v223", "v224", "v225", "v226", "v227", "v228", "v229", "v230", "v231", "v232", "v233", "v234", "v235", "v236", "v237", "v238", "v239", "v0", "v1", "v2", "v3", "v4", "v5", "v6", "v7", "v8", "v9", "v10", "v11", "v12", "v13", "v14", "v15", "v16", "v17", "v18", "v19", "v20", "v21", "v22", "v23", "v24", "v25", "v26", "v27", "v28", "v29", "v30", "v31", "v32", "v33", "v34", "v35", "v36", "v37", "v38", "v39", "v40", "v41", "v42", "v43", "v44", "v45", "v46", "v47", "v48", "v49", "v50", "v51", "v52", "v53", "v54", "v55", "v56", "v57", "v58", "v59", "v60", "v61", "v62", "v63", "v64", "v65", "v66", "v67", "v68", "v69", "v70", "v71", "v72", "v73", "v74", "v75", "v76", "v77", "v78", "v79", "v80", "v81", "v82", "v83", "v84", "v85", "v86", "v87", "v88", "v89", "v90", "v91", "v92", "v93", "v94", "v95", "v96", "v97", "v98", "v99", "v100", "v101", "v102", "v103", "v104", "v105", "v106", "v107", "v108", "v109", "v110", "v111", "v112", "v113", "v114", "v115", "v116", "v117", "v118", "v119", "v120", "v121", "v122", "v123", "v124", "v125", "v126", "v127", "v128", "v129", "v130", "v131", "v132", "v133", "v134", "v135", "v136", "v137", "v138", "v139", "v140", "v141", "v142", "v143",  "m0", "s49", "vcc", "scc", "memory");

        }
        if (wave == 0 && blockIdx.x == 0) {
            int ln_;
            asm volatile("v_mbcnt_lo_u32_b32 %0, -1, 0\n\tv_mbcnt_hi_u32_b32 %0, -1, %0" : "=v"(ln_));
            KARGS
            unsigned long long tg;
            asm volatile("s_load_dwordx2 %0, %1, %2\n\ts_waitcnt lgkmcnt(0)" : "=s"(tg) : "s"(wb), "n"(EPO_OFF) : "memory");
            const unsigned lo = (unsigned)tg + 1u, hi = (unsigned)(tg >> 32);
            const auto wq = __builtin_amdgcn_make_buffer_rsrc((void*)wb, 0, WS_BYTES, 0x00020000);
            for (int it_ = 0; it_ < 100000; ++it_) {
                bool bad = false;
#pragma unroll
                for (int k = 0; k < 2; ++k) {
                    const u32x4 a_ = __builtin_amdgcn_raw_buffer_load_b128(wq, ARR_OFF + ln_ * 16, k * 1024, SC1);
                    bad |= a_[0] != lo || a_[1] != hi || a_[2] != lo || a_[3] != hi;
                }
                if (__builtin_amdgcn_ballot_w64(bad) == 0ull) break;
                __builtin_amdgcn_s_sleep(2);
            }
            if (ln_ == 0) __builtin_amdgcn_raw_buffer_store_b64((u32x2){lo, hi}, wq, EPO_OFF, 0, SC1);
        }
        asm volatile("wk_code_end:");
        __builtin_amdgcn_endpgm();
    } else if (wave < 6) {
        LANE_SETUP
        {
            const unsigned rsb = ldsb + RSX_OFF;
            const unsigned pE_ = fE - X_OFF;
            const unsigned aVAw_ = ldsb + V_OFF + w * 9216 + lane * 16, aVBw_ = ldsb + V_OFF + w * 9216 + 1024 + lane * 8;
            const unsigned pST_ = lane * 16;
            unsigned aRSw_ = rsb + 256 + lane * 4, aRSr_ = rsb + (lane & 48);
            B_SCALARS
            unsigned s44 = 4;
            asm volatile(
            "s_waitcnt lgkmcnt(0)\n\t"
            "s_mov_b32 s83, 0x10000\n\t"
            "s_mov_b32 s84, -47104\n\t"
            "s_mov_b64 s[88:89], s[72:73]\n\t"
            "s_mov_b64 s[90:91], s[74:75]\n\t"
            "s_nop 7\n\t"
            "s_setprio 3\n\t"
            "s_barrier\n\t"
            "buffer_load_dwordx4 v[140:143], v245, s[28:31], s82 offen sc1\n\t"
            "s_add_u32 s49, s33, 0\n\t"
            "buffer_load_dwordx4 v[0:3], v245, s[28:31], s49 offen sc1\n\t"
            "s_add_u32 s49, s33, 6144\n\t"
            "buffer_load_dwordx4 v[24:27], v245, s[28:31], s49 offen sc1\n\t"
            "s_add_u32 s49, s33, 12288\n\t"
            "buffer_load_dwordx4 v[48:51], v245, s[28:31], s49 offen sc1\n\t"
            "s_add_u32 s49, s33, 18432\n\t"
            "buffer_load_dwordx4 v[72:75], v245, s[28:31], s49 offen sc1\n\t"
            "s_add_u32 s49, s33, 24576\n\t"
            "buffer_load_dwordx4 v[96:99], v245, s[28:31], s49 offen sc1\n\t"
            "s_add_u32 s49, s33, 30720\n\t"
            "buffer_load_dwordx4 v[120:123], v245, s[28:31], s49 offen sc1\n\t"
            "s_add_u32 s49, s33, 1024\n\t"
            "buffer_load_dwordx4 v[4:7], v245, s[28:31], s49 offen sc1\n\t"
            "s_add_u32 s49, s33, 7168\n\t"
            "buffer_load_dwordx4 v[28:31], v245, s[28:31], s49 offen sc1\n\t"
            "s_add_u32 s49, s33, 13312\n\t"
            "buffer_load_dwordx4 v[52:55], v245, s[28:31], s49 offen sc1\n\t"
            "s_add_u32 s49, s33, 19456\n\t"
            "buffer_load_dwordx4 v[76:79], v245, s[28:31], s49 offen sc1\n\t"
            "s_add_u32 s49, s33, 25600\n\t"
            "buffer_load_dwordx4 v[100:103], v245, s[28:31], s49 offen sc1\n\t"
            "s_add_u32 s49, s33, 31744\n\t"
            "buffer_load_dwordx4 v[124:127], v245, s[28:31], s49 offen sc1\n\t"
            "s_add_u32 s49, s33, 2048\n\t"
            "buffer_load_dwordx4 v[8:11], v245, s[28:31], s49 offen sc1\n\t"
            "s_add_u32 s49, s33, 8192\n\t"
            "buffer_load_dwordx4 v[32:35], v245, s[28:31], s49 offen sc1\n\t"
            "s_add_u32 s49, s33, 14336\n\t"
            "buffer_load_dwordx4 v[56:59], v245, s[28:31], s49 offen sc1\n\t"
            "s_add_u32 s49, s33, 20480\n\t"
            "buffer_load_dwordx4 v[80:83], v245, s[28:31], s49 offen sc1\n\t"
            "s_add_u32 s49, s33, 26624\n\t"
            "buffer_load_dwordx4 v[104:107], v245, s[28:31], s49 offen sc1\n\t"
            "s_add_u32 s49, s33, 32768\n\t"
            "buffer_load_dwordx4 v[128:131], v245, s[28:31], s49 offen sc1\n\t"
            "s_add_u32 s49, s33, 3072\n\t"
            "buffer_load_dwordx4 v[12:15], v245, s[28:31], s49 offen sc1\n\t"
            "s_add_u32 s49, s33, 9216\n\t"
            "buffer_load_dwordx4 v[36:39], v245, s[28:31], s49 offen sc1\n\t"
            "s_add_u32 s49, s33, 15360\n\t"
            "buffer_load_dwordx4 v[60:63], v245, s[28:31], s49 offen sc1\n\t"
            "s_add_u32 s49, s33, 21504\n\t"
            "buffer_load_dwordx4 v[84:87], v245, s[28:31], s49 offen sc1\n\t"
            "s_add_u32 s49, s33, 27648\n\t"
            "buffer_load_dwordx4 v[108:111], v245, s[28:31], s49 offen sc1\n\t"
            "s_add_u32 s49, s33, 33792\n\t"
            "buffer_load_dwordx4 v[132:135], v245, s[28:31], s49 offen sc1\n\t"
            "s_add_u32 s49, s33, 4096\n\t"
            "buffer_load_dwordx4 v[16:19], v245, s[28:31], s49 offen sc1\n\t"
            "s_add_u32 s49, s33, 10240\n\t"
            "buffer_load_dwordx4 v[40:43], v245, s[28:31], s49 offen sc1\n\t"
            "s_add_u32 s49, s33, 16384\n\t"
            "buffer_load_dwordx4 v[64:67], v245, s[28:31], s49 offen sc1\n\t"
            "s_add_u32 s49, s33, 22528\n\t"
            "buffer_load_dwordx4 v[88:91], v245, s[28:31], s49 offen sc1\n\t"
            "s_add_u32 s49, s33, 28672\n\t"
            "buffer_load_dwordx4 v[112:115], v245, s[28:31], s49 offen sc1\n\t"
            "s_add_u32 s49, s33, 34816\n\t"
            "buffer_load_dwordx4 v[136:139], v245, s[28:31], s49 offen sc1\n\t"
            "s_add_u32 s49, s33, 5120\n\t"
            "buffer_load_dwordx4 v[20:23], v245, s[28:31], s49 offen sc1\n\t"
            "s_add_u32 s49, s33, 11264\n\t"
            "buffer_load_dwordx4 v[44:47], v245, s[28:31], s49 offen sc1\n\t"
            "s_add_u32 s49, s33, 17408\n\t"
            "buffer_load_dwordx4 v[68:71], v245, s[28:31], s49 offen sc1\n\t"
            "s_add_u32 s49, s33, 23552\n\t"
            "buffer_load_dwordx4 v[92:95], v245, s[28:31], s49 offen sc1\n\t"
            "s_add_u32 s49, s33, 29696\n\t"
            "buffer_load_dwordx4 v[116:119], v245, s[28:31], s49 offen sc1\n\t"
            "s_add_u32 s49, s33, 35840\n\t"
            "buffer_load_dwordx4 v[140:143], v245, s[28:31], s49 offen sc1\n\t"
            "ds_read_b32 v249, v252 offset:12\n\t"
            "s_waitcnt lgkmcnt(0)\n\t"
            "v_and_b32 v249, s69, v249\n\t"
            "s_mov_b32 exec_hi, 0x3fffff\n\t"
            "s_add_u32 m0, s66, 0\n\t"
            "s_add_u32 s49, s79, 0\n\t"
            "buffer_load_dwordx4 v249, s[72:75], s49 offen lds\n\t"
            "s_mov_b32 exec_hi, -1\n\t"
            "s_mov_b32 exec_hi, 0x3fffff\n\t"
            "s_add_u32 m0, s66, 872\n\t"
            "s_add_u32 s49, s47, 196224\n\t"
            "buffer_load_dwordx4 v249, s[72:75], s49 offen lds\n\t"
            "s_mov_b32 exec_hi, -1\n\t"
            "s_mov_b32 exec_hi, 0x3fffff\n\t"
            "s_add_u32 m0, s66, 1744\n\t"
            "s_add_u32 s49, s47, 392448\n\t"
            "buffer_load_dwordx4 v249, s[72:75], s49 offen lds\n\t"
            "s_mov_b32 exec_hi, -1\n\t"
            "s_mov_b32 exec_hi, 0x3fffff\n\t"
            "s_add_u32 m0, s66, 2616\n\t"
            "s_add_u32 s49, s47, 588672\n\t"
            "buffer_load_dwordx4 v249, s[72:75], s49 offen lds\n\t"
            "s_mov_b32 exec_hi, -1\n\t"
            "s_mov_b32 exec_hi, 0x3fffff\n\t"
            "s_add_u32 m0, s66, 3488\n\t"
            "s_add_u32 s49, s47, 784896\n\t"
            "buffer_load_dwordx4 v249, s[72:75], s49 offen lds\n\t"
            "s_mov_b32 exec_hi, -1\n\t"
            "s_mov_b32 exec_hi, 0x3fffff\n\t"
            "s_add_u32 m0, s66, 4360\n\t"
            "s_add_u32 s49, s47, 981120\n\t"
            "buffer_load_dwordx4 v249, s[72:75], s49 offen lds\n\t"
            "s_mov_b32 exec_hi, -1\n\t"
            "s_mov_b32 exec_hi, 0x3fffff\n\t"
            "s_add_u32 m0, s66, 5232\n\t"
            "s_add_u32 s49, s47, 1177344\n\t"
            "buffer_load_dwordx4 v249, s[72:75], s49 offen lds\n\t"
            "s_mov_b32 exec_hi, -1\n\t"
            "s_mov_b32 exec_hi, 0x3fffff\n\t"
            "s_add_u32 m0, s66, 6104\n\t"
            "s_add_u32 s49, s47, 1373568\n\t"
            "buffer_load_dwordx4 v249, s[72:75], s49 offen lds\n\t"
            "s_mov_b32 exec_hi, -1\n\t"
            "s_mov_b32 exec_hi, 0x3fffff\n\t"
            "s_add_u32 m0, s66, 6976\n\t"
            "s_add_u32 s49, s47, 1569792\n\t"
            "buffer_load_dwordx4 v249, s[72:75], s49 offen lds\n\t"
            "s_mov_b32 exec_hi, -1\n\t"
            "ds_read_b128 v[168:171], v254\n\t"
            "ds_read_b128 v[172:175], v254 offset:64\n\t"
            "v_add_u32 v180, s48, v245\n\t"
            "v_add_u32 v232, s48, v242\n\t"
            "v_xor_b32 v233, 64, v232\n\t"
            "v_xor_b32 v246, 0x100, v246\n\t"
            "ds_read_b128 v[176:179], v180 offset:18816\n\t"
            "ds_read_b128 v[180:183], v180 offset:19840\n\t"
            "ds_read_b128 v[192:195], v232\n\t"
            "ds_read_b128 v[196:199], v233\n\t"
            "ds_read_b128 v[224:227], v232 offset:128\n\t"
            "ds_read_b128 v[228:231], v233 offset:128\n\t"
            "s_waitcnt lgkmcnt(5)\n\t"
            "v_add_f32 v176, v176, v178\n\t"
            "s_waitcnt lgkmcnt(4)\n\t"
            "v_add_f32 v180, v180, v182\n\t"
            "v_add_f32 v176, v176, v180\n\t"
            "v_add_f32 v177, v177, v179\n\t"
            "v_add_f32 v181, v181, v183\n\t"
            "v_mul_f32 v176, 0x3baaaaab, v176\n\t"
            "v_add_f32 v177, v177, v181\n\t"
            "v_mul_f32_e64 v176, v176, -v176\n\t"
            "v_fmac_f32 v176, 0x3baaaaab, v177\n\t"
            "v_max_f32 v176, 0, v176\n\t"
            "v_add_f32 v176, 0x3727c5ac, v176\n\t"
            "v_rsq_f32 v176, v176\n\t"
            "s_nop 0\n\t"
            "ds_write_b32 v246, v176\n\t"
            "s_setprio 3\n\t"
            "s_waitcnt vmcnt(9)\n\t"
            "s_waitcnt lgkmcnt(3)\n\t"
            "v_mfma_f32_16x16x32_bf16 v[200:203], v[192:195], v[0:3], 0\n\t"
            "v_mfma_f32_16x16x32_bf16 v[200:203], v[196:199], v[4:7], v[200:203]\n\t"
            "v_mfma_f32_16x16x32_bf16 v[204:207], v[196:199], v[28:31], 0\n\t"
            "v_mfma_f32_16x16x32_bf16 v[204:207], v[192:195], v[24:27], v[204:207]\n\t"
            "v_mfma_f32_16x16x32_bf16 v[208:211], v[192:195], v[48:51], 0\n\t"
            "v_mfma_f32_16x16x32_bf16 v[208:211], v[196:199], v[52:55], v[208:211]\n\t"
            "v_mfma_f32_16x16x32_bf16 v[212:215], v[196:199], v[76:79], 0\n\t"
            "v_mfma_f32_16x16x32_bf16 v[212:215], v[192:195], v[72:75], v[212:215]\n\t"
            "v_mfma_f32_16x16x32_bf16 v[216:219], v[192:195], v[96:99], 0\n\t"
            "v_mfma_f32_16x16x32_bf16 v[216:219], v[196:199], v[100:103], v[216:219]\n\t"
            "v_mfma_f32_16x16x32_bf16 v[220:223], v[196:199], v[124:127], 0\n\t"
            "v_mfma_f32_16x16x32_bf16 v[220:223], v[192:195], v[120:123], v[220:223]\n\t"
            "s_waitcnt lgkmcnt(1)\n\t"
            "v_mfma_f32_16x16x32_bf16 v[200:203], v[224:227], v[8:11], v[200:203]\n\t"
            "v_mfma_f32_16x16x32_bf16 v[200:203], v[228:231], v[12:15], v[200:203]\n\t"
            "v_mfma_f32_16x16x32_bf16 v[204:207], v[228:231], v[36:39], v[204:207]\n\t"
            "v_mfma_f32_16x16x32_bf16 v[204:207], v[224:227], v[32:35], v[204:207]\n\t"
            "v_mfma_f32_16x16x32_bf16 v[208:211], v[224:227], v[56:59], v[208:211]\n\t"
            "v_mfma_f32_16x16x32_bf16 v[208:211], v[228:231], v[60:63], v[208:211]\n\t"
            "v_mfma_f32_16x16x32_bf16 v[212:215], v[228:231], v[84:87], v[212:215]\n\t"
            "v_mfma_f32_16x16x32_bf16 v[212:215], v[224:227], v[80:83], v[212:215]\n\t"
            "v_mfma_f32_16x16x32_bf16 v[216:219], v[224:227], v[104:107], v[216:219]\n\t"
            "s_waitcnt vmcnt(0)\n\t"
            "ds_read_b64 v[144:145], v253\n\t"
            "v_mfma_f32_16x16x32_bf16 v[216:219], v[228:231], v[108:111], v[216:219]\n\t"
            "ds_read_b64 v[146:147], v253 offset:96\n\t"
            "ds_read_b64 v[148:149], v253 offset:1744\n\t"
            "v_mfma_f32_16x16x32_bf16 v[220:223], v[228:231], v[132:135], v[220:223]\n\t"
            "ds_read_b128 v[228:231], v233 offset:256\n\t"
            "ds_read_b64 v[150:151], v253 offset:1840\n\t"
            "v_mfma_f32_16x16x32_bf16 v[220:223], v[224:227], v[128:131], v[220:223]\n\t"
            "ds_read_b128 v[224:227], v232 offset:256\n\t"
            "ds_read_b64 v[152:153], v253 offset:8\n\t"
            "ds_read_b64 v[154:155], v253 offset:104\n\t"
            "ds_read_b64 v[156:157], v253 offset:1752\n\t"
            "ds_read_b64 v[158:159], v253 offset:1848\n\t"
            "ds_read_b64 v[160:161], v253 offset:16\n\t"
            "ds_read_b64 v[162:163], v253 offset:112\n\t"
            "ds_read_b64 v[164:165], v253 offset:1760\n\t"
            "ds_read_b64 v[166:167], v253 offset:1856\n\t"
            "s_waitcnt lgkmcnt(0)\n\t"
            "s_nop 7\n\t"
            ".p2align 6\n\t"
            "s_nop 0\n\t"
            "s_nop 0\n\t"
            "s_nop 0\n\t"
            "s_nop 0\n\t"
            ".Lwc_loop%=:\n\t"
            "s_setprio 1\n\t"
            "s_setprio 3\n\t"
            "v_mfma_f32_16x16x32_bf16 v[200:203], v[224:227], v[16:19], v[200:203]\n\t"
            "v_mov_b64 v[176:177], 0\n\t"
            "v_mov_b64 v[178:179], 0\n\t"
            "v_mfma_f32_16x16x32_bf16 v[200:203], v[228:231], v[20:23], v[200:203]\n\t"
            "v_mov_b64 v[180:181], 0\n\t"
            "v_mov_b64 v[182:183], 0\n\t"
            "s_waitcnt lgkmcnt(0)\n\t"
            "v_smfmac_f32_16x16x64_bf16 v[176:179], v[168:171], v[144:151], v255\n\t"
            "ds_read_b128 v[168:171], v254 offset:128\n\t"
            "v_smfmac_f32_16x16x64_bf16 v[180:183], v[172:175], v[152:159], v255\n\t"
            "ds_read_b64 v[144:145], v253 offset:24\n\t"
            "v_mfma_f32_16x16x32_bf16 v[204:207], v[228:231], v[44:47], v[204:207]\n\t"
            "ds_read_b64 v[146:147], v253 offset:120\n\t"
            "ds_read_b64 v[148:149], v253 offset:1768\n\t"
            "v_mfma_f32_16x16x32_bf16 v[204:207], v[224:227], v[40:43], v[204:207]\n\t"
            "ds_read_b64 v[150:151], v253 offset:1864\n\t"
            "ds_read_b128 v[172:175], v254 offset:192\n\t"
            "v_mfma_f32_16x16x32_bf16 v[208:211], v[224:227], v[64:67], v[208:211]\n\t"
            "ds_read_b64 v[152:153], v253 offset:32\n\t"
            "ds_read_b64 v[154:155], v253 offset:128\n\t"
            "v_mfma_f32_16x16x32_bf16 v[208:211], v[228:231], v[68:71], v[208:211]\n\t"
            "ds_read_b64 v[156:157], v253 offset:1776\n\t"
            "ds_read_b64 v[158:159], v253 offset:1872\n\t"
            "v_mfma_f32_16x16x32_bf16 v[212:215], v[228:231], v[92:95], v[212:215]\n\t"
            "v_mov_b32 v249, s76\n\t"
            "v_mov_b64 v[184:185], 0\n\t"
            "v_mfma_f32_16x16x32_bf16 v[212:215], v[224:227], v[88:91], v[212:215]\n\t"
            "ds_read_b32 v249, v249\n\t"
            "v_mov_b64 v[186:187], 0\n\t"
            "v_mfma_f32_16x16x32_bf16 v[216:219], v[224:227], v[112:115], v[216:219]\n\t"
            "v_mov_b64 v[188:189], 0\n\t"
            "v_mov_b64 v[190:191], 0\n\t"
            "v_mfma_f32_16x16x32_bf16 v[216:219], v[228:231], v[116:119], v[216:219]\n\t"
            "v_cvt_pk_bf16_f32 v176, v176, v177\n\t"
            "v_add_u32 v236, s67, v250\n\t"
            "s_waitcnt lgkmcnt(10)\n\t"
            "v_smfmac_f32_16x16x64_bf16 v[184:187], v[168:171], v[160:167], v255\n\t"
            "ds_read_b128 v[168:171], v254 offset:256\n\t"
            "s_waitcnt lgkmcnt(6)\n\t"
            "v_smfmac_f32_16x16x64_bf16 v[188:191], v[172:175], v[144:151], v255\n\t"
            "ds_read_b64 v[160:161], v253 offset:40\n\t"
            "v_mfma_f32_16x16x32_bf16 v[220:223], v[228:231], v[140:143], v[220:223]\n\t"
            "ds_read_b64 v[162:163], v253 offset:136\n\t"
            "ds_read_b64 v[164:165], v253 offset:1784\n\t"
            "v_mfma_f32_16x16x32_bf16 v[220:223], v[224:227], v[136:139], v[220:223]\n\t"
            "s_setprio 1\n\t"
            "ds_read_b64 v[166:167], v253 offset:1880\n\t"
            "ds_read_b128 v[172:175], v254 offset:320\n\t"
            "ds_read_b64 v[144:145], v253 offset:48\n\t"
            "ds_read_b64 v[146:147], v253 offset:144\n\t"
            "ds_read_b64 v[148:149], v253 offset:1792\n\t"
            "ds_read_b64 v[150:151], v253 offset:1888\n\t"
            "s_waitcnt lgkmcnt(14)\n\t"
            "ds_read_b128 v[224:227], v232 offset:6272\n\t"
            "s_waitcnt lgkmcnt(14)\n\t"
            "ds_read_b128 v[228:231], v233 offset:6272\n\t"
            "s_waitcnt lgkmcnt(12)\n\t"
            "v_readfirstlane_b32 s26, v249\n\t"
            "v_cvt_pk_bf16_f32 v177, v178, v179\n\t"
            "v_cvt_pk_bf16_f32 v178, v180, v181\n\t"
            "s_and_b32 s68, s26, 0x7ffffff\n\t"
            "s_add_u32 s68, s68, s78\n\t"
            "s_and_b32 s69, s26, 0x38000000\n\t"
            "s_or_b32 s69, s69, s39\n\t"
            "s_bitcmp1_b32 s26, 27\n\t"
            "s_cselect_b32 s79, 2.0, s68\n\t"
            "s_setprio 3\n\t"
            "v_mfma_f32_4x4x4_16b_bf16 v[196:199], v[176:177], v[176:177], 0\n\t"
            "s_setprio 1\n\t"
            "ds_read_b32 v249, v252 offset:12\n\t"
            "s_and_b32 s57, s56, 3\n\t"
            "s_lshl_b32 s58, s57, 3\n\t"
            "s_lshr_b32 m0, s54, s58\n\t"
            "s_lshl_b32 s58, s57, 4\n\t"
            "s_lshr_b64 s[36:37], s[50:51], s58\n\t"
            "s_and_b32 s57, s36, 0xffff\n\t"
            "s_add_u32 s57, s57, s67\n\t"
            "v_writelane_b32 v236, s57, 63\n\t"
            "v_writelane_b32 v236, s59, m0\n\t"
            "v_cvt_pk_bf16_f32 v179, v182, v183\n\t"
            "v_mov_b64 v[180:181], 0\n\t"
            "v_mov_b64 v[182:183], 0\n\t"
            "s_setprio 3\n\t"
            "v_mfma_f32_16x16x32_bf16 v[192:195], v[238:241], v[176:179], 0\n\t"
            "s_lshr_b64 s[36:37], s[52:53], s58\n\t"
            "s_and_b32 s58, s36, 0xffff\n\t"
            "s_add_u32 s58, s58, s67\n\t"
            "v_mfma_f32_4x4x4_16b_bf16 v[196:199], v[178:179], v[178:179], v[196:199]\n\t"
            "ds_write_b128 v236, v[176:179]\n\t"
            "s_waitcnt lgkmcnt(8)\n\t"
            "v_smfmac_f32_16x16x64_bf16 v[180:183], v[172:175], v[160:167], v255\n\t"
            "s_setprio 1\n\t"
            "s_and_b32 s38, s52, 0xffff\n\t"
            "s_add_u32 s38, s38, s67\n\t"
            "ds_read_b128 v[172:175], v254 offset:448\n\t"
            "ds_read_b64 v[160:161], v253 offset:64\n\t"
            "ds_read_b64 v[162:163], v253 offset:160\n\t"
            "ds_read_b64 v[164:165], v253 offset:1808\n\t"
            "ds_read_b64 v[166:167], v253 offset:1904\n\t"
            "v_mov_b64 v[176:177], 0\n\t"
            "v_mov_b64 v[178:179], 0\n\t"
            "v_cvt_pk_bf16_f32 v184, v184, v185\n\t"
            "v_cvt_pk_bf16_f32 v185, v186, v187\n\t"
            "s_setprio 3\n\t"
            "v_smfmac_f32_16x16x64_bf16 v[176:179], v[168:171], v[152:159], v255\n\t"
            "ds_read_b128 v[168:171], v254 offset:384\n\t"
            "v_mfma_f32_4x4x4_16b_bf16 v[196:199], v[184:185], v[184:185], v[196:199]\n\t"
            "s_setprio 1\n\t"
            "ds_read_b64 v[152:153], v253 offset:56\n\t"
            "s_waitcnt lgkmcnt(14)\n\t"
            "ds_read_b64 v[154:155], v253 offset:152\n\t"
            "s_waitcnt lgkmcnt(14)\n\t"
            "ds_read_b64 v[156:157], v253 offset:1800\n\t"
            "s_waitcnt lgkmcnt(14)\n\t"
            "ds_read_b64 v[158:159], v253 offset:1896\n\t"
            "v_xor_b32 v237, 16, v236\n\t"
            "v_cvt_pk_bf16_f32 v186, v188, v189\n\t"
            "v_cvt_pk_bf16_f32 v187, v190, v191\n\t"
            "v_mov_b64 v[188:189], 0\n\t"
            "v_mov_b64 v[190:191], 0\n\t"
            "s_setprio 3\n\t"
            "v_mfma_f32_16x16x32_bf16 v[192:195], v[238:241], v[184:187], v[192:195]\n\t"
            "s_waitcnt lgkmcnt(14)\n\t"
            "ds_write_b128 v237, v[184:187]\n\t"
            "v_mfma_f32_4x4x4_16b_bf16 v[196:199], v[186:187], v[186:187], v[196:199]\n\t"
            "s_setprio 1\n\t"
            "v_mov_b64 v[184:185], 0\n\t"
            "v_mov_b64 v[186:187], 0\n\t"
            "v_cvt_pk_bf16_f32 v176, v176, v177\n\t"
            "v_cvt_pk_bf16_f32 v177, v178, v179\n\t"
            "v_cvt_pk_bf16_f32 v178, v180, v181\n\t"
            "v_cvt_pk_bf16_f32 v179, v182, v183\n\t"
            "s_setprio 3\n\t"
            "v_mfma_f32_4x4x4_16b_bf16 v[196:199], v[176:177], v[176:177], v[196:199]\n\t"
            "s_waitcnt lgkmcnt(14)\n\t"
            "ds_write_b128 v236, v[176:179] offset:128\n\t"
            "s_waitcnt lgkmcnt(6)\n\t"
            "v_smfmac_f32_16x16x64_bf16 v[184:187], v[168:171], v[144:151], v255\n\t"
            "ds_read_b128 v[168:171], v254 offset:512\n\t"
            "s_waitcnt lgkmcnt(3)\n\t"
            "v_smfmac_f32_16x16x64_bf16 v[188:191], v[172:175], v[152:159], v255\n\t"
            "ds_read_b64 v[144:145], v253 offset:72\n\t"
            "v_mfma_f32_16x16x32_bf16 v[192:195], v[238:241], v[176:179], v[192:195]\n\t"
            "ds_read_b64 v[146:147], v253 offset:168\n\t"
            "ds_read_b64 v[148:149], v253 offset:1816\n\t"
            "v_mfma_f32_4x4x4_16b_bf16 v[196:199], v[178:179], v[178:179], v[196:199]\n\t"
            "s_setprio 1\n\t"
            "ds_read_b64 v[150:151], v253 offset:1912\n\t"
            "ds_read_b128 v[172:175], v254 offset:576\n\t"
            "ds_read_b64 v[152:153], v253 offset:80\n\t"
            "ds_read_b64 v[154:155], v253 offset:176\n\t"
            "ds_read_b64 v[156:157], v253 offset:1824\n\t"
            "ds_read_b64 v[158:159], v253 offset:1920\n\t"
            "v_mov_b64 v[176:177], 0\n\t"
            "v_mov_b64 v[178:179], 0\n\t"
            "v_mov_b64 v[180:181], 0\n\t"
            "v_mov_b64 v[182:183], 0\n\t"
            "v_cvt_pk_bf16_f32 v184, v184, v185\n\t"
            "v_cvt_pk_bf16_f32 v185, v186, v187\n\t"
            "v_cvt_pk_bf16_f32 v186, v188, v189\n\t"
            "v_cvt_pk_bf16_f32 v187, v190, v191\n\t"
            "s_setprio 3\n\t"
            "v_mfma_f32_4x4x4_16b_bf16 v[196:199], v[184:185], v[184:185], v[196:199]\n\t"
            "ds_write_b128 v237, v[184:187] offset:128\n\t"
            "s_waitcnt lgkmcnt(10)\n\t"
            "v_smfmac_f32_16x16x64_bf16 v[176:179], v[168:171], v[160:167], v255\n\t"
            "ds_read_b128 v[168:171], v254 offset:640\n\t"
            "v_mfma_f32_16x16x32_bf16 v[192:195], v[238:241], v[184:187], v[192:195]\n\t"
            "ds_read_b64 v[160:161], v253 offset:88\n\t"
            "ds_read_b64 v[162:163], v253 offset:184\n\t"
            "s_waitcnt lgkmcnt(8)\n\t"
            "v_smfmac_f32_16x16x64_bf16 v[180:183], v[172:175], v[144:151], v255\n\t"
            "ds_read_b64 v[164:165], v253 offset:1832\n\t"
            "v_mfma_f32_4x4x4_16b_bf16 v[196:199], v[186:187], v[186:187], v[196:199]\n\t"
            "s_setprio 1\n\t"
            "ds_read_b64 v[166:167], v253 offset:1928\n\t"
            "ds_read_b128 v[172:175], v254 offset:704\n\t"
            "v_mov_b64 v[184:185], 0\n\t"
            "v_mov_b64 v[186:187], 0\n\t"
            "v_mov_b64 v[188:189], 0\n\t"
            "v_mov_b64 v[190:191], 0\n\t"
            "v_add_u32 v248, s67, v251\n\t"
            "v_writelane_b32 v248, s58, 63\n\t"
            "v_writelane_b32 v248, s38, m0\n\t"
            "v_and_b32 v249, s69, v249\n\t"
            "v_cvt_pk_bf16_f32 v176, v176, v177\n\t"
            "v_cvt_pk_bf16_f32 v177, v178, v179\n\t"
            "v_cvt_pk_bf16_f32 v178, v180, v181\n\t"
            "v_cvt_pk_bf16_f32 v179, v182, v183\n\t"
            "s_setprio 3\n\t"
            "v_mfma_f32_4x4x4_16b_bf16 v[196:199], v[176:177], v[176:177], v[196:199]\n\t"
            "ds_write_b128 v236, v[176:179] offset:256\n\t"
            "s_waitcnt lgkmcnt(6)\n\t"
            "v_smfmac_f32_16x16x64_bf16 v[184:187], v[168:171], v[152:159], v255\n\t"
            "v_mov_b32 v234, 0\n\t"
            "v_mfma_f32_4x4x4_16b_bf16 v[196:199], v[178:179], v[178:179], v[196:199]\n\t"
            "v_xor_b32 v246, 0x100, v246\n\t"
            "v_mfma_f32_16x16x32_bf16 v[192:195], v[238:241], v[176:179], v[192:195]\n\t"
            "s_waitcnt lgkmcnt(1)\n\t"
            "v_smfmac_f32_16x16x64_bf16 v[188:191], v[172:175], v[160:167], v255\n\t"
            "s_setprio 1\n\t"
            "s_mov_b32 exec_hi, 0x3fffff\n\t"
            "s_add_u32 m0, s66, 0\n\t"
            "s_add_u32 s49, s79, 0\n\t"
            "buffer_load_dwordx4 v249, s[88:91], s49 offen lds\n\t"
            "s_mov_b32 exec_hi, -1\n\t"
            "s_mov_b32 exec_hi, 0x3fffff\n\t"
            "s_add_u32 m0, s66, 872\n\t"
            "s_add_u32 s49, s68, 196224\n\t"
            "buffer_load_dwordx4 v249, s[72:75], s49 offen lds\n\t"
            "s_mov_b32 exec_hi, -1\n\t"
            "s_mov_b32 exec_hi, 0x3fffff\n\t"
            "s_add_u32 m0, s66, 1744\n\t"
            "s_add_u32 s49, s68, 392448\n\t"
            "buffer_load_dwordx4 v249, s[72:75], s49 offen lds\n\t"
            "s_mov_b32 exec_hi, -1\n\t"
            "s_mov_b32 exec_hi, 0x3fffff\n\t"
            "s_add_u32 m0, s66, 2616\n\t"
            "s_add_u32 s49, s68, 588672\n\t"
            "buffer_load_dwordx4 v249, s[72:75], s49 offen lds\n\t"
            "s_mov_b32 exec_hi, -1\n\t"
            "s_mov_b32 exec_hi, 0x3fffff\n\t"
            "s_add_u32 m0, s66, 3488\n\t"
            "s_add_u32 s49, s68, 784896\n\t"
            "buffer_load_dwordx4 v249, s[72:75], s49 offen lds\n\t"
            "s_mov_b32 exec_hi, -1\n\t"
            "v_cvt_pk_bf16_f32 v184, v184, v185\n\t"
            "v_cvt_pk_bf16_f32 v185, v186, v187\n\t"
            "v_cvt_pk_bf16_f32 v186, v188, v189\n\t"
            "v_cvt_pk_bf16_f32 v187, v190, v191\n\t"
            "s_setprio 3\n\t"
            "v_mfma_f32_4x4x4_16b_bf16 v[196:199], v[184:185], v[184:185], v[196:199]\n\t"
            "s_mov_b32 exec_hi, 0x3fffff\n\t"
            "s_add_u32 m0, s66, 4360\n\t"
            "s_add_u32 s49, s68, 981120\n\t"
            "buffer_load_dwordx4 v249, s[72:75], s49 offen lds\n\t"
            "s_mov_b32 exec_hi, -1\n\t"
            "v_mfma_f32_4x4x4_16b_bf16 v[196:199], v[186:187], v[186:187], v[196:199]\n\t"
            "s_mov_b32 exec_hi, 0x3fffff\n\t"
            "s_add_u32 m0, s66, 5232\n\t"
            "s_add_u32 s49, s68, 1177344\n\t"
            "buffer_load_dwordx4 v249, s[72:75], s49 offen lds\n\t"
            "s_mov_b32 exec_hi, -1\n\t"
            "v_mfma_f32_16x16x32_bf16 v[192:195], v[238:241], v[184:187], v[192:195]\n\t"
            "s_setprio 1\n\t"
            "s_mov_b32 exec_hi, 0x3fffff\n\t"
            "s_add_u32 m0, s66, 6104\n\t"
            "s_add_u32 s49, s68, 1373568\n\t"
            "buffer_load_dwordx4 v249, s[72:75], s49 offen lds\n\t"
            "s_mov_b32 exec_hi, -1\n\t"
            "s_mov_b32 exec_hi, 0x3fffff\n\t"
            "s_add_u32 m0, s66, 6976\n\t"
            "s_add_u32 s49, s68, 1569792\n\t"
            "buffer_load_dwordx4 v249, s[72:75], s49 offen lds\n\t"
            "s_mov_b32 exec_hi, -1\n\t"
            "v_cndmask_b32_e64 v193, v196, v197, s[60:61]\n\t"
            "v_cndmask_b32_e64 v193, v193, v198, s[62:63]\n\t"
            "v_cndmask_b32_e64 v193, v193, v199, s[64:65]\n\t"
            "s_setprio 1\n\t"
            "ds_read_b128 v[196:199], v233 offset:6144\n\t"
            "ds_write_b64 v248, v[192:193] offset:18816\n\t"
            "ds_read_b128 v[192:195], v232 offset:6144\n\t"
            "ds_read_b64 v[248:249], v247 offset:8\n\t"
            "ds_write_b128 v237, v[184:187] offset:256\n\t"
            "ds_read_b64 v[236:237], v247\n\t"
            "s_mov_b64 exec, 1\n\t"
            "ds_inc_u32 v255, v238 offset:7860\n\t"
            "s_mov_b64 exec, -1\n\t"
            "s_waitcnt lgkmcnt(4)\n\t"
            "v_mfma_f32_16x16x32_bf16 v[144:147], v[192:195], v[0:3], 0\n\t"
            "s_waitcnt lgkmcnt(3)\n\t"
            "v_mul_f32 v202, v248, v202\n\t"
            "v_mul_f32 v203, v249, v203\n\t"
            "v_mfma_f32_16x16x32_bf16 v[144:147], v[196:199], v[4:7], v[144:147]\n\t"
            "v_mul_f32 v206, v248, v206\n\t"
            "s_waitcnt lgkmcnt(1)\n\t"
            "v_mul_f32 v200, v236, v200\n\t"
            "v_mfma_f32_16x16x32_bf16 v[148:151], v[196:199], v[28:31], 0\n\t"
            "v_mul_f32 v201, v237, v201\n\t"
            "v_mul_f32 v204, v236, v204\n\t"
            "v_mfma_f32_16x16x32_bf16 v[148:151], v[192:195], v[24:27], v[148:151]\n\t"
            "v_mul_f32 v205, v237, v205\n\t"
            "v_mul_f32 v208, v236, v208\n\t"
            "v_mfma_f32_16x16x32_bf16 v[168:171], v[192:195], v[48:51], 0\n\t"
            "v_mul_f32 v209, v237, v209\n\t"
            "v_mul_f32 v212, v236, v212\n\t"
            "v_mfma_f32_16x16x32_bf16 v[168:171], v[196:199], v[52:55], v[168:171]\n\t"
            "v_mul_f32 v213, v237, v213\n\t"
            "v_mul_f32 v216, v236, v216\n\t"
            "v_mfma_f32_16x16x32_bf16 v[172:175], v[196:199], v[76:79], 0\n\t"
            "v_mul_f32 v217, v237, v217\n\t"
            "v_mul_f32 v220, v236, v220\n\t"
            "v_mfma_f32_16x16x32_bf16 v[172:175], v[192:195], v[72:75], v[172:175]\n\t"
            "v_mul_f32 v221, v237, v221\n\t"
            "v_mul_f32 v207, v249, v207\n\t"
            "v_mfma_f32_16x16x32_bf16 v[152:155], v[192:195], v[96:99], 0\n\t"
            "ds_read_b64 v[236:237], v247 offset:64\n\t"
            "v_mul_f32 v210, v248, v210\n\t"
            "v_mfma_f32_16x16x32_bf16 v[152:155], v[196:199], v[100:103], v[152:155]\n\t"
            "v_mul_f32 v211, v249, v211\n\t"
            "v_mul_f32 v214, v248, v214\n\t"
            "v_mfma_f32_16x16x32_bf16 v[156:159], v[196:199], v[124:127], 0\n\t"
            "ds_read_b128 v[196:199], v233 offset:6400\n\t"
            "v_mul_f32 v215, v249, v215\n\t"
            "v_mfma_f32_16x16x32_bf16 v[156:159], v[192:195], v[120:123], v[156:159]\n\t"
            "ds_read_b128 v[192:195], v232 offset:6400\n\t"
            "v_mul_f32 v218, v248, v218\n\t"
            "v_mfma_f32_16x16x32_bf16 v[144:147], v[224:227], v[8:11], v[144:147]\n\t"
            "v_mul_f32 v219, v249, v219\n\t"
            "v_mul_f32 v222, v248, v222\n\t"
            "v_mfma_f32_16x16x32_bf16 v[144:147], v[228:231], v[12:15], v[144:147]\n\t"
            "v_mul_f32 v223, v249, v223\n\t"
            "v_cvt_pk_bf16_f32 v212, v212, v213\n\t"
            "v_mfma_f32_16x16x32_bf16 v[148:151], v[228:231], v[36:39], v[148:151]\n\t"
            "ds_read_b64 v[248:249], v247 offset:72\n\t"
            "v_cvt_pk_bf16_f32 v208, v208, v209\n\t"
            "v_mfma_f32_16x16x32_bf16 v[148:151], v[224:227], v[32:35], v[148:151]\n\t"
            "v_cvt_pk_bf16_f32 v213, v214, v215\n\t"
            "v_cvt_pk_bf16_f32 v209, v210, v211\n\t"
            "v_mfma_f32_16x16x32_bf16 v[168:171], v[224:227], v[56:59], v[168:171]\n\t"
            "v_cvt_pk_bf16_f32 v200, v200, v201\n\t"
            "v_cvt_pk_bf16_f32 v204, v204, v205\n\t"
            "v_mfma_f32_16x16x32_bf16 v[168:171], v[228:231], v[60:63], v[168:171]\n\t"
            "v_cvt_pk_bf16_f32 v216, v216, v217\n\t"
            "v_cvt_pk_bf16_f32 v220, v220, v221\n\t"
            "v_mfma_f32_16x16x32_bf16 v[172:175], v[228:231], v[84:87], v[172:175]\n\t"
            "v_cvt_pk_bf16_f32 v201, v202, v203\n\t"
            "v_cvt_pk_bf16_f32 v205, v206, v207\n\t"
            "v_mfma_f32_16x16x32_bf16 v[172:175], v[224:227], v[80:83], v[172:175]\n\t"
            "v_cvt_pk_bf16_f32 v217, v218, v219\n\t"
            "v_cvt_pk_bf16_f32 v221, v222, v223\n\t"
            "v_mfma_f32_16x16x32_bf16 v[152:155], v[224:227], v[104:107], v[152:155]\n\t"
            "v_mfma_f32_16x16x32_bf16 v[152:155], v[228:231], v[108:111], v[152:155]\n\t"
            "v_mfma_f32_16x16x32_bf16 v[156:159], v[228:231], v[132:135], v[156:159]\n\t"
            "ds_read_b128 v[228:231], v233 offset:12288\n\t"
            "v_mfma_f32_16x16x32_bf16 v[156:159], v[224:227], v[128:131], v[156:159]\n\t"
            "ds_read_b128 v[224:227], v232 offset:12288\n\t"
            "s_waitcnt lgkmcnt(3)\n\t"
            "v_mfma_f32_16x16x32_bf16 v[144:147], v[192:195], v[16:19], v[144:147]\n\t"
            "v_mfma_f32_16x16x32_bf16 v[144:147], v[196:199], v[20:23], v[144:147]\n\t"
            "v_mfma_f32_16x16x32_bf16 v[148:151], v[196:199], v[44:47], v[148:151]\n\t"
            "v_mfma_f32_16x16x32_bf16 v[148:151], v[192:195], v[40:43], v[148:151]\n\t"
            "v_mfma_f32_16x16x32_bf16 v[168:171], v[192:195], v[64:67], v[168:171]\n\t"
            "v_mfma_f32_16x16x32_bf16 v[168:171], v[196:199], v[68:71], v[168:171]\n\t"
            "ds_read_b64 v[234:235], v234 offset:7856\n\t"
            "v_mfma_f32_16x16x32_bf16 v[172:175], v[196:199], v[92:95], v[172:175]\n\t"
            "s_nop 1\n\t"
            "v_mul_f32 v144, v236, v144\n\t"
            "v_mfma_f32_16x16x32_bf16 v[172:175], v[192:195], v[88:91], v[172:175]\n\t"
            "v_mul_f32 v145, v237, v145\n\t"
            "s_waitcnt lgkmcnt(3)\n\t"
            "v_mul_f32 v146, v248, v146\n\t"
            "v_mfma_f32_16x16x32_bf16 v[152:155], v[192:195], v[112:115], v[152:155]\n\t"
            "v_mul_f32 v168, v236, v168\n\t"
            "v_mul_f32 v169, v237, v169\n\t"
            "v_mfma_f32_16x16x32_bf16 v[152:155], v[196:199], v[116:119], v[152:155]\n\t"
            "v_mul_f32 v170, v248, v170\n\t"
            "v_mul_f32 v171, v249, v171\n\t"
            "v_mfma_f32_16x16x32_bf16 v[156:159], v[196:199], v[140:143], v[156:159]\n\t"
            "ds_read_b128 v[196:199], v233 offset:12416\n\t"
            "v_mul_f32 v172, v236, v172\n\t"
            "v_mfma_f32_16x16x32_bf16 v[156:159], v[192:195], v[136:139], v[156:159]\n\t"
            "ds_read_b128 v[192:195], v232 offset:12416\n\t"
            "v_mul_f32 v173, v237, v173\n\t"
            "s_waitcnt lgkmcnt(3)\n\t"
            "v_mfma_f32_16x16x32_bf16 v[160:163], v[224:227], v[0:3], 0\n\t"
            "v_cvt_pk_bf16_f32 v214, v172, v173\n\t"
            "v_cvt_pk_bf16_f32 v210, v168, v169\n\t"
            "v_mfma_f32_16x16x32_bf16 v[160:163], v[228:231], v[4:7], v[160:163]\n\t"
            "s_waitcnt lgkmcnt(2)\n\t"
            "v_readfirstlane_b32 s77, v234\n\t"
            "v_readfirstlane_b32 s38, v235\n\t"
            "s_add_u32 s77, s77, s38\n\t"
            "s_cmp_ge_u32 s77, s44\n\t"
            "s_cbranch_scc0 .Lwc_spin%=\n\t"
            ".Lwc_go%=:\n\t"
            "v_mfma_f32_16x16x32_bf16 v[164:167], v[228:231], v[28:31], 0\n\t"
            "s_add_u32 s59, s59, s84\n\t"
            "s_movk_i32 s84, 800\n\t"
            "v_cvt_pk_bf16_f32 v211, v170, v171\n\t"
            "v_mfma_f32_16x16x32_bf16 v[164:167], v[224:227], v[24:27], v[164:167]\n\t"
            "s_add_u32 s45, s46, s27\n\t"
            "s_mov_b32 s46, s47\n\t"
            "s_mov_b32 s47, s68\n\t"
            "v_mfma_f32_16x16x32_bf16 v[176:179], v[224:227], v[48:51], 0\n\t"
            "v_mul_f32 v174, v248, v174\n\t"
            "v_mul_f32 v175, v249, v175\n\t"
            "v_mfma_f32_16x16x32_bf16 v[176:179], v[228:231], v[52:55], v[176:179]\n\t"
            "v_add_u32 v172, s67, v245\n\t"
            "v_cvt_pk_bf16_f32 v215, v174, v175\n\t"
            "v_mfma_f32_16x16x32_bf16 v[180:183], v[228:231], v[76:79], 0\n\t"
            "ds_read_b128 v[168:171], v172 offset:18816\n\t"
            "ds_read_b128 v[172:175], v172 offset:19840\n\t"
            "v_mfma_f32_16x16x32_bf16 v[180:183], v[224:227], v[72:75], v[180:183]\n\t"
            "v_mul_f32 v148, v236, v148\n\t"
            "v_mul_f32 v149, v237, v149\n\t"
            "v_mfma_f32_16x16x32_bf16 v[184:187], v[224:227], v[96:99], 0\n\t"
            "v_mul_f32 v152, v236, v152\n\t"
            "v_mul_f32 v153, v237, v153\n\t"
            "v_mfma_f32_16x16x32_bf16 v[184:187], v[228:231], v[100:103], v[184:187]\n\t"
            "v_mul_f32 v156, v236, v156\n\t"
            "v_mul_f32 v157, v237, v157\n\t"
            "v_mfma_f32_16x16x32_bf16 v[188:191], v[228:231], v[124:127], 0\n\t"
            "ds_read_b128 v[228:231], v233 offset:12544\n\t"
            "ds_read_b64 v[236:237], v247 offset:128\n\t"
            "v_mfma_f32_16x16x32_bf16 v[188:191], v[224:227], v[120:123], v[188:191]\n\t"
            "ds_read_b128 v[224:227], v232 offset:12544\n\t"
            "v_mul_f32 v147, v249, v147\n\t"
            "s_waitcnt lgkmcnt(5)\n\t"
            "v_mfma_f32_16x16x32_bf16 v[160:163], v[192:195], v[8:11], v[160:163]\n\t"
            "v_mul_f32 v150, v248, v150\n\t"
            "v_mul_f32 v151, v249, v151\n\t"
            "v_mfma_f32_16x16x32_bf16 v[160:163], v[196:199], v[12:15], v[160:163]\n\t"
            "v_mul_f32 v154, v248, v154\n\t"
            "v_mul_f32 v155, v249, v155\n\t"
            "v_mfma_f32_16x16x32_bf16 v[164:167], v[196:199], v[36:39], v[164:167]\n\t"
            "v_mul_f32 v158, v248, v158\n\t"
            "v_mul_f32 v159, v249, v159\n\t"
            "v_mfma_f32_16x16x32_bf16 v[164:167], v[192:195], v[32:35], v[164:167]\n\t"
            "ds_read_b64 v[248:249], v247 offset:136\n\t"
            "s_waitcnt lgkmcnt(5)\n\t"
            "v_add_f32 v168, v168, v170\n\t"
            "v_mfma_f32_16x16x32_bf16 v[176:179], v[192:195], v[56:59], v[176:179]\n\t"
            "s_waitcnt lgkmcnt(4)\n\t"
            "v_add_f32 v172, v172, v174\n\t"
            "v_add_f32 v168, v168, v172\n\t"
            "v_mfma_f32_16x16x32_bf16 v[176:179], v[196:199], v[60:63], v[176:179]\n\t"
            "v_add_f32 v169, v169, v171\n\t"
            "v_add_f32 v173, v173, v175\n\t"
            "v_mfma_f32_16x16x32_bf16 v[180:183], v[196:199], v[84:87], v[180:183]\n\t"
            "v_mul_f32 v168, 0x3baaaaab, v168\n\t"
            "v_add_f32 v169, v169, v173\n\t"
            "v_mfma_f32_16x16x32_bf16 v[180:183], v[192:195], v[80:83], v[180:183]\n\t"
            "ds_read_b128 v[172:175], v254 offset:64\n\t"
            "v_mul_f32_e64 v168, v168, -v168\n\t"
            "v_mfma_f32_16x16x32_bf16 v[184:187], v[192:195], v[104:107], v[184:187]\n\t"
            "v_fmac_f32 v168, 0x3baaaaab, v169\n\t"
            "v_max_f32 v168, 0, v168\n\t"
            "v_mfma_f32_16x16x32_bf16 v[184:187], v[196:199], v[108:111], v[184:187]\n\t"
            "v_cvt_pk_bf16_f32 v202, v144, v145\n\t"
            "v_cvt_pk_bf16_f32 v203, v146, v147\n\t"
            "v_mfma_f32_16x16x32_bf16 v[188:191], v[196:199], v[132:135], v[188:191]\n\t"
            "v_cvt_pk_bf16_f32 v206, v148, v149\n\t"
            "v_cvt_pk_bf16_f32 v207, v150, v151\n\t"
            "v_mfma_f32_16x16x32_bf16 v[188:191], v[192:195], v[128:131], v[188:191]\n\t"
            "v_cvt_pk_bf16_f32 v218, v152, v153\n\t"
            "v_cvt_pk_bf16_f32 v219, v154, v155\n\t"
            "s_waitcnt lgkmcnt(2)\n\t"
            "v_mfma_f32_16x16x32_bf16 v[160:163], v[224:227], v[16:19], v[160:163]\n\t"
            "v_cvt_pk_bf16_f32 v222, v156, v157\n\t"
            "v_cvt_pk_bf16_f32 v223, v158, v159\n\t"
            "v_mfma_f32_16x16x32_bf16 v[160:163], v[228:231], v[20:23], v[160:163]\n\t"
            "v_add_f32 v168, 0x3727c5ac, v168\n\t"
            "v_rsq_f32 v168, v168\n\t"
            "v_mfma_f32_16x16x32_bf16 v[164:167], v[228:231], v[44:47], v[164:167]\n\t"
            "v_add_u32 v232, s67, v242\n\t"
            "v_xor_b32 v233, 64, v232\n\t"
            "v_mfma_f32_16x16x32_bf16 v[164:167], v[224:227], v[40:43], v[164:167]\n\t"
            "s_xor_b32 s48, s48, s71\n\t"
            "s_xor_b32 s67, s67, s71\n\t"
            "s_add_u32 s76, s76, 4\n\t"
            "s_add_u32 s44, s44, 8\n\t"
            "v_mfma_f32_16x16x32_bf16 v[176:179], v[224:227], v[64:67], v[176:179]\n\t"
            "s_lshl_b32 s56, s56, 2\n\t"
            "s_lshr_b32 s49, s26, 30\n\t"
            "s_or_b32 s56, s56, s49\n\t"
            "v_mfma_f32_16x16x32_bf16 v[176:179], v[228:231], v[68:71], v[176:179]\n\t"
            "ds_read_b128 v[192:195], v232\n\t"
            "ds_read_b128 v[196:199], v233\n\t"
            "v_mfma_f32_16x16x32_bf16 v[180:183], v[228:231], v[92:95], v[180:183]\n\t"
            "v_mul_f32 v160, v236, v160\n\t"
            "v_mul_f32 v161, v237, v161\n\t"
            "v_mfma_f32_16x16x32_bf16 v[180:183], v[224:227], v[88:91], v[180:183]\n\t"
            "v_mul_f32 v164, v236, v164\n\t"
            "v_mul_f32 v165, v237, v165\n\t"
            "v_mfma_f32_16x16x32_bf16 v[184:187], v[224:227], v[112:115], v[184:187]\n\t"
            "v_mul_f32 v176, v236, v176\n\t"
            "v_mul_f32 v177, v237, v177\n\t"
            "v_mfma_f32_16x16x32_bf16 v[184:187], v[228:231], v[116:119], v[184:187]\n\t"
            "ds_write_b128 v243, v[200:203]\n\t"
            "v_mfma_f32_16x16x32_bf16 v[188:191], v[228:231], v[140:143], v[188:191]\n\t"
            "ds_read_b128 v[228:231], v233 offset:128\n\t"
            "v_mul_f32 v180, v236, v180\n\t"
            "v_mfma_f32_16x16x32_bf16 v[188:191], v[224:227], v[136:139], v[188:191]\n\t"
            "ds_read_b128 v[224:227], v232 offset:128\n\t"
            "v_mul_f32 v181, v237, v181\n\t"
            "s_waitcnt lgkmcnt(3)\n\t"
            "v_mfma_f32_16x16x32_bf16 v[200:203], v[192:195], v[0:3], 0\n\t"
            "v_mul_f32 v184, v236, v184\n\t"
            "v_mul_f32 v185, v237, v185\n\t"
            "v_mfma_f32_16x16x32_bf16 v[200:203], v[196:199], v[4:7], v[200:203]\n\t"
            "ds_write_b128 v243, v[204:207] offset:1536\n\t"
            "v_mfma_f32_16x16x32_bf16 v[204:207], v[196:199], v[28:31], 0\n\t"
            "v_mul_f32 v188, v236, v188\n\t"
            "v_mul_f32 v189, v237, v189\n\t"
            "v_mfma_f32_16x16x32_bf16 v[204:207], v[192:195], v[24:27], v[204:207]\n\t"
            "ds_write_b128 v243, v[208:211] offset:3072\n\t"
            "v_mfma_f32_16x16x32_bf16 v[208:211], v[192:195], v[48:51], 0\n\t"
            "v_mul_f32 v162, v248, v162\n\t"
            "v_mul_f32 v163, v249, v163\n\t"
            "v_mfma_f32_16x16x32_bf16 v[208:211], v[196:199], v[52:55], v[208:211]\n\t"
            "ds_write_b128 v243, v[212:215] offset:4608\n\t"
            "v_mfma_f32_16x16x32_bf16 v[212:215], v[196:199], v[76:79], 0\n\t"
            "v_cvt_pk_bf16_f32 v160, v160, v161\n\t"
            "v_mul_f32 v166, v248, v166\n\t"
            "v_mfma_f32_16x16x32_bf16 v[212:215], v[192:195], v[72:75], v[212:215]\n\t"
            "ds_write_b128 v243, v[216:219] offset:6144\n\t"
            "v_mfma_f32_16x16x32_bf16 v[216:219], v[192:195], v[96:99], 0\n\t"
            "v_mul_f32 v167, v249, v167\n\t"
            "v_cvt_pk_bf16_f32 v164, v164, v165\n\t"
            "v_mfma_f32_16x16x32_bf16 v[216:219], v[196:199], v[100:103], v[216:219]\n\t"
            "ds_write_b128 v243, v[220:223] offset:7680\n\t"
            "v_mfma_f32_16x16x32_bf16 v[220:223], v[196:199], v[124:127], 0\n\t"
            "v_mul_f32 v178, v248, v178\n\t"
            "v_mul_f32 v179, v249, v179\n\t"
            "v_mfma_f32_16x16x32_bf16 v[220:223], v[192:195], v[120:123], v[220:223]\n\t"
            "v_cvt_pk_bf16_f32 v176, v176, v177\n\t"
            "v_mul_f32 v182, v248, v182\n\t"
            "s_waitcnt lgkmcnt(5)\n\t"
            "v_mfma_f32_16x16x32_bf16 v[200:203], v[224:227], v[8:11], v[200:203]\n\t"
            "s_waitcnt vmcnt(0)\n\t"
            "ds_read_b64 v[144:145], v253\n\t"
            "ds_read_b64 v[146:147], v253 offset:96\n\t"
            "v_mfma_f32_16x16x32_bf16 v[200:203], v[228:231], v[12:15], v[200:203]\n\t"
            "ds_read_b64 v[148:149], v253 offset:1744\n\t"
            "ds_read_b64 v[150:151], v253 offset:1840\n\t"
            "v_mfma_f32_16x16x32_bf16 v[204:207], v[228:231], v[36:39], v[204:207]\n\t"
            "ds_read_b64 v[152:153], v253 offset:8\n\t"
            "ds_read_b64 v[154:155], v253 offset:104\n\t"
            "v_mfma_f32_16x16x32_bf16 v[204:207], v[224:227], v[32:35], v[204:207]\n\t"
            "ds_read_b64 v[156:157], v253 offset:1752\n\t"
            "ds_read_b64 v[158:159], v253 offset:1848\n\t"
            "v_mfma_f32_16x16x32_bf16 v[208:211], v[224:227], v[56:59], v[208:211]\n\t"
            "v_mul_f32 v183, v249, v183\n\t"
            "v_cvt_pk_bf16_f32 v180, v180, v181\n\t"
            "v_mfma_f32_16x16x32_bf16 v[208:211], v[228:231], v[60:63], v[208:211]\n\t"
            "v_mul_f32 v186, v248, v186\n\t"
            "v_mul_f32 v187, v249, v187\n\t"
            "v_mfma_f32_16x16x32_bf16 v[212:215], v[228:231], v[84:87], v[212:215]\n\t"
            "v_cvt_pk_bf16_f32 v184, v184, v185\n\t"
            "v_mul_f32 v190, v248, v190\n\t"
            "v_mfma_f32_16x16x32_bf16 v[212:215], v[224:227], v[80:83], v[212:215]\n\t"
            "v_mul_f32 v191, v249, v191\n\t"
            "v_cvt_pk_bf16_f32 v188, v188, v189\n\t"
            "v_mfma_f32_16x16x32_bf16 v[216:219], v[224:227], v[104:107], v[216:219]\n\t"
            "v_cvt_pk_bf16_f32 v161, v162, v163\n\t"
            "v_cvt_pk_bf16_f32 v165, v166, v167\n\t"
            "v_mfma_f32_16x16x32_bf16 v[216:219], v[228:231], v[108:111], v[216:219]\n\t"
            "ds_read_b64 v[162:163], v253 offset:112\n\t"
            "ds_read_b64 v[166:167], v253 offset:1856\n\t"
            "v_mfma_f32_16x16x32_bf16 v[220:223], v[228:231], v[132:135], v[220:223]\n\t"
            "s_waitcnt lgkmcnt(14)\n\t"
            "ds_read_b128 v[228:231], v233 offset:256\n\t"
            "v_cvt_pk_bf16_f32 v177, v178, v179\n\t"
            "v_mfma_f32_16x16x32_bf16 v[220:223], v[224:227], v[128:131], v[220:223]\n\t"
            "s_waitcnt lgkmcnt(14)\n\t"
            "ds_read_b128 v[224:227], v232 offset:256\n\t"
            "v_cvt_pk_bf16_f32 v181, v182, v183\n\t"
            "v_cvt_pk_bf16_f32 v185, v186, v187\n\t"
            "v_cvt_pk_bf16_f32 v189, v190, v191\n\t"
            "v_xor_b32 v247, 0x100, v247\n\t"
            "s_waitcnt lgkmcnt(14)\n\t"
            "ds_write_b64 v244, v[160:161]\n\t"
            "s_waitcnt lgkmcnt(14)\n\t"
            "ds_read_b64 v[160:161], v253 offset:16\n\t"
            "s_waitcnt lgkmcnt(14)\n\t"
            "ds_write_b64 v244, v[164:165] offset:1536\n\t"
            "s_waitcnt lgkmcnt(14)\n\t"
            "ds_read_b64 v[164:165], v253 offset:1760\n\t"
            "s_waitcnt lgkmcnt(14)\n\t"
            "ds_write_b64 v244, v[176:177] offset:3072\n\t"
            "s_waitcnt lgkmcnt(14)\n\t"
            "ds_write_b64 v244, v[180:181] offset:4608\n\t"
            "s_waitcnt lgkmcnt(14)\n\t"
            "ds_write_b64 v244, v[184:185] offset:6144\n\t"
            "s_waitcnt lgkmcnt(14)\n\t"
            "ds_write_b64 v244, v[188:189] offset:7680\n\t"
            "s_waitcnt lgkmcnt(14)\n\t"
            "ds_write_b32 v246, v168\n\t"
            "s_waitcnt lgkmcnt(14)\n\t"
            "ds_read_b128 v[168:171], v254\n\t"
            "s_waitcnt lgkmcnt(1)\n\t"
            "s_barrier\n\t"
            "s_cmp_lt_u32 s46, 0x7000000\n\t"
            "s_cbranch_scc1 .Lwc_loop%=\n\t"
            "s_branch .Lwc_end%=\n\t"
            ".Lwc_spin%=:\n\t"
            "s_cmp_eq_u32 s83, 0\n\t"
            "s_cbranch_scc1 .Lwc_go%=\n\t"
            "s_sub_u32 s83, s83, 1\n\t"
            "s_sleep 2\n\t"
            "v_mov_b32 v234, 0\n\t"
            "ds_read_b64 v[234:235], v234 offset:7856\n\t"
            "s_waitcnt lgkmcnt(0)\n\t"
            "v_readfirstlane_b32 s77, v234\n\t"
            "v_readfirstlane_b32 s38, v235\n\t"
            "s_add_u32 s77, s77, s38\n\t"
            "s_cmp_ge_u32 s77, s44\n\t"
            "s_cbranch_scc0 .Lwc_spin%=\n\t"
            "s_branch .Lwc_go%=\n\t"
            ".Lwc_end%=:\n\t"
            "s_waitcnt lgkmcnt(0)\n\t"
            "s_nop 7\n\t"
            : "+{v246}"(aRSw_), "+{v247}"(aRSr_), "+{s44}"(s44), B_SIO
            : "{v242}"(pE_), "{v243}"(aVAw_), "{v244}"(aVBw_), "{v245}"(pST_), "{v250}"(pXw_), "{v251}"(pSTw_), "{v252}"(aCA_), "{v253}"(aB_), "{v254}"(aA_), "{v255}"(idx_), B_SIN, "{s82}"(touch)
            : "v144", "v145", "v146", "v147", "v148", "v149", "v150", "v151", "v152", "v153", "v154", "v155", "v156", "v157", "v158", "v159", "v160", "v161", "v162", "v163", "v164", "v165", "v166", "v167", "v168", "v169", "v170", "v171", "v172", "v173", "v174", "v175", "v176", "v177", "v178", "v179", "v180", "v181", "v182", "v183", "v184", "v185", "v186", "v187", "v188", "v189", "v190", "v191", "v192", "v193", "v194", "v195", "v196", "v197", "v198", "v199", "v236", "v237", "v248", "v249", "v0", "v1", "v2", "v3", "v4", "v5", "v6", "v7", "v8", "v9", "v10", "v11", "v12", "v13", "v14", "v15", "v16", "v17", "v18", "v19", "v20", "v21", "v22", "v23", "v24", "v25", "v26", "v27", "v28", "v29", "v30", "v31", "v32", "v33", "v34", "v35", "v36", "v37", "v38", "v39", "v40", "v41", "v42", "v43", "v44", "v45", "v46", "v47", "v48", "v49", "v50", "v51", "v52", "v53", "v54", "v55", "v56", "v57", "v58", "v59", "v60", "v61", "v62", "v63", "v64", "v65", "v66", "v67", "v68", "v69", "v70", "v71", "v72", "v73", "v74", "v75", "v76", "v77", "v78", "v79", "v80", "v81", "v82", "v83", "v84", "v85", "v86", "v87", "v88", "v89", "v90", "v91", "v92", "v93", "v94", "v95", "v96", "v97", "v98", "v99", "v100", "v101", "v102", "v103", "v104", "v105", "v106", "v107", "v108", "v109", "v110", "v111", "v112", "v113", "v114", "v115", "v116", "v117", "v118", "v119", "v120", "v121", "v122", "v123", "v124", "v125", "v126", "v127", "v128", "v129", "v130", "v131", "v132", "v133", "v134", "v135", "v136", "v137", "v138", "v139", "v140", "v141", "v142", "v143", "v200", "v201", "v202", "v203", "v204", "v205", "v206", "v207", "v208", "v209", "v210", "v211", "v212", "v213", "v214", "v215", "v216", "v217", "v218", "v219", "v220", "v221", "v222", "v223", "v224", "v225", "v226", "v227", "v228", "v229", "v230", "v231", "v232", "v233", "v234", "v235", B_SCLOB);
        }
        {
            __builtin_amdgcn_s_setprio(3);
            int ln_;
            asm volatile("v_mbcnt_lo_u32_b32 %0, -1, 0\n\tv_mbcnt_hi_u32_b32 %0, -1, %0" : "=v"(ln_));
            const int l15_ = ln_ & 15, g_ = ln_ >> 4;
            KARGS
            const auto orsrc = __builtin_amdgcn_make_buffer_rsrc((void*)ob, 0, IMG * IMG * CH * 2, 0x00020000);
            const auto wq = __builtin_amdgcn_make_buffer_rsrc((void*)wb, 0, WS_BYTES, 0x00020000);
            bf16x8 W[36];
#pragma unroll
            for (int ks = 0; ks < 6; ++ks)
#pragma unroll
                for (int j = 0; j < 6; ++j) W[j * 6 + ks] = BC8(__builtin_amdgcn_raw_buffer_load_b128(wq, ln_ * 16, (wave + 2) * 36864 + (j * 6 + ks) * 1024, SC1));
            SB();
            int lE[2];
            unsigned o[2];
            f32x4 acc[2][6];
#pragma unroll
            for (int lt = 0; lt < 2; ++lt) {
                int i = 16 * lt + l15_;
                const unsigned en = *(const unsigned*)(smem + WL_OFF + i * 4);
                const bool ok = i < cnt;
                i = ok ? i : 0;
                lE[lt] = OL_OFF + i * 384 + ((g_ ^ ((i >> 1) & 7)) << 4);
                o[lt] = ok ? (en & 0x7ffffffu) + LPOS(en >> 31) * ROWBYTES + LPOS(en >> 30) * (CH * 2) + 192 * w + 16 * g_ : OOB;
#pragma unroll
                for (int k = 0; k < 3; ++k) {
                    const int c8 = 12 * w + 4 * k + g_, ws = (c8 * 43) >> 8, r6 = c8 - 6 * ws;
                    const u32x4 r = *(const u32x4*)(smem + LX_OFF(i) + (r6 >> 1) * 128 + ((2 * ws + (r6 & 1)) << 4));
                    acc[lt][2 * k] = (f32x4){bflo(r[0]), bfhi(r[0]), bflo(r[1]), bfhi(r[1])};
                    acc[lt][2 * k + 1] = (f32x4){bflo(r[2]), bfhi(r[2]), bflo(r[3]), bfhi(r[3])};
                }
            }
            SB();
            for (int it_ = 0; it_ < 100000; ++it_) {
                unsigned n_;
                asm volatile("ds_read_b32 %0, %1\n\ts_waitcnt lgkmcnt(0)" : "=v"(n_) : "v"(ldsb + FLAG_OFF) : "memory");
                if (__builtin_amdgcn_readfirstlane(n_) >= 4 * cnt) break;
                __builtin_amdgcn_s_sleep(1);
            }
            bf16x8 of[2][6];
#pragma unroll
            for (int ks = 0; ks < 6; ++ks)
#pragma unroll
                for (int lt = 0; lt < 2; ++lt) of[lt][ks] = *(const bf16x8*)(smem + ((ks & 1) ? (lE[lt] ^ 64) : lE[lt]) + (ks >> 1) * 128);
            SB();
#pragma unroll
            for (int lt = 0; lt < 2; ++lt) {
#pragma unroll
                for (int ks = 0; ks < 6; ++ks)
#pragma unroll
                    for (int j = 0; j < 6; ++j) acc[lt][j] = MFMA32(W[j * 6 + ks], of[lt][ks], acc[lt][j]);
#pragma unroll
                for (int k = 0; k < 3; ++k) {
                    const f32x4 a = acc[lt][2 * k], b = acc[lt][2 * k + 1];
                    const u32x4 ov = {pk2(a[0], a[1]), pk2(a[2], a[3]), pk2(b[0], b[1]), pk2(b[2], b[3])};
                    __builtin_amdgcn_raw_buffer_store_b128(ov, orsrc, o[lt] + 64 * k, 0, SC1);
                }
            }
        }
        __builtin_amdgcn_endpgm();
    } else {
        LANE_SETUP
        {
            const unsigned oE_ = ldsb + fE + (O_OFF - X_OFF), oO_ = oE_ ^ 64;
            unsigned l16_ = lane * 16;
            unsigned pr[3];
#pragma unroll
            for (int k = 0; k < 3; ++k) {
                const int c8 = 12 * (w - 2) + 4 * k + g;
                const int ws = c8 / 6, r6 = c8 - 6 * ws;
                pr[k] = l15 * 384 + (r6 >> 1) * 128 + (((2 * ws + (r6 & 1)) ^ sw) << 4);
            }
            const unsigned pR0_ = pr[0], pR1_ = pr[1], pR2_ = pr[2];
            unsigned q0 = ldsb + PF_OFF + 1024 + lane * 4, q1 = ldsb + O_OFF + lane * 16, q2 = oE_, q3 = ldsb + PF_OFF + 1024 + l15 * 4;
            unsigned q4 = ldsb + OL_OFF + l15 * 384 + (((4 * (w - 2) + (g >> 1)) ^ sw) << 4) + 8 * (g & 1);
            B_SCALARS
            asm volatile(
            "s_waitcnt lgkmcnt(0)\n\t"
            "s_mov_b32 s83, 0x10000\n\t"
            "s_mov_b32 s84, -47104\n\t"
            "s_mov_b64 s[88:89], s[72:73]\n\t"
            "s_mov_b64 s[90:91], s[74:75]\n\t"
            "s_nop 7\n\t"
            "s_barrier\n\t"
            "ds_read_b64 v[236:237], v252 offset:2560\n\t"
            "s_waitcnt lgkmcnt(0)\n\t"
            "s_mov_b64 exec, 1\n\t"
            "buffer_store_dwordx2 v[236:237], off, s[28:31], s44 sc1\n\t"
            "s_mov_b64 exec, -1\n\t"
            "s_sleep 30\n\t"
            "ds_read_b32 v249, v252 offset:12\n\t"
            "s_waitcnt lgkmcnt(0)\n\t"
            "v_and_b32 v249, s69, v249\n\t"
            "s_mov_b32 exec_hi, 0x3fffff\n\t"
            "s_add_u32 m0, s66, 0\n\t"
            "s_add_u32 s49, s79, 0\n\t"
            "buffer_load_dwordx4 v249, s[72:75], s49 offen lds\n\t"
            "s_mov_b32 exec_hi, -1\n\t"
            "s_mov_b32 exec_hi, 0x3fffff\n\t"
            "s_add_u32 m0, s66, 872\n\t"
            "s_add_u32 s49, s47, 196224\n\t"
            "buffer_load_dwordx4 v249, s[72:75], s49 offen lds\n\t"
            "s_mov_b32 exec_hi, -1\n\t"
            "s_mov_b32 exec_hi, 0x3fffff\n\t"
            "s_add_u32 m0, s66, 1744\n\t"
            "s_add_u32 s49, s47, 392448\n\t"
            "buffer_load_dwordx4 v249, s[72:75], s49 offen lds\n\t"
            "s_mov_b32 exec_hi, -1\n\t"
            "s_mov_b32 exec_hi, 0x3fffff\n\t"
            "s_add_u32 m0, s66, 2616\n\t"
            "s_add_u32 s49, s47, 588672\n\t"
            "buffer_load_dwordx4 v249, s[72:75], s49 offen lds\n\t"
            "s_mov_b32 exec_hi, -1\n\t"
            "s_mov_b32 exec_hi, 0x3fffff\n\t"
            "s_add_u32 m0, s66, 3488\n\t"
            "s_add_u32 s49, s47, 784896\n\t"
            "buffer_load_dwordx4 v249, s[72:75], s49 offen lds\n\t"
            "s_mov_b32 exec_hi, -1\n\t"
            "s_mov_b32 exec_hi, 0x3fffff\n\t"
            "s_add_u32 m0, s66, 4360\n\t"
            "s_add_u32 s49, s47, 981120\n\t"
            "buffer_load_dwordx4 v249, s[72:75], s49 offen lds\n\t"
            "s_mov_b32 exec_hi, -1\n\t"
            "s_mov_b32 exec_hi, 0x3fffff\n\t"
            "s_add_u32 m0, s66, 5232\n\t"
            "s_add_u32 s49, s47, 1177344\n\t"
            "buffer_load_dwordx4 v249, s[72:75], s49 offen lds\n\t"
            "s_mov_b32 exec_hi, -1\n\t"
            "s_mov_b32 exec_hi, 0x3fffff\n\t"
            "s_add_u32 m0, s66, 6104\n\t"
            "s_add_u32 s49, s47, 1373568\n\t"
            "buffer_load_dwordx4 v249, s[72:75], s49 offen lds\n\t"
            "s_mov_b32 exec_hi, -1\n\t"
            "s_mov_b32 exec_hi, 0x3fffff\n\t"
            "s_add_u32 m0, s66, 6976\n\t"
            "s_add_u32 s49, s47, 1569792\n\t"
            "buffer_load_dwordx4 v249, s[72:75], s49 offen lds\n\t"
            "s_mov_b32 exec_hi, -1\n\t"
            "s_add_u32 s49, s33, -73728\n\t"
            "buffer_load_dwordx4 v[0:3], v247, s[28:31], s49 offen sc1\n\t"
            "s_add_u32 s49, s33, -67584\n\t"
            "buffer_load_dwordx4 v[24:27], v247, s[28:31], s49 offen sc1\n\t"
            "s_add_u32 s49, s33, -61440\n\t"
            "buffer_load_dwordx4 v[48:51], v247, s[28:31], s49 offen sc1\n\t"
            "s_add_u32 s49, s33, -55296\n\t"
            "buffer_load_dwordx4 v[72:75], v247, s[28:31], s49 offen sc1\n\t"
            "s_add_u32 s49, s33, -49152\n\t"
            "buffer_load_dwordx4 v[96:99], v247, s[28:31], s49 offen sc1\n\t"
            "s_add_u32 s49, s33, -43008\n\t"
            "buffer_load_dwordx4 v[120:123], v247, s[28:31], s49 offen sc1\n\t"
            "s_add_u32 s49, s33, -72704\n\t"
            "buffer_load_dwordx4 v[4:7], v247, s[28:31], s49 offen sc1\n\t"
            "s_add_u32 s49, s33, -66560\n\t"
            "buffer_load_dwordx4 v[28:31], v247, s[28:31], s49 offen sc1\n\t"
            "s_add_u32 s49, s33, -60416\n\t"
            "buffer_load_dwordx4 v[52:55], v247, s[28:31], s49 offen sc1\n\t"
            "s_add_u32 s49, s33, -54272\n\t"
            "buffer_load_dwordx4 v[76:79], v247, s[28:31], s49 offen sc1\n\t"
            "s_add_u32 s49, s33, -48128\n\t"
            "buffer_load_dwordx4 v[100:103], v247, s[28:31], s49 offen sc1\n\t"
            "s_add_u32 s49, s33, -41984\n\t"
            "buffer_load_dwordx4 v[124:127], v247, s[28:31], s49 offen sc1\n\t"
            "s_add_u32 s49, s33, -71680\n\t"
            "buffer_load_dwordx4 v[8:11], v247, s[28:31], s49 offen sc1\n\t"
            "s_add_u32 s49, s33, -65536\n\t"
            "buffer_load_dwordx4 v[32:35], v247, s[28:31], s49 offen sc1\n\t"
            "s_add_u32 s49, s33, -59392\n\t"
            "buffer_load_dwordx4 v[56:59], v247, s[28:31], s49 offen sc1\n\t"
            "s_add_u32 s49, s33, -53248\n\t"
            "buffer_load_dwordx4 v[80:83], v247, s[28:31], s49 offen sc1\n\t"
            "s_add_u32 s49, s33, -47104\n\t"
            "buffer_load_dwordx4 v[104:107], v247, s[28:31], s49 offen sc1\n\t"
            "s_add_u32 s49, s33, -40960\n\t"
            "buffer_load_dwordx4 v[128:131], v247, s[28:31], s49 offen sc1\n\t"
            "s_add_u32 s49, s33, -70656\n\t"
            "buffer_load_dwordx4 v[12:15], v247, s[28:31], s49 offen sc1\n\t"
            "s_add_u32 s49, s33, -64512\n\t"
            "buffer_load_dwordx4 v[36:39], v247, s[28:31], s49 offen sc1\n\t"
            "s_add_u32 s49, s33, -58368\n\t"
            "buffer_load_dwordx4 v[60:63], v247, s[28:31], s49 offen sc1\n\t"
            "s_add_u32 s49, s33, -52224\n\t"
            "buffer_load_dwordx4 v[84:87], v247, s[28:31], s49 offen sc1\n\t"
            "s_add_u32 s49, s33, -46080\n\t"
            "buffer_load_dwordx4 v[108:111], v247, s[28:31], s49 offen sc1\n\t"
            "s_add_u32 s49, s33, -39936\n\t"
            "buffer_load_dwordx4 v[132:135], v247, s[28:31], s49 offen sc1\n\t"
            "s_add_u32 s49, s33, -69632\n\t"
            "buffer_load_dwordx4 v[16:19], v247, s[28:31], s49 offen sc1\n\t"
            "s_add_u32 s49, s33, -63488\n\t"
            "buffer_load_dwordx4 v[40:43], v247, s[28:31], s49 offen sc1\n\t"
            "s_add_u32 s49, s33, -57344\n\t"
            "buffer_load_dwordx4 v[64:67], v247, s[28:31], s49 offen sc1\n\t"
            "s_add_u32 s49, s33, -51200\n\t"
            "buffer_load_dwordx4 v[88:91], v247, s[28:31], s49 offen sc1\n\t"
            "s_add_u32 s49, s33, -45056\n\t"
            "buffer_load_dwordx4 v[112:115], v247, s[28:31], s49 offen sc1\n\t"
            "s_add_u32 s49, s33, -38912\n\t"
            "buffer_load_dwordx4 v[136:139], v247, s[28:31], s49 offen sc1\n\t"
            "s_add_u32 s49, s33, -68608\n\t"
            "buffer_load_dwordx4 v[20:23], v247, s[28:31], s49 offen sc1\n\t"
            "s_add_u32 s49, s33, -62464\n\t"
            "buffer_load_dwordx4 v[44:47], v247, s[28:31], s49 offen sc1\n\t"
            "s_add_u32 s49, s33, -56320\n\t"
            "buffer_load_dwordx4 v[68:71], v247, s[28:31], s49 offen sc1\n\t"
            "s_add_u32 s49, s33, -50176\n\t"
            "buffer_load_dwordx4 v[92:95], v247, s[28:31], s49 offen sc1\n\t"
            "s_add_u32 s49, s33, -44032\n\t"
            "buffer_load_dwordx4 v[116:119], v247, s[28:31], s49 offen sc1\n\t"
            "s_add_u32 s49, s33, -37888\n\t"
            "buffer_load_dwordx4 v[140:143], v247, s[28:31], s49 offen sc1\n\t"
            "s_setprio 3\n\t"
            "ds_read_b128 v[168:171], v254\n\t"
            "ds_read_b128 v[172:175], v254 offset:64\n\t"
            "ds_read_b128 v[220:223], v229 offset:12288\n\t"
            "ds_read_b128 v[224:227], v229 offset:13312\n\t"
            "ds_read_b128 v[200:203], v232\n\t"
            "ds_read_b128 v[208:211], v232 offset:128\n\t"
            "ds_read_b128 v[216:219], v232 offset:256\n\t"
            "s_waitcnt vmcnt(36)\n\t"
            "ds_read_b64 v[144:145], v253\n\t"
            "ds_read_b64 v[146:147], v253 offset:96\n\t"
            "ds_read_b64 v[148:149], v253 offset:1744\n\t"
            "ds_read_b64 v[150:151], v253 offset:1840\n\t"
            "ds_read_b64 v[152:153], v253 offset:8\n\t"
            "ds_read_b64 v[154:155], v253 offset:104\n\t"
            "ds_read_b64 v[156:157], v253 offset:1752\n\t"
            "ds_read_b64 v[158:159], v253 offset:1848\n\t"
            "s_waitcnt lgkmcnt(14)\n\t"
            "ds_read_b64 v[160:161], v253 offset:16\n\t"
            "s_waitcnt lgkmcnt(14)\n\t"
            "ds_read_b64 v[162:163], v253 offset:112\n\t"
            "s_waitcnt lgkmcnt(14)\n\t"
            "ds_read_b64 v[164:165], v253 offset:1760\n\t"
            "s_waitcnt lgkmcnt(14)\n\t"
            "ds_read_b64 v[166:167], v253 offset:1856\n\t"
            "v_mov_b64 v[176:177], 0\n\t"
            "v_mov_b64 v[178:179], 0\n\t"
            "v_mov_b64 v[180:181], 0\n\t"
            "v_mov_b64 v[182:183], 0\n\t"
            "v_mov_b32 v249, s76\n\t"
            "v_mov_b64 v[184:185], 0\n\t"
            "v_mov_b64 v[186:187], 0\n\t"
            "v_mov_b64 v[188:189], 0\n\t"
            "v_mov_b64 v[190:191], 0\n\t"
            "v_add_u32 v236, s67, v250\n\t"
            "v_add_u32 v248, s67, v251\n\t"
            "v_xor_b32 v235, 64, v232\n\t"
            "s_nop 0\n\t"
            "s_waitcnt lgkmcnt(14)\n\t"
            "ds_read_b32 v249, v249\n\t"
            "s_waitcnt lgkmcnt(14)\n\t"
            "ds_read_b128 v[204:207], v235\n\t"
            "s_waitcnt lgkmcnt(14)\n\t"
            "ds_read_b128 v[212:215], v235 offset:128\n\t"
            "v_add_f32 v220, v220, v222\n\t"
            "v_add_f32 v224, v224, v226\n\t"
            "v_add_f32 v220, v220, v224\n\t"
            "v_add_f32 v221, v221, v223\n\t"
            "v_add_f32 v225, v225, v227\n\t"
            "v_mul_f32 v220, 0x3baaaaab, v220\n\t"
            "v_add_f32 v221, v221, v225\n\t"
            "v_mul_f32_e64 v220, v220, -v220\n\t"
            "v_fmac_f32 v220, 0x3baaaaab, v221\n\t"
            "s_waitcnt lgkmcnt(11)\n\t"
            "v_smfmac_f32_16x16x64_bf16 v[176:179], v[168:171], v[144:151], v255\n\t"
            "ds_read_b128 v[168:171], v254 offset:128\n\t"
            "s_waitcnt lgkmcnt(8)\n\t"
            "v_smfmac_f32_16x16x64_bf16 v[180:183], v[172:175], v[152:159], v255\n\t"
            "ds_read_b64 v[144:145], v253 offset:24\n\t"
            "ds_read_b64 v[146:147], v253 offset:120\n\t"
            "ds_read_b64 v[148:149], v253 offset:1768\n\t"
            "ds_read_b64 v[150:151], v253 offset:1864\n\t"
            "ds_read_b128 v[172:175], v254 offset:192\n\t"
            "ds_read_b64 v[152:153], v253 offset:32\n\t"
            "ds_read_b64 v[154:155], v253 offset:128\n\t"
            "s_waitcnt lgkmcnt(14)\n\t"
            "ds_read_b64 v[156:157], v253 offset:1776\n\t"
            "s_waitcnt lgkmcnt(14)\n\t"
            "ds_read_b64 v[158:159], v253 offset:1872\n\t"
            "s_waitcnt lgkmcnt(14)\n\t"
            "ds_read_b128 v[224:227], v232 offset:6144\n\t"
            "v_cvt_pk_bf16_f32 v176, v176, v177\n\t"
            "v_cvt_pk_bf16_f32 v177, v178, v179\n\t"
            "v_cvt_pk_bf16_f32 v178, v180, v181\n\t"
            "v_cvt_pk_bf16_f32 v179, v182, v183\n\t"
            "v_mfma_f32_4x4x4_16b_bf16 v[196:199], v[176:177], v[176:177], 0\n\t"
            "v_mov_b64 v[180:181], 0\n\t"
            "v_mfma_f32_16x16x32_bf16 v[192:195], v[238:241], v[176:179], 0\n\t"
            "s_waitcnt lgkmcnt(13)\n\t"
            "v_readfirstlane_b32 s26, v249\n\t"
            "v_mov_b64 v[182:183], 0\n\t"
            "v_mfma_f32_4x4x4_16b_bf16 v[196:199], v[178:179], v[178:179], v[196:199]\n\t"
            "ds_read_b32 v249, v252 offset:12\n\t"
            "s_and_b32 s68, s26, 0x7ffffff\n\t"
            "s_add_u32 s68, s68, s78\n\t"
            "s_and_b32 s69, s26, 0x38000000\n\t"
            "s_or_b32 s69, s69, s39\n\t"
            "s_bitcmp1_b32 s26, 27\n\t"
            "s_cselect_b32 s79, 2.0, s68\n\t"
            "s_and_b32 s57, s56, 3\n\t"
            "s_lshl_b32 s58, s57, 3\n\t"
            "s_lshr_b32 m0, s54, s58\n\t"
            "s_lshl_b32 s58, s57, 4\n\t"
            "s_lshr_b64 s[36:37], s[50:51], s58\n\t"
            "s_and_b32 s57, s36, 0xffff\n\t"
            "s_add_u32 s57, s57, s67\n\t"
            "v_writelane_b32 v236, s57, 63\n\t"
            "v_writelane_b32 v236, s59, m0\n\t"
            "v_xor_b32 v237, 16, v236\n\t"
            "s_lshr_b64 s[36:37], s[52:53], s58\n\t"
            "s_and_b32 s58, s36, 0xffff\n\t"
            "s_add_u32 s58, s58, s67\n\t"
            "s_waitcnt lgkmcnt(11)\n\t"
            "v_smfmac_f32_16x16x64_bf16 v[184:187], v[168:171], v[160:167], v255\n\t"
            "s_and_b32 s38, s52, 0xffff\n\t"
            "s_add_u32 s38, s38, s67\n\t"
            "ds_read_b128 v[168:171], v254 offset:256\n\t"
            "ds_read_b64 v[160:161], v253 offset:40\n\t"
            "ds_read_b64 v[162:163], v253 offset:136\n\t"
            "ds_read_b64 v[164:165], v253 offset:1784\n\t"
            "s_waitcnt lgkmcnt(14)\n\t"
            "ds_read_b64 v[166:167], v253 offset:1880\n\t"
            "s_waitcnt lgkmcnt(11)\n\t"
            "v_smfmac_f32_16x16x64_bf16 v[188:191], v[172:175], v[144:151], v255\n\t"
            "ds_read_b128 v[172:175], v254 offset:320\n\t"
            "ds_read_b64 v[144:145], v253 offset:48\n\t"
            "ds_read_b64 v[146:147], v253 offset:144\n\t"
            "ds_read_b64 v[148:149], v253 offset:1792\n\t"
            "s_waitcnt lgkmcnt(14)\n\t"
            "ds_read_b64 v[150:151], v253 offset:1888\n\t"
            "s_waitcnt lgkmcnt(14)\n\t"
            "ds_write_b128 v236, v[176:179]\n\t"
            "v_mov_b64 v[176:177], 0\n\t"
            "v_mov_b64 v[178:179], 0\n\t"
            "v_cvt_pk_bf16_f32 v184, v184, v185\n\t"
            "v_cvt_pk_bf16_f32 v185, v186, v187\n\t"
            "v_cvt_pk_bf16_f32 v186, v188, v189\n\t"
            "v_cvt_pk_bf16_f32 v187, v190, v191\n\t"
            "v_mfma_f32_4x4x4_16b_bf16 v[196:199], v[184:185], v[184:185], v[196:199]\n\t"
            "s_waitcnt lgkmcnt(14)\n\t"
            "ds_write_b128 v237, v[184:187]\n\t"
            "v_mfma_f32_16x16x32_bf16 v[192:195], v[238:241], v[184:187], v[192:195]\n\t"
            "v_mov_b64 v[184:185], 0\n\t"
            "v_mov_b64 v[188:189], 0\n\t"
            "v_mfma_f32_4x4x4_16b_bf16 v[196:199], v[186:187], v[186:187], v[196:199]\n\t"
            "v_mov_b64 v[186:187], 0\n\t"
            "v_mov_b64 v[190:191], 0\n\t"
            "v_writelane_b32 v248, s58, 63\n\t"
            "v_writelane_b32 v248, s38, m0\n\t"
            "s_waitcnt lgkmcnt(12)\n\t"
            "v_and_b32 v249, s69, v249\n\t"
            "v_max_f32 v220, 0, v220\n\t"
            "v_add_f32 v220, 0x3727c5ac, v220\n\t"
            "v_rsq_f32 v220, v220\n\t"
            "s_waitcnt lgkmcnt(11)\n\t"
            "v_smfmac_f32_16x16x64_bf16 v[176:179], v[168:171], v[152:159], v255\n\t"
            "ds_read_b128 v[168:171], v254 offset:384\n\t"
            "ds_read_b64 v[152:153], v253 offset:56\n\t"
            "ds_read_b64 v[154:155], v253 offset:152\n\t"
            "ds_read_b64 v[156:157], v253 offset:1800\n\t"
            "s_waitcnt lgkmcnt(14)\n\t"
            "ds_read_b64 v[158:159], v253 offset:1896\n\t"
            "s_waitcnt lgkmcnt(11)\n\t"
            "v_smfmac_f32_16x16x64_bf16 v[180:183], v[172:175], v[160:167], v255\n\t"
            "ds_read_b128 v[172:175], v254 offset:448\n\t"
            "ds_read_b64 v[160:161], v253 offset:64\n\t"
            "ds_read_b64 v[162:163], v253 offset:160\n\t"
            "ds_read_b64 v[164:165], v253 offset:1808\n\t"
            "s_waitcnt lgkmcnt(14)\n\t"
            "ds_read_b64 v[166:167], v253 offset:1904\n\t"
            "v_cvt_pk_bf16_f32 v176, v176, v177\n\t"
            "v_cvt_pk_bf16_f32 v177, v178, v179\n\t"
            "v_cvt_pk_bf16_f32 v178, v180, v181\n\t"
            "v_cvt_pk_bf16_f32 v179, v182, v183\n\t"
            "v_mfma_f32_4x4x4_16b_bf16 v[196:199], v[176:177], v[176:177], v[196:199]\n\t"
            "s_waitcnt lgkmcnt(14)\n\t"
            "ds_write_b128 v236, v[176:179] offset:128\n\t"
            "v_mfma_f32_16x16x32_bf16 v[192:195], v[238:241], v[176:179], v[192:195]\n\t"
            "v_mov_b64 v[176:177], 0\n\t"
            "v_mov_b64 v[180:181], 0\n\t"
            "v_mfma_f32_4x4x4_16b_bf16 v[196:199], v[178:179], v[178:179], v[196:199]\n\t"
            "v_mov_b64 v[178:179], 0\n\t"
            "v_mov_b64 v[182:183], 0\n\t"
            "s_waitcnt lgkmcnt(14)\n\t"
            "ds_write_b32 v228, v220\n\t"
            "s_waitcnt lgkmcnt(14)\n\t"
            "ds_read_b128 v[220:223], v235 offset:256\n\t"
            "s_waitcnt lgkmcnt(14)\n\t"
            "ds_read_b32 v230, v233\n\t"
            "s_waitcnt lgkmcnt(14)\n\t"
            "ds_read_b32 v231, v233 offset:64\n\t"
            "v_xor_b32 v228, 32, v234\n\t"
            "s_waitcnt lgkmcnt(14)\n\t"
            "v_smfmac_f32_16x16x64_bf16 v[184:187], v[168:171], v[144:151], v255\n\t"
            "ds_read_b128 v[168:171], v254 offset:512\n\t"
            "s_waitcnt lgkmcnt(14)\n\t"
            "ds_read_b64 v[144:145], v253 offset:72\n\t"
            "s_waitcnt lgkmcnt(14)\n\t"
            "ds_read_b64 v[146:147], v253 offset:168\n\t"
            "s_waitcnt lgkmcnt(14)\n\t"
            "ds_read_b64 v[148:149], v253 offset:1816\n\t"
            "s_waitcnt lgkmcnt(14)\n\t"
            "ds_read_b64 v[150:151], v253 offset:1912\n\t"
            "s_waitcnt lgkmcnt(14)\n\t"
            "v_smfmac_f32_16x16x64_bf16 v[188:191], v[172:175], v[152:159], v255\n\t"
            "ds_read_b128 v[172:175], v254 offset:576\n\t"
            "s_waitcnt lgkmcnt(14)\n\t"
            "ds_read_b64 v[152:153], v253 offset:80\n\t"
            "s_waitcnt lgkmcnt(14)\n\t"
            "ds_read_b64 v[154:155], v253 offset:176\n\t"
            "s_waitcnt lgkmcnt(14)\n\t"
            "ds_read_b64 v[156:157], v253 offset:1824\n\t"
            "s_waitcnt lgkmcnt(14)\n\t"
            "ds_read_b64 v[158:159], v253 offset:1920\n\t"
            "v_cvt_pk_bf16_f32 v184, v184, v185\n\t"
            "v_cvt_pk_bf16_f32 v185, v186, v187\n\t"
            "v_cvt_pk_bf16_f32 v186, v188, v189\n\t"
            "v_cvt_pk_bf16_f32 v187, v190, v191\n\t"
            "v_mfma_f32_4x4x4_16b_bf16 v[196:199], v[184:185], v[184:185], v[196:199]\n\t"
            "s_waitcnt lgkmcnt(14)\n\t"
            "ds_write_b128 v237, v[184:187] offset:128\n\t"
            "v_mfma_f32_16x16x32_bf16 v[192:195], v[238:241], v[184:187], v[192:195]\n\t"
            "v_mov_b64 v[184:185], 0\n\t"
            "v_mov_b64 v[188:189], 0\n\t"
            "v_mfma_f32_4x4x4_16b_bf16 v[196:199], v[186:187], v[186:187], v[196:199]\n\t"
            "v_mov_b64 v[186:187], 0\n\t"
            "v_mov_b64 v[190:191], 0\n\t"
            "s_waitcnt lgkmcnt(10)\n\t"
            "v_smfmac_f32_16x16x64_bf16 v[176:179], v[168:171], v[160:167], v255\n\t"
            "ds_read_b128 v[168:171], v254 offset:640\n\t"
            "ds_read_b64 v[160:161], v253 offset:88\n\t"
            "ds_read_b64 v[162:163], v253 offset:184\n\t"
            "ds_read_b64 v[164:165], v253 offset:1832\n\t"
            "ds_read_b64 v[166:167], v253 offset:1928\n\t"
            "s_waitcnt lgkmcnt(10)\n\t"
            "v_smfmac_f32_16x16x64_bf16 v[180:183], v[172:175], v[144:151], v255\n\t"
            "ds_read_b128 v[172:175], v254 offset:704\n\t"
            "v_cvt_pk_bf16_f32 v176, v176, v177\n\t"
            "v_cvt_pk_bf16_f32 v177, v178, v179\n\t"
            "s_nop 1\n\t"
            "v_mfma_f32_4x4x4_16b_bf16 v[196:199], v[176:177], v[176:177], v[196:199]\n\t"
            "s_nop 1\n\t"
            "v_cvt_pk_bf16_f32 v178, v180, v181\n\t"
            "v_cvt_pk_bf16_f32 v179, v182, v183\n\t"
            "s_nop 1\n\t"
            "v_mfma_f32_4x4x4_16b_bf16 v[196:199], v[178:179], v[178:179], v[196:199]\n\t"
            "ds_write_b128 v236, v[176:179] offset:256\n\t"
            "v_mfma_f32_16x16x32_bf16 v[192:195], v[238:241], v[176:179], v[192:195]\n\t"
            "v_add_u32 v236, s48, v244\n\t"
            "s_waitcnt lgkmcnt(6)\n\t"
            "v_smfmac_f32_16x16x64_bf16 v[184:187], v[168:171], v[152:159], v255\n\t"
            "s_waitcnt lgkmcnt(1)\n\t"
            "v_smfmac_f32_16x16x64_bf16 v[188:191], v[172:175], v[160:167], v255\n\t"
            "s_mov_b32 exec_hi, 0x3fffff\n\t"
            "s_add_u32 m0, s66, 0\n\t"
            "s_add_u32 s49, s79, 0\n\t"
            "buffer_load_dwordx4 v249, s[88:91], s49 offen lds\n\t"
            "s_mov_b32 exec_hi, -1\n\t"
            "s_mov_b32 exec_hi, 0x3fffff\n\t"
            "s_add_u32 m0, s66, 872\n\t"
            "s_add_u32 s49, s68, 196224\n\t"
            "buffer_load_dwordx4 v249, s[72:75], s49 offen lds\n\t"
            "s_mov_b32 exec_hi, -1\n\t"
            "s_mov_b32 exec_hi, 0x3fffff\n\t"
            "s_add_u32 m0, s66, 1744\n\t"
            "s_add_u32 s49, s68, 392448\n\t"
            "buffer_load_dwordx4 v249, s[72:75], s49 offen lds\n\t"
            "s_mov_b32 exec_hi, -1\n\t"
            "s_mov_b32 exec_hi, 0x3fffff\n\t"
            "s_add_u32 m0, s66, 2616\n\t"
            "s_add_u32 s49, s68, 588672\n\t"
            "buffer_load_dwordx4 v249, s[72:75], s49 offen lds\n\t"
            "s_mov_b32 exec_hi, -1\n\t"
            "s_mov_b32 exec_hi, 0x3fffff\n\t"
            "s_add_u32 m0, s66, 3488\n\t"
            "s_add_u32 s49, s68, 784896\n\t"
            "buffer_load_dwordx4 v249, s[72:75], s49 offen lds\n\t"
            "s_mov_b32 exec_hi, -1\n\t"
            "s_mov_b32 exec_hi, 0x3fffff\n\t"
            "s_add_u32 m0, s66, 4360\n\t"
            "s_add_u32 s49, s68, 981120\n\t"
            "buffer_load_dwordx4 v249, s[72:75], s49 offen lds\n\t"
            "s_mov_b32 exec_hi, -1\n\t"
            "s_mov_b32 exec_hi, 0x3fffff\n\t"
            "s_add_u32 m0, s66, 5232\n\t"
            "s_add_u32 s49, s68, 1177344\n\t"
            "buffer_load_dwordx4 v249, s[72:75], s49 offen lds\n\t"
            "s_mov_b32 exec_hi, -1\n\t"
            "v_cvt_pk_bf16_f32 v184, v184, v185\n\t"
            "v_cvt_pk_bf16_f32 v185, v186, v187\n\t"
            "v_cvt_pk_bf16_f32 v186, v188, v189\n\t"
            "v_cvt_pk_bf16_f32 v187, v190, v191\n\t"
            "v_mfma_f32_4x4x4_16b_bf16 v[196:199], v[184:185], v[184:185], v[196:199]\n\t"
            "s_mov_b32 exec_hi, 0x3fffff\n\t"
            "s_add_u32 m0, s66, 6104\n\t"
            "s_add_u32 s49, s68, 1373568\n\t"
            "buffer_load_dwordx4 v249, s[72:75], s49 offen lds\n\t"
            "s_mov_b32 exec_hi, -1\n\t"
            "v_mfma_f32_4x4x4_16b_bf16 v[196:199], v[186:187], v[186:187], v[196:199]\n\t"
            "s_mov_b32 exec_hi, 0x3fffff\n\t"
            "s_add_u32 m0, s66, 6976\n\t"
            "s_add_u32 s49, s68, 1569792\n\t"
            "buffer_load_dwordx4 v249, s[72:75], s49 offen lds\n\t"
            "s_mov_b32 exec_hi, -1\n\t"
            "v_mfma_f32_16x16x32_bf16 v[192:195], v[238:241], v[184:187], v[192:195]\n\t"
            "s_waitcnt vmcnt(9)\n\t"
            "ds_write_b128 v237, v[184:187] offset:256\n\t"
            "v_mfma_f32_16x16x32_bf16 v[144:147], v[0:3], v[200:203], 0\n\t"
            "v_add_u32 v237, s48, v245\n\t"
            "v_mfma_f32_16x16x32_bf16 v[144:147], v[4:7], v[204:207], v[144:147]\n\t"
            "v_mfma_f32_16x16x32_bf16 v[148:151], v[24:27], v[200:203], 0\n\t"
            "s_nop 1\n\t"
            "v_cndmask_b32_e64 v193, v196, v197, s[60:61]\n\t"
            "v_mfma_f32_16x16x32_bf16 v[148:151], v[28:31], v[204:207], v[148:151]\n\t"
            "v_cndmask_b32_e64 v193, v193, v198, s[62:63]\n\t"
            "v_cndmask_b32_e64 v193, v193, v199, s[64:65]\n\t"
            "v_mfma_f32_16x16x32_bf16 v[168:171], v[48:51], v[200:203], 0\n\t"
            "ds_read_b128 v[196:199], v232 offset:6272\n\t"
            "v_mfma_f32_16x16x32_bf16 v[168:171], v[52:55], v[204:207], v[168:171]\n\t"
            "ds_write_b64 v248, v[192:193] offset:18816\n\t"
            "v_mfma_f32_16x16x32_bf16 v[172:175], v[72:75], v[200:203], 0\n\t"
            "ds_read_b128 v[192:195], v235 offset:6144\n\t"
            "v_add_u32 v248, s48, v246\n\t"
            "v_mfma_f32_16x16x32_bf16 v[172:175], v[76:79], v[204:207], v[172:175]\n\t"
            "s_mov_b64 exec, 1\n\t"
            "ds_inc_u32 v255, v238 offset:7860\n\t"
            "s_mov_b64 exec, -1\n\t"
            "v_mfma_f32_16x16x32_bf16 v[152:155], v[96:99], v[200:203], 0\n\t"
            "v_mfma_f32_16x16x32_bf16 v[152:155], v[100:103], v[204:207], v[152:155]\n\t"
            "v_mfma_f32_16x16x32_bf16 v[156:159], v[120:123], v[200:203], 0\n\t"
            "ds_read_b128 v[200:203], v235 offset:6272\n\t"
            "v_mfma_f32_16x16x32_bf16 v[156:159], v[124:127], v[204:207], v[156:159]\n\t"
            "ds_read_b128 v[204:207], v232 offset:6400\n\t"
            "v_mfma_f32_16x16x32_bf16 v[144:147], v[8:11], v[208:211], v[144:147]\n\t"
            "v_mfma_f32_16x16x32_bf16 v[144:147], v[12:15], v[212:215], v[144:147]\n\t"
            "v_mfma_f32_16x16x32_bf16 v[148:151], v[32:35], v[208:211], v[148:151]\n\t"
            "v_mfma_f32_16x16x32_bf16 v[148:151], v[36:39], v[212:215], v[148:151]\n\t"
            "v_mfma_f32_16x16x32_bf16 v[168:171], v[56:59], v[208:211], v[168:171]\n\t"
            "v_mfma_f32_16x16x32_bf16 v[168:171], v[60:63], v[212:215], v[168:171]\n\t"
            "v_mfma_f32_16x16x32_bf16 v[172:175], v[80:83], v[208:211], v[172:175]\n\t"
            "v_mfma_f32_16x16x32_bf16 v[172:175], v[84:87], v[212:215], v[172:175]\n\t"
            "v_mfma_f32_16x16x32_bf16 v[152:155], v[104:107], v[208:211], v[152:155]\n\t"
            "v_mfma_f32_16x16x32_bf16 v[152:155], v[108:111], v[212:215], v[152:155]\n\t"
            "v_mfma_f32_16x16x32_bf16 v[156:159], v[128:131], v[208:211], v[156:159]\n\t"
            "ds_read_b128 v[208:211], v235 offset:6400\n\t"
            "v_mfma_f32_16x16x32_bf16 v[156:159], v[132:135], v[212:215], v[156:159]\n\t"
            "ds_read_b128 v[212:215], v236 offset:6144\n\t"
            "v_mfma_f32_16x16x32_bf16 v[144:147], v[16:19], v[216:219], v[144:147]\n\t"
            "v_mfma_f32_16x16x32_bf16 v[144:147], v[20:23], v[220:223], v[144:147]\n\t"
            "v_mfma_f32_16x16x32_bf16 v[148:151], v[40:43], v[216:219], v[148:151]\n\t"
            "v_mfma_f32_16x16x32_bf16 v[148:151], v[44:47], v[220:223], v[148:151]\n\t"
            "v_mfma_f32_16x16x32_bf16 v[168:171], v[64:67], v[216:219], v[168:171]\n\t"
            "v_mfma_f32_16x16x32_bf16 v[168:171], v[68:71], v[220:223], v[168:171]\n\t"
            "v_mfma_f32_16x16x32_bf16 v[172:175], v[88:91], v[216:219], v[172:175]\n\t"
            "v_mfma_f32_16x16x32_bf16 v[172:175], v[92:95], v[220:223], v[172:175]\n\t"
            "s_nop 1\n\t"
            "v_mul_f32 v144, v230, v144\n\t"
            "v_mfma_f32_16x16x32_bf16 v[152:155], v[112:115], v[216:219], v[152:155]\n\t"
            "v_mul_f32 v145, v230, v145\n\t"
            "v_mul_f32 v146, v230, v146\n\t"
            "v_mfma_f32_16x16x32_bf16 v[152:155], v[116:119], v[220:223], v[152:155]\n\t"
            "v_mul_f32 v148, v230, v148\n\t"
            "v_mul_f32 v149, v230, v149\n\t"
            "v_mfma_f32_16x16x32_bf16 v[156:159], v[136:139], v[216:219], v[156:159]\n\t"
            "ds_read_b128 v[216:219], v237 offset:6144\n\t"
            "v_mul_f32 v168, v230, v168\n\t"
            "v_mfma_f32_16x16x32_bf16 v[156:159], v[140:143], v[220:223], v[156:159]\n\t"
            "ds_read_b128 v[220:223], v248 offset:6144\n\t"
            "v_mul_f32 v169, v230, v169\n\t"
            "s_waitcnt lgkmcnt(7)\n\t"
            "v_mfma_f32_16x16x32_bf16 v[160:163], v[0:3], v[224:227], 0\n\t"
            "v_mul_f32 v172, v230, v172\n\t"
            "v_mul_f32 v173, v230, v173\n\t"
            "v_mfma_f32_16x16x32_bf16 v[160:163], v[4:7], v[192:195], v[160:163]\n\t"
            "v_mul_f32 v152, v230, v152\n\t"
            "v_mul_f32 v153, v230, v153\n\t"
            "v_mfma_f32_16x16x32_bf16 v[164:167], v[24:27], v[224:227], 0\n\t"
            "v_mul_f32 v156, v230, v156\n\t"
            "v_mul_f32 v157, v230, v157\n\t"
            "v_mfma_f32_16x16x32_bf16 v[164:167], v[28:31], v[192:195], v[164:167]\n\t"
            "v_mul_f32 v147, v230, v147\n\t"
            "v_cvt_pk_bf16_f32 v144, v144, v145\n\t"
            "v_mfma_f32_16x16x32_bf16 v[176:179], v[48:51], v[224:227], 0\n\t"
            "v_mul_f32 v150, v230, v150\n\t"
            "v_mul_f32 v151, v230, v151\n\t"
            "v_mfma_f32_16x16x32_bf16 v[176:179], v[52:55], v[192:195], v[176:179]\n\t"
            "v_cvt_pk_bf16_f32 v148, v148, v149\n\t"
            "v_mul_f32 v170, v230, v170\n\t"
            "v_mfma_f32_16x16x32_bf16 v[180:183], v[72:75], v[224:227], 0\n\t"
            "v_mul_f32 v171, v230, v171\n\t"
            "v_cvt_pk_bf16_f32 v168, v168, v169\n\t"
            "v_mfma_f32_16x16x32_bf16 v[180:183], v[76:79], v[192:195], v[180:183]\n\t"
            "v_mul_f32 v174, v230, v174\n\t"
            "v_mul_f32 v175, v230, v175\n\t"
            "v_mfma_f32_16x16x32_bf16 v[184:187], v[96:99], v[224:227], 0\n\t"
            "v_cvt_pk_bf16_f32 v172, v172, v173\n\t"
            "v_mul_f32 v154, v230, v154\n\t"
            "v_mfma_f32_16x16x32_bf16 v[184:187], v[100:103], v[192:195], v[184:187]\n\t"
            "v_mul_f32 v155, v230, v155\n\t"
            "v_cvt_pk_bf16_f32 v152, v152, v153\n\t"
            "v_mfma_f32_16x16x32_bf16 v[188:191], v[120:123], v[224:227], 0\n\t"
            "ds_read_b128 v[224:227], v236 offset:12288\n\t"
            "v_mul_f32 v158, v230, v158\n\t"
            "v_mfma_f32_16x16x32_bf16 v[188:191], v[124:127], v[192:195], v[188:191]\n\t"
            "v_mul_f32 v159, v230, v159\n\t"
            "v_cvt_pk_bf16_f32 v156, v156, v157\n\t"
            "s_waitcnt lgkmcnt(6)\n\t"
            "v_mfma_f32_16x16x32_bf16 v[160:163], v[8:11], v[196:199], v[160:163]\n\t"
            "v_cvt_pk_bf16_f32 v145, v146, v147\n\t"
            "v_cvt_pk_bf16_f32 v149, v150, v151\n\t"
            "v_mfma_f32_16x16x32_bf16 v[160:163], v[12:15], v[200:203], v[160:163]\n\t"
            "v_cvt_pk_bf16_f32 v169, v170, v171\n\t"
            "v_cvt_pk_bf16_f32 v173, v174, v175\n\t"
            "v_mfma_f32_16x16x32_bf16 v[164:167], v[32:35], v[196:199], v[164:167]\n\t"
            "v_cvt_pk_bf16_f32 v153, v154, v155\n\t"
            "v_cvt_pk_bf16_f32 v157, v158, v159\n\t"
            "v_mfma_f32_16x16x32_bf16 v[164:167], v[36:39], v[200:203], v[164:167]\n\t"
            "s_mov_b64 exec, -1\n\t"
            "ds_write_b64 v234, v[144:145]\n\t"
            "ds_write_b64 v234, v[148:149] offset:128\n\t"
            "ds_write_b64 v234, v[168:169] offset:256\n\t"
            "ds_write_b64 v228, v[172:173]\n\t"
            "ds_write_b64 v228, v[152:153] offset:128\n\t"
            "ds_write_b64 v228, v[156:157] offset:256\n\t"
            "s_mov_b64 exec, -1\n\t"
            "v_mfma_f32_16x16x32_bf16 v[176:179], v[56:59], v[196:199], v[176:179]\n\t"
            "v_mfma_f32_16x16x32_bf16 v[176:179], v[60:63], v[200:203], v[176:179]\n\t"
            "s_add_u32 s49, s33, 0\n\t"
            "buffer_load_dwordx4 v[0:3], v247, s[28:31], s49 offen sc1\n\t"
            "v_mfma_f32_16x16x32_bf16 v[180:183], v[80:83], v[196:199], v[180:183]\n\t"
            "v_mfma_f32_16x16x32_bf16 v[180:183], v[84:87], v[200:203], v[180:183]\n\t"
            "s_add_u32 s49, s33, 6144\n\t"
            "buffer_load_dwordx4 v[24:27], v247, s[28:31], s49 offen sc1\n\t"
            "v_mfma_f32_16x16x32_bf16 v[184:187], v[104:107], v[196:199], v[184:187]\n\t"
            "v_mfma_f32_16x16x32_bf16 v[184:187], v[108:111], v[200:203], v[184:187]\n\t"
            "s_add_u32 s49, s33, 12288\n\t"
            "buffer_load_dwordx4 v[48:51], v247, s[28:31], s49 offen sc1\n\t"
            "v_mfma_f32_16x16x32_bf16 v[188:191], v[128:131], v[196:199], v[188:191]\n\t"
            "v_mfma_f32_16x16x32_bf16 v[188:191], v[132:135], v[200:203], v[188:191]\n\t"
            "ds_read_b128 v[200:203], v236\n\t"
            "s_add_u32 s49, s33, 18432\n\t"
            "buffer_load_dwordx4 v[72:75], v247, s[28:31], s49 offen sc1\n\t"
            "s_waitcnt lgkmcnt(11)\n\t"
            "v_mfma_f32_16x16x32_bf16 v[160:163], v[16:19], v[204:207], v[160:163]\n\t"
            "v_mfma_f32_16x16x32_bf16 v[160:163], v[20:23], v[208:211], v[160:163]\n\t"
            "s_add_u32 s49, s33, 24576\n\t"
            "buffer_load_dwordx4 v[96:99], v247, s[28:31], s49 offen sc1\n\t"
            "v_mfma_f32_16x16x32_bf16 v[164:167], v[40:43], v[204:207], v[164:167]\n\t"
            "v_mfma_f32_16x16x32_bf16 v[164:167], v[44:47], v[208:211], v[164:167]\n\t"
            "s_add_u32 s49, s33, 30720\n\t"
            "buffer_load_dwordx4 v[120:123], v247, s[28:31], s49 offen sc1\n\t"
            "v_mfma_f32_16x16x32_bf16 v[176:179], v[64:67], v[204:207], v[176:179]\n\t"
            "s_nop 0\n\t"
            "v_mul_f32 v160, v231, v160\n\t"
            "v_mul_f32 v161, v231, v161\n\t"
            "v_mfma_f32_16x16x32_bf16 v[176:179], v[68:71], v[208:211], v[176:179]\n\t"
            "v_mul_f32 v162, v231, v162\n\t"
            "v_mul_f32 v163, v231, v163\n\t"
            "v_mfma_f32_16x16x32_bf16 v[180:183], v[88:91], v[204:207], v[180:183]\n\t"
            "v_mul_f32 v164, v231, v164\n\t"
            "v_mul_f32 v165, v231, v165\n\t"
            "v_mfma_f32_16x16x32_bf16 v[180:183], v[92:95], v[208:211], v[180:183]\n\t"
            "v_cvt_pk_bf16_f32 v160, v160, v161\n\t"
            "v_mul_f32 v166, v231, v166\n\t"
            "v_mfma_f32_16x16x32_bf16 v[184:187], v[112:115], v[204:207], v[184:187]\n\t"
            "v_mul_f32 v176, v231, v176\n\t"
            "v_mul_f32 v177, v231, v177\n\t"
            "v_mfma_f32_16x16x32_bf16 v[184:187], v[116:119], v[208:211], v[184:187]\n\t"
            "v_mul_f32 v167, v231, v167\n\t"
            "v_cvt_pk_bf16_f32 v164, v164, v165\n\t"
            "v_mfma_f32_16x16x32_bf16 v[188:191], v[136:139], v[204:207], v[188:191]\n\t"
            "ds_read_b128 v[204:207], v237\n\t"
            "v_mul_f32 v180, v231, v180\n\t"
            "v_mfma_f32_16x16x32_bf16 v[188:191], v[140:143], v[208:211], v[188:191]\n\t"
            "ds_read_b128 v[208:211], v248\n\t"
            "v_mul_f32 v181, v231, v181\n\t"
            "v_mul_f32 v184, v231, v184\n\t"
            "v_mul_f32 v185, v231, v185\n\t"
            "v_mul_f32 v178, v231, v178\n\t"
            "v_mul_f32 v179, v231, v179\n\t"
            "v_cvt_pk_bf16_f32 v176, v176, v177\n\t"
            "v_mul_f32 v182, v231, v182\n\t"
            "v_mul_f32 v188, v231, v188\n\t"
            "v_mul_f32 v189, v231, v189\n\t"
            "v_mul_f32 v183, v231, v183\n\t"
            "v_cvt_pk_bf16_f32 v180, v180, v181\n\t"
            "v_mul_f32 v186, v231, v186\n\t"
            "v_mul_f32 v187, v231, v187\n\t"
            "v_cvt_pk_bf16_f32 v184, v184, v185\n\t"
            "v_mul_f32 v190, v231, v190\n\t"
            "v_mul_f32 v191, v231, v191\n\t"
            "v_cvt_pk_bf16_f32 v188, v188, v189\n\t"
            "v_cvt_pk_bf16_f32 v161, v162, v163\n\t"
            "v_cvt_pk_bf16_f32 v165, v166, v167\n\t"
            "v_cvt_pk_bf16_f32 v177, v178, v179\n\t"
            "v_cvt_pk_bf16_f32 v181, v182, v183\n\t"
            "v_cvt_pk_bf16_f32 v185, v186, v187\n\t"
            "v_cvt_pk_bf16_f32 v189, v190, v191\n\t"
            "s_waitcnt lgkmcnt(9)\n\t"
            "s_mov_b64 exec, s[80:81]\n\t"
            "ds_write_b64 v234, v[160:161] offset:6144\n\t"
            "ds_write_b64 v234, v[164:165] offset:6272\n\t"
            "ds_write_b64 v234, v[176:177] offset:6400\n\t"
            "ds_write_b64 v228, v[180:181] offset:6144\n\t"
            "ds_write_b64 v228, v[184:185] offset:6272\n\t"
            "ds_write_b64 v228, v[188:189] offset:6400\n\t"
            "s_mov_b64 exec, -1\n\t"
            "s_waitcnt lgkmcnt(14)\n\t"
            "ds_read_b128 v[228:231], v237 offset:12288\n\t"
            "s_waitcnt lgkmcnt(14)\n\t"
            "ds_read_b128 v[232:235], v248 offset:12288\n\t"
            "s_add_u32 s49, s33, 1024\n\t"
            "buffer_load_dwordx4 v[4:7], v247, s[28:31], s49 offen sc1\n\t"
            "s_add_u32 s49, s33, 7168\n\t"
            "buffer_load_dwordx4 v[28:31], v247, s[28:31], s49 offen sc1\n\t"
            "s_waitcnt lgkmcnt(0)\n\t"
            "s_barrier\n\t"
            "s_setprio 3\n\t"
            "s_add_u32 s49, s33, 13312\n\t"
            "buffer_load_dwordx4 v[52:55], v247, s[28:31], s49 offen sc1\n\t"
            "s_add_u32 s49, s33, 19456\n\t"
            "buffer_load_dwordx4 v[76:79], v247, s[28:31], s49 offen sc1\n\t"
            "s_add_u32 s49, s33, 25600\n\t"
            "buffer_load_dwordx4 v[100:103], v247, s[28:31], s49 offen sc1\n\t"
            "s_add_u32 s49, s33, 31744\n\t"
            "buffer_load_dwordx4 v[124:127], v247, s[28:31], s49 offen sc1\n\t"
            "s_add_u32 s49, s33, 2048\n\t"
            "buffer_load_dwordx4 v[8:11], v247, s[28:31], s49 offen sc1\n\t"
            "s_add_u32 s49, s33, 8192\n\t"
            "buffer_load_dwordx4 v[32:35], v247, s[28:31], s49 offen sc1\n\t"
            "s_add_u32 s49, s33, 14336\n\t"
            "buffer_load_dwordx4 v[56:59], v247, s[28:31], s49 offen sc1\n\t"
            "s_add_u32 s49, s33, 20480\n\t"
            "buffer_load_dwordx4 v[80:83], v247, s[28:31], s49 offen sc1\n\t"
            "s_add_u32 s49, s33, 26624\n\t"
            "buffer_load_dwordx4 v[104:107], v247, s[28:31], s49 offen sc1\n\t"
            "s_add_u32 s49, s33, 32768\n\t"
            "buffer_load_dwordx4 v[128:131], v247, s[28:31], s49 offen sc1\n\t"
            "s_add_u32 s49, s33, 3072\n\t"
            "buffer_load_dwordx4 v[12:15], v247, s[28:31], s49 offen sc1\n\t"
            "s_add_u32 s49, s33, 9216\n\t"
            "buffer_load_dwordx4 v[36:39], v247, s[28:31], s49 offen sc1\n\t"
            "s_add_u32 s49, s33, 15360\n\t"
            "buffer_load_dwordx4 v[60:63], v247, s[28:31], s49 offen sc1\n\t"
            "s_add_u32 s49, s33, 21504\n\t"
            "buffer_load_dwordx4 v[84:87], v247, s[28:31], s49 offen sc1\n\t"
            "s_add_u32 s49, s33, 27648\n\t"
            "buffer_load_dwordx4 v[108:111], v247, s[28:31], s49 offen sc1\n\t"
            "s_add_u32 s49, s33, 33792\n\t"
            "buffer_load_dwordx4 v[132:135], v247, s[28:31], s49 offen sc1\n\t"
            "s_add_u32 s49, s33, 4096\n\t"
            "buffer_load_dwordx4 v[16:19], v247, s[28:31], s49 offen sc1\n\t"
            "s_add_u32 s49, s33, 10240\n\t"
            "buffer_load_dwordx4 v[40:43], v247, s[28:31], s49 offen sc1\n\t"
            "s_add_u32 s49, s33, 16384\n\t"
            "buffer_load_dwordx4 v[64:67], v247, s[28:31], s49 offen sc1\n\t"
            "s_add_u32 s49, s33, 22528\n\t"
            "buffer_load_dwordx4 v[88:91], v247, s[28:31], s49 offen sc1\n\t"
            "s_add_u32 s49, s33, 28672\n\t"
            "buffer_load_dwordx4 v[112:115], v247, s[28:31], s49 offen sc1\n\t"
            "s_add_u32 s49, s33, 34816\n\t"
            "buffer_load_dwordx4 v[136:139], v247, s[28:31], s49 offen sc1\n\t"
            "s_add_u32 s49, s33, 5120\n\t"
            "buffer_load_dwordx4 v[20:23], v247, s[28:31], s49 offen sc1\n\t"
            "s_add_u32 s49, s33, 11264\n\t"
            "buffer_load_dwordx4 v[44:47], v247, s[28:31], s49 offen sc1\n\t"
            "s_add_u32 s49, s33, 17408\n\t"
            "buffer_load_dwordx4 v[68:71], v247, s[28:31], s49 offen sc1\n\t"
            "s_add_u32 s49, s33, 23552\n\t"
            "buffer_load_dwordx4 v[92:95], v247, s[28:31], s49 offen sc1\n\t"
            "s_add_u32 s49, s33, 29696\n\t"
            "buffer_load_dwordx4 v[116:119], v247, s[28:31], s49 offen sc1\n\t"
            "s_add_u32 s49, s33, 35840\n\t"
            "buffer_load_dwordx4 v[140:143], v247, s[28:31], s49 offen sc1\n\t"
            "s_add_u32 s59, s59, s84\n\t"
            "s_movk_i32 s84, 800\n\t"
            "s_add_u32 s45, s46, s27\n\t"
            "s_mov_b32 s46, s47\n\t"
            "s_mov_b32 s47, s68\n\t"
            "s_xor_b32 s48, s48, s71\n\t"
            "s_xor_b32 s67, s67, s71\n\t"
            "s_add_u32 s76, s76, 4\n\t"
            "s_add_u32 s70, s70, 4\n\t"
            "s_lshl_b32 s56, s56, 2\n\t"
            "s_lshr_b32 s49, s26, 30\n\t"
            "s_or_b32 s56, s56, s49\n\t"
            "s_waitcnt vmcnt(0)\n\t"
            "s_nop 7\n\t"
            ".p2align 6\n\t"
            "s_nop 0\n\t"
            "s_nop 0\n\t"
            "s_nop 0\n\t"
            "s_nop 0\n\t"
            "s_nop 0\n\t"
            "s_nop 0\n\t"
            "s_nop 0\n\t"
            ".Lwb_loop%=:\n\t"
            "s_setprio 1\n\t"
            "ds_read_b128 v[168:171], v254\n\t"
            "ds_read_b128 v[172:175], v254 offset:64\n\t"
            "s_waitcnt vmcnt(9)\n\t"
            "ds_read_b64 v[144:145], v253\n\t"
            "ds_read_b64 v[146:147], v253 offset:96\n\t"
            "ds_read_b64 v[148:149], v253 offset:1744\n\t"
            "ds_read_b64 v[150:151], v253 offset:1840\n\t"
            "ds_read_b64 v[152:153], v253 offset:8\n\t"
            "ds_read_b64 v[154:155], v253 offset:104\n\t"
            "ds_read_b64 v[156:157], v253 offset:1752\n\t"
            "ds_read_b64 v[158:159], v253 offset:1848\n\t"
            "ds_read_b64 v[160:161], v253 offset:16\n\t"
            "ds_read_b64 v[162:163], v253 offset:112\n\t"
            "ds_read_b64 v[164:165], v253 offset:1760\n\t"
            "ds_read_b64 v[166:167], v253 offset:1856\n\t"
            "v_mov_b64 v[176:177], 0\n\t"
            "v_mov_b64 v[178:179], 0\n\t"
            "v_mov_b64 v[180:181], 0\n\t"
            "v_mov_b64 v[182:183], 0\n\t"
            "v_mov_b32 v249, s76\n\t"
            "v_mov_b64 v[184:185], 0\n\t"
            "v_mov_b64 v[186:187], 0\n\t"
            "v_mov_b64 v[188:189], 0\n\t"
            "v_mov_b64 v[190:191], 0\n\t"
            "v_add_u32 v236, s67, v250\n\t"
            "v_add_u32 v248, s67, v251\n\t"
            "v_mov_b32 v247, 0\n\t"
            "ds_read_b32 v249, v249\n\t"
            "s_setprio 3\n\t"
            "s_waitcnt lgkmcnt(9)\n\t"
            "v_smfmac_f32_16x16x64_bf16 v[176:179], v[168:171], v[144:151], v255\n\t"
            "ds_read_b128 v[168:171], v254 offset:128\n\t"
            "s_waitcnt lgkmcnt(6)\n\t"
            "v_smfmac_f32_16x16x64_bf16 v[180:183], v[172:175], v[152:159], v255\n\t"
            "s_setprio 1\n\t"
            "ds_read_b64 v[144:145], v253 offset:24\n\t"
            "ds_read_b64 v[146:147], v253 offset:120\n\t"
            "ds_read_b64 v[148:149], v253 offset:1768\n\t"
            "ds_read_b64 v[150:151], v253 offset:1864\n\t"
            "ds_read_b128 v[172:175], v254 offset:192\n\t"
            "ds_read_b64 v[152:153], v253 offset:32\n\t"
            "ds_read_b64 v[154:155], v253 offset:128\n\t"
            "ds_read_b64 v[156:157], v253 offset:1776\n\t"
            "ds_read_b64 v[158:159], v253 offset:1872\n\t"
            "v_cvt_pk_bf16_f32 v176, v176, v177\n\t"
            "v_cvt_pk_bf16_f32 v177, v178, v179\n\t"
            "v_cvt_pk_bf16_f32 v178, v180, v181\n\t"
            "v_cvt_pk_bf16_f32 v179, v182, v183\n\t"
            "s_setprio 3\n\t"
            "v_mfma_f32_4x4x4_16b_bf16 v[196:199], v[176:177], v[176:177], 0\n\t"
            "v_mov_b64 v[180:181], 0\n\t"
            "v_mfma_f32_16x16x32_bf16 v[192:195], v[238:241], v[176:179], 0\n\t"
            "s_waitcnt lgkmcnt(10)\n\t"
            "v_readfirstlane_b32 s26, v249\n\t"
            "v_mov_b64 v[182:183], 0\n\t"
            "v_mfma_f32_4x4x4_16b_bf16 v[196:199], v[178:179], v[178:179], v[196:199]\n\t"
            "ds_read_b32 v249, v252 offset:12\n\t"
            "s_and_b32 s68, s26, 0x7ffffff\n\t"
            "s_add_u32 s68, s68, s78\n\t"
            "s_and_b32 s69, s26, 0x38000000\n\t"
            "s_or_b32 s69, s69, s39\n\t"
            "s_bitcmp1_b32 s26, 27\n\t"
            "s_cselect_b32 s79, 2.0, s68\n\t"
            "s_and_b32 s57, s56, 3\n\t"
            "s_lshl_b32 s58, s57, 3\n\t"
            "s_lshr_b32 m0, s54, s58\n\t"
            "s_lshl_b32 s58, s57, 4\n\t"
            "s_lshr_b64 s[36:37], s[50:51], s58\n\t"
            "s_and_b32 s57, s36, 0xffff\n\t"
            "s_add_u32 s57, s57, s67\n\t"
            "v_writelane_b32 v236, s57, 63\n\t"
            "v_writelane_b32 v236, s59, m0\n\t"
            "v_xor_b32 v237, 16, v236\n\t"
            "s_lshr_b64 s[36:37], s[52:53], s58\n\t"
            "s_and_b32 s58, s36, 0xffff\n\t"
            "s_add_u32 s58, s58, s67\n\t"
            "ds_write_b128 v236, v[176:179]\n\t"
            "s_waitcnt lgkmcnt(11)\n\t"
            "v_smfmac_f32_16x16x64_bf16 v[184:187], v[168:171], v[160:167], v255\n\t"
            "s_and_b32 s38, s52, 0xffff\n\t"
            "s_add_u32 s38, s38, s67\n\t"
            "ds_read_b128 v[168:171], v254 offset:256\n\t"
            "s_waitcnt lgkmcnt(7)\n\t"
            "v_smfmac_f32_16x16x64_bf16 v[188:191], v[172:175], v[144:151], v255\n\t"
            "s_setprio 1\n\t"
            "ds_read_b64 v[160:161], v253 offset:40\n\t"
            "ds_read_b64 v[162:163], v253 offset:136\n\t"
            "ds_read_b64 v[164:165], v253 offset:1784\n\t"
            "ds_read_b64 v[166:167], v253 offset:1880\n\t"
            "ds_read_b128 v[172:175], v254 offset:320\n\t"
            "ds_read_b64 v[144:145], v253 offset:48\n\t"
            "ds_read_b64 v[146:147], v253 offset:144\n\t"
            "ds_read_b64 v[148:149], v253 offset:1792\n\t"
            "s_waitcnt lgkmcnt(14)\n\t"
            "ds_read_b64 v[150:151], v253 offset:1888\n\t"
            "v_mov_b64 v[176:177], 0\n\t"
            "v_mov_b64 v[178:179], 0\n\t"
            "v_cvt_pk_bf16_f32 v184, v184, v185\n\t"
            "v_cvt_pk_bf16_f32 v185, v186, v187\n\t"
            "v_cvt_pk_bf16_f32 v186, v188, v189\n\t"
            "v_cvt_pk_bf16_f32 v187, v190, v191\n\t"
            "s_setprio 3\n\t"
            "v_mfma_f32_4x4x4_16b_bf16 v[196:199], v[184:185], v[184:185], v[196:199]\n\t"
            "s_waitcnt lgkmcnt(14)\n\t"
            "ds_write_b128 v237, v[184:187]\n\t"
            "v_mfma_f32_16x16x32_bf16 v[192:195], v[238:241], v[184:187], v[192:195]\n\t"
            "v_mov_b64 v[184:185], 0\n\t"
            "v_mov_b64 v[188:189], 0\n\t"
            "v_mfma_f32_4x4x4_16b_bf16 v[196:199], v[186:187], v[186:187], v[196:199]\n\t"
            "v_mov_b64 v[186:187], 0\n\t"
            "v_mov_b64 v[190:191], 0\n\t"
            "v_writelane_b32 v248, s58, 63\n\t"
            "v_writelane_b32 v248, s38, m0\n\t"
            "s_waitcnt lgkmcnt(12)\n\t"
            "v_and_b32 v249, s69, v249\n\t"
            "s_waitcnt lgkmcnt(10)\n\t"
            "v_smfmac_f32_16x16x64_bf16 v[176:179], v[168:171], v[152:159], v255\n\t"
            "ds_read_b128 v[168:171], v254 offset:384\n\t"
            "ds_read_b64 v[152:153], v253 offset:56\n\t"
            "ds_read_b64 v[154:155], v253 offset:152\n\t"
            "ds_read_b64 v[156:157], v253 offset:1800\n\t"
            "ds_read_b64 v[158:159], v253 offset:1896\n\t"
            "s_waitcnt lgkmcnt(10)\n\t"
            "v_smfmac_f32_16x16x64_bf16 v[180:183], v[172:175], v[160:167], v255\n\t"
            "s_setprio 1\n\t"
            "ds_read_b128 v[172:175], v254 offset:448\n\t"
            "ds_read_b64 v[160:161], v253 offset:64\n\t"
            "ds_read_b64 v[162:163], v253 offset:160\n\t"
            "ds_read_b64 v[164:165], v253 offset:1808\n\t"
            "ds_read_b64 v[166:167], v253 offset:1904\n\t"
            "v_cvt_pk_bf16_f32 v176, v176, v177\n\t"
            "v_cvt_pk_bf16_f32 v177, v178, v179\n\t"
            "v_cvt_pk_bf16_f32 v178, v180, v181\n\t"
            "v_cvt_pk_bf16_f32 v179, v182, v183\n\t"
            "s_setprio 3\n\t"
            "v_mfma_f32_4x4x4_16b_bf16 v[196:199], v[176:177], v[176:177], v[196:199]\n\t"
            "s_waitcnt lgkmcnt(14)\n\t"
            "ds_write_b128 v236, v[176:179] offset:128\n\t"
            "v_mfma_f32_16x16x32_bf16 v[192:195], v[238:241], v[176:179], v[192:195]\n\t"
            "v_mov_b64 v[176:177], 0\n\t"
            "v_mov_b64 v[180:181], 0\n\t"
            "v_mfma_f32_4x4x4_16b_bf16 v[196:199], v[178:179], v[178:179], v[196:199]\n\t"
            "v_mov_b64 v[178:179], 0\n\t"
            "v_mov_b64 v[182:183], 0\n\t"
            "s_waitcnt lgkmcnt(10)\n\t"
            "v_smfmac_f32_16x16x64_bf16 v[184:187], v[168:171], v[144:151], v255\n\t"
            "ds_read_b128 v[168:171], v254 offset:512\n\t"
            "ds_read_b64 v[144:145], v253 offset:72\n\t"
            "ds_read_b64 v[146:147], v253 offset:168\n\t"
            "ds_read_b64 v[148:149], v253 offset:1816\n\t"
            "ds_read_b64 v[150:151], v253 offset:1912\n\t"
            "s_waitcnt lgkmcnt(10)\n\t"
            "v_smfmac_f32_16x16x64_bf16 v[188:191], v[172:175], v[152:159], v255\n\t"
            "s_setprio 1\n\t"
            "ds_read_b128 v[172:175], v254 offset:576\n\t"
            "ds_read_b64 v[152:153], v253 offset:80\n\t"
            "ds_read_b64 v[154:155], v253 offset:176\n\t"
            "ds_read_b64 v[156:157], v253 offset:1824\n\t"
            "ds_read_b64 v[158:159], v253 offset:1920\n\t"
            "v_cvt_pk_bf16_f32 v184, v184, v185\n\t"
            "v_cvt_pk_bf16_f32 v185, v186, v187\n\t"
            "v_cvt_pk_bf16_f32 v186, v188, v189\n\t"
            "v_cvt_pk_bf16_f32 v187, v190, v191\n\t"
            "s_setprio 3\n\t"
            "v_mfma_f32_4x4x4_16b_bf16 v[196:199], v[184:185], v[184:185], v[196:199]\n\t"
            "s_waitcnt lgkmcnt(14)\n\t"
            "ds_write_b128 v237, v[184:187] offset:128\n\t"
            "v_mfma_f32_16x16x32_bf16 v[192:195], v[238:241], v[184:187], v[192:195]\n\t"
            "v_mov_b64 v[184:185], 0\n\t"
            "v_mov_b64 v[188:189], 0\n\t"
            "v_mfma_f32_4x4x4_16b_bf16 v[196:199], v[186:187], v[186:187], v[196:199]\n\t"
            "v_mov_b64 v[186:187], 0\n\t"
            "v_mov_b64 v[190:191], 0\n\t"
            "s_waitcnt lgkmcnt(10)\n\t"
            "v_smfmac_f32_16x16x64_bf16 v[176:179], v[168:171], v[160:167], v255\n\t"
            "ds_read_b128 v[168:171], v254 offset:640\n\t"
            "ds_read_b64 v[160:161], v253 offset:88\n\t"
            "ds_read_b64 v[162:163], v253 offset:184\n\t"
            "ds_read_b64 v[164:165], v253 offset:1832\n\t"
            "ds_read_b64 v[166:167], v253 offset:1928\n\t"
            "s_waitcnt lgkmcnt(10)\n\t"
            "v_smfmac_f32_16x16x64_bf16 v[180:183], v[172:175], v[144:151], v255\n\t"
            "s_setprio 1\n\t"
            "ds_read_b128 v[172:175], v254 offset:704\n\t"
            "v_lshlrev_b32 v144, 16, v212\n\t"
            "v_cvt_pk_bf16_f32 v176, v176, v177\n\t"
            "v_cvt_pk_bf16_f32 v177, v178, v179\n\t"
            "v_and_b32 v145, 0xffff0000, v212\n\t"
            "v_lshlrev_b32 v146, 16, v213\n\t"
            "s_setprio 3\n\t"
            "v_mfma_f32_4x4x4_16b_bf16 v[196:199], v[176:177], v[176:177], v[196:199]\n\t"
            "v_and_b32 v147, 0xffff0000, v213\n\t"
            "v_cvt_pk_bf16_f32 v178, v180, v181\n\t"
            "v_cvt_pk_bf16_f32 v179, v182, v183\n\t"
            "v_lshlrev_b32 v180, 16, v210\n\t"
            "v_and_b32 v181, 0xffff0000, v210\n\t"
            "v_mfma_f32_4x4x4_16b_bf16 v[196:199], v[178:179], v[178:179], v[196:199]\n\t"
            "ds_write_b128 v236, v[176:179] offset:256\n\t"
            "v_mfma_f32_16x16x32_bf16 v[192:195], v[238:241], v[176:179], v[192:195]\n\t"
            "s_setprio 1\n\t"
            "v_lshlrev_b32 v176, 16, v208\n\t"
            "v_and_b32 v177, 0xffff0000, v208\n\t"
            "v_lshlrev_b32 v178, 16, v209\n\t"
            "v_and_b32 v179, 0xffff0000, v209\n\t"
            "v_lshlrev_b32 v182, 16, v211\n\t"
            "v_and_b32 v183, 0xffff0000, v211\n\t"
            "v_lshlrev_b32 v148, 16, v214\n\t"
            "v_and_b32 v149, 0xffff0000, v214\n\t"
            "v_lshlrev_b32 v150, 16, v215\n\t"
            "v_and_b32 v151, 0xffff0000, v215\n\t"
            "v_add_u32 v236, s48, v244\n\t"
            "ds_read_b128 v[212:215], v236 offset:6144\n\t"
            "s_setprio 3\n\t"
            "s_waitcnt lgkmcnt(7)\n\t"
            "v_smfmac_f32_16x16x64_bf16 v[184:187], v[168:171], v[152:159], v255\n\t"
            "v_lshlrev_b32 v152, 16, v200\n\t"
            "v_and_b32 v153, 0xffff0000, v200\n\t"
            "v_lshlrev_b32 v154, 16, v201\n\t"
            "v_and_b32 v155, 0xffff0000, v201\n\t"
            "v_lshlrev_b32 v156, 16, v202\n\t"
            "s_waitcnt lgkmcnt(2)\n\t"
            "v_smfmac_f32_16x16x64_bf16 v[188:191], v[172:175], v[160:167], v255\n\t"
            "s_setprio 1\n\t"
            "s_mov_b32 exec_hi, 0x3fffff\n\t"
            "s_add_u32 m0, s66, 0\n\t"
            "s_add_u32 s49, s79, 0\n\t"
            "buffer_load_dwordx4 v249, s[88:91], s49 offen lds\n\t"
            "s_mov_b32 exec_hi, -1\n\t"
            "s_mov_b32 exec_hi, 0x3fffff\n\t"
            "s_add_u32 m0, s66, 872\n\t"
            "s_add_u32 s49, s68, 196224\n\t"
            "buffer_load_dwordx4 v249, s[72:75], s49 offen lds\n\t"
            "s_mov_b32 exec_hi, -1\n\t"
            "s_mov_b32 exec_hi, 0x3fffff\n\t"
            "s_add_u32 m0, s66, 1744\n\t"
            "s_add_u32 s49, s68, 392448\n\t"
            "buffer_load_dwordx4 v249, s[72:75], s49 offen lds\n\t"
            "s_mov_b32 exec_hi, -1\n\t"
            "s_mov_b32 exec_hi, 0x3fffff\n\t"
            "s_add_u32 m0, s66, 2616\n\t"
            "s_add_u32 s49, s68, 588672\n\t"
            "buffer_load_dwordx4 v249, s[72:75], s49 offen lds\n\t"
            "s_mov_b32 exec_hi, -1\n\t"
            "ds_read_b32 v247, v247 offset:7856\n\t"
            "s_mov_b32 exec_hi, 0x3fffff\n\t"
            "s_add_u32 m0, s66, 3488\n\t"
            "s_add_u32 s49, s68, 784896\n\t"
            "buffer_load_dwordx4 v249, s[72:75], s49 offen lds\n\t"
            "s_mov_b32 exec_hi, -1\n\t"
            "s_mov_b32 exec_hi, 0x3fffff\n\t"
            "s_add_u32 m0, s66, 4360\n\t"
            "s_add_u32 s49, s68, 981120\n\t"
            "buffer_load_dwordx4 v249, s[72:75], s49 offen lds\n\t"
            "s_mov_b32 exec_hi, -1\n\t"
            "s_mov_b32 exec_hi, 0x3fffff\n\t"
            "s_add_u32 m0, s66, 5232\n\t"
            "s_add_u32 s49, s68, 1177344\n\t"
            "buffer_load_dwordx4 v249, s[72:75], s49 offen lds\n\t"
            "s_mov_b32 exec_hi, -1\n\t"
            "s_mov_b32 exec_hi, 0x3fffff\n\t"
            "s_add_u32 m0, s66, 6104\n\t"
            "s_add_u32 s49, s68, 1373568\n\t"
            "buffer_load_dwordx4 v249, s[72:75], s49 offen lds\n\t"
            "s_mov_b32 exec_hi, -1\n\t"
            "s_mov_b32 exec_hi, 0x3fffff\n\t"
            "s_add_u32 m0, s66, 6976\n\t"
            "s_add_u32 s49, s68, 1569792\n\t"
            "buffer_load_dwordx4 v249, s[72:75], s49 offen lds\n\t"
            "s_mov_b32 exec_hi, -1\n\t"
            "v_cvt_pk_bf16_f32 v184, v184, v185\n\t"
            "v_cvt_pk_bf16_f32 v185, v186, v187\n\t"
            "v_cvt_pk_bf16_f32 v186, v188, v189\n\t"
            "v_cvt_pk_bf16_f32 v187, v190, v191\n\t"
            "s_setprio 3\n\t"
            "v_mfma_f32_4x4x4_16b_bf16 v[196:199], v[184:185], v[184:185], v[196:199]\n\t"
            "ds_write_b128 v237, v[184:187] offset:256\n\t"
            "v_mfma_f32_16x16x32_bf16 v[192:195], v[238:241], v[184:187], v[192:195]\n\t"
            "s_waitcnt lgkmcnt(1)\n\t"
            "v_readfirstlane_b32 s77, v247\n\t"
            "s_cmp_ge_u32 s77, s70\n\t"
            "s_cbranch_scc0 .Lwb_spin%=\n\t"
            ".Lwb_go%=:\n\t"
            "v_mfma_f32_4x4x4_16b_bf16 v[196:199], v[186:187], v[186:187], v[196:199]\n\t"
            "s_setprio 1\n\t"
            "ds_read_b128 v[184:187], v242\n\t"
            "ds_read_b128 v[188:191], v243\n\t"
            "ds_read_b128 v[172:175], v243 offset:128\n\t"
            "v_and_b32 v157, 0xffff0000, v202\n\t"
            "v_lshlrev_b32 v158, 16, v203\n\t"
            "v_and_b32 v159, 0xffff0000, v203\n\t"
            "v_lshlrev_b32 v160, 16, v204\n\t"
            "v_and_b32 v161, 0xffff0000, v204\n\t"
            "v_lshlrev_b32 v162, 16, v205\n\t"
            "v_cndmask_b32_e64 v193, v196, v197, s[60:61]\n\t"
            "v_cndmask_b32_e64 v193, v193, v198, s[62:63]\n\t"
            "v_cndmask_b32_e64 v193, v193, v199, s[64:65]\n\t"
            "v_and_b32 v163, 0xffff0000, v205\n\t"
            "v_lshlrev_b32 v164, 16, v206\n\t"
            "v_and_b32 v165, 0xffff0000, v206\n\t"
            "v_lshlrev_b32 v166, 16, v207\n\t"
            "v_and_b32 v167, 0xffff0000, v207\n\t"
            "v_lshlrev_b32 v168, 16, v216\n\t"
            "v_and_b32 v169, 0xffff0000, v216\n\t"
            "v_lshlrev_b32 v170, 16, v217\n\t"
            "v_and_b32 v171, 0xffff0000, v217\n\t"
            "s_bfe_u32 s57, s56, 0x20004\n\t"
            "s_lshl_b32 s58, s57, 3\n\t"
            "s_lshr_b32 s58, s55, s58\n\t"
            "s_lshl_b64 s[36:37], s[34:35], s58\n\t"
            "ds_read_b128 v[196:199], v252\n\t"
            "ds_read_b128 v[200:203], v236\n\t"
            "ds_write_b64 v248, v[192:193] offset:18816\n\t"
            "ds_read_b128 v[192:195], v242 offset:128\n\t"
            "s_bitcmp1_b32 s57, 1\n\t"
            "s_cselect_b32 s58, 1177344, 0\n\t"
            "s_bitcmp1_b32 s57, 0\n\t"
            "s_cselect_b32 s57, 2304, 0\n\t"
            "s_add_u32 s57, s57, s58\n\t"
            "s_mov_b64 exec, 1\n\t"
            "ds_inc_u32 v255, v238 offset:7860\n\t"
            "s_mov_b64 exec, -1\n\t"
            "v_add_u32 v237, s48, v245\n\t"
            "v_add_u32 v248, s48, v246\n\t"
            "s_add_u32 s59, s59, s84\n\t"
            "s_movk_i32 s84, 800\n\t"
            "ds_read_b128 v[204:207], v237\n\t"
            "s_setprio 3\n\t"
            "s_waitcnt lgkmcnt(7)\n\t"
            "v_mfma_f32_16x16x32_bf16 v[152:155], v[0:3], v[184:187], v[152:155]\n\t"
            "s_setprio 1\n\t"
            "s_setprio 1\n\t"
            "ds_read_b128 v[208:211], v248\n\t"
            "v_mfma_f32_16x16x32_bf16 v[152:155], v[4:7], v[188:191], v[152:155]\n\t"
            "v_mfma_f32_16x16x32_bf16 v[156:159], v[28:31], v[188:191], v[156:159]\n\t"
            "v_mfma_f32_16x16x32_bf16 v[156:159], v[24:27], v[184:187], v[156:159]\n\t"
            "v_mfma_f32_16x16x32_bf16 v[160:163], v[48:51], v[184:187], v[160:163]\n\t"
            "v_mfma_f32_16x16x32_bf16 v[160:163], v[52:55], v[188:191], v[160:163]\n\t"
            "v_mfma_f32_16x16x32_bf16 v[164:167], v[76:79], v[188:191], v[164:167]\n\t"
            "s_waitcnt lgkmcnt(6)\n\t"
            "s_mov_b64 exec, s[36:37]\n\t"
            "v_add_u32 v196, s57, v196\n\t"
            "s_mov_b64 exec, -1\n\t"
            "v_mfma_f32_16x16x32_bf16 v[164:167], v[72:75], v[184:187], v[164:167]\n\t"
            "v_mfma_f32_16x16x32_bf16 v[176:179], v[96:99], v[184:187], v[176:179]\n\t"
            "v_mfma_f32_16x16x32_bf16 v[176:179], v[100:103], v[188:191], v[176:179]\n\t"
            "v_mfma_f32_16x16x32_bf16 v[180:183], v[124:127], v[188:191], v[180:183]\n\t"
            "ds_read_b128 v[188:191], v243 offset:256\n\t"
            "v_mfma_f32_16x16x32_bf16 v[180:183], v[120:123], v[184:187], v[180:183]\n\t"
            "ds_read_b128 v[184:187], v242 offset:256\n\t"
            "s_waitcnt lgkmcnt(5)\n\t"
            "v_mfma_f32_16x16x32_bf16 v[152:155], v[8:11], v[192:195], v[152:155]\n\t"
            "v_mfma_f32_16x16x32_bf16 v[152:155], v[12:15], v[172:175], v[152:155]\n\t"
            "v_mfma_f32_16x16x32_bf16 v[156:159], v[36:39], v[172:175], v[156:159]\n\t"
            "v_mfma_f32_16x16x32_bf16 v[156:159], v[32:35], v[192:195], v[156:159]\n\t"
            "v_mfma_f32_16x16x32_bf16 v[160:163], v[56:59], v[192:195], v[160:163]\n\t"
            "v_mfma_f32_16x16x32_bf16 v[160:163], v[60:63], v[172:175], v[160:163]\n\t"
            "v_mfma_f32_16x16x32_bf16 v[164:167], v[84:87], v[172:175], v[164:167]\n\t"
            "v_mfma_f32_16x16x32_bf16 v[164:167], v[80:83], v[192:195], v[164:167]\n\t"
            "v_mfma_f32_16x16x32_bf16 v[176:179], v[104:107], v[192:195], v[176:179]\n\t"
            "v_mfma_f32_16x16x32_bf16 v[176:179], v[108:111], v[172:175], v[176:179]\n\t"
            "v_mfma_f32_16x16x32_bf16 v[180:183], v[132:135], v[172:175], v[180:183]\n\t"
            "ds_read_b128 v[172:175], v243 offset:6144\n\t"
            "v_mfma_f32_16x16x32_bf16 v[180:183], v[128:131], v[192:195], v[180:183]\n\t"
            "ds_read_b128 v[192:195], v242 offset:6144\n\t"
            "s_waitcnt lgkmcnt(2)\n\t"
            "v_mfma_f32_16x16x32_bf16 v[152:155], v[16:19], v[184:187], v[152:155]\n\t"
            "v_mfma_f32_16x16x32_bf16 v[152:155], v[20:23], v[188:191], v[152:155]\n\t"
            "v_mfma_f32_16x16x32_bf16 v[156:159], v[44:47], v[188:191], v[156:159]\n\t"
            "v_mfma_f32_16x16x32_bf16 v[156:159], v[40:43], v[184:187], v[156:159]\n\t"
            "v_mfma_f32_16x16x32_bf16 v[160:163], v[64:67], v[184:187], v[160:163]\n\t"
            "v_mfma_f32_16x16x32_bf16 v[160:163], v[68:71], v[188:191], v[160:163]\n\t"
            "v_mfma_f32_16x16x32_bf16 v[164:167], v[92:95], v[188:191], v[164:167]\n\t"
            "v_mfma_f32_16x16x32_bf16 v[164:167], v[88:91], v[184:187], v[164:167]\n\t"
            "s_nop 1\n\t"
            "v_cvt_pk_bf16_f32 v152, v152, v153\n\t"
            "v_mfma_f32_16x16x32_bf16 v[176:179], v[112:115], v[184:187], v[176:179]\n\t"
            "v_cvt_pk_bf16_f32 v153, v154, v155\n\t"
            "v_mfma_f32_16x16x32_bf16 v[176:179], v[116:119], v[188:191], v[176:179]\n\t"
            "v_cvt_pk_bf16_f32 v154, v156, v157\n\t"
            "v_cvt_pk_bf16_f32 v155, v158, v159\n\t"
            "v_mfma_f32_16x16x32_bf16 v[180:183], v[140:143], v[188:191], v[180:183]\n\t"
            "ds_read_b128 v[188:191], v243 offset:6272\n\t"
            "v_cvt_pk_bf16_f32 v160, v160, v161\n\t"
            "v_mfma_f32_16x16x32_bf16 v[180:183], v[136:139], v[184:187], v[180:183]\n\t"
            "ds_read_b128 v[184:187], v242 offset:6272\n\t"
            "v_cvt_pk_bf16_f32 v161, v162, v163\n\t"
            "s_waitcnt lgkmcnt(2)\n\t"
            "v_mfma_f32_16x16x32_bf16 v[144:147], v[0:3], v[192:195], v[144:147]\n\t"
            "v_cvt_pk_bf16_f32 v176, v176, v177\n\t"
            "v_cvt_pk_bf16_f32 v177, v178, v179\n\t"
            "v_mfma_f32_16x16x32_bf16 v[144:147], v[4:7], v[172:175], v[144:147]\n\t"
            "v_cvt_pk_bf16_f32 v162, v164, v165\n\t"
            "v_cvt_pk_bf16_f32 v163, v166, v167\n\t"
            "v_mfma_f32_16x16x32_bf16 v[148:151], v[28:31], v[172:175], v[148:151]\n\t"
            "v_cvt_pk_bf16_f32 v178, v180, v181\n\t"
            "v_cvt_pk_bf16_f32 v179, v182, v183\n\t"
            "v_mfma_f32_16x16x32_bf16 v[148:151], v[24:27], v[192:195], v[148:151]\n\t"
            "v_lshlrev_b32 v156, 16, v218\n\t"
            "v_and_b32 v157, 0xffff0000, v218\n\t"
            "v_mfma_f32_16x16x32_bf16 v[168:171], v[48:51], v[192:195], v[168:171]\n\t"
            "v_lshlrev_b32 v158, 16, v219\n\t"
            "v_and_b32 v159, 0xffff0000, v219\n\t"
            "v_mfma_f32_16x16x32_bf16 v[168:171], v[52:55], v[172:175], v[168:171]\n\t"
            "ds_read_b128 v[216:219], v237 offset:6144\n\t"
            "v_lshlrev_b32 v164, 16, v220\n\t"
            "v_mfma_f32_16x16x32_bf16 v[156:159], v[76:79], v[172:175], v[156:159]\n\t"
            "v_and_b32 v165, 0xffff0000, v220\n\t"
            "v_lshlrev_b32 v166, 16, v221\n\t"
            "v_mfma_f32_16x16x32_bf16 v[156:159], v[72:75], v[192:195], v[156:159]\n\t"
            "v_and_b32 v167, 0xffff0000, v221\n\t"
            "v_lshlrev_b32 v180, 16, v222\n\t"
            "v_and_b32 v181, 0xffff0000, v222\n\t"
            "v_mfma_f32_16x16x32_bf16 v[164:167], v[96:99], v[192:195], v[164:167]\n\t"
            "v_lshlrev_b32 v182, 16, v223\n\t"
            "v_and_b32 v183, 0xffff0000, v223\n\t"
            "v_mfma_f32_16x16x32_bf16 v[164:167], v[100:103], v[172:175], v[164:167]\n\t"
            "ds_read_b128 v[220:223], v248 offset:6144\n\t"
            "buffer_store_dwordx4 v[152:155], v196, s[40:43], s45 offen sc1\n\t"
            "v_mfma_f32_16x16x32_bf16 v[180:183], v[124:127], v[172:175], v[180:183]\n\t"
            "ds_read_b128 v[172:175], v243 offset:6400\n\t"
            "v_lshlrev_b32 v152, 16, v224\n\t"
            "v_mfma_f32_16x16x32_bf16 v[180:183], v[120:123], v[192:195], v[180:183]\n\t"
            "ds_read_b128 v[192:195], v242 offset:6400\n\t"
            "buffer_store_dwordx4 v[160:163], v196, s[40:43], s45 offen offset:64 sc1\n\t"
            "s_waitcnt lgkmcnt(4)\n\t"
            "v_mfma_f32_16x16x32_bf16 v[144:147], v[8:11], v[184:187], v[144:147]\n\t"
            "v_and_b32 v153, 0xffff0000, v224\n\t"
            "v_lshlrev_b32 v154, 16, v225\n\t"
            "v_mfma_f32_16x16x32_bf16 v[144:147], v[12:15], v[188:191], v[144:147]\n\t"
            "buffer_store_dwordx4 v[176:179], v196, s[40:43], s45 offen offset:128 sc1\n\t"
            "v_mfma_f32_16x16x32_bf16 v[148:151], v[36:39], v[188:191], v[148:151]\n\t"
            "v_and_b32 v155, 0xffff0000, v225\n\t"
            "v_lshlrev_b32 v160, 16, v226\n\t"
            "v_mfma_f32_16x16x32_bf16 v[148:151], v[32:35], v[184:187], v[148:151]\n\t"
            "v_and_b32 v161, 0xffff0000, v226\n\t"
            "v_lshlrev_b32 v162, 16, v227\n\t"
            "v_mfma_f32_16x16x32_bf16 v[168:171], v[56:59], v[184:187], v[168:171]\n\t"
            "v_and_b32 v163, 0xffff0000, v227\n\t"
            "v_lshlrev_b32 v176, 16, v228\n\t"
            "v_mfma_f32_16x16x32_bf16 v[168:171], v[60:63], v[188:191], v[168:171]\n\t"
            "ds_read_b128 v[224:227], v236 offset:12288\n\t"
            "v_and_b32 v177, 0xffff0000, v228\n\t"
            "v_mfma_f32_16x16x32_bf16 v[156:159], v[84:87], v[188:191], v[156:159]\n\t"
            "v_lshlrev_b32 v178, 16, v229\n\t"
            "v_and_b32 v179, 0xffff0000, v229\n\t"
            "v_mfma_f32_16x16x32_bf16 v[156:159], v[80:83], v[184:187], v[156:159]\n\t"
            "v_mfma_f32_16x16x32_bf16 v[164:167], v[104:107], v[184:187], v[164:167]\n\t"
            "v_mfma_f32_16x16x32_bf16 v[164:167], v[108:111], v[188:191], v[164:167]\n\t"
            "v_mfma_f32_16x16x32_bf16 v[180:183], v[132:135], v[188:191], v[180:183]\n\t"
            "ds_read_b128 v[188:191], v243 offset:12288\n\t"
            "v_mfma_f32_16x16x32_bf16 v[180:183], v[128:131], v[184:187], v[180:183]\n\t"
            "ds_read_b128 v[184:187], v242 offset:12288\n\t"
            "s_waitcnt lgkmcnt(3)\n\t"
            "v_mfma_f32_16x16x32_bf16 v[144:147], v[16:19], v[192:195], v[144:147]\n\t"
            "v_mfma_f32_16x16x32_bf16 v[144:147], v[20:23], v[172:175], v[144:147]\n\t"
            "v_mfma_f32_16x16x32_bf16 v[168:171], v[64:67], v[192:195], v[168:171]\n\t"
            "v_mfma_f32_16x16x32_bf16 v[168:171], v[68:71], v[172:175], v[168:171]\n\t"
            "v_mfma_f32_16x16x32_bf16 v[164:167], v[112:115], v[192:195], v[164:167]\n\t"
            "v_mfma_f32_16x16x32_bf16 v[164:167], v[116:119], v[172:175], v[164:167]\n\t"
            "v_mfma_f32_16x16x32_bf16 v[148:151], v[44:47], v[172:175], v[148:151]\n\t"
            "v_mfma_f32_16x16x32_bf16 v[148:151], v[40:43], v[192:195], v[148:151]\n\t"
            "s_nop 1\n\t"
            "v_cvt_pk_bf16_f32 v144, v144, v145\n\t"
            "v_mfma_f32_16x16x32_bf16 v[156:159], v[92:95], v[172:175], v[156:159]\n\t"
            "v_cvt_pk_bf16_f32 v145, v146, v147\n\t"
            "v_mfma_f32_16x16x32_bf16 v[156:159], v[88:91], v[192:195], v[156:159]\n\t"
            "v_cvt_pk_bf16_f32 v168, v168, v169\n\t"
            "v_cvt_pk_bf16_f32 v169, v170, v171\n\t"
            "v_mfma_f32_16x16x32_bf16 v[180:183], v[140:143], v[172:175], v[180:183]\n\t"
            "ds_read_b128 v[172:175], v243 offset:12416\n\t"
            "v_cvt_pk_bf16_f32 v164, v164, v165\n\t"
            "v_mfma_f32_16x16x32_bf16 v[180:183], v[136:139], v[192:195], v[180:183]\n\t"
            "ds_read_b128 v[192:195], v242 offset:12416\n\t"
            "v_cvt_pk_bf16_f32 v165, v166, v167\n\t"
            "s_waitcnt lgkmcnt(2)\n\t"
            "v_mfma_f32_16x16x32_bf16 v[152:155], v[0:3], v[184:187], v[152:155]\n\t"
            "v_cvt_pk_bf16_f32 v146, v148, v149\n\t"
            "v_cvt_pk_bf16_f32 v147, v150, v151\n\t"
            "v_mfma_f32_16x16x32_bf16 v[152:155], v[4:7], v[188:191], v[152:155]\n\t"
            "v_cvt_pk_bf16_f32 v170, v156, v157\n\t"
            "v_cvt_pk_bf16_f32 v171, v158, v159\n\t"
            "v_mfma_f32_16x16x32_bf16 v[160:163], v[28:31], v[188:191], v[160:163]\n\t"
            "v_cvt_pk_bf16_f32 v166, v180, v181\n\t"
            "v_cvt_pk_bf16_f32 v167, v182, v183\n\t"
            "v_mfma_f32_16x16x32_bf16 v[160:163], v[24:27], v[184:187], v[160:163]\n\t"
            "v_lshlrev_b32 v148, 16, v230\n\t"
            "v_and_b32 v149, 0xffff0000, v230\n\t"
            "v_mfma_f32_16x16x32_bf16 v[176:179], v[48:51], v[184:187], v[176:179]\n\t"
            "v_lshlrev_b32 v150, 16, v231\n\t"
            "v_and_b32 v151, 0xffff0000, v231\n\t"
            "v_mfma_f32_16x16x32_bf16 v[176:179], v[52:55], v[188:191], v[176:179]\n\t"
            "ds_read_b128 v[228:231], v237 offset:12288\n\t"
            "v_lshlrev_b32 v156, 16, v232\n\t"
            "v_mfma_f32_16x16x32_bf16 v[148:151], v[76:79], v[188:191], v[148:151]\n\t"
            "v_and_b32 v157, 0xffff0000, v232\n\t"
            "v_lshlrev_b32 v158, 16, v233\n\t"
            "v_mfma_f32_16x16x32_bf16 v[148:151], v[72:75], v[184:187], v[148:151]\n\t"
            "v_and_b32 v159, 0xffff0000, v233\n\t"
            "v_lshlrev_b32 v180, 16, v234\n\t"
            "v_and_b32 v181, 0xffff0000, v234\n\t"
            "v_mfma_f32_16x16x32_bf16 v[156:159], v[96:99], v[184:187], v[156:159]\n\t"
            "v_lshlrev_b32 v182, 16, v235\n\t"
            "v_and_b32 v183, 0xffff0000, v235\n\t"
            "v_mfma_f32_16x16x32_bf16 v[156:159], v[100:103], v[188:191], v[156:159]\n\t"
            "ds_read_b128 v[232:235], v248 offset:12288\n\t"
            "buffer_store_dwordx4 v[144:147], v197, s[40:43], s45 offen sc1\n\t"
            "v_mfma_f32_16x16x32_bf16 v[180:183], v[124:127], v[188:191], v[180:183]\n\t"
            "ds_read_b128 v[188:191], v243 offset:12544\n\t"
            "v_mfma_f32_16x16x32_bf16 v[180:183], v[120:123], v[184:187], v[180:183]\n\t"
            "ds_read_b128 v[184:187], v242 offset:12544\n\t"
            "buffer_store_dwordx4 v[168:171], v197, s[40:43], s45 offen offset:64 sc1\n\t"
            "s_waitcnt lgkmcnt(4)\n\t"
            "v_mfma_f32_16x16x32_bf16 v[152:155], v[8:11], v[192:195], v[152:155]\n\t"
            "v_mfma_f32_16x16x32_bf16 v[152:155], v[12:15], v[172:175], v[152:155]\n\t"
            "buffer_store_dwordx4 v[164:167], v197, s[40:43], s45 offen offset:128 sc1\n\t"
            "v_mfma_f32_16x16x32_bf16 v[160:163], v[36:39], v[172:175], v[160:163]\n\t"
            "v_mfma_f32_16x16x32_bf16 v[160:163], v[32:35], v[192:195], v[160:163]\n\t"
            "s_waitcnt lgkmcnt(0)\n\t"
            "s_barrier\n\t"
            "s_setprio 3\n\t"
            "v_mfma_f32_16x16x32_bf16 v[176:179], v[56:59], v[192:195], v[176:179]\n\t"
            "v_mfma_f32_16x16x32_bf16 v[176:179], v[60:63], v[172:175], v[176:179]\n\t"
            "v_mfma_f32_16x16x32_bf16 v[148:151], v[84:87], v[172:175], v[148:151]\n\t"
            "v_mfma_f32_16x16x32_bf16 v[148:151], v[80:83], v[192:195], v[148:151]\n\t"
            "v_mfma_f32_16x16x32_bf16 v[156:159], v[104:107], v[192:195], v[156:159]\n\t"
            "v_mfma_f32_16x16x32_bf16 v[156:159], v[108:111], v[172:175], v[156:159]\n\t"
            "v_mfma_f32_16x16x32_bf16 v[152:155], v[16:19], v[184:187], v[152:155]\n\t"
            "v_mfma_f32_16x16x32_bf16 v[152:155], v[20:23], v[188:191], v[152:155]\n\t"
            "v_mfma_f32_16x16x32_bf16 v[160:163], v[44:47], v[188:191], v[160:163]\n\t"
            "v_mfma_f32_16x16x32_bf16 v[160:163], v[40:43], v[184:187], v[160:163]\n\t"
            "v_mfma_f32_16x16x32_bf16 v[180:183], v[132:135], v[172:175], v[180:183]\n\t"
            "v_mfma_f32_16x16x32_bf16 v[180:183], v[128:131], v[192:195], v[180:183]\n\t"
            "v_mfma_f32_16x16x32_bf16 v[176:179], v[64:67], v[184:187], v[176:179]\n\t"
            "v_mfma_f32_16x16x32_bf16 v[176:179], v[68:71], v[188:191], v[176:179]\n\t"
            "s_nop 1\n\t"
            "v_cvt_pk_bf16_f32 v152, v152, v153\n\t"
            "v_mfma_f32_16x16x32_bf16 v[148:151], v[92:95], v[188:191], v[148:151]\n\t"
            "v_cvt_pk_bf16_f32 v153, v154, v155\n\t"
            "v_mfma_f32_16x16x32_bf16 v[148:151], v[88:91], v[184:187], v[148:151]\n\t"
            "v_cvt_pk_bf16_f32 v154, v160, v161\n\t"
            "v_cvt_pk_bf16_f32 v155, v162, v163\n\t"
            "v_mfma_f32_16x16x32_bf16 v[156:159], v[112:115], v[184:187], v[156:159]\n\t"
            "v_cvt_pk_bf16_f32 v176, v176, v177\n\t"
            "v_cvt_pk_bf16_f32 v177, v178, v179\n\t"
            "v_mfma_f32_16x16x32_bf16 v[156:159], v[116:119], v[188:191], v[156:159]\n\t"
            "buffer_store_dwordx4 v[152:155], v198, s[40:43], s45 offen sc1\n\t"
            "v_mfma_f32_16x16x32_bf16 v[180:183], v[140:143], v[188:191], v[180:183]\n\t"
            "v_cvt_pk_bf16_f32 v178, v148, v149\n\t"
            "v_cvt_pk_bf16_f32 v179, v150, v151\n\t"
            "v_mfma_f32_16x16x32_bf16 v[180:183], v[136:139], v[184:187], v[180:183]\n\t"
            "buffer_store_dwordx4 v[176:179], v198, s[40:43], s45 offen offset:64 sc1\n\t"
            "s_nop 1\n\t"
            "v_cvt_pk_bf16_f32 v156, v156, v157\n\t"
            "v_cvt_pk_bf16_f32 v157, v158, v159\n\t"
            "s_nop 2\n\t"
            "v_cvt_pk_bf16_f32 v158, v180, v181\n\t"
            "v_cvt_pk_bf16_f32 v159, v182, v183\n\t"
            "buffer_store_dwordx4 v[156:159], v198, s[40:43], s45 offen offset:128 sc1\n\t"
            "s_add_u32 s45, s46, s27\n\t"
            "s_mov_b32 s46, s47\n\t"
            "s_mov_b32 s47, s68\n\t"
            "s_xor_b32 s48, s48, s71\n\t"
            "s_xor_b32 s67, s67, s71\n\t"
            "s_add_u32 s76, s76, 4\n\t"
            "s_add_u32 s70, s70, 4\n\t"
            "s_lshl_b32 s56, s56, 2\n\t"
            "s_lshr_b32 s49, s26, 30\n\t"
            "s_or_b32 s56, s56, s49\n\t"
            "s_cmp_lt_u32 s46, 0x7000000\n\t"
            "s_cbranch_scc1 .Lwb_loop%=\n\t"
            "s_branch .Lwb_end%=\n\t"
            ".Lwb_spin%=:\n\t"
            "s_cmp_eq_u32 s83, 0\n\t"
            "s_cbranch_scc1 .Lwb_go%=\n\t"
            "s_sub_u32 s83, s83, 1\n\t"
            "s_sleep 2\n\t"
            "v_mov_b32 v247, 0\n\t"
            "ds_read_b32 v247, v247 offset:7856\n\t"
            "s_waitcnt lgkmcnt(0)\n\t"
            "v_readfirstlane_b32 s77, v247\n\t"
            "s_cmp_ge_u32 s77, s70\n\t"
            "s_cbranch_scc0 .Lwb_spin%=\n\t"
            "s_branch .Lwb_go%=\n\t"
            ".Lwb_end%=:\n\t"
            "s_waitcnt lgkmcnt(0)\n\t"
            "s_nop 7\n\t"
            "ds_read_b128 v[196:199], v252\n\t"
            "s_bfe_u32 s57, s56, 0x20004\n\t"
            "s_lshl_b32 s58, s57, 3\n\t"
            "s_lshr_b32 s58, s55, s58\n\t"
            "s_lshl_b64 s[36:37], s[34:35], s58\n\t"
            "s_bitcmp1_b32 s57, 1\n\t"
            "s_cselect_b32 s58, 1177344, 0\n\t"
            "s_bitcmp1_b32 s57, 0\n\t"
            "s_cselect_b32 s57, 2304, 0\n\t"
            "s_add_u32 s57, s57, s58\n\t"
            "v_lshlrev_b32 v152, 16, v200\n\t"
            "v_and_b32 v153, 0xffff0000, v200\n\t"
            "v_lshlrev_b32 v154, 16, v201\n\t"
            "v_and_b32 v155, 0xffff0000, v201\n\t"
            "v_lshlrev_b32 v156, 16, v202\n\t"
            "v_and_b32 v157, 0xffff0000, v202\n\t"
            "v_lshlrev_b32 v158, 16, v203\n\t"
            "v_and_b32 v159, 0xffff0000, v203\n\t"
            "v_lshlrev_b32 v160, 16, v204\n\t"
            "v_and_b32 v161, 0xffff0000, v204\n\t"
            "v_lshlrev_b32 v162, 16, v205\n\t"
            "v_and_b32 v163, 0xffff0000, v205\n\t"
            "v_lshlrev_b32 v164, 16, v206\n\t"
            "v_and_b32 v165, 0xffff0000, v206\n\t"
            "v_lshlrev_b32 v166, 16, v207\n\t"
            "v_and_b32 v167, 0xffff0000, v207\n\t"
            "v_lshlrev_b32 v176, 16, v208\n\t"
            "v_and_b32 v177, 0xffff0000, v208\n\t"
            "v_lshlrev_b32 v178, 16, v209\n\t"
            "v_and_b32 v179, 0xffff0000, v209\n\t"
            "v_lshlrev_b32 v180, 16, v210\n\t"
            "v_and_b32 v181, 0xffff0000, v210\n\t"
            "v_lshlrev_b32 v182, 16, v211\n\t"
            "v_and_b32 v183, 0xffff0000, v211\n\t"
            "v_lshlrev_b32 v144, 16, v212\n\t"
            "v_and_b32 v145, 0xffff0000, v212\n\t"
            "v_lshlrev_b32 v146, 16, v213\n\t"
            "v_and_b32 v147, 0xffff0000, v213\n\t"
            "v_lshlrev_b32 v148, 16, v214\n\t"
            "v_and_b32 v149, 0xffff0000, v214\n\t"
            "v_lshlrev_b32 v150, 16, v215\n\t"
            "v_and_b32 v151, 0xffff0000, v215\n\t"
            "v_lshlrev_b32 v168, 16, v216\n\t"
            "v_and_b32 v169, 0xffff0000, v216\n\t"
            "v_lshlrev_b32 v170, 16, v217\n\t"
            "s_waitcnt lgkmcnt(0)\n\t"
            "s_mov_b64 exec, s[36:37]\n\t"
            "v_add_u32 v196, s57, v196\n\t"
            "s_mov_b64 exec, -1\n\t"
            ".Lwk_t1%=:\n\t"
            "v_mov_b32 v247, 0\n\t"
            "ds_read_b32 v247, v247 offset:7856\n\t"
            "s_waitcnt lgkmcnt(0)\n\t"
            "v_readfirstlane_b32 s77, v247\n\t"
            "s_cmp_ge_u32 s77, s70\n\t"
            "s_cbranch_scc1 .Lwk_t2%=\n\t"
            "s_cmp_eq_u32 s83, 0\n\t"
            "s_cbranch_scc1 .Lwk_t2%=\n\t"
            "s_sub_u32 s83, s83, 1\n\t"
            "s_sleep 1\n\t"
            "s_branch .Lwk_t1%=\n\t"
            ".Lwk_t2%=:\n\t"
            "v_and_b32 v171, 0xffff0000, v217\n\t"
            "ds_read_b128 v[184:187], v242\n\t"
            "ds_read_b128 v[188:191], v243\n\t"
            "ds_read_b128 v[192:195], v242 offset:128\n\t"
            "ds_read_b128 v[172:175], v243 offset:128\n\t"
            "s_waitcnt lgkmcnt(2)\n\t"
            "v_mfma_f32_16x16x32_bf16 v[152:155], v[0:3], v[184:187], v[152:155]\n\t"
            "v_mfma_f32_16x16x32_bf16 v[152:155], v[4:7], v[188:191], v[152:155]\n\t"
            "v_mfma_f32_16x16x32_bf16 v[156:159], v[28:31], v[188:191], v[156:159]\n\t"
            "v_mfma_f32_16x16x32_bf16 v[156:159], v[24:27], v[184:187], v[156:159]\n\t"
            "v_mfma_f32_16x16x32_bf16 v[160:163], v[48:51], v[184:187], v[160:163]\n\t"
            "v_mfma_f32_16x16x32_bf16 v[160:163], v[52:55], v[188:191], v[160:163]\n\t"
            "v_mfma_f32_16x16x32_bf16 v[164:167], v[76:79], v[188:191], v[164:167]\n\t"
            "v_mfma_f32_16x16x32_bf16 v[164:167], v[72:75], v[184:187], v[164:167]\n\t"
            "v_mfma_f32_16x16x32_bf16 v[176:179], v[96:99], v[184:187], v[176:179]\n\t"
            "v_mfma_f32_16x16x32_bf16 v[176:179], v[100:103], v[188:191], v[176:179]\n\t"
            "v_mfma_f32_16x16x32_bf16 v[180:183], v[124:127], v[188:191], v[180:183]\n\t"
            "ds_read_b128 v[188:191], v243 offset:256\n\t"
            "v_mfma_f32_16x16x32_bf16 v[180:183], v[120:123], v[184:187], v[180:183]\n\t"
            "ds_read_b128 v[184:187], v242 offset:256\n\t"
            "s_waitcnt lgkmcnt(2)\n\t"
            "v_mfma_f32_16x16x32_bf16 v[152:155], v[8:11], v[192:195], v[152:155]\n\t"
            "v_mfma_f32_16x16x32_bf16 v[152:155], v[12:15], v[172:175], v[152:155]\n\t"
            "v_mfma_f32_16x16x32_bf16 v[156:159], v[36:39], v[172:175], v[156:159]\n\t"
            "v_mfma_f32_16x16x32_bf16 v[156:159], v[32:35], v[192:195], v[156:159]\n\t"
            "v_mfma_f32_16x16x32_bf16 v[160:163], v[56:59], v[192:195], v[160:163]\n\t"
            "v_mfma_f32_16x16x32_bf16 v[160:163], v[60:63], v[172:175], v[160:163]\n\t"
            "v_mfma_f32_16x16x32_bf16 v[164:167], v[84:87], v[172:175], v[164:167]\n\t"
            "v_mfma_f32_16x16x32_bf16 v[164:167], v[80:83], v[192:195], v[164:167]\n\t"
            "v_mfma_f32_16x16x32_bf16 v[176:179], v[104:107], v[192:195], v[176:179]\n\t"
            "v_mfma_f32_16x16x32_bf16 v[176:179], v[108:111], v[172:175], v[176:179]\n\t"
            "v_mfma_f32_16x16x32_bf16 v[180:183], v[132:135], v[172:175], v[180:183]\n\t"
            "ds_read_b128 v[172:175], v243 offset:6144\n\t"
            "v_mfma_f32_16x16x32_bf16 v[180:183], v[128:131], v[192:195], v[180:183]\n\t"
            "ds_read_b128 v[192:195], v242 offset:6144\n\t"
            "s_waitcnt lgkmcnt(2)\n\t"
            "v_mfma_f32_16x16x32_bf16 v[152:155], v[16:19], v[184:187], v[152:155]\n\t"
            "v_mfma_f32_16x16x32_bf16 v[152:155], v[20:23], v[188:191], v[152:155]\n\t"
            "v_mfma_f32_16x16x32_bf16 v[156:159], v[44:47], v[188:191], v[156:159]\n\t"
            "v_mfma_f32_16x16x32_bf16 v[156:159], v[40:43], v[184:187], v[156:159]\n\t"
            "v_mfma_f32_16x16x32_bf16 v[160:163], v[64:67], v[184:187], v[160:163]\n\t"
            "v_mfma_f32_16x16x32_bf16 v[160:163], v[68:71], v[188:191], v[160:163]\n\t"
            "v_mfma_f32_16x16x32_bf16 v[164:167], v[92:95], v[188:191], v[164:167]\n\t"
            "v_mfma_f32_16x16x32_bf16 v[164:167], v[88:91], v[184:187], v[164:167]\n\t"
            "s_nop 1\n\t"
            "v_cvt_pk_bf16_f32 v152, v152, v153\n\t"
            "v_mfma_f32_16x16x32_bf16 v[176:179], v[112:115], v[184:187], v[176:179]\n\t"
            "v_cvt_pk_bf16_f32 v153, v154, v155\n\t"
            "v_mfma_f32_16x16x32_bf16 v[176:179], v[116:119], v[188:191], v[176:179]\n\t"
            "v_cvt_pk_bf16_f32 v154, v156, v157\n\t"
            "v_cvt_pk_bf16_f32 v155, v158, v159\n\t"
            "v_mfma_f32_16x16x32_bf16 v[180:183], v[140:143], v[188:191], v[180:183]\n\t"
            "ds_read_b128 v[188:191], v243 offset:6272\n\t"
            "v_cvt_pk_bf16_f32 v160, v160, v161\n\t"
            "v_mfma_f32_16x16x32_bf16 v[180:183], v[136:139], v[184:187], v[180:183]\n\t"
            "ds_read_b128 v[184:187], v242 offset:6272\n\t"
            "v_cvt_pk_bf16_f32 v161, v162, v163\n\t"
            "s_waitcnt lgkmcnt(2)\n\t"
            "v_mfma_f32_16x16x32_bf16 v[144:147], v[0:3], v[192:195], v[144:147]\n\t"
            "v_cvt_pk_bf16_f32 v176, v176, v177\n\t"
            "v_cvt_pk_bf16_f32 v177, v178, v179\n\t"
            "v_mfma_f32_16x16x32_bf16 v[144:147], v[4:7], v[172:175], v[144:147]\n\t"
            "v_cvt_pk_bf16_f32 v162, v164, v165\n\t"
            "v_cvt_pk_bf16_f32 v163, v166, v167\n\t"
            "v_mfma_f32_16x16x32_bf16 v[148:151], v[28:31], v[172:175], v[148:151]\n\t"
            "v_cvt_pk_bf16_f32 v178, v180, v181\n\t"
            "v_cvt_pk_bf16_f32 v179, v182, v183\n\t"
            "v_mfma_f32_16x16x32_bf16 v[148:151], v[24:27], v[192:195], v[148:151]\n\t"
            "v_lshlrev_b32 v156, 16, v218\n\t"
            "v_and_b32 v157, 0xffff0000, v218\n\t"
            "v_mfma_f32_16x16x32_bf16 v[168:171], v[48:51], v[192:195], v[168:171]\n\t"
            "v_lshlrev_b32 v158, 16, v219\n\t"
            "v_and_b32 v159, 0xffff0000, v219\n\t"
            "v_mfma_f32_16x16x32_bf16 v[168:171], v[52:55], v[172:175], v[168:171]\n\t"
            "v_lshlrev_b32 v164, 16, v220\n\t"
            "v_and_b32 v165, 0xffff0000, v220\n\t"
            "v_mfma_f32_16x16x32_bf16 v[156:159], v[76:79], v[172:175], v[156:159]\n\t"
            "v_lshlrev_b32 v166, 16, v221\n\t"
            "v_and_b32 v167, 0xffff0000, v221\n\t"
            "v_mfma_f32_16x16x32_bf16 v[156:159], v[72:75], v[192:195], v[156:159]\n\t"
            "v_lshlrev_b32 v180, 16, v222\n\t"
            "v_and_b32 v181, 0xffff0000, v222\n\t"
            "v_mfma_f32_16x16x32_bf16 v[164:167], v[96:99], v[192:195], v[164:167]\n\t"
            "v_lshlrev_b32 v182, 16, v223\n\t"
            "v_and_b32 v183, 0xffff0000, v223\n\t"
            "v_mfma_f32_16x16x32_bf16 v[164:167], v[100:103], v[172:175], v[164:167]\n\t"
            "buffer_store_dwordx4 v[152:155], v196, s[40:43], s45 offen sc1\n\t"
            "v_mfma_f32_16x16x32_bf16 v[180:183], v[124:127], v[172:175], v[180:183]\n\t"
            "ds_read_b128 v[172:175], v243 offset:6400\n\t"
            "v_lshlrev_b32 v152, 16, v224\n\t"
            "v_mfma_f32_16x16x32_bf16 v[180:183], v[120:123], v[192:195], v[180:183]\n\t"
            "ds_read_b128 v[192:195], v242 offset:6400\n\t"
            "buffer_store_dwordx4 v[160:163], v196, s[40:43], s45 offen offset:64 sc1\n\t"
            "s_waitcnt lgkmcnt(2)\n\t"
            "v_mfma_f32_16x16x32_bf16 v[144:147], v[8:11], v[184:187], v[144:147]\n\t"
            "v_and_b32 v153, 0xffff0000, v224\n\t"
            "v_lshlrev_b32 v154, 16, v225\n\t"
            "v_mfma_f32_16x16x32_bf16 v[144:147], v[12:15], v[188:191], v[144:147]\n\t"
            "buffer_store_dwordx4 v[176:179], v196, s[40:43], s45 offen offset:128 sc1\n\t"
            "v_mfma_f32_16x16x32_bf16 v[148:151], v[36:39], v[188:191], v[148:151]\n\t"
            "v_and_b32 v155, 0xffff0000, v225\n\t"
            "v_lshlrev_b32 v160, 16, v226\n\t"
            "v_mfma_f32_16x16x32_bf16 v[148:151], v[32:35], v[184:187], v[148:151]\n\t"
            "v_and_b32 v161, 0xffff0000, v226\n\t"
            "v_lshlrev_b32 v162, 16, v227\n\t"
            "v_mfma_f32_16x16x32_bf16 v[168:171], v[56:59], v[184:187], v[168:171]\n\t"
            "v_and_b32 v163, 0xffff0000, v227\n\t"
            "v_lshlrev_b32 v176, 16, v228\n\t"
            "v_mfma_f32_16x16x32_bf16 v[168:171], v[60:63], v[188:191], v[168:171]\n\t"
            "v_and_b32 v177, 0xffff0000, v228\n\t"
            "v_lshlrev_b32 v178, 16, v229\n\t"
            "v_mfma_f32_16x16x32_bf16 v[156:159], v[84:87], v[188:191], v[156:159]\n\t"
            "v_and_b32 v179, 0xffff0000, v229\n\t"
            "v_mfma_f32_16x16x32_bf16 v[156:159], v[80:83], v[184:187], v[156:159]\n\t"
            "v_mfma_f32_16x16x32_bf16 v[164:167], v[104:107], v[184:187], v[164:167]\n\t"
            "v_mfma_f32_16x16x32_bf16 v[164:167], v[108:111], v[188:191], v[164:167]\n\t"
            "v_mfma_f32_16x16x32_bf16 v[180:183], v[132:135], v[188:191], v[180:183]\n\t"
            "ds_read_b128 v[188:191], v243 offset:12288\n\t"
            "v_mfma_f32_16x16x32_bf16 v[180:183], v[128:131], v[184:187], v[180:183]\n\t"
            "ds_read_b128 v[184:187], v242 offset:12288\n\t"
            "s_waitcnt lgkmcnt(2)\n\t"
            "v_mfma_f32_16x16x32_bf16 v[144:147], v[16:19], v[192:195], v[144:147]\n\t"
            "v_mfma_f32_16x16x32_bf16 v[144:147], v[20:23], v[172:175], v[144:147]\n\t"
            "v_mfma_f32_16x16x32_bf16 v[168:171], v[64:67], v[192:195], v[168:171]\n\t"
            "v_mfma_f32_16x16x32_bf16 v[168:171], v[68:71], v[172:175], v[168:171]\n\t"
            "v_mfma_f32_16x16x32_bf16 v[164:167], v[112:115], v[192:195], v[164:167]\n\t"
            "v_mfma_f32_16x16x32_bf16 v[164:167], v[116:119], v[172:175], v[164:167]\n\t"
            "v_mfma_f32_16x16x32_bf16 v[148:151], v[44:47], v[172:175], v[148:151]\n\t"
            "v_mfma_f32_16x16x32_bf16 v[148:151], v[40:43], v[192:195], v[148:151]\n\t"
            "s_nop 1\n\t"
            "v_cvt_pk_bf16_f32 v144, v144, v145\n\t"
            "v_mfma_f32_16x16x32_bf16 v[156:159], v[92:95], v[172:175], v[156:159]\n\t"
            "v_cvt_pk_bf16_f32 v145, v146, v147\n\t"
            "v_mfma_f32_16x16x32_bf16 v[156:159], v[88:91], v[192:195], v[156:159]\n\t"
            "v_cvt_pk_bf16_f32 v168, v168, v169\n\t"
            "v_cvt_pk_bf16_f32 v169, v170, v171\n\t"
            "v_mfma_f32_16x16x32_bf16 v[180:183], v[140:143], v[172:175], v[180:183]\n\t"
            "ds_read_b128 v[172:175], v243 offset:12416\n\t"
            "v_cvt_pk_bf16_f32 v164, v164, v165\n\t"
            "v_mfma_f32_16x16x32_bf16 v[180:183], v[136:139], v[192:195], v[180:183]\n\t"
            "ds_read_b128 v[192:195], v242 offset:12416\n\t"
            "v_cvt_pk_bf16_f32 v165, v166, v167\n\t"
            "s_waitcnt lgkmcnt(2)\n\t"
            "v_mfma_f32_16x16x32_bf16 v[152:155], v[0:3], v[184:187], v[152:155]\n\t"
            "v_cvt_pk_bf16_f32 v146, v148, v149\n\t"
            "v_cvt_pk_bf16_f32 v147, v150, v151\n\t"
            "v_mfma_f32_16x16x32_bf16 v[152:155], v[4:7], v[188:191], v[152:155]\n\t"
            "v_cvt_pk_bf16_f32 v170, v156, v157\n\t"
            "v_cvt_pk_bf16_f32 v171, v158, v159\n\t"
            "v_mfma_f32_16x16x32_bf16 v[160:163], v[28:31], v[188:191], v[160:163]\n\t"
            "v_cvt_pk_bf16_f32 v166, v180, v181\n\t"
            "v_cvt_pk_bf16_f32 v167, v182, v183\n\t"
            "v_mfma_f32_16x16x32_bf16 v[160:163], v[24:27], v[184:187], v[160:163]\n\t"
            "v_lshlrev_b32 v148, 16, v230\n\t"
            "v_and_b32 v149, 0xffff0000, v230\n\t"
            "v_mfma_f32_16x16x32_bf16 v[176:179], v[48:51], v[184:187], v[176:179]\n\t"
            "v_lshlrev_b32 v150, 16, v231\n\t"
            "v_and_b32 v151, 0xffff0000, v231\n\t"
            "v_mfma_f32_16x16x32_bf16 v[176:179], v[52:55], v[188:191], v[176:179]\n\t"
            "v_lshlrev_b32 v156, 16, v232\n\t"
            "v_and_b32 v157, 0xffff0000, v232\n\t"
            "v_mfma_f32_16x16x32_bf16 v[148:151], v[76:79], v[188:191], v[148:151]\n\t"
            "v_lshlrev_b32 v158, 16, v233\n\t"
            "v_and_b32 v159, 0xffff0000, v233\n\t"
            "v_mfma_f32_16x16x32_bf16 v[148:151], v[72:75], v[184:187], v[148:151]\n\t"
            "v_lshlrev_b32 v180, 16, v234\n\t"
            "v_and_b32 v181, 0xffff0000, v234\n\t"
            "v_mfma_f32_16x16x32_bf16 v[156:159], v[96:99], v[184:187], v[156:159]\n\t"
            "v_lshlrev_b32 v182, 16, v235\n\t"
            "v_and_b32 v183, 0xffff0000, v235\n\t"
            "v_mfma_f32_16x16x32_bf16 v[156:159], v[100:103], v[188:191], v[156:159]\n\t"
            "buffer_store_dwordx4 v[144:147], v197, s[40:43], s45 offen sc1\n\t"
            "v_mfma_f32_16x16x32_bf16 v[180:183], v[124:127], v[188:191], v[180:183]\n\t"
            "ds_read_b128 v[188:191], v243 offset:12544\n\t"
            "v_mfma_f32_16x16x32_bf16 v[180:183], v[120:123], v[184:187], v[180:183]\n\t"
            "ds_read_b128 v[184:187], v242 offset:12544\n\t"
            "buffer_store_dwordx4 v[168:171], v197, s[40:43], s45 offen offset:64 sc1\n\t"
            "s_waitcnt lgkmcnt(2)\n\t"
            "v_mfma_f32_16x16x32_bf16 v[152:155], v[8:11], v[192:195], v[152:155]\n\t"
            "v_mfma_f32_16x16x32_bf16 v[152:155], v[12:15], v[172:175], v[152:155]\n\t"
            "buffer_store_dwordx4 v[164:167], v197, s[40:43], s45 offen offset:128 sc1\n\t"
            "v_mfma_f32_16x16x32_bf16 v[160:163], v[36:39], v[172:175], v[160:163]\n\t"
            "v_mfma_f32_16x16x32_bf16 v[160:163], v[32:35], v[192:195], v[160:163]\n\t"
            "v_mfma_f32_16x16x32_bf16 v[176:179], v[56:59], v[192:195], v[176:179]\n\t"
            "v_mfma_f32_16x16x32_bf16 v[176:179], v[60:63], v[172:175], v[176:179]\n\t"
            "v_mfma_f32_16x16x32_bf16 v[148:151], v[84:87], v[172:175], v[148:151]\n\t"
            "v_mfma_f32_16x16x32_bf16 v[148:151], v[80:83], v[192:195], v[148:151]\n\t"
            "v_mfma_f32_16x16x32_bf16 v[156:159], v[104:107], v[192:195], v[156:159]\n\t"
            "v_mfma_f32_16x16x32_bf16 v[156:159], v[108:111], v[172:175], v[156:159]\n\t"
            "s_waitcnt lgkmcnt(0)\n\t"
            "v_mfma_f32_16x16x32_bf16 v[152:155], v[16:19], v[184:187], v[152:155]\n\t"
            "v_mfma_f32_16x16x32_bf16 v[152:155], v[20:23], v[188:191], v[152:155]\n\t"
            "v_mfma_f32_16x16x32_bf16 v[160:163], v[44:47], v[188:191], v[160:163]\n\t"
            "v_mfma_f32_16x16x32_bf16 v[160:163], v[40:43], v[184:187], v[160:163]\n\t"
            "v_mfma_f32_16x16x32_bf16 v[180:183], v[132:135], v[172:175], v[180:183]\n\t"
            "v_mfma_f32_16x16x32_bf16 v[180:183], v[128:131], v[192:195], v[180:183]\n\t"
            "v_mfma_f32_16x16x32_bf16 v[176:179], v[64:67], v[184:187], v[176:179]\n\t"
            "v_mfma_f32_16x16x32_bf16 v[176:179], v[68:71], v[188:191], v[176:179]\n\t"
            "s_nop 1\n\t"
            "v_cvt_pk_bf16_f32 v152, v152, v153\n\t"
            "v_mfma_f32_16x16x32_bf16 v[148:151], v[92:95], v[188:191], v[148:151]\n\t"
            "v_cvt_pk_bf16_f32 v153, v154, v155\n\t"
            "v_mfma_f32_16x16x32_bf16 v[148:151], v[88:91], v[184:187], v[148:151]\n\t"
            "v_cvt_pk_bf16_f32 v154, v160, v161\n\t"
            "v_cvt_pk_bf16_f32 v155, v162, v163\n\t"
            "v_mfma_f32_16x16x32_bf16 v[156:159], v[112:115], v[184:187], v[156:159]\n\t"
            "v_cvt_pk_bf16_f32 v176, v176, v177\n\t"
            "v_cvt_pk_bf16_f32 v177, v178, v179\n\t"
            "v_mfma_f32_16x16x32_bf16 v[156:159], v[116:119], v[188:191], v[156:159]\n\t"
            "buffer_store_dwordx4 v[152:155], v198, s[40:43], s45 offen sc1\n\t"
            "v_mfma_f32_16x16x32_bf16 v[180:183], v[140:143], v[188:191], v[180:183]\n\t"
            "v_cvt_pk_bf16_f32 v178, v148, v149\n\t"
            "v_cvt_pk_bf16_f32 v179, v150, v151\n\t"
            "v_mfma_f32_16x16x32_bf16 v[180:183], v[136:139], v[184:187], v[180:183]\n\t"
            "buffer_store_dwordx4 v[176:179], v198, s[40:43], s45 offen offset:64 sc1\n\t"
            "s_nop 1\n\t"
            "v_cvt_pk_bf16_f32 v156, v156, v157\n\t"
            "v_cvt_pk_bf16_f32 v157, v158, v159\n\t"
            "s_nop 2\n\t"
            "v_cvt_pk_bf16_f32 v158, v180, v181\n\t"
            "v_cvt_pk_bf16_f32 v159, v182, v183\n\t"
            "buffer_store_dwordx4 v[156:159], v198, s[40:43], s45 offen offset:128 sc1\n\t"
            "s_nop 7\n\t"
            : B_SIO, "+{v247}"(l16_), "+{v228}"(q0), "+{v229}"(q1), "+{v232}"(q2), "+{v233}"(q3), "+{v234}"(q4)
            : "{v242}"(oE_), "{v243}"(oO_), "{v244}"(pR0_), "{v245}"(pR1_), "{v246}"(pR2_), "{v250}"(pXw_), "{v251}"(pSTw_), "{v252}"(aCA_), "{v253}"(aB_), "{v254}"(aA_), "{v255}"(idx_), B_SIN, "{s44}"(ARR_OFF + blockIdx.x * 8), "{s[80:81]}"(mrow)
            : "v144", "v145", "v146", "v147", "v148", "v149", "v150", "v151", "v152", "v153", "v154", "v155", "v156", "v157", "v158", "v159", "v160", "v161", "v162", "v163", "v164", "v165", "v166", "v167", "v168", "v169", "v170", "v171", "v172", "v173", "v174", "v175", "v176", "v177", "v178", "v179", "v180", "v181", "v182", "v183", "v184", "v185", "v186", "v187", "v188", "v189", "v190", "v191", "v192", "v193", "v194", "v195", "v196", "v197", "v198", "v199", "v236", "v237", "v248", "v249", "v0", "v1", "v2", "v3", "v4", "v5", "v6", "v7", "v8", "v9", "v10", "v11", "v12", "v13", "v14", "v15", "v16", "v17", "v18", "v19", "v20", "v21", "v22", "v23", "v24", "v25", "v26", "v27", "v28", "v29", "v30", "v31", "v32", "v33", "v34", "v35", "v36", "v37", "v38", "v39", "v40", "v41", "v42", "v43", "v44", "v45", "v46", "v47", "v48", "v49", "v50", "v51", "v52", "v53", "v54", "v55", "v56", "v57", "v58", "v59", "v60", "v61", "v62", "v63", "v64", "v65", "v66", "v67", "v68", "v69", "v70", "v71", "v72", "v73", "v74", "v75", "v76", "v77", "v78", "v79", "v80", "v81", "v82", "v83", "v84", "v85", "v86", "v87", "v88", "v89", "v90", "v91", "v92", "v93", "v94", "v95", "v96", "v97", "v98", "v99", "v100", "v101", "v102", "v103", "v104", "v105", "v106", "v107", "v108", "v109", "v110", "v111", "v112", "v113", "v114", "v115", "v116", "v117", "v118", "v119", "v120", "v121", "v122", "v123", "v124", "v125", "v126", "v127", "v128", "v129", "v130", "v131", "v132", "v133", "v134", "v135", "v136", "v137", "v138", "v139", "v140", "v141", "v142", "v143", "v200", "v201", "v202", "v203", "v204", "v205", "v206", "v207", "v208", "v209", "v210", "v211", "v212", "v213", "v214", "v215", "v216", "v217", "v218", "v219", "v220", "v221", "v222", "v223", "v224", "v225", "v226", "v227", "v230", "v231", "v235", B_SCLOB);
        }
        __builtin_amdgcn_endpgm();
    }
}

extern "C" void kernel_launch(void* const* d_in, const int* in_sizes, int n_in,
                              void* d_out, int out_size, void* d_ws, size_t ws_size,
                              hipStream_t stream) {
    const unsigned short* x = (const unsigned short*)d_in[0];
    const unsigned short* dw = (const unsigned short*)d_in[1];
    const unsigned short* wqkv = (const unsigned short*)d_in[2];
    const unsigned short* wo = (const unsigned short*)d_in[3];
    unsigned short* out = (unsigned short*)d_out;
    u32x4* wf = (u32x4*)d_ws;
    win_attn_kernel<<<GRID, 512, 0, stream>>>(x, dw, wqkv, wo, wf, out);
}
```

```cpp
#include <hip/hip_runtime.h>
#include <hip/hip_bf16.h>
#include <stdint.h>

typedef short bf16x8 __attribute__((ext_vector_type(8)));
typedef float f32x4 __attribute__((ext_vector_type(4)));
typedef float f32x2 __attribute__((ext_vector_type(2)));
typedef unsigned u32x2 __attribute__((ext_vector_type(2)));
typedef unsigned u32x4 __attribute__((ext_vector_type(4)));

constexpr int IMG = 511;
constexpr int CH = 192;
constexpr int NWX = 73;
constexpr int NWIN = NWX * NWX;
constexpr int GRID = 256;
constexpr int ROWBYTES = IMG * CH * 2;
constexpr int XBIAS = (IMG + 1) * CH * 2;
constexpr unsigned OOB = 0x40000000u;

constexpr int FLAG_OFF = 7856;
constexpr int X_OFF = 32768;
constexpr int X1_OFF = 53632;
constexpr int O_OFF = 74496;
constexpr int V_OFF = 92928;
constexpr int KQL_OFF = 111360;
constexpr int OL_OFF = 129024;
constexpr int AT_OFF = 137472;
constexpr int CT_OFF = AT_OFF + 19584;
constexpr int RS_OFF = CT_OFF + 4096;
constexpr int RSX_OFF = 162816;
constexpr int WL_OFF = RS_OFF + 2304 + 64;
constexpr int LDS_BYTES = WL_OFF + 128;
#define RS_BASE (RS_OFF + wave * 384)
constexpr int STL_OFF = O_OFF + 12288;
constexpr int DW_OFF = O_OFF + 8448;
#define LX_OFF(i) ((i) < 2 ? CT_OFF + 1024 + (i) * 384 : KQL_OFF + ((i) - 2) * 800)
constexpr int PF_OFF = CT_OFF + 2048;
constexpr int PSLEEP = 4;
#define LB_OFF (w < 3 ? V_OFF + w * 13952 : X1_OFF)
#define LPOS(wc) (((wc) & 1) ? 0 : 6)
constexpr unsigned CROW = 0x060a0230u;
constexpr unsigned CLANE = 0x00132C3Fu;

constexpr unsigned long long TOKMAP[6] = {0x20001c190f2a2202ull, 0x052517121f061a13ull, 0x181e08151d0b260cull, 0x1b2b2903102d0901ull, 0x04162e0e2824142cull, 0x110a072127232f0dull};
constexpr unsigned long long ROWMAP[8] = {0x08321b240f1c0006ull, 0x23310e2a09170d14ull, 0x210c2f030a273418ull, 0x1d2b11221f043739ull, 0x3234363a103e192dull, 0x291a20022c3f1605ull, 0x303a3c2628122e15ull, 0x39253d1e01070b13ull};

constexpr int WF_BYTES = 8 * 36 * 1024;
constexpr int FLG_OFF = WF_BYTES;
constexpr int ARR_OFF = FLG_OFF + 4096;
constexpr int EPO_OFF = ARR_OFF + 2048;
constexpr int CTR_OFF = EPO_OFF + 128;
constexpr int REC_OFF = WF_BYTES + 8192;
constexpr int WS_BYTES = REC_OFF + 209 * 1280;
constexpr int SC1 = 16;

#define MFMA32(a, b, c) __builtin_amdgcn_mfma_f32_16x16x32_bf16((a), (b), (c), 0, 0, 0)
#define BC8(v) __builtin_bit_cast(bf16x8, (v))
#define BAR() asm volatile("s_waitcnt lgkmcnt(0)\n\ts_barrier" ::: "memory")
#define SB() __builtin_amdgcn_sched_barrier(0)
#define OPQ(x) asm volatile("" : "+v"(x))
#define ZERO4 ((f32x4){0.f, 0.f, 0.f, 0.f})

typedef __bf16 nbf16x2 __attribute__((ext_vector_type(2)));
__device__ __forceinline__ unsigned pk2(float a, float b) {
    f32x2 v = {a, b};
    nbf16x2 r = __builtin_convertvector(v, nbf16x2);
    return __builtin_bit_cast(unsigned, r);
}
__device__ __forceinline__ float bflo(unsigned u) { return __uint_as_float(u << 16); }
__device__ __forceinline__ float bfhi(unsigned u) { return __uint_as_float(u & 0xffff0000u); }

__device__ __forceinline__ float bf2f(unsigned short v) { return __uint_as_float((unsigned)v << 16); }
__device__ __forceinline__ unsigned byte_of(unsigned long long a, unsigned long long b, int i) {
    return (unsigned)(((i & 8) ? b : a) >> (8 * (i & 7))) & 0xFFu;
}
#define M24(a, b) ((unsigned)__mul24((int)(a), (int)(b)))
__device__ __forceinline__ void seq_to_win(int seq, int& wy, int& wx) {
    const bool last = seq >= 5110;
    const int s = last ? 35 : M24(seq, 28729) >> 22, r = seq - M24(146, s);
    const int kx = last ? M24(r, 171) >> 10 : r >> 2, pos = r - M24(last ? 6 : 4, kx);
    const bool low = last && pos >= (kx == 36 ? 2 : 4);
    wy = low ? 72 : 2 * s + (pos & 1);
    wx = 2 * kx + (low ? (kx == 36 ? 0 : pos - 4) : pos >> 1);
}
__device__ __forceinline__ int win_to_seq(int wy, int wx) {
    const int kx = wx >> 1;
    return M24(146, wy == 72 ? 35 : wy >> 1) + M24(wy >= 70 ? 6 : 4, kx) + (wy == 72 ? (kx == 36 ? 2 : 4 + (wx & 1)) : 2 * (wx & 1) + (wy & 1));
}
__device__ __forceinline__ int block_of(int wy, int wx) { return wy >= 70 ? 1295 + (wx & ~1) + (wy == 72) : M24(37, wy >> 1) + (wx >> 1); }
__device__ __forceinline__ void block_pos(int b, int& ky, int& kx) {
    const int s = M24(b, 1772) >> 16, l = b - 1295;
    kx = l >= 0 ? l >> 1 : b - M24(37, s);
    ky = l >= 0 ? 70 + 2 * (l & 1) : 2 * s;
}
__device__ __forceinline__ unsigned win_org(int wy, int wx) { return M24(wy, 7 * ROWBYTES) + M24(wx, 7 * CH * 2); }
__device__ __forceinline__ unsigned wl_entry(int wy, int wx) {
    return win_org(wy, wx) | (wy == 0 ? 0x08000000u : 0u) | (wx == 0 ? 0x10000000u : 0u) |
           (wx == NWX - 1 ? 0x20000000u : 0u) | ((unsigned)(wx & 1) << 30) | ((unsigned)(wy & 1) << 31);
}
__device__ __forceinline__ unsigned dma_off(int lane) {
    const int col = (lane * 43) >> 8;
    return (unsigned)(col * (CH * 2 - 96) + 16 * lane) | (col == 0 ? 0x10000000u : 0u) | (col == 8 ? 0x20000000u : 0u);
}

#if defined(__HIP_DEVICE_COMPILE__)
#define NOPK __attribute__((target("no-packed-fp32-ops")))
#else
#define NOPK
#endif
__global__ __launch_bounds__(512, 2) NOPK void win_attn_kernel(const unsigned short* __restrict__ x,
                                                          const unsigned short* __restrict__ dw, const unsigned short* __restrict__ wqkv,
                                                          const unsigned short* __restrict__ wo, u32x4* wf,
                                                          unsigned short* __restrict__ out) {
    __shared__ __attribute__((aligned(16))) char smem[LDS_BYTES];

    asm volatile("wk_code_begin:");
    const int tid = threadIdx.x;
    const int wave = __builtin_amdgcn_readfirstlane(tid >> 6);
    const int w = wave & 3;
    const unsigned ldsb = (unsigned)(size_t)smem;
    {
        unsigned long long pc;
        unsigned len, rel;
        asm volatile("s_getpc_b64 %0\n.Lwk_here%=:\n\ts_mov_b32 %1, wk_code_end-wk_code_begin\n\ts_mov_b32 %2, .Lwk_here%=-wk_code_begin" : "=s"(pc), "=s"(len), "=s"(rel));
        pc -= rel;
        const u32x4 cr = {(unsigned)pc, (unsigned)(pc >> 32) & 0xffffu, len, 0x00020000u};
        asm volatile("s_mov_b32 m0, %0\n\ts_nop 0\n\tbuffer_load_dwordx4 %1, %2, %3 offen lds"
                     :: "s"(ldsb + X1_OFF + 14336 + w * 1024), "v"((tid & 63) * 16), "s"(cr), "s"((wave * 32 + (blockIdx.x >> 3)) * 1024) : "m0", "memory");
    }
    *(unsigned*)(smem + PF_OFF + tid * 4) = 0x9E3779B9u * (tid + 1);
    *(unsigned*)(smem + (tid < 192 ? AT_OFF + 18816 + tid * 4 : FLAG_OFF + (tid & 3) * 4)) = 0u;
    BAR();
    const unsigned flg = FLG_OFF + (blockIdx.x & 7) * 512;
    asm volatile("" :: "s"(x), "s"(dw), "s"(wqkv), "s"(wo), "s"(wf));
    const auto wrsrc = __builtin_amdgcn_make_buffer_rsrc((void*)wf, 0, WS_BYTES, 0x00020000);
    unsigned tag_lo = 0, tag_hi = 0;
#define TAG_READY                                                                                  \
    {                                                                                              \
        unsigned long long tag_;                                                                   \
        asm volatile("s_load_dwordx2 %0, %1, %2\n\ts_waitcnt lgkmcnt(0)" : "=s"(tag_) : "s"(wf), "n"(EPO_OFF) : "memory"); \
        tag_lo = (unsigned)tag_ + 1u; tag_hi = (unsigned)(tag_ >> 32);                             \
    }
    const int pb = blockIdx.x;
    if (pb < 36) {
        const int lane = tid & 63, n = lane & 15, g = lane >> 4;
        const bool isO = wave >= 6;
        const int fr = isO ? 216 + 2 * pb + (wave - 6) : 6 * pb + wave;
        const int tile = fr / 6, ks = fr - 6 * tile, tw = tile / 6, mdt = tile - 6 * tw;
        const int col = isO ? 96 * (tw - 6) + 32 * (mdt >> 1) + 8 * (n >> 2) + 4 * (mdt & 1) + (n & 3)
                            : (tw < 4 ? (mdt < 3 ? 192 : -48) + 48 * tw : 96 * tw) + 16 * mdt + n;
        const int rb = isO ? 384 : 1152;
        const auto rr = __builtin_amdgcn_make_buffer_rsrc((void*)(isO ? wo : wqkv), 0, 192 * rb, 0x00020000);
        const int vo = (48 * (g >> 1) + 8 * (g & 1)) * rb + 2 * col;
        float v[8];
#pragma unroll
        for (int j = 0; j < 8; ++j) v[j] = bf2f(__builtin_amdgcn_raw_buffer_load_b16(rr, vo, (96 * (ks & 1) + 16 * (ks >> 1) + j) * rb, 0));
        TAG_READY
        float* red = (float*)smem;
        red[n * 36 + wave * 4 + g] = ((v[0] + v[1]) + (v[2] + v[3])) + ((v[4] + v[5]) + (v[6] + v[7]));
        BAR();
        float mean = 0.f;
#pragma unroll
        for (int i = 0; i < 6; ++i) {
            const f32x4 p = *(const f32x4*)(red + n * 36 + 4 * i);
            mean += (p[0] + p[1]) + (p[2] + p[3]);
        }
        mean = isO ? 0.f : mean * (1.f / 192.f);
        u32x4 o;
#pragma unroll
        for (int j = 0; j < 4; ++j) o[j] = pk2(v[2 * j] - mean, v[2 * j + 1] - mean);
        __builtin_amdgcn_raw_buffer_store_b128(o, wrsrc, lane * 16, fr * 1024, SC1);
        if (pb == 0 && tid == 448) __builtin_amdgcn_raw_buffer_store_b32(0u, wrsrc, CTR_OFF, 0, SC1);
        asm volatile("s_waitcnt vmcnt(0)" ::: "memory");
        BAR();
        if (tid < 8) __builtin_amdgcn_raw_buffer_store_b64((u32x2){tag_lo, tag_hi}, wrsrc, FLG_OFF + tid * 512 + pb * 8, 0, SC1);
    }
    const int xi = blockIdx.x >> 3, nshort = (int)(0x0506060606060606ull >> (8 * (blockIdx.x & 7))) & 63;
    const int start = 672 * (blockIdx.x & 7) - ((int)(0x2a241e18120c0600ull >> (8 * (blockIdx.x & 7))) & 63) + 21 * xi - (xi < nshort ? xi : nshort);
    const int cnt = xi < nshort ? 20 : 21;

    const auto xrsrc = __builtin_amdgcn_make_buffer_rsrc((void*)((const char*)x - XBIAS), 0, IMG * IMG * CH * 2 + XBIAS, 0x00020000);
#define POLL_ISSUE                                                                                  \
    asm volatile("s_mov_b32 m0, %0\n\ts_mov_b64 exec, 0x3ffff\n\tbuffer_load_dwordx4 %1, %2, %3 offen sc1 lds\n\ts_mov_b64 exec, -1" \
                 :: "s"(ldsb + PF_OFF), "v"(lane * 16), "s"(wrsrc), "s"(flg) : "m0", "memory");
#define POLL_W                                                                                      \
    {                                                                                               \
        const int fi_ = lane < 36 ? lane : 0;                                                       \
        for (int it_ = 0; it_ < 100000; ++it_) {                                                    \
            u32x2 f_;                                                                               \
            if (it_ < 48) asm volatile("ds_read_b64 %0, %1\n\ts_waitcnt lgkmcnt(0)" : "=v"(f_) : "v"(ldsb + PF_OFF + fi_ * 8) : "memory"); \
            else f_ = __builtin_amdgcn_raw_buffer_load_b64(wrsrc, fi_ * 8, flg, SC1);        \
            if (__builtin_amdgcn_ballot_w64(f_[0] != tag_lo || f_[1] != tag_hi) == 0ull) break;     \
            if (it_ < 47 && wave == 0) POLL_ISSUE                                                              \
            __builtin_amdgcn_s_sleep(PSLEEP);                                                       \
        }                                                                                           \
        *(u32x2*)(smem + PF_OFF + 512) = (u32x2){tag_lo, tag_hi};                                   \
        __builtin_amdgcn_fence(__ATOMIC_ACQUIRE, "wavefront");                                      \
        SB();                                                                                       \
    }
#define LANE_SETUP                                                                                 \
    int lane;                                                                                      \
    asm volatile("v_mbcnt_lo_u32_b32 %0, -1, 0\n\tv_mbcnt_hi_u32_b32 %0, -1, %0" : "=v"(lane));    \
    __builtin_assume(lane >= 0 && lane < 64);                                                      \
    const int g = lane >> 4, l15 = lane & 15, sw = l15 >> 1;                                       \
    int fE = X_OFF + l15 * 384 + ((g ^ sw) << 4);                                                  \
    OPQ(fE);

#define DMA_SLICE(en)                                                                              \
        {                                                                                          \
            const unsigned vo_ = dma_off(lane) & (0x401fffffu | ((en) & 0x38000000u));             \
            const unsigned so_ = ((en) & 0x7ffffffu) + 96 * w;                                     \
            _Pragma("unroll") for (int r = 0; r < 9; ++r) {                                        \
                unsigned m0_ = ldsb + w * 8192 + r * 872;                                          \
                unsigned sr_ = (r == 0 && ((en) & 0x08000000u)) ? OOB : so_ + r * ROWBYTES;        \
                asm volatile("s_mov_b32 m0, %0\n\ts_mov_b32 exec_hi, 0x3fffff\n\tbuffer_load_dwordx4 %1, %2, %3 offen lds\n\ts_mov_b32 exec_hi, -1" \
                             :: "s"(m0_), "v"(vo_), "s"(xrsrc), "s"(sr_) : "m0", "memory");        \
            }                                                                                      \
        }
#define XPAT(r) (((r) < 48 ? (r) : 48) * 384 + (((2 * w) ^ (((r) >> 1) & 7)) << 4))
#define SPAT(r) ((w >> 1) * 1024 + (w & 1) * 8 + (r) * 16)
#define CONV_CONSTS(var) CONV_ROWS(var) CONV_OPS
#define CONV_ROWS(var) \
            const unsigned long long rlo = g == 0 ? ROWMAP[0] : (g == 1 ? ROWMAP[2] : (g == 2 ? ROWMAP[4] : ROWMAP[6])); \
            const unsigned long long rhi = g == 0 ? ROWMAP[1] : (g == 1 ? ROWMAP[3] : (g == 2 ? ROWMAP[5] : ROWMAP[7])); \
            int xrow = byte_of(rlo, rhi, l15); \
            if (lane == (int)((CLANE >> (8 * (var))) & 0xFF)) xrow = 48; \
            if (lane == 63) xrow = (CROW >> (8 * (var))) & 0xFF; \
            const unsigned pXw_ = XPAT(xrow); \
            const unsigned pSTw_ = (w >> 1) * 1024 + (w & 1) * 8 + xrow * 16; \
            const int oy = (0x5320 >> (4 * (l15 >> 2))) & 15, ox = (0x5320 >> (4 * (l15 & 3))) & 15; \
            const unsigned aB_ = ldsb + w * 8192 + (oy + (g >> 1)) * 872 + (ox + 2 * (g & 1)) * 96;
#define CONV_OPS \
            const int dyi = g - (l15 >> 3); \
            const unsigned aA_ = ldsb + AT_OFF + (l15 & 3) * 16 + \
                                 ((dyi < 0 || dyi > 2) ? 18816 : (dyi == 0 ? 0 : (dyi == 1 ? 6208 : 12544)) + ((l15 >> 2) & 1) * 3072 + w * 768); \
            const unsigned idx_ = (l15 & 3) * 0x5555u; \
            const unsigned se = (l15 == 4 * g) ? 0x3F803F80u : 0u; \
            const u32x4 sel4 = {se, se, se, se};

    unsigned en0, en1;
    {
        LANE_SETUP
        const int px = (lane * 43) >> 8;

        int wy, wx;
        seq_to_win(start + (lane < cnt ? lane : cnt - 1), wy, wx);
        const unsigned en = wl_entry(wy, wx);
        en0 = __builtin_amdgcn_readlane(en, 0);
        en1 = __builtin_amdgcn_readlane(en, 1);
        unsigned cB, cX, cS;
        if (wave >= 4) {
            const int bk = block_of(wy, wx);
            const int key0 = __builtin_amdgcn_readlane(bk, 0), nblk = __builtin_amdgcn_readlane(bk, 63) - key0 + 1;
            const int b = l15 < nblk ? l15 : 0;
            int ky, kx;
            block_pos(key0 + b, ky, kx);
            const unsigned org = l15 < nblk ? (win_org(ky, 2 * kx) + (5 * ROWBYTES + 5 * CH * 2 + XBIAS)) | (kx == 36 ? OOB : 0u) : OOB + 0x8000000u;
            const unsigned lo = (px & 3) * (CH * 2) + (lane - 6 * px) * 16, keep = (px & 2) ? ~0u : ~OOB;
#pragma unroll
            for (int p = 0; p < 4; ++p) {
                if (p < 3 || nblk > 6) {
                    const unsigned oa = __builtin_amdgcn_readlane(org, 2 * p), ob = __builtin_amdgcn_readlane(org, 2 * p + 1);
                    const unsigned vo_ = ((lane < 24 ? oa : ob) & keep) + lo;
#pragma unroll
                    for (int r = 0; r < 4; ++r) {
                        unsigned m0_ = ldsb + LB_OFF + (4 * p + r) * 872;
                        asm volatile("s_mov_b32 m0, %0\n\ts_mov_b32 exec_hi, 0xffff\n\tbuffer_load_dwordx4 %1, %2, %3 offen lds\n\ts_mov_b32 exec_hi, -1"
                                     :: "s"(m0_), "v"(vo_), "s"(xrsrc), "s"(96 * w + r * ROWBYTES) : "m0", "memory");
                    }
                }
            }
            SB();
            const int ly = ky + (g >> 1), lx = 2 * kx + (g & 1), li = win_to_seq(ly, lx) - start;
            const bool ok = ly < NWX && lx < NWX && li >= 0 && li < cnt && l15 < nblk;
            const int row = ok ? li : 47;
            cB = ldsb + LB_OFF + (4 * (b >> 1) + (g >> 1)) * 872 + (b & 1) * 384 + (g & 1) * 192;
            cX = XPAT(row);
            cS = SPAT(row) + (unsigned)(STL_OFF - O_OFF - 18816);
            for (int it_ = 0; it_ < 100000; ++it_) {
                unsigned n_;
                asm volatile("ds_read_b32 %0, %1\n\ts_waitcnt lgkmcnt(0)" : "=v"(n_) : "v"(ldsb + FLAG_OFF + 8) : "memory");
                if (__builtin_amdgcn_readfirstlane(n_) >= 4) break;
                __builtin_amdgcn_s_sleep(1);
            }
        } else {
            {
                const auto dr = __builtin_amdgcn_make_buffer_rsrc((void*)dw, 0, 9 * 384, 0x00020000);
                unsigned m0_ = ldsb + DW_OFF + w * 864, vo_ = px * 288 + lane * 16;
                asm volatile("s_mov_b32 m0, %0\n\ts_mov_b32 exec_hi, 0x3fffff\n\tbuffer_load_dwordx4 %1, %2, %3 offen lds\n\ts_mov_b32 exec_hi, -1"
                             :: "s"(m0_), "v"(vo_), "s"(dr), "s"(96 * w) : "m0", "memory");
            }
            DMA_SLICE(en0)
            SB();
            if (wave == 2) {
                u32x4 so;
#pragma unroll
                for (int e = 0; e < 3; ++e) {
                    const int tk = byte_of(TOKMAP[2 * e], TOKMAP[2 * e + 1], l15);
                    const int ty = (tk * 37) >> 8, tx = tk - 7 * ty;
                    so[e] = (unsigned)(ty * ROWBYTES + tx * (CH * 2)) + 16 * g;
                }
                so[3] = dma_off(lane);
                *(u32x4*)(smem + CT_OFF + lane * 16) = so;
            }
            if (wave == 1 && lane < 32) *(unsigned*)(smem + WL_OFF + lane * 4) = lane < 20 ? en : 0x7ffffffu;
            if (wave == 1 && lane == 20) *(unsigned*)(smem + PF_OFF + 520) = en;
            CONV_ROWS(en0 >> 30)
            cB = aB_;
            cX = lane == (int)((CLANE >> (8 * (en0 >> 30))) & 0xFF) ? LX_OFF(0) - X_OFF + 32 * w : pXw_;
            cS = pSTw_;
            TAG_READY
            asm volatile("s_waitcnt vmcnt(9)" ::: "memory");
            if (lane < 48) {
                unsigned short dv[9];
#pragma unroll
                for (int i = 0; i < 9; ++i) dv[i] = *(const unsigned short*)(smem + DW_OFF + w * 864 + i * 96 + lane * 2);
#pragma unroll
                for (int dy = 0; dy < 3; ++dy) {
                    const unsigned a = dv[3 * dy], b2 = dv[3 * dy + 1] | (dy == 1 ? 0x3F800000u : 0u), c = dv[3 * dy + 2];
                    char* p = smem + AT_OFF + (dy == 0 ? 0 : (dy == 1 ? 6208 : 12544)) + w * 768 + lane * 16;
                    *(u32x4*)p = (u32x4){a, b2, c, 0u};
                    *(u32x4*)(p + 3072) = (u32x4){0u, a, b2, c};
                }
            }
            asm volatile("s_mov_b64 exec, 1\n\tds_add_u32 %0, %1\n\ts_mov_b64 exec, -1" :: "v"(ldsb + FLAG_OFF + 8), "v"(1) : "memory");
        }
        CONV_OPS
        asm volatile("s_waitcnt vmcnt(0) lgkmcnt(0)" ::: "memory");
        if (wave == 0) POLL_ISSUE
        asm volatile(
            "s_mov_b32 m0, s73\n\t"
            "s_cmp_eq_u32 s73, 0\n\t"
            "s_nop 7\n\t"
            "ds_read_b128 v[168:171], v254\n\t"
            "ds_read_b64 v[144:145], v253\n\t"
            "ds_read_b64 v[146:147], v253 offset:96\n\t"
            "ds_read_b64 v[148:149], v253 offset:1744\n\t"
            "ds_read_b64 v[150:151], v253 offset:1840\n\t"
            "ds_read_b128 v[172:175], v254 offset:64\n\t"
            "ds_read_b64 v[152:153], v253 offset:8\n\t"
            "ds_read_b64 v[154:155], v253 offset:104\n\t"
            "ds_read_b64 v[156:157], v253 offset:1752\n\t"
            "ds_read_b64 v[158:159], v253 offset:1848\n\t"
            "ds_read_b64 v[160:161], v253 offset:16\n\t"
            "ds_read_b64 v[162:163], v253 offset:112\n\t"
            "ds_read_b64 v[164:165], v253 offset:1760\n\t"
            "ds_read_b64 v[166:167], v253 offset:1856\n\t"
            "v_mov_b64 v[176:177], 0\n\t"
            "v_mov_b64 v[178:179], 0\n\t"
            "v_mov_b64 v[180:181], 0\n\t"
            "v_mov_b64 v[182:183], 0\n\t"
            "v_mov_b64 v[184:185], 0\n\t"
            "v_mov_b64 v[186:187], 0\n\t"
            "v_mov_b64 v[188:189], 0\n\t"
            "v_mov_b64 v[190:191], 0\n\t"
            "v_add_u32 v236, s67, v250\n\t"
            "v_xor_b32 v237, 16, v236\n\t"
            "v_add_u32 v248, s67, v251\n\t"
            "s_waitcnt lgkmcnt(9)\n\t"
            "v_smfmac_f32_16x16x64_bf16 v[176:179], v[168:171], v[144:151], v255\n\t"
            "ds_read_b128 v[168:171], v254 offset:128\n\t"
            "ds_read_b64 v[144:145], v253 offset:24\n\t"
            "s_waitcnt lgkmcnt(6)\n\t"
            "v_smfmac_f32_16x16x64_bf16 v[180:183], v[172:175], v[152:159], v255\n\t"
            "ds_read_b64 v[146:147], v253 offset:120\n\t"
            "ds_read_b64 v[148:149], v253 offset:1768\n\t"
            "ds_read_b64 v[150:151], v253 offset:1864\n\t"
            "ds_read_b128 v[172:175], v254 offset:192\n\t"
            "ds_read_b64 v[152:153], v253 offset:32\n\t"
            "ds_read_b64 v[154:155], v253 offset:128\n\t"
            "ds_read_b64 v[156:157], v253 offset:1776\n\t"
            "ds_read_b64 v[158:159], v253 offset:1872\n\t"
            "v_cvt_pk_bf16_f32 v176, v176, v177\n\t"
            "v_cvt_pk_bf16_f32 v177, v178, v179\n\t"
            "v_cvt_pk_bf16_f32 v178, v180, v181\n\t"
            "v_cvt_pk_bf16_f32 v179, v182, v183\n\t"
            "v_mfma_f32_4x4x4_16b_bf16 v[196:199], v[176:177], v[176:177], 0\n\t"
            "ds_write_b128 v236, v[176:179]\n\t"
            "v_mfma_f32_16x16x32_bf16 v[192:195], v[238:241], v[176:179], 0\n\t"
            "v_mov_b64 v[176:177], 0\n\t"
            "v_mov_b64 v[180:181], 0\n\t"
            "v_mfma_f32_4x4x4_16b_bf16 v[196:199], v[178:179], v[178:179], v[196:199]\n\t"
            "v_mov_b64 v[178:179], 0\n\t"
            "v_mov_b64 v[182:183], 0\n\t"
            "s_waitcnt lgkmcnt(10)\n\t"
            "v_smfmac_f32_16x16x64_bf16 v[184:187], v[168:171], v[160:167], v255\n\t"
            "ds_read_b128 v[168:171], v254 offset:256\n\t"
            "ds_read_b64 v[160:161], v253 offset:40\n\t"
            "ds_read_b64 v[162:163], v253 offset:136\n\t"
            "ds_read_b64 v[164:165], v253 offset:1784\n\t"
            "ds_read_b64 v[166:167], v253 offset:1880\n\t"
            "s_waitcnt lgkmcnt(10)\n\t"
            "v_smfmac_f32_16x16x64_bf16 v[188:191], v[172:175], v[144:151], v255\n\t"
            "ds_read_b128 v[172:175], v254 offset:320\n\t"
            "ds_read_b64 v[144:145], v253 offset:48\n\t"
            "ds_read_b64 v[146:147], v253 offset:144\n\t"
            "s_cbranch_scc1 .Lwk_np1%=\n\t"
            "s_mov_b64 exec, 0x3ffff\n\t"
            "buffer_load_dwordx4 v252, s[68:71], s72 offen sc1 lds\n\t"
            "s_mov_b64 exec, -1\n\t"
            ".Lwk_np1%=:\n\t"
            "ds_read_b64 v[148:149], v253 offset:1792\n\t"
            "ds_read_b64 v[150:151], v253 offset:1888\n\t"
            "v_cvt_pk_bf16_f32 v184, v184, v185\n\t"
            "v_cvt_pk_bf16_f32 v185, v186, v187\n\t"
            "s_nop 0\n\t"
            "v_cvt_pk_bf16_f32 v186, v188, v189\n\t"
            "v_cvt_pk_bf16_f32 v187, v190, v191\n\t"
            "v_mfma_f32_4x4x4_16b_bf16 v[196:199], v[184:185], v[184:185], v[196:199]\n\t"
            "s_waitcnt lgkmcnt(14)\n\t"
            "ds_write_b128 v237, v[184:187]\n\t"
            "v_mfma_f32_16x16x32_bf16 v[192:195], v[238:241], v[184:187], v[192:195]\n\t"
            "v_mov_b64 v[184:185], 0\n\t"
            "v_mov_b64 v[188:189], 0\n\t"
            "v_mfma_f32_4x4x4_16b_bf16 v[196:199], v[186:187], v[186:187], v[196:199]\n\t"
            "v_mov_b64 v[186:187], 0\n\t"
            "v_mov_b64 v[190:191], 0\n\t"
            "s_waitcnt lgkmcnt(10)\n\t"
            "v_smfmac_f32_16x16x64_bf16 v[176:179], v[168:171], v[152:159], v255\n\t"
            "ds_read_b128 v[168:171], v254 offset:384\n\t"
            "ds_read_b64 v[152:153], v253 offset:56\n\t"
            "ds_read_b64 v[154:155], v253 offset:152\n\t"
            "ds_read_b64 v[156:157], v253 offset:1800\n\t"
            "ds_read_b64 v[158:159], v253 offset:1896\n\t"
            "s_waitcnt lgkmcnt(10)\n\t"
            "v_smfmac_f32_16x16x64_bf16 v[180:183], v[172:175], v[160:167], v255\n\t"
            "ds_read_b128 v[172:175], v254 offset:448\n\t"
            "ds_read_b64 v[160:161], v253 offset:64\n\t"
            "ds_read_b64 v[162:163], v253 offset:160\n\t"
            "ds_read_b64 v[164:165], v253 offset:1808\n\t"
            "ds_read_b64 v[166:167], v253 offset:1904\n\t"
            "v_cvt_pk_bf16_f32 v176, v176, v177\n\t"
            "v_cvt_pk_bf16_f32 v177, v178, v179\n\t"
            "s_nop 0\n\t"
            "v_cvt_pk_bf16_f32 v178, v180, v181\n\t"
            "v_cvt_pk_bf16_f32 v179, v182, v183\n\t"
            "v_mfma_f32_4x4x4_16b_bf16 v[196:199], v[176:177], v[176:177], v[196:199]\n\t"
            "s_waitcnt lgkmcnt(14)\n\t"
            "ds_write_b128 v236, v[176:179] offset:128\n\t"
            "v_mfma_f32_16x16x32_bf16 v[192:195], v[238:241], v[176:179], v[192:195]\n\t"
            "v_mov_b64 v[176:177], 0\n\t"
            "v_mov_b64 v[180:181], 0\n\t"
            "v_mfma_f32_4x4x4_16b_bf16 v[196:199], v[178:179], v[178:179], v[196:199]\n\t"
            "v_mov_b64 v[178:179], 0\n\t"
            "v_mov_b64 v[182:183], 0\n\t"
            "s_waitcnt lgkmcnt(10)\n\t"
            "v_smfmac_f32_16x16x64_bf16 v[184:187], v[168:171], v[144:151], v255\n\t"
            "ds_read_b128 v[168:171], v254 offset:512\n\t"
            "ds_read_b64 v[144:145], v253 offset:72\n\t"
            "ds_read_b64 v[146:147], v253 offset:168\n\t"
            "ds_read_b64 v[148:149], v253 offset:1816\n\t"
            "ds_read_b64 v[150:151], v253 offset:1912\n\t"
            "s_waitcnt lgkmcnt(10)\n\t"
            "v_smfmac_f32_16x16x64_bf16 v[188:191], v[172:175], v[152:159], v255\n\t"
            "ds_read_b128 v[172:175], v254 offset:576\n\t"
            "ds_read_b64 v[152:153], v253 offset:80\n\t"
            "ds_read_b64 v[154:155], v253 offset:176\n\t"
            "ds_read_b64 v[156:157], v253 offset:1824\n\t"
            "ds_read_b64 v[158:159], v253 offset:1920\n\t"
            "v_cvt_pk_bf16_f32 v184, v184, v185\n\t"
            "v_cvt_pk_bf16_f32 v185, v186, v187\n\t"
            "s_cbranch_scc1 .Lwk_np0%=\n\t"
            "s_mov_b64 exec, 0x3ffff\n\t"
            "buffer_load_dwordx4 v252, s[68:71], s72 offen sc1 lds\n\t"
            "s_mov_b64 exec, -1\n\t"
            ".Lwk_np0%=:\n\t"
            "s_nop 0\n\t"
            "v_cvt_pk_bf16_f32 v186, v188, v189\n\t"
            "v_cvt_pk_bf16_f32 v187, v190, v191\n\t"
            "v_mfma_f32_4x4x4_16b_bf16 v[196:199], v[184:185], v[184:185], v[196:199]\n\t"
            "s_waitcnt lgkmcnt(14)\n\t"
            "ds_write_b128 v237, v[184:187] offset:128\n\t"
            "v_mfma_f32_16x16x32_bf16 v[192:195], v[238:241], v[184:187], v[192:195]\n\t"
            "v_mov_b64 v[184:185], 0\n\t"
            "v_mov_b64 v[188:189], 0\n\t"
            "v_mfma_f32_4x4x4_16b_bf16 v[196:199], v[186:187], v[186:187], v[196:199]\n\t"
            "v_mov_b64 v[186:187], 0\n\t"
            "v_mov_b64 v[190:191], 0\n\t"
            "s_waitcnt lgkmcnt(10)\n\t"
            "v_smfmac_f32_16x16x64_bf16 v[176:179], v[168:171], v[160:167], v255\n\t"
            "ds_read_b128 v[168:171], v254 offset:640\n\t"
            "ds_read_b64 v[160:161], v253 offset:88\n\t"
            "ds_read_b64 v[162:163], v253 offset:184\n\t"
            "ds_read_b64 v[164:165], v253 offset:1832\n\t"
            "ds_read_b64 v[166:167], v253 offset:1928\n\t"
            "s_waitcnt lgkmcnt(10)\n\t"
            "v_smfmac_f32_16x16x64_bf16 v[180:183], v[172:175], v[144:151], v255\n\t"
            "ds_read_b128 v[172:175], v254 offset:704\n\t"
            "v_cvt_pk_bf16_f32 v176, v176, v177\n\t"
            "v_cvt_pk_bf16_f32 v177, v178, v179\n\t"
            "s_nop 1\n\t"
            "v_mfma_f32_4x4x4_16b_bf16 v[196:199], v[176:177], v[176:177], v[196:199]\n\t"
            "s_nop 1\n\t"
            "v_cvt_pk_bf16_f32 v178, v180, v181\n\t"
            "v_cvt_pk_bf16_f32 v179, v182, v183\n\t"
            "s_nop 1\n\t"
            "v_mfma_f32_4x4x4_16b_bf16 v[196:199], v[178:179], v[178:179], v[196:199]\n\t"
            "ds_write_b128 v236, v[176:179] offset:256\n\t"
            "v_mfma_f32_16x16x32_bf16 v[192:195], v[238:241], v[176:179], v[192:195]\n\t"
            "s_waitcnt lgkmcnt(6)\n\t"
            "v_smfmac_f32_16x16x64_bf16 v[184:187], v[168:171], v[152:159], v255\n\t"
            "s_waitcnt lgkmcnt(1)\n\t"
            "v_smfmac_f32_16x16x64_bf16 v[188:191], v[172:175], v[160:167], v255\n\t"
            "s_nop 5\n\t"
            "v_cvt_pk_bf16_f32 v184, v184, v185\n\t"
            "v_cvt_pk_bf16_f32 v185, v186, v187\n\t"
            "s_nop 1\n\t"
            "v_mfma_f32_4x4x4_16b_bf16 v[196:199], v[184:185], v[184:185], v[196:199]\n\t"
            "v_cvt_pk_bf16_f32 v186, v188, v189\n\t"
            "v_cvt_pk_bf16_f32 v187, v190, v191\n\t"
            "s_nop 1\n\t"
            "v_mfma_f32_4x4x4_16b_bf16 v[196:199], v[186:187], v[186:187], v[196:199]\n\t"
            "v_mfma_f32_16x16x32_bf16 v[192:195], v[238:241], v[184:187], v[192:195]\n\t"
            "ds_write_b128 v237, v[184:187] offset:256\n\t"
            "s_nop 6\n\t"
            "v_cndmask_b32_e64 v193, v196, v197, s[60:61]\n\t"
            "v_cndmask_b32_e64 v193, v193, v198, s[62:63]\n\t"
            "v_cndmask_b32_e64 v193, v193, v199, s[64:65]\n\t"
            "ds_write_b64 v248, v[192:193] offset:18816\n\t"
            "s_waitcnt lgkmcnt(0)\n\t"
            "s_nop 7\n\t"
            :
            : "{v250}"(cX), "{v251}"(cS), "{v253}"(cB), "{v254}"(aA_), "{v255}"(idx_), "{v[238:241]}"(sel4), "{s67}"(ldsb + (wave < 4 ? X_OFF : O_OFF)),
              "{v252}"(lane * 16), "{s[68:71]}"(wrsrc), "{s72}"(flg), "{s73}"(wave == 0 ? ldsb + PF_OFF : 0u),
              "{s[60:61]}"(0x2222222222222222ull), "{s[62:63]}"(0x4444444444444444ull), "{s[64:65]}"(0x8888888888888888ull)
            : "v144", "v145", "v146", "v147", "v148", "v149", "v150", "v151", "v152", "v153", "v154", "v155", "v156", "v157", "v158", "v159", "v160", "v161", "v162", "v163", "v164", "v165", "v166", "v167", "v168", "v169", "v170", "v171", "v172", "v173", "v174", "v175", "v176", "v177", "v178", "v179", "v180", "v181", "v182", "v183", "v184", "v185", "v186", "v187", "v188", "v189", "v190", "v191", "v192", "v193", "v194", "v195", "v196", "v197", "v198", "v199", "v236", "v237", "v248", "v249", "m0", "scc", "memory");
        if (wave < 4) {
            POLL_W
        }
    }
    const unsigned long long mrow = 0x0001000100010001ull * ((1u << (cnt - 16)) - 1u);
#define KARGS                                                                                      \
    unsigned long long xb, wb, ob;                                                                 \
    asm volatile("s_load_dwordx2 %0, %3, 0x0\n\ts_load_dwordx2 %1, %3, 0x20\n\ts_load_dwordx2 %2, %3, 0x28\n\ts_waitcnt lgkmcnt(0)" \
                 : "=&s"(xb), "=&s"(wb), "=&s"(ob) : "s"(__builtin_amdgcn_kernarg_segment_ptr()) : "memory"); \
    xb -= XBIAS;                                                                                   \
    const u32x4 wr = {(unsigned)wb, (unsigned)(wb >> 32) & 0xffffu, (unsigned)WS_BYTES, 0x00020000u}; \
    const u32x4 orr = {(unsigned)ob, (unsigned)(ob >> 32) & 0xffffu, (unsigned)(IMG * IMG * CH * 2), 0x00020000u}; \
    const u32x4 xr = {(unsigned)xb, (unsigned)(xb >> 32) & 0xffffu, (unsigned)(IMG * IMG * CH * 2 + XBIAS), 0x00020000u};
    const int tk_ = (xi + 4) / 6;
    const unsigned touch = wave * 36864 + ((xi + 4 - 6 * tk_) * 6 + tk_) * 1024;
#define B_SCALARS \
            CONV_CONSTS(0) \
            const unsigned aCA_ = ldsb + CT_OFF + lane * 16; \
            KARGS \
            unsigned s45 = OOB, s46 = (en0 & 0x7ffffffu) + 96 * w, s47 = (en1 & 0x7ffffffu) + 96 * w; \
            unsigned s48 = ldsb + X_OFF, s67 = ldsb + X1_OFF, s76 = ldsb + WL_OFF + 8, s70 = 0; \
            unsigned s56 = __builtin_amdgcn_readfirstlane((en0 >> 30) * 4 + (en1 >> 30)); \
            unsigned s69 = 0x401fffffu | (en1 & 0x38000000u), s79 = (en1 & 0x08000000u) ? OOB : s47, s59 = ldsb + LX_OFF(1) + 32 * w; \
            unsigned long long wbase = wb + 6 * 36864; \
            unsigned wlen = 2 * 36864; \
            if (wave >= 6) { \
                unsigned rel; \
                asm volatile("s_getpc_b64 %0\n.Lwk_h2%=:\n\ts_mov_b32 %1, wk_code_end-wk_code_begin\n\ts_mov_b32 %2, .Lwk_h2%=-wk_code_begin" : "=s"(wbase), "=s"(wlen), "=s"(rel)); \
                wbase -= rel; \
            } \
            const unsigned wo_ = xi * 3456 + 96 + 96 * (w & 1); \
            wbase = (wbase + wo_) & 0xffffffffffffull; \
            wlen = __builtin_amdgcn_readfirstlane(wlen > wo_ ? wlen - wo_ : 0u); \
            unsigned long long xp = 0, sp = 0; \
            _Pragma("unroll") for (int v = 0; v < 4; ++v) { \
                const unsigned r = (CROW >> (8 * v)) & 0xFF; \
                xp |= (unsigned long long)XPAT(r) << (16 * v); \
                sp |= (unsigned long long)SPAT(r) << (16 * v); \
            }
#define B_SIO "+{s45}"(s45), "+{s46}"(s46), "+{s47}"(s47), "+{s48}"(s48), "+{s67}"(s67), "+{s76}"(s76), "+{s56}"(s56), "+{s70}"(s70), "+{s69}"(s69), "+{s79}"(s79), "+{s59}"(s59)
#define B_SIN "{v[238:241]}"(sel4), "{s[40:43]}"(orr), "{s[72:75]}"(xr), "{s39}"(0x401fffffu), "{s[28:31]}"(wr), "{s33}"(wave * 36864), "{s78}"(96 * w), \
              "{s27}"(96 * w - 384), "{s[50:51]}"(xp), "{s[52:53]}"(sp), "{s54}"(CLANE), "{s55}"(CROW & 0x0F0F0F0Fu), "{s[34:35]}"(0x0001000100010001ull), \
              "{s[60:61]}"(0x2222222222222222ull), "{s[62:63]}"(0x4444444444444444ull), "{s[64:65]}"(0x8888888888888888ull), \
              "{s66}"(ldsb + w * 8192), "{s71}"((ldsb + X_OFF) ^ (ldsb + X1_OFF)), "{s[86:87]}"(wbase), "{s85}"(wlen)
#define B_SCLOB "s83", "s84", "s88", "s89", "s90", "s91", "s26", "s77", "s68", "s36", "s37", "s38", "s57", "s58", "s49", "vcc", "scc", "m0", "memory"

    if (wave < 4) {
        LANE_SETUP
        const int dW = X_OFF + l15 * 384 + (((2 * w + (g >> 1)) ^ sw) << 4) + 8 * (g & 1);
        const int vB = V_OFF + w * 4608 + 1024 + lane * 8, rsR = RS_BASE + l15 * 4;
        {
            const unsigned rsb = ldsb + RS_BASE;
            const unsigned pE_ = fE - X_OFF, aO_ = ldsb + dW + (O_OFF - X_OFF), sel_ = (l15 & 3) ? 0u : 1u, aVB_ = ldsb + vB;
            const unsigned pST_ = lane * 16, aPLw_ = rsb + lane * 2, aRSr_ = ldsb + rsR;
            unsigned aRSw_ = rsb + lane * 4;
            const unsigned g16_ = (l15 & 3) ? ldsb + AT_OFF + 18816 : g * 16, g8_ = (l15 & 3) ? ldsb + AT_OFF + 18816 : g * 8;
            const unsigned loVL_ = ((2 * w + (l15 >> 3)) << 4) + (l15 & 7) * 2, loOL_ = g * 128;
            const unsigned on = (l15 & 3) ? 0u : 0x3F803F80u;
            const u32x4 ones4 = {on, on, on, on};
            unsigned s48 = ldsb + X_OFF, s70 = 0;
            unsigned s72 = ldsb + KQL_OFF + w * 192, s73 = ldsb + OL_OFF, s74 = ldsb + KQL_OFF + 784, s77 = ldsb + OL_OFF, s76 = ldsb + KQL_OFF + 768 + w * 4;
            KARGS
            asm volatile(
            "s_nop 4\n\t"
            "buffer_load_dwordx4 v[140:143], v248, s[84:87], s88 offen sc1\n\t"
            "s_add_u32 s49, s46, 0\n\t"
            "buffer_load_dwordx4 v[0:3], v248, s[84:87], s49 offen sc1\n\t"
            "s_add_u32 s49, s46, 6144\n\t"
            "buffer_load_dwordx4 v[24:27], v248, s[84:87], s49 offen sc1\n\t"
            "s_add_u32 s49, s46, 12288\n\t"
            "buffer_load_dwordx4 v[48:51], v248, s[84:87], s49 offen sc1\n\t"
            "s_add_u32 s49, s46, 18432\n\t"
            "buffer_load_dwordx4 v[72:75], v248, s[84:87], s49 offen sc1\n\t"
            "s_add_u32 s49, s46, 24576\n\t"
            "buffer_load_dwordx4 v[96:99], v248, s[84:87], s49 offen sc1\n\t"
            "s_add_u32 s49, s46, 30720\n\t"
            "buffer_load_dwordx4 v[120:123], v248, s[84:87], s49 offen sc1\n\t"
            "s_add_u32 s49, s46, 1024\n\t"
            "buffer_load_dwordx4 v[4:7], v248, s[84:87], s49 offen sc1\n\t"
            "s_add_u32 s49, s46, 7168\n\t"
            "buffer_load_dwordx4 v[28:31], v248, s[84:87], s49 offen sc1\n\t"
            "s_add_u32 s49, s46, 13312\n\t"
            "buffer_load_dwordx4 v[52:55], v248, s[84:87], s49 offen sc1\n\t"
            "s_add_u32 s49, s46, 19456\n\t"
            "buffer_load_dwordx4 v[76:79], v248, s[84:87], s49 offen sc1\n\t"
            "s_add_u32 s49, s46, 25600\n\t"
            "buffer_load_dwordx4 v[100:103], v248, s[84:87], s49 offen sc1\n\t"
            "s_add_u32 s49, s46, 31744\n\t"
            "buffer_load_dwordx4 v[124:127], v248, s[84:87], s49 offen sc1\n\t"
            "s_add_u32 s49, s46, 2048\n\t"
            "buffer_load_dwordx4 v[8:11], v248, s[84:87], s49 offen sc1\n\t"
            "s_add_u32 s49, s46, 8192\n\t"
            "buffer_load_dwordx4 v[32:35], v248, s[84:87], s49 offen sc1\n\t"
            "s_add_u32 s49, s46, 14336\n\t"
            "buffer_load_dwordx4 v[56:59], v248, s[84:87], s49 offen sc1\n\t"
            "s_add_u32 s49, s46, 20480\n\t"
            "buffer_load_dwordx4 v[80:83], v248, s[84:87], s49 offen sc1\n\t"
            "s_add_u32 s49, s46, 26624\n\t"
            "buffer_load_dwordx4 v[104:107], v248, s[84:87], s49 offen sc1\n\t"
            "s_add_u32 s49, s46, 32768\n\t"
            "buffer_load_dwordx4 v[128:131], v248, s[84:87], s49 offen sc1\n\t"
            "s_add_u32 s49, s46, 3072\n\t"
            "buffer_load_dwordx4 v[12:15], v248, s[84:87], s49 offen sc1\n\t"
            "s_add_u32 s49, s46, 9216\n\t"
            "buffer_load_dwordx4 v[36:39], v248, s[84:87], s49 offen sc1\n\t"
            "s_add_u32 s49, s46, 15360\n\t"
            "buffer_load_dwordx4 v[60:63], v248, s[84:87], s49 offen sc1\n\t"
            "s_add_u32 s49, s46, 21504\n\t"
            "buffer_load_dwordx4 v[84:87], v248, s[84:87], s49 offen sc1\n\t"
            "s_add_u32 s49, s46, 27648\n\t"
            "buffer_load_dwordx4 v[108:111], v248, s[84:87], s49 offen sc1\n\t"
            "s_add_u32 s49, s46, 33792\n\t"
            "buffer_load_dwordx4 v[132:135], v248, s[84:87], s49 offen sc1\n\t"
            "s_add_u32 s49, s46, 4096\n\t"
            "buffer_load_dwordx4 v[16:19], v248, s[84:87], s49 offen sc1\n\t"
            "s_add_u32 s49, s46, 10240\n\t"
            "buffer_load_dwordx4 v[40:43], v248, s[84:87], s49 offen sc1\n\t"
            "s_add_u32 s49, s46, 16384\n\t"
            "buffer_load_dwordx4 v[64:67], v248, s[84:87], s49 offen sc1\n\t"
            "s_add_u32 s49, s46, 22528\n\t"
            "buffer_load_dwordx4 v[88:91], v248, s[84:87], s49 offen sc1\n\t"
            "s_add_u32 s49, s46, 28672\n\t"
            "buffer_load_dwordx4 v[112:115], v248, s[84:87], s49 offen sc1\n\t"
            "s_add_u32 s49, s46, 34816\n\t"
            "buffer_load_dwordx4 v[136:139], v248, s[84:87], s49 offen sc1\n\t"
            "s_add_u32 s49, s46, 5120\n\t"
            "buffer_load_dwordx4 v[20:23], v248, s[84:87], s49 offen sc1\n\t"
            "s_add_u32 s49, s46, 11264\n\t"
            "buffer_load_dwordx4 v[44:47], v248, s[84:87], s49 offen sc1\n\t"
            "s_add_u32 s49, s46, 17408\n\t"
            "buffer_load_dwordx4 v[68:71], v248, s[84:87], s49 offen sc1\n\t"
            "s_add_u32 s49, s46, 23552\n\t"
            "buffer_load_dwordx4 v[92:95], v248, s[84:87], s49 offen sc1\n\t"
            "s_add_u32 s49, s46, 29696\n\t"
            "buffer_load_dwordx4 v[116:119], v248, s[84:87], s49 offen sc1\n\t"
            "s_add_u32 s49, s46, 35840\n\t"
            "buffer_load_dwordx4 v[140:143], v248, s[84:87], s49 offen sc1\n\t"
            "s_waitcnt lgkmcnt(0)\n\t"
            "s_barrier\n\t"
            "s_setprio 2\n\t"
            "v_add_u32 v235, s71, v248\n\t"
            "v_add_u32 v232, s71, v244\n\t"
            "v_xor_b32 v233, 64, v232\n\t"
            "v_subrev_u32 v239, s75, v251\n\t"
            "v_mul_u32_u24 v239, 200, v239\n\t"
            "v_lshrrev_b32 v234, 3, v255\n\t"
            "v_add3_u32 v234, v234, v239, s72\n\t"
            "ds_read_b128 v[228:231], v235 offset:12288\n\t"
            "ds_read_b128 v[144:147], v232\n\t"
            "ds_read_b128 v[148:151], v233\n\t"
            "ds_read_b128 v[204:207], v232 offset:6144\n\t"
            "ds_read_b128 v[208:211], v233 offset:6144\n\t"
            "ds_read_b128 v[152:155], v232 offset:128\n\t"
            "ds_read_b128 v[156:159], v233 offset:128\n\t"
            "ds_read_b128 v[212:215], v232 offset:6272\n\t"
            "ds_read_b128 v[224:227], v233 offset:6272\n\t"
            "ds_read_b128 v[160:163], v232 offset:256\n\t"
            "ds_read_b128 v[164:167], v233 offset:256\n\t"
            "s_waitcnt vmcnt(24)\n\t"
            "s_waitcnt lgkmcnt(10)\n\t"
            "v_add_f32 v236, v228, v230\n\t"
            "v_add_f32 v237, v229, v231\n\t"
            "s_waitcnt lgkmcnt(8)\n\t"
            "v_mfma_f32_16x16x32_bf16 v[168:171], v[0:3], v[144:147], 0\n\t"
            "ds_read_b128 v[228:231], v235 offset:13312\n\t"
            "v_lshrrev_b32 v235, 4, v255\n\t"
            "v_mfma_f32_16x16x32_bf16 v[168:171], v[4:7], v[148:151], v[168:171]\n\t"
            "v_add3_u32 v235, v235, v239, s72\n\t"
            "v_mfma_f32_16x16x32_bf16 v[176:179], v[48:51], v[144:147], 0\n\t"
            "v_mfma_f32_16x16x32_bf16 v[176:179], v[52:55], v[148:151], v[176:179]\n\t"
            "v_mfma_f32_16x16x32_bf16 v[172:175], v[24:27], v[144:147], 0\n\t"
            "v_mfma_f32_16x16x32_bf16 v[172:175], v[28:31], v[148:151], v[172:175]\n\t"
            "v_mfma_f32_16x16x32_bf16 v[180:183], v[72:75], v[144:147], 0\n\t"
            "v_mfma_f32_16x16x32_bf16 v[180:183], v[76:79], v[148:151], v[180:183]\n\t"
            "v_mfma_f32_16x16x32_bf16 v[184:187], v[96:99], v[144:147], 0\n\t"
            "v_mfma_f32_16x16x32_bf16 v[184:187], v[100:103], v[148:151], v[184:187]\n\t"
            "v_mfma_f32_16x16x32_bf16 v[188:191], v[120:123], v[144:147], 0\n\t"
            "s_waitcnt lgkmcnt(0)\n\t"
            "v_add_f32 v228, v228, v230\n\t"
            "v_add_f32 v236, v236, v228\n\t"
            "v_mfma_f32_16x16x32_bf16 v[188:191], v[124:127], v[148:151], v[188:191]\n\t"
            "v_add_f32 v229, v229, v231\n\t"
            "v_mul_f32 v236, 0x3baaaaab, v236\n\t"
            "v_mfma_f32_16x16x32_bf16 v[192:195], v[0:3], v[204:207], 0\n\t"
            "v_add_f32 v237, v237, v229\n\t"
            "v_mul_f32_e64 v236, v236, -v236\n\t"
            "v_mfma_f32_16x16x32_bf16 v[192:195], v[4:7], v[208:211], v[192:195]\n\t"
            "v_fmac_f32 v236, 0x3baaaaab, v237\n\t"
            "v_max_f32 v236, 0, v236\n\t"
            "v_mfma_f32_16x16x32_bf16 v[196:199], v[24:27], v[204:207], 0\n\t"
            "v_add_f32 v236, 0x3727c5ac, v236\n\t"
            "v_rsq_f32 v236, v236\n\t"
            "v_mfma_f32_16x16x32_bf16 v[196:199], v[28:31], v[208:211], v[196:199]\n\t"
            "ds_write_b32 v249, v236\n\t"
            "v_mfma_f32_16x16x32_bf16 v[200:203], v[48:51], v[204:207], 0\n\t"
            "ds_read_b32 v236, v251\n\t"
            "ds_read_b32 v237, v251 offset:64\n\t"
            "v_mfma_f32_16x16x32_bf16 v[200:203], v[52:55], v[208:211], v[200:203]\n\t"
            "v_mfma_f32_16x16x32_bf16 v[216:219], v[72:75], v[204:207], 0\n\t"
            "v_mfma_f32_16x16x32_bf16 v[216:219], v[76:79], v[208:211], v[216:219]\n\t"
            "v_mfma_f32_16x16x32_bf16 v[220:223], v[96:99], v[204:207], 0\n\t"
            "v_mfma_f32_16x16x32_bf16 v[220:223], v[100:103], v[208:211], v[220:223]\n\t"
            "v_mfma_f32_16x16x32_bf16 v[228:231], v[120:123], v[204:207], 0\n\t"
            "ds_read_b128 v[204:207], v232 offset:6400\n\t"
            "v_add_u32 v232, s48, v244\n\t"
            "v_mfma_f32_16x16x32_bf16 v[228:231], v[124:127], v[208:211], v[228:231]\n\t"
            "ds_read_b128 v[208:211], v233 offset:6400\n\t"
            "s_waitcnt vmcnt(12)\n\t"
            "v_mfma_f32_16x16x32_bf16 v[168:171], v[8:11], v[152:155], v[168:171]\n\t"
            "v_xor_b32 v233, 64, v232\n\t"
            "v_mfma_f32_16x16x32_bf16 v[168:171], v[12:15], v[156:159], v[168:171]\n\t"
            "v_mfma_f32_16x16x32_bf16 v[176:179], v[56:59], v[152:155], v[176:179]\n\t"
            "v_mfma_f32_16x16x32_bf16 v[176:179], v[60:63], v[156:159], v[176:179]\n\t"
            "v_mfma_f32_16x16x32_bf16 v[172:175], v[32:35], v[152:155], v[172:175]\n\t"
            "v_mfma_f32_16x16x32_bf16 v[172:175], v[36:39], v[156:159], v[172:175]\n\t"
            "v_mfma_f32_16x16x32_bf16 v[180:183], v[80:83], v[152:155], v[180:183]\n\t"
            "v_mfma_f32_16x16x32_bf16 v[180:183], v[84:87], v[156:159], v[180:183]\n\t"
            "v_mfma_f32_16x16x32_bf16 v[184:187], v[104:107], v[152:155], v[184:187]\n\t"
            "v_mfma_f32_16x16x32_bf16 v[184:187], v[108:111], v[156:159], v[184:187]\n\t"
            "v_mfma_f32_16x16x32_bf16 v[188:191], v[128:131], v[152:155], v[188:191]\n\t"
            "v_mfma_f32_16x16x32_bf16 v[188:191], v[132:135], v[156:159], v[188:191]\n\t"
            "v_mfma_f32_16x16x32_bf16 v[192:195], v[8:11], v[212:215], v[192:195]\n\t"
            "v_mfma_f32_16x16x32_bf16 v[192:195], v[12:15], v[224:227], v[192:195]\n\t"
            "v_mfma_f32_16x16x32_bf16 v[196:199], v[32:35], v[212:215], v[196:199]\n\t"
            "v_mfma_f32_16x16x32_bf16 v[196:199], v[36:39], v[224:227], v[196:199]\n\t"
            "v_mfma_f32_16x16x32_bf16 v[200:203], v[56:59], v[212:215], v[200:203]\n\t"
            "v_mfma_f32_16x16x32_bf16 v[200:203], v[60:63], v[224:227], v[200:203]\n\t"
            "v_mfma_f32_16x16x32_bf16 v[216:219], v[80:83], v[212:215], v[216:219]\n\t"
            "v_mfma_f32_16x16x32_bf16 v[216:219], v[84:87], v[224:227], v[216:219]\n\t"
            "v_mfma_f32_16x16x32_bf16 v[220:223], v[104:107], v[212:215], v[220:223]\n\t"
            "v_mfma_f32_16x16x32_bf16 v[220:223], v[108:111], v[224:227], v[220:223]\n\t"
            "v_mfma_f32_16x16x32_bf16 v[228:231], v[128:131], v[212:215], v[228:231]\n\t"
            "ds_read_b128 v[212:215], v232 offset:256\n\t"
            "v_mfma_f32_16x16x32_bf16 v[228:231], v[132:135], v[224:227], v[228:231]\n\t"
            "ds_read_b128 v[224:227], v233 offset:256\n\t"
            "s_waitcnt vmcnt(0)\n\t"
            "v_mfma_f32_16x16x32_bf16 v[168:171], v[16:19], v[160:163], v[168:171]\n\t"
            "v_mfma_f32_16x16x32_bf16 v[168:171], v[20:23], v[164:167], v[168:171]\n\t"
            "v_mfma_f32_16x16x32_bf16 v[176:179], v[64:67], v[160:163], v[176:179]\n\t"
            "v_mfma_f32_16x16x32_bf16 v[176:179], v[68:71], v[164:167], v[176:179]\n\t"
            "v_mfma_f32_16x16x32_bf16 v[180:183], v[88:91], v[160:163], v[180:183]\n\t"
            "v_mfma_f32_16x16x32_bf16 v[180:183], v[92:95], v[164:167], v[180:183]\n\t"
            "v_mfma_f32_16x16x32_bf16 v[172:175], v[40:43], v[160:163], v[172:175]\n\t"
            "v_mfma_f32_16x16x32_bf16 v[172:175], v[44:47], v[164:167], v[172:175]\n\t"
            "s_waitcnt lgkmcnt(5)\n\t"
            "s_nop 0\n\t"
            "v_mul_f32 v168, v236, v168\n\t"
            "v_mfma_f32_16x16x32_bf16 v[184:187], v[112:115], v[160:163], v[184:187]\n\t"
            "v_mul_f32 v169, v236, v169\n\t"
            "v_mul_f32 v170, v236, v170\n\t"
            "v_mfma_f32_16x16x32_bf16 v[184:187], v[116:119], v[164:167], v[184:187]\n\t"
            "v_mul_f32 v176, v236, v176\n\t"
            "v_mul_f32 v177, v236, v177\n\t"
            "v_mfma_f32_16x16x32_bf16 v[188:191], v[136:139], v[160:163], v[188:191]\n\t"
            "v_mul_f32 v171, v236, v171\n\t"
            "v_mul_f32 v178, v236, v178\n\t"
            "v_mfma_f32_16x16x32_bf16 v[188:191], v[140:143], v[164:167], v[188:191]\n\t"
            "v_mul_f32 v179, v236, v179\n\t"
            "v_cvt_pk_bf16_f32 v168, v168, v169\n\t"
            "s_waitcnt lgkmcnt(2)\n\t"
            "v_mfma_f32_16x16x32_bf16 v[192:195], v[16:19], v[204:207], v[192:195]\n\t"
            "v_cvt_pk_bf16_f32 v176, v176, v177\n\t"
            "v_cvt_pk_bf16_f32 v180, v180, v181\n\t"
            "v_mfma_f32_16x16x32_bf16 v[192:195], v[20:23], v[208:211], v[192:195]\n\t"
            "v_mul_f32 v172, v236, v172\n\t"
            "v_mul_f32 v173, v236, v173\n\t"
            "v_mfma_f32_16x16x32_bf16 v[196:199], v[40:43], v[204:207], v[196:199]\n\t"
            "v_mul_f32 v174, v236, v174\n\t"
            "v_mul_f32 v175, v236, v175\n\t"
            "v_mfma_f32_16x16x32_bf16 v[196:199], v[44:47], v[208:211], v[196:199]\n\t"
            "v_cvt_pk_bf16_f32 v169, v170, v171\n\t"
            "v_cvt_pk_bf16_f32 v177, v178, v179\n\t"
            "v_mfma_f32_16x16x32_bf16 v[200:203], v[64:67], v[204:207], v[200:203]\n\t"
            "v_cvt_pk_bf16_f32 v181, v182, v183\n\t"
            "v_cvt_pk_bf16_f32 v170, v172, v173\n\t"
            "v_mfma_f32_16x16x32_bf16 v[200:203], v[68:71], v[208:211], v[200:203]\n\t"
            "v_cvt_pk_bf16_f32 v171, v174, v175\n\t"
            "v_cvt_pk_bf16_f32 v182, v184, v185\n\t"
            "v_mfma_f32_16x16x32_bf16 v[216:219], v[88:91], v[204:207], v[216:219]\n\t"
            "v_cvt_pk_bf16_f32 v183, v186, v187\n\t"
            "v_cvt_pk_bf16_f32 v178, v188, v189\n\t"
            "v_mfma_f32_16x16x32_bf16 v[216:219], v[92:95], v[208:211], v[216:219]\n\t"
            "v_cvt_pk_bf16_f32 v179, v190, v191\n\t"
            "v_mul_f32 v236, 0x3e553b94, v236\n\t"
            "v_mfma_f32_16x16x32_bf16 v[220:223], v[112:115], v[204:207], v[220:223]\n\t"
            "v_add_u32 v184, s76, v239\n\t"
            "v_add_u32 v185, s74, v239\n\t"
            "v_mfma_f32_16x16x32_bf16 v[220:223], v[116:119], v[208:211], v[220:223]\n\t"
            "s_mov_b64 exec, -1\n\t"
            "ds_write_b128 v234, v[168:171]\n\t"
            "ds_write_b128 v234, v[180:183] offset:64\n\t"
            "ds_write_b64 v235, v[176:177] offset:128\n\t"
            "ds_write_b64 v235, v[178:179] offset:160\n\t"
            "s_mov_b64 exec, -1\n\t"
            "v_mfma_f32_16x16x16_bf16 v[172:175], v[176:177], v[178:179], 0\n\t"
            "v_mul_f32 v192, v237, v192\n\t"
            "v_mul_f32 v193, v237, v193\n\t"
            "v_mul_f32 v200, v237, v200\n\t"
            "v_mfma_f32_16x16x32_bf16 v[228:231], v[136:139], v[204:207], v[228:231]\n\t"
            "ds_read_b128 v[204:207], v232\n\t"
            "v_mul_f32 v201, v237, v201\n\t"
            "v_mfma_f32_16x16x32_bf16 v[228:231], v[140:143], v[208:211], v[228:231]\n\t"
            "ds_read_b128 v[208:211], v233\n\t"
            "v_mul_f32 v194, v237, v194\n\t"
            "v_mfma_f32_16x16x32_bf16 v[172:175], v[168:171], v[180:183], v[172:175]\n\t"
            "v_mul_f32 v195, v237, v195\n\t"
            "v_mul_f32 v202, v237, v202\n\t"
            "v_mul_f32 v203, v237, v203\n\t"
            "v_cvt_pk_bf16_f32 v192, v192, v193\n\t"
            "v_cvt_pk_bf16_f32 v200, v200, v201\n\t"
            "v_cvt_pk_bf16_f32 v216, v216, v217\n\t"
            "v_mul_f32 v196, v237, v196\n\t"
            "v_mul_f32 v197, v237, v197\n\t"
            "v_mul_f32 v198, v237, v198\n\t"
            "v_mul_f32 v199, v237, v199\n\t"
            "v_cvt_pk_bf16_f32 v193, v194, v195\n\t"
            "v_cvt_pk_bf16_f32 v201, v202, v203\n\t"
            "v_cvt_pk_bf16_f32 v217, v218, v219\n\t"
            "v_cvt_pk_bf16_f32 v194, v196, v197\n\t"
            "v_cvt_pk_bf16_f32 v195, v198, v199\n\t"
            "v_cvt_pk_bf16_f32 v218, v220, v221\n\t"
            "v_cvt_pk_bf16_f32 v219, v222, v223\n\t"
            "v_cvt_pk_bf16_f32 v202, v228, v229\n\t"
            "v_cvt_pk_bf16_f32 v203, v230, v231\n\t"
            "v_mul_f32 v237, 0x3e553b94, v237\n\t"
            "v_add_u32 v220, s76, v239\n\t"
            "v_mfma_f32_16x16x16_bf16 v[196:199], v[200:201], v[202:203], 0\n\t"
            "s_and_b32 exec_lo, s56, 0x11111111\n\t"
            "s_and_b32 exec_hi, s57, 0x11111111\n\t"
            "ds_write_b32 v184, v172 offset:0\n\t"
            "s_and_b32 exec_lo, s56, 0x22222222\n\t"
            "s_and_b32 exec_hi, s57, 0x22222222\n\t"
            "ds_write_b32 v184, v173 offset:0\n\t"
            "s_and_b32 exec_lo, s56, 0x44444444\n\t"
            "s_and_b32 exec_hi, s57, 0x44444444\n\t"
            "ds_write_b32 v184, v174 offset:0\n\t"
            "s_and_b32 exec_lo, s56, 0x88888888\n\t"
            "s_and_b32 exec_hi, s57, 0x88888888\n\t"
            "ds_write_b32 v184, v175 offset:0\n\t"
            "s_mov_b64 exec, s[56:57]\n\t"
            "ds_write_b32 v185, v236 offset:0\n\t"
            "s_mov_b64 exec, -1\n\t"
            "s_waitcnt lgkmcnt(5)\n\t"
            "v_mfma_f32_16x16x32_bf16 v[144:147], v[0:3], v[204:207], 0\n\t"
            "v_add_u32 v221, s74, v239\n\t"
            "v_mfma_f32_16x16x32_bf16 v[144:147], v[4:7], v[208:211], v[144:147]\n\t"
            "s_mov_b64 exec, s[54:55]\n\t"
            "ds_write_b128 v234, v[192:195] offset:12800\n\t"
            "ds_write_b128 v234, v[216:219] offset:12864\n\t"
            "ds_write_b64 v235, v[200:201] offset:12928\n\t"
            "ds_write_b64 v235, v[202:203] offset:12960\n\t"
            "s_mov_b64 exec, -1\n\t"
            "v_mfma_f32_16x16x32_bf16 v[148:151], v[28:31], v[208:211], 0\n\t"
            "v_add_u32 v235, s48, v248\n\t"
            "v_mfma_f32_16x16x32_bf16 v[148:151], v[24:27], v[204:207], v[148:151]\n\t"
            "ds_read_b128 v[228:231], v235 offset:18816\n\t"
            "v_mfma_f32_16x16x32_bf16 v[152:155], v[48:51], v[204:207], 0\n\t"
            "v_mfma_f32_16x16x32_bf16 v[152:155], v[52:55], v[208:211], v[152:155]\n\t"
            "v_mfma_f32_16x16x32_bf16 v[156:159], v[76:79], v[208:211], 0\n\t"
            "v_mfma_f32_16x16x32_bf16 v[156:159], v[72:75], v[204:207], v[156:159]\n\t"
            "v_mfma_f32_16x16x32_bf16 v[160:163], v[96:99], v[204:207], 0\n\t"
            "v_mfma_f32_16x16x32_bf16 v[160:163], v[100:103], v[208:211], v[160:163]\n\t"
            "v_mfma_f32_16x16x32_bf16 v[164:167], v[124:127], v[208:211], 0\n\t"
            "ds_read_b128 v[208:211], v233 offset:128\n\t"
            "v_mfma_f32_16x16x32_bf16 v[164:167], v[120:123], v[204:207], v[164:167]\n\t"
            "ds_read_b128 v[204:207], v232 offset:128\n\t"
            "v_mfma_f32_16x16x32_bf16 v[196:199], v[192:195], v[216:219], v[196:199]\n\t"
            "s_waitcnt lgkmcnt(2)\n\t"
            "v_add_f32 v236, v228, v230\n\t"
            "s_nop 5\n\t"
            "s_and_b32 exec_lo, s78, 0x11111111\n\t"
            "s_and_b32 exec_hi, s79, 0x11111111\n\t"
            "ds_write_b32 v220, v196 offset:12800\n\t"
            "s_and_b32 exec_lo, s78, 0x22222222\n\t"
            "s_and_b32 exec_hi, s79, 0x22222222\n\t"
            "ds_write_b32 v220, v197 offset:12800\n\t"
            "s_and_b32 exec_lo, s78, 0x44444444\n\t"
            "s_and_b32 exec_hi, s79, 0x44444444\n\t"
            "ds_write_b32 v220, v198 offset:12800\n\t"
            "s_and_b32 exec_lo, s78, 0x88888888\n\t"
            "s_and_b32 exec_hi, s79, 0x88888888\n\t"
            "ds_write_b32 v220, v199 offset:12800\n\t"
            "s_mov_b64 exec, s[78:79]\n\t"
            "ds_write_b32 v221, v237 offset:12800\n\t"
            "s_mov_b64 exec, -1\n\t"
            "s_waitcnt lgkmcnt(5)\n\t"
            "v_mfma_f32_16x16x32_bf16 v[144:147], v[8:11], v[204:207], v[144:147]\n\t"
            "v_add_f32 v237, v229, v231\n\t"
            "v_mfma_f32_16x16x32_bf16 v[144:147], v[12:15], v[208:211], v[144:147]\n\t"
            "ds_read_b128 v[228:231], v235 offset:19840\n\t"
            "v_mfma_f32_16x16x32_bf16 v[148:151], v[36:39], v[208:211], v[148:151]\n\t"
            "v_mfma_f32_16x16x32_bf16 v[148:151], v[32:35], v[204:207], v[148:151]\n\t"
            "v_mfma_f32_16x16x32_bf16 v[152:155], v[56:59], v[204:207], v[152:155]\n\t"
            "v_mfma_f32_16x16x32_bf16 v[152:155], v[60:63], v[208:211], v[152:155]\n\t"
            "v_mfma_f32_16x16x32_bf16 v[156:159], v[84:87], v[208:211], v[156:159]\n\t"
            "v_mfma_f32_16x16x32_bf16 v[156:159], v[80:83], v[204:207], v[156:159]\n\t"
            "v_mfma_f32_16x16x32_bf16 v[160:163], v[104:107], v[204:207], v[160:163]\n\t"
            "v_mfma_f32_16x16x32_bf16 v[160:163], v[108:111], v[208:211], v[160:163]\n\t"
            "v_mfma_f32_16x16x32_bf16 v[164:167], v[132:135], v[208:211], v[164:167]\n\t"
            "ds_read_b128 v[208:211], v233 offset:6144\n\t"
            "v_mfma_f32_16x16x32_bf16 v[164:167], v[128:131], v[204:207], v[164:167]\n\t"
            "ds_read_b128 v[204:207], v232 offset:6144\n\t"
            "s_waitcnt lgkmcnt(2)\n\t"
            "v_add_f32 v228, v228, v230\n\t"
            "v_mfma_f32_16x16x32_bf16 v[144:147], v[16:19], v[212:215], v[144:147]\n\t"
            "v_add_f32 v236, v236, v228\n\t"
            "v_add_f32 v229, v229, v231\n\t"
            "v_mfma_f32_16x16x32_bf16 v[144:147], v[20:23], v[224:227], v[144:147]\n\t"
            "v_mul_f32 v236, 0x3baaaaab, v236\n\t"
            "v_add_f32 v237, v237, v229\n\t"
            "v_mfma_f32_16x16x32_bf16 v[148:151], v[44:47], v[224:227], v[148:151]\n\t"
            "v_mul_f32_e64 v236, v236, -v236\n\t"
            "v_fmac_f32 v236, 0x3baaaaab, v237\n\t"
            "v_mfma_f32_16x16x32_bf16 v[148:151], v[40:43], v[212:215], v[148:151]\n\t"
            "v_max_f32 v236, 0, v236\n\t"
            "v_add_f32 v236, 0x3727c5ac, v236\n\t"
            "v_mfma_f32_16x16x32_bf16 v[152:155], v[64:67], v[212:215], v[152:155]\n\t"
            "v_rsq_f32 v236, v236\n\t"
            "v_mfma_f32_16x16x32_bf16 v[152:155], v[68:71], v[224:227], v[152:155]\n\t"
            "ds_write_b32 v249, v236\n\t"
            "v_mfma_f32_16x16x32_bf16 v[156:159], v[92:95], v[224:227], v[156:159]\n\t"
            "ds_read_b32 v236, v251\n\t"
            "ds_read_b32 v237, v251 offset:64\n\t"
            "v_mfma_f32_16x16x32_bf16 v[156:159], v[88:91], v[212:215], v[156:159]\n\t"
            "ds_read_b32 v238, v251 offset:128\n\t"
            "v_mfma_f32_16x16x32_bf16 v[160:163], v[112:115], v[212:215], v[160:163]\n\t"
            "v_mfma_f32_16x16x32_bf16 v[160:163], v[116:119], v[224:227], v[160:163]\n\t"
            "v_mfma_f32_16x16x32_bf16 v[164:167], v[140:143], v[224:227], v[164:167]\n\t"
            "ds_read_b128 v[224:227], v233 offset:6272\n\t"
            "v_mfma_f32_16x16x32_bf16 v[164:167], v[136:139], v[212:215], v[164:167]\n\t"
            "ds_read_b128 v[212:215], v232 offset:6272\n\t"
            "s_waitcnt lgkmcnt(6)\n\t"
            "v_mfma_f32_16x16x32_bf16 v[168:171], v[0:3], v[204:207], 0\n\t"
            "v_cvt_pk_bf16_f32 v156, v156, v157\n\t"
            "v_cvt_pk_bf16_f32 v157, v158, v159\n\t"
            "v_mfma_f32_16x16x32_bf16 v[168:171], v[4:7], v[208:211], v[168:171]\n\t"
            "v_cvt_pk_bf16_f32 v158, v160, v161\n\t"
            "v_cvt_pk_bf16_f32 v159, v162, v163\n\t"
            "v_mfma_f32_16x16x32_bf16 v[172:175], v[28:31], v[208:211], 0\n\t"
            "v_mfma_f32_16x16x32_bf16 v[172:175], v[24:27], v[204:207], v[172:175]\n\t"
            "v_mfma_f32_16x16x32_bf16 v[176:179], v[48:51], v[204:207], 0\n\t"
            "s_waitcnt lgkmcnt(4)\n\t"
            "v_mul_f32 v144, v236, v144\n\t"
            "v_mul_f32 v145, v236, v145\n\t"
            "v_mfma_f32_16x16x32_bf16 v[176:179], v[52:55], v[208:211], v[176:179]\n\t"
            "v_mul_f32 v152, v236, v152\n\t"
            "v_mul_f32 v153, v236, v153\n\t"
            "v_mfma_f32_16x16x32_bf16 v[180:183], v[76:79], v[208:211], 0\n\t"
            "v_mul_f32 v146, v236, v146\n\t"
            "v_mul_f32 v147, v236, v147\n\t"
            "v_mfma_f32_16x16x32_bf16 v[180:183], v[72:75], v[204:207], v[180:183]\n\t"
            "v_mul_f32 v154, v236, v154\n\t"
            "v_mul_f32 v155, v236, v155\n\t"
            "v_mfma_f32_16x16x32_bf16 v[184:187], v[96:99], v[204:207], 0\n\t"
            "v_cvt_pk_bf16_f32 v144, v144, v145\n\t"
            "v_cvt_pk_bf16_f32 v152, v152, v153\n\t"
            "v_mfma_f32_16x16x32_bf16 v[184:187], v[100:103], v[208:211], v[184:187]\n\t"
            "v_mul_f32 v148, v236, v148\n\t"
            "v_mul_f32 v149, v236, v149\n\t"
            "v_mfma_f32_16x16x32_bf16 v[188:191], v[124:127], v[208:211], 0\n\t"
            "ds_read_b128 v[208:211], v233 offset:6400\n\t"
            "v_mul_f32 v150, v236, v150\n\t"
            "v_mfma_f32_16x16x32_bf16 v[188:191], v[120:123], v[204:207], v[188:191]\n\t"
            "ds_read_b128 v[204:207], v232 offset:6400\n\t"
            "v_mul_f32 v151, v236, v151\n\t"
            "s_waitcnt lgkmcnt(2)\n\t"
            "v_mfma_f32_16x16x32_bf16 v[168:171], v[8:11], v[212:215], v[168:171]\n\t"
            "v_cvt_pk_bf16_f32 v145, v146, v147\n\t"
            "v_cvt_pk_bf16_f32 v153, v154, v155\n\t"
            "v_mfma_f32_16x16x32_bf16 v[168:171], v[12:15], v[224:227], v[168:171]\n\t"
            "v_cvt_pk_bf16_f32 v146, v148, v149\n\t"
            "v_cvt_pk_bf16_f32 v147, v150, v151\n\t"
            "v_mfma_f32_16x16x32_bf16 v[172:175], v[36:39], v[224:227], v[172:175]\n\t"
            "v_cvt_pk_bf16_f32 v154, v164, v165\n\t"
            "v_cvt_pk_bf16_f32 v155, v166, v167\n\t"
            "v_mfma_f32_16x16x32_bf16 v[172:175], v[32:35], v[212:215], v[172:175]\n\t"
            "v_mul_f32 v236, 0x3e553b94, v236\n\t"
            "v_mfma_f32_16x16x32_bf16 v[176:179], v[56:59], v[212:215], v[176:179]\n\t"
            "v_mfma_f32_16x16x32_bf16 v[176:179], v[60:63], v[224:227], v[176:179]\n\t"
            "v_mfma_f32_16x16x32_bf16 v[180:183], v[84:87], v[224:227], v[180:183]\n\t"
            "v_mfma_f32_16x16x32_bf16 v[180:183], v[80:83], v[212:215], v[180:183]\n\t"
            "v_mfma_f32_16x16x32_bf16 v[184:187], v[104:107], v[212:215], v[184:187]\n\t"
            "v_mfma_f32_16x16x32_bf16 v[184:187], v[108:111], v[224:227], v[184:187]\n\t"
            "v_mfma_f32_16x16x32_bf16 v[188:191], v[132:135], v[224:227], v[188:191]\n\t"
            "ds_read_b128 v[224:227], v233 offset:12288\n\t"
            "v_mfma_f32_16x16x32_bf16 v[188:191], v[128:131], v[212:215], v[188:191]\n\t"
            "ds_read_b128 v[212:215], v232 offset:12288\n\t"
            "s_waitcnt lgkmcnt(2)\n\t"
            "v_mfma_f32_16x16x32_bf16 v[168:171], v[16:19], v[204:207], v[168:171]\n\t"
            "v_mfma_f32_16x16x32_bf16 v[168:171], v[20:23], v[208:211], v[168:171]\n\t"
            "v_mfma_f32_16x16x32_bf16 v[172:175], v[44:47], v[208:211], v[172:175]\n\t"
            "v_mfma_f32_16x16x32_bf16 v[172:175], v[40:43], v[204:207], v[172:175]\n\t"
            "v_mfma_f32_16x16x32_bf16 v[176:179], v[64:67], v[204:207], v[176:179]\n\t"
            "v_mfma_f32_16x16x32_bf16 v[176:179], v[68:71], v[208:211], v[176:179]\n\t"
            "v_mfma_f32_16x16x32_bf16 v[180:183], v[92:95], v[208:211], v[180:183]\n\t"
            "v_mfma_f32_16x16x32_bf16 v[180:183], v[88:91], v[204:207], v[180:183]\n\t"
            "v_mfma_f32_16x16x32_bf16 v[184:187], v[112:115], v[204:207], v[184:187]\n\t"
            "v_mfma_f32_16x16x32_bf16 v[184:187], v[116:119], v[208:211], v[184:187]\n\t"
            "v_mfma_f32_16x16x32_bf16 v[188:191], v[140:143], v[208:211], v[188:191]\n\t"
            "ds_read_b128 v[208:211], v233 offset:12416\n\t"
            "v_mfma_f32_16x16x32_bf16 v[188:191], v[136:139], v[204:207], v[188:191]\n\t"
            "ds_read_b128 v[204:207], v232 offset:12416\n\t"
            "s_waitcnt lgkmcnt(0)\n\t"
            "v_subrev_u32 v249, s75, v251\n\t"
            "v_add_u32 v249, 162816, v249\n\t"
            "s_mov_b32 s90, 2\n\t"
            "s_mov_b32 s81, 0\n\t"
            "s_mov_b32 s91, 163524\n\t"
            "s_nop 7\n\t"
            ".p2align 6\n\t"
            "s_nop 0\n\t"
            "s_nop 0\n\t"
            "s_nop 0\n\t"
            ".Lwa_loop%=:\n\t"
            "s_waitcnt lgkmcnt(2)\n\t"
            "v_mfma_f32_16x16x32_bf16 v[192:195], v[0:3], v[212:215], 0\n\t"
            "s_bfe_u32 s49, s70, 0x30001\n\t"
            "s_lshl_b32 s49, s49, 4\n\t"
            "s_or_b32 s73, s77, s49\n\t"
            "v_mfma_f32_16x16x32_bf16 v[192:195], v[4:7], v[224:227], v[192:195]\n\t"
            "v_mad_u32_u24 v220, v246, s72, v253\n\t"
            "v_mad_u32_u24 v216, v246, s72, v252\n\t"
            "v_mfma_f32_16x16x32_bf16 v[148:151], v[28:31], v[224:227], 0\n\t"
            "ds_read_b64 v[222:223], v220 offset:160\n\t"
            "ds_read_b64 v[220:221], v220 offset:128\n\t"
            "v_mfma_f32_16x16x32_bf16 v[148:151], v[24:27], v[212:215], v[148:151]\n\t"
            "v_mul_f32 v176, v237, v176\n\t"
            "v_mul_f32 v177, v237, v177\n\t"
            "v_mfma_f32_16x16x32_bf16 v[196:199], v[48:51], v[212:215], 0\n\t"
            "v_mul_f32 v178, v237, v178\n\t"
            "v_mul_f32 v179, v237, v179\n\t"
            "v_mfma_f32_16x16x32_bf16 v[196:199], v[52:55], v[224:227], v[196:199]\n\t"
            "v_cvt_pk_bf16_f32 v176, v176, v177\n\t"
            "v_mul_f32 v168, v237, v168\n\t"
            "v_mfma_f32_16x16x32_bf16 v[200:203], v[76:79], v[224:227], 0\n\t"
            "v_mul_f32 v169, v237, v169\n\t"
            "v_cvt_pk_bf16_f32 v177, v178, v179\n\t"
            "v_mfma_f32_16x16x32_bf16 v[200:203], v[72:75], v[212:215], v[200:203]\n\t"
            "v_mul_f32 v170, v237, v170\n\t"
            "v_mul_f32 v171, v237, v171\n\t"
            "v_mfma_f32_16x16x32_bf16 v[160:163], v[96:99], v[212:215], 0\n\t"
            "v_cvt_pk_bf16_f32 v168, v168, v169\n\t"
            "v_cvt_pk_bf16_f32 v180, v180, v181\n\t"
            "v_mfma_f32_16x16x32_bf16 v[160:163], v[100:103], v[224:227], v[160:163]\n\t"
            "v_cvt_pk_bf16_f32 v178, v188, v189\n\t"
            "v_cvt_pk_bf16_f32 v179, v190, v191\n\t"
            "v_mfma_f32_16x16x32_bf16 v[164:167], v[124:127], v[224:227], 0\n\t"
            "ds_read_b128 v[224:227], v233 offset:12544\n\t"
            "v_mul_f32 v172, v237, v172\n\t"
            "v_mfma_f32_16x16x32_bf16 v[164:167], v[120:123], v[212:215], v[164:167]\n\t"
            "ds_read_b128 v[212:215], v232 offset:12544\n\t"
            "v_mul_f32 v173, v237, v173\n\t"
            "s_waitcnt lgkmcnt(4)\n\t"
            "v_mfma_f32_16x16x32_bf16 v[192:195], v[8:11], v[204:207], v[192:195]\n\t"
            "v_mul_f32 v174, v237, v174\n\t"
            "v_mul_f32 v175, v237, v175\n\t"
            "v_mfma_f32_16x16x32_bf16 v[192:195], v[12:15], v[208:211], v[192:195]\n\t"
            "v_cvt_pk_bf16_f32 v169, v170, v171\n\t"
            "v_cvt_pk_bf16_f32 v181, v182, v183\n\t"
            "v_mfma_f32_16x16x32_bf16 v[196:199], v[56:59], v[204:207], v[196:199]\n\t"
            "v_cvt_pk_bf16_f32 v170, v172, v173\n\t"
            "v_cvt_pk_bf16_f32 v171, v174, v175\n\t"
            "v_mfma_f32_16x16x32_bf16 v[196:199], v[60:63], v[208:211], v[196:199]\n\t"
            "v_cvt_pk_bf16_f32 v182, v184, v185\n\t"
            "v_cvt_pk_bf16_f32 v183, v186, v187\n\t"
            "v_mfma_f32_16x16x32_bf16 v[160:163], v[104:107], v[204:207], v[160:163]\n\t"
            "v_mov_b32 v234, s91\n\t"
            "v_mov_b32 v230, s76\n\t"
            "v_mfma_f32_16x16x32_bf16 v[160:163], v[108:111], v[208:211], v[160:163]\n\t"
            "ds_read_b32 v234, v234\n\t"
            "ds_read_b32 v230, v230\n\t"
            "v_mfma_f32_16x16x32_bf16 v[148:151], v[36:39], v[208:211], v[148:151]\n\t"
            "v_mul_f32 v237, 0x3e553b94, v237\n\t"
            "v_mov_b32 v239, s74\n\t"
            "v_mfma_f32_16x16x32_bf16 v[148:151], v[32:35], v[204:207], v[148:151]\n\t"
            "s_add_u32 s70, s70, 1\n\t"
            "s_add_u32 s72, s72, 800\n\t"
            "s_add_u32 s74, s74, 800\n\t"
            "s_add_u32 s76, s76, 800\n\t"
            "v_mfma_f32_16x16x32_bf16 v[200:203], v[84:87], v[208:211], v[200:203]\n\t"
            "s_add_u32 s77, s77, 384\n\t"
            "s_xor_b32 s48, s48, s47\n\t"
            "s_add_u32 s91, s91, 4\n\t"
            "v_mfma_f32_16x16x32_bf16 v[200:203], v[80:83], v[204:207], v[200:203]\n\t"
            "ds_read_b32 v239, v239\n\t"
            "v_mfma_f32_16x16x32_bf16 v[164:167], v[132:135], v[208:211], v[164:167]\n\t"
            "v_mfma_f32_16x16x32_bf16 v[164:167], v[128:131], v[204:207], v[164:167]\n\t"
            "s_waitcnt lgkmcnt(3)\n\t"
            "v_mfma_f32_16x16x32_bf16 v[192:195], v[16:19], v[212:215], v[192:195]\n\t"
            "v_mfma_f32_16x16x32_bf16 v[192:195], v[20:23], v[224:227], v[192:195]\n\t"
            "v_mfma_f32_16x16x32_bf16 v[196:199], v[64:67], v[212:215], v[196:199]\n\t"
            "s_waitcnt lgkmcnt(2)\n\t"
            "v_readfirstlane_b32 s44, v234\n\t"
            "v_mfma_f32_16x16x32_bf16 v[196:199], v[68:71], v[224:227], v[196:199]\n\t"
            "v_mfma_f32_16x16x32_bf16 v[160:163], v[112:115], v[212:215], v[160:163]\n\t"
            "v_mfma_f32_16x16x32_bf16 v[160:163], v[116:119], v[224:227], v[160:163]\n\t"
            "v_mfma_f32_16x16x32_bf16 v[148:151], v[44:47], v[224:227], v[148:151]\n\t"
            "s_nop 0\n\t"
            "v_mul_f32 v192, v238, v192\n\t"
            "v_mfma_f32_16x16x32_bf16 v[148:151], v[40:43], v[212:215], v[148:151]\n\t"
            "v_mul_f32 v193, v238, v193\n\t"
            "v_mul_f32 v194, v238, v194\n\t"
            "v_mfma_f32_16x16x32_bf16 v[200:203], v[92:95], v[224:227], v[200:203]\n\t"
            "v_mul_f32 v195, v238, v195\n\t"
            "v_cvt_pk_bf16_f32 v192, v192, v193\n\t"
            "v_mfma_f32_16x16x32_bf16 v[200:203], v[88:91], v[212:215], v[200:203]\n\t"
            "v_cvt_pk_bf16_f32 v193, v194, v195\n\t"
            "v_mul_f32 v196, v238, v196\n\t"
            "v_mfma_f32_16x16x32_bf16 v[164:167], v[140:143], v[224:227], v[164:167]\n\t"
            "ds_read_b128 v[224:227], v216 offset:64\n\t"
            "ds_read_b128 v[216:219], v216\n\t"
            "v_mfma_f32_16x16x32_bf16 v[164:167], v[136:139], v[212:215], v[164:167]\n\t"
            "v_mul_f32 v148, v238, v148\n\t"
            "v_mul_f32 v149, v238, v149\n\t"
            "v_mfma_f32_16x16x16_bf16 v[172:175], v[220:221], v[154:155], 0\n\t"
            "v_mul_f32 v150, v238, v150\n\t"
            "v_mul_f32 v151, v238, v151\n\t"
            "v_mul_f32 v197, v238, v197\n\t"
            "v_mfma_f32_16x16x16_bf16 v[184:187], v[152:153], v[178:179], 0\n\t"
            "v_cvt_pk_bf16_f32 v194, v148, v149\n\t"
            "v_cvt_pk_bf16_f32 v195, v150, v151\n\t"
            "v_cvt_pk_bf16_f32 v200, v200, v201\n\t"
            "v_mfma_f32_16x16x16_bf16 v[148:151], v[152:153], v[154:155], 0\n\t"
            "v_mul_f32 v198, v238, v198\n\t"
            "v_mul_f32 v199, v238, v199\n\t"
            "v_cvt_pk_bf16_f32 v196, v196, v197\n\t"
            "v_mfma_f32_16x16x16_bf16 v[208:211], v[220:221], v[178:179], 0\n\t"
            "v_cvt_pk_bf16_f32 v201, v202, v203\n\t"
            "v_cvt_pk_bf16_f32 v197, v198, v199\n\t"
            "v_cvt_pk_bf16_f32 v202, v160, v161\n\t"
            "v_mfma_f32_16x16x16_bf16 v[204:207], v[176:177], v[178:179], 0\n\t"
            "v_cvt_pk_bf16_f32 v203, v162, v163\n\t"
            "v_cvt_pk_bf16_f32 v198, v164, v165\n\t"
            "v_cvt_pk_bf16_f32 v199, v166, v167\n\t"
            "v_mfma_f32_16x16x16_bf16 v[160:163], v[176:177], v[154:155], 0\n\t"
            "v_mul_f32 v238, 0x3e553b94, v238\n\t"
            "s_setprio 0\n\t"
            "s_setprio 3\n\t"
            "v_mfma_f32_16x16x16_bf16 v[164:167], v[196:197], v[154:155], 0\n\t"
            "v_mfma_f32_16x16x16_bf16 v[188:191], v[196:197], v[178:179], 0\n\t"
            "v_mfma_f32_16x16x32_bf16 v[148:151], v[144:147], v[156:159], v[148:151]\n\t"
            "s_waitcnt lgkmcnt(0)\n\t"
            "v_mfma_f32_16x16x32_bf16 v[172:175], v[216:219], v[156:159], v[172:175]\n\t"
            "v_mfma_f32_16x16x32_bf16 v[184:187], v[144:147], v[180:183], v[184:187]\n\t"
            "v_mfma_f32_16x16x32_bf16 v[208:211], v[216:219], v[180:183], v[208:211]\n\t"
            "v_mfma_f32_16x16x32_bf16 v[160:163], v[168:171], v[156:159], v[160:163]\n\t"
            "v_mfma_f32_16x16x32_bf16 v[204:207], v[168:171], v[180:183], v[204:207]\n\t"
            "v_mfma_f32_16x16x32_bf16 v[164:167], v[192:195], v[156:159], v[164:167]\n\t"
            "v_mfma_f32_16x16x32_bf16 v[188:191], v[192:195], v[180:183], v[188:191]\n\t"
            "s_nop 1\n\t"
            "v_max3_f32 v228, v172, v148, v149\n\t"
            "v_mfma_f32_16x16x16_bf16 v[212:215], v[222:223], v[152:153], 0\n\t"
            "v_max3_f32 v228, v228, v150, v151\n\t"
            "v_max3_f32 v229, v208, v184, v185\n\t"
            "v_mfma_f32_16x16x16_bf16 v[156:159], v[222:223], v[196:197], 0\n\t"
            "v_max3_f32 v229, v229, v186, v187\n\t"
            "v_max3_f32 v228, v228, v160, v161\n\t"
            "v_max3_f32 v229, v229, v204, v205\n\t"
            "v_mfma_f32_16x16x16_bf16 v[180:183], v[152:153], v[198:199], 0\n\t"
            "v_max3_f32 v228, v228, v162, v163\n\t"
            "v_max3_f32 v229, v229, v206, v207\n\t"
            "v_max3_f32 v228, v228, v164, v165\n\t"
            "v_mfma_f32_16x16x32_bf16 v[212:215], v[224:227], v[144:147], v[212:215]\n\t"
            "v_max3_f32 v229, v229, v188, v189\n\t"
            "v_max3_f32 v228, v228, v166, v167\n\t"
            "v_mfma_f32_16x16x32_bf16 v[156:159], v[224:227], v[192:195], v[156:159]\n\t"
            "v_max3_f32 v229, v229, v190, v191\n\t"
            "v_mfma_f32_16x16x32_bf16 v[180:183], v[144:147], v[200:203], v[180:183]\n\t"
            "s_setprio 0\n\t"
            "v_permlane32_swap_b32 v228, v229\n\t"
            "v_max_f32 v228, v228, v229\n\t"
            "v_mov_b32 v231, v228\n\t"
            "s_nop 1\n\t"
            "v_permlane16_swap_b32 v228, v231\n\t"
            "v_max_f32 v228, v228, v231\n\t"
            "v_mov_b32 v229, v228\n\t"
            "s_nop 1\n\t"
            "v_permlane32_swap_b32 v228, v229\n\t"
            "v_mul_f32_e64 v228, -v228, v236\n\t"
            "v_fma_f32 v148, v148, v236, v228\n\t"
            "v_fma_f32 v149, v149, v236, v228\n\t"
            "v_exp_f32 v148, v148\n\t"
            "v_exp_f32 v149, v149\n\t"
            "v_fma_f32 v150, v150, v236, v228\n\t"
            "v_fma_f32 v151, v151, v236, v228\n\t"
            "v_exp_f32 v150, v150\n\t"
            "v_exp_f32 v151, v151\n\t"
            "v_fma_f32 v160, v160, v236, v228\n\t"
            "v_fma_f32 v161, v161, v236, v228\n\t"
            "v_fma_f32 v162, v162, v236, v228\n\t"
            "v_fma_f32 v163, v163, v236, v228\n\t"
            "v_mul_f32_e64 v229, -v229, v237\n\t"
            "v_exp_f32 v160, v160\n\t"
            "v_exp_f32 v161, v161\n\t"
            "v_exp_f32 v162, v162\n\t"
            "v_exp_f32 v163, v163\n\t"
            "v_fma_f32 v188, v188, v237, v229\n\t"
            "v_fma_f32 v189, v189, v237, v229\n\t"
            "v_cvt_pk_bf16_f32 v148, v148, v149\n\t"
            "v_exp_f32 v188, v188\n\t"
            "v_exp_f32 v189, v189\n\t"
            "v_fma_f32 v190, v190, v237, v229\n\t"
            "v_fma_f32 v191, v191, v237, v229\n\t"
            "v_fma_f32 v208, v208, v237, v229\n\t"
            "v_cvt_pk_bf16_f32 v149, v150, v151\n\t"
            "v_fma_f32 v184, v184, v237, v229\n\t"
            "v_fma_f32 v185, v185, v237, v229\n\t"
            "v_exp_f32 v190, v190\n\t"
            "v_exp_f32 v191, v191\n\t"
            "v_exp_f32 v208, v208\n\t"
            "v_cvt_pk_bf16_f32 v150, v160, v161\n\t"
            "v_cvt_pk_bf16_f32 v151, v162, v163\n\t"
            "s_setprio 3\n\t"
            "v_mfma_f32_16x16x16_bf16 v[160:163], v[222:223], v[176:177], 0\n\t"
            "s_setprio 0\n\t"
            "v_exp_f32 v184, v184\n\t"
            "v_exp_f32 v185, v185\n\t"
            "v_fma_f32 v186, v186, v237, v229\n\t"
            "v_fma_f32 v187, v187, v237, v229\n\t"
            "v_fma_f32 v164, v164, v236, v228\n\t"
            "v_fma_f32 v165, v165, v236, v228\n\t"
            "s_setprio 3\n\t"
            "v_mfma_f32_16x16x32_bf16 v[160:163], v[224:227], v[168:171], v[160:163]\n\t"
            "s_setprio 0\n\t"
            "v_exp_f32 v186, v186\n\t"
            "v_exp_f32 v187, v187\n\t"
            "v_fma_f32 v204, v204, v237, v229\n\t"
            "v_fma_f32 v205, v205, v237, v229\n\t"
            "v_fma_f32 v206, v206, v237, v229\n\t"
            "v_fma_f32 v207, v207, v237, v229\n\t"
            "v_cvt_pk_bf16_f32 v188, v188, v189\n\t"
            "v_exp_f32 v164, v164\n\t"
            "v_exp_f32 v165, v165\n\t"
            "v_fma_f32 v166, v166, v236, v228\n\t"
            "v_fma_f32 v167, v167, v236, v228\n\t"
            "v_fma_f32 v172, v172, v236, v228\n\t"
            "v_exp_f32 v204, v204\n\t"
            "v_exp_f32 v205, v205\n\t"
            "v_exp_f32 v206, v206\n\t"
            "v_exp_f32 v207, v207\n\t"
            "v_cvt_pk_bf16_f32 v189, v190, v191\n\t"
            "v_cvt_pk_bf16_f32 v208, v208, 0\n\t"
            "v_exp_f32 v166, v166\n\t"
            "v_exp_f32 v167, v167\n\t"
            "v_exp_f32 v172, v172\n\t"
            "v_cvt_pk_bf16_f32 v184, v184, v185\n\t"
            "v_cndmask_b32_e64 v190, 0, v208, s[58:59]\n\t"
            "s_setprio 3\n\t"
            "v_mfma_f32_16x16x16_bf16 v[208:211], v[220:221], v[198:199], 0\n\t"
            "v_cvt_pk_bf16_f32 v185, v186, v187\n\t"
            "v_cvt_pk_bf16_f32 v164, v164, v165\n\t"
            "v_cvt_pk_bf16_f32 v186, v204, v205\n\t"
            "v_cvt_pk_bf16_f32 v187, v206, v207\n\t"
            "v_mfma_f32_16x16x16_bf16 v[204:207], v[176:177], v[198:199], 0\n\t"
            "v_cvt_pk_bf16_f32 v165, v166, v167\n\t"
            "v_cvt_pk_bf16_f32 v172, v172, 0\n\t"
            "v_cndmask_b32_e64 v166, 0, v172, s[58:59]\n\t"
            "v_mfma_f32_16x16x16_bf16 v[172:175], v[196:197], v[198:199], 0\n\t"
            "v_cndmask_b32_e64 v235, v212, v160, s[60:61]\n\t"
            "v_max3_f32 v234, v212, v160, v156\n\t"
            "v_cndmask_b32_e64 v235, v235, v156, s[62:63]\n\t"
            "v_mfma_f32_16x16x32_bf16 v[208:211], v[216:219], v[200:203], v[208:211]\n\t"
            "v_max_f32 v234, v234, v230\n\t"
            "v_cndmask_b32_e64 v235, v235, v230, s[64:65]\n\t"
            "v_mfma_f32_16x16x32_bf16 v[204:207], v[168:171], v[200:203], v[204:207]\n\t"
            "v_max_f32_dpp v234, v234, v234 row_ror:8 row_mask:0xf bank_mask:0xf\n\t"
            "v_mov_b32 v167, 0\n\t"
            "v_mfma_f32_16x16x32_bf16 v[172:175], v[192:195], v[200:203], v[172:175]\n\t"
            "s_setprio 0\n\t"
            "v_max_f32_dpp v234, v234, v234 row_ror:4 row_mask:0xf bank_mask:0xf\n\t"
            "v_mov_b32 v191, 0\n\t"
            "v_max3_f32 v230, v208, v180, v181\n\t"
            "v_max3_f32 v230, v230, v182, v183\n\t"
            "v_max_f32_dpp v234, v234, v234 row_ror:2 row_mask:0xf bank_mask:0xf\n\t"
            "v_max3_f32 v230, v230, v204, v205\n\t"
            "v_max3_f32 v230, v230, v206, v207\n\t"
            "v_max_f32_dpp v234, v234, v234 row_ror:1 row_mask:0xf bank_mask:0xf\n\t"
            "v_max3_f32 v230, v230, v172, v173\n\t"
            "v_max3_f32 v230, v230, v174, v175\n\t"
            "v_mov_b32 v231, v230\n\t"
            "v_sub_f32 v235, v235, v234\n\t"
            "v_mul_f32 v235, v239, v235\n\t"
            "v_permlane32_swap_b32 v230, v231\n\t"
            "v_max_f32 v230, v230, v231\n\t"
            "v_mov_b32 v231, v230\n\t"
            "v_exp_f32 v235, v235\n\t"
            "s_nop 0\n\t"
            "v_permlane16_swap_b32 v230, v231\n\t"
            "v_max_f32 v230, v230, v231\n\t"
            "v_mul_f32_e64 v230, -v230, v238\n\t"
            "v_fma_f32 v180, v180, v238, v230\n\t"
            "v_fma_f32 v181, v181, v238, v230\n\t"
            "v_fma_f32 v182, v182, v238, v230\n\t"
            "v_fma_f32 v183, v183, v238, v230\n\t"
            "v_fma_f32 v204, v204, v238, v230\n\t"
            "v_fma_f32 v205, v205, v238, v230\n\t"
            "v_fma_f32 v206, v206, v238, v230\n\t"
            "v_fma_f32 v207, v207, v238, v230\n\t"
            "v_fma_f32 v172, v172, v238, v230\n\t"
            "v_fma_f32 v173, v173, v238, v230\n\t"
            "v_fma_f32 v174, v174, v238, v230\n\t"
            "v_fma_f32 v175, v175, v238, v230\n\t"
            "v_fma_f32 v208, v208, v238, v230\n\t"
            "v_cvt_pk_bf16_f32 v235, v235, 0\n\t"
            "v_cndmask_b32_e64 v235, 0, v235, s[66:67]\n\t"
            "v_exp_f32 v180, v180\n\t"
            "v_exp_f32 v181, v181\n\t"
            "v_exp_f32 v172, v172\n\t"
            "v_exp_f32 v173, v173\n\t"
            "v_exp_f32 v182, v182\n\t"
            "v_exp_f32 v183, v183\n\t"
            "v_exp_f32 v174, v174\n\t"
            "v_exp_f32 v175, v175\n\t"
            "v_exp_f32 v208, v208\n\t"
            "v_exp_f32 v204, v204\n\t"
            "v_exp_f32 v205, v205\n\t"
            "v_exp_f32 v206, v206\n\t"
            "v_exp_f32 v207, v207\n\t"
            "v_cvt_pk_bf16_f32 v180, v180, v181\n\t"
            "v_cvt_pk_bf16_f32 v172, v172, v173\n\t"
            "v_cvt_pk_bf16_f32 v181, v182, v183\n\t"
            "v_cvt_pk_bf16_f32 v173, v174, v175\n\t"
            "v_cvt_pk_bf16_f32 v208, v208, 0\n\t"
            "v_cvt_pk_bf16_f32 v182, v204, v205\n\t"
            "v_cvt_pk_bf16_f32 v183, v206, v207\n\t"
            "v_cndmask_b32_e64 v174, 0, v208, s[58:59]\n\t"
            "v_mov_b32 v175, 0\n\t"
            "ds_write_b16 v250, v235 offset:256\n\t"
            "s_barrier\n\t"
            "s_setprio 2\n\t"
            "s_cmp_eq_u32 s44, 0x7ffffff\n\t"
            "s_cbranch_scc1 .Lwa_last%=\n\t"
            "v_mfma_f32_16x16x32_bf16 v[200:203], v[240:243], v[148:151], 0\n\t"
            "s_cmp_ge_u32 s70, s80\n\t"
            "s_cbranch_scc1 .Lwa_h1%=\n\t"
            ".Lwa_r1%=:\n\t"
            "ds_read_b64 v[176:177], v247\n\t"
            "v_mfma_f32_16x16x32_bf16 v[200:203], v[240:243], v[164:167], v[200:203]\n\t"
            "ds_read_b64 v[196:197], v247 offset:1536\n\t"
            "ds_read_b64 v[220:221], v247 offset:3072\n\t"
            "v_xor_b32 v234, s73, v254\n\t"
            "v_add_u32 v235, s45, v248\n\t"
            "v_add_u32 v232, s48, v244\n\t"
            "v_xor_b32 v233, 64, v232\n\t"
            "v_mov_b32 v179, 0\n\t"
            "v_mov_b32 v199, 0\n\t"
            "v_mov_b32 v223, 0\n\t"
            "v_rcp_f32 v201, v200\n\t"
            "v_xor_b32 v249, 0x100, v249\n\t"
            "ds_read_b128 v[168:171], v235\n\t"
            "ds_read_u16 v178, v234\n\t"
            "ds_read_b128 v[192:195], v235 offset:1536\n\t"
            "ds_read_u16 v198, v234 offset:128\n\t"
            "ds_read_b128 v[216:219], v235 offset:3072\n\t"
            "ds_read_u16 v222, v234 offset:256\n\t"
            "ds_read_b128 v[204:207], v232\n\t"
            "ds_read_b128 v[208:211], v233\n\t"
            "ds_read_b32 v236, v249\n\t"
            "ds_read_b32 v237, v249 offset:64\n\t"
            "ds_read_b32 v238, v249 offset:128\n\t"
            "v_mad_u32_u24 v234, v246, s75, v253\n\t"
            "s_waitcnt lgkmcnt(9)\n\t"
            "v_mfma_f32_16x16x32_bf16 v[224:227], v[168:171], v[148:151], 0\n\t"
            "v_mfma_f32_16x16x32_bf16 v[224:227], v[176:179], v[164:167], v[224:227]\n\t"
            "s_waitcnt lgkmcnt(7)\n\t"
            "v_mfma_f32_16x16x32_bf16 v[212:215], v[192:195], v[148:151], 0\n\t"
            "v_mfma_f32_16x16x32_bf16 v[212:215], v[196:199], v[164:167], v[212:215]\n\t"
            "s_waitcnt lgkmcnt(5)\n\t"
            "v_mfma_f32_16x16x32_bf16 v[148:151], v[216:219], v[148:151], 0\n\t"
            "v_mfma_f32_16x16x32_bf16 v[148:151], v[220:223], v[164:167], v[148:151]\n\t"
            "s_waitcnt lgkmcnt(3)\n\t"
            "v_mfma_f32_16x16x32_bf16 v[144:147], v[0:3], v[204:207], 0\n\t"
            "v_mfma_f32_16x16x32_bf16 v[144:147], v[4:7], v[208:211], v[144:147]\n\t"
            "v_mul_f32 v224, v201, v224\n\t"
            "v_mfma_f32_16x16x32_bf16 v[152:155], v[48:51], v[204:207], 0\n\t"
            "v_mul_f32 v225, v201, v225\n\t"
            "v_mul_f32 v226, v201, v226\n\t"
            "v_mfma_f32_16x16x32_bf16 v[152:155], v[52:55], v[208:211], v[152:155]\n\t"
            "v_mul_f32 v212, v201, v212\n\t"
            "v_mul_f32 v213, v201, v213\n\t"
            "v_mfma_f32_16x16x32_bf16 v[156:159], v[76:79], v[208:211], 0\n\t"
            "v_mul_f32 v227, v201, v227\n\t"
            "v_cvt_pk_bf16_f32 v224, v224, v225\n\t"
            "v_mfma_f32_16x16x32_bf16 v[156:159], v[72:75], v[204:207], v[156:159]\n\t"
            "v_mul_f32 v214, v201, v214\n\t"
            "v_mul_f32 v215, v201, v215\n\t"
            "v_mfma_f32_16x16x32_bf16 v[160:163], v[96:99], v[204:207], 0\n\t"
            "v_cvt_pk_bf16_f32 v212, v212, v213\n\t"
            "v_cvt_pk_bf16_f32 v225, v226, v227\n\t"
            "v_mfma_f32_16x16x32_bf16 v[160:163], v[100:103], v[208:211], v[160:163]\n\t"
            "v_cvt_pk_bf16_f32 v213, v214, v215\n\t"
            "v_mul_f32 v148, v201, v148\n\t"
            "v_mfma_f32_16x16x32_bf16 v[164:167], v[124:127], v[208:211], 0\n\t"
            "v_mul_f32 v149, v201, v149\n\t"
            "v_mul_f32 v150, v201, v150\n\t"
            "v_mfma_f32_16x16x32_bf16 v[164:167], v[120:123], v[204:207], v[164:167]\n\t"
            "ds_write_b64 v245, v[224:225]\n\t"
            "v_mfma_f32_16x16x32_bf16 v[224:227], v[168:171], v[184:187], 0\n\t"
            "v_mul_f32 v151, v201, v151\n\t"
            "v_cvt_pk_bf16_f32 v148, v148, v149\n\t"
            "v_mfma_f32_16x16x32_bf16 v[224:227], v[176:179], v[188:191], v[224:227]\n\t"
            "ds_write_b64 v245, v[212:213] offset:128\n\t"
            "v_mfma_f32_16x16x32_bf16 v[200:203], v[240:243], v[184:187], 0\n\t"
            "v_cvt_pk_bf16_f32 v149, v150, v151\n\t"
            "v_mfma_f32_16x16x32_bf16 v[200:203], v[240:243], v[188:191], v[200:203]\n\t"
            "ds_write_b64 v245, v[148:149] offset:256\n\t"
            "v_mfma_f32_16x16x32_bf16 v[212:215], v[192:195], v[184:187], 0\n\t"
            "v_mfma_f32_16x16x32_bf16 v[212:215], v[196:199], v[188:191], v[212:215]\n\t"
            "v_mfma_f32_16x16x32_bf16 v[184:187], v[216:219], v[184:187], 0\n\t"
            "v_mfma_f32_16x16x32_bf16 v[184:187], v[220:223], v[188:191], v[184:187]\n\t"
            "v_mfma_f32_16x16x32_bf16 v[148:151], v[28:31], v[208:211], 0\n\t"
            "ds_read_b128 v[208:211], v233 offset:128\n\t"
            "v_mfma_f32_16x16x32_bf16 v[148:151], v[24:27], v[204:207], v[148:151]\n\t"
            "ds_read_b128 v[204:207], v232 offset:128\n\t"
            "v_rcp_f32 v201, v200\n\t"
            "v_mfma_f32_16x16x32_bf16 v[188:191], v[192:195], v[180:183], 0\n\t"
            "v_mul_f32 v224, v201, v224\n\t"
            "v_mul_f32 v225, v201, v225\n\t"
            "v_mfma_f32_16x16x32_bf16 v[188:191], v[196:199], v[172:175], v[188:191]\n\t"
            "v_mul_f32 v226, v201, v226\n\t"
            "v_mul_f32 v227, v201, v227\n\t"
            "v_mul_f32 v212, v201, v212\n\t"
            "v_mul_f32 v213, v201, v213\n\t"
            "v_mul_f32 v214, v201, v214\n\t"
            "v_mul_f32 v215, v201, v215\n\t"
            "v_mul_f32 v184, v201, v184\n\t"
            "v_mul_f32 v185, v201, v185\n\t"
            "v_mul_f32 v186, v201, v186\n\t"
            "v_mul_f32 v187, v201, v187\n\t"
            "v_mfma_f32_16x16x32_bf16 v[200:203], v[240:243], v[180:183], 0\n\t"
            "v_cvt_pk_bf16_f32 v224, v224, v225\n\t"
            "v_cvt_pk_bf16_f32 v225, v226, v227\n\t"
            "v_mfma_f32_16x16x32_bf16 v[200:203], v[240:243], v[172:175], v[200:203]\n\t"
            "ds_write_b64 v245, v[224:225] offset:6144\n\t"
            "v_mfma_f32_16x16x32_bf16 v[224:227], v[168:171], v[180:183], 0\n\t"
            "v_cvt_pk_bf16_f32 v184, v184, v185\n\t"
            "v_cvt_pk_bf16_f32 v185, v186, v187\n\t"
            "v_mfma_f32_16x16x32_bf16 v[224:227], v[176:179], v[172:175], v[224:227]\n\t"
            "ds_read_b64 v[186:187], v234 offset:288\n\t"
            "ds_write_b64 v245, v[184:185] offset:6400\n\t"
            "s_waitcnt lgkmcnt(3)\n\t"
            "v_mfma_f32_16x16x32_bf16 v[144:147], v[8:11], v[204:207], v[144:147]\n\t"
            "ds_read_b64 v[184:185], v234 offset:256\n\t"
            "v_rcp_f32 v201, v200\n\t"
            "v_mfma_f32_16x16x32_bf16 v[144:147], v[12:15], v[208:211], v[144:147]\n\t"
            "v_mul_f32 v188, v201, v188\n\t"
            "v_mul_f32 v189, v201, v189\n\t"
            "v_mfma_f32_16x16x32_bf16 v[148:151], v[36:39], v[208:211], v[148:151]\n\t"
            "v_mul_f32 v190, v201, v190\n\t"
            "v_mul_f32 v191, v201, v191\n\t"
            "v_mfma_f32_16x16x32_bf16 v[148:151], v[32:35], v[204:207], v[148:151]\n\t"
            "v_cvt_pk_bf16_f32 v188, v188, v189\n\t"
            "v_cvt_pk_bf16_f32 v189, v190, v191\n\t"
            "v_mfma_f32_16x16x32_bf16 v[152:155], v[56:59], v[204:207], v[152:155]\n\t"
            "ds_read_b64 v[190:191], v234 offset:352\n\t"
            "v_mul_f32 v224, v201, v224\n\t"
            "v_mfma_f32_16x16x32_bf16 v[152:155], v[60:63], v[208:211], v[152:155]\n\t"
            "ds_write_b64 v245, v[188:189] offset:12416\n\t"
            "v_mfma_f32_16x16x32_bf16 v[156:159], v[84:87], v[208:211], v[156:159]\n\t"
            "ds_read_b64 v[188:189], v234 offset:320\n\t"
            "v_mul_f32 v225, v201, v225\n\t"
            "v_mfma_f32_16x16x32_bf16 v[156:159], v[80:83], v[204:207], v[156:159]\n\t"
            "v_cvt_pk_bf16_f32 v212, v212, v213\n\t"
            "v_mul_f32 v226, v201, v226\n\t"
            "v_mfma_f32_16x16x32_bf16 v[160:163], v[104:107], v[204:207], v[160:163]\n\t"
            "v_mul_f32 v227, v201, v227\n\t"
            "v_cvt_pk_bf16_f32 v224, v224, v225\n\t"
            "v_mfma_f32_16x16x32_bf16 v[160:163], v[108:111], v[208:211], v[160:163]\n\t"
            "v_cvt_pk_bf16_f32 v213, v214, v215\n\t"
            "v_cvt_pk_bf16_f32 v225, v226, v227\n\t"
            "v_mfma_f32_16x16x32_bf16 v[164:167], v[132:135], v[208:211], v[164:167]\n\t"
            "ds_read_b128 v[208:211], v233 offset:6144\n\t"
            "v_mfma_f32_16x16x32_bf16 v[164:167], v[128:131], v[204:207], v[164:167]\n\t"
            "ds_read_b128 v[204:207], v232 offset:6144\n\t"
            "ds_write_b64 v245, v[212:213] offset:6272\n\t"
            "v_mfma_f32_16x16x32_bf16 v[180:183], v[216:219], v[180:183], 0\n\t"
            "ds_read_b128 v[212:215], v232 offset:256\n\t"
            "v_mfma_f32_16x16x32_bf16 v[180:183], v[220:223], v[172:175], v[180:183]\n\t"
            "ds_write_b64 v245, v[224:225] offset:12288\n\t"
            "ds_read_b128 v[224:227], v233 offset:256\n\t"
            "s_waitcnt lgkmcnt(6)\n\t"
            "v_mfma_f32_16x16x32_bf16 v[168:171], v[184:187], v[168:171], 0\n\t"
            "v_mfma_f32_16x16x32_bf16 v[168:171], v[188:191], v[176:179], v[168:171]\n\t"
            "s_cmp_ge_u32 s70, s80\n\t"
            "s_cbranch_scc1 .Lwa_h2%=\n\t"
            ".Lwa_r2%=:\n\t"
            "v_mfma_f32_16x16x32_bf16 v[192:195], v[184:187], v[192:195], 0\n\t"
            "v_mul_f32 v180, v201, v180\n\t"
            "v_mul_f32 v181, v201, v181\n\t"
            "v_mfma_f32_16x16x32_bf16 v[192:195], v[188:191], v[196:199], v[192:195]\n\t"
            "v_mul_f32 v182, v201, v182\n\t"
            "v_mul_f32 v183, v201, v183\n\t"
            "v_mfma_f32_16x16x32_bf16 v[200:203], v[240:243], v[184:187], 0\n\t"
            "v_cvt_pk_bf16_f32 v180, v180, v181\n\t"
            "v_cvt_pk_bf16_f32 v181, v182, v183\n\t"
            "v_mfma_f32_16x16x32_bf16 v[200:203], v[240:243], v[188:191], v[200:203]\n\t"
            "ds_write_b64 v245, v[180:181] offset:12544\n\t"
            "v_mfma_f32_16x16x32_bf16 v[216:219], v[184:187], v[216:219], 0\n\t"
            "v_mfma_f32_16x16x32_bf16 v[216:219], v[188:191], v[220:223], v[216:219]\n\t"
            "s_waitcnt lgkmcnt(1)\n\t"
            "v_mfma_f32_16x16x32_bf16 v[144:147], v[16:19], v[212:215], v[144:147]\n\t"
            "v_mfma_f32_16x16x32_bf16 v[144:147], v[20:23], v[224:227], v[144:147]\n\t"
            "v_mfma_f32_16x16x32_bf16 v[148:151], v[44:47], v[224:227], v[148:151]\n\t"
            "s_nop 0\n\t"
            "v_cndmask_b32_e64 v203, v168, v192, s[60:61]\n\t"
            "v_mfma_f32_16x16x32_bf16 v[148:151], v[40:43], v[212:215], v[148:151]\n\t"
            "v_rcp_f32 v201, v200\n\t"
            "v_mfma_f32_16x16x32_bf16 v[152:155], v[64:67], v[212:215], v[152:155]\n\t"
            "v_cndmask_b32_e64 v203, v203, v216, s[62:63]\n\t"
            "v_xad_u32 v202, s73, v254, v255\n\t"
            "v_mfma_f32_16x16x32_bf16 v[152:155], v[68:71], v[224:227], v[152:155]\n\t"
            "v_mul_f32_dpp v203, v201, v203 quad_perm:[0,0,0,0] row_mask:0xf bank_mask:0xf\n\t"
            "v_cvt_pk_bf16_f32 v203, v203, 0\n\t"
            "v_mfma_f32_16x16x32_bf16 v[156:159], v[92:95], v[224:227], v[156:159]\n\t"
            "v_mul_f32 v144, v236, v144\n\t"
            "v_mul_f32 v145, v236, v145\n\t"
            "v_mfma_f32_16x16x32_bf16 v[156:159], v[88:91], v[212:215], v[156:159]\n\t"
            "s_mov_b64 exec, s[68:69]\n\t"
            "ds_write_b16 v202, v203\n\t"
            "s_mov_b64 exec, 1\n\t"
            "ds_add_u32 v253, v246 offset:7856\n\t"
            "s_mov_b64 exec, -1\n\t"
            "v_mfma_f32_16x16x32_bf16 v[160:163], v[112:115], v[212:215], v[160:163]\n\t"
            "v_mul_f32 v152, v236, v152\n\t"
            "v_mul_f32 v153, v236, v153\n\t"
            "v_mfma_f32_16x16x32_bf16 v[160:163], v[116:119], v[224:227], v[160:163]\n\t"
            "v_mul_f32 v146, v236, v146\n\t"
            "v_mul_f32 v147, v236, v147\n\t"
            "v_mfma_f32_16x16x32_bf16 v[164:167], v[140:143], v[224:227], v[164:167]\n\t"
            "ds_read_b128 v[224:227], v233 offset:6272\n\t"
            "v_mul_f32 v154, v236, v154\n\t"
            "v_mfma_f32_16x16x32_bf16 v[164:167], v[136:139], v[212:215], v[164:167]\n\t"
            "ds_read_b128 v[212:215], v232 offset:6272\n\t"
            "v_mul_f32 v155, v236, v155\n\t"
            "v_mfma_f32_16x16x32_bf16 v[168:171], v[0:3], v[204:207], 0\n\t"
            "v_cvt_pk_bf16_f32 v144, v144, v145\n\t"
            "v_cvt_pk_bf16_f32 v152, v152, v153\n\t"
            "v_mfma_f32_16x16x32_bf16 v[168:171], v[4:7], v[208:211], v[168:171]\n\t"
            "v_cvt_pk_bf16_f32 v156, v156, v157\n\t"
            "v_mul_f32 v148, v236, v148\n\t"
            "v_mfma_f32_16x16x32_bf16 v[172:175], v[28:31], v[208:211], 0\n\t"
            "v_mul_f32 v149, v236, v149\n\t"
            "v_mul_f32 v150, v236, v150\n\t"
            "v_mfma_f32_16x16x32_bf16 v[172:175], v[24:27], v[204:207], v[172:175]\n\t"
            "v_mul_f32 v151, v236, v151\n\t"
            "v_cvt_pk_bf16_f32 v145, v146, v147\n\t"
            "v_mfma_f32_16x16x32_bf16 v[176:179], v[48:51], v[204:207], 0\n\t"
            "v_cvt_pk_bf16_f32 v153, v154, v155\n\t"
            "v_cvt_pk_bf16_f32 v157, v158, v159\n\t"
            "v_mfma_f32_16x16x32_bf16 v[176:179], v[52:55], v[208:211], v[176:179]\n\t"
            "v_cvt_pk_bf16_f32 v146, v148, v149\n\t"
            "v_cvt_pk_bf16_f32 v147, v150, v151\n\t"
            "v_mfma_f32_16x16x32_bf16 v[180:183], v[76:79], v[208:211], 0\n\t"
            "v_cvt_pk_bf16_f32 v158, v160, v161\n\t"
            "v_cvt_pk_bf16_f32 v159, v162, v163\n\t"
            "v_mfma_f32_16x16x32_bf16 v[180:183], v[72:75], v[204:207], v[180:183]\n\t"
            "v_cvt_pk_bf16_f32 v154, v164, v165\n\t"
            "v_cvt_pk_bf16_f32 v155, v166, v167\n\t"
            "v_mfma_f32_16x16x32_bf16 v[184:187], v[96:99], v[204:207], 0\n\t"
            "v_mul_f32 v236, 0x3e553b94, v236\n\t"
            "v_mfma_f32_16x16x32_bf16 v[184:187], v[100:103], v[208:211], v[184:187]\n\t"
            "v_mfma_f32_16x16x32_bf16 v[188:191], v[124:127], v[208:211], 0\n\t"
            "ds_read_b128 v[208:211], v233 offset:6400\n\t"
            "v_mfma_f32_16x16x32_bf16 v[188:191], v[120:123], v[204:207], v[188:191]\n\t"
            "ds_read_b128 v[204:207], v232 offset:6400\n\t"
            "s_waitcnt lgkmcnt(2)\n\t"
            "v_mfma_f32_16x16x32_bf16 v[168:171], v[8:11], v[212:215], v[168:171]\n\t"
            "v_mfma_f32_16x16x32_bf16 v[168:171], v[12:15], v[224:227], v[168:171]\n\t"
            "v_mfma_f32_16x16x32_bf16 v[172:175], v[36:39], v[224:227], v[172:175]\n\t"
            "v_mfma_f32_16x16x32_bf16 v[172:175], v[32:35], v[212:215], v[172:175]\n\t"
            "v_mfma_f32_16x16x32_bf16 v[176:179], v[56:59], v[212:215], v[176:179]\n\t"
            "v_mfma_f32_16x16x32_bf16 v[176:179], v[60:63], v[224:227], v[176:179]\n\t"
            "v_mfma_f32_16x16x32_bf16 v[180:183], v[84:87], v[224:227], v[180:183]\n\t"
            "v_mfma_f32_16x16x32_bf16 v[180:183], v[80:83], v[212:215], v[180:183]\n\t"
            "v_mfma_f32_16x16x32_bf16 v[184:187], v[104:107], v[212:215], v[184:187]\n\t"
            "v_mfma_f32_16x16x32_bf16 v[184:187], v[108:111], v[224:227], v[184:187]\n\t"
            "v_mfma_f32_16x16x32_bf16 v[188:191], v[132:135], v[224:227], v[188:191]\n\t"
            "ds_read_b128 v[224:227], v233 offset:12288\n\t"
            "v_mfma_f32_16x16x32_bf16 v[188:191], v[128:131], v[212:215], v[188:191]\n\t"
            "ds_read_b128 v[212:215], v232 offset:12288\n\t"
            "s_waitcnt lgkmcnt(2)\n\t"
            "v_mfma_f32_16x16x32_bf16 v[168:171], v[16:19], v[204:207], v[168:171]\n\t"
            "v_mfma_f32_16x16x32_bf16 v[168:171], v[20:23], v[208:211], v[168:171]\n\t"
            "v_mfma_f32_16x16x32_bf16 v[172:175], v[44:47], v[208:211], v[172:175]\n\t"
            "v_mfma_f32_16x16x32_bf16 v[172:175], v[40:43], v[204:207], v[172:175]\n\t"
            "v_mfma_f32_16x16x32_bf16 v[176:179], v[64:67], v[204:207], v[176:179]\n\t"
            "v_mfma_f32_16x16x32_bf16 v[176:179], v[68:71], v[208:211], v[176:179]\n\t"
            "v_mfma_f32_16x16x32_bf16 v[180:183], v[92:95], v[208:211], v[180:183]\n\t"
            "v_mfma_f32_16x16x32_bf16 v[180:183], v[88:91], v[204:207], v[180:183]\n\t"
            "v_mfma_f32_16x16x32_bf16 v[184:187], v[112:115], v[204:207], v[184:187]\n\t"
            "v_mfma_f32_16x16x32_bf16 v[184:187], v[116:119], v[208:211], v[184:187]\n\t"
            "v_mfma_f32_16x16x32_bf16 v[188:191], v[140:143], v[208:211], v[188:191]\n\t"
            "ds_read_b128 v[208:211], v233 offset:12416\n\t"
            "v_mfma_f32_16x16x32_bf16 v[188:191], v[136:139], v[204:207], v[188:191]\n\t"
            "ds_read_b128 v[204:207], v232 offset:12416\n\t"
            "s_cmp_ge_u32 s70, s80\n\t"
            "s_cbranch_scc1 .Lwa_h3%=\n\t"
            "s_branch .Lwa_loop%=\n\t"
            ".Lwa_h1%=:\n\t"
            "s_cmp_eq_u32 s90, 0\n\t"
            "s_cbranch_scc1 .Lwa_r1%=\n\t"
            "s_nop 4\n\t"
            "v_mov_b32 v228, 1\n\t"
            "s_mov_b32 s49, 301184\n\t"
            "s_mov_b64 exec, 1\n\t"
            "buffer_atomic_add v228, off, s[84:87], s49 sc0\n\t"
            "s_mov_b64 exec, -1\n\t"
            "s_branch .Lwa_r1%=\n\t"
            ".Lwa_h2%=:\n\t"
            "s_waitcnt vmcnt(0)\n\t"
            "s_mov_b32 s81, 0\n\t"
            "s_cmp_eq_u32 s90, 0\n\t"
            "s_cbranch_scc1 .Lwa_r2%=\n\t"
            "v_readfirstlane_b32 s49, v228\n\t"
            "s_sub_u32 s90, s90, 1\n\t"
            "s_cmp_lt_u32 s49, 209\n\t"
            "s_cselect_b32 s90, s90, 0\n\t"
            "s_cbranch_scc0 .Lwa_r2%=\n\t"
            "s_mul_i32 s81, s49, 1280\n\t"
            "s_add_u32 s81, s81, 303104\n\t"
            "buffer_load_dwordx4 v[228:231], off, s[84:87], s81 offset:1216 sc1\n\t"
            "s_branch .Lwa_r2%=\n\t"
            ".Lwa_h3%=:\n\t"
            "s_cmp_eq_u32 s81, 0\n\t"
            "s_cbranch_scc1 .Lwa_loop%=\n\t"
            "s_mov_b32 s89, 0x10000\n\t"
            ".Lwa_h4%=:\n\t"
            "s_waitcnt vmcnt(0)\n\t"
            "v_readfirstlane_b32 s49, v228\n\t"
            "v_readfirstlane_b32 vcc_lo, v229\n\t"
            "s_xor_b32 s49, s49, s82\n\t"
            "s_xor_b32 vcc_lo, vcc_lo, s83\n\t"
            "s_or_b32 s49, s49, vcc_lo\n\t"
            "s_cbranch_scc0 .Lwa_h5%=\n\t"
            "s_sub_u32 s89, s89, 1\n\t"
            "s_cbranch_scc1 .Lwa_h5%=\n\t"
            "s_sleep 2\n\t"
            "buffer_load_dwordx4 v[228:231], off, s[84:87], s81 offset:1216 sc1\n\t"
            "s_branch .Lwa_h4%=\n\t"
            ".Lwa_h5%=:\n\t"
            "v_mov_b32 v228, s91\n\t"
            "s_mul_i32 s49, s70, 800\n\t"
            "s_add_u32 m0, s49, 113760\n\t"
            "ds_write_b32 v228, v230 offset:8\n\t"
            "s_mov_b32 exec_hi, 0x3ffff\n\t"
            "buffer_load_dwordx4 v248, s[84:87], s81 offen sc1 lds\n\t"
            "s_mul_i32 s49, s70, 384\n\t"
            "s_add_u32 m0, s49, 130176\n\t"
            "s_add_u32 s81, s81, 800\n\t"
            "s_mov_b64 exec, 0xffffff\n\t"
            "buffer_load_dwordx4 v248, s[84:87], s81 offen sc1 lds\n\t"
            "s_mov_b64 exec, -1\n\t"
            "s_waitcnt lgkmcnt(0)\n\t"
            "s_branch .Lwa_loop%=\n\t"
            ".Lwa_last%=:\n\t"
            "s_setprio 2\n\t"
            "s_waitcnt lgkmcnt(0)\n\t"
            "s_nop 7\n\t"
            "v_mfma_f32_16x16x32_bf16 v[156:159], v[240:243], v[184:187], 0\n\t"
            "ds_read_b64 v[176:177], v247\n\t"
            "ds_read_b64 v[196:197], v247 offset:1536\n\t"
            "v_mfma_f32_16x16x32_bf16 v[156:159], v[240:243], v[188:191], v[156:159]\n\t"
            "ds_read_b64 v[220:221], v247 offset:3072\n\t"
            "v_xor_b32 v234, s73, v254\n\t"
            "v_mfma_f32_16x16x32_bf16 v[228:231], v[240:243], v[148:151], 0\n\t"
            "ds_read_u16 v178, v234\n\t"
            "ds_read_u16 v198, v234 offset:128\n\t"
            "v_mfma_f32_16x16x32_bf16 v[228:231], v[240:243], v[164:167], v[228:231]\n\t"
            "ds_read_u16 v222, v234 offset:256\n\t"
            "v_add_u32 v235, s45, v248\n\t"
            "v_mfma_f32_16x16x32_bf16 v[208:211], v[240:243], v[180:183], 0\n\t"
            "ds_read_b128 v[168:171], v235\n\t"
            "ds_read_b128 v[192:195], v235 offset:1536\n\t"
            "v_mfma_f32_16x16x32_bf16 v[208:211], v[240:243], v[172:175], v[208:211]\n\t"
            "ds_read_b128 v[216:219], v235 offset:3072\n\t"
            "v_mov_b32 v179, 0\n\t"
            "v_mov_b32 v199, 0\n\t"
            "v_mov_b32 v223, 0\n\t"
            "v_rcp_f32 v157, v156\n\t"
            "v_mad_u32_u24 v234, v246, s75, v253\n\t"
            "v_rcp_f32 v229, v228\n\t"
            "s_nop 0\n\t"
            "v_rcp_f32 v209, v208\n\t"
            "s_waitcnt lgkmcnt(2)\n\t"
            "v_mfma_f32_16x16x32_bf16 v[144:147], v[168:171], v[184:187], 0\n\t"
            "v_mfma_f32_16x16x32_bf16 v[144:147], v[176:179], v[188:191], v[144:147]\n\t"
            "s_waitcnt lgkmcnt(1)\n\t"
            "v_mfma_f32_16x16x32_bf16 v[152:155], v[192:195], v[184:187], 0\n\t"
            "v_mfma_f32_16x16x32_bf16 v[152:155], v[196:199], v[188:191], v[152:155]\n\t"
            "s_waitcnt lgkmcnt(0)\n\t"
            "v_mfma_f32_16x16x32_bf16 v[184:187], v[216:219], v[184:187], 0\n\t"
            "v_mfma_f32_16x16x32_bf16 v[184:187], v[220:223], v[188:191], v[184:187]\n\t"
            "ds_read_b64 v[188:189], v234 offset:320\n\t"
            "ds_read_b64 v[190:191], v234 offset:352\n\t"
            "v_mfma_f32_16x16x32_bf16 v[224:227], v[168:171], v[148:151], 0\n\t"
            "v_mul_f32 v144, v157, v144\n\t"
            "v_mul_f32 v145, v157, v145\n\t"
            "v_mfma_f32_16x16x32_bf16 v[224:227], v[176:179], v[164:167], v[224:227]\n\t"
            "v_mul_f32 v152, v157, v152\n\t"
            "v_mul_f32 v153, v157, v153\n\t"
            "v_mfma_f32_16x16x32_bf16 v[212:215], v[192:195], v[148:151], 0\n\t"
            "v_mul_f32 v184, v157, v184\n\t"
            "v_mul_f32 v185, v157, v185\n\t"
            "v_mfma_f32_16x16x32_bf16 v[212:215], v[196:199], v[164:167], v[212:215]\n\t"
            "v_mul_f32 v186, v157, v186\n\t"
            "v_mul_f32 v187, v157, v187\n\t"
            "v_mfma_f32_16x16x32_bf16 v[160:163], v[168:171], v[180:183], 0\n\t"
            "v_cvt_pk_bf16_f32 v184, v184, v185\n\t"
            "v_cvt_pk_bf16_f32 v185, v186, v187\n\t"
            "v_mfma_f32_16x16x32_bf16 v[160:163], v[176:179], v[172:175], v[160:163]\n\t"
            "ds_read_b64 v[186:187], v234 offset:288\n\t"
            "ds_write_b64 v245, v[184:185] offset:6400\n\t"
            "v_mfma_f32_16x16x32_bf16 v[204:207], v[192:195], v[180:183], 0\n\t"
            "ds_read_b64 v[184:185], v234 offset:256\n\t"
            "v_mul_f32 v224, v229, v224\n\t"
            "v_mfma_f32_16x16x32_bf16 v[204:207], v[196:199], v[172:175], v[204:207]\n\t"
            "v_mul_f32 v225, v229, v225\n\t"
            "v_mul_f32 v212, v229, v212\n\t"
            "v_mfma_f32_16x16x32_bf16 v[148:151], v[216:219], v[148:151], 0\n\t"
            "v_mul_f32 v213, v229, v213\n\t"
            "v_mul_f32 v160, v209, v160\n\t"
            "v_mfma_f32_16x16x32_bf16 v[148:151], v[220:223], v[164:167], v[148:151]\n\t"
            "v_mul_f32 v161, v209, v161\n\t"
            "v_mul_f32 v226, v229, v226\n\t"
            "v_mfma_f32_16x16x32_bf16 v[180:183], v[216:219], v[180:183], 0\n\t"
            "v_mul_f32 v204, v209, v204\n\t"
            "v_mul_f32 v205, v209, v205\n\t"
            "v_mfma_f32_16x16x32_bf16 v[180:183], v[220:223], v[172:175], v[180:183]\n\t"
            "v_mul_f32 v227, v229, v227\n\t"
            "v_cvt_pk_bf16_f32 v224, v224, v225\n\t"
            "v_mul_f32 v148, v229, v148\n\t"
            "v_mul_f32 v149, v229, v149\n\t"
            "v_mul_f32 v214, v229, v214\n\t"
            "v_mul_f32 v215, v229, v215\n\t"
            "v_cvt_pk_bf16_f32 v212, v212, v213\n\t"
            "v_mul_f32 v150, v229, v150\n\t"
            "v_mul_f32 v180, v209, v180\n\t"
            "v_mul_f32 v181, v209, v181\n\t"
            "v_mul_f32 v151, v229, v151\n\t"
            "v_cvt_pk_bf16_f32 v148, v148, v149\n\t"
            "v_mul_f32 v146, v157, v146\n\t"
            "v_mul_f32 v147, v157, v147\n\t"
            "v_cvt_pk_bf16_f32 v144, v144, v145\n\t"
            "v_mul_f32 v154, v157, v154\n\t"
            "v_mul_f32 v155, v157, v155\n\t"
            "s_waitcnt lgkmcnt(0)\n\t"
            "v_mfma_f32_16x16x32_bf16 v[200:203], v[240:243], v[184:187], 0\n\t"
            "v_cvt_pk_bf16_f32 v152, v152, v153\n\t"
            "v_mul_f32 v162, v209, v162\n\t"
            "v_mfma_f32_16x16x32_bf16 v[200:203], v[240:243], v[188:191], v[200:203]\n\t"
            "v_mul_f32 v163, v209, v163\n\t"
            "v_cvt_pk_bf16_f32 v160, v160, v161\n\t"
            "v_mfma_f32_16x16x32_bf16 v[168:171], v[184:187], v[168:171], 0\n\t"
            "v_mul_f32 v206, v209, v206\n\t"
            "v_mul_f32 v207, v209, v207\n\t"
            "v_mfma_f32_16x16x32_bf16 v[168:171], v[188:191], v[176:179], v[168:171]\n\t"
            "v_cvt_pk_bf16_f32 v204, v204, v205\n\t"
            "v_mul_f32 v182, v209, v182\n\t"
            "v_mfma_f32_16x16x32_bf16 v[192:195], v[184:187], v[192:195], 0\n\t"
            "v_rcp_f32 v201, v200\n\t"
            "v_mfma_f32_16x16x32_bf16 v[192:195], v[188:191], v[196:199], v[192:195]\n\t"
            "v_mul_f32 v183, v209, v183\n\t"
            "v_cvt_pk_bf16_f32 v180, v180, v181\n\t"
            "v_mfma_f32_16x16x32_bf16 v[216:219], v[184:187], v[216:219], 0\n\t"
            "v_cvt_pk_bf16_f32 v225, v226, v227\n\t"
            "v_cvt_pk_bf16_f32 v213, v214, v215\n\t"
            "v_mfma_f32_16x16x32_bf16 v[216:219], v[188:191], v[220:223], v[216:219]\n\t"
            "v_cvt_pk_bf16_f32 v149, v150, v151\n\t"
            "v_cvt_pk_bf16_f32 v145, v146, v147\n\t"
            "v_cndmask_b32_e64 v203, v168, v192, s[60:61]\n\t"
            "v_cvt_pk_bf16_f32 v153, v154, v155\n\t"
            "v_cvt_pk_bf16_f32 v161, v162, v163\n\t"
            "v_cvt_pk_bf16_f32 v205, v206, v207\n\t"
            "v_cvt_pk_bf16_f32 v181, v182, v183\n\t"
            "v_xad_u32 v202, s73, v254, v255\n\t"
            "v_cndmask_b32_e64 v203, v203, v216, s[62:63]\n\t"
            "ds_write_b64 v245, v[224:225]\n\t"
            "ds_write_b64 v245, v[212:213] offset:128\n\t"
            "ds_write_b64 v245, v[148:149] offset:256\n\t"
            "ds_write_b64 v245, v[144:145] offset:6144\n\t"
            "ds_write_b64 v245, v[152:153] offset:6272\n\t"
            "ds_write_b64 v245, v[160:161] offset:12288\n\t"
            "ds_write_b64 v245, v[204:205] offset:12416\n\t"
            "ds_write_b64 v245, v[180:181] offset:12544\n\t"
            "v_mul_f32_dpp v203, v201, v203 quad_perm:[0,0,0,0] row_mask:0xf bank_mask:0xf\n\t"
            "v_cvt_pk_bf16_f32 v203, v203, 0\n\t"
            "s_mov_b64 exec, s[68:69]\n\t"
            "ds_write_b16 v202, v203\n\t"
            "s_mov_b64 exec, 1\n\t"
            "ds_add_u32 v253, v246 offset:7856\n\t"
            "s_mov_b64 exec, -1\n\t"
            ".Lwa_done%=:\n\t"
            "s_waitcnt lgkmcnt(0)\n\t"
            "s_nop 7\n\t"
            : "+{v249}"(aRSw_), "+{s48}"(s48), "+{s70}"(s70), "+{s72}"(s72), "+{s73}"(s73), "+{s74}"(s74), "+{s76}"(s76), "+{s77}"(s77)
            : "{v244}"(pE_), "{v245}"(aO_), "{v246}"(sel_), "{v247}"(aVB_), "{v248}"(pST_), "{v250}"(aPLw_), "{v251}"(aRSr_), "{v252}"(g16_), "{v253}"(g8_), "{v254}"(loVL_), "{v255}"(loOL_), "{v[240:243]}"(ones4), "{s47}"((ldsb + X_OFF) ^ (ldsb + X1_OFF)),
              "{s[58:59]}"(0xFFFFull), "{s[60:61]}"(0xFFFF0000ull), "{s[62:63]}"(0xFFFF00000000ull), "{s[64:65]}"(0xFFFF000000000000ull),
              "{s[66:67]}"(0x1FFFFFFFFFFFFull), "{s75}"(rsb), "{s45}"(ldsb + V_OFF + w * 4608), "{s[68:69]}"(0xFFFFFFFFFFFFull),
              "{s[84:87]}"(wr), "{s46}"(wave * 36864), "{s88}"(touch), "{s80}"(wave == 0 ? 17 : 1000), "{s82}"(tag_lo), "{s83}"(tag_hi), "{s71}"(ldsb + O_OFF), "{s[54:55]}"(mrow), "{s[56:57]}"(0xF0000F0000F0000Full),
              "{s[78:79]}"(mrow & 0xF0000F0000F0000Full)
            : "v144", "v145", "v146", "v147", "v148", "v149", "v150", "v151", "v152", "v153", "v154", "v155", "v156", "v157", "v158", "v159", "v160", "v161", "v162", "v163", "v164", "v165", "v166", "v167", "v168", "v169", "v170", "v171", "v172", "v173", "v174", "v175", "v176", "v177", "v178", "v179", "v180", "v181", "v182", "v183", "v184", "v185", "v186", "v187", "v188", "v189", "v190", "v191", "v192", "v193", "v194", "v195", "v196", "v197", "v198", "v199", "v200", "v201", "v202", "v203", "v204", "v205", "v206", "v207", "v208", "v209", "v210", "v211", "v212", "v213", "v214", "v215", "v216", "v217", "v218", "v219", "v220", "v221", "v222", "v223", "v224", "v225", "v226", "v227", "v228", "v229", "v230", "v231", "v232", "v233", "v234", "v235", "v236", "v237", "v238", "v239", "v0", "v1", "v2", "v3", "v4", "v5", "v6", "v7", "v8", "v9", "v10", "v11", "v12", "v13", "v14", "v15", "v16", "v17", "v18", "v19", "v20", "v21", "v22", "v23", "v24", "v25", "v26", "v27", "v28", "v29", "v30", "v31", "v32", "v33", "v34", "v35", "v36", "v37", "v38", "v39", "v40", "v41", "v42", "v43", "v44", "v45", "v46", "v47", "v48", "v49", "v50", "v51", "v52", "v53", "v54", "v55", "v56", "v57", "v58", "v59", "v60", "v61", "v62", "v63", "v64", "v65", "v66", "v67", "v68", "v69", "v70", "v71", "v72", "v73", "v74", "v75", "v76", "v77", "v78", "v79", "v80", "v81", "v82", "v83", "v84", "v85", "v86", "v87", "v88", "v89", "v90", "v91", "v92", "v93", "v94", "v95", "v96", "v97", "v98", "v99", "v100", "v101", "v102", "v103", "v104", "v105", "v106", "v107", "v108", "v109", "v110", "v111", "v112", "v113", "v114", "v115", "v116", "v117", "v118", "v119", "v120", "v121", "v122", "v123", "v124", "v125", "v126", "v127", "v128", "v129", "v130", "v131", "v132", "v133", "v134", "v135", "v136", "v137", "v138", "v139", "v140", "v141", "v142", "v143",  "m0", "s44", "s81", "s89", "s90", "s91", "s49", "vcc", "scc", "memory");

        }
        if (wave == 0 && blockIdx.x == 0) {
            int ln_;
            asm volatile("v_mbcnt_lo_u32_b32 %0, -1, 0\n\tv_mbcnt_hi_u32_b32 %0, -1, %0" : "=v"(ln_));
            KARGS
            unsigned long long tg;
            asm volatile("s_load_dwordx2 %0, %1, %2\n\ts_waitcnt lgkmcnt(0)" : "=s"(tg) : "s"(wb), "n"(EPO_OFF) : "memory");
            const unsigned lo = (unsigned)tg + 1u, hi = (unsigned)(tg >> 32);
            const auto wq = __builtin_amdgcn_make_buffer_rsrc((void*)wb, 0, WS_BYTES, 0x00020000);
            for (int it_ = 0; it_ < 100000; ++it_) {
                bool bad = false;
#pragma unroll
                for (int k = 0; k < 2; ++k) {
                    const u32x4 a_ = __builtin_amdgcn_raw_buffer_load_b128(wq, ARR_OFF + ln_ * 16, k * 1024, SC1);
                    bad |= a_[0] != lo || a_[1] != hi || a_[2] != lo || a_[3] != hi;
                }
                if (__builtin_amdgcn_ballot_w64(bad) == 0ull) break;
                __builtin_amdgcn_s_sleep(2);
            }
            if (ln_ == 0) __builtin_amdgcn_raw_buffer_store_b64((u32x2){lo, hi}, wq, EPO_OFF, 0, SC1);
        }
        asm volatile("wk_code_end:");
        __builtin_amdgcn_endpgm();
    } else if (wave < 6) {
        LANE_SETUP
        {
            const unsigned rsb = ldsb + RSX_OFF;
            const unsigned pE_ = fE - X_OFF;
            const unsigned aVAw_ = ldsb + V_OFF + w * 9216 + lane * 16, aVBw_ = ldsb + V_OFF + w * 9216 + 1024 + lane * 8;
            const unsigned pST_ = lane * 16;
            unsigned aRSw_ = rsb + 256 + lane * 4, aRSr_ = rsb + (lane & 48);
            B_SCALARS
            unsigned s44 = 4;
            asm volatile(
            "s_waitcnt lgkmcnt(0)\n\t"
            "s_mov_b32 s83, 0x10000\n\t"
            "s_mov_b32 s84, -47104\n\t"
            "s_mov_b64 s[88:89], s[72:73]\n\t"
            "s_mov_b64 s[90:91], s[74:75]\n\t"
            "s_nop 7\n\t"
            "s_setprio 3\n\t"
            "s_barrier\n\t"
            "buffer_load_dwordx4 v[140:143], v245, s[28:31], s82 offen sc1\n\t"
            "s_add_u32 s49, s33, 0\n\t"
            "buffer_load_dwordx4 v[0:3], v245, s[28:31], s49 offen sc1\n\t"
            "s_add_u32 s49, s33, 6144\n\t"
            "buffer_load_dwordx4 v[24:27], v245, s[28:31], s49 offen sc1\n\t"
            "s_add_u32 s49, s33, 12288\n\t"
            "buffer_load_dwordx4 v[48:51], v245, s[28:31], s49 offen sc1\n\t"
            "s_add_u32 s49, s33, 18432\n\t"
            "buffer_load_dwordx4 v[72:75], v245, s[28:31], s49 offen sc1\n\t"
            "s_add_u32 s49, s33, 24576\n\t"
            "buffer_load_dwordx4 v[96:99], v245, s[28:31], s49 offen sc1\n\t"
            "s_add_u32 s49, s33, 30720\n\t"
            "buffer_load_dwordx4 v[120:123], v245, s[28:31], s49 offen sc1\n\t"
            "s_add_u32 s49, s33, 1024\n\t"
            "buffer_load_dwordx4 v[4:7], v245, s[28:31], s49 offen sc1\n\t"
            "s_add_u32 s49, s33, 7168\n\t"
            "buffer_load_dwordx4 v[28:31], v245, s[28:31], s49 offen sc1\n\t"
            "s_add_u32 s49, s33, 13312\n\t"
            "buffer_load_dwordx4 v[52:55], v245, s[28:31], s49 offen sc1\n\t"
            "s_add_u32 s49, s33, 19456\n\t"
            "buffer_load_dwordx4 v[76:79], v245, s[28:31], s49 offen sc1\n\t"
            "s_add_u32 s49, s33, 25600\n\t"
            "buffer_load_dwordx4 v[100:103], v245, s[28:31], s49 offen sc1\n\t"
            "s_add_u32 s49, s33, 31744\n\t"
            "buffer_load_dwordx4 v[124:127], v245, s[28:31], s49 offen sc1\n\t"
            "s_add_u32 s49, s33, 2048\n\t"
            "buffer_load_dwordx4 v[8:11], v245, s[28:31], s49 offen sc1\n\t"
            "s_add_u32 s49, s33, 8192\n\t"
            "buffer_load_dwordx4 v[32:35], v245, s[28:31], s49 offen sc1\n\t"
            "s_add_u32 s49, s33, 14336\n\t"
            "buffer_load_dwordx4 v[56:59], v245, s[28:31], s49 offen sc1\n\t"
            "s_add_u32 s49, s33, 20480\n\t"
            "buffer_load_dwordx4 v[80:83], v245, s[28:31], s49 offen sc1\n\t"
            "s_add_u32 s49, s33, 26624\n\t"
            "buffer_load_dwordx4 v[104:107], v245, s[28:31], s49 offen sc1\n\t"
            "s_add_u32 s49, s33, 32768\n\t"
            "buffer_load_dwordx4 v[128:131], v245, s[28:31], s49 offen sc1\n\t"
            "s_add_u32 s49, s33, 3072\n\t"
            "buffer_load_dwordx4 v[12:15], v245, s[28:31], s49 offen sc1\n\t"
            "s_add_u32 s49, s33, 9216\n\t"
            "buffer_load_dwordx4 v[36:39], v245, s[28:31], s49 offen sc1\n\t"
            "s_add_u32 s49, s33, 15360\n\t"
            "buffer_load_dwordx4 v[60:63], v245, s[28:31], s49 offen sc1\n\t"
            "s_add_u32 s49, s33, 21504\n\t"
            "buffer_load_dwordx4 v[84:87], v245, s[28:31], s49 offen sc1\n\t"
            "s_add_u32 s49, s33, 27648\n\t"
            "buffer_load_dwordx4 v[108:111], v245, s[28:31], s49 offen sc1\n\t"
            "s_add_u32 s49, s33, 33792\n\t"
            "buffer_load_dwordx4 v[132:135], v245, s[28:31], s49 offen sc1\n\t"
            "s_add_u32 s49, s33, 4096\n\t"
            "buffer_load_dwordx4 v[16:19], v245, s[28:31], s49 offen sc1\n\t"
            "s_add_u32 s49, s33, 10240\n\t"
            "buffer_load_dwordx4 v[40:43], v245, s[28:31], s49 offen sc1\n\t"
            "s_add_u32 s49, s33, 16384\n\t"
            "buffer_load_dwordx4 v[64:67], v245, s[28:31], s49 offen sc1\n\t"
            "s_add_u32 s49, s33, 22528\n\t"
            "buffer_load_dwordx4 v[88:91], v245, s[28:31], s49 offen sc1\n\t"
            "s_add_u32 s49, s33, 28672\n\t"
            "buffer_load_dwordx4 v[112:115], v245, s[28:31], s49 offen sc1\n\t"
            "s_add_u32 s49, s33, 34816\n\t"
            "buffer_load_dwordx4 v[136:139], v245, s[28:31], s49 offen sc1\n\t"
            "s_add_u32 s49, s33, 5120\n\t"
            "buffer_load_dwordx4 v[20:23], v245, s[28:31], s49 offen sc1\n\t"
            "s_add_u32 s49, s33, 11264\n\t"
            "buffer_load_dwordx4 v[44:47], v245, s[28:31], s49 offen sc1\n\t"
            "s_add_u32 s49, s33, 17408\n\t"
            "buffer_load_dwordx4 v[68:71], v245, s[28:31], s49 offen sc1\n\t"
            "s_add_u32 s49, s33, 23552\n\t"
            "buffer_load_dwordx4 v[92:95], v245, s[28:31], s49 offen sc1\n\t"
            "s_add_u32 s49, s33, 29696\n\t"
            "buffer_load_dwordx4 v[116:119], v245, s[28:31], s49 offen sc1\n\t"
            "s_add_u32 s49, s33, 35840\n\t"
            "buffer_load_dwordx4 v[140:143], v245, s[28:31], s49 offen sc1\n\t"
            "ds_read_b32 v249, v252 offset:12\n\t"
            "s_waitcnt lgkmcnt(0)\n\t"
            "v_and_b32 v249, s69, v249\n\t"
            "s_mov_b32 exec_hi, 0x3fffff\n\t"
            "s_add_u32 m0, s66, 0\n\t"
            "s_add_u32 s49, s79, 0\n\t"
            "buffer_load_dwordx4 v249, s[72:75], s49 offen lds\n\t"
            "s_mov_b32 exec_hi, -1\n\t"
            "s_mov_b32 exec_hi, 0x3fffff\n\t"
            "s_add_u32 m0, s66, 872\n\t"
            "s_add_u32 s49, s47, 196224\n\t"
            "buffer_load_dwordx4 v249, s[72:75], s49 offen lds\n\t"
            "s_mov_b32 exec_hi, -1\n\t"
            "s_mov_b32 exec_hi, 0x3fffff\n\t"
            "s_add_u32 m0, s66, 1744\n\t"
            "s_add_u32 s49, s47, 392448\n\t"
            "buffer_load_dwordx4 v249, s[72:75], s49 offen lds\n\t"
            "s_mov_b32 exec_hi, -1\n\t"
            "s_mov_b32 exec_hi, 0x3fffff\n\t"
            "s_add_u32 m0, s66, 2616\n\t"
            "s_add_u32 s49, s47, 588672\n\t"
            "buffer_load_dwordx4 v249, s[72:75], s49 offen lds\n\t"
            "s_mov_b32 exec_hi, -1\n\t"
            "s_mov_b32 exec_hi, 0x3fffff\n\t"
            "s_add_u32 m0, s66, 3488\n\t"
            "s_add_u32 s49, s47, 784896\n\t"
            "buffer_load_dwordx4 v249, s[72:75], s49 offen lds\n\t"
            "s_mov_b32 exec_hi, -1\n\t"
            "s_mov_b32 exec_hi, 0x3fffff\n\t"
            "s_add_u32 m0, s66, 4360\n\t"
            "s_add_u32 s49, s47, 981120\n\t"
            "buffer_load_dwordx4 v249, s[72:75], s49 offen lds\n\t"
            "s_mov_b32 exec_hi, -1\n\t"
            "s_mov_b32 exec_hi, 0x3fffff\n\t"
            "s_add_u32 m0, s66, 5232\n\t"
            "s_add_u32 s49, s47, 1177344\n\t"
            "buffer_load_dwordx4 v249, s[72:75], s49 offen lds\n\t"
            "s_mov_b32 exec_hi, -1\n\t"
            "s_mov_b32 exec_hi, 0x3fffff\n\t"
            "s_add_u32 m0, s66, 6104\n\t"
            "s_add_u32 s49, s47, 1373568\n\t"
            "buffer_load_dwordx4 v249, s[72:75], s49 offen lds\n\t"
            "s_mov_b32 exec_hi, -1\n\t"
            "s_mov_b32 exec_hi, 0x3fffff\n\t"
            "s_add_u32 m0, s66, 6976\n\t"
            "s_add_u32 s49, s47, 1569792\n\t"
            "buffer_load_dwordx4 v249, s[72:75], s49 offen lds\n\t"
            "s_mov_b32 exec_hi, -1\n\t"
            "ds_read_b128 v[168:171], v254\n\t"
            "ds_read_b128 v[172:175], v254 offset:64\n\t"
            "v_add_u32 v180, s48, v245\n\t"
            "v_add_u32 v232, s48, v242\n\t"
            "v_xor_b32 v233, 64, v232\n\t"
            "v_xor_b32 v246, 0x100, v246\n\t"
            "ds_read_b128 v[176:179], v180 offset:18816\n\t"
            "ds_read_b128 v[180:183], v180 offset:19840\n\t"
            "ds_read_b128 v[192:195], v232\n\t"
            "ds_read_b128 v[196:199], v233\n\t"
            "ds_read_b128 v[224:227], v232 offset:128\n\t"
            "ds_read_b128 v[228:231], v233 offset:128\n\t"
            "s_waitcnt lgkmcnt(5)\n\t"
            "v_add_f32 v176, v176, v178\n\t"
            "s_waitcnt lgkmcnt(4)\n\t"
            "v_add_f32 v180, v180, v182\n\t"
            "v_add_f32 v176, v176, v180\n\t"
            "v_add_f32 v177, v177, v179\n\t"
            "v_add_f32 v181, v181, v183\n\t"
            "v_mul_f32 v176, 0x3baaaaab, v176\n\t"
            "v_add_f32 v177, v177, v181\n\t"
            "v_mul_f32_e64 v176, v176, -v176\n\t"
            "v_fmac_f32 v176, 0x3baaaaab, v177\n\t"
            "v_max_f32 v176, 0, v176\n\t"
            "v_add_f32 v176, 0x3727c5ac, v176\n\t"
            "v_rsq_f32 v176, v176\n\t"
            "s_nop 0\n\t"
            "ds_write_b32 v246, v176\n\t"
            "s_setprio 3\n\t"
            "s_waitcnt vmcnt(9)\n\t"
            "s_waitcnt lgkmcnt(3)\n\t"
            "v_mfma_f32_16x16x32_bf16 v[200:203], v[192:195], v[0:3], 0\n\t"
            "v_mfma_f32_16x16x32_bf16 v[200:203], v[196:199], v[4:7], v[200:203]\n\t"
            "v_mfma_f32_16x16x32_bf16 v[204:207], v[196:199], v[28:31], 0\n\t"
            "v_mfma_f32_16x16x32_bf16 v[204:207], v[192:195], v[24:27], v[204:207]\n\t"
            "v_mfma_f32_16x16x32_bf16 v[208:211], v[192:195], v[48:51], 0\n\t"
            "v_mfma_f32_16x16x32_bf16 v[208:211], v[196:199], v[52:55], v[208:211]\n\t"
            "v_mfma_f32_16x16x32_bf16 v[212:215], v[196:199], v[76:79], 0\n\t"
            "v_mfma_f32_16x16x32_bf16 v[212:215], v[192:195], v[72:75], v[212:215]\n\t"
            "v_mfma_f32_16x16x32_bf16 v[216:219], v[192:195], v[96:99], 0\n\t"
            "v_mfma_f32_16x16x32_bf16 v[216:219], v[196:199], v[100:103], v[216:219]\n\t"
            "v_mfma_f32_16x16x32_bf16 v[220:223], v[196:199], v[124:127], 0\n\t"
            "v_mfma_f32_16x16x32_bf16 v[220:223], v[192:195], v[120:123], v[220:223]\n\t"
            "s_waitcnt lgkmcnt(1)\n\t"
            "v_mfma_f32_16x16x32_bf16 v[200:203], v[224:227], v[8:11], v[200:203]\n\t"
            "v_mfma_f32_16x16x32_bf16 v[200:203], v[228:231], v[12:15], v[200:203]\n\t"
            "v_mfma_f32_16x16x32_bf16 v[204:207], v[228:231], v[36:39], v[204:207]\n\t"
            "v_mfma_f32_16x16x32_bf16 v[204:207], v[224:227], v[32:35], v[204:207]\n\t"
            "v_mfma_f32_16x16x32_bf16 v[208:211], v[224:227], v[56:59], v[208:211]\n\t"
            "v_mfma_f32_16x16x32_bf16 v[208:211], v[228:231], v[60:63], v[208:211]\n\t"
            "v_mfma_f32_16x16x32_bf16 v[212:215], v[228:231], v[84:87], v[212:215]\n\t"
            "v_mfma_f32_16x16x32_bf16 v[212:215], v[224:227], v[80:83], v[212:215]\n\t"
            "v_mfma_f32_16x16x32_bf16 v[216:219], v[224:227], v[104:107], v[216:219]\n\t"
            "s_waitcnt vmcnt(0)\n\t"
            "ds_read_b64 v[144:145], v253\n\t"
            "v_mfma_f32_16x16x32_bf16 v[216:219], v[228:231], v[108:111], v[216:219]\n\t"
            "ds_read_b64 v[146:147], v253 offset:96\n\t"
            "ds_read_b64 v[148:149], v253 offset:1744\n\t"
            "v_mfma_f32_16x16x32_bf16 v[220:223], v[228:231], v[132:135], v[220:223]\n\t"
            "ds_read_b128 v[228:231], v233 offset:256\n\t"
            "ds_read_b64 v[150:151], v253 offset:1840\n\t"
            "v_mfma_f32_16x16x32_bf16 v[220:223], v[224:227], v[128:131], v[220:223]\n\t"
            "ds_read_b128 v[224:227], v232 offset:256\n\t"
            "ds_read_b64 v[152:153], v253 offset:8\n\t"
            "ds_read_b64 v[154:155], v253 offset:104\n\t"
            "ds_read_b64 v[156:157], v253 offset:1752\n\t"
            "ds_read_b64 v[158:159], v253 offset:1848\n\t"
            "ds_read_b64 v[160:161], v253 offset:16\n\t"
            "ds_read_b64 v[162:163], v253 offset:112\n\t"
            "ds_read_b64 v[164:165], v253 offset:1760\n\t"
            "ds_read_b64 v[166:167], v253 offset:1856\n\t"
            "s_waitcnt lgkmcnt(0)\n\t"
            "s_nop 7\n\t"
            ".p2align 6\n\t"
            "s_nop 0\n\t"
            "s_nop 0\n\t"
            "s_nop 0\n\t"
            "s_nop 0\n\t"
            ".Lwc_loop%=:\n\t"
            "s_setprio 1\n\t"
            "s_setprio 3\n\t"
            "v_mfma_f32_16x16x32_bf16 v[200:203], v[224:227], v[16:19], v[200:203]\n\t"
            "v_mov_b64 v[176:177], 0\n\t"
            "v_mov_b64 v[178:179], 0\n\t"
            "v_mfma_f32_16x16x32_bf16 v[200:203], v[228:231], v[20:23], v[200:203]\n\t"
            "v_mov_b64 v[180:181], 0\n\t"
            "v_mov_b64 v[182:183], 0\n\t"
            "s_waitcnt lgkmcnt(0)\n\t"
            "v_smfmac_f32_16x16x64_bf16 v[176:179], v[168:171], v[144:151], v255\n\t"
            "ds_read_b128 v[168:171], v254 offset:128\n\t"
            "v_smfmac_f32_16x16x64_bf16 v[180:183], v[172:175], v[152:159], v255\n\t"
            "ds_read_b64 v[144:145], v253 offset:24\n\t"
            "v_mfma_f32_16x16x32_bf16 v[204:207], v[228:231], v[44:47], v[204:207]\n\t"
            "ds_read_b64 v[146:147], v253 offset:120\n\t"
            "ds_read_b64 v[148:149], v253 offset:1768\n\t"
            "v_mfma_f32_16x16x32_bf16 v[204:207], v[224:227], v[40:43], v[204:207]\n\t"
            "ds_read_b64 v[150:151], v253 offset:1864\n\t"
            "ds_read_b128 v[172:175], v254 offset:192\n\t"
            "v_mfma_f32_16x16x32_bf16 v[208:211], v[224:227], v[64:67], v[208:211]\n\t"
            "ds_read_b64 v[152:153], v253 offset:32\n\t"
            "ds_read_b64 v[154:155], v253 offset:128\n\t"
            "v_mfma_f32_16x16x32_bf16 v[208:211], v[228:231], v[68:71], v[208:211]\n\t"
            "ds_read_b64 v[156:157], v253 offset:1776\n\t"
            "ds_read_b64 v[158:159], v253 offset:1872\n\t"
            "v_mfma_f32_16x16x32_bf16 v[212:215], v[228:231], v[92:95], v[212:215]\n\t"
            "v_mov_b32 v249, s76\n\t"
            "v_mov_b64 v[184:185], 0\n\t"
            "v_mfma_f32_16x16x32_bf16 v[212:215], v[224:227], v[88:91], v[212:215]\n\t"
            "ds_read_b32 v249, v249\n\t"
            "v_mov_b64 v[186:187], 0\n\t"
            "v_mfma_f32_16x16x32_bf16 v[216:219], v[224:227], v[112:115], v[216:219]\n\t"
            "v_mov_b64 v[188:189], 0\n\t"
            "v_mov_b64 v[190:191], 0\n\t"
            "v_mfma_f32_16x16x32_bf16 v[216:219], v[228:231], v[116:119], v[216:219]\n\t"
            "v_cvt_pk_bf16_f32 v176, v176, v177\n\t"
            "v_add_u32 v236, s67, v250\n\t"
            "s_waitcnt lgkmcnt(10)\n\t"
            "v_smfmac_f32_16x16x64_bf16 v[184:187], v[168:171], v[160:167], v255\n\t"
            "ds_read_b128 v[168:171], v254 offset:256\n\t"
            "s_waitcnt lgkmcnt(6)\n\t"
            "v_smfmac_f32_16x16x64_bf16 v[188:191], v[172:175], v[144:151], v255\n\t"
            "ds_read_b64 v[160:161], v253 offset:40\n\t"
            "v_mfma_f32_16x16x32_bf16 v[220:223], v[228:231], v[140:143], v[220:223]\n\t"
            "ds_read_b64 v[162:163], v253 offset:136\n\t"
            "ds_read_b64 v[164:165], v253 offset:1784\n\t"
            "v_mfma_f32_16x16x32_bf16 v[220:223], v[224:227], v[136:139], v[220:223]\n\t"
            "s_setprio 1\n\t"
            "ds_read_b64 v[166:167], v253 offset:1880\n\t"
            "ds_read_b128 v[172:175], v254 offset:320\n\t"
            "ds_read_b64 v[144:145], v253 offset:48\n\t"
            "ds_read_b64 v[146:147], v253 offset:144\n\t"
            "ds_read_b64 v[148:149], v253 offset:1792\n\t"
            "ds_read_b64 v[150:151], v253 offset:1888\n\t"
            "s_waitcnt lgkmcnt(14)\n\t"
            "ds_read_b128 v[224:227], v232 offset:6272\n\t"
            "s_waitcnt lgkmcnt(14)\n\t"
            "ds_read_b128 v[228:231], v233 offset:6272\n\t"
            "s_waitcnt lgkmcnt(12)\n\t"
            "v_readfirstlane_b32 s26, v249\n\t"
            "v_cvt_pk_bf16_f32 v177, v178, v179\n\t"
            "v_cvt_pk_bf16_f32 v178, v180, v181\n\t"
            "s_and_b32 s68, s26, 0x7ffffff\n\t"
            "s_add_u32 s68, s68, s78\n\t"
            "s_and_b32 s69, s26, 0x38000000\n\t"
            "s_or_b32 s69, s69, s39\n\t"
            "s_bitcmp1_b32 s26, 27\n\t"
            "s_cselect_b32 s79, 2.0, s68\n\t"
            "s_setprio 3\n\t"
            "v_mfma_f32_4x4x4_16b_bf16 v[196:199], v[176:177], v[176:177], 0\n\t"
            "s_setprio 1\n\t"
            "ds_read_b32 v249, v252 offset:12\n\t"
            "s_and_b32 s57, s56, 3\n\t"
            "s_lshl_b32 s58, s57, 3\n\t"
            "s_lshr_b32 m0, s54, s58\n\t"
            "s_lshl_b32 s58, s57, 4\n\t"
            "s_lshr_b64 s[36:37], s[50:51], s58\n\t"
            "s_and_b32 s57, s36, 0xffff\n\t"
            "s_add_u32 s57, s57, s67\n\t"
            "v_writelane_b32 v236, s57, 63\n\t"
            "v_writelane_b32 v236, s59, m0\n\t"
            "v_cvt_pk_bf16_f32 v179, v182, v183\n\t"
            "v_mov_b64 v[180:181], 0\n\t"
            "v_mov_b64 v[182:183], 0\n\t"
            "s_setprio 3\n\t"
            "v_mfma_f32_16x16x32_bf16 v[192:195], v[238:241], v[176:179], 0\n\t"
            "s_lshr_b64 s[36:37], s[52:53], s58\n\t"
            "s_and_b32 s58, s36, 0xffff\n\t"
            "s_add_u32 s58, s58, s67\n\t"
            "v_mfma_f32_4x4x4_16b_bf16 v[196:199], v[178:179], v[178:179], v[196:199]\n\t"
            "ds_write_b128 v236, v[176:179]\n\t"
            "s_waitcnt lgkmcnt(8)\n\t"
            "v_smfmac_f32_16x16x64_bf16 v[180:183], v[172:175], v[160:167], v255\n\t"
            "s_setprio 1\n\t"
            "s_and_b32 s38, s52, 0xffff\n\t"
            "s_add_u32 s38, s38, s67\n\t"
            "ds_read_b128 v[172:175], v254 offset:448\n\t"
            "ds_read_b64 v[160:161], v253 offset:64\n\t"
            "ds_read_b64 v[162:163], v253 offset:160\n\t"
            "ds_read_b64 v[164:165], v253 offset:1808\n\t"
            "ds_read_b64 v[166:167], v253 offset:1904\n\t"
            "v_mov_b64 v[176:177], 0\n\t"
            "v_mov_b64 v[178:179], 0\n\t"
            "v_cvt_pk_bf16_f32 v184, v184, v185\n\t"
            "v_cvt_pk_bf16_f32 v185, v186, v187\n\t"
            "s_setprio 3\n\t"
            "v_smfmac_f32_16x16x64_bf16 v[176:179], v[168:171], v[152:159], v255\n\t"
            "ds_read_b128 v[168:171], v254 offset:384\n\t"
            "v_mfma_f32_4x4x4_16b_bf16 v[196:199], v[184:185], v[184:185], v[196:199]\n\t"
            "s_setprio 1\n\t"
            "ds_read_b64 v[152:153], v253 offset:56\n\t"
            "s_waitcnt lgkmcnt(14)\n\t"
            "ds_read_b64 v[154:155], v253 offset:152\n\t"
            "s_waitcnt lgkmcnt(14)\n\t"
            "ds_read_b64 v[156:157], v253 offset:1800\n\t"
            "s_waitcnt lgkmcnt(14)\n\t"
            "ds_read_b64 v[158:159], v253 offset:1896\n\t"
            "v_xor_b32 v237, 16, v236\n\t"
            "v_cvt_pk_bf16_f32 v186, v188, v189\n\t"
            "v_cvt_pk_bf16_f32 v187, v190, v191\n\t"
            "v_mov_b64 v[188:189], 0\n\t"
            "v_mov_b64 v[190:191], 0\n\t"
            "s_setprio 3\n\t"
            "v_mfma_f32_16x16x32_bf16 v[192:195], v[238:241], v[184:187], v[192:195]\n\t"
            "s_waitcnt lgkmcnt(14)\n\t"
            "ds_write_b128 v237, v[184:187]\n\t"
            "v_mfma_f32_4x4x4_16b_bf16 v[196:199], v[186:187], v[186:187], v[196:199]\n\t"
            "s_setprio 1\n\t"
            "v_mov_b64 v[184:185], 0\n\t"
            "v_mov_b64 v[186:187], 0\n\t"
            "v_cvt_pk_bf16_f32 v176, v176, v177\n\t"
            "v_cvt_pk_bf16_f32 v177, v178, v179\n\t"
            "v_cvt_pk_bf16_f32 v178, v180, v181\n\t"
            "v_cvt_pk_bf16_f32 v179, v182, v183\n\t"
            "s_setprio 3\n\t"
            "v_mfma_f32_4x4x4_16b_bf16 v[196:199], v[176:177], v[176:177], v[196:199]\n\t"
            "s_waitcnt lgkmcnt(14)\n\t"
            "ds_write_b128 v236, v[176:179] offset:128\n\t"
            "s_waitcnt lgkmcnt(6)\n\t"
            "v_smfmac_f32_16x16x64_bf16 v[184:187], v[168:171], v[144:151], v255\n\t"
            "ds_read_b128 v[168:171], v254 offset:512\n\t"
            "s_waitcnt lgkmcnt(3)\n\t"
            "v_smfmac_f32_16x16x64_bf16 v[188:191], v[172:175], v[152:159], v255\n\t"
            "ds_read_b64 v[144:145], v253 offset:72\n\t"
            "v_mfma_f32_16x16x32_bf16 v[192:195], v[238:241], v[176:179], v[192:195]\n\t"
            "ds_read_b64 v[146:147], v253 offset:168\n\t"
            "ds_read_b64 v[148:149], v253 offset:1816\n\t"
            "v_mfma_f32_4x4x4_16b_bf16 v[196:199], v[178:179], v[178:179], v[196:199]\n\t"
            "s_setprio 1\n\t"
            "ds_read_b64 v[150:151], v253 offset:1912\n\t"
            "ds_read_b128 v[172:175], v254 offset:576\n\t"
            "ds_read_b64 v[152:153], v253 offset:80\n\t"
            "ds_read_b64 v[154:155], v253 offset:176\n\t"
            "ds_read_b64 v[156:157], v253 offset:1824\n\t"
            "ds_read_b64 v[158:159], v253 offset:1920\n\t"
            "v_mov_b64 v[176:177], 0\n\t"
            "v_mov_b64 v[178:179], 0\n\t"
            "v_mov_b64 v[180:181], 0\n\t"
            "v_mov_b64 v[182:183], 0\n\t"
            "v_cvt_pk_bf16_f32 v184, v184, v185\n\t"
            "v_cvt_pk_bf16_f32 v185, v186, v187\n\t"
            "v_cvt_pk_bf16_f32 v186, v188, v189\n\t"
            "v_cvt_pk_bf16_f32 v187, v190, v191\n\t"
            "s_setprio 3\n\t"
            "v_mfma_f32_4x4x4_16b_bf16 v[196:199], v[184:185], v[184:185], v[196:199]\n\t"
            "ds_write_b128 v237, v[184:187] offset:128\n\t"
            "s_waitcnt lgkmcnt(10)\n\t"
            "v_smfmac_f32_16x16x64_bf16 v[176:179], v[168:171], v[160:167], v255\n\t"
            "ds_read_b128 v[168:171], v254 offset:640\n\t"
            "v_mfma_f32_16x16x32_bf16 v[192:195], v[238:241], v[184:187], v[192:195]\n\t"
            "ds_read_b64 v[160:161], v253 offset:88\n\t"
            "ds_read_b64 v[162:163], v253 offset:184\n\t"
            "s_waitcnt lgkmcnt(8)\n\t"
            "v_smfmac_f32_16x16x64_bf16 v[180:183], v[172:175], v[144:151], v255\n\t"
            "ds_read_b64 v[164:165], v253 offset:1832\n\t"
            "v_mfma_f32_4x4x4_16b_bf16 v[196:199], v[186:187], v[186:187], v[196:199]\n\t"
            "s_setprio 1\n\t"
            "ds_read_b64 v[166:167], v253 offset:1928\n\t"
            "ds_read_b128 v[172:175], v254 offset:704\n\t"
            "v_mov_b64 v[184:185], 0\n\t"
            "v_mov_b64 v[186:187], 0\n\t"
            "v_mov_b64 v[188:189], 0\n\t"
            "v_mov_b64 v[190:191], 0\n\t"
            "v_add_u32 v248, s67, v251\n\t"
            "v_writelane_b32 v248, s58, 63\n\t"
            "v_writelane_b32 v248, s38, m0\n\t"
            "v_and_b32 v249, s69, v249\n\t"
            "v_cvt_pk_bf16_f32 v176, v176, v177\n\t"
            "v_cvt_pk_bf16_f32 v177, v178, v179\n\t"
            "v_cvt_pk_bf16_f32 v178, v180, v181\n\t"
            "v_cvt_pk_bf16_f32 v179, v182, v183\n\t"
            "s_setprio 3\n\t"
            "v_mfma_f32_4x4x4_16b_bf16 v[196:199], v[176:177], v[176:177], v[196:199]\n\t"
            "ds_write_b128 v236, v[176:179] offset:256\n\t"
            "s_waitcnt lgkmcnt(6)\n\t"
            "v_smfmac_f32_16x16x64_bf16 v[184:187], v[168:171], v[152:159], v255\n\t"
            "v_mov_b32 v234, 0\n\t"
            "v_mfma_f32_4x4x4_16b_bf16 v[196:199], v[178:179], v[178:179], v[196:199]\n\t"
            "v_xor_b32 v246, 0x100, v246\n\t"
            "v_mfma_f32_16x16x32_bf16 v[192:195], v[238:241], v[176:179], v[192:195]\n\t"
            "s_waitcnt lgkmcnt(1)\n\t"
            "v_smfmac_f32_16x16x64_bf16 v[188:191], v[172:175], v[160:167], v255\n\t"
            "s_setprio 1\n\t"
            "s_mov_b32 exec_hi, 0x3fffff\n\t"
            "s_add_u32 m0, s66, 0\n\t"
            "s_add_u32 s49, s79, 0\n\t"
            "buffer_load_dwordx4 v249, s[88:91], s49 offen lds\n\t"
            "s_mov_b32 exec_hi, -1\n\t"
            "s_mov_b32 exec_hi, 0x3fffff\n\t"
            "s_add_u32 m0, s66, 872\n\t"
            "s_add_u32 s49, s68, 196224\n\t"
            "buffer_load_dwordx4 v249, s[72:75], s49 offen lds\n\t"
            "s_mov_b32 exec_hi, -1\n\t"
            "s_mov_b32 exec_hi, 0x3fffff\n\t"
            "s_add_u32 m0, s66, 1744\n\t"
            "s_add_u32 s49, s68, 392448\n\t"
            "buffer_load_dwordx4 v249, s[72:75], s49 offen lds\n\t"
            "s_mov_b32 exec_hi, -1\n\t"
            "s_mov_b32 exec_hi, 0x3fffff\n\t"
            "s_add_u32 m0, s66, 2616\n\t"
            "s_add_u32 s49, s68, 588672\n\t"
            "buffer_load_dwordx4 v249, s[72:75], s49 offen lds\n\t"
            "s_mov_b32 exec_hi, -1\n\t"
            "s_mov_b32 exec_hi, 0x3fffff\n\t"
            "s_add_u32 m0, s66, 3488\n\t"
            "s_add_u32 s49, s68, 784896\n\t"
            "buffer_load_dwordx4 v249, s[72:75], s49 offen lds\n\t"
            "s_mov_b32 exec_hi, -1\n\t"
            "v_cvt_pk_bf16_f32 v184, v184, v185\n\t"
            "v_cvt_pk_bf16_f32 v185, v186, v187\n\t"
            "v_cvt_pk_bf16_f32 v186, v188, v189\n\t"
            "v_cvt_pk_bf16_f32 v187, v190, v191\n\t"
            "s_setprio 3\n\t"
            "v_mfma_f32_4x4x4_16b_bf16 v[196:199], v[184:185], v[184:185], v[196:199]\n\t"
            "s_mov_b32 exec_hi, 0x3fffff\n\t"
            "s_add_u32 m0, s66, 4360\n\t"
            "s_add_u32 s49, s68, 981120\n\t"
            "buffer_load_dwordx4 v249, s[72:75], s49 offen lds\n\t"
            "s_mov_b32 exec_hi, -1\n\t"
            "v_mfma_f32_4x4x4_16b_bf16 v[196:199], v[186:187], v[186:187], v[196:199]\n\t"
            "s_mov_b32 exec_hi, 0x3fffff\n\t"
            "s_add_u32 m0, s66, 5232\n\t"
            "s_add_u32 s49, s68, 1177344\n\t"
            "buffer_load_dwordx4 v249, s[72:75], s49 offen lds\n\t"
            "s_mov_b32 exec_hi, -1\n\t"
            "v_mfma_f32_16x16x32_bf16 v[192:195], v[238:241], v[184:187], v[192:195]\n\t"
            "s_setprio 1\n\t"
            "s_mov_b32 exec_hi, 0x3fffff\n\t"
            "s_add_u32 m0, s66, 6104\n\t"
            "s_add_u32 s49, s68, 1373568\n\t"
            "buffer_load_dwordx4 v249, s[72:75], s49 offen lds\n\t"
            "s_mov_b32 exec_hi, -1\n\t"
            "s_mov_b32 exec_hi, 0x3fffff\n\t"
            "s_add_u32 m0, s66, 6976\n\t"
            "s_add_u32 s49, s68, 1569792\n\t"
            "buffer_load_dwordx4 v249, s[72:75], s49 offen lds\n\t"
            "s_mov_b32 exec_hi, -1\n\t"
            "v_cndmask_b32_e64 v193, v196, v197, s[60:61]\n\t"
            "v_cndmask_b32_e64 v193, v193, v198, s[62:63]\n\t"
            "v_cndmask_b32_e64 v193, v193, v199, s[64:65]\n\t"
            "s_setprio 1\n\t"
            "ds_read_b128 v[196:199], v233 offset:6144\n\t"
            "ds_write_b64 v248, v[192:193] offset:18816\n\t"
            "ds_read_b128 v[192:195], v232 offset:6144\n\t"
            "ds_read_b64 v[248:249], v247 offset:8\n\t"
            "ds_write_b128 v237, v[184:187] offset:256\n\t"
            "ds_read_b64 v[236:237], v247\n\t"
            "s_mov_b64 exec, 1\n\t"
            "ds_inc_u32 v255, v238 offset:7860\n\t"
            "s_mov_b64 exec, -1\n\t"
            "s_waitcnt lgkmcnt(4)\n\t"
            "v_mfma_f32_16x16x32_bf16 v[144:147], v[192:195], v[0:3], 0\n\t"
            "s_waitcnt lgkmcnt(3)\n\t"
            "v_mul_f32 v202, v248, v202\n\t"
            "v_mul_f32 v203, v249, v203\n\t"
            "v_mfma_f32_16x16x32_bf16 v[144:147], v[196:199], v[4:7], v[144:147]\n\t"
            "v_mul_f32 v206, v248, v206\n\t"
            "s_waitcnt lgkmcnt(1)\n\t"
            "v_mul_f32 v200, v236, v200\n\t"
            "v_mfma_f32_16x16x32_bf16 v[148:151], v[196:199], v[28:31], 0\n\t"
            "v_mul_f32 v201, v237, v201\n\t"
            "v_mul_f32 v204, v236, v204\n\t"
            "v_mfma_f32_16x16x32_bf16 v[148:151], v[192:195], v[24:27], v[148:151]\n\t"
            "v_mul_f32 v205, v237, v205\n\t"
            "v_mul_f32 v208, v236, v208\n\t"
            "v_mfma_f32_16x16x32_bf16 v[168:171], v[192:195], v[48:51], 0\n\t"
            "v_mul_f32 v209, v237, v209\n\t"
            "v_mul_f32 v212, v236, v212\n\t"
            "v_mfma_f32_16x16x32_bf16 v[168:171], v[196:199], v[52:55], v[168:171]\n\t"
            "v_mul_f32 v213, v237, v213\n\t"
            "v_mul_f32 v216, v236, v216\n\t"
            "v_mfma_f32_16x16x32_bf16 v[172:175], v[196:199], v[76:79], 0\n\t"
            "v_mul_f32 v217, v237, v217\n\t"
            "v_mul_f32 v220, v236, v220\n\t"
            "v_mfma_f32_16x16x32_bf16 v[172:175], v[192:195], v[72:75], v[172:175]\n\t"
            "v_mul_f32 v221, v237, v221\n\t"
            "v_mul_f32 v207, v249, v207\n\t"
            "v_mfma_f32_16x16x32_bf16 v[152:155], v[192:195], v[96:99], 0\n\t"
            "ds_read_b64 v[236:237], v247 offset:64\n\t"
            "v_mul_f32 v210, v248, v210\n\t"
            "v_mfma_f32_16x16x32_bf16 v[152:155], v[196:199], v[100:103], v[152:155]\n\t"
            "v_mul_f32 v211, v249, v211\n\t"
            "v_mul_f32 v214, v248, v214\n\t"
            "v_mfma_f32_16x16x32_bf16 v[156:159], v[196:199], v[124:127], 0\n\t"
            "ds_read_b128 v[196:199], v233 offset:6400\n\t"
            "v_mul_f32 v215, v249, v215\n\t"
            "v_mfma_f32_16x16x32_bf16 v[156:159], v[192:195], v[120:123], v[156:159]\n\t"
            "ds_read_b128 v[192:195], v232 offset:6400\n\t"
            "v_mul_f32 v218, v248, v218\n\t"
            "v_mfma_f32_16x16x32_bf16 v[144:147], v[224:227], v[8:11], v[144:147]\n\t"
            "v_mul_f32 v219, v249, v219\n\t"
            "v_mul_f32 v222, v248, v222\n\t"
            "v_mfma_f32_16x16x32_bf16 v[144:147], v[228:231], v[12:15], v[144:147]\n\t"
            "v_mul_f32 v223, v249, v223\n\t"
            "v_cvt_pk_bf16_f32 v212, v212, v213\n\t"
            "v_mfma_f32_16x16x32_bf16 v[148:151], v[228:231], v[36:39], v[148:151]\n\t"
            "ds_read_b64 v[248:249], v247 offset:72\n\t"
            "v_cvt_pk_bf16_f32 v208, v208, v209\n\t"
            "v_mfma_f32_16x16x32_bf16 v[148:151], v[224:227], v[32:35], v[148:151]\n\t"
            "v_cvt_pk_bf16_f32 v213, v214, v215\n\t"
            "v_cvt_pk_bf16_f32 v209, v210, v211\n\t"
            "v_mfma_f32_16x16x32_bf16 v[168:171], v[224:227], v[56:59], v[168:171]\n\t"
            "v_cvt_pk_bf16_f32 v200, v200, v201\n\t"
            "v_cvt_pk_bf16_f32 v204, v204, v205\n\t"
            "v_mfma_f32_16x16x32_bf16 v[168:171], v[228:231], v[60:63], v[168:171]\n\t"
            "v_cvt_pk_bf16_f32 v216, v216, v217\n\t"
            "v_cvt_pk_bf16_f32 v220, v220, v221\n\t"
            "v_mfma_f32_16x16x32_bf16 v[172:175], v[228:231], v[84:87], v[172:175]\n\t"
            "v_cvt_pk_bf16_f32 v201, v202, v203\n\t"
            "v_cvt_pk_bf16_f32 v205, v206, v207\n\t"
            "v_mfma_f32_16x16x32_bf16 v[172:175], v[224:227], v[80:83], v[172:175]\n\t"
            "v_cvt_pk_bf16_f32 v217, v218, v219\n\t"
            "v_cvt_pk_bf16_f32 v221, v222, v223\n\t"
            "v_mfma_f32_16x16x32_bf16 v[152:155], v[224:227], v[104:107], v[152:155]\n\t"
            "v_mfma_f32_16x16x32_bf16 v[152:155], v[228:231], v[108:111], v[152:155]\n\t"
            "v_mfma_f32_16x16x32_bf16 v[156:159], v[228:231], v[132:135], v[156:159]\n\t"
            "ds_read_b128 v[228:231], v233 offset:12288\n\t"
            "v_mfma_f32_16x16x32_bf16 v[156:159], v[224:227], v[128:131], v[156:159]\n\t"
            "ds_read_b128 v[224:227], v232 offset:12288\n\t"
            "s_waitcnt lgkmcnt(3)\n\t"
            "v_mfma_f32_16x16x32_bf16 v[144:147], v[192:195], v[16:19], v[144:147]\n\t"
            "v_mfma_f32_16x16x32_bf16 v[144:147], v[196:199], v[20:23], v[144:147]\n\t"
            "v_mfma_f32_16x16x32_bf16 v[148:151], v[196:199], v[44:47], v[148:151]\n\t"
            "v_mfma_f32_16x16x32_bf16 v[148:151], v[192:195], v[40:43], v[148:151]\n\t"
            "v_mfma_f32_16x16x32_bf16 v[168:171], v[192:195], v[64:67], v[168:171]\n\t"
            "v_mfma_f32_16x16x32_bf16 v[168:171], v[196:199], v[68:71], v[168:171]\n\t"
            "ds_read_b64 v[234:235], v234 offset:7856\n\t"
            "v_mfma_f32_16x16x32_bf16 v[172:175], v[196:199], v[92:95], v[172:175]\n\t"
            "s_nop 1\n\t"
            "v_mul_f32 v144, v236, v144\n\t"
            "v_mfma_f32_16x16x32_bf16 v[172:175], v[192:195], v[88:91], v[172:175]\n\t"
            "v_mul_f32 v145, v237, v145\n\t"
            "s_waitcnt lgkmcnt(3)\n\t"
            "v_mul_f32 v146, v248, v146\n\t"
            "v_mfma_f32_16x16x32_bf16 v[152:155], v[192:195], v[112:115], v[152:155]\n\t"
            "v_mul_f32 v168, v236, v168\n\t"
            "v_mul_f32 v169, v237, v169\n\t"
            "v_mfma_f32_16x16x32_bf16 v[152:155], v[196:199], v[116:119], v[152:155]\n\t"
            "v_mul_f32 v170, v248, v170\n\t"
            "v_mul_f32 v171, v249, v171\n\t"
            "v_mfma_f32_16x16x32_bf16 v[156:159], v[196:199], v[140:143], v[156:159]\n\t"
            "ds_read_b128 v[196:199], v233 offset:12416\n\t"
            "v_mul_f32 v172, v236, v172\n\t"
            "v_mfma_f32_16x16x32_bf16 v[156:159], v[192:195], v[136:139], v[156:159]\n\t"
            "ds_read_b128 v[192:195], v232 offset:12416\n\t"
            "v_mul_f32 v173, v237, v173\n\t"
            "s_waitcnt lgkmcnt(3)\n\t"
            "v_mfma_f32_16x16x32_bf16 v[160:163], v[224:227], v[0:3], 0\n\t"
            "v_cvt_pk_bf16_f32 v214, v172, v173\n\t"
            "v_cvt_pk_bf16_f32 v210, v168, v169\n\t"
            "v_mfma_f32_16x16x32_bf16 v[160:163], v[228:231], v[4:7], v[160:163]\n\t"
            "s_waitcnt lgkmcnt(2)\n\t"
            "v_readfirstlane_b32 s77, v234\n\t"
            "v_readfirstlane_b32 s38, v235\n\t"
            "s_add_u32 s77, s77, s38\n\t"
            "s_cmp_ge_u32 s77, s44\n\t"
            "s_cbranch_scc0 .Lwc_spin%=\n\t"
            ".Lwc_go%=:\n\t"
            "v_mfma_f32_16x16x32_bf16 v[164:167], v[228:231], v[28:31], 0\n\t"
            "s_add_u32 s59, s59, s84\n\t"
            "s_movk_i32 s84, 800\n\t"
            "v_cvt_pk_bf16_f32 v211, v170, v171\n\t"
            "v_mfma_f32_16x16x32_bf16 v[164:167], v[224:227], v[24:27], v[164:167]\n\t"
            "s_add_u32 s45, s46, s27\n\t"
            "s_mov_b32 s46, s47\n\t"
            "s_mov_b32 s47, s68\n\t"
            "v_mfma_f32_16x16x32_bf16 v[176:179], v[224:227], v[48:51], 0\n\t"
            "v_mul_f32 v174, v248, v174\n\t"
            "v_mul_f32 v175, v249, v175\n\t"
            "v_mfma_f32_16x16x32_bf16 v[176:179], v[228:231], v[52:55], v[176:179]\n\t"
            "v_add_u32 v172, s67, v245\n\t"
            "v_cvt_pk_bf16_f32 v215, v174, v175\n\t"
            "v_mfma_f32_16x16x32_bf16 v[180:183], v[228:231], v[76:79], 0\n\t"
            "ds_read_b128 v[168:171], v172 offset:18816\n\t"
            "ds_read_b128 v[172:175], v172 offset:19840\n\t"
            "v_mfma_f32_16x16x32_bf16 v[180:183], v[224:227], v[72:75], v[180:183]\n\t"
            "v_mul_f32 v148, v236, v148\n\t"
            "v_mul_f32 v149, v237, v149\n\t"
            "v_mfma_f32_16x16x32_bf16 v[184:187], v[224:227], v[96:99], 0\n\t"
            "v_mul_f32 v152, v236, v152\n\t"
            "v_mul_f32 v153, v237, v153\n\t"
            "v_mfma_f32_16x16x32_bf16 v[184:187], v[228:231], v[100:103], v[184:187]\n\t"
            "v_mul_f32 v156, v236, v156\n\t"
            "v_mul_f32 v157, v237, v157\n\t"
            "v_mfma_f32_16x16x32_bf16 v[188:191], v[228:231], v[124:127], 0\n\t"
            "ds_read_b128 v[228:231], v233 offset:12544\n\t"
            "ds_read_b64 v[236:237], v247 offset:128\n\t"
            "v_mfma_f32_16x16x32_bf16 v[188:191], v[224:227], v[120:123], v[188:191]\n\t"
            "ds_read_b128 v[224:227], v232 offset:12544\n\t"
            "v_mul_f32 v147, v249, v147\n\t"
            "s_waitcnt lgkmcnt(5)\n\t"
            "v_mfma_f32_16x16x32_bf16 v[160:163], v[192:195], v[8:11], v[160:163]\n\t"
            "v_mul_f32 v150, v248, v150\n\t"
            "v_mul_f32 v151, v249, v151\n\t"
            "v_mfma_f32_16x16x32_bf16 v[160:163], v[196:199], v[12:15], v[160:163]\n\t"
            "v_mul_f32 v154, v248, v154\n\t"
            "v_mul_f32 v155, v249, v155\n\t"
            "v_mfma_f32_16x16x32_bf16 v[164:167], v[196:199], v[36:39], v[164:167]\n\t"
            "v_mul_f32 v158, v248, v158\n\t"
            "v_mul_f32 v159, v249, v159\n\t"
            "v_mfma_f32_16x16x32_bf16 v[164:167], v[192:195], v[32:35], v[164:167]\n\t"
            "ds_read_b64 v[248:249], v247 offset:136\n\t"
            "s_waitcnt lgkmcnt(5)\n\t"
            "v_add_f32 v168, v168, v170\n\t"
            "v_mfma_f32_16x16x32_bf16 v[176:179], v[192:195], v[56:59], v[176:179]\n\t"
            "s_waitcnt lgkmcnt(4)\n\t"
            "v_add_f32 v172, v172, v174\n\t"
            "v_add_f32 v168, v168, v172\n\t"
            "v_mfma_f32_16x16x32_bf16 v[176:179], v[196:199], v[60:63], v[176:179]\n\t"
            "v_add_f32 v169, v169, v171\n\t"
            "v_add_f32 v173, v173, v175\n\t"
            "v_mfma_f32_16x16x32_bf16 v[180:183], v[196:199], v[84:87], v[180:183]\n\t"
            "v_mul_f32 v168, 0x3baaaaab, v168\n\t"
            "v_add_f32 v169, v169, v173\n\t"
            "v_mfma_f32_16x16x32_bf16 v[180:183], v[192:195], v[80:83], v[180:183]\n\t"
            "ds_read_b128 v[172:175], v254 offset:64\n\t"
            "v_mul_f32_e64 v168, v168, -v168\n\t"
            "v_mfma_f32_16x16x32_bf16 v[184:187], v[192:195], v[104:107], v[184:187]\n\t"
            "v_fmac_f32 v168, 0x3baaaaab, v169\n\t"
            "v_max_f32 v168, 0, v168\n\t"
            "v_mfma_f32_16x16x32_bf16 v[184:187], v[196:199], v[108:111], v[184:187]\n\t"
            "v_cvt_pk_bf16_f32 v202, v144, v145\n\t"
            "v_cvt_pk_bf16_f32 v203, v146, v147\n\t"
            "v_mfma_f32_16x16x32_bf16 v[188:191], v[196:199], v[132:135], v[188:191]\n\t"
            "v_cvt_pk_bf16_f32 v206, v148, v149\n\t"
            "v_cvt_pk_bf16_f32 v207, v150, v151\n\t"
            "v_mfma_f32_16x16x32_bf16 v[188:191], v[192:195], v[128:131], v[188:191]\n\t"
            "v_cvt_pk_bf16_f32 v218, v152, v153\n\t"
            "v_cvt_pk_bf16_f32 v219, v154, v155\n\t"
            "s_waitcnt lgkmcnt(2)\n\t"
            "v_mfma_f32_16x16x32_bf16 v[160:163], v[224:227], v[16:19], v[160:163]\n\t"
            "v_cvt_pk_bf16_f32 v222, v156, v157\n\t"
            "v_cvt_pk_bf16_f32 v223, v158, v159\n\t"
            "v_mfma_f32_16x16x32_bf16 v[160:163], v[228:231], v[20:23], v[160:163]\n\t"
            "v_add_f32 v168, 0x3727c5ac, v168\n\t"
            "v_rsq_f32 v168, v168\n\t"
            "v_mfma_f32_16x16x32_bf16 v[164:167], v[228:231], v[44:47], v[164:167]\n\t"
            "v_add_u32 v232, s67, v242\n\t"
            "v_xor_b32 v233, 64, v232\n\t"
            "v_mfma_f32_16x16x32_bf16 v[164:167], v[224:227], v[40:43], v[164:167]\n\t"
            "s_xor_b32 s48, s48, s71\n\t"
            "s_xor_b32 s67, s67, s71\n\t"
            "s_add_u32 s76, s76, 4\n\t"
            "s_add_u32 s44, s44, 8\n\t"
            "v_mfma_f32_16x16x32_bf16 v[176:179], v[224:227], v[64:67], v[176:179]\n\t"
            "s_lshl_b32 s56, s56, 2\n\t"
            "s_lshr_b32 s49, s26, 30\n\t"
            "s_or_b32 s56, s56, s49\n\t"
            "v_mfma_f32_16x16x32_bf16 v[176:179], v[228:231], v[68:71], v[176:179]\n\t"
            "ds_read_b128 v[192:195], v232\n\t"
            "ds_read_b128 v[196:199], v233\n\t"
            "v_mfma_f32_16x16x32_bf16 v[180:183], v[228:231], v[92:95], v[180:183]\n\t"
            "v_mul_f32 v160, v236, v160\n\t"
            "v_mul_f32 v161, v237, v161\n\t"
            "v_mfma_f32_16x16x32_bf16 v[180:183], v[224:227], v[88:91], v[180:183]\n\t"
            "v_mul_f32 v164, v236, v164\n\t"
            "v_mul_f32 v165, v237, v165\n\t"
            "v_mfma_f32_16x16x32_bf16 v[184:187], v[224:227], v[112:115], v[184:187]\n\t"
            "v_mul_f32 v176, v236, v176\n\t"
            "v_mul_f32 v177, v237, v177\n\t"
            "v_mfma_f32_16x16x32_bf16 v[184:187], v[228:231], v[116:119], v[184:187]\n\t"
            "ds_write_b128 v243, v[200:203]\n\t"
            "v_mfma_f32_16x16x32_bf16 v[188:191], v[228:231], v[140:143], v[188:191]\n\t"
            "ds_read_b128 v[228:231], v233 offset:128\n\t"
            "v_mul_f32 v180, v236, v180\n\t"
            "v_mfma_f32_16x16x32_bf16 v[188:191], v[224:227], v[136:139], v[188:191]\n\t"
            "ds_read_b128 v[224:227], v232 offset:128\n\t"
            "v_mul_f32 v181, v237, v181\n\t"
            "s_waitcnt lgkmcnt(3)\n\t"
            "v_mfma_f32_16x16x32_bf16 v[200:203], v[192:195], v[0:3], 0\n\t"
            "v_mul_f32 v184, v236, v184\n\t"
            "v_mul_f32 v185, v237, v185\n\t"
            "v_mfma_f32_16x16x32_bf16 v[200:203], v[196:199], v[4:7], v[200:203]\n\t"
            "ds_write_b128 v243, v[204:207] offset:1536\n\t"
            "v_mfma_f32_16x16x32_bf16 v[204:207], v[196:199], v[28:31], 0\n\t"
            "v_mul_f32 v188, v236, v188\n\t"
            "v_mul_f32 v189, v237, v189\n\t"
            "v_mfma_f32_16x16x32_bf16 v[204:207], v[192:195], v[24:27], v[204:207]\n\t"
            "ds_write_b128 v243, v[208:211] offset:3072\n\t"
            "v_mfma_f32_16x16x32_bf16 v[208:211], v[192:195], v[48:51], 0\n\t"
            "v_mul_f32 v162, v248, v162\n\t"
            "v_mul_f32 v163, v249, v163\n\t"
            "v_mfma_f32_16x16x32_bf16 v[208:211], v[196:199], v[52:55], v[208:211]\n\t"
            "ds_write_b128 v243, v[212:215] offset:4608\n\t"
            "v_mfma_f32_16x16x32_bf16 v[212:215], v[196:199], v[76:79], 0\n\t"
            "v_cvt_pk_bf16_f32 v160, v160, v161\n\t"
            "v_mul_f32 v166, v248, v166\n\t"
            "v_mfma_f32_16x16x32_bf16 v[212:215], v[192:195], v[72:75], v[212:215]\n\t"
            "ds_write_b128 v243, v[216:219] offset:6144\n\t"
            "v_mfma_f32_16x16x32_bf16 v[216:219], v[192:195], v[96:99], 0\n\t"
            "v_mul_f32 v167, v249, v167\n\t"
            "v_cvt_pk_bf16_f32 v164, v164, v165\n\t"
            "v_mfma_f32_16x16x32_bf16 v[216:219], v[196:199], v[100:103], v[216:219]\n\t"
            "ds_write_b128 v243, v[220:223] offset:7680\n\t"
            "v_mfma_f32_16x16x32_bf16 v[220:223], v[196:199], v[124:127], 0\n\t"
            "v_mul_f32 v178, v248, v178\n\t"
            "v_mul_f32 v179, v249, v179\n\t"
            "v_mfma_f32_16x16x32_bf16 v[220:223], v[192:195], v[120:123], v[220:223]\n\t"
            "v_cvt_pk_bf16_f32 v176, v176, v177\n\t"
            "v_mul_f32 v182, v248, v182\n\t"
            "s_waitcnt lgkmcnt(5)\n\t"
            "v_mfma_f32_16x16x32_bf16 v[200:203], v[224:227], v[8:11], v[200:203]\n\t"
            "s_waitcnt vmcnt(0)\n\t"
            "ds_read_b64 v[144:145], v253\n\t"
            "ds_read_b64 v[146:147], v253 offset:96\n\t"
            "v_mfma_f32_16x16x32_bf16 v[200:203], v[228:231], v[12:15], v[200:203]\n\t"
            "ds_read_b64 v[148:149], v253 offset:1744\n\t"
            "ds_read_b64 v[150:151], v253 offset:1840\n\t"
            "v_mfma_f32_16x16x32_bf16 v[204:207], v[228:231], v[36:39], v[204:207]\n\t"
            "ds_read_b64 v[152:153], v253 offset:8\n\t"
            "ds_read_b64 v[154:155], v253 offset:104\n\t"
            "v_mfma_f32_16x16x32_bf16 v[204:207], v[224:227], v[32:35], v[204:207]\n\t"
            "ds_read_b64 v[156:157], v253 offset:1752\n\t"
            "ds_read_b64 v[158:159], v253 offset:1848\n\t"
            "v_mfma_f32_16x16x32_bf16 v[208:211], v[224:227], v[56:59], v[208:211]\n\t"
            "v_mul_f32 v183, v249, v183\n\t"
            "v_cvt_pk_bf16_f32 v180, v180, v181\n\t"
            "v_mfma_f32_16x16x32_bf16 v[208:211], v[228:231], v[60:63], v[208:211]\n\t"
            "v_mul_f32 v186, v248, v186\n\t"
            "v_mul_f32 v187, v249, v187\n\t"
            "v_mfma_f32_16x16x32_bf16 v[212:215], v[228:231], v[84:87], v[212:215]\n\t"
            "v_cvt_pk_bf16_f32 v184, v184, v185\n\t"
            "v_mul_f32 v190, v248, v190\n\t"
            "v_mfma_f32_16x16x32_bf16 v[212:215], v[224:227], v[80:83], v[212:215]\n\t"
            "v_mul_f32 v191, v249, v191\n\t"
            "v_cvt_pk_bf16_f32 v188, v188, v189\n\t"
            "v_mfma_f32_16x16x32_bf16 v[216:219], v[224:227], v[104:107], v[216:219]\n\t"
            "v_cvt_pk_bf16_f32 v161, v162, v163\n\t"
            "v_cvt_pk_bf16_f32 v165, v166, v167\n\t"
            "v_mfma_f32_16x16x32_bf16 v[216:219], v[228:231], v[108:111], v[216:219]\n\t"
            "ds_read_b64 v[162:163], v253 offset:112\n\t"
            "ds_read_b64 v[166:167], v253 offset:1856\n\t"
            "v_mfma_f32_16x16x32_bf16 v[220:223], v[228:231], v[132:135], v[220:223]\n\t"
            "s_waitcnt lgkmcnt(14)\n\t"
            "ds_read_b128 v[228:231], v233 offset:256\n\t"
            "v_cvt_pk_bf16_f32 v177, v178, v179\n\t"
            "v_mfma_f32_16x16x32_bf16 v[220:223], v[224:227], v[128:131], v[220:223]\n\t"
            "s_waitcnt lgkmcnt(14)\n\t"
            "ds_read_b128 v[224:227], v232 offset:256\n\t"
            "v_cvt_pk_bf16_f32 v181, v182, v183\n\t"
            "v_cvt_pk_bf16_f32 v185, v186, v187\n\t"
            "v_cvt_pk_bf16_f32 v189, v190, v191\n\t"
            "v_xor_b32 v247, 0x100, v247\n\t"
            "s_waitcnt lgkmcnt(14)\n\t"
            "ds_write_b64 v244, v[160:161]\n\t"
            "s_waitcnt lgkmcnt(14)\n\t"
            "ds_read_b64 v[160:161], v253 offset:16\n\t"
            "s_waitcnt lgkmcnt(14)\n\t"
            "ds_write_b64 v244, v[164:165] offset:1536\n\t"
            "s_waitcnt lgkmcnt(14)\n\t"
            "ds_read_b64 v[164:165], v253 offset:1760\n\t"
            "s_waitcnt lgkmcnt(14)\n\t"
            "ds_write_b64 v244, v[176:177] offset:3072\n\t"
            "s_waitcnt lgkmcnt(14)\n\t"
            "ds_write_b64 v244, v[180:181] offset:4608\n\t"
            "s_waitcnt lgkmcnt(14)\n\t"
            "ds_write_b64 v244, v[184:185] offset:6144\n\t"
            "s_waitcnt lgkmcnt(14)\n\t"
            "ds_write_b64 v244, v[188:189] offset:7680\n\t"
            "s_waitcnt lgkmcnt(14)\n\t"
            "ds_write_b32 v246, v168\n\t"
            "s_waitcnt lgkmcnt(14)\n\t"
            "ds_read_b128 v[168:171], v254\n\t"
            "s_waitcnt lgkmcnt(1)\n\t"
            "s_barrier\n\t"
            "s_cmp_lt_u32 s46, 0x7000000\n\t"
            "s_cbranch_scc1 .Lwc_loop%=\n\t"
            "s_branch .Lwc_end%=\n\t"
            ".Lwc_spin%=:\n\t"
            "s_cmp_eq_u32 s83, 0\n\t"
            "s_cbranch_scc1 .Lwc_go%=\n\t"
            "s_sub_u32 s83, s83, 1\n\t"
            "s_sleep 2\n\t"
            "v_mov_b32 v234, 0\n\t"
            "ds_read_b64 v[234:235], v234 offset:7856\n\t"
            "s_waitcnt lgkmcnt(0)\n\t"
            "v_readfirstlane_b32 s77, v234\n\t"
            "v_readfirstlane_b32 s38, v235\n\t"
            "s_add_u32 s77, s77, s38\n\t"
            "s_cmp_ge_u32 s77, s44\n\t"
            "s_cbranch_scc0 .Lwc_spin%=\n\t"
            "s_branch .Lwc_go%=\n\t"
            ".Lwc_end%=:\n\t"
            "s_waitcnt lgkmcnt(0)\n\t"
            "s_nop 7\n\t"
            : "+{v246}"(aRSw_), "+{v247}"(aRSr_), "+{s44}"(s44), B_SIO
            : "{v242}"(pE_), "{v243}"(aVAw_), "{v244}"(aVBw_), "{v245}"(pST_), "{v250}"(pXw_), "{v251}"(pSTw_), "{v252}"(aCA_), "{v253}"(aB_), "{v254}"(aA_), "{v255}"(idx_), B_SIN, "{s82}"(touch)
            : "v144", "v145", "v146", "v147", "v148", "v149", "v150", "v151", "v152", "v153", "v154", "v155", "v156", "v157", "v158", "v159", "v160", "v161", "v162", "v163", "v164", "v165", "v166", "v167", "v168", "v169", "v170", "v171", "v172", "v173", "v174", "v175", "v176", "v177", "v178", "v179", "v180", "v181", "v182", "v183", "v184", "v185", "v186", "v187", "v188", "v189", "v190", "v191", "v192", "v193", "v194", "v195", "v196", "v197", "v198", "v199", "v236", "v237", "v248", "v249", "v0", "v1", "v2", "v3", "v4", "v5", "v6", "v7", "v8", "v9", "v10", "v11", "v12", "v13", "v14", "v15", "v16", "v17", "v18", "v19", "v20", "v21", "v22", "v23", "v24", "v25", "v26", "v27", "v28", "v29", "v30", "v31", "v32", "v33", "v34", "v35", "v36", "v37", "v38", "v39", "v40", "v41", "v42", "v43", "v44", "v45", "v46", "v47", "v48", "v49", "v50", "v51", "v52", "v53", "v54", "v55", "v56", "v57", "v58", "v59", "v60", "v61", "v62", "v63", "v64", "v65", "v66", "v67", "v68", "v69", "v70", "v71", "v72", "v73", "v74", "v75", "v76", "v77", "v78", "v79", "v80", "v81", "v82", "v83", "v84", "v85", "v86", "v87", "v88", "v89", "v90", "v91", "v92", "v93", "v94", "v95", "v96", "v97", "v98", "v99", "v100", "v101", "v102", "v103", "v104", "v105", "v106", "v107", "v108", "v109", "v110", "v111", "v112", "v113", "v114", "v115", "v116", "v117", "v118", "v119", "v120", "v121", "v122", "v123", "v124", "v125", "v126", "v127", "v128", "v129", "v130", "v131", "v132", "v133", "v134", "v135", "v136", "v137", "v138", "v139", "v140", "v141", "v142", "v143", "v200", "v201", "v202", "v203", "v204", "v205", "v206", "v207", "v208", "v209", "v210", "v211", "v212", "v213", "v214", "v215", "v216", "v217", "v218", "v219", "v220", "v221", "v222", "v223", "v224", "v225", "v226", "v227", "v228", "v229", "v230", "v231", "v232", "v233", "v234", "v235", B_SCLOB);
        }
        {
            __builtin_amdgcn_s_setprio(3);
            int ln_;
            asm volatile("v_mbcnt_lo_u32_b32 %0, -1, 0\n\tv_mbcnt_hi_u32_b32 %0, -1, %0" : "=v"(ln_));
            const int l15_ = ln_ & 15, g_ = ln_ >> 4;
            KARGS
            const auto orsrc = __builtin_amdgcn_make_buffer_rsrc((void*)ob, 0, IMG * IMG * CH * 2, 0x00020000);
            const auto wq = __builtin_amdgcn_make_buffer_rsrc((void*)wb, 0, WS_BYTES, 0x00020000);
            bf16x8 W[36];
#pragma unroll
            for (int ks = 0; ks < 6; ++ks)
#pragma unroll
                for (int j = 0; j < 6; ++j) W[j * 6 + ks] = BC8(__builtin_amdgcn_raw_buffer_load_b128(wq, ln_ * 16, (wave + 2) * 36864 + (j * 6 + ks) * 1024, SC1));
            SB();
            int lE[2], nw;
            unsigned o[2];
            f32x4 acc[2][6];
#pragma unroll
            for (int lt = 0; lt < 2; ++lt) {
                int i = 16 * lt + l15_;
                const unsigned en = *(const unsigned*)(smem + WL_OFF + i * 4);
                const bool ok = en != 0x7ffffffu;
                if (lt == 1) nw = 16 + __builtin_popcount((unsigned)__builtin_amdgcn_ballot_w64(ok) & 0xffffu);
                i = ok ? i : 0;
                lE[lt] = OL_OFF + i * 384 + ((g_ ^ ((i >> 1) & 7)) << 4);
                o[lt] = ok ? (en & 0x7ffffffu) + LPOS(en >> 31) * ROWBYTES + LPOS(en >> 30) * (CH * 2) + 192 * w + 16 * g_ : OOB;
#pragma unroll
                for (int k = 0; k < 3; ++k) {
                    const int c8 = 12 * w + 4 * k + g_, ws = (c8 * 43) >> 8, r6 = c8 - 6 * ws;
                    const u32x4 r = *(const u32x4*)(smem + LX_OFF(i) + (r6 >> 1) * 128 + ((2 * ws + (r6 & 1)) << 4));
                    acc[lt][2 * k] = (f32x4){bflo(r[0]), bfhi(r[0]), bflo(r[1]), bfhi(r[1])};
                    acc[lt][2 * k + 1] = (f32x4){bflo(r[2]), bfhi(r[2]), bflo(r[3]), bfhi(r[3])};
                }
            }
            SB();
            for (int it_ = 0; it_ < 100000; ++it_) {
                unsigned n_;
                asm volatile("ds_read_b32 %0, %1\n\ts_waitcnt lgkmcnt(0)" : "=v"(n_) : "v"(ldsb + FLAG_OFF) : "memory");
                if (__builtin_amdgcn_readfirstlane(n_) >= 4 * nw) break;
                __builtin_amdgcn_s_sleep(1);
            }
            bf16x8 of[2][6];
#pragma unroll
            for (int ks = 0; ks < 6; ++ks)
#pragma unroll
                for (int lt = 0; lt < 2; ++lt) of[lt][ks] = *(const bf16x8*)(smem + ((ks & 1) ? (lE[lt] ^ 64) : lE[lt]) + (ks >> 1) * 128);
            SB();
#pragma unroll
            for (int lt = 0; lt < 2; ++lt) {
#pragma unroll
                for (int ks = 0; ks < 6; ++ks)
#pragma unroll
                    for (int j = 0; j < 6; ++j) acc[lt][j] = MFMA32(W[j * 6 + ks], of[lt][ks], acc[lt][j]);
#pragma unroll
                for (int k = 0; k < 3; ++k) {
                    const f32x4 a = acc[lt][2 * k], b = acc[lt][2 * k + 1];
                    const u32x4 ov = {pk2(a[0], a[1]), pk2(a[2], a[3]), pk2(b[0], b[1]), pk2(b[2], b[3])};
                    __builtin_amdgcn_raw_buffer_store_b128(ov, orsrc, o[lt] + 64 * k, 0, SC1);
                }
            }
        }
        __builtin_amdgcn_endpgm();
    } else {
        LANE_SETUP
        {
            const unsigned oE_ = ldsb + fE + (O_OFF - X_OFF), oO_ = oE_ ^ 64;
            unsigned l16_ = lane * 16;
            unsigned pr[3];
#pragma unroll
            for (int k = 0; k < 3; ++k) {
                const int c8 = 12 * (w - 2) + 4 * k + g;
                const int ws = c8 / 6, r6 = c8 - 6 * ws;
                pr[k] = l15 * 384 + (r6 >> 1) * 128 + (((2 * ws + (r6 & 1)) ^ sw) << 4);
            }
            const unsigned pR0_ = pr[0], pR1_ = pr[1], pR2_ = pr[2];
            unsigned q0 = ldsb + PF_OFF + 1024 + lane * 4, q1 = ldsb + O_OFF + lane * 16, q2 = oE_, q3 = ldsb + PF_OFF + 1024 + l15 * 4;
            unsigned q4 = ldsb + OL_OFF + l15 * 384 + (((4 * (w - 2) + (g >> 1)) ^ sw) << 4) + 8 * (g & 1);
            B_SCALARS
            asm volatile(
            "s_waitcnt lgkmcnt(0)\n\t"
            "s_mov_b32 s83, 0x10000\n\t"
            "s_mov_b32 s84, -47104\n\t"
            "s_mov_b64 s[88:89], s[72:73]\n\t"
            "s_mov_b64 s[90:91], s[74:75]\n\t"
            "s_nop 7\n\t"
            "s_barrier\n\t"
            "ds_read_b64 v[236:237], v252 offset:2560\n\t"
            "s_waitcnt lgkmcnt(0)\n\t"
            "s_mov_b64 exec, 1\n\t"
            "buffer_store_dwordx2 v[236:237], off, s[28:31], s44 sc1\n\t"
            "s_mov_b64 exec, -1\n\t"
            "s_sleep 30\n\t"
            "ds_read_b32 v249, v252 offset:12\n\t"
            "s_waitcnt lgkmcnt(0)\n\t"
            "v_and_b32 v249, s69, v249\n\t"
            "s_mov_b32 exec_hi, 0x3fffff\n\t"
            "s_add_u32 m0, s66, 0\n\t"
            "s_add_u32 s49, s79, 0\n\t"
            "buffer_load_dwordx4 v249, s[72:75], s49 offen lds\n\t"
            "s_mov_b32 exec_hi, -1\n\t"
            "s_mov_b32 exec_hi, 0x3fffff\n\t"
            "s_add_u32 m0, s66, 872\n\t"
            "s_add_u32 s49, s47, 196224\n\t"
            "buffer_load_dwordx4 v249, s[72:75], s49 offen lds\n\t"
            "s_mov_b32 exec_hi, -1\n\t"
            "s_mov_b32 exec_hi, 0x3fffff\n\t"
            "s_add_u32 m0, s66, 1744\n\t"
            "s_add_u32 s49, s47, 392448\n\t"
            "buffer_load_dwordx4 v249, s[72:75], s49 offen lds\n\t"
            "s_mov_b32 exec_hi, -1\n\t"
            "s_mov_b32 exec_hi, 0x3fffff\n\t"
            "s_add_u32 m0, s66, 2616\n\t"
            "s_add_u32 s49, s47, 588672\n\t"
            "buffer_load_dwordx4 v249, s[72:75], s49 offen lds\n\t"
            "s_mov_b32 exec_hi, -1\n\t"
            "s_mov_b32 exec_hi, 0x3fffff\n\t"
            "s_add_u32 m0, s66, 3488\n\t"
            "s_add_u32 s49, s47, 784896\n\t"
            "buffer_load_dwordx4 v249, s[72:75], s49 offen lds\n\t"
            "s_mov_b32 exec_hi, -1\n\t"
            "s_mov_b32 exec_hi, 0x3fffff\n\t"
            "s_add_u32 m0, s66, 4360\n\t"
            "s_add_u32 s49, s47, 981120\n\t"
            "buffer_load_dwordx4 v249, s[72:75], s49 offen lds\n\t"
            "s_mov_b32 exec_hi, -1\n\t"
            "s_mov_b32 exec_hi, 0x3fffff\n\t"
            "s_add_u32 m0, s66, 5232\n\t"
            "s_add_u32 s49, s47, 1177344\n\t"
            "buffer_load_dwordx4 v249, s[72:75], s49 offen lds\n\t"
            "s_mov_b32 exec_hi, -1\n\t"
            "s_mov_b32 exec_hi, 0x3fffff\n\t"
            "s_add_u32 m0, s66, 6104\n\t"
            "s_add_u32 s49, s47, 1373568\n\t"
            "buffer_load_dwordx4 v249, s[72:75], s49 offen lds\n\t"
            "s_mov_b32 exec_hi, -1\n\t"
            "s_mov_b32 exec_hi, 0x3fffff\n\t"
            "s_add_u32 m0, s66, 6976\n\t"
            "s_add_u32 s49, s47, 1569792\n\t"
            "buffer_load_dwordx4 v249, s[72:75], s49 offen lds\n\t"
            "s_mov_b32 exec_hi, -1\n\t"
            "s_add_u32 s49, s33, -73728\n\t"
            "buffer_load_dwordx4 v[0:3], v247, s[28:31], s49 offen sc1\n\t"
            "s_add_u32 s49, s33, -67584\n\t"
            "buffer_load_dwordx4 v[24:27], v247, s[28:31], s49 offen sc1\n\t"
            "s_add_u32 s49, s33, -61440\n\t"
            "buffer_load_dwordx4 v[48:51], v247, s[28:31], s49 offen sc1\n\t"
            "s_add_u32 s49, s33, -55296\n\t"
            "buffer_load_dwordx4 v[72:75], v247, s[28:31], s49 offen sc1\n\t"
            "s_add_u32 s49, s33, -49152\n\t"
            "buffer_load_dwordx4 v[96:99], v247, s[28:31], s49 offen sc1\n\t"
            "s_add_u32 s49, s33, -43008\n\t"
            "buffer_load_dwordx4 v[120:123], v247, s[28:31], s49 offen sc1\n\t"
            "s_add_u32 s49, s33, -72704\n\t"
            "buffer_load_dwordx4 v[4:7], v247, s[28:31], s49 offen sc1\n\t"
            "s_add_u32 s49, s33, -66560\n\t"
            "buffer_load_dwordx4 v[28:31], v247, s[28:31], s49 offen sc1\n\t"
            "s_add_u32 s49, s33, -60416\n\t"
            "buffer_load_dwordx4 v[52:55], v247, s[28:31], s49 offen sc1\n\t"
            "s_add_u32 s49, s33, -54272\n\t"
            "buffer_load_dwordx4 v[76:79], v247, s[28:31], s49 offen sc1\n\t"
            "s_add_u32 s49, s33, -48128\n\t"
            "buffer_load_dwordx4 v[100:103], v247, s[28:31], s49 offen sc1\n\t"
            "s_add_u32 s49, s33, -41984\n\t"
            "buffer_load_dwordx4 v[124:127], v247, s[28:31], s49 offen sc1\n\t"
            "s_add_u32 s49, s33, -71680\n\t"
            "buffer_load_dwordx4 v[8:11], v247, s[28:31], s49 offen sc1\n\t"
            "s_add_u32 s49, s33, -65536\n\t"
            "buffer_load_dwordx4 v[32:35], v247, s[28:31], s49 offen sc1\n\t"
            "s_add_u32 s49, s33, -59392\n\t"
            "buffer_load_dwordx4 v[56:59], v247, s[28:31], s49 offen sc1\n\t"
            "s_add_u32 s49, s33, -53248\n\t"
            "buffer_load_dwordx4 v[80:83], v247, s[28:31], s49 offen sc1\n\t"
            "s_add_u32 s49, s33, -47104\n\t"
            "buffer_load_dwordx4 v[104:107], v247, s[28:31], s49 offen sc1\n\t"
            "s_add_u32 s49, s33, -40960\n\t"
            "buffer_load_dwordx4 v[128:131], v247, s[28:31], s49 offen sc1\n\t"
            "s_add_u32 s49, s33, -70656\n\t"
            "buffer_load_dwordx4 v[12:15], v247, s[28:31], s49 offen sc1\n\t"
            "s_add_u32 s49, s33, -64512\n\t"
            "buffer_load_dwordx4 v[36:39], v247, s[28:31], s49 offen sc1\n\t"
            "s_add_u32 s49, s33, -58368\n\t"
            "buffer_load_dwordx4 v[60:63], v247, s[28:31], s49 offen sc1\n\t"
            "s_add_u32 s49, s33, -52224\n\t"
            "buffer_load_dwordx4 v[84:87], v247, s[28:31], s49 offen sc1\n\t"
            "s_add_u32 s49, s33, -46080\n\t"
            "buffer_load_dwordx4 v[108:111], v247, s[28:31], s49 offen sc1\n\t"
            "s_add_u32 s49, s33, -39936\n\t"
            "buffer_load_dwordx4 v[132:135], v247, s[28:31], s49 offen sc1\n\t"
            "s_add_u32 s49, s33, -69632\n\t"
            "buffer_load_dwordx4 v[16:19], v247, s[28:31], s49 offen sc1\n\t"
            "s_add_u32 s49, s33, -63488\n\t"
            "buffer_load_dwordx4 v[40:43], v247, s[28:31], s49 offen sc1\n\t"
            "s_add_u32 s49, s33, -57344\n\t"
            "buffer_load_dwordx4 v[64:67], v247, s[28:31], s49 offen sc1\n\t"
            "s_add_u32 s49, s33, -51200\n\t"
            "buffer_load_dwordx4 v[88:91], v247, s[28:31], s49 offen sc1\n\t"
            "s_add_u32 s49, s33, -45056\n\t"
            "buffer_load_dwordx4 v[112:115], v247, s[28:31], s49 offen sc1\n\t"
            "s_add_u32 s49, s33, -38912\n\t"
            "buffer_load_dwordx4 v[136:139], v247, s[28:31], s49 offen sc1\n\t"
            "s_add_u32 s49, s33, -68608\n\t"
            "buffer_load_dwordx4 v[20:23], v247, s[28:31], s49 offen sc1\n\t"
            "s_add_u32 s49, s33, -62464\n\t"
            "buffer_load_dwordx4 v[44:47], v247, s[28:31], s49 offen sc1\n\t"
            "s_add_u32 s49, s33, -56320\n\t"
            "buffer_load_dwordx4 v[68:71], v247, s[28:31], s49 offen sc1\n\t"
            "s_add_u32 s49, s33, -50176\n\t"
            "buffer_load_dwordx4 v[92:95], v247, s[28:31], s49 offen sc1\n\t"
            "s_add_u32 s49, s33, -44032\n\t"
            "buffer_load_dwordx4 v[116:119], v247, s[28:31], s49 offen sc1\n\t"
            "s_add_u32 s49, s33, -37888\n\t"
            "buffer_load_dwordx4 v[140:143], v247, s[28:31], s49 offen sc1\n\t"
            "s_setprio 3\n\t"
            "ds_read_b128 v[168:171], v254\n\t"
            "ds_read_b128 v[172:175], v254 offset:64\n\t"
            "ds_read_b128 v[220:223], v229 offset:12288\n\t"
            "ds_read_b128 v[224:227], v229 offset:13312\n\t"
            "ds_read_b128 v[200:203], v232\n\t"
            "ds_read_b128 v[208:211], v232 offset:128\n\t"
            "ds_read_b128 v[216:219], v232 offset:256\n\t"
            "s_waitcnt vmcnt(36)\n\t"
            "ds_read_b64 v[144:145], v253\n\t"
            "ds_read_b64 v[146:147], v253 offset:96\n\t"
            "ds_read_b64 v[148:149], v253 offset:1744\n\t"
            "ds_read_b64 v[150:151], v253 offset:1840\n\t"
            "ds_read_b64 v[152:153], v253 offset:8\n\t"
            "ds_read_b64 v[154:155], v253 offset:104\n\t"
            "ds_read_b64 v[156:157], v253 offset:1752\n\t"
            "ds_read_b64 v[158:159], v253 offset:1848\n\t"
            "s_waitcnt lgkmcnt(14)\n\t"
            "ds_read_b64 v[160:161], v253 offset:16\n\t"
            "s_waitcnt lgkmcnt(14)\n\t"
            "ds_read_b64 v[162:163], v253 offset:112\n\t"
            "s_waitcnt lgkmcnt(14)\n\t"
            "ds_read_b64 v[164:165], v253 offset:1760\n\t"
            "s_waitcnt lgkmcnt(14)\n\t"
            "ds_read_b64 v[166:167], v253 offset:1856\n\t"
            "v_mov_b64 v[176:177], 0\n\t"
            "v_mov_b64 v[178:179], 0\n\t"
            "v_mov_b64 v[180:181], 0\n\t"
            "v_mov_b64 v[182:183], 0\n\t"
            "v_mov_b32 v249, s76\n\t"
            "v_mov_b64 v[184:185], 0\n\t"
            "v_mov_b64 v[186:187], 0\n\t"
            "v_mov_b64 v[188:189], 0\n\t"
            "v_mov_b64 v[190:191], 0\n\t"
            "v_add_u32 v236, s67, v250\n\t"
            "v_add_u32 v248, s67, v251\n\t"
            "v_xor_b32 v235, 64, v232\n\t"
            "s_nop 0\n\t"
            "s_waitcnt lgkmcnt(14)\n\t"
            "ds_read_b32 v249, v249\n\t"
            "s_waitcnt lgkmcnt(14)\n\t"
            "ds_read_b128 v[204:207], v235\n\t"
            "s_waitcnt lgkmcnt(14)\n\t"
            "ds_read_b128 v[212:215], v235 offset:128\n\t"
            "v_add_f32 v220, v220, v222\n\t"
            "v_add_f32 v224, v224, v226\n\t"
            "v_add_f32 v220, v220, v224\n\t"
            "v_add_f32 v221, v221, v223\n\t"
            "v_add_f32 v225, v225, v227\n\t"
            "v_mul_f32 v220, 0x3baaaaab, v220\n\t"
            "v_add_f32 v221, v221, v225\n\t"
            "v_mul_f32_e64 v220, v220, -v220\n\t"
            "v_fmac_f32 v220, 0x3baaaaab, v221\n\t"
            "s_waitcnt lgkmcnt(11)\n\t"
            "v_smfmac_f32_16x16x64_bf16 v[176:179], v[168:171], v[144:151], v255\n\t"
            "ds_read_b128 v[168:171], v254 offset:128\n\t"
            "s_waitcnt lgkmcnt(8)\n\t"
            "v_smfmac_f32_16x16x64_bf16 v[180:183], v[172:175], v[152:159], v255\n\t"
            "ds_read_b64 v[144:145], v253 offset:24\n\t"
            "ds_read_b64 v[146:147], v253 offset:120\n\t"
            "ds_read_b64 v[148:149], v253 offset:1768\n\t"
            "ds_read_b64 v[150:151], v253 offset:1864\n\t"
            "ds_read_b128 v[172:175], v254 offset:192\n\t"
            "ds_read_b64 v[152:153], v253 offset:32\n\t"
            "ds_read_b64 v[154:155], v253 offset:128\n\t"
            "s_waitcnt lgkmcnt(14)\n\t"
            "ds_read_b64 v[156:157], v253 offset:1776\n\t"
            "s_waitcnt lgkmcnt(14)\n\t"
            "ds_read_b64 v[158:159], v253 offset:1872\n\t"
            "s_waitcnt lgkmcnt(14)\n\t"
            "ds_read_b128 v[224:227], v232 offset:6144\n\t"
            "v_cvt_pk_bf16_f32 v176, v176, v177\n\t"
            "v_cvt_pk_bf16_f32 v177, v178, v179\n\t"
            "v_cvt_pk_bf16_f32 v178, v180, v181\n\t"
            "v_cvt_pk_bf16_f32 v179, v182, v183\n\t"
            "v_mfma_f32_4x4x4_16b_bf16 v[196:199], v[176:177], v[176:177], 0\n\t"
            "v_mov_b64 v[180:181], 0\n\t"
            "v_mfma_f32_16x16x32_bf16 v[192:195], v[238:241], v[176:179], 0\n\t"
            "s_waitcnt lgkmcnt(13)\n\t"
            "v_readfirstlane_b32 s26, v249\n\t"
            "v_mov_b64 v[182:183], 0\n\t"
            "v_mfma_f32_4x4x4_16b_bf16 v[196:199], v[178:179], v[178:179], v[196:199]\n\t"
            "ds_read_b32 v249, v252 offset:12\n\t"
            "s_and_b32 s68, s26, 0x7ffffff\n\t"
            "s_add_u32 s68, s68, s78\n\t"
            "s_and_b32 s69, s26, 0x38000000\n\t"
            "s_or_b32 s69, s69, s39\n\t"
            "s_bitcmp1_b32 s26, 27\n\t"
            "s_cselect_b32 s79, 2.0, s68\n\t"
            "s_and_b32 s57, s56, 3\n\t"
            "s_lshl_b32 s58, s57, 3\n\t"
            "s_lshr_b32 m0, s54, s58\n\t"
            "s_lshl_b32 s58, s57, 4\n\t"
            "s_lshr_b64 s[36:37], s[50:51], s58\n\t"
            "s_and_b32 s57, s36, 0xffff\n\t"
            "s_add_u32 s57, s57, s67\n\t"
            "v_writelane_b32 v236, s57, 63\n\t"
            "v_writelane_b32 v236, s59, m0\n\t"
            "v_xor_b32 v237, 16, v236\n\t"
            "s_lshr_b64 s[36:37], s[52:53], s58\n\t"
            "s_and_b32 s58, s36, 0xffff\n\t"
            "s_add_u32 s58, s58, s67\n\t"
            "s_waitcnt lgkmcnt(11)\n\t"
            "v_smfmac_f32_16x16x64_bf16 v[184:187], v[168:171], v[160:167], v255\n\t"
            "s_and_b32 s38, s52, 0xffff\n\t"
            "s_add_u32 s38, s38, s67\n\t"
            "ds_read_b128 v[168:171], v254 offset:256\n\t"
            "ds_read_b64 v[160:161], v253 offset:40\n\t"
            "ds_read_b64 v[162:163], v253 offset:136\n\t"
            "ds_read_b64 v[164:165], v253 offset:1784\n\t"
            "s_waitcnt lgkmcnt(14)\n\t"
            "ds_read_b64 v[166:167], v253 offset:1880\n\t"
            "s_waitcnt lgkmcnt(11)\n\t"
            "v_smfmac_f32_16x16x64_bf16 v[188:191], v[172:175], v[144:151], v255\n\t"
            "ds_read_b128 v[172:175], v254 offset:320\n\t"
            "ds_read_b64 v[144:145], v253 offset:48\n\t"
            "ds_read_b64 v[146:147], v253 offset:144\n\t"
            "ds_read_b64 v[148:149], v253 offset:1792\n\t"
            "s_waitcnt lgkmcnt(14)\n\t"
            "ds_read_b64 v[150:151], v253 offset:1888\n\t"
            "s_waitcnt lgkmcnt(14)\n\t"
            "ds_write_b128 v236, v[176:179]\n\t"
            "v_mov_b64 v[176:177], 0\n\t"
            "v_mov_b64 v[178:179], 0\n\t"
            "v_cvt_pk_bf16_f32 v184, v184, v185\n\t"
            "v_cvt_pk_bf16_f32 v185, v186, v187\n\t"
            "v_cvt_pk_bf16_f32 v186, v188, v189\n\t"
            "v_cvt_pk_bf16_f32 v187, v190, v191\n\t"
            "v_mfma_f32_4x4x4_16b_bf16 v[196:199], v[184:185], v[184:185], v[196:199]\n\t"
            "s_waitcnt lgkmcnt(14)\n\t"
            "ds_write_b128 v237, v[184:187]\n\t"
            "v_mfma_f32_16x16x32_bf16 v[192:195], v[238:241], v[184:187], v[192:195]\n\t"
            "v_mov_b64 v[184:185], 0\n\t"
            "v_mov_b64 v[188:189], 0\n\t"
            "v_mfma_f32_4x4x4_16b_bf16 v[196:199], v[186:187], v[186:187], v[196:199]\n\t"
            "v_mov_b64 v[186:187], 0\n\t"
            "v_mov_b64 v[190:191], 0\n\t"
            "v_writelane_b32 v248, s58, 63\n\t"
            "v_writelane_b32 v248, s38, m0\n\t"
            "s_waitcnt lgkmcnt(12)\n\t"
            "v_and_b32 v249, s69, v249\n\t"
            "v_max_f32 v220, 0, v220\n\t"
            "v_add_f32 v220, 0x3727c5ac, v220\n\t"
            "v_rsq_f32 v220, v220\n\t"
            "s_waitcnt lgkmcnt(11)\n\t"
            "v_smfmac_f32_16x16x64_bf16 v[176:179], v[168:171], v[152:159], v255\n\t"
            "ds_read_b128 v[168:171], v254 offset:384\n\t"
            "ds_read_b64 v[152:153], v253 offset:56\n\t"
            "ds_read_b64 v[154:155], v253 offset:152\n\t"
            "ds_read_b64 v[156:157], v253 offset:1800\n\t"
            "s_waitcnt lgkmcnt(14)\n\t"
            "ds_read_b64 v[158:159], v253 offset:1896\n\t"
            "s_waitcnt lgkmcnt(11)\n\t"
            "v_smfmac_f32_16x16x64_bf16 v[180:183], v[172:175], v[160:167], v255\n\t"
            "ds_read_b128 v[172:175], v254 offset:448\n\t"
            "ds_read_b64 v[160:161], v253 offset:64\n\t"
            "ds_read_b64 v[162:163], v253 offset:160\n\t"
            "ds_read_b64 v[164:165], v253 offset:1808\n\t"
            "s_waitcnt lgkmcnt(14)\n\t"
            "ds_read_b64 v[166:167], v253 offset:1904\n\t"
            "v_cvt_pk_bf16_f32 v176, v176, v177\n\t"
            "v_cvt_pk_bf16_f32 v177, v178, v179\n\t"
            "v_cvt_pk_bf16_f32 v178, v180, v181\n\t"
            "v_cvt_pk_bf16_f32 v179, v182, v183\n\t"
            "v_mfma_f32_4x4x4_16b_bf16 v[196:199], v[176:177], v[176:177], v[196:199]\n\t"
            "s_waitcnt lgkmcnt(14)\n\t"
            "ds_write_b128 v236, v[176:179] offset:128\n\t"
            "v_mfma_f32_16x16x32_bf16 v[192:195], v[238:241], v[176:179], v[192:195]\n\t"
            "v_mov_b64 v[176:177], 0\n\t"
            "v_mov_b64 v[180:181], 0\n\t"
            "v_mfma_f32_4x4x4_16b_bf16 v[196:199], v[178:179], v[178:179], v[196:199]\n\t"
            "v_mov_b64 v[178:179], 0\n\t"
            "v_mov_b64 v[182:183], 0\n\t"
            "s_waitcnt lgkmcnt(14)\n\t"
            "ds_write_b32 v228, v220\n\t"
            "s_waitcnt lgkmcnt(14)\n\t"
            "ds_read_b128 v[220:223], v235 offset:256\n\t"
            "s_waitcnt lgkmcnt(14)\n\t"
            "ds_read_b32 v230, v233\n\t"
            "s_waitcnt lgkmcnt(14)\n\t"
            "ds_read_b32 v231, v233 offset:64\n\t"
            "v_xor_b32 v228, 32, v234\n\t"
            "s_waitcnt lgkmcnt(14)\n\t"
            "v_smfmac_f32_16x16x64_bf16 v[184:187], v[168:171], v[144:151], v255\n\t"
            "ds_read_b128 v[168:171], v254 offset:512\n\t"
            "s_waitcnt lgkmcnt(14)\n\t"
            "ds_read_b64 v[144:145], v253 offset:72\n\t"
            "s_waitcnt lgkmcnt(14)\n\t"
            "ds_read_b64 v[146:147], v253 offset:168\n\t"
            "s_waitcnt lgkmcnt(14)\n\t"
            "ds_read_b64 v[148:149], v253 offset:1816\n\t"
            "s_waitcnt lgkmcnt(14)\n\t"
            "ds_read_b64 v[150:151], v253 offset:1912\n\t"
            "s_waitcnt lgkmcnt(14)\n\t"
            "v_smfmac_f32_16x16x64_bf16 v[188:191], v[172:175], v[152:159], v255\n\t"
            "ds_read_b128 v[172:175], v254 offset:576\n\t"
            "s_waitcnt lgkmcnt(14)\n\t"
            "ds_read_b64 v[152:153], v253 offset:80\n\t"
            "s_waitcnt lgkmcnt(14)\n\t"
            "ds_read_b64 v[154:155], v253 offset:176\n\t"
            "s_waitcnt lgkmcnt(14)\n\t"
            "ds_read_b64 v[156:157], v253 offset:1824\n\t"
            "s_waitcnt lgkmcnt(14)\n\t"
            "ds_read_b64 v[158:159], v253 offset:1920\n\t"
            "v_cvt_pk_bf16_f32 v184, v184, v185\n\t"
            "v_cvt_pk_bf16_f32 v185, v186, v187\n\t"
            "v_cvt_pk_bf16_f32 v186, v188, v189\n\t"
            "v_cvt_pk_bf16_f32 v187, v190, v191\n\t"
            "v_mfma_f32_4x4x4_16b_bf16 v[196:199], v[184:185], v[184:185], v[196:199]\n\t"
            "s_waitcnt lgkmcnt(14)\n\t"
            "ds_write_b128 v237, v[184:187] offset:128\n\t"
            "v_mfma_f32_16x16x32_bf16 v[192:195], v[238:241], v[184:187], v[192:195]\n\t"
            "v_mov_b64 v[184:185], 0\n\t"
            "v_mov_b64 v[188:189], 0\n\t"
            "v_mfma_f32_4x4x4_16b_bf16 v[196:199], v[186:187], v[186:187], v[196:199]\n\t"
            "v_mov_b64 v[186:187], 0\n\t"
            "v_mov_b64 v[190:191], 0\n\t"
            "s_waitcnt lgkmcnt(10)\n\t"
            "v_smfmac_f32_16x16x64_bf16 v[176:179], v[168:171], v[160:167], v255\n\t"
            "ds_read_b128 v[168:171], v254 offset:640\n\t"
            "ds_read_b64 v[160:161], v253 offset:88\n\t"
            "ds_read_b64 v[162:163], v253 offset:184\n\t"
            "ds_read_b64 v[164:165], v253 offset:1832\n\t"
            "ds_read_b64 v[166:167], v253 offset:1928\n\t"
            "s_waitcnt lgkmcnt(10)\n\t"
            "v_smfmac_f32_16x16x64_bf16 v[180:183], v[172:175], v[144:151], v255\n\t"
            "ds_read_b128 v[172:175], v254 offset:704\n\t"
            "v_cvt_pk_bf16_f32 v176, v176, v177\n\t"
            "v_cvt_pk_bf16_f32 v177, v178, v179\n\t"
            "s_nop 1\n\t"
            "v_mfma_f32_4x4x4_16b_bf16 v[196:199], v[176:177], v[176:177], v[196:199]\n\t"
            "s_nop 1\n\t"
            "v_cvt_pk_bf16_f32 v178, v180, v181\n\t"
            "v_cvt_pk_bf16_f32 v179, v182, v183\n\t"
            "s_nop 1\n\t"
            "v_mfma_f32_4x4x4_16b_bf16 v[196:199], v[178:179], v[178:179], v[196:199]\n\t"
            "ds_write_b128 v236, v[176:179] offset:256\n\t"
            "v_mfma_f32_16x16x32_bf16 v[192:195], v[238:241], v[176:179], v[192:195]\n\t"
            "v_add_u32 v236, s48, v244\n\t"
            "s_waitcnt lgkmcnt(6)\n\t"
            "v_smfmac_f32_16x16x64_bf16 v[184:187], v[168:171], v[152:159], v255\n\t"
            "s_waitcnt lgkmcnt(1)\n\t"
            "v_smfmac_f32_16x16x64_bf16 v[188:191], v[172:175], v[160:167], v255\n\t"
            "s_mov_b32 exec_hi, 0x3fffff\n\t"
            "s_add_u32 m0, s66, 0\n\t"
            "s_add_u32 s49, s79, 0\n\t"
            "buffer_load_dwordx4 v249, s[88:91], s49 offen lds\n\t"
            "s_mov_b32 exec_hi, -1\n\t"
            "s_mov_b32 exec_hi, 0x3fffff\n\t"
            "s_add_u32 m0, s66, 872\n\t"
            "s_add_u32 s49, s68, 196224\n\t"
            "buffer_load_dwordx4 v249, s[72:75], s49 offen lds\n\t"
            "s_mov_b32 exec_hi, -1\n\t"
            "s_mov_b32 exec_hi, 0x3fffff\n\t"
            "s_add_u32 m0, s66, 1744\n\t"
            "s_add_u32 s49, s68, 392448\n\t"
            "buffer_load_dwordx4 v249, s[72:75], s49 offen lds\n\t"
            "s_mov_b32 exec_hi, -1\n\t"
            "s_mov_b32 exec_hi, 0x3fffff\n\t"
            "s_add_u32 m0, s66, 2616\n\t"
            "s_add_u32 s49, s68, 588672\n\t"
            "buffer_load_dwordx4 v249, s[72:75], s49 offen lds\n\t"
            "s_mov_b32 exec_hi, -1\n\t"
            "s_mov_b32 exec_hi, 0x3fffff\n\t"
            "s_add_u32 m0, s66, 3488\n\t"
            "s_add_u32 s49, s68, 784896\n\t"
            "buffer_load_dwordx4 v249, s[72:75], s49 offen lds\n\t"
            "s_mov_b32 exec_hi, -1\n\t"
            "s_mov_b32 exec_hi, 0x3fffff\n\t"
            "s_add_u32 m0, s66, 4360\n\t"
            "s_add_u32 s49, s68, 981120\n\t"
            "buffer_load_dwordx4 v249, s[72:75], s49 offen lds\n\t"
            "s_mov_b32 exec_hi, -1\n\t"
            "s_mov_b32 exec_hi, 0x3fffff\n\t"
            "s_add_u32 m0, s66, 5232\n\t"
            "s_add_u32 s49, s68, 1177344\n\t"
            "buffer_load_dwordx4 v249, s[72:75], s49 offen lds\n\t"
            "s_mov_b32 exec_hi, -1\n\t"
            "v_cvt_pk_bf16_f32 v184, v184, v185\n\t"
            "v_cvt_pk_bf16_f32 v185, v186, v187\n\t"
            "v_cvt_pk_bf16_f32 v186, v188, v189\n\t"
            "v_cvt_pk_bf16_f32 v187, v190, v191\n\t"
            "v_mfma_f32_4x4x4_16b_bf16 v[196:199], v[184:185], v[184:185], v[196:199]\n\t"
            "s_mov_b32 exec_hi, 0x3fffff\n\t"
            "s_add_u32 m0, s66, 6104\n\t"
            "s_add_u32 s49, s68, 1373568\n\t"
            "buffer_load_dwordx4 v249, s[72:75], s49 offen lds\n\t"
            "s_mov_b32 exec_hi, -1\n\t"
            "v_mfma_f32_4x4x4_16b_bf16 v[196:199], v[186:187], v[186:187], v[196:199]\n\t"
            "s_mov_b32 exec_hi, 0x3fffff\n\t"
            "s_add_u32 m0, s66, 6976\n\t"
            "s_add_u32 s49, s68, 1569792\n\t"
            "buffer_load_dwordx4 v249, s[72:75], s49 offen lds\n\t"
            "s_mov_b32 exec_hi, -1\n\t"
            "v_mfma_f32_16x16x32_bf16 v[192:195], v[238:241], v[184:187], v[192:195]\n\t"
            "s_waitcnt vmcnt(9)\n\t"
            "ds_write_b128 v237, v[184:187] offset:256\n\t"
            "v_mfma_f32_16x16x32_bf16 v[144:147], v[0:3], v[200:203], 0\n\t"
            "v_add_u32 v237, s48, v245\n\t"
            "v_mfma_f32_16x16x32_bf16 v[144:147], v[4:7], v[204:207], v[144:147]\n\t"
            "v_mfma_f32_16x16x32_bf16 v[148:151], v[24:27], v[200:203], 0\n\t"
            "s_nop 1\n\t"
            "v_cndmask_b32_e64 v193, v196, v197, s[60:61]\n\t"
            "v_mfma_f32_16x16x32_bf16 v[148:151], v[28:31], v[204:207], v[148:151]\n\t"
            "v_cndmask_b32_e64 v193, v193, v198, s[62:63]\n\t"
            "v_cndmask_b32_e64 v193, v193, v199, s[64:65]\n\t"
            "v_mfma_f32_16x16x32_bf16 v[168:171], v[48:51], v[200:203], 0\n\t"
            "ds_read_b128 v[196:199], v232 offset:6272\n\t"
            "v_mfma_f32_16x16x32_bf16 v[168:171], v[52:55], v[204:207], v[168:171]\n\t"
            "ds_write_b64 v248, v[192:193] offset:18816\n\t"
            "v_mfma_f32_16x16x32_bf16 v[172:175], v[72:75], v[200:203], 0\n\t"
            "ds_read_b128 v[192:195], v235 offset:6144\n\t"
            "v_add_u32 v248, s48, v246\n\t"
            "v_mfma_f32_16x16x32_bf16 v[172:175], v[76:79], v[204:207], v[172:175]\n\t"
            "s_mov_b64 exec, 1\n\t"
            "ds_inc_u32 v255, v238 offset:7860\n\t"
            "s_mov_b64 exec, -1\n\t"
            "v_mfma_f32_16x16x32_bf16 v[152:155], v[96:99], v[200:203], 0\n\t"
            "v_mfma_f32_16x16x32_bf16 v[152:155], v[100:103], v[204:207], v[152:155]\n\t"
            "v_mfma_f32_16x16x32_bf16 v[156:159], v[120:123], v[200:203], 0\n\t"
            "ds_read_b128 v[200:203], v235 offset:6272\n\t"
            "v_mfma_f32_16x16x32_bf16 v[156:159], v[124:127], v[204:207], v[156:159]\n\t"
            "ds_read_b128 v[204:207], v232 offset:6400\n\t"
            "v_mfma_f32_16x16x32_bf16 v[144:147], v[8:11], v[208:211], v[144:147]\n\t"
            "v_mfma_f32_16x16x32_bf16 v[144:147], v[12:15], v[212:215], v[144:147]\n\t"
            "v_mfma_f32_16x16x32_bf16 v[148:151], v[32:35], v[208:211], v[148:151]\n\t"
            "v_mfma_f32_16x16x32_bf16 v[148:151], v[36:39], v[212:215], v[148:151]\n\t"
            "v_mfma_f32_16x16x32_bf16 v[168:171], v[56:59], v[208:211], v[168:171]\n\t"
            "v_mfma_f32_16x16x32_bf16 v[168:171], v[60:63], v[212:215], v[168:171]\n\t"
            "v_mfma_f32_16x16x32_bf16 v[172:175], v[80:83], v[208:211], v[172:175]\n\t"
            "v_mfma_f32_16x16x32_bf16 v[172:175], v[84:87], v[212:215], v[172:175]\n\t"
            "v_mfma_f32_16x16x32_bf16 v[152:155], v[104:107], v[208:211], v[152:155]\n\t"
            "v_mfma_f32_16x16x32_bf16 v[152:155], v[108:111], v[212:215], v[152:155]\n\t"
            "v_mfma_f32_16x16x32_bf16 v[156:159], v[128:131], v[208:211], v[156:159]\n\t"
            "ds_read_b128 v[208:211], v235 offset:6400\n\t"
            "v_mfma_f32_16x16x32_bf16 v[156:159], v[132:135], v[212:215], v[156:159]\n\t"
            "ds_read_b128 v[212:215], v236 offset:6144\n\t"
            "v_mfma_f32_16x16x32_bf16 v[144:147], v[16:19], v[216:219], v[144:147]\n\t"
            "v_mfma_f32_16x16x32_bf16 v[144:147], v[20:23], v[220:223], v[144:147]\n\t"
            "v_mfma_f32_16x16x32_bf16 v[148:151], v[40:43], v[216:219], v[148:151]\n\t"
            "v_mfma_f32_16x16x32_bf16 v[148:151], v[44:47], v[220:223], v[148:151]\n\t"
            "v_mfma_f32_16x16x32_bf16 v[168:171], v[64:67], v[216:219], v[168:171]\n\t"
            "v_mfma_f32_16x16x32_bf16 v[168:171], v[68:71], v[220:223], v[168:171]\n\t"
            "v_mfma_f32_16x16x32_bf16 v[172:175], v[88:91], v[216:219], v[172:175]\n\t"
            "v_mfma_f32_16x16x32_bf16 v[172:175], v[92:95], v[220:223], v[172:175]\n\t"
            "s_nop 1\n\t"
            "v_mul_f32 v144, v230, v144\n\t"
            "v_mfma_f32_16x16x32_bf16 v[152:155], v[112:115], v[216:219], v[152:155]\n\t"
            "v_mul_f32 v145, v230, v145\n\t"
            "v_mul_f32 v146, v230, v146\n\t"
            "v_mfma_f32_16x16x32_bf16 v[152:155], v[116:119], v[220:223], v[152:155]\n\t"
            "v_mul_f32 v148, v230, v148\n\t"
            "v_mul_f32 v149, v230, v149\n\t"
            "v_mfma_f32_16x16x32_bf16 v[156:159], v[136:139], v[216:219], v[156:159]\n\t"
            "ds_read_b128 v[216:219], v237 offset:6144\n\t"
            "v_mul_f32 v168, v230, v168\n\t"
            "v_mfma_f32_16x16x32_bf16 v[156:159], v[140:143], v[220:223], v[156:159]\n\t"
            "ds_read_b128 v[220:223], v248 offset:6144\n\t"
            "v_mul_f32 v169, v230, v169\n\t"
            "s_waitcnt lgkmcnt(7)\n\t"
            "v_mfma_f32_16x16x32_bf16 v[160:163], v[0:3], v[224:227], 0\n\t"
            "v_mul_f32 v172, v230, v172\n\t"
            "v_mul_f32 v173, v230, v173\n\t"
            "v_mfma_f32_16x16x32_bf16 v[160:163], v[4:7], v[192:195], v[160:163]\n\t"
            "v_mul_f32 v152, v230, v152\n\t"
            "v_mul_f32 v153, v230, v153\n\t"
            "v_mfma_f32_16x16x32_bf16 v[164:167], v[24:27], v[224:227], 0\n\t"
            "v_mul_f32 v156, v230, v156\n\t"
            "v_mul_f32 v157, v230, v157\n\t"
            "v_mfma_f32_16x16x32_bf16 v[164:167], v[28:31], v[192:195], v[164:167]\n\t"
            "v_mul_f32 v147, v230, v147\n\t"
            "v_cvt_pk_bf16_f32 v144, v144, v145\n\t"
            "v_mfma_f32_16x16x32_bf16 v[176:179], v[48:51], v[224:227], 0\n\t"
            "v_mul_f32 v150, v230, v150\n\t"
            "v_mul_f32 v151, v230, v151\n\t"
            "v_mfma_f32_16x16x32_bf16 v[176:179], v[52:55], v[192:195], v[176:179]\n\t"
            "v_cvt_pk_bf16_f32 v148, v148, v149\n\t"
            "v_mul_f32 v170, v230, v170\n\t"
            "v_mfma_f32_16x16x32_bf16 v[180:183], v[72:75], v[224:227], 0\n\t"
            "v_mul_f32 v171, v230, v171\n\t"
            "v_cvt_pk_bf16_f32 v168, v168, v169\n\t"
            "v_mfma_f32_16x16x32_bf16 v[180:183], v[76:79], v[192:195], v[180:183]\n\t"
            "v_mul_f32 v174, v230, v174\n\t"
            "v_mul_f32 v175, v230, v175\n\t"
            "v_mfma_f32_16x16x32_bf16 v[184:187], v[96:99], v[224:227], 0\n\t"
            "v_cvt_pk_bf16_f32 v172, v172, v173\n\t"
            "v_mul_f32 v154, v230, v154\n\t"
            "v_mfma_f32_16x16x32_bf16 v[184:187], v[100:103], v[192:195], v[184:187]\n\t"
            "v_mul_f32 v155, v230, v155\n\t"
            "v_cvt_pk_bf16_f32 v152, v152, v153\n\t"
            "v_mfma_f32_16x16x32_bf16 v[188:191], v[120:123], v[224:227], 0\n\t"
            "ds_read_b128 v[224:227], v236 offset:12288\n\t"
            "v_mul_f32 v158, v230, v158\n\t"
            "v_mfma_f32_16x16x32_bf16 v[188:191], v[124:127], v[192:195], v[188:191]\n\t"
            "v_mul_f32 v159, v230, v159\n\t"
            "v_cvt_pk_bf16_f32 v156, v156, v157\n\t"
            "s_waitcnt lgkmcnt(6)\n\t"
            "v_mfma_f32_16x16x32_bf16 v[160:163], v[8:11], v[196:199], v[160:163]\n\t"
            "v_cvt_pk_bf16_f32 v145, v146, v147\n\t"
            "v_cvt_pk_bf16_f32 v149, v150, v151\n\t"
            "v_mfma_f32_16x16x32_bf16 v[160:163], v[12:15], v[200:203], v[160:163]\n\t"
            "v_cvt_pk_bf16_f32 v169, v170, v171\n\t"
            "v_cvt_pk_bf16_f32 v173, v174, v175\n\t"
            "v_mfma_f32_16x16x32_bf16 v[164:167], v[32:35], v[196:199], v[164:167]\n\t"
            "v_cvt_pk_bf16_f32 v153, v154, v155\n\t"
            "v_cvt_pk_bf16_f32 v157, v158, v159\n\t"
            "v_mfma_f32_16x16x32_bf16 v[164:167], v[36:39], v[200:203], v[164:167]\n\t"
            "s_mov_b64 exec, -1\n\t"
            "ds_write_b64 v234, v[144:145]\n\t"
            "ds_write_b64 v234, v[148:149] offset:128\n\t"
            "ds_write_b64 v234, v[168:169] offset:256\n\t"
            "ds_write_b64 v228, v[172:173]\n\t"
            "ds_write_b64 v228, v[152:153] offset:128\n\t"
            "ds_write_b64 v228, v[156:157] offset:256\n\t"
            "s_mov_b64 exec, -1\n\t"
            "v_mfma_f32_16x16x32_bf16 v[176:179], v[56:59], v[196:199], v[176:179]\n\t"
            "v_mfma_f32_16x16x32_bf16 v[176:179], v[60:63], v[200:203], v[176:179]\n\t"
            "s_add_u32 s49, s33, 0\n\t"
            "buffer_load_dwordx4 v[0:3], v247, s[28:31], s49 offen sc1\n\t"
            "v_mfma_f32_16x16x32_bf16 v[180:183], v[80:83], v[196:199], v[180:183]\n\t"
            "v_mfma_f32_16x16x32_bf16 v[180:183], v[84:87], v[200:203], v[180:183]\n\t"
            "s_add_u32 s49, s33, 6144\n\t"
            "buffer_load_dwordx4 v[24:27], v247, s[28:31], s49 offen sc1\n\t"
            "v_mfma_f32_16x16x32_bf16 v[184:187], v[104:107], v[196:199], v[184:187]\n\t"
            "v_mfma_f32_16x16x32_bf16 v[184:187], v[108:111], v[200:203], v[184:187]\n\t"
            "s_add_u32 s49, s33, 12288\n\t"
            "buffer_load_dwordx4 v[48:51], v247, s[28:31], s49 offen sc1\n\t"
            "v_mfma_f32_16x16x32_bf16 v[188:191], v[128:131], v[196:199], v[188:191]\n\t"
            "v_mfma_f32_16x16x32_bf16 v[188:191], v[132:135], v[200:203], v[188:191]\n\t"
            "ds_read_b128 v[200:203], v236\n\t"
            "s_add_u32 s49, s33, 18432\n\t"
            "buffer_load_dwordx4 v[72:75], v247, s[28:31], s49 offen sc1\n\t"
            "s_waitcnt lgkmcnt(11)\n\t"
            "v_mfma_f32_16x16x32_bf16 v[160:163], v[16:19], v[204:207], v[160:163]\n\t"
            "v_mfma_f32_16x16x32_bf16 v[160:163], v[20:23], v[208:211], v[160:163]\n\t"
            "s_add_u32 s49, s33, 24576\n\t"
            "buffer_load_dwordx4 v[96:99], v247, s[28:31], s49 offen sc1\n\t"
            "v_mfma_f32_16x16x32_bf16 v[164:167], v[40:43], v[204:207], v[164:167]\n\t"
            "v_mfma_f32_16x16x32_bf16 v[164:167], v[44:47], v[208:211], v[164:167]\n\t"
            "s_add_u32 s49, s33, 30720\n\t"
            "buffer_load_dwordx4 v[120:123], v247, s[28:31], s49 offen sc1\n\t"
            "v_mfma_f32_16x16x32_bf16 v[176:179], v[64:67], v[204:207], v[176:179]\n\t"
            "s_nop 0\n\t"
            "v_mul_f32 v160, v231, v160\n\t"
            "v_mul_f32 v161, v231, v161\n\t"
            "v_mfma_f32_16x16x32_bf16 v[176:179], v[68:71], v[208:211], v[176:179]\n\t"
            "v_mul_f32 v162, v231, v162\n\t"
            "v_mul_f32 v163, v231, v163\n\t"
            "v_mfma_f32_16x16x32_bf16 v[180:183], v[88:91], v[204:207], v[180:183]\n\t"
            "v_mul_f32 v164, v231, v164\n\t"
            "v_mul_f32 v165, v231, v165\n\t"
            "v_mfma_f32_16x16x32_bf16 v[180:183], v[92:95], v[208:211], v[180:183]\n\t"
            "v_cvt_pk_bf16_f32 v160, v160, v161\n\t"
            "v_mul_f32 v166, v231, v166\n\t"
            "v_mfma_f32_16x16x32_bf16 v[184:187], v[112:115], v[204:207], v[184:187]\n\t"
            "v_mul_f32 v176, v231, v176\n\t"
            "v_mul_f32 v177, v231, v177\n\t"
            "v_mfma_f32_16x16x32_bf16 v[184:187], v[116:119], v[208:211], v[184:187]\n\t"
            "v_mul_f32 v167, v231, v167\n\t"
            "v_cvt_pk_bf16_f32 v164, v164, v165\n\t"
            "v_mfma_f32_16x16x32_bf16 v[188:191], v[136:139], v[204:207], v[188:191]\n\t"
            "ds_read_b128 v[204:207], v237\n\t"
            "v_mul_f32 v180, v231, v180\n\t"
            "v_mfma_f32_16x16x32_bf16 v[188:191], v[140:143], v[208:211], v[188:191]\n\t"
            "ds_read_b128 v[208:211], v248\n\t"
            "v_mul_f32 v181, v231, v181\n\t"
            "v_mul_f32 v184, v231, v184\n\t"
            "v_mul_f32 v185, v231, v185\n\t"
            "v_mul_f32 v178, v231, v178\n\t"
            "v_mul_f32 v179, v231, v179\n\t"
            "v_cvt_pk_bf16_f32 v176, v176, v177\n\t"
            "v_mul_f32 v182, v231, v182\n\t"
            "v_mul_f32 v188, v231, v188\n\t"
            "v_mul_f32 v189, v231, v189\n\t"
            "v_mul_f32 v183, v231, v183\n\t"
            "v_cvt_pk_bf16_f32 v180, v180, v181\n\t"
            "v_mul_f32 v186, v231, v186\n\t"
            "v_mul_f32 v187, v231, v187\n\t"
            "v_cvt_pk_bf16_f32 v184, v184, v185\n\t"
            "v_mul_f32 v190, v231, v190\n\t"
            "v_mul_f32 v191, v231, v191\n\t"
            "v_cvt_pk_bf16_f32 v188, v188, v189\n\t"
            "v_cvt_pk_bf16_f32 v161, v162, v163\n\t"
            "v_cvt_pk_bf16_f32 v165, v166, v167\n\t"
            "v_cvt_pk_bf16_f32 v177, v178, v179\n\t"
            "v_cvt_pk_bf16_f32 v181, v182, v183\n\t"
            "v_cvt_pk_bf16_f32 v185, v186, v187\n\t"
            "v_cvt_pk_bf16_f32 v189, v190, v191\n\t"
            "s_waitcnt lgkmcnt(9)\n\t"
            "s_mov_b64 exec, s[80:81]\n\t"
            "ds_write_b64 v234, v[160:161] offset:6144\n\t"
            "ds_write_b64 v234, v[164:165] offset:6272\n\t"
            "ds_write_b64 v234, v[176:177] offset:6400\n\t"
            "ds_write_b64 v228, v[180:181] offset:6144\n\t"
            "ds_write_b64 v228, v[184:185] offset:6272\n\t"
            "ds_write_b64 v228, v[188:189] offset:6400\n\t"
            "s_mov_b64 exec, -1\n\t"
            "s_waitcnt lgkmcnt(14)\n\t"
            "ds_read_b128 v[228:231], v237 offset:12288\n\t"
            "s_waitcnt lgkmcnt(14)\n\t"
            "ds_read_b128 v[232:235], v248 offset:12288\n\t"
            "s_add_u32 s49, s33, 1024\n\t"
            "buffer_load_dwordx4 v[4:7], v247, s[28:31], s49 offen sc1\n\t"
            "s_add_u32 s49, s33, 7168\n\t"
            "buffer_load_dwordx4 v[28:31], v247, s[28:31], s49 offen sc1\n\t"
            "s_waitcnt lgkmcnt(0)\n\t"
            "s_barrier\n\t"
            "s_setprio 3\n\t"
            "s_add_u32 s49, s33, 13312\n\t"
            "buffer_load_dwordx4 v[52:55], v247, s[28:31], s49 offen sc1\n\t"
            "s_add_u32 s49, s33, 19456\n\t"
            "buffer_load_dwordx4 v[76:79], v247, s[28:31], s49 offen sc1\n\t"
            "s_add_u32 s49, s33, 25600\n\t"
            "buffer_load_dwordx4 v[100:103], v247, s[28:31], s49 offen sc1\n\t"
            "s_add_u32 s49, s33, 31744\n\t"
            "buffer_load_dwordx4 v[124:127], v247, s[28:31], s49 offen sc1\n\t"
            "s_add_u32 s49, s33, 2048\n\t"
            "buffer_load_dwordx4 v[8:11], v247, s[28:31], s49 offen sc1\n\t"
            "s_add_u32 s49, s33, 8192\n\t"
            "buffer_load_dwordx4 v[32:35], v247, s[28:31], s49 offen sc1\n\t"
            "s_add_u32 s49, s33, 14336\n\t"
            "buffer_load_dwordx4 v[56:59], v247, s[28:31], s49 offen sc1\n\t"
            "s_add_u32 s49, s33, 20480\n\t"
            "buffer_load_dwordx4 v[80:83], v247, s[28:31], s49 offen sc1\n\t"
            "s_add_u32 s49, s33, 26624\n\t"
            "buffer_load_dwordx4 v[104:107], v247, s[28:31], s49 offen sc1\n\t"
            "s_add_u32 s49, s33, 32768\n\t"
            "buffer_load_dwordx4 v[128:131], v247, s[28:31], s49 offen sc1\n\t"
            "s_add_u32 s49, s33, 3072\n\t"
            "buffer_load_dwordx4 v[12:15], v247, s[28:31], s49 offen sc1\n\t"
            "s_add_u32 s49, s33, 9216\n\t"
            "buffer_load_dwordx4 v[36:39], v247, s[28:31], s49 offen sc1\n\t"
            "s_add_u32 s49, s33, 15360\n\t"
            "buffer_load_dwordx4 v[60:63], v247, s[28:31], s49 offen sc1\n\t"
            "s_add_u32 s49, s33, 21504\n\t"
            "buffer_load_dwordx4 v[84:87], v247, s[28:31], s49 offen sc1\n\t"
            "s_add_u32 s49, s33, 27648\n\t"
            "buffer_load_dwordx4 v[108:111], v247, s[28:31], s49 offen sc1\n\t"
            "s_add_u32 s49, s33, 33792\n\t"
            "buffer_load_dwordx4 v[132:135], v247, s[28:31], s49 offen sc1\n\t"
            "s_add_u32 s49, s33, 4096\n\t"
            "buffer_load_dwordx4 v[16:19], v247, s[28:31], s49 offen sc1\n\t"
            "s_add_u32 s49, s33, 10240\n\t"
            "buffer_load_dwordx4 v[40:43], v247, s[28:31], s49 offen sc1\n\t"
            "s_add_u32 s49, s33, 16384\n\t"
            "buffer_load_dwordx4 v[64:67], v247, s[28:31], s49 offen sc1\n\t"
            "s_add_u32 s49, s33, 22528\n\t"
            "buffer_load_dwordx4 v[88:91], v247, s[28:31], s49 offen sc1\n\t"
            "s_add_u32 s49, s33, 28672\n\t"
            "buffer_load_dwordx4 v[112:115], v247, s[28:31], s49 offen sc1\n\t"
            "s_add_u32 s49, s33, 34816\n\t"
            "buffer_load_dwordx4 v[136:139], v247, s[28:31], s49 offen sc1\n\t"
            "s_add_u32 s49, s33, 5120\n\t"
            "buffer_load_dwordx4 v[20:23], v247, s[28:31], s49 offen sc1\n\t"
            "s_add_u32 s49, s33, 11264\n\t"
            "buffer_load_dwordx4 v[44:47], v247, s[28:31], s49 offen sc1\n\t"
            "s_add_u32 s49, s33, 17408\n\t"
            "buffer_load_dwordx4 v[68:71], v247, s[28:31], s49 offen sc1\n\t"
            "s_add_u32 s49, s33, 23552\n\t"
            "buffer_load_dwordx4 v[92:95], v247, s[28:31], s49 offen sc1\n\t"
            "s_add_u32 s49, s33, 29696\n\t"
            "buffer_load_dwordx4 v[116:119], v247, s[28:31], s49 offen sc1\n\t"
            "s_add_u32 s49, s33, 35840\n\t"
            "buffer_load_dwordx4 v[140:143], v247, s[28:31], s49 offen sc1\n\t"
            "s_add_u32 s59, s59, s84\n\t"
            "s_movk_i32 s84, 800\n\t"
            "s_add_u32 s45, s46, s27\n\t"
            "s_mov_b32 s46, s47\n\t"
            "s_mov_b32 s47, s68\n\t"
            "s_xor_b32 s48, s48, s71\n\t"
            "s_xor_b32 s67, s67, s71\n\t"
            "s_add_u32 s76, s76, 4\n\t"
            "s_add_u32 s70, s70, 4\n\t"
            "s_lshl_b32 s56, s56, 2\n\t"
            "s_lshr_b32 s49, s26, 30\n\t"
            "s_or_b32 s56, s56, s49\n\t"
            "s_cmp_eq_u32 s82, 0\n\t"
            "s_cbranch_scc1 .Lwk_nr%=\n\t"
            "s_waitcnt lgkmcnt(0)\n\t"
            "s_nop 7\n\t"
            "v_add_u32 v236, 127360, v247\n\t"
            "v_add_u32 v237, 136704, v247\n\t"
            "ds_read_b128 v[144:147], v236\n\t"
            "ds_read_b128 v[148:151], v237\n\t"
            "s_waitcnt lgkmcnt(0)\n\t"
            "s_mov_b32 exec_hi, 0x3ffff\n\t"
            "buffer_store_dwordx4 v[144:147], v247, s[28:31], s82 offen sc1\n\t"
            "s_mov_b64 exec, 0xffffff\n\t"
            "buffer_store_dwordx4 v[148:151], v247, s[28:31], s82 offen offset:800 sc1\n\t"
            "s_mov_b64 exec, -1\n\t"
            "ds_read_b128 v[152:155], v252 offset:2560\n\t"
            "s_waitcnt vmcnt(0) lgkmcnt(0)\n\t"
            "s_mov_b64 exec, 1\n\t"
            "buffer_store_dwordx4 v[152:155], off, s[28:31], s82 offset:1216 sc1\n\t"
            "s_mov_b64 exec, -1\n\t"
            ".Lwk_nr%=:\n\t"
            "s_waitcnt vmcnt(0)\n\t"
            "s_nop 7\n\t"
            ".p2align 6\n\t"
            "s_nop 0\n\t"
            "s_nop 0\n\t"
            "s_nop 0\n\t"
            "s_nop 0\n\t"
            "s_nop 0\n\t"
            "s_nop 0\n\t"
            "s_nop 0\n\t"
            ".Lwb_loop%=:\n\t"
            "s_setprio 1\n\t"
            "ds_read_b128 v[168:171], v254\n\t"
            "ds_read_b128 v[172:175], v254 offset:64\n\t"
            "s_waitcnt vmcnt(9)\n\t"
            "ds_read_b64 v[144:145], v253\n\t"
            "ds_read_b64 v[146:147], v253 offset:96\n\t"
            "ds_read_b64 v[148:149], v253 offset:1744\n\t"
            "ds_read_b64 v[150:151], v253 offset:1840\n\t"
            "ds_read_b64 v[152:153], v253 offset:8\n\t"
            "ds_read_b64 v[154:155], v253 offset:104\n\t"
            "ds_read_b64 v[156:157], v253 offset:1752\n\t"
            "ds_read_b64 v[158:159], v253 offset:1848\n\t"
            "ds_read_b64 v[160:161], v253 offset:16\n\t"
            "ds_read_b64 v[162:163], v253 offset:112\n\t"
            "ds_read_b64 v[164:165], v253 offset:1760\n\t"
            "ds_read_b64 v[166:167], v253 offset:1856\n\t"
            "v_mov_b64 v[176:177], 0\n\t"
            "v_mov_b64 v[178:179], 0\n\t"
            "v_mov_b64 v[180:181], 0\n\t"
            "v_mov_b64 v[182:183], 0\n\t"
            "v_mov_b32 v249, s76\n\t"
            "v_mov_b64 v[184:185], 0\n\t"
            "v_mov_b64 v[186:187], 0\n\t"
            "v_mov_b64 v[188:189], 0\n\t"
            "v_mov_b64 v[190:191], 0\n\t"
            "v_add_u32 v236, s67, v250\n\t"
            "v_add_u32 v248, s67, v251\n\t"
            "v_mov_b32 v247, 0\n\t"
            "ds_read_b32 v249, v249\n\t"
            "s_setprio 3\n\t"
            "s_waitcnt lgkmcnt(9)\n\t"
            "v_smfmac_f32_16x16x64_bf16 v[176:179], v[168:171], v[144:151], v255\n\t"
            "ds_read_b128 v[168:171], v254 offset:128\n\t"
            "s_waitcnt lgkmcnt(6)\n\t"
            "v_smfmac_f32_16x16x64_bf16 v[180:183], v[172:175], v[152:159], v255\n\t"
            "s_setprio 1\n\t"
            "ds_read_b64 v[144:145], v253 offset:24\n\t"
            "ds_read_b64 v[146:147], v253 offset:120\n\t"
            "ds_read_b64 v[148:149], v253 offset:1768\n\t"
            "ds_read_b64 v[150:151], v253 offset:1864\n\t"
            "ds_read_b128 v[172:175], v254 offset:192\n\t"
            "ds_read_b64 v[152:153], v253 offset:32\n\t"
            "ds_read_b64 v[154:155], v253 offset:128\n\t"
            "ds_read_b64 v[156:157], v253 offset:1776\n\t"
            "ds_read_b64 v[158:159], v253 offset:1872\n\t"
            "v_cvt_pk_bf16_f32 v176, v176, v177\n\t"
            "v_cvt_pk_bf16_f32 v177, v178, v179\n\t"
            "v_cvt_pk_bf16_f32 v178, v180, v181\n\t"
            "v_cvt_pk_bf16_f32 v179, v182, v183\n\t"
            "s_setprio 3\n\t"
            "v_mfma_f32_4x4x4_16b_bf16 v[196:199], v[176:177], v[176:177], 0\n\t"
            "v_mov_b64 v[180:181], 0\n\t"
            "v_mfma_f32_16x16x32_bf16 v[192:195], v[238:241], v[176:179], 0\n\t"
            "s_waitcnt lgkmcnt(10)\n\t"
            "v_readfirstlane_b32 s26, v249\n\t"
            "v_mov_b64 v[182:183], 0\n\t"
            "v_mfma_f32_4x4x4_16b_bf16 v[196:199], v[178:179], v[178:179], v[196:199]\n\t"
            "ds_read_b32 v249, v252 offset:12\n\t"
            "s_and_b32 s68, s26, 0x7ffffff\n\t"
            "s_add_u32 s68, s68, s78\n\t"
            "s_and_b32 s69, s26, 0x38000000\n\t"
            "s_or_b32 s69, s69, s39\n\t"
            "s_bitcmp1_b32 s26, 27\n\t"
            "s_cselect_b32 s79, 2.0, s68\n\t"
            "s_and_b32 s57, s56, 3\n\t"
            "s_lshl_b32 s58, s57, 3\n\t"
            "s_lshr_b32 m0, s54, s58\n\t"
            "s_lshl_b32 s58, s57, 4\n\t"
            "s_lshr_b64 s[36:37], s[50:51], s58\n\t"
            "s_and_b32 s57, s36, 0xffff\n\t"
            "s_add_u32 s57, s57, s67\n\t"
            "v_writelane_b32 v236, s57, 63\n\t"
            "v_writelane_b32 v236, s59, m0\n\t"
            "v_xor_b32 v237, 16, v236\n\t"
            "s_lshr_b64 s[36:37], s[52:53], s58\n\t"
            "s_and_b32 s58, s36, 0xffff\n\t"
            "s_add_u32 s58, s58, s67\n\t"
            "ds_write_b128 v236, v[176:179]\n\t"
            "s_waitcnt lgkmcnt(11)\n\t"
            "v_smfmac_f32_16x16x64_bf16 v[184:187], v[168:171], v[160:167], v255\n\t"
            "s_and_b32 s38, s52, 0xffff\n\t"
            "s_add_u32 s38, s38, s67\n\t"
            "ds_read_b128 v[168:171], v254 offset:256\n\t"
            "s_waitcnt lgkmcnt(7)\n\t"
            "v_smfmac_f32_16x16x64_bf16 v[188:191], v[172:175], v[144:151], v255\n\t"
            "s_setprio 1\n\t"
            "ds_read_b64 v[160:161], v253 offset:40\n\t"
            "ds_read_b64 v[162:163], v253 offset:136\n\t"
            "ds_read_b64 v[164:165], v253 offset:1784\n\t"
            "ds_read_b64 v[166:167], v253 offset:1880\n\t"
            "ds_read_b128 v[172:175], v254 offset:320\n\t"
            "ds_read_b64 v[144:145], v253 offset:48\n\t"
            "ds_read_b64 v[146:147], v253 offset:144\n\t"
            "ds_read_b64 v[148:149], v253 offset:1792\n\t"
            "s_waitcnt lgkmcnt(14)\n\t"
            "ds_read_b64 v[150:151], v253 offset:1888\n\t"
            "v_mov_b64 v[176:177], 0\n\t"
            "v_mov_b64 v[178:179], 0\n\t"
            "v_cvt_pk_bf16_f32 v184, v184, v185\n\t"
            "v_cvt_pk_bf16_f32 v185, v186, v187\n\t"
            "v_cvt_pk_bf16_f32 v186, v188, v189\n\t"
            "v_cvt_pk_bf16_f32 v187, v190, v191\n\t"
            "s_setprio 3\n\t"
            "v_mfma_f32_4x4x4_16b_bf16 v[196:199], v[184:185], v[184:185], v[196:199]\n\t"
            "s_waitcnt lgkmcnt(14)\n\t"
            "ds_write_b128 v237, v[184:187]\n\t"
            "v_mfma_f32_16x16x32_bf16 v[192:195], v[238:241], v[184:187], v[192:195]\n\t"
            "v_mov_b64 v[184:185], 0\n\t"
            "v_mov_b64 v[188:189], 0\n\t"
            "v_mfma_f32_4x4x4_16b_bf16 v[196:199], v[186:187], v[186:187], v[196:199]\n\t"
            "v_mov_b64 v[186:187], 0\n\t"
            "v_mov_b64 v[190:191], 0\n\t"
            "v_writelane_b32 v248, s58, 63\n\t"
            "v_writelane_b32 v248, s38, m0\n\t"
            "s_waitcnt lgkmcnt(12)\n\t"
            "v_and_b32 v249, s69, v249\n\t"
            "s_waitcnt lgkmcnt(10)\n\t"
            "v_smfmac_f32_16x16x64_bf16 v[176:179], v[168:171], v[152:159], v255\n\t"
            "ds_read_b128 v[168:171], v254 offset:384\n\t"
            "ds_read_b64 v[152:153], v253 offset:56\n\t"
            "ds_read_b64 v[154:155], v253 offset:152\n\t"
            "ds_read_b64 v[156:157], v253 offset:1800\n\t"
            "ds_read_b64 v[158:159], v253 offset:1896\n\t"
            "s_waitcnt lgkmcnt(10)\n\t"
            "v_smfmac_f32_16x16x64_bf16 v[180:183], v[172:175], v[160:167], v255\n\t"
            "s_setprio 1\n\t"
            "ds_read_b128 v[172:175], v254 offset:448\n\t"
            "ds_read_b64 v[160:161], v253 offset:64\n\t"
            "ds_read_b64 v[162:163], v253 offset:160\n\t"
            "ds_read_b64 v[164:165], v253 offset:1808\n\t"
            "ds_read_b64 v[166:167], v253 offset:1904\n\t"
            "v_cvt_pk_bf16_f32 v176, v176, v177\n\t"
            "v_cvt_pk_bf16_f32 v177, v178, v179\n\t"
            "v_cvt_pk_bf16_f32 v178, v180, v181\n\t"
            "v_cvt_pk_bf16_f32 v179, v182, v183\n\t"
            "s_setprio 3\n\t"
            "v_mfma_f32_4x4x4_16b_bf16 v[196:199], v[176:177], v[176:177], v[196:199]\n\t"
            "s_waitcnt lgkmcnt(14)\n\t"
            "ds_write_b128 v236, v[176:179] offset:128\n\t"
            "v_mfma_f32_16x16x32_bf16 v[192:195], v[238:241], v[176:179], v[192:195]\n\t"
            "v_mov_b64 v[176:177], 0\n\t"
            "v_mov_b64 v[180:181], 0\n\t"
            "v_mfma_f32_4x4x4_16b_bf16 v[196:199], v[178:179], v[178:179], v[196:199]\n\t"
            "v_mov_b64 v[178:179], 0\n\t"
            "v_mov_b64 v[182:183], 0\n\t"
            "s_waitcnt lgkmcnt(10)\n\t"
            "v_smfmac_f32_16x16x64_bf16 v[184:187], v[168:171], v[144:151], v255\n\t"
            "ds_read_b128 v[168:171], v254 offset:512\n\t"
            "ds_read_b64 v[144:145], v253 offset:72\n\t"
            "ds_read_b64 v[146:147], v253 offset:168\n\t"
            "ds_read_b64 v[148:149], v253 offset:1816\n\t"
            "ds_read_b64 v[150:151], v253 offset:1912\n\t"
            "s_waitcnt lgkmcnt(10)\n\t"
            "v_smfmac_f32_16x16x64_bf16 v[188:191], v[172:175], v[152:159], v255\n\t"
            "s_setprio 1\n\t"
            "ds_read_b128 v[172:175], v254 offset:576\n\t"
            "ds_read_b64 v[152:153], v253 offset:80\n\t"
            "ds_read_b64 v[154:155], v253 offset:176\n\t"
            "ds_read_b64 v[156:157], v253 offset:1824\n\t"
            "ds_read_b64 v[158:159], v253 offset:1920\n\t"
            "v_cvt_pk_bf16_f32 v184, v184, v185\n\t"
            "v_cvt_pk_bf16_f32 v185, v186, v187\n\t"
            "v_cvt_pk_bf16_f32 v186, v188, v189\n\t"
            "v_cvt_pk_bf16_f32 v187, v190, v191\n\t"
            "s_setprio 3\n\t"
            "v_mfma_f32_4x4x4_16b_bf16 v[196:199], v[184:185], v[184:185], v[196:199]\n\t"
            "s_waitcnt lgkmcnt(14)\n\t"
            "ds_write_b128 v237, v[184:187] offset:128\n\t"
            "v_mfma_f32_16x16x32_bf16 v[192:195], v[238:241], v[184:187], v[192:195]\n\t"
            "v_mov_b64 v[184:185], 0\n\t"
            "v_mov_b64 v[188:189], 0\n\t"
            "v_mfma_f32_4x4x4_16b_bf16 v[196:199], v[186:187], v[186:187], v[196:199]\n\t"
            "v_mov_b64 v[186:187], 0\n\t"
            "v_mov_b64 v[190:191], 0\n\t"
            "s_waitcnt lgkmcnt(10)\n\t"
            "v_smfmac_f32_16x16x64_bf16 v[176:179], v[168:171], v[160:167], v255\n\t"
            "ds_read_b128 v[168:171], v254 offset:640\n\t"
            "ds_read_b64 v[160:161], v253 offset:88\n\t"
            "ds_read_b64 v[162:163], v253 offset:184\n\t"
            "ds_read_b64 v[164:165], v253 offset:1832\n\t"
            "ds_read_b64 v[166:167], v253 offset:1928\n\t"
            "s_waitcnt lgkmcnt(10)\n\t"
            "v_smfmac_f32_16x16x64_bf16 v[180:183], v[172:175], v[144:151], v255\n\t"
            "s_setprio 1\n\t"
            "ds_read_b128 v[172:175], v254 offset:704\n\t"
            "v_lshlrev_b32 v144, 16, v212\n\t"
            "v_cvt_pk_bf16_f32 v176, v176, v177\n\t"
            "v_cvt_pk_bf16_f32 v177, v178, v179\n\t"
            "v_and_b32 v145, 0xffff0000, v212\n\t"
            "v_lshlrev_b32 v146, 16, v213\n\t"
            "s_setprio 3\n\t"
            "v_mfma_f32_4x4x4_16b_bf16 v[196:199], v[176:177], v[176:177], v[196:199]\n\t"
            "v_and_b32 v147, 0xffff0000, v213\n\t"
            "v_cvt_pk_bf16_f32 v178, v180, v181\n\t"
            "v_cvt_pk_bf16_f32 v179, v182, v183\n\t"
            "v_lshlrev_b32 v180, 16, v210\n\t"
            "v_and_b32 v181, 0xffff0000, v210\n\t"
            "v_mfma_f32_4x4x4_16b_bf16 v[196:199], v[178:179], v[178:179], v[196:199]\n\t"
            "ds_write_b128 v236, v[176:179] offset:256\n\t"
            "v_mfma_f32_16x16x32_bf16 v[192:195], v[238:241], v[176:179], v[192:195]\n\t"
            "s_setprio 1\n\t"
            "v_lshlrev_b32 v176, 16, v208\n\t"
            "v_and_b32 v177, 0xffff0000, v208\n\t"
            "v_lshlrev_b32 v178, 16, v209\n\t"
            "v_and_b32 v179, 0xffff0000, v209\n\t"
            "v_lshlrev_b32 v182, 16, v211\n\t"
            "v_and_b32 v183, 0xffff0000, v211\n\t"
            "v_lshlrev_b32 v148, 16, v214\n\t"
            "v_and_b32 v149, 0xffff0000, v214\n\t"
            "v_lshlrev_b32 v150, 16, v215\n\t"
            "v_and_b32 v151, 0xffff0000, v215\n\t"
            "v_add_u32 v236, s48, v244\n\t"
            "ds_read_b128 v[212:215], v236 offset:6144\n\t"
            "s_setprio 3\n\t"
            "s_waitcnt lgkmcnt(7)\n\t"
            "v_smfmac_f32_16x16x64_bf16 v[184:187], v[168:171], v[152:159], v255\n\t"
            "v_lshlrev_b32 v152, 16, v200\n\t"
            "v_and_b32 v153, 0xffff0000, v200\n\t"
            "v_lshlrev_b32 v154, 16, v201\n\t"
            "v_and_b32 v155, 0xffff0000, v201\n\t"
            "v_lshlrev_b32 v156, 16, v202\n\t"
            "s_waitcnt lgkmcnt(2)\n\t"
            "v_smfmac_f32_16x16x64_bf16 v[188:191], v[172:175], v[160:167], v255\n\t"
            "s_setprio 1\n\t"
            "s_mov_b32 exec_hi, 0x3fffff\n\t"
            "s_add_u32 m0, s66, 0\n\t"
            "s_add_u32 s49, s79, 0\n\t"
            "buffer_load_dwordx4 v249, s[88:91], s49 offen lds\n\t"
            "s_mov_b32 exec_hi, -1\n\t"
            "s_mov_b32 exec_hi, 0x3fffff\n\t"
            "s_add_u32 m0, s66, 872\n\t"
            "s_add_u32 s49, s68, 196224\n\t"
            "buffer_load_dwordx4 v249, s[72:75], s49 offen lds\n\t"
            "s_mov_b32 exec_hi, -1\n\t"
            "s_mov_b32 exec_hi, 0x3fffff\n\t"
            "s_add_u32 m0, s66, 1744\n\t"
            "s_add_u32 s49, s68, 392448\n\t"
            "buffer_load_dwordx4 v249, s[72:75], s49 offen lds\n\t"
            "s_mov_b32 exec_hi, -1\n\t"
            "s_mov_b32 exec_hi, 0x3fffff\n\t"
            "s_add_u32 m0, s66, 2616\n\t"
            "s_add_u32 s49, s68, 588672\n\t"
            "buffer_load_dwordx4 v249, s[72:75], s49 offen lds\n\t"
            "s_mov_b32 exec_hi, -1\n\t"
            "ds_read_b32 v247, v247 offset:7856\n\t"
            "s_mov_b32 exec_hi, 0x3fffff\n\t"
            "s_add_u32 m0, s66, 3488\n\t"
            "s_add_u32 s49, s68, 784896\n\t"
            "buffer_load_dwordx4 v249, s[72:75], s49 offen lds\n\t"
            "s_mov_b32 exec_hi, -1\n\t"
            "s_mov_b32 exec_hi, 0x3fffff\n\t"
            "s_add_u32 m0, s66, 4360\n\t"
            "s_add_u32 s49, s68, 981120\n\t"
            "buffer_load_dwordx4 v249, s[72:75], s49 offen lds\n\t"
            "s_mov_b32 exec_hi, -1\n\t"
            "s_mov_b32 exec_hi, 0x3fffff\n\t"
            "s_add_u32 m0, s66, 5232\n\t"
            "s_add_u32 s49, s68, 1177344\n\t"
            "buffer_load_dwordx4 v249, s[72:75], s49 offen lds\n\t"
            "s_mov_b32 exec_hi, -1\n\t"
            "s_mov_b32 exec_hi, 0x3fffff\n\t"
            "s_add_u32 m0, s66, 6104\n\t"
            "s_add_u32 s49, s68, 1373568\n\t"
            "buffer_load_dwordx4 v249, s[72:75], s49 offen lds\n\t"
            "s_mov_b32 exec_hi, -1\n\t"
            "s_mov_b32 exec_hi, 0x3fffff\n\t"
            "s_add_u32 m0, s66, 6976\n\t"
            "s_add_u32 s49, s68, 1569792\n\t"
            "buffer_load_dwordx4 v249, s[72:75], s49 offen lds\n\t"
            "s_mov_b32 exec_hi, -1\n\t"
            "v_cvt_pk_bf16_f32 v184, v184, v185\n\t"
            "v_cvt_pk_bf16_f32 v185, v186, v187\n\t"
            "v_cvt_pk_bf16_f32 v186, v188, v189\n\t"
            "v_cvt_pk_bf16_f32 v187, v190, v191\n\t"
            "s_setprio 3\n\t"
            "v_mfma_f32_4x4x4_16b_bf16 v[196:199], v[184:185], v[184:185], v[196:199]\n\t"
            "ds_write_b128 v237, v[184:187] offset:256\n\t"
            "v_mfma_f32_16x16x32_bf16 v[192:195], v[238:241], v[184:187], v[192:195]\n\t"
            "s_waitcnt lgkmcnt(1)\n\t"
            "v_readfirstlane_b32 s77, v247\n\t"
            "s_cmp_ge_u32 s77, s70\n\t"
            "s_cbranch_scc0 .Lwb_spin%=\n\t"
            ".Lwb_go%=:\n\t"
            "v_mfma_f32_4x4x4_16b_bf16 v[196:199], v[186:187], v[186:187], v[196:199]\n\t"
            "s_setprio 1\n\t"
            "ds_read_b128 v[184:187], v242\n\t"
            "ds_read_b128 v[188:191], v243\n\t"
            "ds_read_b128 v[172:175], v243 offset:128\n\t"
            "v_and_b32 v157, 0xffff0000, v202\n\t"
            "v_lshlrev_b32 v158, 16, v203\n\t"
            "v_and_b32 v159, 0xffff0000, v203\n\t"
            "v_lshlrev_b32 v160, 16, v204\n\t"
            "v_and_b32 v161, 0xffff0000, v204\n\t"
            "v_lshlrev_b32 v162, 16, v205\n\t"
            "v_cndmask_b32_e64 v193, v196, v197, s[60:61]\n\t"
            "v_cndmask_b32_e64 v193, v193, v198, s[62:63]\n\t"
            "v_cndmask_b32_e64 v193, v193, v199, s[64:65]\n\t"
            "v_and_b32 v163, 0xffff0000, v205\n\t"
            "v_lshlrev_b32 v164, 16, v206\n\t"
            "v_and_b32 v165, 0xffff0000, v206\n\t"
            "v_lshlrev_b32 v166, 16, v207\n\t"
            "v_and_b32 v167, 0xffff0000, v207\n\t"
            "v_lshlrev_b32 v168, 16, v216\n\t"
            "v_and_b32 v169, 0xffff0000, v216\n\t"
            "v_lshlrev_b32 v170, 16, v217\n\t"
            "v_and_b32 v171, 0xffff0000, v217\n\t"
            "s_bfe_u32 s57, s56, 0x20004\n\t"
            "s_lshl_b32 s58, s57, 3\n\t"
            "s_lshr_b32 s58, s55, s58\n\t"
            "s_lshl_b64 s[36:37], s[34:35], s58\n\t"
            "ds_read_b128 v[196:199], v252\n\t"
            "ds_read_b128 v[200:203], v236\n\t"
            "ds_write_b64 v248, v[192:193] offset:18816\n\t"
            "ds_read_b128 v[192:195], v242 offset:128\n\t"
            "s_bitcmp1_b32 s57, 1\n\t"
            "s_cselect_b32 s58, 1177344, 0\n\t"
            "s_bitcmp1_b32 s57, 0\n\t"
            "s_cselect_b32 s57, 2304, 0\n\t"
            "s_add_u32 s57, s57, s58\n\t"
            "s_mov_b64 exec, 1\n\t"
            "ds_inc_u32 v255, v238 offset:7860\n\t"
            "s_mov_b64 exec, -1\n\t"
            "v_add_u32 v237, s48, v245\n\t"
            "v_add_u32 v248, s48, v246\n\t"
            "s_add_u32 s59, s59, s84\n\t"
            "s_movk_i32 s84, 800\n\t"
            "ds_read_b128 v[204:207], v237\n\t"
            "s_setprio 3\n\t"
            "s_waitcnt lgkmcnt(7)\n\t"
            "v_mfma_f32_16x16x32_bf16 v[152:155], v[0:3], v[184:187], v[152:155]\n\t"
            "s_setprio 1\n\t"
            "s_setprio 1\n\t"
            "ds_read_b128 v[208:211], v248\n\t"
            "v_mfma_f32_16x16x32_bf16 v[152:155], v[4:7], v[188:191], v[152:155]\n\t"
            "v_mfma_f32_16x16x32_bf16 v[156:159], v[28:31], v[188:191], v[156:159]\n\t"
            "v_mfma_f32_16x16x32_bf16 v[156:159], v[24:27], v[184:187], v[156:159]\n\t"
            "v_mfma_f32_16x16x32_bf16 v[160:163], v[48:51], v[184:187], v[160:163]\n\t"
            "v_mfma_f32_16x16x32_bf16 v[160:163], v[52:55], v[188:191], v[160:163]\n\t"
            "v_mfma_f32_16x16x32_bf16 v[164:167], v[76:79], v[188:191], v[164:167]\n\t"
            "s_waitcnt lgkmcnt(6)\n\t"
            "s_mov_b64 exec, s[36:37]\n\t"
            "v_add_u32 v196, s57, v196\n\t"
            "s_mov_b64 exec, -1\n\t"
            "v_mfma_f32_16x16x32_bf16 v[164:167], v[72:75], v[184:187], v[164:167]\n\t"
            "v_mfma_f32_16x16x32_bf16 v[176:179], v[96:99], v[184:187], v[176:179]\n\t"
            "v_mfma_f32_16x16x32_bf16 v[176:179], v[100:103], v[188:191], v[176:179]\n\t"
            "v_mfma_f32_16x16x32_bf16 v[180:183], v[124:127], v[188:191], v[180:183]\n\t"
            "ds_read_b128 v[188:191], v243 offset:256\n\t"
            "v_mfma_f32_16x16x32_bf16 v[180:183], v[120:123], v[184:187], v[180:183]\n\t"
            "ds_read_b128 v[184:187], v242 offset:256\n\t"
            "s_waitcnt lgkmcnt(5)\n\t"
            "v_mfma_f32_16x16x32_bf16 v[152:155], v[8:11], v[192:195], v[152:155]\n\t"
            "v_mfma_f32_16x16x32_bf16 v[152:155], v[12:15], v[172:175], v[152:155]\n\t"
            "v_mfma_f32_16x16x32_bf16 v[156:159], v[36:39], v[172:175], v[156:159]\n\t"
            "v_mfma_f32_16x16x32_bf16 v[156:159], v[32:35], v[192:195], v[156:159]\n\t"
            "v_mfma_f32_16x16x32_bf16 v[160:163], v[56:59], v[192:195], v[160:163]\n\t"
            "v_mfma_f32_16x16x32_bf16 v[160:163], v[60:63], v[172:175], v[160:163]\n\t"
            "v_mfma_f32_16x16x32_bf16 v[164:167], v[84:87], v[172:175], v[164:167]\n\t"
            "v_mfma_f32_16x16x32_bf16 v[164:167], v[80:83], v[192:195], v[164:167]\n\t"
            "v_mfma_f32_16x16x32_bf16 v[176:179], v[104:107], v[192:195], v[176:179]\n\t"
            "v_mfma_f32_16x16x32_bf16 v[176:179], v[108:111], v[172:175], v[176:179]\n\t"
            "v_mfma_f32_16x16x32_bf16 v[180:183], v[132:135], v[172:175], v[180:183]\n\t"
            "ds_read_b128 v[172:175], v243 offset:6144\n\t"
            "v_mfma_f32_16x16x32_bf16 v[180:183], v[128:131], v[192:195], v[180:183]\n\t"
            "ds_read_b128 v[192:195], v242 offset:6144\n\t"
            "s_waitcnt lgkmcnt(2)\n\t"
            "v_mfma_f32_16x16x32_bf16 v[152:155], v[16:19], v[184:187], v[152:155]\n\t"
            "v_mfma_f32_16x16x32_bf16 v[152:155], v[20:23], v[188:191], v[152:155]\n\t"
            "v_mfma_f32_16x16x32_bf16 v[156:159], v[44:47], v[188:191], v[156:159]\n\t"
            "v_mfma_f32_16x16x32_bf16 v[156:159], v[40:43], v[184:187], v[156:159]\n\t"
            "v_mfma_f32_16x16x32_bf16 v[160:163], v[64:67], v[184:187], v[160:163]\n\t"
            "v_mfma_f32_16x16x32_bf16 v[160:163], v[68:71], v[188:191], v[160:163]\n\t"
            "v_mfma_f32_16x16x32_bf16 v[164:167], v[92:95], v[188:191], v[164:167]\n\t"
            "v_mfma_f32_16x16x32_bf16 v[164:167], v[88:91], v[184:187], v[164:167]\n\t"
            "s_nop 1\n\t"
            "v_cvt_pk_bf16_f32 v152, v152, v153\n\t"
            "v_mfma_f32_16x16x32_bf16 v[176:179], v[112:115], v[184:187], v[176:179]\n\t"
            "v_cvt_pk_bf16_f32 v153, v154, v155\n\t"
            "v_mfma_f32_16x16x32_bf16 v[176:179], v[116:119], v[188:191], v[176:179]\n\t"
            "v_cvt_pk_bf16_f32 v154, v156, v157\n\t"
            "v_cvt_pk_bf16_f32 v155, v158, v159\n\t"
            "v_mfma_f32_16x16x32_bf16 v[180:183], v[140:143], v[188:191], v[180:183]\n\t"
            "ds_read_b128 v[188:191], v243 offset:6272\n\t"
            "v_cvt_pk_bf16_f32 v160, v160, v161\n\t"
            "v_mfma_f32_16x16x32_bf16 v[180:183], v[136:139], v[184:187], v[180:183]\n\t"
            "ds_read_b128 v[184:187], v242 offset:6272\n\t"
            "v_cvt_pk_bf16_f32 v161, v162, v163\n\t"
            "s_waitcnt lgkmcnt(2)\n\t"
            "v_mfma_f32_16x16x32_bf16 v[144:147], v[0:3], v[192:195], v[144:147]\n\t"
            "v_cvt_pk_bf16_f32 v176, v176, v177\n\t"
            "v_cvt_pk_bf16_f32 v177, v178, v179\n\t"
            "v_mfma_f32_16x16x32_bf16 v[144:147], v[4:7], v[172:175], v[144:147]\n\t"
            "v_cvt_pk_bf16_f32 v162, v164, v165\n\t"
            "v_cvt_pk_bf16_f32 v163, v166, v167\n\t"
            "v_mfma_f32_16x16x32_bf16 v[148:151], v[28:31], v[172:175], v[148:151]\n\t"
            "v_cvt_pk_bf16_f32 v178, v180, v181\n\t"
            "v_cvt_pk_bf16_f32 v179, v182, v183\n\t"
            "v_mfma_f32_16x16x32_bf16 v[148:151], v[24:27], v[192:195], v[148:151]\n\t"
            "v_lshlrev_b32 v156, 16, v218\n\t"
            "v_and_b32 v157, 0xffff0000, v218\n\t"
            "v_mfma_f32_16x16x32_bf16 v[168:171], v[48:51], v[192:195], v[168:171]\n\t"
            "v_lshlrev_b32 v158, 16, v219\n\t"
            "v_and_b32 v159, 0xffff0000, v219\n\t"
            "v_mfma_f32_16x16x32_bf16 v[168:171], v[52:55], v[172:175], v[168:171]\n\t"
            "ds_read_b128 v[216:219], v237 offset:6144\n\t"
            "v_lshlrev_b32 v164, 16, v220\n\t"
            "v_mfma_f32_16x16x32_bf16 v[156:159], v[76:79], v[172:175], v[156:159]\n\t"
            "v_and_b32 v165, 0xffff0000, v220\n\t"
            "v_lshlrev_b32 v166, 16, v221\n\t"
            "v_mfma_f32_16x16x32_bf16 v[156:159], v[72:75], v[192:195], v[156:159]\n\t"
            "v_and_b32 v167, 0xffff0000, v221\n\t"
            "v_lshlrev_b32 v180, 16, v222\n\t"
            "v_and_b32 v181, 0xffff0000, v222\n\t"
            "v_mfma_f32_16x16x32_bf16 v[164:167], v[96:99], v[192:195], v[164:167]\n\t"
            "v_lshlrev_b32 v182, 16, v223\n\t"
            "v_and_b32 v183, 0xffff0000, v223\n\t"
            "v_mfma_f32_16x16x32_bf16 v[164:167], v[100:103], v[172:175], v[164:167]\n\t"
            "ds_read_b128 v[220:223], v248 offset:6144\n\t"
            "buffer_store_dwordx4 v[152:155], v196, s[40:43], s45 offen sc1\n\t"
            "v_mfma_f32_16x16x32_bf16 v[180:183], v[124:127], v[172:175], v[180:183]\n\t"
            "ds_read_b128 v[172:175], v243 offset:6400\n\t"
            "v_lshlrev_b32 v152, 16, v224\n\t"
            "v_mfma_f32_16x16x32_bf16 v[180:183], v[120:123], v[192:195], v[180:183]\n\t"
            "ds_read_b128 v[192:195], v242 offset:6400\n\t"
            "buffer_store_dwordx4 v[160:163], v196, s[40:43], s45 offen offset:64 sc1\n\t"
            "s_waitcnt lgkmcnt(4)\n\t"
            "v_mfma_f32_16x16x32_bf16 v[144:147], v[8:11], v[184:187], v[144:147]\n\t"
            "v_and_b32 v153, 0xffff0000, v224\n\t"
            "v_lshlrev_b32 v154, 16, v225\n\t"
            "v_mfma_f32_16x16x32_bf16 v[144:147], v[12:15], v[188:191], v[144:147]\n\t"
            "buffer_store_dwordx4 v[176:179], v196, s[40:43], s45 offen offset:128 sc1\n\t"
            "v_mfma_f32_16x16x32_bf16 v[148:151], v[36:39], v[188:191], v[148:151]\n\t"
            "v_and_b32 v155, 0xffff0000, v225\n\t"
            "v_lshlrev_b32 v160, 16, v226\n\t"
            "v_mfma_f32_16x16x32_bf16 v[148:151], v[32:35], v[184:187], v[148:151]\n\t"
            "v_and_b32 v161, 0xffff0000, v226\n\t"
            "v_lshlrev_b32 v162, 16, v227\n\t"
            "v_mfma_f32_16x16x32_bf16 v[168:171], v[56:59], v[184:187], v[168:171]\n\t"
            "v_and_b32 v163, 0xffff0000, v227\n\t"
            "v_lshlrev_b32 v176, 16, v228\n\t"
            "v_mfma_f32_16x16x32_bf16 v[168:171], v[60:63], v[188:191], v[168:171]\n\t"
            "ds_read_b128 v[224:227], v236 offset:12288\n\t"
            "v_and_b32 v177, 0xffff0000, v228\n\t"
            "v_mfma_f32_16x16x32_bf16 v[156:159], v[84:87], v[188:191], v[156:159]\n\t"
            "v_lshlrev_b32 v178, 16, v229\n\t"
            "v_and_b32 v179, 0xffff0000, v229\n\t"
            "v_mfma_f32_16x16x32_bf16 v[156:159], v[80:83], v[184:187], v[156:159]\n\t"
            "v_mfma_f32_16x16x32_bf16 v[164:167], v[104:107], v[184:187], v[164:167]\n\t"
            "v_mfma_f32_16x16x32_bf16 v[164:167], v[108:111], v[188:191], v[164:167]\n\t"
            "v_mfma_f32_16x16x32_bf16 v[180:183], v[132:135], v[188:191], v[180:183]\n\t"
            "ds_read_b128 v[188:191], v243 offset:12288\n\t"
            "v_mfma_f32_16x16x32_bf16 v[180:183], v[128:131], v[184:187], v[180:183]\n\t"
            "ds_read_b128 v[184:187], v242 offset:12288\n\t"
            "s_waitcnt lgkmcnt(3)\n\t"
            "v_mfma_f32_16x16x32_bf16 v[144:147], v[16:19], v[192:195], v[144:147]\n\t"
            "v_mfma_f32_16x16x32_bf16 v[144:147], v[20:23], v[172:175], v[144:147]\n\t"
            "v_mfma_f32_16x16x32_bf16 v[168:171], v[64:67], v[192:195], v[168:171]\n\t"
            "v_mfma_f32_16x16x32_bf16 v[168:171], v[68:71], v[172:175], v[168:171]\n\t"
            "v_mfma_f32_16x16x32_bf16 v[164:167], v[112:115], v[192:195], v[164:167]\n\t"
            "v_mfma_f32_16x16x32_bf16 v[164:167], v[116:119], v[172:175], v[164:167]\n\t"
            "v_mfma_f32_16x16x32_bf16 v[148:151], v[44:47], v[172:175], v[148:151]\n\t"
            "v_mfma_f32_16x16x32_bf16 v[148:151], v[40:43], v[192:195], v[148:151]\n\t"
            "s_nop 1\n\t"
            "v_cvt_pk_bf16_f32 v144, v144, v145\n\t"
            "v_mfma_f32_16x16x32_bf16 v[156:159], v[92:95], v[172:175], v[156:159]\n\t"
            "v_cvt_pk_bf16_f32 v145, v146, v147\n\t"
            "v_mfma_f32_16x16x32_bf16 v[156:159], v[88:91], v[192:195], v[156:159]\n\t"
            "v_cvt_pk_bf16_f32 v168, v168, v169\n\t"
            "v_cvt_pk_bf16_f32 v169, v170, v171\n\t"
            "v_mfma_f32_16x16x32_bf16 v[180:183], v[140:143], v[172:175], v[180:183]\n\t"
            "ds_read_b128 v[172:175], v243 offset:12416\n\t"
            "v_cvt_pk_bf16_f32 v164, v164, v165\n\t"
            "v_mfma_f32_16x16x32_bf16 v[180:183], v[136:139], v[192:195], v[180:183]\n\t"
            "ds_read_b128 v[192:195], v242 offset:12416\n\t"
            "v_cvt_pk_bf16_f32 v165, v166, v167\n\t"
            "s_waitcnt lgkmcnt(2)\n\t"
            "v_mfma_f32_16x16x32_bf16 v[152:155], v[0:3], v[184:187], v[152:155]\n\t"
            "v_cvt_pk_bf16_f32 v146, v148, v149\n\t"
            "v_cvt_pk_bf16_f32 v147, v150, v151\n\t"
            "v_mfma_f32_16x16x32_bf16 v[152:155], v[4:7], v[188:191], v[152:155]\n\t"
            "v_cvt_pk_bf16_f32 v170, v156, v157\n\t"
            "v_cvt_pk_bf16_f32 v171, v158, v159\n\t"
            "v_mfma_f32_16x16x32_bf16 v[160:163], v[28:31], v[188:191], v[160:163]\n\t"
            "v_cvt_pk_bf16_f32 v166, v180, v181\n\t"
            "v_cvt_pk_bf16_f32 v167, v182, v183\n\t"
            "v_mfma_f32_16x16x32_bf16 v[160:163], v[24:27], v[184:187], v[160:163]\n\t"
            "v_lshlrev_b32 v148, 16, v230\n\t"
            "v_and_b32 v149, 0xffff0000, v230\n\t"
            "v_mfma_f32_16x16x32_bf16 v[176:179], v[48:51], v[184:187], v[176:179]\n\t"
            "v_lshlrev_b32 v150, 16, v231\n\t"
            "v_and_b32 v151, 0xffff0000, v231\n\t"
            "v_mfma_f32_16x16x32_bf16 v[176:179], v[52:55], v[188:191], v[176:179]\n\t"
            "ds_read_b128 v[228:231], v237 offset:12288\n\t"
            "v_lshlrev_b32 v156, 16, v232\n\t"
            "v_mfma_f32_16x16x32_bf16 v[148:151], v[76:79], v[188:191], v[148:151]\n\t"
            "v_and_b32 v157, 0xffff0000, v232\n\t"
            "v_lshlrev_b32 v158, 16, v233\n\t"
            "v_mfma_f32_16x16x32_bf16 v[148:151], v[72:75], v[184:187], v[148:151]\n\t"
            "v_and_b32 v159, 0xffff0000, v233\n\t"
            "v_lshlrev_b32 v180, 16, v234\n\t"
            "v_and_b32 v181, 0xffff0000, v234\n\t"
            "v_mfma_f32_16x16x32_bf16 v[156:159], v[96:99], v[184:187], v[156:159]\n\t"
            "v_lshlrev_b32 v182, 16, v235\n\t"
            "v_and_b32 v183, 0xffff0000, v235\n\t"
            "v_mfma_f32_16x16x32_bf16 v[156:159], v[100:103], v[188:191], v[156:159]\n\t"
            "ds_read_b128 v[232:235], v248 offset:12288\n\t"
            "buffer_store_dwordx4 v[144:147], v197, s[40:43], s45 offen sc1\n\t"
            "v_mfma_f32_16x16x32_bf16 v[180:183], v[124:127], v[188:191], v[180:183]\n\t"
            "ds_read_b128 v[188:191], v243 offset:12544\n\t"
            "v_mfma_f32_16x16x32_bf16 v[180:183], v[120:123], v[184:187], v[180:183]\n\t"
            "ds_read_b128 v[184:187], v242 offset:12544\n\t"
            "buffer_store_dwordx4 v[168:171], v197, s[40:43], s45 offen offset:64 sc1\n\t"
            "s_waitcnt lgkmcnt(4)\n\t"
            "v_mfma_f32_16x16x32_bf16 v[152:155], v[8:11], v[192:195], v[152:155]\n\t"
            "v_mfma_f32_16x16x32_bf16 v[152:155], v[12:15], v[172:175], v[152:155]\n\t"
            "buffer_store_dwordx4 v[164:167], v197, s[40:43], s45 offen offset:128 sc1\n\t"
            "v_mfma_f32_16x16x32_bf16 v[160:163], v[36:39], v[172:175], v[160:163]\n\t"
            "v_mfma_f32_16x16x32_bf16 v[160:163], v[32:35], v[192:195], v[160:163]\n\t"
            "s_waitcnt lgkmcnt(0)\n\t"
            "s_barrier\n\t"
            "s_setprio 3\n\t"
            "v_mfma_f32_16x16x32_bf16 v[176:179], v[56:59], v[192:195], v[176:179]\n\t"
            "v_mfma_f32_16x16x32_bf16 v[176:179], v[60:63], v[172:175], v[176:179]\n\t"
            "v_mfma_f32_16x16x32_bf16 v[148:151], v[84:87], v[172:175], v[148:151]\n\t"
            "v_mfma_f32_16x16x32_bf16 v[148:151], v[80:83], v[192:195], v[148:151]\n\t"
            "v_mfma_f32_16x16x32_bf16 v[156:159], v[104:107], v[192:195], v[156:159]\n\t"
            "v_mfma_f32_16x16x32_bf16 v[156:159], v[108:111], v[172:175], v[156:159]\n\t"
            "v_mfma_f32_16x16x32_bf16 v[152:155], v[16:19], v[184:187], v[152:155]\n\t"
            "v_mfma_f32_16x16x32_bf16 v[152:155], v[20:23], v[188:191], v[152:155]\n\t"
            "v_mfma_f32_16x16x32_bf16 v[160:163], v[44:47], v[188:191], v[160:163]\n\t"
            "v_mfma_f32_16x16x32_bf16 v[160:163], v[40:43], v[184:187], v[160:163]\n\t"
            "v_mfma_f32_16x16x32_bf16 v[180:183], v[132:135], v[172:175], v[180:183]\n\t"
            "v_mfma_f32_16x16x32_bf16 v[180:183], v[128:131], v[192:195], v[180:183]\n\t"
            "v_mfma_f32_16x16x32_bf16 v[176:179], v[64:67], v[184:187], v[176:179]\n\t"
            "v_mfma_f32_16x16x32_bf16 v[176:179], v[68:71], v[188:191], v[176:179]\n\t"
            "s_nop 1\n\t"
            "v_cvt_pk_bf16_f32 v152, v152, v153\n\t"
            "v_mfma_f32_16x16x32_bf16 v[148:151], v[92:95], v[188:191], v[148:151]\n\t"
            "v_cvt_pk_bf16_f32 v153, v154, v155\n\t"
            "v_mfma_f32_16x16x32_bf16 v[148:151], v[88:91], v[184:187], v[148:151]\n\t"
            "v_cvt_pk_bf16_f32 v154, v160, v161\n\t"
            "v_cvt_pk_bf16_f32 v155, v162, v163\n\t"
            "v_mfma_f32_16x16x32_bf16 v[156:159], v[112:115], v[184:187], v[156:159]\n\t"
            "v_cvt_pk_bf16_f32 v176, v176, v177\n\t"
            "v_cvt_pk_bf16_f32 v177, v178, v179\n\t"
            "v_mfma_f32_16x16x32_bf16 v[156:159], v[116:119], v[188:191], v[156:159]\n\t"
            "buffer_store_dwordx4 v[152:155], v198, s[40:43], s45 offen sc1\n\t"
            "v_mfma_f32_16x16x32_bf16 v[180:183], v[140:143], v[188:191], v[180:183]\n\t"
            "v_cvt_pk_bf16_f32 v178, v148, v149\n\t"
            "v_cvt_pk_bf16_f32 v179, v150, v151\n\t"
            "v_mfma_f32_16x16x32_bf16 v[180:183], v[136:139], v[184:187], v[180:183]\n\t"
            "buffer_store_dwordx4 v[176:179], v198, s[40:43], s45 offen offset:64 sc1\n\t"
            "s_nop 1\n\t"
            "v_cvt_pk_bf16_f32 v156, v156, v157\n\t"
            "v_cvt_pk_bf16_f32 v157, v158, v159\n\t"
            "s_nop 2\n\t"
            "v_cvt_pk_bf16_f32 v158, v180, v181\n\t"
            "v_cvt_pk_bf16_f32 v159, v182, v183\n\t"
            "buffer_store_dwordx4 v[156:159], v198, s[40:43], s45 offen offset:128 sc1\n\t"
            "s_add_u32 s45, s46, s27\n\t"
            "s_mov_b32 s46, s47\n\t"
            "s_mov_b32 s47, s68\n\t"
            "s_xor_b32 s48, s48, s71\n\t"
            "s_xor_b32 s67, s67, s71\n\t"
            "s_add_u32 s76, s76, 4\n\t"
            "s_add_u32 s70, s70, 4\n\t"
            "s_lshl_b32 s56, s56, 2\n\t"
            "s_lshr_b32 s49, s26, 30\n\t"
            "s_or_b32 s56, s56, s49\n\t"
            "s_cmp_lt_u32 s46, 0x7000000\n\t"
            "s_cbranch_scc1 .Lwb_loop%=\n\t"
            "s_branch .Lwb_end%=\n\t"
            ".Lwb_spin%=:\n\t"
            "s_cmp_eq_u32 s83, 0\n\t"
            "s_cbranch_scc1 .Lwb_go%=\n\t"
            "s_sub_u32 s83, s83, 1\n\t"
            "s_sleep 2\n\t"
            "v_mov_b32 v247, 0\n\t"
            "ds_read_b32 v247, v247 offset:7856\n\t"
            "s_waitcnt lgkmcnt(0)\n\t"
            "v_readfirstlane_b32 s77, v247\n\t"
            "s_cmp_ge_u32 s77, s70\n\t"
            "s_cbranch_scc0 .Lwb_spin%=\n\t"
            "s_branch .Lwb_go%=\n\t"
            ".Lwb_end%=:\n\t"
            "s_waitcnt lgkmcnt(0)\n\t"
            "s_nop 7\n\t"
            "ds_read_b128 v[196:199], v252\n\t"
            "s_bfe_u32 s57, s56, 0x20004\n\t"
            "s_lshl_b32 s58, s57, 3\n\t"
            "s_lshr_b32 s58, s55, s58\n\t"
            "s_lshl_b64 s[36:37], s[34:35], s58\n\t"
            "s_bitcmp1_b32 s57, 1\n\t"
            "s_cselect_b32 s58, 1177344, 0\n\t"
            "s_bitcmp1_b32 s57, 0\n\t"
            "s_cselect_b32 s57, 2304, 0\n\t"
            "s_add_u32 s57, s57, s58\n\t"
            "v_lshlrev_b32 v152, 16, v200\n\t"
            "v_and_b32 v153, 0xffff0000, v200\n\t"
            "v_lshlrev_b32 v154, 16, v201\n\t"
            "v_and_b32 v155, 0xffff0000, v201\n\t"
            "v_lshlrev_b32 v156, 16, v202\n\t"
            "v_and_b32 v157, 0xffff0000, v202\n\t"
            "v_lshlrev_b32 v158, 16, v203\n\t"
            "v_and_b32 v159, 0xffff0000, v203\n\t"
            "v_lshlrev_b32 v160, 16, v204\n\t"
            "v_and_b32 v161, 0xffff0000, v204\n\t"
            "v_lshlrev_b32 v162, 16, v205\n\t"
            "v_and_b32 v163, 0xffff0000, v205\n\t"
            "v_lshlrev_b32 v164, 16, v206\n\t"
            "v_and_b32 v165, 0xffff0000, v206\n\t"
            "v_lshlrev_b32 v166, 16, v207\n\t"
            "v_and_b32 v167, 0xffff0000, v207\n\t"
            "v_lshlrev_b32 v176, 16, v208\n\t"
            "v_and_b32 v177, 0xffff0000, v208\n\t"
            "v_lshlrev_b32 v178, 16, v209\n\t"
            "v_and_b32 v179, 0xffff0000, v209\n\t"
            "v_lshlrev_b32 v180, 16, v210\n\t"
            "v_and_b32 v181, 0xffff0000, v210\n\t"
            "v_lshlrev_b32 v182, 16, v211\n\t"
            "v_and_b32 v183, 0xffff0000, v211\n\t"
            "v_lshlrev_b32 v144, 16, v212\n\t"
            "v_and_b32 v145, 0xffff0000, v212\n\t"
            "v_lshlrev_b32 v146, 16, v213\n\t"
            "v_and_b32 v147, 0xffff0000, v213\n\t"
            "v_lshlrev_b32 v148, 16, v214\n\t"
            "v_and_b32 v149, 0xffff0000, v214\n\t"
            "v_lshlrev_b32 v150, 16, v215\n\t"
            "v_and_b32 v151, 0xffff0000, v215\n\t"
            "v_lshlrev_b32 v168, 16, v216\n\t"
            "v_and_b32 v169, 0xffff0000, v216\n\t"
            "v_lshlrev_b32 v170, 16, v217\n\t"
            "s_waitcnt lgkmcnt(0)\n\t"
            "s_mov_b64 exec, s[36:37]\n\t"
            "v_add_u32 v196, s57, v196\n\t"
            "s_mov_b64 exec, -1\n\t"
            ".Lwk_t1%=:\n\t"
            "v_mov_b32 v247, 0\n\t"
            "ds_read_b32 v247, v247 offset:7856\n\t"
            "s_waitcnt lgkmcnt(0)\n\t"
            "v_readfirstlane_b32 s77, v247\n\t"
            "s_cmp_ge_u32 s77, s70\n\t"
            "s_cbranch_scc1 .Lwk_t2%=\n\t"
            "s_cmp_eq_u32 s83, 0\n\t"
            "s_cbranch_scc1 .Lwk_t2%=\n\t"
            "s_sub_u32 s83, s83, 1\n\t"
            "s_sleep 1\n\t"
            "s_branch .Lwk_t1%=\n\t"
            ".Lwk_t2%=:\n\t"
            "v_and_b32 v171, 0xffff0000, v217\n\t"
            "ds_read_b128 v[184:187], v242\n\t"
            "ds_read_b128 v[188:191], v243\n\t"
            "ds_read_b128 v[192:195], v242 offset:128\n\t"
            "ds_read_b128 v[172:175], v243 offset:128\n\t"
            "s_waitcnt lgkmcnt(2)\n\t"
            "v_mfma_f32_16x16x32_bf16 v[152:155], v[0:3], v[184:187], v[152:155]\n\t"
            "v_mfma_f32_16x16x32_bf16 v[152:155], v[4:7], v[188:191], v[152:155]\n\t"
            "v_mfma_f32_16x16x32_bf16 v[156:159], v[28:31], v[188:191], v[156:159]\n\t"
            "v_mfma_f32_16x16x32_bf16 v[156:159], v[24:27], v[184:187], v[156:159]\n\t"
            "v_mfma_f32_16x16x32_bf16 v[160:163], v[48:51], v[184:187], v[160:163]\n\t"
            "v_mfma_f32_16x16x32_bf16 v[160:163], v[52:55], v[188:191], v[160:163]\n\t"
            "v_mfma_f32_16x16x32_bf16 v[164:167], v[76:79], v[188:191], v[164:167]\n\t"
            "v_mfma_f32_16x16x32_bf16 v[164:167], v[72:75], v[184:187], v[164:167]\n\t"
            "v_mfma_f32_16x16x32_bf16 v[176:179], v[96:99], v[184:187], v[176:179]\n\t"
            "v_mfma_f32_16x16x32_bf16 v[176:179], v[100:103], v[188:191], v[176:179]\n\t"
            "v_mfma_f32_16x16x32_bf16 v[180:183], v[124:127], v[188:191], v[180:183]\n\t"
            "ds_read_b128 v[188:191], v243 offset:256\n\t"
            "v_mfma_f32_16x16x32_bf16 v[180:183], v[120:123], v[184:187], v[180:183]\n\t"
            "ds_read_b128 v[184:187], v242 offset:256\n\t"
            "s_waitcnt lgkmcnt(2)\n\t"
            "v_mfma_f32_16x16x32_bf16 v[152:155], v[8:11], v[192:195], v[152:155]\n\t"
            "v_mfma_f32_16x16x32_bf16 v[152:155], v[12:15], v[172:175], v[152:155]\n\t"
            "v_mfma_f32_16x16x32_bf16 v[156:159], v[36:39], v[172:175], v[156:159]\n\t"
            "v_mfma_f32_16x16x32_bf16 v[156:159], v[32:35], v[192:195], v[156:159]\n\t"
            "v_mfma_f32_16x16x32_bf16 v[160:163], v[56:59], v[192:195], v[160:163]\n\t"
            "v_mfma_f32_16x16x32_bf16 v[160:163], v[60:63], v[172:175], v[160:163]\n\t"
            "v_mfma_f32_16x16x32_bf16 v[164:167], v[84:87], v[172:175], v[164:167]\n\t"
            "v_mfma_f32_16x16x32_bf16 v[164:167], v[80:83], v[192:195], v[164:167]\n\t"
            "v_mfma_f32_16x16x32_bf16 v[176:179], v[104:107], v[192:195], v[176:179]\n\t"
            "v_mfma_f32_16x16x32_bf16 v[176:179], v[108:111], v[172:175], v[176:179]\n\t"
            "v_mfma_f32_16x16x32_bf16 v[180:183], v[132:135], v[172:175], v[180:183]\n\t"
            "ds_read_b128 v[172:175], v243 offset:6144\n\t"
            "v_mfma_f32_16x16x32_bf16 v[180:183], v[128:131], v[192:195], v[180:183]\n\t"
            "ds_read_b128 v[192:195], v242 offset:6144\n\t"
            "s_waitcnt lgkmcnt(2)\n\t"
            "v_mfma_f32_16x16x32_bf16 v[152:155], v[16:19], v[184:187], v[152:155]\n\t"
            "v_mfma_f32_16x16x32_bf16 v[152:155], v[20:23], v[188:191], v[152:155]\n\t"
            "v_mfma_f32_16x16x32_bf16 v[156:159], v[44:47], v[188:191], v[156:159]\n\t"
            "v_mfma_f32_16x16x32_bf16 v[156:159], v[40:43], v[184:187], v[156:159]\n\t"
            "v_mfma_f32_16x16x32_bf16 v[160:163], v[64:67], v[184:187], v[160:163]\n\t"
            "v_mfma_f32_16x16x32_bf16 v[160:163], v[68:71], v[188:191], v[160:163]\n\t"
            "v_mfma_f32_16x16x32_bf16 v[164:167], v[92:95], v[188:191], v[164:167]\n\t"
            "v_mfma_f32_16x16x32_bf16 v[164:167], v[88:91], v[184:187], v[164:167]\n\t"
            "s_nop 1\n\t"
            "v_cvt_pk_bf16_f32 v152, v152, v153\n\t"
            "v_mfma_f32_16x16x32_bf16 v[176:179], v[112:115], v[184:187], v[176:179]\n\t"
            "v_cvt_pk_bf16_f32 v153, v154, v155\n\t"
            "v_mfma_f32_16x16x32_bf16 v[176:179], v[116:119], v[188:191], v[176:179]\n\t"
            "v_cvt_pk_bf16_f32 v154, v156, v157\n\t"
            "v_cvt_pk_bf16_f32 v155, v158, v159\n\t"
            "v_mfma_f32_16x16x32_bf16 v[180:183], v[140:143], v[188:191], v[180:183]\n\t"
            "ds_read_b128 v[188:191], v243 offset:6272\n\t"
            "v_cvt_pk_bf16_f32 v160, v160, v161\n\t"
            "v_mfma_f32_16x16x32_bf16 v[180:183], v[136:139], v[184:187], v[180:183]\n\t"
            "ds_read_b128 v[184:187], v242 offset:6272\n\t"
            "v_cvt_pk_bf16_f32 v161, v162, v163\n\t"
            "s_waitcnt lgkmcnt(2)\n\t"
            "v_mfma_f32_16x16x32_bf16 v[144:147], v[0:3], v[192:195], v[144:147]\n\t"
            "v_cvt_pk_bf16_f32 v176, v176, v177\n\t"
            "v_cvt_pk_bf16_f32 v177, v178, v179\n\t"
            "v_mfma_f32_16x16x32_bf16 v[144:147], v[4:7], v[172:175], v[144:147]\n\t"
            "v_cvt_pk_bf16_f32 v162, v164, v165\n\t"
            "v_cvt_pk_bf16_f32 v163, v166, v167\n\t"
            "v_mfma_f32_16x16x32_bf16 v[148:151], v[28:31], v[172:175], v[148:151]\n\t"
            "v_cvt_pk_bf16_f32 v178, v180, v181\n\t"
            "v_cvt_pk_bf16_f32 v179, v182, v183\n\t"
            "v_mfma_f32_16x16x32_bf16 v[148:151], v[24:27], v[192:195], v[148:151]\n\t"
            "v_lshlrev_b32 v156, 16, v218\n\t"
            "v_and_b32 v157, 0xffff0000, v218\n\t"
            "v_mfma_f32_16x16x32_bf16 v[168:171], v[48:51], v[192:195], v[168:171]\n\t"
            "v_lshlrev_b32 v158, 16, v219\n\t"
            "v_and_b32 v159, 0xffff0000, v219\n\t"
            "v_mfma_f32_16x16x32_bf16 v[168:171], v[52:55], v[172:175], v[168:171]\n\t"
            "v_lshlrev_b32 v164, 16, v220\n\t"
            "v_and_b32 v165, 0xffff0000, v220\n\t"
            "v_mfma_f32_16x16x32_bf16 v[156:159], v[76:79], v[172:175], v[156:159]\n\t"
            "v_lshlrev_b32 v166, 16, v221\n\t"
            "v_and_b32 v167, 0xffff0000, v221\n\t"
            "v_mfma_f32_16x16x32_bf16 v[156:159], v[72:75], v[192:195], v[156:159]\n\t"
            "v_lshlrev_b32 v180, 16, v222\n\t"
            "v_and_b32 v181, 0xffff0000, v222\n\t"
            "v_mfma_f32_16x16x32_bf16 v[164:167], v[96:99], v[192:195], v[164:167]\n\t"
            "v_lshlrev_b32 v182, 16, v223\n\t"
            "v_and_b32 v183, 0xffff0000, v223\n\t"
            "v_mfma_f32_16x16x32_bf16 v[164:167], v[100:103], v[172:175], v[164:167]\n\t"
            "buffer_store_dwordx4 v[152:155], v196, s[40:43], s45 offen sc1\n\t"
            "v_mfma_f32_16x16x32_bf16 v[180:183], v[124:127], v[172:175], v[180:183]\n\t"
            "ds_read_b128 v[172:175], v243 offset:6400\n\t"
            "v_lshlrev_b32 v152, 16, v224\n\t"
            "v_mfma_f32_16x16x32_bf16 v[180:183], v[120:123], v[192:195], v[180:183]\n\t"
            "ds_read_b128 v[192:195], v242 offset:6400\n\t"
            "buffer_store_dwordx4 v[160:163], v196, s[40:43], s45 offen offset:64 sc1\n\t"
            "s_waitcnt lgkmcnt(2)\n\t"
            "v_mfma_f32_16x16x32_bf16 v[144:147], v[8:11], v[184:187], v[144:147]\n\t"
            "v_and_b32 v153, 0xffff0000, v224\n\t"
            "v_lshlrev_b32 v154, 16, v225\n\t"
            "v_mfma_f32_16x16x32_bf16 v[144:147], v[12:15], v[188:191], v[144:147]\n\t"
            "buffer_store_dwordx4 v[176:179], v196, s[40:43], s45 offen offset:128 sc1\n\t"
            "v_mfma_f32_16x16x32_bf16 v[148:151], v[36:39], v[188:191], v[148:151]\n\t"
            "v_and_b32 v155, 0xffff0000, v225\n\t"
            "v_lshlrev_b32 v160, 16, v226\n\t"
            "v_mfma_f32_16x16x32_bf16 v[148:151], v[32:35], v[184:187], v[148:151]\n\t"
            "v_and_b32 v161, 0xffff0000, v226\n\t"
            "v_lshlrev_b32 v162, 16, v227\n\t"
            "v_mfma_f32_16x16x32_bf16 v[168:171], v[56:59], v[184:187], v[168:171]\n\t"
            "v_and_b32 v163, 0xffff0000, v227\n\t"
            "v_lshlrev_b32 v176, 16, v228\n\t"
            "v_mfma_f32_16x16x32_bf16 v[168:171], v[60:63], v[188:191], v[168:171]\n\t"
            "v_and_b32 v177, 0xffff0000, v228\n\t"
            "v_lshlrev_b32 v178, 16, v229\n\t"
            "v_mfma_f32_16x16x32_bf16 v[156:159], v[84:87], v[188:191], v[156:159]\n\t"
            "v_and_b32 v179, 0xffff0000, v229\n\t"
            "v_mfma_f32_16x16x32_bf16 v[156:159], v[80:83], v[184:187], v[156:159]\n\t"
            "v_mfma_f32_16x16x32_bf16 v[164:167], v[104:107], v[184:187], v[164:167]\n\t"
            "v_mfma_f32_16x16x32_bf16 v[164:167], v[108:111], v[188:191], v[164:167]\n\t"
            "v_mfma_f32_16x16x32_bf16 v[180:183], v[132:135], v[188:191], v[180:183]\n\t"
            "ds_read_b128 v[188:191], v243 offset:12288\n\t"
            "v_mfma_f32_16x16x32_bf16 v[180:183], v[128:131], v[184:187], v[180:183]\n\t"
            "ds_read_b128 v[184:187], v242 offset:12288\n\t"
            "s_waitcnt lgkmcnt(2)\n\t"
            "v_mfma_f32_16x16x32_bf16 v[144:147], v[16:19], v[192:195], v[144:147]\n\t"
            "v_mfma_f32_16x16x32_bf16 v[144:147], v[20:23], v[172:175], v[144:147]\n\t"
            "v_mfma_f32_16x16x32_bf16 v[168:171], v[64:67], v[192:195], v[168:171]\n\t"
            "v_mfma_f32_16x16x32_bf16 v[168:171], v[68:71], v[172:175], v[168:171]\n\t"
            "v_mfma_f32_16x16x32_bf16 v[164:167], v[112:115], v[192:195], v[164:167]\n\t"
            "v_mfma_f32_16x16x32_bf16 v[164:167], v[116:119], v[172:175], v[164:167]\n\t"
            "v_mfma_f32_16x16x32_bf16 v[148:151], v[44:47], v[172:175], v[148:151]\n\t"
            "v_mfma_f32_16x16x32_bf16 v[148:151], v[40:43], v[192:195], v[148:151]\n\t"
            "s_nop 1\n\t"
            "v_cvt_pk_bf16_f32 v144, v144, v145\n\t"
            "v_mfma_f32_16x16x32_bf16 v[156:159], v[92:95], v[172:175], v[156:159]\n\t"
            "v_cvt_pk_bf16_f32 v145, v146, v147\n\t"
            "v_mfma_f32_16x16x32_bf16 v[156:159], v[88:91], v[192:195], v[156:159]\n\t"
            "v_cvt_pk_bf16_f32 v168, v168, v169\n\t"
            "v_cvt_pk_bf16_f32 v169, v170, v171\n\t"
            "v_mfma_f32_16x16x32_bf16 v[180:183], v[140:143], v[172:175], v[180:183]\n\t"
            "ds_read_b128 v[172:175], v243 offset:12416\n\t"
            "v_cvt_pk_bf16_f32 v164, v164, v165\n\t"
            "v_mfma_f32_16x16x32_bf16 v[180:183], v[136:139], v[192:195], v[180:183]\n\t"
            "ds_read_b128 v[192:195], v242 offset:12416\n\t"
            "v_cvt_pk_bf16_f32 v165, v166, v167\n\t"
            "s_waitcnt lgkmcnt(2)\n\t"
            "v_mfma_f32_16x16x32_bf16 v[152:155], v[0:3], v[184:187], v[152:155]\n\t"
            "v_cvt_pk_bf16_f32 v146, v148, v149\n\t"
            "v_cvt_pk_bf16_f32 v147, v150, v151\n\t"
            "v_mfma_f32_16x16x32_bf16 v[152:155], v[4:7], v[188:191], v[152:155]\n\t"
            "v_cvt_pk_bf16_f32 v170, v156, v157\n\t"
            "v_cvt_pk_bf16_f32 v171, v158, v159\n\t"
            "v_mfma_f32_16x16x32_bf16 v[160:163], v[28:31], v[188:191], v[160:163]\n\t"
            "v_cvt_pk_bf16_f32 v166, v180, v181\n\t"
            "v_cvt_pk_bf16_f32 v167, v182, v183\n\t"
            "v_mfma_f32_16x16x32_bf16 v[160:163], v[24:27], v[184:187], v[160:163]\n\t"
            "v_lshlrev_b32 v148, 16, v230\n\t"
            "v_and_b32 v149, 0xffff0000, v230\n\t"
            "v_mfma_f32_16x16x32_bf16 v[176:179], v[48:51], v[184:187], v[176:179]\n\t"
            "v_lshlrev_b32 v150, 16, v231\n\t"
            "v_and_b32 v151, 0xffff0000, v231\n\t"
            "v_mfma_f32_16x16x32_bf16 v[176:179], v[52:55], v[188:191], v[176:179]\n\t"
            "v_lshlrev_b32 v156, 16, v232\n\t"
            "v_and_b32 v157, 0xffff0000, v232\n\t"
            "v_mfma_f32_16x16x32_bf16 v[148:151], v[76:79], v[188:191], v[148:151]\n\t"
            "v_lshlrev_b32 v158, 16, v233\n\t"
            "v_and_b32 v159, 0xffff0000, v233\n\t"
            "v_mfma_f32_16x16x32_bf16 v[148:151], v[72:75], v[184:187], v[148:151]\n\t"
            "v_lshlrev_b32 v180, 16, v234\n\t"
            "v_and_b32 v181, 0xffff0000, v234\n\t"
            "v_mfma_f32_16x16x32_bf16 v[156:159], v[96:99], v[184:187], v[156:159]\n\t"
            "v_lshlrev_b32 v182, 16, v235\n\t"
            "v_and_b32 v183, 0xffff0000, v235\n\t"
            "v_mfma_f32_16x16x32_bf16 v[156:159], v[100:103], v[188:191], v[156:159]\n\t"
            "buffer_store_dwordx4 v[144:147], v197, s[40:43], s45 offen sc1\n\t"
            "v_mfma_f32_16x16x32_bf16 v[180:183], v[124:127], v[188:191], v[180:183]\n\t"
            "ds_read_b128 v[188:191], v243 offset:12544\n\t"
            "v_mfma_f32_16x16x32_bf16 v[180:183], v[120:123], v[184:187], v[180:183]\n\t"
            "ds_read_b128 v[184:187], v242 offset:12544\n\t"
            "buffer_store_dwordx4 v[168:171], v197, s[40:43], s45 offen offset:64 sc1\n\t"
            "s_waitcnt lgkmcnt(2)\n\t"
            "v_mfma_f32_16x16x32_bf16 v[152:155], v[8:11], v[192:195], v[152:155]\n\t"
            "v_mfma_f32_16x16x32_bf16 v[152:155], v[12:15], v[172:175], v[152:155]\n\t"
            "buffer_store_dwordx4 v[164:167], v197, s[40:43], s45 offen offset:128 sc1\n\t"
            "v_mfma_f32_16x16x32_bf16 v[160:163], v[36:39], v[172:175], v[160:163]\n\t"
            "v_mfma_f32_16x16x32_bf16 v[160:163], v[32:35], v[192:195], v[160:163]\n\t"
            "v_mfma_f32_16x16x32_bf16 v[176:179], v[56:59], v[192:195], v[176:179]\n\t"
            "v_mfma_f32_16x16x32_bf16 v[176:179], v[60:63], v[172:175], v[176:179]\n\t"
            "v_mfma_f32_16x16x32_bf16 v[148:151], v[84:87], v[172:175], v[148:151]\n\t"
            "v_mfma_f32_16x16x32_bf16 v[148:151], v[80:83], v[192:195], v[148:151]\n\t"
            "v_mfma_f32_16x16x32_bf16 v[156:159], v[104:107], v[192:195], v[156:159]\n\t"
            "v_mfma_f32_16x16x32_bf16 v[156:159], v[108:111], v[172:175], v[156:159]\n\t"
            "s_waitcnt lgkmcnt(0)\n\t"
            "v_mfma_f32_16x16x32_bf16 v[152:155], v[16:19], v[184:187], v[152:155]\n\t"
            "v_mfma_f32_16x16x32_bf16 v[152:155], v[20:23], v[188:191], v[152:155]\n\t"
            "v_mfma_f32_16x16x32_bf16 v[160:163], v[44:47], v[188:191], v[160:163]\n\t"
            "v_mfma_f32_16x16x32_bf16 v[160:163], v[40:43], v[184:187], v[160:163]\n\t"
            "v_mfma_f32_16x16x32_bf16 v[180:183], v[132:135], v[172:175], v[180:183]\n\t"
            "v_mfma_f32_16x16x32_bf16 v[180:183], v[128:131], v[192:195], v[180:183]\n\t"
            "v_mfma_f32_16x16x32_bf16 v[176:179], v[64:67], v[184:187], v[176:179]\n\t"
            "v_mfma_f32_16x16x32_bf16 v[176:179], v[68:71], v[188:191], v[176:179]\n\t"
            "s_nop 1\n\t"
            "v_cvt_pk_bf16_f32 v152, v152, v153\n\t"
            "v_mfma_f32_16x16x32_bf16 v[148:151], v[92:95], v[188:191], v[148:151]\n\t"
            "v_cvt_pk_bf16_f32 v153, v154, v155\n\t"
            "v_mfma_f32_16x16x32_bf16 v[148:151], v[88:91], v[184:187], v[148:151]\n\t"
            "v_cvt_pk_bf16_f32 v154, v160, v161\n\t"
            "v_cvt_pk_bf16_f32 v155, v162, v163\n\t"
            "v_mfma_f32_16x16x32_bf16 v[156:159], v[112:115], v[184:187], v[156:159]\n\t"
            "v_cvt_pk_bf16_f32 v176, v176, v177\n\t"
            "v_cvt_pk_bf16_f32 v177, v178, v179\n\t"
            "v_mfma_f32_16x16x32_bf16 v[156:159], v[116:119], v[188:191], v[156:159]\n\t"
            "buffer_store_dwordx4 v[152:155], v198, s[40:43], s45 offen sc1\n\t"
            "v_mfma_f32_16x16x32_bf16 v[180:183], v[140:143], v[188:191], v[180:183]\n\t"
            "v_cvt_pk_bf16_f32 v178, v148, v149\n\t"
            "v_cvt_pk_bf16_f32 v179, v150, v151\n\t"
            "v_mfma_f32_16x16x32_bf16 v[180:183], v[136:139], v[184:187], v[180:183]\n\t"
            "buffer_store_dwordx4 v[176:179], v198, s[40:43], s45 offen offset:64 sc1\n\t"
            "s_nop 1\n\t"
            "v_cvt_pk_bf16_f32 v156, v156, v157\n\t"
            "v_cvt_pk_bf16_f32 v157, v158, v159\n\t"
            "s_nop 2\n\t"
            "v_cvt_pk_bf16_f32 v158, v180, v181\n\t"
            "v_cvt_pk_bf16_f32 v159, v182, v183\n\t"
            "buffer_store_dwordx4 v[156:159], v198, s[40:43], s45 offen offset:128 sc1\n\t"
            "s_nop 7\n\t"
            : B_SIO, "+{v247}"(l16_), "+{v228}"(q0), "+{v229}"(q1), "+{v232}"(q2), "+{v233}"(q3), "+{v234}"(q4)
            : "{v242}"(oE_), "{v243}"(oO_), "{v244}"(pR0_), "{v245}"(pR1_), "{v246}"(pR2_), "{v250}"(pXw_), "{v251}"(pSTw_), "{v252}"(aCA_), "{v253}"(aB_), "{v254}"(aA_), "{v255}"(idx_), B_SIN, "{s44}"(ARR_OFF + blockIdx.x * 8), "{s[80:81]}"(mrow), "{s82}"(wave == 6 && cnt == 21 ? REC_OFF + (start - 20 * ((blockIdx.x & 7) * 32 + xi)) * 1280 : 0)
            : "v144", "v145", "v146", "v147", "v148", "v149", "v150", "v151", "v152", "v153", "v154", "v155", "v156", "v157", "v158", "v159", "v160", "v161", "v162", "v163", "v164", "v165", "v166", "v167", "v168", "v169", "v170", "v171", "v172", "v173", "v174", "v175", "v176", "v177", "v178", "v179", "v180", "v181", "v182", "v183", "v184", "v185", "v186", "v187", "v188", "v189", "v190", "v191", "v192", "v193", "v194", "v195", "v196", "v197", "v198", "v199", "v236", "v237", "v248", "v249", "v0", "v1", "v2", "v3", "v4", "v5", "v6", "v7", "v8", "v9", "v10", "v11", "v12", "v13", "v14", "v15", "v16", "v17", "v18", "v19", "v20", "v21", "v22", "v23", "v24", "v25", "v26", "v27", "v28", "v29", "v30", "v31", "v32", "v33", "v34", "v35", "v36", "v37", "v38", "v39", "v40", "v41", "v42", "v43", "v44", "v45", "v46", "v47", "v48", "v49", "v50", "v51", "v52", "v53", "v54", "v55", "v56", "v57", "v58", "v59", "v60", "v61", "v62", "v63", "v64", "v65", "v66", "v67", "v68", "v69", "v70", "v71", "v72", "v73", "v74", "v75", "v76", "v77", "v78", "v79", "v80", "v81", "v82", "v83", "v84", "v85", "v86", "v87", "v88", "v89", "v90", "v91", "v92", "v93", "v94", "v95", "v96", "v97", "v98", "v99", "v100", "v101", "v102", "v103", "v104", "v105", "v106", "v107", "v108", "v109", "v110", "v111", "v112", "v113", "v114", "v115", "v116", "v117", "v118", "v119", "v120", "v121", "v122", "v123", "v124", "v125", "v126", "v127", "v128", "v129", "v130", "v131", "v132", "v133", "v134", "v135", "v136", "v137", "v138", "v139", "v140", "v141", "v142", "v143", "v200", "v201", "v202", "v203", "v204", "v205", "v206", "v207", "v208", "v209", "v210", "v211", "v212", "v213", "v214", "v215", "v216", "v217", "v218", "v219", "v220", "v221", "v222", "v223", "v224", "v225", "v226", "v227", "v230", "v231", "v235", B_SCLOB);
        }
        __builtin_amdgcn_endpgm();
    }
}

extern "C" void kernel_launch(void* const* d_in, const int* in_sizes, int n_in,
                              void* d_out, int out_size, void* d_ws, size_t ws_size,
                              hipStream_t stream) {
    const unsigned short* x = (const unsigned short*)d_in[0];
    const unsigned short* dw = (const unsigned short*)d_in[1];
    const unsigned short* wqkv = (const unsigned short*)d_in[2];
    const unsigned short* wo = (const unsigned short*)d_in[3];
    unsigned short* out = (unsigned short*)d_out;
    u32x4* wf = (u32x4*)d_ws;
    win_attn_kernel<<<GRID, 512, 0, stream>>>(x, dw, wqkv, wo, wf, out);
}
```

```cpp
#include <hip/hip_runtime.h>
#include <hip/hip_bf16.h>
#include <stdint.h>

typedef short bf16x8 __attribute__((ext_vector_type(8)));
typedef float f32x4 __attribute__((ext_vector_type(4)));
typedef float f32x2 __attribute__((ext_vector_type(2)));
typedef unsigned u32x2 __attribute__((ext_vector_type(2)));
typedef unsigned u32x4 __attribute__((ext_vector_type(4)));

constexpr int IMG = 511;
constexpr int CH = 192;
constexpr int NWX = 73;
constexpr int NWIN = NWX * NWX;
constexpr int GRID = 256;
constexpr int ROWBYTES = IMG * CH * 2;
constexpr int XBIAS = (IMG + 1) * CH * 2;
constexpr unsigned OOB = 0x40000000u;

constexpr int FLAG_OFF = 7856;
constexpr int X_OFF = 32768;
constexpr int X1_OFF = 53632;
constexpr int O_OFF = 74496;
constexpr int V_OFF = 92928;
constexpr int KQL_OFF = 111360;
constexpr int OL_OFF = 129024;
constexpr int AT_OFF = 137472;
constexpr int CT_OFF = AT_OFF + 19584;
constexpr int RS_OFF = CT_OFF + 4096;
constexpr int RSX_OFF = 162816;
constexpr int WL_OFF = RS_OFF + 2304 + 64;
constexpr int LDS_BYTES = WL_OFF + 128;
#define RS_BASE (RS_OFF + wave * 384)
constexpr int STL_OFF = O_OFF + 12288;
constexpr int DW_OFF = O_OFF + 8448;
#define LX_OFF(i) ((i) < 2 ? CT_OFF + 1024 + (i) * 384 : KQL_OFF + ((i) - 2) * 800)
constexpr int PF_OFF = CT_OFF + 2048;
constexpr int PSLEEP = 4;
#define LB_OFF (w < 3 ? V_OFF + w * 13952 : X1_OFF)
#define LPOS(wc) (((wc) & 1) ? 0 : 6)
constexpr unsigned CROW = 0x060a0230u;
constexpr unsigned CLANE = 0x00132C3Fu;

constexpr unsigned long long TOKMAP[6] = {0x20001c190f2a2202ull, 0x052517121f061a13ull, 0x181e08151d0b260cull, 0x1b2b2903102d0901ull, 0x04162e0e2824142cull, 0x110a072127232f0dull};
constexpr unsigned long long ROWMAP[8] = {0x08321b240f1c0006ull, 0x23310e2a09170d14ull, 0x210c2f030a273418ull, 0x1d2b11221f043739ull, 0x3234363a103e192dull, 0x291a20022c3f1605ull, 0x303a3c2628122e15ull, 0x39253d1e01070b13ull};

constexpr int WF_BYTES = 8 * 36 * 1024;
constexpr int FLG_OFF = WF_BYTES;
constexpr int ARR_OFF = FLG_OFF + 4096;
constexpr int EPO_OFF = ARR_OFF + 2048;
constexpr int CTR_OFF = EPO_OFF + 128;
constexpr int REC_OFF = WF_BYTES + 8192;
constexpr int WS_BYTES = REC_OFF + 209 * 1280;
constexpr int SC1 = 16;

#define MFMA32(a, b, c) __builtin_amdgcn_mfma_f32_16x16x32_bf16((a), (b), (c), 0, 0, 0)
#define BC8(v) __builtin_bit_cast(bf16x8, (v))
#define BAR() asm volatile("s_waitcnt lgkmcnt(0)\n\ts_barrier" ::: "memory")
#define SB() __builtin_amdgcn_sched_barrier(0)
#define OPQ(x) asm volatile("" : "+v"(x))
#define ZERO4 ((f32x4){0.f, 0.f, 0.f, 0.f})

typedef __bf16 nbf16x2 __attribute__((ext_vector_type(2)));
__device__ __forceinline__ unsigned pk2(float a, float b) {
    f32x2 v = {a, b};
    nbf16x2 r = __builtin_convertvector(v, nbf16x2);
    return __builtin_bit_cast(unsigned, r);
}
__device__ __forceinline__ float bflo(unsigned u) { return __uint_as_float(u << 16); }
__device__ __forceinline__ float bfhi(unsigned u) { return __uint_as_float(u & 0xffff0000u); }

__device__ __forceinline__ float bf2f(unsigned short v) { return __uint_as_float((unsigned)v << 16); }
__device__ __forceinline__ unsigned byte_of(unsigned long long a, unsigned long long b, int i) {
    return (unsigned)(((i & 8) ? b : a) >> (8 * (i & 7))) & 0xFFu;
}
#define M24(a, b) ((unsigned)__mul24((int)(a), (int)(b)))
__device__ __forceinline__ void seq_to_win(int seq, int& wy, int& wx) {
    const bool last = seq >= 5110;
    const int s = last ? 35 : M24(seq, 28729) >> 22, r = seq - M24(146, s);
    const int kx = last ? M24(r, 171) >> 10 : r >> 2, pos = r - M24(last ? 6 : 4, kx);
    const bool low = last && pos >= (kx == 36 ? 2 : 4);
    wy = low ? 72 : 2 * s + (pos & 1);
    wx = 2 * kx + (low ? (kx == 36 ? 0 : pos - 4) : pos >> 1);
}
__device__ __forceinline__ int win_to_seq(int wy, int wx) {
    const int kx = wx >> 1;
    return M24(146, wy == 72 ? 35 : wy >> 1) + M24(wy >= 70 ? 6 : 4, kx) + (wy == 72 ? (kx == 36 ? 2 : 4 + (wx & 1)) : 2 * (wx & 1) + (wy & 1));
}
__device__ __forceinline__ int block_of(int wy, int wx) { return wy >= 70 ? 1295 + (wx & ~1) + (wy == 72) : M24(37, wy >> 1) + (wx >> 1); }
__device__ __forceinline__ void block_pos(int b, int& ky, int& kx) {
    const int s = M24(b, 1772) >> 16, l = b - 1295;
    kx = l >= 0 ? l >> 1 : b - M24(37, s);
    ky = l >= 0 ? 70 + 2 * (l & 1) : 2 * s;
}
__device__ __forceinline__ unsigned win_org(int wy, int wx) { return M24(wy, 7 * ROWBYTES) + M24(wx, 7 * CH * 2); }
__device__ __forceinline__ unsigned wl_entry(int wy, int wx) {
    return win_org(wy, wx) | (wy == 0 ? 0x08000000u : 0u) | (wx == 0 ? 0x10000000u : 0u) |
           (wx == NWX - 1 ? 0x20000000u : 0u) | ((unsigned)(wx & 1) << 30) | ((unsigned)(wy & 1) << 31);
}
__device__ __forceinline__ unsigned dma_off(int lane) {
    const int col = (lane * 43) >> 8;
    return (unsigned)(col * (CH * 2 - 96) + 16 * lane) | (col == 0 ? 0x10000000u : 0u) | (col == 8 ? 0x20000000u : 0u);
}

#if defined(__HIP_DEVICE_COMPILE__)
#define NOPK __attribute__((target("no-packed-fp32-ops")))
#else
#define NOPK
#endif
__global__ __launch_bounds__(512, 2) NOPK void win_attn_kernel(const unsigned short* __restrict__ x,
                                                          const unsigned short* __restrict__ dw, const unsigned short* __restrict__ wqkv,
                                                          const unsigned short* __restrict__ wo, u32x4* wf,
                                                          unsigned short* __restrict__ out) {
    __shared__ __attribute__((aligned(16))) char smem[LDS_BYTES];

    asm volatile("wk_code_begin:");
    const int tid = threadIdx.x;
    const int wave = __builtin_amdgcn_readfirstlane(tid >> 6);
    const int w = wave & 3;
    const unsigned ldsb = (unsigned)(size_t)smem;
    {
        unsigned long long pc;
        unsigned len, rel;
        asm volatile("s_getpc_b64 %0\n.Lwk_here%=:\n\ts_mov_b32 %1, wk_code_end-wk_code_begin\n\ts_mov_b32 %2, .Lwk_here%=-wk_code_begin" : "=s"(pc), "=s"(len), "=s"(rel));
        pc -= rel;
        const u32x4 cr = {(unsigned)pc, (unsigned)(pc >> 32) & 0xffffu, len, 0x00020000u};
        asm volatile("s_mov_b32 m0, %0\n\ts_nop 0\n\tbuffer_load_dwordx4 %1, %2, %3 offen lds"
                     :: "s"(ldsb + X1_OFF + 14336 + w * 1024), "v"((tid & 63) * 16), "s"(cr), "s"((wave * 32 + (blockIdx.x >> 3)) * 1024) : "m0", "memory");
    }
    *(unsigned*)(smem + PF_OFF + tid * 4) = 0x9E3779B9u * (tid + 1);
    *(unsigned*)(smem + (tid < 192 ? AT_OFF + 18816 + tid * 4 : FLAG_OFF + (tid & 3) * 4)) = 0u;
    BAR();
    const unsigned flg = FLG_OFF + (blockIdx.x & 7) * 512;
    asm volatile("" :: "s"(x), "s"(dw), "s"(wqkv), "s"(wo), "s"(wf));
    const auto wrsrc = __builtin_amdgcn_make_buffer_rsrc((void*)wf, 0, WS_BYTES, 0x00020000);
    unsigned tag_lo = 0, tag_hi = 0;
#define TAG_READY                                                                                  \
    {                                                                                              \
        unsigned long long tag_;                                                                   \
        asm volatile("s_load_dwordx2 %0, %1, %2\n\ts_waitcnt lgkmcnt(0)" : "=s"(tag_) : "s"(wf), "n"(EPO_OFF) : "memory"); \
        tag_lo = (unsigned)tag_ + 1u; tag_hi = (unsigned)(tag_ >> 32);                             \
    }
    const int pb = blockIdx.x;
    if (pb < 36) {
        const int lane = tid & 63, n = lane & 15, g = lane >> 4;
        const bool isO = wave >= 6;
        const int fr = isO ? 216 + 2 * pb + (wave - 6) : 6 * pb + wave;
        const int tile = fr / 6, ks = fr - 6 * tile, tw = tile / 6, mdt = tile - 6 * tw;
        const int col = isO ? 96 * (tw - 6) + 32 * (mdt >> 1) + 8 * (n >> 2) + 4 * (mdt & 1) + (n & 3)
                            : (tw < 4 ? (mdt < 3 ? 192 : -48) + 48 * tw : 96 * tw) + 16 * mdt + n;
        const int rb = isO ? 384 : 1152;
        const auto rr = __builtin_amdgcn_make_buffer_rsrc((void*)(isO ? wo : wqkv), 0, 192 * rb, 0x00020000);
        const int vo = (48 * (g >> 1) + 8 * (g & 1)) * rb + 2 * col;
        float v[8];
#pragma unroll
        for (int j = 0; j < 8; ++j) v[j] = bf2f(__builtin_amdgcn_raw_buffer_load_b16(rr, vo, (96 * (ks & 1) + 16 * (ks >> 1) + j) * rb, 0));
        TAG_READY
        float* red = (float*)smem;
        red[n * 36 + wave * 4 + g] = ((v[0] + v[1]) + (v[2] + v[3])) + ((v[4] + v[5]) + (v[6] + v[7]));
        BAR();
        float mean = 0.f;
#pragma unroll
        for (int i = 0; i < 6; ++i) {
            const f32x4 p = *(const f32x4*)(red + n * 36 + 4 * i);
            mean += (p[0] + p[1]) + (p[2] + p[3]);
        }
        mean = isO ? 0.f : mean * (1.f / 192.f);
        u32x4 o;
#pragma unroll
        for (int j = 0; j < 4; ++j) o[j] = pk2(v[2 * j] - mean, v[2 * j + 1] - mean);
        __builtin_amdgcn_raw_buffer_store_b128(o, wrsrc, lane * 16, fr * 1024, SC1);
        if (pb == 0 && tid == 448) __builtin_amdgcn_raw_buffer_store_b32(0u, wrsrc, CTR_OFF, 0, SC1);
        asm volatile("s_waitcnt vmcnt(0)" ::: "memory");
        BAR();
        if (tid < 8) __builtin_amdgcn_raw_buffer_store_b64((u32x2){tag_lo, tag_hi}, wrsrc, FLG_OFF + tid * 512 + pb * 8, 0, SC1);
    }
    const int xi = blockIdx.x >> 3, nshort = (int)(0x0506060606060606ull >> (8 * (blockIdx.x & 7))) & 63;
    const int start = 672 * (blockIdx.x & 7) - ((int)(0x2a241e18120c0600ull >> (8 * (blockIdx.x & 7))) & 63) + 21 * xi - (xi < nshort ? xi : nshort);
    const int cnt = xi < nshort ? 20 : 21;

    const auto xrsrc = __builtin_amdgcn_make_buffer_rsrc((void*)((const char*)x - XBIAS), 0, IMG * IMG * CH * 2 + XBIAS, 0x00020000);
#define POLL_ISSUE                                                                                  \
    asm volatile("s_mov_b32 m0, %0\n\ts_mov_b64 exec, 0x3ffff\n\tbuffer_load_dwordx4 %1, %2, %3 offen sc1 lds\n\ts_mov_b64 exec, -1" \
                 :: "s"(ldsb + PF_OFF), "v"(lane * 16), "s"(wrsrc), "s"(flg) : "m0", "memory");
#define POLL_W                                                                                      \
    {                                                                                               \
        const int fi_ = lane < 36 ? lane : 0;                                                       \
        for (int it_ = 0; it_ < 100000; ++it_) {                                                    \
            u32x2 f_;                                                                               \
            if (it_ < 48) asm volatile("ds_read_b64 %0, %1\n\ts_waitcnt lgkmcnt(0)" : "=v"(f_) : "v"(ldsb + PF_OFF + fi_ * 8) : "memory"); \
            else f_ = __builtin_amdgcn_raw_buffer_load_b64(wrsrc, fi_ * 8, flg, SC1);        \
            if (__builtin_amdgcn_ballot_w64(f_[0] != tag_lo || f_[1] != tag_hi) == 0ull) break;     \
            if (it_ < 47 && wave == 0) POLL_ISSUE                                                              \
            __builtin_amdgcn_s_sleep(PSLEEP);                                                       \
        }                                                                                           \
        *(u32x2*)(smem + PF_OFF + 512) = (u32x2){tag_lo, tag_hi};                                   \
        __builtin_amdgcn_fence(__ATOMIC_ACQUIRE, "wavefront");                                      \
        SB();                                                                                       \
    }
#define LANE_SETUP                                                                                 \
    int lane;                                                                                      \
    asm volatile("v_mbcnt_lo_u32_b32 %0, -1, 0\n\tv_mbcnt_hi_u32_b32 %0, -1, %0" : "=v"(lane));    \
    __builtin_assume(lane >= 0 && lane < 64);                                                      \
    const int g = lane >> 4, l15 = lane & 15, sw = l15 >> 1;                                       \
    int fE = X_OFF + l15 * 384 + ((g ^ sw) << 4);                                                  \
    OPQ(fE);

#define DMA_SLICE(en)                                                                              \
        {                                                                                          \
            const unsigned vo_ = dma_off(lane) & (0x401fffffu | ((en) & 0x38000000u));             \
            const unsigned so_ = ((en) & 0x7ffffffu) + 96 * w;                                     \
            _Pragma("unroll") for (int r = 0; r < 9; ++r) {                                        \
                unsigned m0_ = ldsb + w * 8192 + r * 872;                                          \
                unsigned sr_ = (r == 0 && ((en) & 0x08000000u)) ? OOB : so_ + r * ROWBYTES;        \
                asm volatile("s_mov_b32 m0, %0\n\ts_mov_b32 exec_hi, 0x3fffff\n\tbuffer_load_dwordx4 %1, %2, %3 offen lds\n\ts_mov_b32 exec_hi, -1" \
                             :: "s"(m0_), "v"(vo_), "s"(xrsrc), "s"(sr_) : "m0", "memory");        \
            }                                                                                      \
        }
#define XPAT(r) (((r) < 48 ? (r) : 48) * 384 + (((2 * w) ^ (((r) >> 1) & 7)) << 4))
#define SPAT(r) ((w >> 1) * 1024 + (w & 1) * 8 + (r) * 16)
#define CONV_CONSTS(var) CONV_ROWS(var) CONV_OPS
#define CONV_ROWS(var) \
            const unsigned long long rlo = g == 0 ? ROWMAP[0] : (g == 1 ? ROWMAP[2] : (g == 2 ? ROWMAP[4] : ROWMAP[6])); \
            const unsigned long long rhi = g == 0 ? ROWMAP[1] : (g == 1 ? ROWMAP[3] : (g == 2 ? ROWMAP[5] : ROWMAP[7])); \
            int xrow = byte_of(rlo, rhi, l15); \
            if (lane == (int)((CLANE >> (8 * (var))) & 0xFF)) xrow = 48; \
            if (lane == 63) xrow = (CROW >> (8 * (var))) & 0xFF; \
            const unsigned pXw_ = XPAT(xrow); \
            const unsigned pSTw_ = (w >> 1) * 1024 + (w & 1) * 8 + xrow * 16; \
            const int oy = (0x5320 >> (4 * (l15 >> 2))) & 15, ox = (0x5320 >> (4 * (l15 & 3))) & 15; \
            const unsigned aB_ = ldsb + w * 8192 + (oy + (g >> 1)) * 872 + (ox + 2 * (g & 1)) * 96;
#define CONV_OPS \
            const int dyi = g - (l15 >> 3); \
            const unsigned aA_ = ldsb + AT_OFF + (l15 & 3) * 16 + \
                                 ((dyi < 0 || dyi > 2) ? 18816 : (dyi == 0 ? 0 : (dyi == 1 ? 6208 : 12544)) + ((l15 >> 2) & 1) * 3072 + w * 768); \
            const unsigned idx_ = (l15 & 3) * 0x5555u; \
            const unsigned se = (l15 == 4 * g) ? 0x3F803F80u : 0u; \
            const u32x4 sel4 = {se, se, se, se};

    unsigned en0, en1;
    {
        LANE_SETUP
        const int px = (lane * 43) >> 8;

        int wy, wx;
        seq_to_win(start + (lane < cnt ? lane : cnt - 1), wy, wx);
        const unsigned en = wl_entry(wy, wx);
        en0 = __builtin_amdgcn_readlane(en, 0);
        en1 = __builtin_amdgcn_readlane(en, 1);
        unsigned cB, cX, cS;
        if (wave >= 4) {
            const int bk = block_of(wy, wx);
            const int key0 = __builtin_amdgcn_readlane(bk, 0), nblk = __builtin_amdgcn_readlane(bk, 63) - key0 + 1;
            const int b = l15 < nblk ? l15 : 0;
            int ky, kx;
            block_pos(key0 + b, ky, kx);
            const unsigned org = l15 < nblk ? (win_org(ky, 2 * kx) + (5 * ROWBYTES + 5 * CH * 2 + XBIAS)) | (kx == 36 ? OOB : 0u) : OOB + 0x8000000u;
            const unsigned lo = (px & 3) * (CH * 2) + (lane - 6 * px) * 16, keep = (px & 2) ? ~0u : ~OOB;
#pragma unroll
            for (int p = 0; p < 4; ++p) {
                if (p < 3 || nblk > 6) {
                    const unsigned oa = __builtin_amdgcn_readlane(org, 2 * p), ob = __builtin_amdgcn_readlane(org, 2 * p + 1);
                    const unsigned vo_ = ((lane < 24 ? oa : ob) & keep) + lo;
#pragma unroll
                    for (int r = 0; r < 4; ++r) {
                        unsigned m0_ = ldsb + LB_OFF + (4 * p + r) * 872;
                        asm volatile("s_mov_b32 m0, %0\n\ts_mov_b32 exec_hi, 0xffff\n\tbuffer_load_dwordx4 %1, %2, %3 offen lds\n\ts_mov_b32 exec_hi, -1"
                                     :: "s"(m0_), "v"(vo_), "s"(xrsrc), "s"(96 * w + r * ROWBYTES) : "m0", "memory");
                    }
                }
            }
            SB();
            const int ly = ky + (g >> 1), lx = 2 * kx + (g & 1), li = win_to_seq(ly, lx) - start;
            const bool ok = ly < NWX && lx < NWX && li >= 0 && li < cnt && l15 < nblk;
            const int row = ok ? li : 47;
            cB = ldsb + LB_OFF + (4 * (b >> 1) + (g >> 1)) * 872 + (b & 1) * 384 + (g & 1) * 192;
            cX = XPAT(row);
            cS = SPAT(row) + (unsigned)(STL_OFF - O_OFF - 18816);
            for (int it_ = 0; it_ < 100000; ++it_) {
                unsigned n_;
                asm volatile("ds_read_b32 %0, %1\n\ts_waitcnt lgkmcnt(0)" : "=v"(n_) : "v"(ldsb + FLAG_OFF + 8) : "memory");
                if (__builtin_amdgcn_readfirstlane(n_) >= 4) break;
                __builtin_amdgcn_s_sleep(1);
            }
        } else {
            {
                const auto dr = __builtin_amdgcn_make_buffer_rsrc((void*)dw, 0, 9 * 384, 0x00020000);
                unsigned m0_ = ldsb + DW_OFF + w * 864, vo_ = px * 288 + lane * 16;
                asm volatile("s_mov_b32 m0, %0\n\ts_mov_b32 exec_hi, 0x3fffff\n\tbuffer_load_dwordx4 %1, %2, %3 offen lds\n\ts_mov_b32 exec_hi, -1"
                             :: "s"(m0_), "v"(vo_), "s"(dr), "s"(96 * w) : "m0", "memory");
            }
            DMA_SLICE(en0)
            SB();
            if (wave == 2) {
                u32x4 so;
#pragma unroll
                for (int e = 0; e < 3; ++e) {
                    const int tk = byte_of(TOKMAP[2 * e], TOKMAP[2 * e + 1], l15);
                    const int ty = (tk * 37) >> 8, tx = tk - 7 * ty;
                    so[e] = (unsigned)(ty * ROWBYTES + tx * (CH * 2)) + 16 * g;
                }
                so[3] = dma_off(lane);
                *(u32x4*)(smem + CT_OFF + lane * 16) = so;
            }
            if (wave == 1 && lane < 32) *(unsigned*)(smem + WL_OFF + lane * 4) = lane < 20 ? en : 0x7ffffffu;
            if (wave == 1 && lane == 20) *(unsigned*)(smem + PF_OFF + 520) = en;
            CONV_ROWS(en0 >> 30)
            cB = aB_;
            cX = lane == (int)((CLANE >> (8 * (en0 >> 30))) & 0xFF) ? LX_OFF(0) - X_OFF + 32 * w : pXw_;
            cS = pSTw_;
            TAG_READY
            asm volatile("s_waitcnt vmcnt(9)" ::: "memory");
            if (lane < 48) {
                unsigned short dv[9];
#pragma unroll
                for (int i = 0; i < 9; ++i) dv[i] = *(const unsigned short*)(smem + DW_OFF + w * 864 + i * 96 + lane * 2);
#pragma unroll
                for (int dy = 0; dy < 3; ++dy) {
                    const unsigned a = dv[3 * dy], b2 = dv[3 * dy + 1] | (dy == 1 ? 0x3F800000u : 0u), c = dv[3 * dy + 2];
                    char* p = smem + AT_OFF + (dy == 0 ? 0 : (dy == 1 ? 6208 : 12544)) + w * 768 + lane * 16;
                    *(u32x4*)p = (u32x4){a, b2, c, 0u};
                    *(u32x4*)(p + 3072) = (u32x4){0u, a, b2, c};
                }
            }
            asm volatile("s_mov_b64 exec, 1\n\tds_add_u32 %0, %1\n\ts_mov_b64 exec, -1" :: "v"(ldsb + FLAG_OFF + 8), "v"(1) : "memory");
        }
        CONV_OPS
        asm volatile("s_waitcnt vmcnt(0) lgkmcnt(0)" ::: "memory");
        if (wave == 0) POLL_ISSUE
        asm volatile(
            "s_mov_b32 m0, s73\n\t"
            "s_cmp_eq_u32 s73, 0\n\t"
            "s_nop 7\n\t"
            "ds_read_b128 v[168:171], v254\n\t"
            "ds_read_b64 v[144:145], v253\n\t"
            "ds_read_b64 v[146:147], v253 offset:96\n\t"
            "ds_read_b64 v[148:149], v253 offset:1744\n\t"
            "ds_read_b64 v[150:151], v253 offset:1840\n\t"
            "ds_read_b128 v[172:175], v254 offset:64\n\t"
            "ds_read_b64 v[152:153], v253 offset:8\n\t"
            "ds_read_b64 v[154:155], v253 offset:104\n\t"
            "ds_read_b64 v[156:157], v253 offset:1752\n\t"
            "ds_read_b64 v[158:159], v253 offset:1848\n\t"
            "ds_read_b64 v[160:161], v253 offset:16\n\t"
            "ds_read_b64 v[162:163], v253 offset:112\n\t"
            "ds_read_b64 v[164:165], v253 offset:1760\n\t"
            "ds_read_b64 v[166:167], v253 offset:1856\n\t"
            "v_mov_b64 v[176:177], 0\n\t"
            "v_mov_b64 v[178:179], 0\n\t"
            "v_mov_b64 v[180:181], 0\n\t"
            "v_mov_b64 v[182:183], 0\n\t"
            "v_mov_b64 v[184:185], 0\n\t"
            "v_mov_b64 v[186:187], 0\n\t"
            "v_mov_b64 v[188:189], 0\n\t"
            "v_mov_b64 v[190:191], 0\n\t"
            "v_add_u32 v236, s67, v250\n\t"
            "v_xor_b32 v237, 16, v236\n\t"
            "v_add_u32 v248, s67, v251\n\t"
            "s_waitcnt lgkmcnt(9)\n\t"
            "v_smfmac_f32_16x16x64_bf16 v[176:179], v[168:171], v[144:151], v255\n\t"
            "ds_read_b128 v[168:171], v254 offset:128\n\t"
            "ds_read_b64 v[144:145], v253 offset:24\n\t"
            "s_waitcnt lgkmcnt(6)\n\t"
            "v_smfmac_f32_16x16x64_bf16 v[180:183], v[172:175], v[152:159], v255\n\t"
            "ds_read_b64 v[146:147], v253 offset:120\n\t"
            "ds_read_b64 v[148:149], v253 offset:1768\n\t"
            "ds_read_b64 v[150:151], v253 offset:1864\n\t"
            "ds_read_b128 v[172:175], v254 offset:192\n\t"
            "ds_read_b64 v[152:153], v253 offset:32\n\t"
            "ds_read_b64 v[154:155], v253 offset:128\n\t"
            "ds_read_b64 v[156:157], v253 offset:1776\n\t"
            "ds_read_b64 v[158:159], v253 offset:1872\n\t"
            "v_cvt_pk_bf16_f32 v176, v176, v177\n\t"
            "v_cvt_pk_bf16_f32 v177, v178, v179\n\t"
            "v_cvt_pk_bf16_f32 v178, v180, v181\n\t"
            "v_cvt_pk_bf16_f32 v179, v182, v183\n\t"
            "v_mfma_f32_4x4x4_16b_bf16 v[196:199], v[176:177], v[176:177], 0\n\t"
            "ds_write_b128 v236, v[176:179]\n\t"
            "v_mfma_f32_16x16x32_bf16 v[192:195], v[238:241], v[176:179], 0\n\t"
            "v_mov_b64 v[176:177], 0\n\t"
            "v_mov_b64 v[180:181], 0\n\t"
            "v_mfma_f32_4x4x4_16b_bf16 v[196:199], v[178:179], v[178:179], v[196:199]\n\t"
            "v_mov_b64 v[178:179], 0\n\t"
            "v_mov_b64 v[182:183], 0\n\t"
            "s_waitcnt lgkmcnt(10)\n\t"
            "v_smfmac_f32_16x16x64_bf16 v[184:187], v[168:171], v[160:167], v255\n\t"
            "ds_read_b128 v[168:171], v254 offset:256\n\t"
            "ds_read_b64 v[160:161], v253 offset:40\n\t"
            "ds_read_b64 v[162:163], v253 offset:136\n\t"
            "ds_read_b64 v[164:165], v253 offset:1784\n\t"
            "ds_read_b64 v[166:167], v253 offset:1880\n\t"
            "s_waitcnt lgkmcnt(10)\n\t"
            "v_smfmac_f32_16x16x64_bf16 v[188:191], v[172:175], v[144:151], v255\n\t"
            "ds_read_b128 v[172:175], v254 offset:320\n\t"
            "ds_read_b64 v[144:145], v253 offset:48\n\t"
            "ds_read_b64 v[146:147], v253 offset:144\n\t"
            "s_cbranch_scc1 .Lwk_np1%=\n\t"
            "s_mov_b64 exec, 0x3ffff\n\t"
            "buffer_load_dwordx4 v252, s[68:71], s72 offen sc1 lds\n\t"
            "s_mov_b64 exec, -1\n\t"
            ".Lwk_np1%=:\n\t"
            "ds_read_b64 v[148:149], v253 offset:1792\n\t"
            "ds_read_b64 v[150:151], v253 offset:1888\n\t"
            "v_cvt_pk_bf16_f32 v184, v184, v185\n\t"
            "v_cvt_pk_bf16_f32 v185, v186, v187\n\t"
            "s_nop 0\n\t"
            "v_cvt_pk_bf16_f32 v186, v188, v189\n\t"
            "v_cvt_pk_bf16_f32 v187, v190, v191\n\t"
            "v_mfma_f32_4x4x4_16b_bf16 v[196:199], v[184:185], v[184:185], v[196:199]\n\t"
            "s_waitcnt lgkmcnt(14)\n\t"
            "ds_write_b128 v237, v[184:187]\n\t"
            "v_mfma_f32_16x16x32_bf16 v[192:195], v[238:241], v[184:187], v[192:195]\n\t"
            "v_mov_b64 v[184:185], 0\n\t"
            "v_mov_b64 v[188:189], 0\n\t"
            "v_mfma_f32_4x4x4_16b_bf16 v[196:199], v[186:187], v[186:187], v[196:199]\n\t"
            "v_mov_b64 v[186:187], 0\n\t"
            "v_mov_b64 v[190:191], 0\n\t"
            "s_waitcnt lgkmcnt(10)\n\t"
            "v_smfmac_f32_16x16x64_bf16 v[176:179], v[168:171], v[152:159], v255\n\t"
            "ds_read_b128 v[168:171], v254 offset:384\n\t"
            "ds_read_b64 v[152:153], v253 offset:56\n\t"
            "ds_read_b64 v[154:155], v253 offset:152\n\t"
            "ds_read_b64 v[156:157], v253 offset:1800\n\t"
            "ds_read_b64 v[158:159], v253 offset:1896\n\t"
            "s_waitcnt lgkmcnt(10)\n\t"
            "v_smfmac_f32_16x16x64_bf16 v[180:183], v[172:175], v[160:167], v255\n\t"
            "ds_read_b128 v[172:175], v254 offset:448\n\t"
            "ds_read_b64 v[160:161], v253 offset:64\n\t"
            "ds_read_b64 v[162:163], v253 offset:160\n\t"
            "ds_read_b64 v[164:165], v253 offset:1808\n\t"
            "ds_read_b64 v[166:167], v253 offset:1904\n\t"
            "v_cvt_pk_bf16_f32 v176, v176, v177\n\t"
            "v_cvt_pk_bf16_f32 v177, v178, v179\n\t"
            "s_nop 0\n\t"
            "v_cvt_pk_bf16_f32 v178, v180, v181\n\t"
            "v_cvt_pk_bf16_f32 v179, v182, v183\n\t"
            "v_mfma_f32_4x4x4_16b_bf16 v[196:199], v[176:177], v[176:177], v[196:199]\n\t"
            "s_waitcnt lgkmcnt(14)\n\t"
            "ds_write_b128 v236, v[176:179] offset:128\n\t"
            "v_mfma_f32_16x16x32_bf16 v[192:195], v[238:241], v[176:179], v[192:195]\n\t"
            "v_mov_b64 v[176:177], 0\n\t"
            "v_mov_b64 v[180:181], 0\n\t"
            "v_mfma_f32_4x4x4_16b_bf16 v[196:199], v[178:179], v[178:179], v[196:199]\n\t"
            "v_mov_b64 v[178:179], 0\n\t"
            "v_mov_b64 v[182:183], 0\n\t"
            "s_waitcnt lgkmcnt(10)\n\t"
            "v_smfmac_f32_16x16x64_bf16 v[184:187], v[168:171], v[144:151], v255\n\t"
            "ds_read_b128 v[168:171], v254 offset:512\n\t"
            "ds_read_b64 v[144:145], v253 offset:72\n\t"
            "ds_read_b64 v[146:147], v253 offset:168\n\t"
            "ds_read_b64 v[148:149], v253 offset:1816\n\t"
            "ds_read_b64 v[150:151], v253 offset:1912\n\t"
            "s_waitcnt lgkmcnt(10)\n\t"
            "v_smfmac_f32_16x16x64_bf16 v[188:191], v[172:175], v[152:159], v255\n\t"
            "ds_read_b128 v[172:175], v254 offset:576\n\t"
            "ds_read_b64 v[152:153], v253 offset:80\n\t"
            "ds_read_b64 v[154:155], v253 offset:176\n\t"
            "ds_read_b64 v[156:157], v253 offset:1824\n\t"
            "ds_read_b64 v[158:159], v253 offset:1920\n\t"
            "v_cvt_pk_bf16_f32 v184, v184, v185\n\t"
            "v_cvt_pk_bf16_f32 v185, v186, v187\n\t"
            "s_cbranch_scc1 .Lwk_np0%=\n\t"
            "s_mov_b64 exec, 0x3ffff\n\t"
            "buffer_load_dwordx4 v252, s[68:71], s72 offen sc1 lds\n\t"
            "s_mov_b64 exec, -1\n\t"
            ".Lwk_np0%=:\n\t"
            "s_nop 0\n\t"
            "v_cvt_pk_bf16_f32 v186, v188, v189\n\t"
            "v_cvt_pk_bf16_f32 v187, v190, v191\n\t"
            "v_mfma_f32_4x4x4_16b_bf16 v[196:199], v[184:185], v[184:185], v[196:199]\n\t"
            "s_waitcnt lgkmcnt(14)\n\t"
            "ds_write_b128 v237, v[184:187] offset:128\n\t"
            "v_mfma_f32_16x16x32_bf16 v[192:195], v[238:241], v[184:187], v[192:195]\n\t"
            "v_mov_b64 v[184:185], 0\n\t"
            "v_mov_b64 v[188:189], 0\n\t"
            "v_mfma_f32_4x4x4_16b_bf16 v[196:199], v[186:187], v[186:187], v[196:199]\n\t"
            "v_mov_b64 v[186:187], 0\n\t"
            "v_mov_b64 v[190:191], 0\n\t"
            "s_waitcnt lgkmcnt(10)\n\t"
            "v_smfmac_f32_16x16x64_bf16 v[176:179], v[168:171], v[160:167], v255\n\t"
            "ds_read_b128 v[168:171], v254 offset:640\n\t"
            "ds_read_b64 v[160:161], v253 offset:88\n\t"
            "ds_read_b64 v[162:163], v253 offset:184\n\t"
            "ds_read_b64 v[164:165], v253 offset:1832\n\t"
            "ds_read_b64 v[166:167], v253 offset:1928\n\t"
            "s_waitcnt lgkmcnt(10)\n\t"
            "v_smfmac_f32_16x16x64_bf16 v[180:183], v[172:175], v[144:151], v255\n\t"
            "ds_read_b128 v[172:175], v254 offset:704\n\t"
            "v_cvt_pk_bf16_f32 v176, v176, v177\n\t"
            "v_cvt_pk_bf16_f32 v177, v178, v179\n\t"
            "s_nop 1\n\t"
            "v_mfma_f32_4x4x4_16b_bf16 v[196:199], v[176:177], v[176:177], v[196:199]\n\t"
            "s_nop 1\n\t"
            "v_cvt_pk_bf16_f32 v178, v180, v181\n\t"
            "v_cvt_pk_bf16_f32 v179, v182, v183\n\t"
            "s_nop 1\n\t"
            "v_mfma_f32_4x4x4_16b_bf16 v[196:199], v[178:179], v[178:179], v[196:199]\n\t"
            "ds_write_b128 v236, v[176:179] offset:256\n\t"
            "v_mfma_f32_16x16x32_bf16 v[192:195], v[238:241], v[176:179], v[192:195]\n\t"
            "s_waitcnt lgkmcnt(6)\n\t"
            "v_smfmac_f32_16x16x64_bf16 v[184:187], v[168:171], v[152:159], v255\n\t"
            "s_waitcnt lgkmcnt(1)\n\t"
            "v_smfmac_f32_16x16x64_bf16 v[188:191], v[172:175], v[160:167], v255\n\t"
            "s_nop 5\n\t"
            "v_cvt_pk_bf16_f32 v184, v184, v185\n\t"
            "v_cvt_pk_bf16_f32 v185, v186, v187\n\t"
            "s_nop 1\n\t"
            "v_mfma_f32_4x4x4_16b_bf16 v[196:199], v[184:185], v[184:185], v[196:199]\n\t"
            "v_cvt_pk_bf16_f32 v186, v188, v189\n\t"
            "v_cvt_pk_bf16_f32 v187, v190, v191\n\t"
            "s_nop 1\n\t"
            "v_mfma_f32_4x4x4_16b_bf16 v[196:199], v[186:187], v[186:187], v[196:199]\n\t"
            "v_mfma_f32_16x16x32_bf16 v[192:195], v[238:241], v[184:187], v[192:195]\n\t"
            "ds_write_b128 v237, v[184:187] offset:256\n\t"
            "s_nop 6\n\t"
            "v_cndmask_b32_e64 v193, v196, v197, s[60:61]\n\t"
            "v_cndmask_b32_e64 v193, v193, v198, s[62:63]\n\t"
            "v_cndmask_b32_e64 v193, v193, v199, s[64:65]\n\t"
            "ds_write_b64 v248, v[192:193] offset:18816\n\t"
            "s_waitcnt lgkmcnt(0)\n\t"
            "s_nop 7\n\t"
            :
            : "{v250}"(cX), "{v251}"(cS), "{v253}"(cB), "{v254}"(aA_), "{v255}"(idx_), "{v[238:241]}"(sel4), "{s67}"(ldsb + (wave < 4 ? X_OFF : O_OFF)),
              "{v252}"(lane * 16), "{s[68:71]}"(wrsrc), "{s72}"(flg), "{s73}"(wave == 0 ? ldsb + PF_OFF : 0u),
              "{s[60:61]}"(0x2222222222222222ull), "{s[62:63]}"(0x4444444444444444ull), "{s[64:65]}"(0x8888888888888888ull)
            : "v144", "v145", "v146", "v147", "v148", "v149", "v150", "v151", "v152", "v153", "v154", "v155", "v156", "v157", "v158", "v159", "v160", "v161", "v162", "v163", "v164", "v165", "v166", "v167", "v168", "v169", "v170", "v171", "v172", "v173", "v174", "v175", "v176", "v177", "v178", "v179", "v180", "v181", "v182", "v183", "v184", "v185", "v186", "v187", "v188", "v189", "v190", "v191", "v192", "v193", "v194", "v195", "v196", "v197", "v198", "v199", "v236", "v237", "v248", "v249", "m0", "scc", "memory");
        if (wave < 4) {
            POLL_W
        }
    }
    const unsigned long long mrow = 0x0001000100010001ull * ((1u << (cnt - 16)) - 1u);
#define KARGS                                                                                      \
    unsigned long long xb, wb, ob;                                                                 \
    asm volatile("s_load_dwordx2 %0, %3, 0x0\n\ts_load_dwordx2 %1, %3, 0x20\n\ts_load_dwordx2 %2, %3, 0x28\n\ts_waitcnt lgkmcnt(0)" \
                 : "=&s"(xb), "=&s"(wb), "=&s"(ob) : "s"(__builtin_amdgcn_kernarg_segment_ptr()) : "memory"); \
    xb -= XBIAS;                                                                                   \
    const u32x4 wr = {(unsigned)wb, (unsigned)(wb >> 32) & 0xffffu, (unsigned)WS_BYTES, 0x00020000u}; \
    const u32x4 orr = {(unsigned)ob, (unsigned)(ob >> 32) & 0xffffu, (unsigned)(IMG * IMG * CH * 2), 0x00020000u}; \
    const u32x4 xr = {(unsigned)xb, (unsigned)(xb >> 32) & 0xffffu, (unsigned)(IMG * IMG * CH * 2 + XBIAS), 0x00020000u};
    const int tk_ = (xi + 4) / 6;
    const unsigned touch = wave * 36864 + ((xi + 4 - 6 * tk_) * 6 + tk_) * 1024;
#define B_SCALARS \
            CONV_CONSTS(0) \
            const unsigned aCA_ = ldsb + CT_OFF + lane * 16; \
            KARGS \
            unsigned s45 = OOB, s46 = (en0 & 0x7ffffffu) + 96 * w, s47 = (en1 & 0x7ffffffu) + 96 * w; \
            unsigned s48 = ldsb + X_OFF, s67 = ldsb + X1_OFF, s76 = ldsb + WL_OFF + 8, s70 = 0; \
            unsigned s56 = __builtin_amdgcn_readfirstlane((en0 >> 30) * 4 + (en1 >> 30)); \
            unsigned s69 = 0x401fffffu | (en1 & 0x38000000u), s79 = (en1 & 0x08000000u) ? OOB : s47, s59 = ldsb + LX_OFF(1) + 32 * w; \
            unsigned long long wbase = wb + 6 * 36864; \
            unsigned wlen = 2 * 36864; \
            if (wave >= 6) { \
                unsigned rel; \
                asm volatile("s_getpc_b64 %0\n.Lwk_h2%=:\n\ts_mov_b32 %1, wk_code_end-wk_code_begin\n\ts_mov_b32 %2, .Lwk_h2%=-wk_code_begin" : "=s"(wbase), "=s"(wlen), "=s"(rel)); \
                wbase -= rel; \
            } \
            const unsigned wo_ = xi * 3456 + 96 + 96 * (w & 1); \
            wbase = (wbase + wo_) & 0xffffffffffffull; \
            wlen = __builtin_amdgcn_readfirstlane(wlen > wo_ ? wlen - wo_ : 0u); \
            unsigned long long xp = 0, sp = 0; \
            _Pragma("unroll") for (int v = 0; v < 4; ++v) { \
                const unsigned r = (CROW >> (8 * v)) & 0xFF; \
                xp |= (unsigned long long)XPAT(r) << (16 * v); \
                sp |= (unsigned long long)SPAT(r) << (16 * v); \
            }
#define B_SIO "+{s45}"(s45), "+{s46}"(s46), "+{s47}"(s47), "+{s48}"(s48), "+{s67}"(s67), "+{s76}"(s76), "+{s56}"(s56), "+{s70}"(s70), "+{s69}"(s69), "+{s79}"(s79), "+{s59}"(s59)
#define B_SIN "{v[238:241]}"(sel4), "{s[40:43]}"(orr), "{s[72:75]}"(xr), "{s39}"(0x401fffffu), "{s[28:31]}"(wr), "{s33}"(wave * 36864), "{s78}"(96 * w), \
              "{s27}"(96 * w - 384), "{s[50:51]}"(xp), "{s[52:53]}"(sp), "{s54}"(CLANE), "{s55}"(CROW & 0x0F0F0F0Fu), "{s[34:35]}"(0x0001000100010001ull), \
              "{s[60:61]}"(0x2222222222222222ull), "{s[62:63]}"(0x4444444444444444ull), "{s[64:65]}"(0x8888888888888888ull), \
              "{s66}"(ldsb + w * 8192), "{s71}"((ldsb + X_OFF) ^ (ldsb + X1_OFF)), "{s[86:87]}"(wbase), "{s85}"(wlen)
#define B_SCLOB "s83", "s84", "s88", "s89", "s90", "s91", "s26", "s77", "s68", "s36", "s37", "s38", "s57", "s58", "s49", "vcc", "scc", "m0", "memory"

    if (wave < 4) {
        LANE_SETUP
        const int dW = X_OFF + l15 * 384 + (((2 * w + (g >> 1)) ^ sw) << 4) + 8 * (g & 1);
        const int vB = V_OFF + w * 4608 + 1024 + lane * 8, rsR = RS_BASE + l15 * 4;
        {
            const unsigned rsb = ldsb + RS_BASE;
            const unsigned pE_ = fE - X_OFF, aO_ = ldsb + dW + (O_OFF - X_OFF), sel_ = (l15 & 3) ? 0u : 1u, aVB_ = ldsb + vB;
            const unsigned pST_ = lane * 16, aPLw_ = rsb + lane * 2, aRSr_ = ldsb + rsR;
            unsigned aRSw_ = rsb + lane * 4;
            const unsigned g16_ = (l15 & 3) ? ldsb + AT_OFF + 18816 : g * 16, g8_ = (l15 & 3) ? ldsb + AT_OFF + 18816 : g * 8;
            const unsigned loVL_ = ((2 * w + (l15 >> 3)) << 4) + (l15 & 7) * 2, loOL_ = g * 128;
            const unsigned on = (l15 & 3) ? 0u : 0x3F803F80u;
            const u32x4 ones4 = {on, on, on, on};
            unsigned s48 = ldsb + X_OFF, s70 = 0;
            unsigned s72 = ldsb + KQL_OFF + w * 192, s73 = ldsb + OL_OFF, s74 = ldsb + KQL_OFF + 784, s77 = ldsb + OL_OFF, s76 = ldsb + KQL_OFF + 768 + w * 4;
            KARGS
            asm volatile(
            "s_nop 4\n\t"
            "buffer_load_dwordx4 v[140:143], v248, s[84:87], s88 offen sc1\n\t"
            "s_add_u32 s49, s46, 0\n\t"
            "buffer_load_dwordx4 v[0:3], v248, s[84:87], s49 offen sc1\n\t"
            "s_add_u32 s49, s46, 6144\n\t"
            "buffer_load_dwordx4 v[24:27], v248, s[84:87], s49 offen sc1\n\t"
            "s_add_u32 s49, s46, 12288\n\t"
            "buffer_load_dwordx4 v[48:51], v248, s[84:87], s49 offen sc1\n\t"
            "s_add_u32 s49, s46, 18432\n\t"
            "buffer_load_dwordx4 v[72:75], v248, s[84:87], s49 offen sc1\n\t"
            "s_add_u32 s49, s46, 24576\n\t"
            "buffer_load_dwordx4 v[96:99], v248, s[84:87], s49 offen sc1\n\t"
            "s_add_u32 s49, s46, 30720\n\t"
            "buffer_load_dwordx4 v[120:123], v248, s[84:87], s49 offen sc1\n\t"
            "s_add_u32 s49, s46, 1024\n\t"
            "buffer_load_dwordx4 v[4:7], v248, s[84:87], s49 offen sc1\n\t"
            "s_add_u32 s49, s46, 7168\n\t"
            "buffer_load_dwordx4 v[28:31], v248, s[84:87], s49 offen sc1\n\t"
            "s_add_u32 s49, s46, 13312\n\t"
            "buffer_load_dwordx4 v[52:55], v248, s[84:87], s49 offen sc1\n\t"
            "s_add_u32 s49, s46, 19456\n\t"
            "buffer_load_dwordx4 v[76:79], v248, s[84:87], s49 offen sc1\n\t"
            "s_add_u32 s49, s46, 25600\n\t"
            "buffer_load_dwordx4 v[100:103], v248, s[84:87], s49 offen sc1\n\t"
            "s_add_u32 s49, s46, 31744\n\t"
            "buffer_load_dwordx4 v[124:127], v248, s[84:87], s49 offen sc1\n\t"
            "s_add_u32 s49, s46, 2048\n\t"
            "buffer_load_dwordx4 v[8:11], v248, s[84:87], s49 offen sc1\n\t"
            "s_add_u32 s49, s46, 8192\n\t"
            "buffer_load_dwordx4 v[32:35], v248, s[84:87], s49 offen sc1\n\t"
            "s_add_u32 s49, s46, 14336\n\t"
            "buffer_load_dwordx4 v[56:59], v248, s[84:87], s49 offen sc1\n\t"
            "s_add_u32 s49, s46, 20480\n\t"
            "buffer_load_dwordx4 v[80:83], v248, s[84:87], s49 offen sc1\n\t"
            "s_add_u32 s49, s46, 26624\n\t"
            "buffer_load_dwordx4 v[104:107], v248, s[84:87], s49 offen sc1\n\t"
            "s_add_u32 s49, s46, 32768\n\t"
            "buffer_load_dwordx4 v[128:131], v248, s[84:87], s49 offen sc1\n\t"
            "s_add_u32 s49, s46, 3072\n\t"
            "buffer_load_dwordx4 v[12:15], v248, s[84:87], s49 offen sc1\n\t"
            "s_add_u32 s49, s46, 9216\n\t"
            "buffer_load_dwordx4 v[36:39], v248, s[84:87], s49 offen sc1\n\t"
            "s_add_u32 s49, s46, 15360\n\t"
            "buffer_load_dwordx4 v[60:63], v248, s[84:87], s49 offen sc1\n\t"
            "s_add_u32 s49, s46, 21504\n\t"
            "buffer_load_dwordx4 v[84:87], v248, s[84:87], s49 offen sc1\n\t"
            "s_add_u32 s49, s46, 27648\n\t"
            "buffer_load_dwordx4 v[108:111], v248, s[84:87], s49 offen sc1\n\t"
            "s_add_u32 s49, s46, 33792\n\t"
            "buffer_load_dwordx4 v[132:135], v248, s[84:87], s49 offen sc1\n\t"
            "s_add_u32 s49, s46, 4096\n\t"
            "buffer_load_dwordx4 v[16:19], v248, s[84:87], s49 offen sc1\n\t"
            "s_add_u32 s49, s46, 10240\n\t"
            "buffer_load_dwordx4 v[40:43], v248, s[84:87], s49 offen sc1\n\t"
            "s_add_u32 s49, s46, 16384\n\t"
            "buffer_load_dwordx4 v[64:67], v248, s[84:87], s49 offen sc1\n\t"
            "s_add_u32 s49, s46, 22528\n\t"
            "buffer_load_dwordx4 v[88:91], v248, s[84:87], s49 offen sc1\n\t"
            "s_add_u32 s49, s46, 28672\n\t"
            "buffer_load_dwordx4 v[112:115], v248, s[84:87], s49 offen sc1\n\t"
            "s_add_u32 s49, s46, 34816\n\t"
            "buffer_load_dwordx4 v[136:139], v248, s[84:87], s49 offen sc1\n\t"
            "s_add_u32 s49, s46, 5120\n\t"
            "buffer_load_dwordx4 v[20:23], v248, s[84:87], s49 offen sc1\n\t"
            "s_add_u32 s49, s46, 11264\n\t"
            "buffer_load_dwordx4 v[44:47], v248, s[84:87], s49 offen sc1\n\t"
            "s_add_u32 s49, s46, 17408\n\t"
            "buffer_load_dwordx4 v[68:71], v248, s[84:87], s49 offen sc1\n\t"
            "s_add_u32 s49, s46, 23552\n\t"
            "buffer_load_dwordx4 v[92:95], v248, s[84:87], s49 offen sc1\n\t"
            "s_add_u32 s49, s46, 29696\n\t"
            "buffer_load_dwordx4 v[116:119], v248, s[84:87], s49 offen sc1\n\t"
            "s_add_u32 s49, s46, 35840\n\t"
            "buffer_load_dwordx4 v[140:143], v248, s[84:87], s49 offen sc1\n\t"
            "s_waitcnt lgkmcnt(0)\n\t"
            "s_barrier\n\t"
            "s_setprio 2\n\t"
            "v_add_u32 v235, s71, v248\n\t"
            "v_add_u32 v232, s71, v244\n\t"
            "v_xor_b32 v233, 64, v232\n\t"
            "v_subrev_u32 v239, s75, v251\n\t"
            "v_mul_u32_u24 v239, 200, v239\n\t"
            "v_lshrrev_b32 v234, 3, v255\n\t"
            "v_add3_u32 v234, v234, v239, s72\n\t"
            "ds_read_b128 v[228:231], v235 offset:12288\n\t"
            "ds_read_b128 v[144:147], v232\n\t"
            "ds_read_b128 v[148:151], v233\n\t"
            "ds_read_b128 v[204:207], v232 offset:6144\n\t"
            "ds_read_b128 v[208:211], v233 offset:6144\n\t"
            "ds_read_b128 v[152:155], v232 offset:128\n\t"
            "ds_read_b128 v[156:159], v233 offset:128\n\t"
            "ds_read_b128 v[212:215], v232 offset:6272\n\t"
            "ds_read_b128 v[224:227], v233 offset:6272\n\t"
            "ds_read_b128 v[160:163], v232 offset:256\n\t"
            "ds_read_b128 v[164:167], v233 offset:256\n\t"
            "s_waitcnt vmcnt(24)\n\t"
            "s_waitcnt lgkmcnt(10)\n\t"
            "v_add_f32 v236, v228, v230\n\t"
            "v_add_f32 v237, v229, v231\n\t"
            "s_waitcnt lgkmcnt(8)\n\t"
            "v_mfma_f32_16x16x32_bf16 v[168:171], v[0:3], v[144:147], 0\n\t"
            "ds_read_b128 v[228:231], v235 offset:13312\n\t"
            "v_lshrrev_b32 v235, 4, v255\n\t"
            "v_mfma_f32_16x16x32_bf16 v[168:171], v[4:7], v[148:151], v[168:171]\n\t"
            "v_add3_u32 v235, v235, v239, s72\n\t"
            "v_mfma_f32_16x16x32_bf16 v[176:179], v[48:51], v[144:147], 0\n\t"
            "v_mfma_f32_16x16x32_bf16 v[176:179], v[52:55], v[148:151], v[176:179]\n\t"
            "v_mfma_f32_16x16x32_bf16 v[172:175], v[24:27], v[144:147], 0\n\t"
            "v_mfma_f32_16x16x32_bf16 v[172:175], v[28:31], v[148:151], v[172:175]\n\t"
            "v_mfma_f32_16x16x32_bf16 v[180:183], v[72:75], v[144:147], 0\n\t"
            "v_mfma_f32_16x16x32_bf16 v[180:183], v[76:79], v[148:151], v[180:183]\n\t"
            "v_mfma_f32_16x16x32_bf16 v[184:187], v[96:99], v[144:147], 0\n\t"
            "v_mfma_f32_16x16x32_bf16 v[184:187], v[100:103], v[148:151], v[184:187]\n\t"
            "v_mfma_f32_16x16x32_bf16 v[188:191], v[120:123], v[144:147], 0\n\t"
            "s_waitcnt lgkmcnt(0)\n\t"
            "v_add_f32 v228, v228, v230\n\t"
            "v_add_f32 v236, v236, v228\n\t"
            "v_mfma_f32_16x16x32_bf16 v[188:191], v[124:127], v[148:151], v[188:191]\n\t"
            "v_add_f32 v229, v229, v231\n\t"
            "v_mul_f32 v236, 0x3baaaaab, v236\n\t"
            "v_mfma_f32_16x16x32_bf16 v[192:195], v[0:3], v[204:207], 0\n\t"
            "v_add_f32 v237, v237, v229\n\t"
            "v_mul_f32_e64 v236, v236, -v236\n\t"
            "v_mfma_f32_16x16x32_bf16 v[192:195], v[4:7], v[208:211], v[192:195]\n\t"
            "v_fmac_f32 v236, 0x3baaaaab, v237\n\t"
            "v_max_f32 v236, 0, v236\n\t"
            "v_mfma_f32_16x16x32_bf16 v[196:199], v[24:27], v[204:207], 0\n\t"
            "v_add_f32 v236, 0x3727c5ac, v236\n\t"
            "v_rsq_f32 v236, v236\n\t"
            "v_mfma_f32_16x16x32_bf16 v[196:199], v[28:31], v[208:211], v[196:199]\n\t"
            "ds_write_b32 v249, v236\n\t"
            "v_mfma_f32_16x16x32_bf16 v[200:203], v[48:51], v[204:207], 0\n\t"
            "ds_read_b32 v236, v251\n\t"
            "ds_read_b32 v237, v251 offset:64\n\t"
            "v_mfma_f32_16x16x32_bf16 v[200:203], v[52:55], v[208:211], v[200:203]\n\t"
            "v_mfma_f32_16x16x32_bf16 v[216:219], v[72:75], v[204:207], 0\n\t"
            "v_mfma_f32_16x16x32_bf16 v[216:219], v[76:79], v[208:211], v[216:219]\n\t"
            "v_mfma_f32_16x16x32_bf16 v[220:223], v[96:99], v[204:207], 0\n\t"
            "v_mfma_f32_16x16x32_bf16 v[220:223], v[100:103], v[208:211], v[220:223]\n\t"
            "v_mfma_f32_16x16x32_bf16 v[228:231], v[120:123], v[204:207], 0\n\t"
            "ds_read_b128 v[204:207], v232 offset:6400\n\t"
            "v_add_u32 v232, s48, v244\n\t"
            "v_mfma_f32_16x16x32_bf16 v[228:231], v[124:127], v[208:211], v[228:231]\n\t"
            "ds_read_b128 v[208:211], v233 offset:6400\n\t"
            "s_waitcnt vmcnt(12)\n\t"
            "v_mfma_f32_16x16x32_bf16 v[168:171], v[8:11], v[152:155], v[168:171]\n\t"
            "v_xor_b32 v233, 64, v232\n\t"
            "v_mfma_f32_16x16x32_bf16 v[168:171], v[12:15], v[156:159], v[168:171]\n\t"
            "v_mfma_f32_16x16x32_bf16 v[176:179], v[56:59], v[152:155], v[176:179]\n\t"
            "v_mfma_f32_16x16x32_bf16 v[176:179], v[60:63], v[156:159], v[176:179]\n\t"
            "v_mfma_f32_16x16x32_bf16 v[172:175], v[32:35], v[152:155], v[172:175]\n\t"
            "v_mfma_f32_16x16x32_bf16 v[172:175], v[36:39], v[156:159], v[172:175]\n\t"
            "v_mfma_f32_16x16x32_bf16 v[180:183], v[80:83], v[152:155], v[180:183]\n\t"
            "v_mfma_f32_16x16x32_bf16 v[180:183], v[84:87], v[156:159], v[180:183]\n\t"
            "v_mfma_f32_16x16x32_bf16 v[184:187], v[104:107], v[152:155], v[184:187]\n\t"
            "v_mfma_f32_16x16x32_bf16 v[184:187], v[108:111], v[156:159], v[184:187]\n\t"
            "v_mfma_f32_16x16x32_bf16 v[188:191], v[128:131], v[152:155], v[188:191]\n\t"
            "v_mfma_f32_16x16x32_bf16 v[188:191], v[132:135], v[156:159], v[188:191]\n\t"
            "v_mfma_f32_16x16x32_bf16 v[192:195], v[8:11], v[212:215], v[192:195]\n\t"
            "v_mfma_f32_16x16x32_bf16 v[192:195], v[12:15], v[224:227], v[192:195]\n\t"
            "v_mfma_f32_16x16x32_bf16 v[196:199], v[32:35], v[212:215], v[196:199]\n\t"
            "v_mfma_f32_16x16x32_bf16 v[196:199], v[36:39], v[224:227], v[196:199]\n\t"
            "v_mfma_f32_16x16x32_bf16 v[200:203], v[56:59], v[212:215], v[200:203]\n\t"
            "v_mfma_f32_16x16x32_bf16 v[200:203], v[60:63], v[224:227], v[200:203]\n\t"
            "v_mfma_f32_16x16x32_bf16 v[216:219], v[80:83], v[212:215], v[216:219]\n\t"
            "v_mfma_f32_16x16x32_bf16 v[216:219], v[84:87], v[224:227], v[216:219]\n\t"
            "v_mfma_f32_16x16x32_bf16 v[220:223], v[104:107], v[212:215], v[220:223]\n\t"
            "v_mfma_f32_16x16x32_bf16 v[220:223], v[108:111], v[224:227], v[220:223]\n\t"
            "v_mfma_f32_16x16x32_bf16 v[228:231], v[128:131], v[212:215], v[228:231]\n\t"
            "ds_read_b128 v[212:215], v232 offset:256\n\t"
            "v_mfma_f32_16x16x32_bf16 v[228:231], v[132:135], v[224:227], v[228:231]\n\t"
            "ds_read_b128 v[224:227], v233 offset:256\n\t"
            "s_waitcnt vmcnt(0)\n\t"
            "v_mfma_f32_16x16x32_bf16 v[168:171], v[16:19], v[160:163], v[168:171]\n\t"
            "v_mfma_f32_16x16x32_bf16 v[168:171], v[20:23], v[164:167], v[168:171]\n\t"
            "v_mfma_f32_16x16x32_bf16 v[176:179], v[64:67], v[160:163], v[176:179]\n\t"
            "v_mfma_f32_16x16x32_bf16 v[176:179], v[68:71], v[164:167], v[176:179]\n\t"
            "v_mfma_f32_16x16x32_bf16 v[180:183], v[88:91], v[160:163], v[180:183]\n\t"
            "v_mfma_f32_16x16x32_bf16 v[180:183], v[92:95], v[164:167], v[180:183]\n\t"
            "v_mfma_f32_16x16x32_bf16 v[172:175], v[40:43], v[160:163], v[172:175]\n\t"
            "v_mfma_f32_16x16x32_bf16 v[172:175], v[44:47], v[164:167], v[172:175]\n\t"
            "s_waitcnt lgkmcnt(5)\n\t"
            "s_nop 0\n\t"
            "v_mul_f32 v168, v236, v168\n\t"
            "v_mfma_f32_16x16x32_bf16 v[184:187], v[112:115], v[160:163], v[184:187]\n\t"
            "v_mul_f32 v169, v236, v169\n\t"
            "v_mul_f32 v170, v236, v170\n\t"
            "v_mfma_f32_16x16x32_bf16 v[184:187], v[116:119], v[164:167], v[184:187]\n\t"
            "v_mul_f32 v176, v236, v176\n\t"
            "v_mul_f32 v177, v236, v177\n\t"
            "v_mfma_f32_16x16x32_bf16 v[188:191], v[136:139], v[160:163], v[188:191]\n\t"
            "v_mul_f32 v171, v236, v171\n\t"
            "v_mul_f32 v178, v236, v178\n\t"
            "v_mfma_f32_16x16x32_bf16 v[188:191], v[140:143], v[164:167], v[188:191]\n\t"
            "v_mul_f32 v179, v236, v179\n\t"
            "v_cvt_pk_bf16_f32 v168, v168, v169\n\t"
            "s_waitcnt lgkmcnt(2)\n\t"
            "v_mfma_f32_16x16x32_bf16 v[192:195], v[16:19], v[204:207], v[192:195]\n\t"
            "v_cvt_pk_bf16_f32 v176, v176, v177\n\t"
            "v_cvt_pk_bf16_f32 v180, v180, v181\n\t"
            "v_mfma_f32_16x16x32_bf16 v[192:195], v[20:23], v[208:211], v[192:195]\n\t"
            "v_mul_f32 v172, v236, v172\n\t"
            "v_mul_f32 v173, v236, v173\n\t"
            "v_mfma_f32_16x16x32_bf16 v[196:199], v[40:43], v[204:207], v[196:199]\n\t"
            "v_mul_f32 v174, v236, v174\n\t"
            "v_mul_f32 v175, v236, v175\n\t"
            "v_mfma_f32_16x16x32_bf16 v[196:199], v[44:47], v[208:211], v[196:199]\n\t"
            "v_cvt_pk_bf16_f32 v169, v170, v171\n\t"
            "v_cvt_pk_bf16_f32 v177, v178, v179\n\t"
            "v_mfma_f32_16x16x32_bf16 v[200:203], v[64:67], v[204:207], v[200:203]\n\t"
            "v_cvt_pk_bf16_f32 v181, v182, v183\n\t"
            "v_cvt_pk_bf16_f32 v170, v172, v173\n\t"
            "v_mfma_f32_16x16x32_bf16 v[200:203], v[68:71], v[208:211], v[200:203]\n\t"
            "v_cvt_pk_bf16_f32 v171, v174, v175\n\t"
            "v_cvt_pk_bf16_f32 v182, v184, v185\n\t"
            "v_mfma_f32_16x16x32_bf16 v[216:219], v[88:91], v[204:207], v[216:219]\n\t"
            "v_cvt_pk_bf16_f32 v183, v186, v187\n\t"
            "v_cvt_pk_bf16_f32 v178, v188, v189\n\t"
            "v_mfma_f32_16x16x32_bf16 v[216:219], v[92:95], v[208:211], v[216:219]\n\t"
            "v_cvt_pk_bf16_f32 v179, v190, v191\n\t"
            "v_mul_f32 v236, 0x3e553b94, v236\n\t"
            "v_mfma_f32_16x16x32_bf16 v[220:223], v[112:115], v[204:207], v[220:223]\n\t"
            "v_add_u32 v184, s76, v239\n\t"
            "v_add_u32 v185, s74, v239\n\t"
            "v_mfma_f32_16x16x32_bf16 v[220:223], v[116:119], v[208:211], v[220:223]\n\t"
            "s_mov_b64 exec, -1\n\t"
            "ds_write_b128 v234, v[168:171]\n\t"
            "ds_write_b128 v234, v[180:183] offset:64\n\t"
            "ds_write_b64 v235, v[176:177] offset:128\n\t"
            "ds_write_b64 v235, v[178:179] offset:160\n\t"
            "s_mov_b64 exec, -1\n\t"
            "v_mfma_f32_16x16x16_bf16 v[172:175], v[176:177], v[178:179], 0\n\t"
            "v_mul_f32 v192, v237, v192\n\t"
            "v_mul_f32 v193, v237, v193\n\t"
            "v_mul_f32 v200, v237, v200\n\t"
            "v_mfma_f32_16x16x32_bf16 v[228:231], v[136:139], v[204:207], v[228:231]\n\t"
            "ds_read_b128 v[204:207], v232\n\t"
            "v_mul_f32 v201, v237, v201\n\t"
            "v_mfma_f32_16x16x32_bf16 v[228:231], v[140:143], v[208:211], v[228:231]\n\t"
            "ds_read_b128 v[208:211], v233\n\t"
            "v_mul_f32 v194, v237, v194\n\t"
            "v_mfma_f32_16x16x32_bf16 v[172:175], v[168:171], v[180:183], v[172:175]\n\t"
            "v_mul_f32 v195, v237, v195\n\t"
            "v_mul_f32 v202, v237, v202\n\t"
            "v_mul_f32 v203, v237, v203\n\t"
            "v_cvt_pk_bf16_f32 v192, v192, v193\n\t"
            "v_cvt_pk_bf16_f32 v200, v200, v201\n\t"
            "v_cvt_pk_bf16_f32 v216, v216, v217\n\t"
            "v_mul_f32 v196, v237, v196\n\t"
            "v_mul_f32 v197, v237, v197\n\t"
            "v_mul_f32 v198, v237, v198\n\t"
            "v_mul_f32 v199, v237, v199\n\t"
            "v_cvt_pk_bf16_f32 v193, v194, v195\n\t"
            "v_cvt_pk_bf16_f32 v201, v202, v203\n\t"
            "v_cvt_pk_bf16_f32 v217, v218, v219\n\t"
            "v_cvt_pk_bf16_f32 v194, v196, v197\n\t"
            "v_cvt_pk_bf16_f32 v195, v198, v199\n\t"
            "v_cvt_pk_bf16_f32 v218, v220, v221\n\t"
            "v_cvt_pk_bf16_f32 v219, v222, v223\n\t"
            "v_cvt_pk_bf16_f32 v202, v228, v229\n\t"
            "v_cvt_pk_bf16_f32 v203, v230, v231\n\t"
            "v_mul_f32 v237, 0x3e553b94, v237\n\t"
            "v_add_u32 v220, s76, v239\n\t"
            "v_mfma_f32_16x16x16_bf16 v[196:199], v[200:201], v[202:203], 0\n\t"
            "s_and_b32 exec_lo, s56, 0x11111111\n\t"
            "s_and_b32 exec_hi, s57, 0x11111111\n\t"
            "ds_write_b32 v184, v172 offset:0\n\t"
            "s_and_b32 exec_lo, s56, 0x22222222\n\t"
            "s_and_b32 exec_hi, s57, 0x22222222\n\t"
            "ds_write_b32 v184, v173 offset:0\n\t"
            "s_and_b32 exec_lo, s56, 0x44444444\n\t"
            "s_and_b32 exec_hi, s57, 0x44444444\n\t"
            "ds_write_b32 v184, v174 offset:0\n\t"
            "s_and_b32 exec_lo, s56, 0x88888888\n\t"
            "s_and_b32 exec_hi, s57, 0x88888888\n\t"
            "ds_write_b32 v184, v175 offset:0\n\t"
            "s_mov_b64 exec, s[56:57]\n\t"
            "ds_write_b32 v185, v236 offset:0\n\t"
            "s_mov_b64 exec, -1\n\t"
            "s_waitcnt lgkmcnt(5)\n\t"
            "v_mfma_f32_16x16x32_bf16 v[144:147], v[0:3], v[204:207], 0\n\t"
            "v_add_u32 v221, s74, v239\n\t"
            "v_mfma_f32_16x16x32_bf16 v[144:147], v[4:7], v[208:211], v[144:147]\n\t"
            "s_mov_b64 exec, s[54:55]\n\t"
            "ds_write_b128 v234, v[192:195] offset:12800\n\t"
            "ds_write_b128 v234, v[216:219] offset:12864\n\t"
            "ds_write_b64 v235, v[200:201] offset:12928\n\t"
            "ds_write_b64 v235, v[202:203] offset:12960\n\t"
            "s_mov_b64 exec, -1\n\t"
            "v_mfma_f32_16x16x32_bf16 v[148:151], v[28:31], v[208:211], 0\n\t"
            "v_add_u32 v235, s48, v248\n\t"
            "v_mfma_f32_16x16x32_bf16 v[148:151], v[24:27], v[204:207], v[148:151]\n\t"
            "ds_read_b128 v[228:231], v235 offset:18816\n\t"
            "v_mfma_f32_16x16x32_bf16 v[152:155], v[48:51], v[204:207], 0\n\t"
            "v_mfma_f32_16x16x32_bf16 v[152:155], v[52:55], v[208:211], v[152:155]\n\t"
            "v_mfma_f32_16x16x32_bf16 v[156:159], v[76:79], v[208:211], 0\n\t"
            "v_mfma_f32_16x16x32_bf16 v[156:159], v[72:75], v[204:207], v[156:159]\n\t"
            "v_mfma_f32_16x16x32_bf16 v[160:163], v[96:99], v[204:207], 0\n\t"
            "v_mfma_f32_16x16x32_bf16 v[160:163], v[100:103], v[208:211], v[160:163]\n\t"
            "v_mfma_f32_16x16x32_bf16 v[164:167], v[124:127], v[208:211], 0\n\t"
            "ds_read_b128 v[208:211], v233 offset:128\n\t"
            "v_mfma_f32_16x16x32_bf16 v[164:167], v[120:123], v[204:207], v[164:167]\n\t"
            "ds_read_b128 v[204:207], v232 offset:128\n\t"
            "v_mfma_f32_16x16x32_bf16 v[196:199], v[192:195], v[216:219], v[196:199]\n\t"
            "s_waitcnt lgkmcnt(2)\n\t"
            "v_add_f32 v236, v228, v230\n\t"
            "s_nop 5\n\t"
            "s_and_b32 exec_lo, s78, 0x11111111\n\t"
            "s_and_b32 exec_hi, s79, 0x11111111\n\t"
            "ds_write_b32 v220, v196 offset:12800\n\t"
            "s_and_b32 exec_lo, s78, 0x22222222\n\t"
            "s_and_b32 exec_hi, s79, 0x22222222\n\t"
            "ds_write_b32 v220, v197 offset:12800\n\t"
            "s_and_b32 exec_lo, s78, 0x44444444\n\t"
            "s_and_b32 exec_hi, s79, 0x44444444\n\t"
            "ds_write_b32 v220, v198 offset:12800\n\t"
            "s_and_b32 exec_lo, s78, 0x88888888\n\t"
            "s_and_b32 exec_hi, s79, 0x88888888\n\t"
            "ds_write_b32 v220, v199 offset:12800\n\t"
            "s_mov_b64 exec, s[78:79]\n\t"
            "ds_write_b32 v221, v237 offset:12800\n\t"
            "s_mov_b64 exec, -1\n\t"
            "s_waitcnt lgkmcnt(5)\n\t"
            "v_mfma_f32_16x16x32_bf16 v[144:147], v[8:11], v[204:207], v[144:147]\n\t"
            "v_add_f32 v237, v229, v231\n\t"
            "v_mfma_f32_16x16x32_bf16 v[144:147], v[12:15], v[208:211], v[144:147]\n\t"
            "ds_read_b128 v[228:231], v235 offset:19840\n\t"
            "v_mfma_f32_16x16x32_bf16 v[148:151], v[36:39], v[208:211], v[148:151]\n\t"
            "v_mfma_f32_16x16x32_bf16 v[148:151], v[32:35], v[204:207], v[148:151]\n\t"
            "v_mfma_f32_16x16x32_bf16 v[152:155], v[56:59], v[204:207], v[152:155]\n\t"
            "v_mfma_f32_16x16x32_bf16 v[152:155], v[60:63], v[208:211], v[152:155]\n\t"
            "v_mfma_f32_16x16x32_bf16 v[156:159], v[84:87], v[208:211], v[156:159]\n\t"
            "v_mfma_f32_16x16x32_bf16 v[156:159], v[80:83], v[204:207], v[156:159]\n\t"
            "v_mfma_f32_16x16x32_bf16 v[160:163], v[104:107], v[204:207], v[160:163]\n\t"
            "v_mfma_f32_16x16x32_bf16 v[160:163], v[108:111], v[208:211], v[160:163]\n\t"
            "v_mfma_f32_16x16x32_bf16 v[164:167], v[132:135], v[208:211], v[164:167]\n\t"
            "ds_read_b128 v[208:211], v233 offset:6144\n\t"
            "v_mfma_f32_16x16x32_bf16 v[164:167], v[128:131], v[204:207], v[164:167]\n\t"
            "ds_read_b128 v[204:207], v232 offset:6144\n\t"
            "s_waitcnt lgkmcnt(2)\n\t"
            "v_add_f32 v228, v228, v230\n\t"
            "v_mfma_f32_16x16x32_bf16 v[144:147], v[16:19], v[212:215], v[144:147]\n\t"
            "v_add_f32 v236, v236, v228\n\t"
            "v_add_f32 v229, v229, v231\n\t"
            "v_mfma_f32_16x16x32_bf16 v[144:147], v[20:23], v[224:227], v[144:147]\n\t"
            "v_mul_f32 v236, 0x3baaaaab, v236\n\t"
            "v_add_f32 v237, v237, v229\n\t"
            "v_mfma_f32_16x16x32_bf16 v[148:151], v[44:47], v[224:227], v[148:151]\n\t"
            "v_mul_f32_e64 v236, v236, -v236\n\t"
            "v_fmac_f32 v236, 0x3baaaaab, v237\n\t"
            "v_mfma_f32_16x16x32_bf16 v[148:151], v[40:43], v[212:215], v[148:151]\n\t"
            "v_max_f32 v236, 0, v236\n\t"
            "v_add_f32 v236, 0x3727c5ac, v236\n\t"
            "v_mfma_f32_16x16x32_bf16 v[152:155], v[64:67], v[212:215], v[152:155]\n\t"
            "v_rsq_f32 v236, v236\n\t"
            "v_mfma_f32_16x16x32_bf16 v[152:155], v[68:71], v[224:227], v[152:155]\n\t"
            "ds_write_b32 v249, v236\n\t"
            "v_mfma_f32_16x16x32_bf16 v[156:159], v[92:95], v[224:227], v[156:159]\n\t"
            "ds_read_b32 v236, v251\n\t"
            "ds_read_b32 v237, v251 offset:64\n\t"
            "v_mfma_f32_16x16x32_bf16 v[156:159], v[88:91], v[212:215], v[156:159]\n\t"
            "ds_read_b32 v238, v251 offset:128\n\t"
            "v_mfma_f32_16x16x32_bf16 v[160:163], v[112:115], v[212:215], v[160:163]\n\t"
            "v_mfma_f32_16x16x32_bf16 v[160:163], v[116:119], v[224:227], v[160:163]\n\t"
            "v_mfma_f32_16x16x32_bf16 v[164:167], v[140:143], v[224:227], v[164:167]\n\t"
            "ds_read_b128 v[224:227], v233 offset:6272\n\t"
            "v_mfma_f32_16x16x32_bf16 v[164:167], v[136:139], v[212:215], v[164:167]\n\t"
            "ds_read_b128 v[212:215], v232 offset:6272\n\t"
            "s_waitcnt lgkmcnt(6)\n\t"
            "v_mfma_f32_16x16x32_bf16 v[168:171], v[0:3], v[204:207], 0\n\t"
            "v_cvt_pk_bf16_f32 v156, v156, v157\n\t"
            "v_cvt_pk_bf16_f32 v157, v158, v159\n\t"
            "v_mfma_f32_16x16x32_bf16 v[168:171], v[4:7], v[208:211], v[168:171]\n\t"
            "v_cvt_pk_bf16_f32 v158, v160, v161\n\t"
            "v_cvt_pk_bf16_f32 v159, v162, v163\n\t"
            "v_mfma_f32_16x16x32_bf16 v[172:175], v[28:31], v[208:211], 0\n\t"
            "v_mfma_f32_16x16x32_bf16 v[172:175], v[24:27], v[204:207], v[172:175]\n\t"
            "v_mfma_f32_16x16x32_bf16 v[176:179], v[48:51], v[204:207], 0\n\t"
            "s_waitcnt lgkmcnt(4)\n\t"
            "v_mul_f32 v144, v236, v144\n\t"
            "v_mul_f32 v145, v236, v145\n\t"
            "v_mfma_f32_16x16x32_bf16 v[176:179], v[52:55], v[208:211], v[176:179]\n\t"
            "v_mul_f32 v152, v236, v152\n\t"
            "v_mul_f32 v153, v236, v153\n\t"
            "v_mfma_f32_16x16x32_bf16 v[180:183], v[76:79], v[208:211], 0\n\t"
            "v_mul_f32 v146, v236, v146\n\t"
            "v_mul_f32 v147, v236, v147\n\t"
            "v_mfma_f32_16x16x32_bf16 v[180:183], v[72:75], v[204:207], v[180:183]\n\t"
            "v_mul_f32 v154, v236, v154\n\t"
            "v_mul_f32 v155, v236, v155\n\t"
            "v_mfma_f32_16x16x32_bf16 v[184:187], v[96:99], v[204:207], 0\n\t"
            "v_cvt_pk_bf16_f32 v144, v144, v145\n\t"
            "v_cvt_pk_bf16_f32 v152, v152, v153\n\t"
            "v_mfma_f32_16x16x32_bf16 v[184:187], v[100:103], v[208:211], v[184:187]\n\t"
            "v_mul_f32 v148, v236, v148\n\t"
            "v_mul_f32 v149, v236, v149\n\t"
            "v_mfma_f32_16x16x32_bf16 v[188:191], v[124:127], v[208:211], 0\n\t"
            "ds_read_b128 v[208:211], v233 offset:6400\n\t"
            "v_mul_f32 v150, v236, v150\n\t"
            "v_mfma_f32_16x16x32_bf16 v[188:191], v[120:123], v[204:207], v[188:191]\n\t"
            "ds_read_b128 v[204:207], v232 offset:6400\n\t"
            "v_mul_f32 v151, v236, v151\n\t"
            "s_waitcnt lgkmcnt(2)\n\t"
            "v_mfma_f32_16x16x32_bf16 v[168:171], v[8:11], v[212:215], v[168:171]\n\t"
            "v_cvt_pk_bf16_f32 v145, v146, v147\n\t"
            "v_cvt_pk_bf16_f32 v153, v154, v155\n\t"
            "v_mfma_f32_16x16x32_bf16 v[168:171], v[12:15], v[224:227], v[168:171]\n\t"
            "v_cvt_pk_bf16_f32 v146, v148, v149\n\t"
            "v_cvt_pk_bf16_f32 v147, v150, v151\n\t"
            "v_mfma_f32_16x16x32_bf16 v[172:175], v[36:39], v[224:227], v[172:175]\n\t"
            "v_cvt_pk_bf16_f32 v154, v164, v165\n\t"
            "v_cvt_pk_bf16_f32 v155, v166, v167\n\t"
            "v_mfma_f32_16x16x32_bf16 v[172:175], v[32:35], v[212:215], v[172:175]\n\t"
            "v_mul_f32 v236, 0x3e553b94, v236\n\t"
            "v_mfma_f32_16x16x32_bf16 v[176:179], v[56:59], v[212:215], v[176:179]\n\t"
            "v_mfma_f32_16x16x32_bf16 v[176:179], v[60:63], v[224:227], v[176:179]\n\t"
            "v_mfma_f32_16x16x32_bf16 v[180:183], v[84:87], v[224:227], v[180:183]\n\t"
            "v_mfma_f32_16x16x32_bf16 v[180:183], v[80:83], v[212:215], v[180:183]\n\t"
            "v_mfma_f32_16x16x32_bf16 v[184:187], v[104:107], v[212:215], v[184:187]\n\t"
            "v_mfma_f32_16x16x32_bf16 v[184:187], v[108:111], v[224:227], v[184:187]\n\t"
            "v_mfma_f32_16x16x32_bf16 v[188:191], v[132:135], v[224:227], v[188:191]\n\t"
            "ds_read_b128 v[224:227], v233 offset:12288\n\t"
            "v_mfma_f32_16x16x32_bf16 v[188:191], v[128:131], v[212:215], v[188:191]\n\t"
            "ds_read_b128 v[212:215], v232 offset:12288\n\t"
            "s_waitcnt lgkmcnt(2)\n\t"
            "v_mfma_f32_16x16x32_bf16 v[168:171], v[16:19], v[204:207], v[168:171]\n\t"
            "v_mfma_f32_16x16x32_bf16 v[168:171], v[20:23], v[208:211], v[168:171]\n\t"
            "v_mfma_f32_16x16x32_bf16 v[172:175], v[44:47], v[208:211], v[172:175]\n\t"
            "v_mfma_f32_16x16x32_bf16 v[172:175], v[40:43], v[204:207], v[172:175]\n\t"
            "v_mfma_f32_16x16x32_bf16 v[176:179], v[64:67], v[204:207], v[176:179]\n\t"
            "v_mfma_f32_16x16x32_bf16 v[176:179], v[68:71], v[208:211], v[176:179]\n\t"
            "v_mfma_f32_16x16x32_bf16 v[180:183], v[92:95], v[208:211], v[180:183]\n\t"
            "v_mfma_f32_16x16x32_bf16 v[180:183], v[88:91], v[204:207], v[180:183]\n\t"
            "v_mfma_f32_16x16x32_bf16 v[184:187], v[112:115], v[204:207], v[184:187]\n\t"
            "v_mfma_f32_16x16x32_bf16 v[184:187], v[116:119], v[208:211], v[184:187]\n\t"
            "v_mfma_f32_16x16x32_bf16 v[188:191], v[140:143], v[208:211], v[188:191]\n\t"
            "ds_read_b128 v[208:211], v233 offset:12416\n\t"
            "v_mfma_f32_16x16x32_bf16 v[188:191], v[136:139], v[204:207], v[188:191]\n\t"
            "ds_read_b128 v[204:207], v232 offset:12416\n\t"
            "s_waitcnt lgkmcnt(0)\n\t"
            "v_subrev_u32 v249, s75, v251\n\t"
            "v_add_u32 v249, 162816, v249\n\t"
            "s_mov_b32 s90, 2\n\t"
            "s_mov_b32 s81, 0\n\t"
            "s_mov_b32 s91, 0\n\t"
            "s_nop 7\n\t"
            ".p2align 6\n\t"
            "s_nop 0\n\t"
            "s_nop 0\n\t"
            "s_nop 0\n\t"
            ".Lwa_loop%=:\n\t"
            "s_waitcnt lgkmcnt(2)\n\t"
            "v_mfma_f32_16x16x32_bf16 v[192:195], v[0:3], v[212:215], 0\n\t"
            "s_bfe_u32 s49, s70, 0x30001\n\t"
            "s_lshl_b32 s49, s49, 4\n\t"
            "s_or_b32 s73, s77, s49\n\t"
            "v_mfma_f32_16x16x32_bf16 v[192:195], v[4:7], v[224:227], v[192:195]\n\t"
            "v_mad_u32_u24 v220, v246, s72, v253\n\t"
            "v_mad_u32_u24 v216, v246, s72, v252\n\t"
            "v_mfma_f32_16x16x32_bf16 v[148:151], v[28:31], v[224:227], 0\n\t"
            "ds_read_b64 v[222:223], v220 offset:160\n\t"
            "ds_read_b64 v[220:221], v220 offset:128\n\t"
            "v_mfma_f32_16x16x32_bf16 v[148:151], v[24:27], v[212:215], v[148:151]\n\t"
            "v_mul_f32 v176, v237, v176\n\t"
            "v_mul_f32 v177, v237, v177\n\t"
            "v_mfma_f32_16x16x32_bf16 v[196:199], v[48:51], v[212:215], 0\n\t"
            "v_mul_f32 v178, v237, v178\n\t"
            "v_mul_f32 v179, v237, v179\n\t"
            "v_mfma_f32_16x16x32_bf16 v[196:199], v[52:55], v[224:227], v[196:199]\n\t"
            "v_cvt_pk_bf16_f32 v176, v176, v177\n\t"
            "v_mul_f32 v168, v237, v168\n\t"
            "v_mfma_f32_16x16x32_bf16 v[200:203], v[76:79], v[224:227], 0\n\t"
            "v_mul_f32 v169, v237, v169\n\t"
            "v_cvt_pk_bf16_f32 v177, v178, v179\n\t"
            "v_mfma_f32_16x16x32_bf16 v[200:203], v[72:75], v[212:215], v[200:203]\n\t"
            "v_mul_f32 v170, v237, v170\n\t"
            "v_mul_f32 v171, v237, v171\n\t"
            "v_mfma_f32_16x16x32_bf16 v[160:163], v[96:99], v[212:215], 0\n\t"
            "v_cvt_pk_bf16_f32 v168, v168, v169\n\t"
            "v_cvt_pk_bf16_f32 v180, v180, v181\n\t"
            "v_mfma_f32_16x16x32_bf16 v[160:163], v[100:103], v[224:227], v[160:163]\n\t"
            "v_cvt_pk_bf16_f32 v178, v188, v189\n\t"
            "v_cvt_pk_bf16_f32 v179, v190, v191\n\t"
            "v_mfma_f32_16x16x32_bf16 v[164:167], v[124:127], v[224:227], 0\n\t"
            "ds_read_b128 v[224:227], v233 offset:12544\n\t"
            "v_mul_f32 v172, v237, v172\n\t"
            "v_mfma_f32_16x16x32_bf16 v[164:167], v[120:123], v[212:215], v[164:167]\n\t"
            "ds_read_b128 v[212:215], v232 offset:12544\n\t"
            "v_mul_f32 v173, v237, v173\n\t"
            "s_waitcnt lgkmcnt(4)\n\t"
            "v_mfma_f32_16x16x32_bf16 v[192:195], v[8:11], v[204:207], v[192:195]\n\t"
            "v_mul_f32 v174, v237, v174\n\t"
            "v_mul_f32 v175, v237, v175\n\t"
            "v_mfma_f32_16x16x32_bf16 v[192:195], v[12:15], v[208:211], v[192:195]\n\t"
            "v_cvt_pk_bf16_f32 v169, v170, v171\n\t"
            "v_cvt_pk_bf16_f32 v181, v182, v183\n\t"
            "v_mfma_f32_16x16x32_bf16 v[196:199], v[56:59], v[204:207], v[196:199]\n\t"
            "v_cvt_pk_bf16_f32 v170, v172, v173\n\t"
            "v_cvt_pk_bf16_f32 v171, v174, v175\n\t"
            "v_mfma_f32_16x16x32_bf16 v[196:199], v[60:63], v[208:211], v[196:199]\n\t"
            "v_cvt_pk_bf16_f32 v182, v184, v185\n\t"
            "v_cvt_pk_bf16_f32 v183, v186, v187\n\t"
            "v_mfma_f32_16x16x32_bf16 v[160:163], v[104:107], v[204:207], v[160:163]\n\t"
            "v_mov_b32 v230, s76\n\t"
            "v_mov_b32 v239, s74\n\t"
            "v_mfma_f32_16x16x32_bf16 v[160:163], v[108:111], v[208:211], v[160:163]\n\t"
            "s_add_u32 s70, s70, 1\n\t"
            "s_add_u32 s72, s72, 800\n\t"
            "s_add_u32 s74, s74, 800\n\t"
            "s_add_u32 s76, s76, 800\n\t"
            "v_mfma_f32_16x16x32_bf16 v[148:151], v[36:39], v[208:211], v[148:151]\n\t"
            "s_add_u32 s77, s77, 384\n\t"
            "s_xor_b32 s48, s48, s47\n\t"
            "ds_read_b32 v230, v230\n\t"
            "v_mfma_f32_16x16x32_bf16 v[148:151], v[32:35], v[204:207], v[148:151]\n\t"
            "ds_read_b32 v239, v239\n\t"
            "v_mul_f32 v237, 0x3e553b94, v237\n\t"
            "v_mfma_f32_16x16x32_bf16 v[200:203], v[84:87], v[208:211], v[200:203]\n\t"
            "v_mfma_f32_16x16x32_bf16 v[200:203], v[80:83], v[204:207], v[200:203]\n\t"
            "v_mfma_f32_16x16x32_bf16 v[164:167], v[132:135], v[208:211], v[164:167]\n\t"
            "v_mfma_f32_16x16x32_bf16 v[164:167], v[128:131], v[204:207], v[164:167]\n\t"
            "s_waitcnt lgkmcnt(2)\n\t"
            "v_mfma_f32_16x16x32_bf16 v[192:195], v[16:19], v[212:215], v[192:195]\n\t"
            "v_mfma_f32_16x16x32_bf16 v[192:195], v[20:23], v[224:227], v[192:195]\n\t"
            "v_mfma_f32_16x16x32_bf16 v[196:199], v[64:67], v[212:215], v[196:199]\n\t"
            "v_mfma_f32_16x16x32_bf16 v[196:199], v[68:71], v[224:227], v[196:199]\n\t"
            "v_mfma_f32_16x16x32_bf16 v[160:163], v[112:115], v[212:215], v[160:163]\n\t"
            "v_mfma_f32_16x16x32_bf16 v[160:163], v[116:119], v[224:227], v[160:163]\n\t"
            "v_mfma_f32_16x16x32_bf16 v[148:151], v[44:47], v[224:227], v[148:151]\n\t"
            "v_mfma_f32_16x16x32_bf16 v[148:151], v[40:43], v[212:215], v[148:151]\n\t"
            "s_nop 1\n\t"
            "v_mul_f32 v192, v238, v192\n\t"
            "v_mfma_f32_16x16x32_bf16 v[200:203], v[92:95], v[224:227], v[200:203]\n\t"
            "v_mul_f32 v193, v238, v193\n\t"
            "v_mul_f32 v194, v238, v194\n\t"
            "v_mfma_f32_16x16x32_bf16 v[200:203], v[88:91], v[212:215], v[200:203]\n\t"
            "v_mul_f32 v195, v238, v195\n\t"
            "v_cvt_pk_bf16_f32 v192, v192, v193\n\t"
            "v_mfma_f32_16x16x32_bf16 v[164:167], v[140:143], v[224:227], v[164:167]\n\t"
            "ds_read_b128 v[224:227], v216 offset:64\n\t"
            "ds_read_b128 v[216:219], v216\n\t"
            "v_mfma_f32_16x16x32_bf16 v[164:167], v[136:139], v[212:215], v[164:167]\n\t"
            "v_mul_f32 v148, v238, v148\n\t"
            "v_mul_f32 v149, v238, v149\n\t"
            "v_mfma_f32_16x16x16_bf16 v[172:175], v[220:221], v[154:155], 0\n\t"
            "v_mul_f32 v150, v238, v150\n\t"
            "v_mul_f32 v151, v238, v151\n\t"
            "v_cvt_pk_bf16_f32 v193, v194, v195\n\t"
            "v_mfma_f32_16x16x16_bf16 v[184:187], v[152:153], v[178:179], 0\n\t"
            "v_mul_f32 v196, v238, v196\n\t"
            "v_mul_f32 v197, v238, v197\n\t"
            "v_cvt_pk_bf16_f32 v194, v148, v149\n\t"
            "v_mfma_f32_16x16x16_bf16 v[208:211], v[220:221], v[178:179], 0\n\t"
            "v_cvt_pk_bf16_f32 v195, v150, v151\n\t"
            "v_cvt_pk_bf16_f32 v200, v200, v201\n\t"
            "v_mul_f32 v198, v238, v198\n\t"
            "v_mfma_f32_16x16x16_bf16 v[148:151], v[152:153], v[154:155], 0\n\t"
            "v_mul_f32 v199, v238, v199\n\t"
            "v_cvt_pk_bf16_f32 v196, v196, v197\n\t"
            "v_cvt_pk_bf16_f32 v201, v202, v203\n\t"
            "v_mfma_f32_16x16x16_bf16 v[204:207], v[176:177], v[178:179], 0\n\t"
            "v_cvt_pk_bf16_f32 v197, v198, v199\n\t"
            "v_cvt_pk_bf16_f32 v202, v160, v161\n\t"
            "v_cvt_pk_bf16_f32 v203, v162, v163\n\t"
            "v_mfma_f32_16x16x16_bf16 v[160:163], v[176:177], v[154:155], 0\n\t"
            "v_cvt_pk_bf16_f32 v198, v164, v165\n\t"
            "v_cvt_pk_bf16_f32 v199, v166, v167\n\t"
            "s_setprio 0\n\t"
            "s_setprio 3\n\t"
            "v_mfma_f32_16x16x16_bf16 v[164:167], v[196:197], v[154:155], 0\n\t"
            "v_mul_f32 v238, 0x3e553b94, v238\n\t"
            "v_mfma_f32_16x16x16_bf16 v[188:191], v[196:197], v[178:179], 0\n\t"
            "v_mfma_f32_16x16x32_bf16 v[148:151], v[144:147], v[156:159], v[148:151]\n\t"
            "s_waitcnt lgkmcnt(0)\n\t"
            "v_mfma_f32_16x16x32_bf16 v[172:175], v[216:219], v[156:159], v[172:175]\n\t"
            "v_mfma_f32_16x16x32_bf16 v[184:187], v[144:147], v[180:183], v[184:187]\n\t"
            "v_mfma_f32_16x16x32_bf16 v[208:211], v[216:219], v[180:183], v[208:211]\n\t"
            "v_mfma_f32_16x16x32_bf16 v[160:163], v[168:171], v[156:159], v[160:163]\n\t"
            "v_mfma_f32_16x16x32_bf16 v[204:207], v[168:171], v[180:183], v[204:207]\n\t"
            "v_mfma_f32_16x16x32_bf16 v[164:167], v[192:195], v[156:159], v[164:167]\n\t"
            "v_mfma_f32_16x16x32_bf16 v[188:191], v[192:195], v[180:183], v[188:191]\n\t"
            "s_nop 1\n\t"
            "v_max3_f32 v228, v172, v148, v149\n\t"
            "v_mfma_f32_16x16x16_bf16 v[212:215], v[222:223], v[152:153], 0\n\t"
            "v_max3_f32 v228, v228, v150, v151\n\t"
            "v_max3_f32 v229, v208, v184, v185\n\t"
            "v_mfma_f32_16x16x16_bf16 v[156:159], v[222:223], v[196:197], 0\n\t"
            "v_max3_f32 v229, v229, v186, v187\n\t"
            "v_max3_f32 v228, v228, v160, v161\n\t"
            "v_max3_f32 v229, v229, v204, v205\n\t"
            "v_mfma_f32_16x16x32_bf16 v[212:215], v[224:227], v[144:147], v[212:215]\n\t"
            "v_max3_f32 v228, v228, v162, v163\n\t"
            "v_max3_f32 v229, v229, v206, v207\n\t"
            "v_mfma_f32_16x16x32_bf16 v[156:159], v[224:227], v[192:195], v[156:159]\n\t"
            "v_max3_f32 v228, v228, v164, v165\n\t"
            "v_max3_f32 v229, v229, v188, v189\n\t"
            "v_mfma_f32_16x16x16_bf16 v[180:183], v[152:153], v[198:199], 0\n\t"
            "v_max3_f32 v228, v228, v166, v167\n\t"
            "v_max3_f32 v229, v229, v190, v191\n\t"
            "s_nop 1\n\t"
            "v_permlane32_swap_b32 v228, v229\n\t"
            "v_max_f32 v228, v228, v229\n\t"
            "v_mov_b32 v231, v228\n\t"
            "v_mfma_f32_16x16x32_bf16 v[180:183], v[144:147], v[200:203], v[180:183]\n\t"
            "s_setprio 0\n\t"
            "v_permlane16_swap_b32 v228, v231\n\t"
            "v_max_f32 v228, v228, v231\n\t"
            "v_mov_b32 v229, v228\n\t"
            "s_nop 1\n\t"
            "v_permlane32_swap_b32 v228, v229\n\t"
            "v_mul_f32_e64 v228, -v228, v236\n\t"
            "v_fma_f32 v148, v148, v236, v228\n\t"
            "v_fma_f32 v149, v149, v236, v228\n\t"
            "v_exp_f32 v148, v148\n\t"
            "v_exp_f32 v149, v149\n\t"
            "v_fma_f32 v150, v150, v236, v228\n\t"
            "v_fma_f32 v151, v151, v236, v228\n\t"
            "v_exp_f32 v150, v150\n\t"
            "v_exp_f32 v151, v151\n\t"
            "v_fma_f32 v160, v160, v236, v228\n\t"
            "v_fma_f32 v161, v161, v236, v228\n\t"
            "v_fma_f32 v162, v162, v236, v228\n\t"
            "v_fma_f32 v163, v163, v236, v228\n\t"
            "v_exp_f32 v160, v160\n\t"
            "v_exp_f32 v161, v161\n\t"
            "v_exp_f32 v162, v162\n\t"
            "v_exp_f32 v163, v163\n\t"
            "v_cvt_pk_bf16_f32 v148, v148, v149\n\t"
            "v_cvt_pk_bf16_f32 v149, v150, v151\n\t"
            "v_cvt_pk_bf16_f32 v150, v160, v161\n\t"
            "v_cvt_pk_bf16_f32 v151, v162, v163\n\t"
            "s_setprio 3\n\t"
            "v_mfma_f32_16x16x16_bf16 v[160:163], v[222:223], v[176:177], 0\n\t"
            "s_setprio 0\n\t"
            "v_mul_f32_e64 v229, -v229, v237\n\t"
            "v_fma_f32 v188, v188, v237, v229\n\t"
            "v_fma_f32 v189, v189, v237, v229\n\t"
            "v_exp_f32 v188, v188\n\t"
            "v_exp_f32 v189, v189\n\t"
            "v_fma_f32 v190, v190, v237, v229\n\t"
            "s_setprio 3\n\t"
            "v_mfma_f32_16x16x32_bf16 v[160:163], v[224:227], v[168:171], v[160:163]\n\t"
            "s_setprio 0\n\t"
            "v_fma_f32 v191, v191, v237, v229\n\t"
            "v_fma_f32 v208, v208, v237, v229\n\t"
            "v_fma_f32 v184, v184, v237, v229\n\t"
            "v_fma_f32 v185, v185, v237, v229\n\t"
            "v_exp_f32 v190, v190\n\t"
            "v_exp_f32 v191, v191\n\t"
            "v_exp_f32 v208, v208\n\t"
            "v_exp_f32 v184, v184\n\t"
            "v_max3_f32 v234, v212, v160, v156\n\t"
            "v_max_f32 v234, v234, v230\n\t"
            "v_exp_f32 v185, v185\n\t"
            "v_fma_f32 v186, v186, v237, v229\n\t"
            "v_fma_f32 v187, v187, v237, v229\n\t"
            "v_fma_f32 v164, v164, v236, v228\n\t"
            "v_fma_f32 v165, v165, v236, v228\n\t"
            "v_exp_f32 v186, v186\n\t"
            "v_exp_f32 v187, v187\n\t"
            "v_fma_f32 v204, v204, v237, v229\n\t"
            "v_fma_f32 v205, v205, v237, v229\n\t"
            "v_fma_f32 v206, v206, v237, v229\n\t"
            "v_fma_f32 v207, v207, v237, v229\n\t"
            "v_cvt_pk_bf16_f32 v188, v188, v189\n\t"
            "v_exp_f32 v164, v164\n\t"
            "v_exp_f32 v165, v165\n\t"
            "v_fma_f32 v166, v166, v236, v228\n\t"
            "v_fma_f32 v167, v167, v236, v228\n\t"
            "v_fma_f32 v172, v172, v236, v228\n\t"
            "v_exp_f32 v204, v204\n\t"
            "v_exp_f32 v205, v205\n\t"
            "v_exp_f32 v206, v206\n\t"
            "v_exp_f32 v207, v207\n\t"
            "v_cvt_pk_bf16_f32 v189, v190, v191\n\t"
            "v_cvt_pk_bf16_f32 v208, v208, 0\n\t"
            "v_max_f32_dpp v234, v234, v234 row_ror:8 row_mask:0xf bank_mask:0xf\n\t"
            "v_exp_f32 v166, v166\n\t"
            "v_exp_f32 v167, v167\n\t"
            "v_exp_f32 v172, v172\n\t"
            "v_cvt_pk_bf16_f32 v184, v184, v185\n\t"
            "v_cndmask_b32_e64 v190, 0, v208, s[58:59]\n\t"
            "s_setprio 3\n\t"
            "v_mfma_f32_16x16x16_bf16 v[208:211], v[220:221], v[198:199], 0\n\t"
            "v_cvt_pk_bf16_f32 v185, v186, v187\n\t"
            "v_cvt_pk_bf16_f32 v164, v164, v165\n\t"
            "v_cvt_pk_bf16_f32 v186, v204, v205\n\t"
            "v_cvt_pk_bf16_f32 v187, v206, v207\n\t"
            "v_mfma_f32_16x16x16_bf16 v[204:207], v[176:177], v[198:199], 0\n\t"
            "v_max_f32_dpp v234, v234, v234 row_ror:4 row_mask:0xf bank_mask:0xf\n\t"
            "v_cvt_pk_bf16_f32 v165, v166, v167\n\t"
            "v_cvt_pk_bf16_f32 v172, v172, 0\n\t"
            "v_mfma_f32_16x16x32_bf16 v[208:211], v[216:219], v[200:203], v[208:211]\n\t"
            "v_cndmask_b32_e64 v166, 0, v172, s[58:59]\n\t"
            "v_max_f32_dpp v234, v234, v234 row_ror:2 row_mask:0xf bank_mask:0xf\n\t"
            "v_mfma_f32_16x16x16_bf16 v[172:175], v[196:197], v[198:199], 0\n\t"
            "v_cndmask_b32_e64 v235, v212, v160, s[60:61]\n\t"
            "v_cndmask_b32_e64 v235, v235, v156, s[62:63]\n\t"
            "v_max_f32_dpp v234, v234, v234 row_ror:1 row_mask:0xf bank_mask:0xf\n\t"
            "v_mfma_f32_16x16x32_bf16 v[204:207], v[168:171], v[200:203], v[204:207]\n\t"
            "v_cndmask_b32_e64 v235, v235, v230, s[64:65]\n\t"
            "v_sub_f32 v235, v235, v234\n\t"
            "v_mfma_f32_16x16x32_bf16 v[172:175], v[192:195], v[200:203], v[172:175]\n\t"
            "s_setprio 0\n\t"
            "v_mul_f32 v235, v239, v235\n\t"
            "v_exp_f32 v235, v235\n\t"
            "v_max3_f32 v230, v208, v180, v181\n\t"
            "v_cvt_pk_bf16_f32 v235, v235, 0\n\t"
            "v_cndmask_b32_e64 v235, 0, v235, s[66:67]\n\t"
            "v_max3_f32 v230, v230, v182, v183\n\t"
            "v_max3_f32 v230, v230, v204, v205\n\t"
            "v_max3_f32 v230, v230, v206, v207\n\t"
            "v_max3_f32 v230, v230, v172, v173\n\t"
            "v_max3_f32 v230, v230, v174, v175\n\t"
            "v_mov_b32 v231, v230\n\t"
            "v_mov_b32 v167, 0\n\t"
            "v_mov_b32 v191, 0\n\t"
            "v_permlane32_swap_b32 v230, v231\n\t"
            "v_max_f32 v230, v230, v231\n\t"
            "v_mov_b32 v231, v230\n\t"
            "ds_write_b16 v250, v235 offset:256\n\t"
            "s_nop 0\n\t"
            "v_permlane16_swap_b32 v230, v231\n\t"
            "v_max_f32 v230, v230, v231\n\t"
            "v_mul_f32_e64 v230, -v230, v238\n\t"
            "v_fma_f32 v180, v180, v238, v230\n\t"
            "v_fma_f32 v181, v181, v238, v230\n\t"
            "v_fma_f32 v172, v172, v238, v230\n\t"
            "v_fma_f32 v173, v173, v238, v230\n\t"
            "v_exp_f32 v180, v180\n\t"
            "v_exp_f32 v181, v181\n\t"
            "v_fma_f32 v182, v182, v238, v230\n\t"
            "v_fma_f32 v183, v183, v238, v230\n\t"
            "v_exp_f32 v172, v172\n\t"
            "v_exp_f32 v173, v173\n\t"
            "v_fma_f32 v174, v174, v238, v230\n\t"
            "v_fma_f32 v175, v175, v238, v230\n\t"
            "v_fma_f32 v208, v208, v238, v230\n\t"
            "v_exp_f32 v182, v182\n\t"
            "v_exp_f32 v183, v183\n\t"
            "v_fma_f32 v204, v204, v238, v230\n\t"
            "v_fma_f32 v205, v205, v238, v230\n\t"
            "v_fma_f32 v206, v206, v238, v230\n\t"
            "v_fma_f32 v207, v207, v238, v230\n\t"
            "v_exp_f32 v174, v174\n\t"
            "v_exp_f32 v175, v175\n\t"
            "v_exp_f32 v208, v208\n\t"
            "v_exp_f32 v204, v204\n\t"
            "v_exp_f32 v205, v205\n\t"
            "v_exp_f32 v206, v206\n\t"
            "v_exp_f32 v207, v207\n\t"
            "v_cvt_pk_bf16_f32 v180, v180, v181\n\t"
            "v_cvt_pk_bf16_f32 v172, v172, v173\n\t"
            "v_cvt_pk_bf16_f32 v181, v182, v183\n\t"
            "v_cvt_pk_bf16_f32 v173, v174, v175\n\t"
            "v_cvt_pk_bf16_f32 v208, v208, 0\n\t"
            "v_cvt_pk_bf16_f32 v182, v204, v205\n\t"
            "v_cvt_pk_bf16_f32 v183, v206, v207\n\t"
            "v_cndmask_b32_e64 v174, 0, v208, s[58:59]\n\t"
            "v_mov_b32 v175, 0\n\t"
            "s_cmp_ge_u32 s70, s80\n\t"
            "s_barrier\n\t"
            "s_setprio 2\n\t"
            "s_cbranch_scc1 .Lwa_h1%=\n\t"
            ".Lwa_r1%=:\n\t"
            "v_mfma_f32_16x16x32_bf16 v[200:203], v[240:243], v[148:151], 0\n\t"
            "ds_read_b64 v[176:177], v247\n\t"
            "ds_read_b64 v[196:197], v247 offset:1536\n\t"
            "v_mfma_f32_16x16x32_bf16 v[200:203], v[240:243], v[164:167], v[200:203]\n\t"
            "ds_read_b64 v[220:221], v247 offset:3072\n\t"
            "v_xor_b32 v234, s73, v254\n\t"
            "v_add_u32 v235, s45, v248\n\t"
            "v_add_u32 v232, s48, v244\n\t"
            "v_xor_b32 v233, 64, v232\n\t"
            "v_mov_b32 v179, 0\n\t"
            "v_mov_b32 v199, 0\n\t"
            "v_mov_b32 v223, 0\n\t"
            "v_rcp_f32 v201, v200\n\t"
            "v_xor_b32 v249, 0x100, v249\n\t"
            "ds_read_b128 v[168:171], v235\n\t"
            "ds_read_u16 v178, v234\n\t"
            "ds_read_b128 v[192:195], v235 offset:1536\n\t"
            "ds_read_u16 v198, v234 offset:128\n\t"
            "ds_read_b128 v[216:219], v235 offset:3072\n\t"
            "ds_read_u16 v222, v234 offset:256\n\t"
            "ds_read_b128 v[204:207], v232\n\t"
            "ds_read_b128 v[208:211], v233\n\t"
            "ds_read_b32 v236, v249\n\t"
            "ds_read_b32 v237, v249 offset:64\n\t"
            "ds_read_b32 v238, v249 offset:128\n\t"
            "v_mad_u32_u24 v234, v246, s75, v253\n\t"
            "s_waitcnt lgkmcnt(9)\n\t"
            "v_mfma_f32_16x16x32_bf16 v[224:227], v[168:171], v[148:151], 0\n\t"
            "v_mfma_f32_16x16x32_bf16 v[224:227], v[176:179], v[164:167], v[224:227]\n\t"
            "s_waitcnt lgkmcnt(7)\n\t"
            "v_mfma_f32_16x16x32_bf16 v[212:215], v[192:195], v[148:151], 0\n\t"
            "v_mfma_f32_16x16x32_bf16 v[212:215], v[196:199], v[164:167], v[212:215]\n\t"
            "s_waitcnt lgkmcnt(5)\n\t"
            "v_mfma_f32_16x16x32_bf16 v[148:151], v[216:219], v[148:151], 0\n\t"
            "v_mfma_f32_16x16x32_bf16 v[148:151], v[220:223], v[164:167], v[148:151]\n\t"
            "s_waitcnt lgkmcnt(3)\n\t"
            "v_mfma_f32_16x16x32_bf16 v[144:147], v[0:3], v[204:207], 0\n\t"
            "v_mfma_f32_16x16x32_bf16 v[144:147], v[4:7], v[208:211], v[144:147]\n\t"
            "v_mul_f32 v224, v201, v224\n\t"
            "v_mfma_f32_16x16x32_bf16 v[152:155], v[48:51], v[204:207], 0\n\t"
            "v_mul_f32 v225, v201, v225\n\t"
            "v_mul_f32 v226, v201, v226\n\t"
            "v_mfma_f32_16x16x32_bf16 v[152:155], v[52:55], v[208:211], v[152:155]\n\t"
            "v_mul_f32 v212, v201, v212\n\t"
            "v_mul_f32 v213, v201, v213\n\t"
            "v_mfma_f32_16x16x32_bf16 v[156:159], v[76:79], v[208:211], 0\n\t"
            "v_mul_f32 v227, v201, v227\n\t"
            "v_cvt_pk_bf16_f32 v224, v224, v225\n\t"
            "v_mfma_f32_16x16x32_bf16 v[156:159], v[72:75], v[204:207], v[156:159]\n\t"
            "v_mul_f32 v214, v201, v214\n\t"
            "v_mul_f32 v215, v201, v215\n\t"
            "v_mfma_f32_16x16x32_bf16 v[160:163], v[96:99], v[204:207], 0\n\t"
            "v_cvt_pk_bf16_f32 v212, v212, v213\n\t"
            "v_cvt_pk_bf16_f32 v225, v226, v227\n\t"
            "v_mfma_f32_16x16x32_bf16 v[160:163], v[100:103], v[208:211], v[160:163]\n\t"
            "v_cvt_pk_bf16_f32 v213, v214, v215\n\t"
            "v_mul_f32 v148, v201, v148\n\t"
            "v_mfma_f32_16x16x32_bf16 v[164:167], v[124:127], v[208:211], 0\n\t"
            "v_mul_f32 v149, v201, v149\n\t"
            "v_mul_f32 v150, v201, v150\n\t"
            "v_mfma_f32_16x16x32_bf16 v[164:167], v[120:123], v[204:207], v[164:167]\n\t"
            "ds_write_b64 v245, v[224:225]\n\t"
            "v_mfma_f32_16x16x32_bf16 v[224:227], v[168:171], v[184:187], 0\n\t"
            "v_mul_f32 v151, v201, v151\n\t"
            "v_cvt_pk_bf16_f32 v148, v148, v149\n\t"
            "v_mfma_f32_16x16x32_bf16 v[224:227], v[176:179], v[188:191], v[224:227]\n\t"
            "ds_write_b64 v245, v[212:213] offset:128\n\t"
            "v_mfma_f32_16x16x32_bf16 v[200:203], v[240:243], v[184:187], 0\n\t"
            "v_cvt_pk_bf16_f32 v149, v150, v151\n\t"
            "v_mfma_f32_16x16x32_bf16 v[200:203], v[240:243], v[188:191], v[200:203]\n\t"
            "ds_write_b64 v245, v[148:149] offset:256\n\t"
            "v_mfma_f32_16x16x32_bf16 v[212:215], v[192:195], v[184:187], 0\n\t"
            "v_mfma_f32_16x16x32_bf16 v[212:215], v[196:199], v[188:191], v[212:215]\n\t"
            "v_mfma_f32_16x16x32_bf16 v[184:187], v[216:219], v[184:187], 0\n\t"
            "v_mfma_f32_16x16x32_bf16 v[184:187], v[220:223], v[188:191], v[184:187]\n\t"
            "v_mfma_f32_16x16x32_bf16 v[148:151], v[28:31], v[208:211], 0\n\t"
            "ds_read_b128 v[208:211], v233 offset:128\n\t"
            "v_mfma_f32_16x16x32_bf16 v[148:151], v[24:27], v[204:207], v[148:151]\n\t"
            "ds_read_b128 v[204:207], v232 offset:128\n\t"
            "v_rcp_f32 v201, v200\n\t"
            "v_mfma_f32_16x16x32_bf16 v[188:191], v[192:195], v[180:183], 0\n\t"
            "v_mul_f32 v224, v201, v224\n\t"
            "v_mul_f32 v225, v201, v225\n\t"
            "v_mfma_f32_16x16x32_bf16 v[188:191], v[196:199], v[172:175], v[188:191]\n\t"
            "v_mul_f32 v226, v201, v226\n\t"
            "v_mul_f32 v227, v201, v227\n\t"
            "v_mul_f32 v212, v201, v212\n\t"
            "v_mul_f32 v213, v201, v213\n\t"
            "v_mul_f32 v214, v201, v214\n\t"
            "v_mul_f32 v215, v201, v215\n\t"
            "v_mul_f32 v184, v201, v184\n\t"
            "v_mul_f32 v185, v201, v185\n\t"
            "v_mul_f32 v186, v201, v186\n\t"
            "v_mul_f32 v187, v201, v187\n\t"
            "v_mfma_f32_16x16x32_bf16 v[200:203], v[240:243], v[180:183], 0\n\t"
            "v_cvt_pk_bf16_f32 v224, v224, v225\n\t"
            "v_cvt_pk_bf16_f32 v225, v226, v227\n\t"
            "v_mfma_f32_16x16x32_bf16 v[200:203], v[240:243], v[172:175], v[200:203]\n\t"
            "ds_write_b64 v245, v[224:225] offset:6144\n\t"
            "v_mfma_f32_16x16x32_bf16 v[224:227], v[168:171], v[180:183], 0\n\t"
            "v_cvt_pk_bf16_f32 v184, v184, v185\n\t"
            "v_cvt_pk_bf16_f32 v185, v186, v187\n\t"
            "v_mfma_f32_16x16x32_bf16 v[224:227], v[176:179], v[172:175], v[224:227]\n\t"
            "ds_read_b64 v[186:187], v234 offset:288\n\t"
            "ds_write_b64 v245, v[184:185] offset:6400\n\t"
            "s_waitcnt lgkmcnt(3)\n\t"
            "v_mfma_f32_16x16x32_bf16 v[144:147], v[8:11], v[204:207], v[144:147]\n\t"
            "ds_read_b64 v[184:185], v234 offset:256\n\t"
            "v_rcp_f32 v201, v200\n\t"
            "v_mfma_f32_16x16x32_bf16 v[144:147], v[12:15], v[208:211], v[144:147]\n\t"
            "v_mul_f32 v188, v201, v188\n\t"
            "v_mul_f32 v189, v201, v189\n\t"
            "v_mfma_f32_16x16x32_bf16 v[148:151], v[36:39], v[208:211], v[148:151]\n\t"
            "v_mul_f32 v190, v201, v190\n\t"
            "v_mul_f32 v191, v201, v191\n\t"
            "v_mfma_f32_16x16x32_bf16 v[148:151], v[32:35], v[204:207], v[148:151]\n\t"
            "v_cvt_pk_bf16_f32 v188, v188, v189\n\t"
            "v_cvt_pk_bf16_f32 v189, v190, v191\n\t"
            "v_mfma_f32_16x16x32_bf16 v[152:155], v[56:59], v[204:207], v[152:155]\n\t"
            "ds_read_b64 v[190:191], v234 offset:352\n\t"
            "v_mul_f32 v224, v201, v224\n\t"
            "v_mfma_f32_16x16x32_bf16 v[152:155], v[60:63], v[208:211], v[152:155]\n\t"
            "ds_write_b64 v245, v[188:189] offset:12416\n\t"
            "v_mfma_f32_16x16x32_bf16 v[156:159], v[84:87], v[208:211], v[156:159]\n\t"
            "ds_read_b64 v[188:189], v234 offset:320\n\t"
            "v_mul_f32 v225, v201, v225\n\t"
            "v_mfma_f32_16x16x32_bf16 v[156:159], v[80:83], v[204:207], v[156:159]\n\t"
            "v_cvt_pk_bf16_f32 v212, v212, v213\n\t"
            "v_mul_f32 v226, v201, v226\n\t"
            "v_mfma_f32_16x16x32_bf16 v[160:163], v[104:107], v[204:207], v[160:163]\n\t"
            "v_mul_f32 v227, v201, v227\n\t"
            "v_cvt_pk_bf16_f32 v224, v224, v225\n\t"
            "v_mfma_f32_16x16x32_bf16 v[160:163], v[108:111], v[208:211], v[160:163]\n\t"
            "v_cvt_pk_bf16_f32 v213, v214, v215\n\t"
            "v_cvt_pk_bf16_f32 v225, v226, v227\n\t"
            "v_mfma_f32_16x16x32_bf16 v[164:167], v[132:135], v[208:211], v[164:167]\n\t"
            "ds_read_b128 v[208:211], v233 offset:6144\n\t"
            "v_mfma_f32_16x16x32_bf16 v[164:167], v[128:131], v[204:207], v[164:167]\n\t"
            "ds_read_b128 v[204:207], v232 offset:6144\n\t"
            "ds_write_b64 v245, v[212:213] offset:6272\n\t"
            "v_mfma_f32_16x16x32_bf16 v[180:183], v[216:219], v[180:183], 0\n\t"
            "ds_read_b128 v[212:215], v232 offset:256\n\t"
            "v_mfma_f32_16x16x32_bf16 v[180:183], v[220:223], v[172:175], v[180:183]\n\t"
            "ds_write_b64 v245, v[224:225] offset:12288\n\t"
            "ds_read_b128 v[224:227], v233 offset:256\n\t"
            "s_waitcnt lgkmcnt(6)\n\t"
            "v_mfma_f32_16x16x32_bf16 v[168:171], v[184:187], v[168:171], 0\n\t"
            "v_mfma_f32_16x16x32_bf16 v[168:171], v[188:191], v[176:179], v[168:171]\n\t"
            "v_mfma_f32_16x16x32_bf16 v[192:195], v[184:187], v[192:195], 0\n\t"
            "v_mfma_f32_16x16x32_bf16 v[192:195], v[188:191], v[196:199], v[192:195]\n\t"
            "s_nop 0\n\t"
            "v_mul_f32 v180, v201, v180\n\t"
            "v_mfma_f32_16x16x32_bf16 v[216:219], v[184:187], v[216:219], 0\n\t"
            "v_mul_f32 v181, v201, v181\n\t"
            "v_mul_f32 v182, v201, v182\n\t"
            "v_mfma_f32_16x16x32_bf16 v[216:219], v[188:191], v[220:223], v[216:219]\n\t"
            "v_mul_f32 v183, v201, v183\n\t"
            "v_cvt_pk_bf16_f32 v180, v180, v181\n\t"
            "v_mfma_f32_16x16x32_bf16 v[200:203], v[240:243], v[184:187], 0\n\t"
            "v_cvt_pk_bf16_f32 v181, v182, v183\n\t"
            "v_mfma_f32_16x16x32_bf16 v[200:203], v[240:243], v[188:191], v[200:203]\n\t"
            "ds_write_b64 v245, v[180:181] offset:12544\n\t"
            "s_waitcnt lgkmcnt(1)\n\t"
            "v_mfma_f32_16x16x32_bf16 v[144:147], v[16:19], v[212:215], v[144:147]\n\t"
            "v_mfma_f32_16x16x32_bf16 v[144:147], v[20:23], v[224:227], v[144:147]\n\t"
            "v_mfma_f32_16x16x32_bf16 v[148:151], v[44:47], v[224:227], v[148:151]\n\t"
            "v_mfma_f32_16x16x32_bf16 v[148:151], v[40:43], v[212:215], v[148:151]\n\t"
            "v_mfma_f32_16x16x32_bf16 v[152:155], v[64:67], v[212:215], v[152:155]\n\t"
            "s_nop 0\n\t"
            "v_cndmask_b32_e64 v203, v168, v192, s[60:61]\n\t"
            "v_mfma_f32_16x16x32_bf16 v[152:155], v[68:71], v[224:227], v[152:155]\n\t"
            "v_rcp_f32 v201, v200\n\t"
            "v_mfma_f32_16x16x32_bf16 v[156:159], v[92:95], v[224:227], v[156:159]\n\t"
            "v_cndmask_b32_e64 v203, v203, v216, s[62:63]\n\t"
            "v_xad_u32 v202, s73, v254, v255\n\t"
            "v_mfma_f32_16x16x32_bf16 v[156:159], v[88:91], v[212:215], v[156:159]\n\t"
            "v_mul_f32_dpp v203, v201, v203 quad_perm:[0,0,0,0] row_mask:0xf bank_mask:0xf\n\t"
            "v_cvt_pk_bf16_f32 v203, v203, 0\n\t"
            "v_mfma_f32_16x16x32_bf16 v[160:163], v[112:115], v[212:215], v[160:163]\n\t"
            "v_mul_f32 v144, v236, v144\n\t"
            "v_mul_f32 v145, v236, v145\n\t"
            "v_mfma_f32_16x16x32_bf16 v[160:163], v[116:119], v[224:227], v[160:163]\n\t"
            "s_mov_b64 exec, s[68:69]\n\t"
            "ds_write_b16 v202, v203\n\t"
            "s_mov_b64 exec, 1\n\t"
            "ds_add_u32 v253, v246 offset:7856\n\t"
            "s_mov_b64 exec, -1\n\t"
            "v_mfma_f32_16x16x32_bf16 v[164:167], v[140:143], v[224:227], v[164:167]\n\t"
            "ds_read_b128 v[224:227], v233 offset:6272\n\t"
            "v_mul_f32 v152, v236, v152\n\t"
            "v_mfma_f32_16x16x32_bf16 v[164:167], v[136:139], v[212:215], v[164:167]\n\t"
            "ds_read_b128 v[212:215], v232 offset:6272\n\t"
            "v_mul_f32 v153, v236, v153\n\t"
            "v_mfma_f32_16x16x32_bf16 v[168:171], v[0:3], v[204:207], 0\n\t"
            "v_mul_f32 v146, v236, v146\n\t"
            "v_mul_f32 v147, v236, v147\n\t"
            "v_mfma_f32_16x16x32_bf16 v[168:171], v[4:7], v[208:211], v[168:171]\n\t"
            "v_mul_f32 v154, v236, v154\n\t"
            "v_mul_f32 v155, v236, v155\n\t"
            "v_mfma_f32_16x16x32_bf16 v[172:175], v[28:31], v[208:211], 0\n\t"
            "v_cvt_pk_bf16_f32 v144, v144, v145\n\t"
            "v_cvt_pk_bf16_f32 v152, v152, v153\n\t"
            "v_mfma_f32_16x16x32_bf16 v[172:175], v[24:27], v[204:207], v[172:175]\n\t"
            "v_cvt_pk_bf16_f32 v156, v156, v157\n\t"
            "v_mul_f32 v148, v236, v148\n\t"
            "v_mfma_f32_16x16x32_bf16 v[176:179], v[48:51], v[204:207], 0\n\t"
            "v_mul_f32 v149, v236, v149\n\t"
            "v_mul_f32 v150, v236, v150\n\t"
            "v_mfma_f32_16x16x32_bf16 v[176:179], v[52:55], v[208:211], v[176:179]\n\t"
            "v_mul_f32 v151, v236, v151\n\t"
            "v_cvt_pk_bf16_f32 v145, v146, v147\n\t"
            "v_mfma_f32_16x16x32_bf16 v[180:183], v[76:79], v[208:211], 0\n\t"
            "v_cvt_pk_bf16_f32 v153, v154, v155\n\t"
            "v_cvt_pk_bf16_f32 v157, v158, v159\n\t"
            "v_mfma_f32_16x16x32_bf16 v[180:183], v[72:75], v[204:207], v[180:183]\n\t"
            "v_cvt_pk_bf16_f32 v146, v148, v149\n\t"
            "v_cvt_pk_bf16_f32 v147, v150, v151\n\t"
            "v_mfma_f32_16x16x32_bf16 v[184:187], v[96:99], v[204:207], 0\n\t"
            "v_cvt_pk_bf16_f32 v158, v160, v161\n\t"
            "v_cvt_pk_bf16_f32 v159, v162, v163\n\t"
            "v_mfma_f32_16x16x32_bf16 v[184:187], v[100:103], v[208:211], v[184:187]\n\t"
            "v_cvt_pk_bf16_f32 v154, v164, v165\n\t"
            "v_cvt_pk_bf16_f32 v155, v166, v167\n\t"
            "v_mfma_f32_16x16x32_bf16 v[188:191], v[124:127], v[208:211], 0\n\t"
            "ds_read_b128 v[208:211], v233 offset:6400\n\t"
            "v_mul_f32 v236, 0x3e553b94, v236\n\t"
            "v_mfma_f32_16x16x32_bf16 v[188:191], v[120:123], v[204:207], v[188:191]\n\t"
            "ds_read_b128 v[204:207], v232 offset:6400\n\t"
            "s_waitcnt lgkmcnt(2)\n\t"
            "v_mfma_f32_16x16x32_bf16 v[168:171], v[8:11], v[212:215], v[168:171]\n\t"
            "v_mfma_f32_16x16x32_bf16 v[168:171], v[12:15], v[224:227], v[168:171]\n\t"
            "v_mfma_f32_16x16x32_bf16 v[172:175], v[36:39], v[224:227], v[172:175]\n\t"
            "v_mfma_f32_16x16x32_bf16 v[172:175], v[32:35], v[212:215], v[172:175]\n\t"
            "v_mfma_f32_16x16x32_bf16 v[176:179], v[56:59], v[212:215], v[176:179]\n\t"
            "v_mfma_f32_16x16x32_bf16 v[176:179], v[60:63], v[224:227], v[176:179]\n\t"
            "v_mfma_f32_16x16x32_bf16 v[180:183], v[84:87], v[224:227], v[180:183]\n\t"
            "v_mfma_f32_16x16x32_bf16 v[180:183], v[80:83], v[212:215], v[180:183]\n\t"
            "v_mfma_f32_16x16x32_bf16 v[184:187], v[104:107], v[212:215], v[184:187]\n\t"
            "v_mfma_f32_16x16x32_bf16 v[184:187], v[108:111], v[224:227], v[184:187]\n\t"
            "v_mfma_f32_16x16x32_bf16 v[188:191], v[132:135], v[224:227], v[188:191]\n\t"
            "ds_read_b128 v[224:227], v233 offset:12288\n\t"
            "v_mfma_f32_16x16x32_bf16 v[188:191], v[128:131], v[212:215], v[188:191]\n\t"
            "ds_read_b128 v[212:215], v232 offset:12288\n\t"
            "s_waitcnt lgkmcnt(2)\n\t"
            "v_mfma_f32_16x16x32_bf16 v[168:171], v[16:19], v[204:207], v[168:171]\n\t"
            "v_mfma_f32_16x16x32_bf16 v[168:171], v[20:23], v[208:211], v[168:171]\n\t"
            "v_mfma_f32_16x16x32_bf16 v[172:175], v[44:47], v[208:211], v[172:175]\n\t"
            "v_mfma_f32_16x16x32_bf16 v[172:175], v[40:43], v[204:207], v[172:175]\n\t"
            "v_mfma_f32_16x16x32_bf16 v[176:179], v[64:67], v[204:207], v[176:179]\n\t"
            "v_mfma_f32_16x16x32_bf16 v[176:179], v[68:71], v[208:211], v[176:179]\n\t"
            "v_mfma_f32_16x16x32_bf16 v[180:183], v[92:95], v[208:211], v[180:183]\n\t"
            "v_mfma_f32_16x16x32_bf16 v[180:183], v[88:91], v[204:207], v[180:183]\n\t"
            "v_mfma_f32_16x16x32_bf16 v[184:187], v[112:115], v[204:207], v[184:187]\n\t"
            "v_mfma_f32_16x16x32_bf16 v[184:187], v[116:119], v[208:211], v[184:187]\n\t"
            "v_mfma_f32_16x16x32_bf16 v[188:191], v[140:143], v[208:211], v[188:191]\n\t"
            "ds_read_b128 v[208:211], v233 offset:12416\n\t"
            "v_mfma_f32_16x16x32_bf16 v[188:191], v[136:139], v[204:207], v[188:191]\n\t"
            "ds_read_b128 v[204:207], v232 offset:12416\n\t"
            "s_cmp_eq_u32 s91, 0\n\t"
            "s_cbranch_scc1 .Lwa_loop%=\n\t"
            "s_mov_b32 s91, 0\n\t"
            "s_waitcnt vmcnt(0)\n\t"
            "s_cmp_eq_u32 s81, 0\n\t"
            "s_cbranch_scc1 .Lwa_h6%=\n\t"
            "s_mov_b32 s89, 0x10000\n\t"
            ".Lwa_h4%=:\n\t"
            "v_readfirstlane_b32 s49, v228\n\t"
            "v_readfirstlane_b32 vcc_lo, v229\n\t"
            "s_xor_b32 s49, s49, s82\n\t"
            "s_xor_b32 vcc_lo, vcc_lo, s83\n\t"
            "s_or_b32 s49, s49, vcc_lo\n\t"
            "s_cbranch_scc0 .Lwa_h5%=\n\t"
            "s_sub_u32 s89, s89, 1\n\t"
            "s_cbranch_scc1 .Lwa_h5%=\n\t"
            "s_sleep 2\n\t"
            "buffer_load_dwordx3 v[228:230], off, s[84:87], s81 offset:1216 sc1\n\t"
            "s_waitcnt vmcnt(0)\n\t"
            "s_branch .Lwa_h4%=\n\t"
            ".Lwa_h5%=:\n\t"
            "s_lshl_b32 s49, s70, 2\n\t"
            "s_add_u32 s49, s49, 163520\n\t"
            "v_mov_b32 v228, s49\n\t"
            "s_mul_i32 s49, s70, 800\n\t"
            "s_add_u32 m0, s49, 113760\n\t"
            "ds_write_b32 v228, v230 offset:12\n\t"
            "s_mov_b32 exec_hi, 0x3ffff\n\t"
            "buffer_load_dwordx4 v248, s[84:87], s81 offen sc1 lds\n\t"
            "s_mul_i32 s49, s70, 384\n\t"
            "s_add_u32 m0, s49, 130176\n\t"
            "s_add_u32 s81, s81, 800\n\t"
            "s_mov_b64 exec, 0xffffff\n\t"
            "buffer_load_dwordx4 v248, s[84:87], s81 offen sc1 lds\n\t"
            "s_mov_b64 exec, -1\n\t"
            "s_mov_b32 s81, 0\n\t"
            "s_waitcnt lgkmcnt(0)\n\t"
            ".Lwa_h6%=:\n\t"
            "s_cmp_eq_u32 s90, 0\n\t"
            "s_cbranch_scc1 .Lwa_loop%=\n\t"
            "v_readfirstlane_b32 s49, v231\n\t"
            "s_sub_u32 s90, s90, 1\n\t"
            "s_cmp_lt_u32 s49, 209\n\t"
            "s_cselect_b32 s90, s90, 0\n\t"
            "s_cbranch_scc0 .Lwa_loop%=\n\t"
            "s_mul_i32 s81, s49, 1280\n\t"
            "s_add_u32 s81, s81, 303104\n\t"
            "s_branch .Lwa_loop%=\n\t"
            ".Lwa_h1%=:\n\t"
            "s_cmp_lt_u32 s70, 20\n\t"
            "s_cbranch_scc1 .Lwa_h2%=\n\t"
            "s_lshl_b32 s49, s70, 2\n\t"
            "s_add_u32 s49, s49, 163520\n\t"
            "v_mov_b32 v228, s49\n\t"
            "ds_read_b32 v228, v228\n\t"
            "s_waitcnt lgkmcnt(0)\n\t"
            "v_readfirstlane_b32 s49, v228\n\t"
            "s_cmp_eq_u32 s49, 0x7ffffff\n\t"
            "s_cbranch_scc1 .Lwa_last%=\n\t"
            "s_branch .Lwa_r1%=\n\t"
            ".Lwa_h2%=:\n\t"
            "s_waitcnt vmcnt(0)\n\t"
            "s_or_b32 s91, s81, s90\n\t"
            "s_cmp_eq_u32 s81, 0\n\t"
            "s_cbranch_scc1 .Lwa_h3%=\n\t"
            "buffer_load_dwordx3 v[228:230], off, s[84:87], s81 offset:1216 sc1\n\t"
            ".Lwa_h3%=:\n\t"
            "s_cmp_eq_u32 s90, 0\n\t"
            "s_cbranch_scc1 .Lwa_r1%=\n\t"
            "v_mov_b32 v231, 1\n\t"
            "s_mov_b32 s49, 301184\n\t"
            "s_mov_b64 exec, 1\n\t"
            "buffer_atomic_add v231, off, s[84:87], s49 sc0\n\t"
            "s_mov_b64 exec, -1\n\t"
            "s_branch .Lwa_r1%=\n\t"
            ".Lwa_last%=:\n\t"
            "s_setprio 2\n\t"
            "s_waitcnt lgkmcnt(0)\n\t"
            "s_nop 7\n\t"
            "v_mfma_f32_16x16x32_bf16 v[156:159], v[240:243], v[184:187], 0\n\t"
            "ds_read_b64 v[176:177], v247\n\t"
            "ds_read_b64 v[196:197], v247 offset:1536\n\t"
            "v_mfma_f32_16x16x32_bf16 v[156:159], v[240:243], v[188:191], v[156:159]\n\t"
            "ds_read_b64 v[220:221], v247 offset:3072\n\t"
            "v_xor_b32 v234, s73, v254\n\t"
            "v_mfma_f32_16x16x32_bf16 v[228:231], v[240:243], v[148:151], 0\n\t"
            "ds_read_u16 v178, v234\n\t"
            "ds_read_u16 v198, v234 offset:128\n\t"
            "v_mfma_f32_16x16x32_bf16 v[228:231], v[240:243], v[164:167], v[228:231]\n\t"
            "ds_read_u16 v222, v234 offset:256\n\t"
            "v_add_u32 v235, s45, v248\n\t"
            "v_mfma_f32_16x16x32_bf16 v[208:211], v[240:243], v[180:183], 0\n\t"
            "ds_read_b128 v[168:171], v235\n\t"
            "ds_read_b128 v[192:195], v235 offset:1536\n\t"
            "v_mfma_f32_16x16x32_bf16 v[208:211], v[240:243], v[172:175], v[208:211]\n\t"
            "ds_read_b128 v[216:219], v235 offset:3072\n\t"
            "v_mov_b32 v179, 0\n\t"
            "v_mov_b32 v199, 0\n\t"
            "v_mov_b32 v223, 0\n\t"
            "v_rcp_f32 v157, v156\n\t"
            "v_mad_u32_u24 v234, v246, s75, v253\n\t"
            "v_rcp_f32 v229, v228\n\t"
            "s_nop 0\n\t"
            "v_rcp_f32 v209, v208\n\t"
            "s_waitcnt lgkmcnt(2)\n\t"
            "v_mfma_f32_16x16x32_bf16 v[144:147], v[168:171], v[184:187], 0\n\t"
            "v_mfma_f32_16x16x32_bf16 v[144:147], v[176:179], v[188:191], v[144:147]\n\t"
            "s_waitcnt lgkmcnt(1)\n\t"
            "v_mfma_f32_16x16x32_bf16 v[152:155], v[192:195], v[184:187], 0\n\t"
            "v_mfma_f32_16x16x32_bf16 v[152:155], v[196:199], v[188:191], v[152:155]\n\t"
            "s_waitcnt lgkmcnt(0)\n\t"
            "v_mfma_f32_16x16x32_bf16 v[184:187], v[216:219], v[184:187], 0\n\t"
            "v_mfma_f32_16x16x32_bf16 v[184:187], v[220:223], v[188:191], v[184:187]\n\t"
            "ds_read_b64 v[188:189], v234 offset:320\n\t"
            "ds_read_b64 v[190:191], v234 offset:352\n\t"
            "v_mfma_f32_16x16x32_bf16 v[224:227], v[168:171], v[148:151], 0\n\t"
            "v_mul_f32 v144, v157, v144\n\t"
            "v_mul_f32 v145, v157, v145\n\t"
            "v_mfma_f32_16x16x32_bf16 v[224:227], v[176:179], v[164:167], v[224:227]\n\t"
            "v_mul_f32 v152, v157, v152\n\t"
            "v_mul_f32 v153, v157, v153\n\t"
            "v_mfma_f32_16x16x32_bf16 v[212:215], v[192:195], v[148:151], 0\n\t"
            "v_mul_f32 v184, v157, v184\n\t"
            "v_mul_f32 v185, v157, v185\n\t"
            "v_mfma_f32_16x16x32_bf16 v[212:215], v[196:199], v[164:167], v[212:215]\n\t"
            "v_mul_f32 v186, v157, v186\n\t"
            "v_mul_f32 v187, v157, v187\n\t"
            "v_mfma_f32_16x16x32_bf16 v[160:163], v[168:171], v[180:183], 0\n\t"
            "v_cvt_pk_bf16_f32 v184, v184, v185\n\t"
            "v_cvt_pk_bf16_f32 v185, v186, v187\n\t"
            "v_mfma_f32_16x16x32_bf16 v[160:163], v[176:179], v[172:175], v[160:163]\n\t"
            "ds_read_b64 v[186:187], v234 offset:288\n\t"
            "ds_write_b64 v245, v[184:185] offset:6400\n\t"
            "v_mfma_f32_16x16x32_bf16 v[204:207], v[192:195], v[180:183], 0\n\t"
            "ds_read_b64 v[184:185], v234 offset:256\n\t"
            "v_mul_f32 v224, v229, v224\n\t"
            "v_mfma_f32_16x16x32_bf16 v[204:207], v[196:199], v[172:175], v[204:207]\n\t"
            "v_mul_f32 v225, v229, v225\n\t"
            "v_mul_f32 v212, v229, v212\n\t"
            "v_mfma_f32_16x16x32_bf16 v[148:151], v[216:219], v[148:151], 0\n\t"
            "v_mul_f32 v213, v229, v213\n\t"
            "v_mul_f32 v160, v209, v160\n\t"
            "v_mfma_f32_16x16x32_bf16 v[148:151], v[220:223], v[164:167], v[148:151]\n\t"
            "v_mul_f32 v161, v209, v161\n\t"
            "v_mul_f32 v226, v229, v226\n\t"
            "v_mfma_f32_16x16x32_bf16 v[180:183], v[216:219], v[180:183], 0\n\t"
            "v_mul_f32 v204, v209, v204\n\t"
            "v_mul_f32 v205, v209, v205\n\t"
            "v_mfma_f32_16x16x32_bf16 v[180:183], v[220:223], v[172:175], v[180:183]\n\t"
            "v_mul_f32 v227, v229, v227\n\t"
            "v_cvt_pk_bf16_f32 v224, v224, v225\n\t"
            "v_mul_f32 v148, v229, v148\n\t"
            "v_mul_f32 v149, v229, v149\n\t"
            "v_mul_f32 v214, v229, v214\n\t"
            "v_mul_f32 v215, v229, v215\n\t"
            "v_cvt_pk_bf16_f32 v212, v212, v213\n\t"
            "v_mul_f32 v150, v229, v150\n\t"
            "v_mul_f32 v180, v209, v180\n\t"
            "v_mul_f32 v181, v209, v181\n\t"
            "v_mul_f32 v151, v229, v151\n\t"
            "v_cvt_pk_bf16_f32 v148, v148, v149\n\t"
            "v_mul_f32 v146, v157, v146\n\t"
            "v_mul_f32 v147, v157, v147\n\t"
            "v_cvt_pk_bf16_f32 v144, v144, v145\n\t"
            "v_mul_f32 v154, v157, v154\n\t"
            "v_mul_f32 v155, v157, v155\n\t"
            "s_waitcnt lgkmcnt(0)\n\t"
            "v_mfma_f32_16x16x32_bf16 v[200:203], v[240:243], v[184:187], 0\n\t"
            "v_cvt_pk_bf16_f32 v152, v152, v153\n\t"
            "v_mul_f32 v162, v209, v162\n\t"
            "v_mfma_f32_16x16x32_bf16 v[200:203], v[240:243], v[188:191], v[200:203]\n\t"
            "v_mul_f32 v163, v209, v163\n\t"
            "v_cvt_pk_bf16_f32 v160, v160, v161\n\t"
            "v_mfma_f32_16x16x32_bf16 v[168:171], v[184:187], v[168:171], 0\n\t"
            "v_mul_f32 v206, v209, v206\n\t"
            "v_mul_f32 v207, v209, v207\n\t"
            "v_mfma_f32_16x16x32_bf16 v[168:171], v[188:191], v[176:179], v[168:171]\n\t"
            "v_cvt_pk_bf16_f32 v204, v204, v205\n\t"
            "v_mul_f32 v182, v209, v182\n\t"
            "v_mfma_f32_16x16x32_bf16 v[192:195], v[184:187], v[192:195], 0\n\t"
            "v_rcp_f32 v201, v200\n\t"
            "v_mfma_f32_16x16x32_bf16 v[192:195], v[188:191], v[196:199], v[192:195]\n\t"
            "v_mul_f32 v183, v209, v183\n\t"
            "v_cvt_pk_bf16_f32 v180, v180, v181\n\t"
            "v_mfma_f32_16x16x32_bf16 v[216:219], v[184:187], v[216:219], 0\n\t"
            "v_cvt_pk_bf16_f32 v225, v226, v227\n\t"
            "v_cvt_pk_bf16_f32 v213, v214, v215\n\t"
            "v_mfma_f32_16x16x32_bf16 v[216:219], v[188:191], v[220:223], v[216:219]\n\t"
            "v_cvt_pk_bf16_f32 v149, v150, v151\n\t"
            "v_cvt_pk_bf16_f32 v145, v146, v147\n\t"
            "v_cndmask_b32_e64 v203, v168, v192, s[60:61]\n\t"
            "v_cvt_pk_bf16_f32 v153, v154, v155\n\t"
            "v_cvt_pk_bf16_f32 v161, v162, v163\n\t"
            "v_cvt_pk_bf16_f32 v205, v206, v207\n\t"
            "v_cvt_pk_bf16_f32 v181, v182, v183\n\t"
            "v_xad_u32 v202, s73, v254, v255\n\t"
            "v_cndmask_b32_e64 v203, v203, v216, s[62:63]\n\t"
            "ds_write_b64 v245, v[224:225]\n\t"
            "ds_write_b64 v245, v[212:213] offset:128\n\t"
            "ds_write_b64 v245, v[148:149] offset:256\n\t"
            "ds_write_b64 v245, v[144:145] offset:6144\n\t"
            "ds_write_b64 v245, v[152:153] offset:6272\n\t"
            "ds_write_b64 v245, v[160:161] offset:12288\n\t"
            "ds_write_b64 v245, v[204:205] offset:12416\n\t"
            "ds_write_b64 v245, v[180:181] offset:12544\n\t"
            "v_mul_f32_dpp v203, v201, v203 quad_perm:[0,0,0,0] row_mask:0xf bank_mask:0xf\n\t"
            "v_cvt_pk_bf16_f32 v203, v203, 0\n\t"
            "s_mov_b64 exec, s[68:69]\n\t"
            "ds_write_b16 v202, v203\n\t"
            "s_mov_b64 exec, 1\n\t"
            "ds_add_u32 v253, v246 offset:7856\n\t"
            "s_mov_b64 exec, -1\n\t"
            ".Lwa_done%=:\n\t"
            "s_waitcnt lgkmcnt(0)\n\t"
            "s_nop 7\n\t"
            : "+{v249}"(aRSw_), "+{s48}"(s48), "+{s70}"(s70), "+{s72}"(s72), "+{s73}"(s73), "+{s74}"(s74), "+{s76}"(s76), "+{s77}"(s77)
            : "{v244}"(pE_), "{v245}"(aO_), "{v246}"(sel_), "{v247}"(aVB_), "{v248}"(pST_), "{v250}"(aPLw_), "{v251}"(aRSr_), "{v252}"(g16_), "{v253}"(g8_), "{v254}"(loVL_), "{v255}"(loOL_), "{v[240:243]}"(ones4), "{s47}"((ldsb + X_OFF) ^ (ldsb + X1_OFF)),
              "{s[58:59]}"(0xFFFFull), "{s[60:61]}"(0xFFFF0000ull), "{s[62:63]}"(0xFFFF00000000ull), "{s[64:65]}"(0xFFFF000000000000ull),
              "{s[66:67]}"(0x1FFFFFFFFFFFFull), "{s75}"(rsb), "{s45}"(ldsb + V_OFF + w * 4608), "{s[68:69]}"(0xFFFFFFFFFFFFull),
              "{s[84:87]}"(wr), "{s46}"(wave * 36864), "{s88}"(touch), "{s80}"(wave == 0 ? 16 : 20), "{s82}"(tag_lo), "{s83}"(tag_hi), "{s71}"(ldsb + O_OFF), "{s[54:55]}"(mrow), "{s[56:57]}"(0xF0000F0000F0000Full),
              "{s[78:79]}"(mrow & 0xF0000F0000F0000Full)
            : "v144", "v145", "v146", "v147", "v148", "v149", "v150", "v151", "v152", "v153", "v154", "v155", "v156", "v157", "v158", "v159", "v160", "v161", "v162", "v163", "v164", "v165", "v166", "v167", "v168", "v169", "v170", "v171", "v172", "v173", "v174", "v175", "v176", "v177", "v178", "v179", "v180", "v181", "v182", "v183", "v184", "v185", "v186", "v187", "v188", "v189", "v190", "v191", "v192", "v193", "v194", "v195", "v196", "v197", "v198", "v199", "v200", "v201", "v202", "v203", "v204", "v205", "v206", "v207", "v208", "v209", "v210", "v211", "v212", "v213", "v214", "v215", "v216", "v217", "v218", "v219", "v220", "v221", "v222", "v223", "v224", "v225", "v226", "v227", "v228", "v229", "v230", "v231", "v232", "v233", "v234", "v235", "v236", "v237", "v238", "v239", "v0", "v1", "v2", "v3", "v4", "v5", "v6", "v7", "v8", "v9", "v10", "v11", "v12", "v13", "v14", "v15", "v16", "v17", "v18", "v19", "v20", "v21", "v22", "v23", "v24", "v25", "v26", "v27", "v28", "v29", "v30", "v31", "v32", "v33", "v34", "v35", "v36", "v37", "v38", "v39", "v40", "v41", "v42", "v43", "v44", "v45", "v46", "v47", "v48", "v49", "v50", "v51", "v52", "v53", "v54", "v55", "v56", "v57", "v58", "v59", "v60", "v61", "v62", "v63", "v64", "v65", "v66", "v67", "v68", "v69", "v70", "v71", "v72", "v73", "v74", "v75", "v76", "v77", "v78", "v79", "v80", "v81", "v82", "v83", "v84", "v85", "v86", "v87", "v88", "v89", "v90", "v91", "v92", "v93", "v94", "v95", "v96", "v97", "v98", "v99", "v100", "v101", "v102", "v103", "v104", "v105", "v106", "v107", "v108", "v109", "v110", "v111", "v112", "v113", "v114", "v115", "v116", "v117", "v118", "v119", "v120", "v121", "v122", "v123", "v124", "v125", "v126", "v127", "v128", "v129", "v130", "v131", "v132", "v133", "v134", "v135", "v136", "v137", "v138", "v139", "v140", "v141", "v142", "v143",  "m0", "s81", "s89", "s90", "s91", "s49", "vcc", "scc", "memory");

        }
        if (wave == 0 && blockIdx.x == 0) {
            int ln_;
            asm volatile("v_mbcnt_lo_u32_b32 %0, -1, 0\n\tv_mbcnt_hi_u32_b32 %0, -1, %0" : "=v"(ln_));
            KARGS
            unsigned long long tg;
            asm volatile("s_load_dwordx2 %0, %1, %2\n\ts_waitcnt lgkmcnt(0)" : "=s"(tg) : "s"(wb), "n"(EPO_OFF) : "memory");
            const unsigned lo = (unsigned)tg + 1u, hi = (unsigned)(tg >> 32);
            const auto wq = __builtin_amdgcn_make_buffer_rsrc((void*)wb, 0, WS_BYTES, 0x00020000);
            for (int it_ = 0; it_ < 100000; ++it_) {
                bool bad = false;
#pragma unroll
                for (int k = 0; k < 2; ++k) {
                    const u32x4 a_ = __builtin_amdgcn_raw_buffer_load_b128(wq, ARR_OFF + ln_ * 16, k * 1024, SC1);
                    bad |= a_[0] != lo || a_[1] != hi || a_[2] != lo || a_[3] != hi;
                }
                if (__builtin_amdgcn_ballot_w64(bad) == 0ull) break;
                __builtin_amdgcn_s_sleep(2);
            }
            if (ln_ == 0) __builtin_amdgcn_raw_buffer_store_b64((u32x2){lo, hi}, wq, EPO_OFF, 0, SC1);
        }
        asm volatile("wk_code_end:");
        __builtin_amdgcn_endpgm();
    } else if (wave < 6) {
        LANE_SETUP
        {
            const unsigned rsb = ldsb + RSX_OFF;
            const unsigned pE_ = fE - X_OFF;
            const unsigned aVAw_ = ldsb + V_OFF + w * 9216 + lane * 16, aVBw_ = ldsb + V_OFF + w * 9216 + 1024 + lane * 8;
            const unsigned pST_ = lane * 16;
            unsigned aRSw_ = rsb + 256 + lane * 4, aRSr_ = rsb + (lane & 48);
            B_SCALARS
            unsigned s44 = 4;
            asm volatile(
            "s_waitcnt lgkmcnt(0)\n\t"
            "s_mov_b32 s83, 0x10000\n\t"
            "s_mov_b32 s84, -47104\n\t"
            "s_mov_b64 s[88:89], s[72:73]\n\t"
            "s_mov_b64 s[90:91], s[74:75]\n\t"
            "s_nop 7\n\t"
            "s_setprio 3\n\t"
            "s_barrier\n\t"
            "buffer_load_dwordx4 v[140:143], v245, s[28:31], s82 offen sc1\n\t"
            "s_add_u32 s49, s33, 0\n\t"
            "buffer_load_dwordx4 v[0:3], v245, s[28:31], s49 offen sc1\n\t"
            "s_add_u32 s49, s33, 6144\n\t"
            "buffer_load_dwordx4 v[24:27], v245, s[28:31], s49 offen sc1\n\t"
            "s_add_u32 s49, s33, 12288\n\t"
            "buffer_load_dwordx4 v[48:51], v245, s[28:31], s49 offen sc1\n\t"
            "s_add_u32 s49, s33, 18432\n\t"
            "buffer_load_dwordx4 v[72:75], v245, s[28:31], s49 offen sc1\n\t"
            "s_add_u32 s49, s33, 24576\n\t"
            "buffer_load_dwordx4 v[96:99], v245, s[28:31], s49 offen sc1\n\t"
            "s_add_u32 s49, s33, 30720\n\t"
            "buffer_load_dwordx4 v[120:123], v245, s[28:31], s49 offen sc1\n\t"
            "s_add_u32 s49, s33, 1024\n\t"
            "buffer_load_dwordx4 v[4:7], v245, s[28:31], s49 offen sc1\n\t"
            "s_add_u32 s49, s33, 7168\n\t"
            "buffer_load_dwordx4 v[28:31], v245, s[28:31], s49 offen sc1\n\t"
            "s_add_u32 s49, s33, 13312\n\t"
            "buffer_load_dwordx4 v[52:55], v245, s[28:31], s49 offen sc1\n\t"
            "s_add_u32 s49, s33, 19456\n\t"
            "buffer_load_dwordx4 v[76:79], v245, s[28:31], s49 offen sc1\n\t"
            "s_add_u32 s49, s33, 25600\n\t"
            "buffer_load_dwordx4 v[100:103], v245, s[28:31], s49 offen sc1\n\t"
            "s_add_u32 s49, s33, 31744\n\t"
            "buffer_load_dwordx4 v[124:127], v245, s[28:31], s49 offen sc1\n\t"
            "s_add_u32 s49, s33, 2048\n\t"
            "buffer_load_dwordx4 v[8:11], v245, s[28:31], s49 offen sc1\n\t"
            "s_add_u32 s49, s33, 8192\n\t"
            "buffer_load_dwordx4 v[32:35], v245, s[28:31], s49 offen sc1\n\t"
            "s_add_u32 s49, s33, 14336\n\t"
            "buffer_load_dwordx4 v[56:59], v245, s[28:31], s49 offen sc1\n\t"
            "s_add_u32 s49, s33, 20480\n\t"
            "buffer_load_dwordx4 v[80:83], v245, s[28:31], s49 offen sc1\n\t"
            "s_add_u32 s49, s33, 26624\n\t"
            "buffer_load_dwordx4 v[104:107], v245, s[28:31], s49 offen sc1\n\t"
            "s_add_u32 s49, s33, 32768\n\t"
            "buffer_load_dwordx4 v[128:131], v245, s[28:31], s49 offen sc1\n\t"
            "s_add_u32 s49, s33, 3072\n\t"
            "buffer_load_dwordx4 v[12:15], v245, s[28:31], s49 offen sc1\n\t"
            "s_add_u32 s49, s33, 9216\n\t"
            "buffer_load_dwordx4 v[36:39], v245, s[28:31], s49 offen sc1\n\t"
            "s_add_u32 s49, s33, 15360\n\t"
            "buffer_load_dwordx4 v[60:63], v245, s[28:31], s49 offen sc1\n\t"
            "s_add_u32 s49, s33, 21504\n\t"
            "buffer_load_dwordx4 v[84:87], v245, s[28:31], s49 offen sc1\n\t"
            "s_add_u32 s49, s33, 27648\n\t"
            "buffer_load_dwordx4 v[108:111], v245, s[28:31], s49 offen sc1\n\t"
            "s_add_u32 s49, s33, 33792\n\t"
            "buffer_load_dwordx4 v[132:135], v245, s[28:31], s49 offen sc1\n\t"
            "s_add_u32 s49, s33, 4096\n\t"
            "buffer_load_dwordx4 v[16:19], v245, s[28:31], s49 offen sc1\n\t"
            "s_add_u32 s49, s33, 10240\n\t"
            "buffer_load_dwordx4 v[40:43], v245, s[28:31], s49 offen sc1\n\t"
            "s_add_u32 s49, s33, 16384\n\t"
            "buffer_load_dwordx4 v[64:67], v245, s[28:31], s49 offen sc1\n\t"
            "s_add_u32 s49, s33, 22528\n\t"
            "buffer_load_dwordx4 v[88:91], v245, s[28:31], s49 offen sc1\n\t"
            "s_add_u32 s49, s33, 28672\n\t"
            "buffer_load_dwordx4 v[112:115], v245, s[28:31], s49 offen sc1\n\t"
            "s_add_u32 s49, s33, 34816\n\t"
            "buffer_load_dwordx4 v[136:139], v245, s[28:31], s49 offen sc1\n\t"
            "s_add_u32 s49, s33, 5120\n\t"
            "buffer_load_dwordx4 v[20:23], v245, s[28:31], s49 offen sc1\n\t"
            "s_add_u32 s49, s33, 11264\n\t"
            "buffer_load_dwordx4 v[44:47], v245, s[28:31], s49 offen sc1\n\t"
            "s_add_u32 s49, s33, 17408\n\t"
            "buffer_load_dwordx4 v[68:71], v245, s[28:31], s49 offen sc1\n\t"
            "s_add_u32 s49, s33, 23552\n\t"
            "buffer_load_dwordx4 v[92:95], v245, s[28:31], s49 offen sc1\n\t"
            "s_add_u32 s49, s33, 29696\n\t"
            "buffer_load_dwordx4 v[116:119], v245, s[28:31], s49 offen sc1\n\t"
            "s_add_u32 s49, s33, 35840\n\t"
            "buffer_load_dwordx4 v[140:143], v245, s[28:31], s49 offen sc1\n\t"
            "ds_read_b32 v249, v252 offset:12\n\t"
            "s_waitcnt lgkmcnt(0)\n\t"
            "v_and_b32 v249, s69, v249\n\t"
            "s_mov_b32 exec_hi, 0x3fffff\n\t"
            "s_add_u32 m0, s66, 0\n\t"
            "s_add_u32 s49, s79, 0\n\t"
            "buffer_load_dwordx4 v249, s[72:75], s49 offen lds\n\t"
            "s_mov_b32 exec_hi, -1\n\t"
            "s_mov_b32 exec_hi, 0x3fffff\n\t"
            "s_add_u32 m0, s66, 872\n\t"
            "s_add_u32 s49, s47, 196224\n\t"
            "buffer_load_dwordx4 v249, s[72:75], s49 offen lds\n\t"
            "s_mov_b32 exec_hi, -1\n\t"
            "s_mov_b32 exec_hi, 0x3fffff\n\t"
            "s_add_u32 m0, s66, 1744\n\t"
            "s_add_u32 s49, s47, 392448\n\t"
            "buffer_load_dwordx4 v249, s[72:75], s49 offen lds\n\t"
            "s_mov_b32 exec_hi, -1\n\t"
            "s_mov_b32 exec_hi, 0x3fffff\n\t"
            "s_add_u32 m0, s66, 2616\n\t"
            "s_add_u32 s49, s47, 588672\n\t"
            "buffer_load_dwordx4 v249, s[72:75], s49 offen lds\n\t"
            "s_mov_b32 exec_hi, -1\n\t"
            "s_mov_b32 exec_hi, 0x3fffff\n\t"
            "s_add_u32 m0, s66, 3488\n\t"
            "s_add_u32 s49, s47, 784896\n\t"
            "buffer_load_dwordx4 v249, s[72:75], s49 offen lds\n\t"
            "s_mov_b32 exec_hi, -1\n\t"
            "s_mov_b32 exec_hi, 0x3fffff\n\t"
            "s_add_u32 m0, s66, 4360\n\t"
            "s_add_u32 s49, s47, 981120\n\t"
            "buffer_load_dwordx4 v249, s[72:75], s49 offen lds\n\t"
            "s_mov_b32 exec_hi, -1\n\t"
            "s_mov_b32 exec_hi, 0x3fffff\n\t"
            "s_add_u32 m0, s66, 5232\n\t"
            "s_add_u32 s49, s47, 1177344\n\t"
            "buffer_load_dwordx4 v249, s[72:75], s49 offen lds\n\t"
            "s_mov_b32 exec_hi, -1\n\t"
            "s_mov_b32 exec_hi, 0x3fffff\n\t"
            "s_add_u32 m0, s66, 6104\n\t"
            "s_add_u32 s49, s47, 1373568\n\t"
            "buffer_load_dwordx4 v249, s[72:75], s49 offen lds\n\t"
            "s_mov_b32 exec_hi, -1\n\t"
            "s_mov_b32 exec_hi, 0x3fffff\n\t"
            "s_add_u32 m0, s66, 6976\n\t"
            "s_add_u32 s49, s47, 1569792\n\t"
            "buffer_load_dwordx4 v249, s[72:75], s49 offen lds\n\t"
            "s_mov_b32 exec_hi, -1\n\t"
            "ds_read_b128 v[168:171], v254\n\t"
            "ds_read_b128 v[172:175], v254 offset:64\n\t"
            "v_add_u32 v180, s48, v245\n\t"
            "v_add_u32 v232, s48, v242\n\t"
            "v_xor_b32 v233, 64, v232\n\t"
            "v_xor_b32 v246, 0x100, v246\n\t"
            "ds_read_b128 v[176:179], v180 offset:18816\n\t"
            "ds_read_b128 v[180:183], v180 offset:19840\n\t"
            "ds_read_b128 v[192:195], v232\n\t"
            "ds_read_b128 v[196:199], v233\n\t"
            "ds_read_b128 v[224:227], v232 offset:128\n\t"
            "ds_read_b128 v[228:231], v233 offset:128\n\t"
            "s_waitcnt lgkmcnt(5)\n\t"
            "v_add_f32 v176, v176, v178\n\t"
            "s_waitcnt lgkmcnt(4)\n\t"
            "v_add_f32 v180, v180, v182\n\t"
            "v_add_f32 v176, v176, v180\n\t"
            "v_add_f32 v177, v177, v179\n\t"
            "v_add_f32 v181, v181, v183\n\t"
            "v_mul_f32 v176, 0x3baaaaab, v176\n\t"
            "v_add_f32 v177, v177, v181\n\t"
            "v_mul_f32_e64 v176, v176, -v176\n\t"
            "v_fmac_f32 v176, 0x3baaaaab, v177\n\t"
            "v_max_f32 v176, 0, v176\n\t"
            "v_add_f32 v176, 0x3727c5ac, v176\n\t"
            "v_rsq_f32 v176, v176\n\t"
            "s_nop 0\n\t"
            "ds_write_b32 v246, v176\n\t"
            "s_setprio 3\n\t"
            "s_waitcnt vmcnt(9)\n\t"
            "s_waitcnt lgkmcnt(3)\n\t"
            "v_mfma_f32_16x16x32_bf16 v[200:203], v[192:195], v[0:3], 0\n\t"
            "v_mfma_f32_16x16x32_bf16 v[200:203], v[196:199], v[4:7], v[200:203]\n\t"
            "v_mfma_f32_16x16x32_bf16 v[204:207], v[196:199], v[28:31], 0\n\t"
            "v_mfma_f32_16x16x32_bf16 v[204:207], v[192:195], v[24:27], v[204:207]\n\t"
            "v_mfma_f32_16x16x32_bf16 v[208:211], v[192:195], v[48:51], 0\n\t"
            "v_mfma_f32_16x16x32_bf16 v[208:211], v[196:199], v[52:55], v[208:211]\n\t"
            "v_mfma_f32_16x16x32_bf16 v[212:215], v[196:199], v[76:79], 0\n\t"
            "v_mfma_f32_16x16x32_bf16 v[212:215], v[192:195], v[72:75], v[212:215]\n\t"
            "v_mfma_f32_16x16x32_bf16 v[216:219], v[192:195], v[96:99], 0\n\t"
            "v_mfma_f32_16x16x32_bf16 v[216:219], v[196:199], v[100:103], v[216:219]\n\t"
            "v_mfma_f32_16x16x32_bf16 v[220:223], v[196:199], v[124:127], 0\n\t"
            "v_mfma_f32_16x16x32_bf16 v[220:223], v[192:195], v[120:123], v[220:223]\n\t"
            "s_waitcnt lgkmcnt(1)\n\t"
            "v_mfma_f32_16x16x32_bf16 v[200:203], v[224:227], v[8:11], v[200:203]\n\t"
            "v_mfma_f32_16x16x32_bf16 v[200:203], v[228:231], v[12:15], v[200:203]\n\t"
            "v_mfma_f32_16x16x32_bf16 v[204:207], v[228:231], v[36:39], v[204:207]\n\t"
            "v_mfma_f32_16x16x32_bf16 v[204:207], v[224:227], v[32:35], v[204:207]\n\t"
            "v_mfma_f32_16x16x32_bf16 v[208:211], v[224:227], v[56:59], v[208:211]\n\t"
            "v_mfma_f32_16x16x32_bf16 v[208:211], v[228:231], v[60:63], v[208:211]\n\t"
            "v_mfma_f32_16x16x32_bf16 v[212:215], v[228:231], v[84:87], v[212:215]\n\t"
            "v_mfma_f32_16x16x32_bf16 v[212:215], v[224:227], v[80:83], v[212:215]\n\t"
            "v_mfma_f32_16x16x32_bf16 v[216:219], v[224:227], v[104:107], v[216:219]\n\t"
            "s_waitcnt vmcnt(0)\n\t"
            "ds_read_b64 v[144:145], v253\n\t"
            "v_mfma_f32_16x16x32_bf16 v[216:219], v[228:231], v[108:111], v[216:219]\n\t"
            "ds_read_b64 v[146:147], v253 offset:96\n\t"
            "ds_read_b64 v[148:149], v253 offset:1744\n\t"
            "v_mfma_f32_16x16x32_bf16 v[220:223], v[228:231], v[132:135], v[220:223]\n\t"
            "ds_read_b128 v[228:231], v233 offset:256\n\t"
            "ds_read_b64 v[150:151], v253 offset:1840\n\t"
            "v_mfma_f32_16x16x32_bf16 v[220:223], v[224:227], v[128:131], v[220:223]\n\t"
            "ds_read_b128 v[224:227], v232 offset:256\n\t"
            "ds_read_b64 v[152:153], v253 offset:8\n\t"
            "ds_read_b64 v[154:155], v253 offset:104\n\t"
            "ds_read_b64 v[156:157], v253 offset:1752\n\t"
            "ds_read_b64 v[158:159], v253 offset:1848\n\t"
            "ds_read_b64 v[160:161], v253 offset:16\n\t"
            "ds_read_b64 v[162:163], v253 offset:112\n\t"
            "ds_read_b64 v[164:165], v253 offset:1760\n\t"
            "ds_read_b64 v[166:167], v253 offset:1856\n\t"
            "s_waitcnt lgkmcnt(0)\n\t"
            "s_nop 7\n\t"
            ".p2align 6\n\t"
            "s_nop 0\n\t"
            "s_nop 0\n\t"
            "s_nop 0\n\t"
            "s_nop 0\n\t"
            ".Lwc_loop%=:\n\t"
            "s_setprio 1\n\t"
            "s_setprio 3\n\t"
            "v_mfma_f32_16x16x32_bf16 v[200:203], v[224:227], v[16:19], v[200:203]\n\t"
            "v_mov_b64 v[176:177], 0\n\t"
            "v_mov_b64 v[178:179], 0\n\t"
            "v_mfma_f32_16x16x32_bf16 v[200:203], v[228:231], v[20:23], v[200:203]\n\t"
            "v_mov_b64 v[180:181], 0\n\t"
            "v_mov_b64 v[182:183], 0\n\t"
            "s_waitcnt lgkmcnt(0)\n\t"
            "v_smfmac_f32_16x16x64_bf16 v[176:179], v[168:171], v[144:151], v255\n\t"
            "ds_read_b128 v[168:171], v254 offset:128\n\t"
            "v_smfmac_f32_16x16x64_bf16 v[180:183], v[172:175], v[152:159], v255\n\t"
            "ds_read_b64 v[144:145], v253 offset:24\n\t"
            "v_mfma_f32_16x16x32_bf16 v[204:207], v[228:231], v[44:47], v[204:207]\n\t"
            "ds_read_b64 v[146:147], v253 offset:120\n\t"
            "ds_read_b64 v[148:149], v253 offset:1768\n\t"
            "v_mfma_f32_16x16x32_bf16 v[204:207], v[224:227], v[40:43], v[204:207]\n\t"
            "ds_read_b64 v[150:151], v253 offset:1864\n\t"
            "ds_read_b128 v[172:175], v254 offset:192\n\t"
            "v_mfma_f32_16x16x32_bf16 v[208:211], v[224:227], v[64:67], v[208:211]\n\t"
            "ds_read_b64 v[152:153], v253 offset:32\n\t"
            "ds_read_b64 v[154:155], v253 offset:128\n\t"
            "v_mfma_f32_16x16x32_bf16 v[208:211], v[228:231], v[68:71], v[208:211]\n\t"
            "ds_read_b64 v[156:157], v253 offset:1776\n\t"
            "ds_read_b64 v[158:159], v253 offset:1872\n\t"
            "v_mfma_f32_16x16x32_bf16 v[212:215], v[228:231], v[92:95], v[212:215]\n\t"
            "v_mov_b32 v249, s76\n\t"
            "v_mov_b64 v[184:185], 0\n\t"
            "v_mfma_f32_16x16x32_bf16 v[212:215], v[224:227], v[88:91], v[212:215]\n\t"
            "ds_read_b32 v249, v249\n\t"
            "v_mov_b64 v[186:187], 0\n\t"
            "v_mfma_f32_16x16x32_bf16 v[216:219], v[224:227], v[112:115], v[216:219]\n\t"
            "v_mov_b64 v[188:189], 0\n\t"
            "v_mov_b64 v[190:191], 0\n\t"
            "v_mfma_f32_16x16x32_bf16 v[216:219], v[228:231], v[116:119], v[216:219]\n\t"
            "v_cvt_pk_bf16_f32 v176, v176, v177\n\t"
            "v_add_u32 v236, s67, v250\n\t"
            "s_waitcnt lgkmcnt(10)\n\t"
            "v_smfmac_f32_16x16x64_bf16 v[184:187], v[168:171], v[160:167], v255\n\t"
            "ds_read_b128 v[168:171], v254 offset:256\n\t"
            "s_waitcnt lgkmcnt(6)\n\t"
            "v_smfmac_f32_16x16x64_bf16 v[188:191], v[172:175], v[144:151], v255\n\t"
            "ds_read_b64 v[160:161], v253 offset:40\n\t"
            "v_mfma_f32_16x16x32_bf16 v[220:223], v[228:231], v[140:143], v[220:223]\n\t"
            "ds_read_b64 v[162:163], v253 offset:136\n\t"
            "ds_read_b64 v[164:165], v253 offset:1784\n\t"
            "v_mfma_f32_16x16x32_bf16 v[220:223], v[224:227], v[136:139], v[220:223]\n\t"
            "s_setprio 1\n\t"
            "ds_read_b64 v[166:167], v253 offset:1880\n\t"
            "ds_read_b128 v[172:175], v254 offset:320\n\t"
            "ds_read_b64 v[144:145], v253 offset:48\n\t"
            "ds_read_b64 v[146:147], v253 offset:144\n\t"
            "ds_read_b64 v[148:149], v253 offset:1792\n\t"
            "ds_read_b64 v[150:151], v253 offset:1888\n\t"
            "s_waitcnt lgkmcnt(14)\n\t"
            "ds_read_b128 v[224:227], v232 offset:6272\n\t"
            "s_waitcnt lgkmcnt(14)\n\t"
            "ds_read_b128 v[228:231], v233 offset:6272\n\t"
            "s_waitcnt lgkmcnt(12)\n\t"
            "v_readfirstlane_b32 s26, v249\n\t"
            "v_cvt_pk_bf16_f32 v177, v178, v179\n\t"
            "v_cvt_pk_bf16_f32 v178, v180, v181\n\t"
            "s_and_b32 s68, s26, 0x7ffffff\n\t"
            "s_add_u32 s68, s68, s78\n\t"
            "s_and_b32 s69, s26, 0x38000000\n\t"
            "s_or_b32 s69, s69, s39\n\t"
            "s_bitcmp1_b32 s26, 27\n\t"
            "s_cselect_b32 s79, 2.0, s68\n\t"
            "s_setprio 3\n\t"
            "v_mfma_f32_4x4x4_16b_bf16 v[196:199], v[176:177], v[176:177], 0\n\t"
            "s_setprio 1\n\t"
            "ds_read_b32 v249, v252 offset:12\n\t"
            "s_and_b32 s57, s56, 3\n\t"
            "s_lshl_b32 s58, s57, 3\n\t"
            "s_lshr_b32 m0, s54, s58\n\t"
            "s_lshl_b32 s58, s57, 4\n\t"
            "s_lshr_b64 s[36:37], s[50:51], s58\n\t"
            "s_and_b32 s57, s36, 0xffff\n\t"
            "s_add_u32 s57, s57, s67\n\t"
            "v_writelane_b32 v236, s57, 63\n\t"
            "v_writelane_b32 v236, s59, m0\n\t"
            "v_cvt_pk_bf16_f32 v179, v182, v183\n\t"
            "v_mov_b64 v[180:181], 0\n\t"
            "v_mov_b64 v[182:183], 0\n\t"
            "s_setprio 3\n\t"
            "v_mfma_f32_16x16x32_bf16 v[192:195], v[238:241], v[176:179], 0\n\t"
            "s_lshr_b64 s[36:37], s[52:53], s58\n\t"
            "s_and_b32 s58, s36, 0xffff\n\t"
            "s_add_u32 s58, s58, s67\n\t"
            "v_mfma_f32_4x4x4_16b_bf16 v[196:199], v[178:179], v[178:179], v[196:199]\n\t"
            "ds_write_b128 v236, v[176:179]\n\t"
            "s_waitcnt lgkmcnt(8)\n\t"
            "v_smfmac_f32_16x16x64_bf16 v[180:183], v[172:175], v[160:167], v255\n\t"
            "s_setprio 1\n\t"
            "s_and_b32 s38, s52, 0xffff\n\t"
            "s_add_u32 s38, s38, s67\n\t"
            "ds_read_b128 v[172:175], v254 offset:448\n\t"
            "ds_read_b64 v[160:161], v253 offset:64\n\t"
            "ds_read_b64 v[162:163], v253 offset:160\n\t"
            "ds_read_b64 v[164:165], v253 offset:1808\n\t"
            "ds_read_b64 v[166:167], v253 offset:1904\n\t"
            "v_mov_b64 v[176:177], 0\n\t"
            "v_mov_b64 v[178:179], 0\n\t"
            "v_cvt_pk_bf16_f32 v184, v184, v185\n\t"
            "v_cvt_pk_bf16_f32 v185, v186, v187\n\t"
            "s_setprio 3\n\t"
            "v_smfmac_f32_16x16x64_bf16 v[176:179], v[168:171], v[152:159], v255\n\t"
            "ds_read_b128 v[168:171], v254 offset:384\n\t"
            "v_mfma_f32_4x4x4_16b_bf16 v[196:199], v[184:185], v[184:185], v[196:199]\n\t"
            "s_setprio 1\n\t"
            "ds_read_b64 v[152:153], v253 offset:56\n\t"
            "s_waitcnt lgkmcnt(14)\n\t"
            "ds_read_b64 v[154:155], v253 offset:152\n\t"
            "s_waitcnt lgkmcnt(14)\n\t"
            "ds_read_b64 v[156:157], v253 offset:1800\n\t"
            "s_waitcnt lgkmcnt(14)\n\t"
            "ds_read_b64 v[158:159], v253 offset:1896\n\t"
            "v_xor_b32 v237, 16, v236\n\t"
            "v_cvt_pk_bf16_f32 v186, v188, v189\n\t"
            "v_cvt_pk_bf16_f32 v187, v190, v191\n\t"
            "v_mov_b64 v[188:189], 0\n\t"
            "v_mov_b64 v[190:191], 0\n\t"
            "s_setprio 3\n\t"
            "v_mfma_f32_16x16x32_bf16 v[192:195], v[238:241], v[184:187], v[192:195]\n\t"
            "s_waitcnt lgkmcnt(14)\n\t"
            "ds_write_b128 v237, v[184:187]\n\t"
            "v_mfma_f32_4x4x4_16b_bf16 v[196:199], v[186:187], v[186:187], v[196:199]\n\t"
            "s_setprio 1\n\t"
            "v_mov_b64 v[184:185], 0\n\t"
            "v_mov_b64 v[186:187], 0\n\t"
            "v_cvt_pk_bf16_f32 v176, v176, v177\n\t"
            "v_cvt_pk_bf16_f32 v177, v178, v179\n\t"
            "v_cvt_pk_bf16_f32 v178, v180, v181\n\t"
            "v_cvt_pk_bf16_f32 v179, v182, v183\n\t"
            "s_setprio 3\n\t"
            "v_mfma_f32_4x4x4_16b_bf16 v[196:199], v[176:177], v[176:177], v[196:199]\n\t"
            "s_waitcnt lgkmcnt(14)\n\t"
            "ds_write_b128 v236, v[176:179] offset:128\n\t"
            "s_waitcnt lgkmcnt(6)\n\t"
            "v_smfmac_f32_16x16x64_bf16 v[184:187], v[168:171], v[144:151], v255\n\t"
            "ds_read_b128 v[168:171], v254 offset:512\n\t"
            "s_waitcnt lgkmcnt(3)\n\t"
            "v_smfmac_f32_16x16x64_bf16 v[188:191], v[172:175], v[152:159], v255\n\t"
            "ds_read_b64 v[144:145], v253 offset:72\n\t"
            "v_mfma_f32_16x16x32_bf16 v[192:195], v[238:241], v[176:179], v[192:195]\n\t"
            "ds_read_b64 v[146:147], v253 offset:168\n\t"
            "ds_read_b64 v[148:149], v253 offset:1816\n\t"
            "v_mfma_f32_4x4x4_16b_bf16 v[196:199], v[178:179], v[178:179], v[196:199]\n\t"
            "s_setprio 1\n\t"
            "ds_read_b64 v[150:151], v253 offset:1912\n\t"
            "ds_read_b128 v[172:175], v254 offset:576\n\t"
            "ds_read_b64 v[152:153], v253 offset:80\n\t"
            "ds_read_b64 v[154:155], v253 offset:176\n\t"
            "ds_read_b64 v[156:157], v253 offset:1824\n\t"
            "ds_read_b64 v[158:159], v253 offset:1920\n\t"
            "v_mov_b64 v[176:177], 0\n\t"
            "v_mov_b64 v[178:179], 0\n\t"
            "v_mov_b64 v[180:181], 0\n\t"
            "v_mov_b64 v[182:183], 0\n\t"
            "v_cvt_pk_bf16_f32 v184, v184, v185\n\t"
            "v_cvt_pk_bf16_f32 v185, v186, v187\n\t"
            "v_cvt_pk_bf16_f32 v186, v188, v189\n\t"
            "v_cvt_pk_bf16_f32 v187, v190, v191\n\t"
            "s_setprio 3\n\t"
            "v_mfma_f32_4x4x4_16b_bf16 v[196:199], v[184:185], v[184:185], v[196:199]\n\t"
            "ds_write_b128 v237, v[184:187] offset:128\n\t"
            "s_waitcnt lgkmcnt(10)\n\t"
            "v_smfmac_f32_16x16x64_bf16 v[176:179], v[168:171], v[160:167], v255\n\t"
            "ds_read_b128 v[168:171], v254 offset:640\n\t"
            "v_mfma_f32_16x16x32_bf16 v[192:195], v[238:241], v[184:187], v[192:195]\n\t"
            "ds_read_b64 v[160:161], v253 offset:88\n\t"
            "ds_read_b64 v[162:163], v253 offset:184\n\t"
            "s_waitcnt lgkmcnt(8)\n\t"
            "v_smfmac_f32_16x16x64_bf16 v[180:183], v[172:175], v[144:151], v255\n\t"
            "ds_read_b64 v[164:165], v253 offset:1832\n\t"
            "v_mfma_f32_4x4x4_16b_bf16 v[196:199], v[186:187], v[186:187], v[196:199]\n\t"
            "s_setprio 1\n\t"
            "ds_read_b64 v[166:167], v253 offset:1928\n\t"
            "ds_read_b128 v[172:175], v254 offset:704\n\t"
            "v_mov_b64 v[184:185], 0\n\t"
            "v_mov_b64 v[186:187], 0\n\t"
            "v_mov_b64 v[188:189], 0\n\t"
            "v_mov_b64 v[190:191], 0\n\t"
            "v_add_u32 v248, s67, v251\n\t"
            "v_writelane_b32 v248, s58, 63\n\t"
            "v_writelane_b32 v248, s38, m0\n\t"
            "v_and_b32 v249, s69, v249\n\t"
            "v_cvt_pk_bf16_f32 v176, v176, v177\n\t"
            "v_cvt_pk_bf16_f32 v177, v178, v179\n\t"
            "v_cvt_pk_bf16_f32 v178, v180, v181\n\t"
            "v_cvt_pk_bf16_f32 v179, v182, v183\n\t"
            "s_setprio 3\n\t"
            "v_mfma_f32_4x4x4_16b_bf16 v[196:199], v[176:177], v[176:177], v[196:199]\n\t"
            "ds_write_b128 v236, v[176:179] offset:256\n\t"
            "s_waitcnt lgkmcnt(6)\n\t"
            "v_smfmac_f32_16x16x64_bf16 v[184:187], v[168:171], v[152:159], v255\n\t"
            "v_mov_b32 v234, 0\n\t"
            "v_mfma_f32_4x4x4_16b_bf16 v[196:199], v[178:179], v[178:179], v[196:199]\n\t"
            "v_xor_b32 v246, 0x100, v246\n\t"
            "v_mfma_f32_16x16x32_bf16 v[192:195], v[238:241], v[176:179], v[192:195]\n\t"
            "s_waitcnt lgkmcnt(1)\n\t"
            "v_smfmac_f32_16x16x64_bf16 v[188:191], v[172:175], v[160:167], v255\n\t"
            "s_setprio 1\n\t"
            "s_mov_b32 exec_hi, 0x3fffff\n\t"
            "s_add_u32 m0, s66, 0\n\t"
            "s_add_u32 s49, s79, 0\n\t"
            "buffer_load_dwordx4 v249, s[88:91], s49 offen lds\n\t"
            "s_mov_b32 exec_hi, -1\n\t"
            "s_mov_b32 exec_hi, 0x3fffff\n\t"
            "s_add_u32 m0, s66, 872\n\t"
            "s_add_u32 s49, s68, 196224\n\t"
            "buffer_load_dwordx4 v249, s[72:75], s49 offen lds\n\t"
            "s_mov_b32 exec_hi, -1\n\t"
            "s_mov_b32 exec_hi, 0x3fffff\n\t"
            "s_add_u32 m0, s66, 1744\n\t"
            "s_add_u32 s49, s68, 392448\n\t"
            "buffer_load_dwordx4 v249, s[72:75], s49 offen lds\n\t"
            "s_mov_b32 exec_hi, -1\n\t"
            "s_mov_b32 exec_hi, 0x3fffff\n\t"
            "s_add_u32 m0, s66, 2616\n\t"
            "s_add_u32 s49, s68, 588672\n\t"
            "buffer_load_dwordx4 v249, s[72:75], s49 offen lds\n\t"
            "s_mov_b32 exec_hi, -1\n\t"
            "s_mov_b32 exec_hi, 0x3fffff\n\t"
            "s_add_u32 m0, s66, 3488\n\t"
            "s_add_u32 s49, s68, 784896\n\t"
            "buffer_load_dwordx4 v249, s[72:75], s49 offen lds\n\t"
            "s_mov_b32 exec_hi, -1\n\t"
            "v_cvt_pk_bf16_f32 v184, v184, v185\n\t"
            "v_cvt_pk_bf16_f32 v185, v186, v187\n\t"
            "v_cvt_pk_bf16_f32 v186, v188, v189\n\t"
            "v_cvt_pk_bf16_f32 v187, v190, v191\n\t"
            "s_setprio 3\n\t"
            "v_mfma_f32_4x4x4_16b_bf16 v[196:199], v[184:185], v[184:185], v[196:199]\n\t"
            "s_mov_b32 exec_hi, 0x3fffff\n\t"
            "s_add_u32 m0, s66, 4360\n\t"
            "s_add_u32 s49, s68, 981120\n\t"
            "buffer_load_dwordx4 v249, s[72:75], s49 offen lds\n\t"
            "s_mov_b32 exec_hi, -1\n\t"
            "v_mfma_f32_4x4x4_16b_bf16 v[196:199], v[186:187], v[186:187], v[196:199]\n\t"
            "s_mov_b32 exec_hi, 0x3fffff\n\t"
            "s_add_u32 m0, s66, 5232\n\t"
            "s_add_u32 s49, s68, 1177344\n\t"
            "buffer_load_dwordx4 v249, s[72:75], s49 offen lds\n\t"
            "s_mov_b32 exec_hi, -1\n\t"
            "v_mfma_f32_16x16x32_bf16 v[192:195], v[238:241], v[184:187], v[192:195]\n\t"
            "s_setprio 1\n\t"
            "s_mov_b32 exec_hi, 0x3fffff\n\t"
            "s_add_u32 m0, s66, 6104\n\t"
            "s_add_u32 s49, s68, 1373568\n\t"
            "buffer_load_dwordx4 v249, s[72:75], s49 offen lds\n\t"
            "s_mov_b32 exec_hi, -1\n\t"
            "s_mov_b32 exec_hi, 0x3fffff\n\t"
            "s_add_u32 m0, s66, 6976\n\t"
            "s_add_u32 s49, s68, 1569792\n\t"
            "buffer_load_dwordx4 v249, s[72:75], s49 offen lds\n\t"
            "s_mov_b32 exec_hi, -1\n\t"
            "v_cndmask_b32_e64 v193, v196, v197, s[60:61]\n\t"
            "v_cndmask_b32_e64 v193, v193, v198, s[62:63]\n\t"
            "v_cndmask_b32_e64 v193, v193, v199, s[64:65]\n\t"
            "s_setprio 1\n\t"
            "ds_read_b128 v[196:199], v233 offset:6144\n\t"
            "ds_write_b64 v248, v[192:193] offset:18816\n\t"
            "ds_read_b128 v[192:195], v232 offset:6144\n\t"
            "ds_read_b64 v[248:249], v247 offset:8\n\t"
            "ds_write_b128 v237, v[184:187] offset:256\n\t"
            "ds_read_b64 v[236:237], v247\n\t"
            "s_mov_b64 exec, 1\n\t"
            "ds_inc_u32 v255, v238 offset:7860\n\t"
            "s_mov_b64 exec, -1\n\t"
            "s_waitcnt lgkmcnt(4)\n\t"
            "v_mfma_f32_16x16x32_bf16 v[144:147], v[192:195], v[0:3], 0\n\t"
            "s_waitcnt lgkmcnt(3)\n\t"
            "v_mul_f32 v202, v248, v202\n\t"
            "v_mul_f32 v203, v249, v203\n\t"
            "v_mfma_f32_16x16x32_bf16 v[144:147], v[196:199], v[4:7], v[144:147]\n\t"
            "v_mul_f32 v206, v248, v206\n\t"
            "s_waitcnt lgkmcnt(1)\n\t"
            "v_mul_f32 v200, v236, v200\n\t"
            "v_mfma_f32_16x16x32_bf16 v[148:151], v[196:199], v[28:31], 0\n\t"
            "v_mul_f32 v201, v237, v201\n\t"
            "v_mul_f32 v204, v236, v204\n\t"
            "v_mfma_f32_16x16x32_bf16 v[148:151], v[192:195], v[24:27], v[148:151]\n\t"
            "v_mul_f32 v205, v237, v205\n\t"
            "v_mul_f32 v208, v236, v208\n\t"
            "v_mfma_f32_16x16x32_bf16 v[168:171], v[192:195], v[48:51], 0\n\t"
            "v_mul_f32 v209, v237, v209\n\t"
            "v_mul_f32 v212, v236, v212\n\t"
            "v_mfma_f32_16x16x32_bf16 v[168:171], v[196:199], v[52:55], v[168:171]\n\t"
            "v_mul_f32 v213, v237, v213\n\t"
            "v_mul_f32 v216, v236, v216\n\t"
            "v_mfma_f32_16x16x32_bf16 v[172:175], v[196:199], v[76:79], 0\n\t"
            "v_mul_f32 v217, v237, v217\n\t"
            "v_mul_f32 v220, v236, v220\n\t"
            "v_mfma_f32_16x16x32_bf16 v[172:175], v[192:195], v[72:75], v[172:175]\n\t"
            "v_mul_f32 v221, v237, v221\n\t"
            "v_mul_f32 v207, v249, v207\n\t"
            "v_mfma_f32_16x16x32_bf16 v[152:155], v[192:195], v[96:99], 0\n\t"
            "ds_read_b64 v[236:237], v247 offset:64\n\t"
            "v_mul_f32 v210, v248, v210\n\t"
            "v_mfma_f32_16x16x32_bf16 v[152:155], v[196:199], v[100:103], v[152:155]\n\t"
            "v_mul_f32 v211, v249, v211\n\t"
            "v_mul_f32 v214, v248, v214\n\t"
            "v_mfma_f32_16x16x32_bf16 v[156:159], v[196:199], v[124:127], 0\n\t"
            "ds_read_b128 v[196:199], v233 offset:6400\n\t"
            "v_mul_f32 v215, v249, v215\n\t"
            "v_mfma_f32_16x16x32_bf16 v[156:159], v[192:195], v[120:123], v[156:159]\n\t"
            "ds_read_b128 v[192:195], v232 offset:6400\n\t"
            "v_mul_f32 v218, v248, v218\n\t"
            "v_mfma_f32_16x16x32_bf16 v[144:147], v[224:227], v[8:11], v[144:147]\n\t"
            "v_mul_f32 v219, v249, v219\n\t"
            "v_mul_f32 v222, v248, v222\n\t"
            "v_mfma_f32_16x16x32_bf16 v[144:147], v[228:231], v[12:15], v[144:147]\n\t"
            "v_mul_f32 v223, v249, v223\n\t"
            "v_cvt_pk_bf16_f32 v212, v212, v213\n\t"
            "v_mfma_f32_16x16x32_bf16 v[148:151], v[228:231], v[36:39], v[148:151]\n\t"
            "ds_read_b64 v[248:249], v247 offset:72\n\t"
            "v_cvt_pk_bf16_f32 v208, v208, v209\n\t"
            "v_mfma_f32_16x16x32_bf16 v[148:151], v[224:227], v[32:35], v[148:151]\n\t"
            "v_cvt_pk_bf16_f32 v213, v214, v215\n\t"
            "v_cvt_pk_bf16_f32 v209, v210, v211\n\t"
            "v_mfma_f32_16x16x32_bf16 v[168:171], v[224:227], v[56:59], v[168:171]\n\t"
            "v_cvt_pk_bf16_f32 v200, v200, v201\n\t"
            "v_cvt_pk_bf16_f32 v204, v204, v205\n\t"
            "v_mfma_f32_16x16x32_bf16 v[168:171], v[228:231], v[60:63], v[168:171]\n\t"
            "v_cvt_pk_bf16_f32 v216, v216, v217\n\t"
            "v_cvt_pk_bf16_f32 v220, v220, v221\n\t"
            "v_mfma_f32_16x16x32_bf16 v[172:175], v[228:231], v[84:87], v[172:175]\n\t"
            "v_cvt_pk_bf16_f32 v201, v202, v203\n\t"
            "v_cvt_pk_bf16_f32 v205, v206, v207\n\t"
            "v_mfma_f32_16x16x32_bf16 v[172:175], v[224:227], v[80:83], v[172:175]\n\t"
            "v_cvt_pk_bf16_f32 v217, v218, v219\n\t"
            "v_cvt_pk_bf16_f32 v221, v222, v223\n\t"
            "v_mfma_f32_16x16x32_bf16 v[152:155], v[224:227], v[104:107], v[152:155]\n\t"
            "v_mfma_f32_16x16x32_bf16 v[152:155], v[228:231], v[108:111], v[152:155]\n\t"
            "v_mfma_f32_16x16x32_bf16 v[156:159], v[228:231], v[132:135], v[156:159]\n\t"
            "ds_read_b128 v[228:231], v233 offset:12288\n\t"
            "v_mfma_f32_16x16x32_bf16 v[156:159], v[224:227], v[128:131], v[156:159]\n\t"
            "ds_read_b128 v[224:227], v232 offset:12288\n\t"
            "s_waitcnt lgkmcnt(3)\n\t"
            "v_mfma_f32_16x16x32_bf16 v[144:147], v[192:195], v[16:19], v[144:147]\n\t"
            "v_mfma_f32_16x16x32_bf16 v[144:147], v[196:199], v[20:23], v[144:147]\n\t"
            "v_mfma_f32_16x16x32_bf16 v[148:151], v[196:199], v[44:47], v[148:151]\n\t"
            "v_mfma_f32_16x16x32_bf16 v[148:151], v[192:195], v[40:43], v[148:151]\n\t"
            "v_mfma_f32_16x16x32_bf16 v[168:171], v[192:195], v[64:67], v[168:171]\n\t"
            "v_mfma_f32_16x16x32_bf16 v[168:171], v[196:199], v[68:71], v[168:171]\n\t"
            "ds_read_b64 v[234:235], v234 offset:7856\n\t"
            "v_mfma_f32_16x16x32_bf16 v[172:175], v[196:199], v[92:95], v[172:175]\n\t"
            "s_nop 1\n\t"
            "v_mul_f32 v144, v236, v144\n\t"
            "v_mfma_f32_16x16x32_bf16 v[172:175], v[192:195], v[88:91], v[172:175]\n\t"
            "v_mul_f32 v145, v237, v145\n\t"
            "s_waitcnt lgkmcnt(3)\n\t"
            "v_mul_f32 v146, v248, v146\n\t"
            "v_mfma_f32_16x16x32_bf16 v[152:155], v[192:195], v[112:115], v[152:155]\n\t"
            "v_mul_f32 v168, v236, v168\n\t"
            "v_mul_f32 v169, v237, v169\n\t"
            "v_mfma_f32_16x16x32_bf16 v[152:155], v[196:199], v[116:119], v[152:155]\n\t"
            "v_mul_f32 v170, v248, v170\n\t"
            "v_mul_f32 v171, v249, v171\n\t"
            "v_mfma_f32_16x16x32_bf16 v[156:159], v[196:199], v[140:143], v[156:159]\n\t"
            "ds_read_b128 v[196:199], v233 offset:12416\n\t"
            "v_mul_f32 v172, v236, v172\n\t"
            "v_mfma_f32_16x16x32_bf16 v[156:159], v[192:195], v[136:139], v[156:159]\n\t"
            "ds_read_b128 v[192:195], v232 offset:12416\n\t"
            "v_mul_f32 v173, v237, v173\n\t"
            "s_waitcnt lgkmcnt(3)\n\t"
            "v_mfma_f32_16x16x32_bf16 v[160:163], v[224:227], v[0:3], 0\n\t"
            "v_cvt_pk_bf16_f32 v214, v172, v173\n\t"
            "v_cvt_pk_bf16_f32 v210, v168, v169\n\t"
            "v_mfma_f32_16x16x32_bf16 v[160:163], v[228:231], v[4:7], v[160:163]\n\t"
            "s_waitcnt lgkmcnt(2)\n\t"
            "v_readfirstlane_b32 s77, v234\n\t"
            "v_readfirstlane_b32 s38, v235\n\t"
            "s_add_u32 s77, s77, s38\n\t"
            "s_cmp_ge_u32 s77, s44\n\t"
            "s_cbranch_scc0 .Lwc_spin%=\n\t"
            ".Lwc_go%=:\n\t"
            "v_mfma_f32_16x16x32_bf16 v[164:167], v[228:231], v[28:31], 0\n\t"
            "s_add_u32 s59, s59, s84\n\t"
            "s_movk_i32 s84, 800\n\t"
            "v_cvt_pk_bf16_f32 v211, v170, v171\n\t"
            "v_mfma_f32_16x16x32_bf16 v[164:167], v[224:227], v[24:27], v[164:167]\n\t"
            "s_add_u32 s45, s46, s27\n\t"
            "s_mov_b32 s46, s47\n\t"
            "s_mov_b32 s47, s68\n\t"
            "v_mfma_f32_16x16x32_bf16 v[176:179], v[224:227], v[48:51], 0\n\t"
            "v_mul_f32 v174, v248, v174\n\t"
            "v_mul_f32 v175, v249, v175\n\t"
            "v_mfma_f32_16x16x32_bf16 v[176:179], v[228:231], v[52:55], v[176:179]\n\t"
            "v_add_u32 v172, s67, v245\n\t"
            "v_cvt_pk_bf16_f32 v215, v174, v175\n\t"
            "v_mfma_f32_16x16x32_bf16 v[180:183], v[228:231], v[76:79], 0\n\t"
            "ds_read_b128 v[168:171], v172 offset:18816\n\t"
            "ds_read_b128 v[172:175], v172 offset:19840\n\t"
            "v_mfma_f32_16x16x32_bf16 v[180:183], v[224:227], v[72:75], v[180:183]\n\t"
            "v_mul_f32 v148, v236, v148\n\t"
            "v_mul_f32 v149, v237, v149\n\t"
            "v_mfma_f32_16x16x32_bf16 v[184:187], v[224:227], v[96:99], 0\n\t"
            "v_mul_f32 v152, v236, v152\n\t"
            "v_mul_f32 v153, v237, v153\n\t"
            "v_mfma_f32_16x16x32_bf16 v[184:187], v[228:231], v[100:103], v[184:187]\n\t"
            "v_mul_f32 v156, v236, v156\n\t"
            "v_mul_f32 v157, v237, v157\n\t"
            "v_mfma_f32_16x16x32_bf16 v[188:191], v[228:231], v[124:127], 0\n\t"
            "ds_read_b128 v[228:231], v233 offset:12544\n\t"
            "ds_read_b64 v[236:237], v247 offset:128\n\t"
            "v_mfma_f32_16x16x32_bf16 v[188:191], v[224:227], v[120:123], v[188:191]\n\t"
            "ds_read_b128 v[224:227], v232 offset:12544\n\t"
            "v_mul_f32 v147, v249, v147\n\t"
            "s_waitcnt lgkmcnt(5)\n\t"
            "v_mfma_f32_16x16x32_bf16 v[160:163], v[192:195], v[8:11], v[160:163]\n\t"
            "v_mul_f32 v150, v248, v150\n\t"
            "v_mul_f32 v151, v249, v151\n\t"
            "v_mfma_f32_16x16x32_bf16 v[160:163], v[196:199], v[12:15], v[160:163]\n\t"
            "v_mul_f32 v154, v248, v154\n\t"
            "v_mul_f32 v155, v249, v155\n\t"
            "v_mfma_f32_16x16x32_bf16 v[164:167], v[196:199], v[36:39], v[164:167]\n\t"
            "v_mul_f32 v158, v248, v158\n\t"
            "v_mul_f32 v159, v249, v159\n\t"
            "v_mfma_f32_16x16x32_bf16 v[164:167], v[192:195], v[32:35], v[164:167]\n\t"
            "ds_read_b64 v[248:249], v247 offset:136\n\t"
            "s_waitcnt lgkmcnt(5)\n\t"
            "v_add_f32 v168, v168, v170\n\t"
            "v_mfma_f32_16x16x32_bf16 v[176:179], v[192:195], v[56:59], v[176:179]\n\t"
            "s_waitcnt lgkmcnt(4)\n\t"
            "v_add_f32 v172, v172, v174\n\t"
            "v_add_f32 v168, v168, v172\n\t"
            "v_mfma_f32_16x16x32_bf16 v[176:179], v[196:199], v[60:63], v[176:179]\n\t"
            "v_add_f32 v169, v169, v171\n\t"
            "v_add_f32 v173, v173, v175\n\t"
            "v_mfma_f32_16x16x32_bf16 v[180:183], v[196:199], v[84:87], v[180:183]\n\t"
            "v_mul_f32 v168, 0x3baaaaab, v168\n\t"
            "v_add_f32 v169, v169, v173\n\t"
            "v_mfma_f32_16x16x32_bf16 v[180:183], v[192:195], v[80:83], v[180:183]\n\t"
            "ds_read_b128 v[172:175], v254 offset:64\n\t"
            "v_mul_f32_e64 v168, v168, -v168\n\t"
            "v_mfma_f32_16x16x32_bf16 v[184:187], v[192:195], v[104:107], v[184:187]\n\t"
            "v_fmac_f32 v168, 0x3baaaaab, v169\n\t"
            "v_max_f32 v168, 0, v168\n\t"
            "v_mfma_f32_16x16x32_bf16 v[184:187], v[196:199], v[108:111], v[184:187]\n\t"
            "v_cvt_pk_bf16_f32 v202, v144, v145\n\t"
            "v_cvt_pk_bf16_f32 v203, v146, v147\n\t"
            "v_mfma_f32_16x16x32_bf16 v[188:191], v[196:199], v[132:135], v[188:191]\n\t"
            "v_cvt_pk_bf16_f32 v206, v148, v149\n\t"
            "v_cvt_pk_bf16_f32 v207, v150, v151\n\t"
            "v_mfma_f32_16x16x32_bf16 v[188:191], v[192:195], v[128:131], v[188:191]\n\t"
            "v_cvt_pk_bf16_f32 v218, v152, v153\n\t"
            "v_cvt_pk_bf16_f32 v219, v154, v155\n\t"
            "s_waitcnt lgkmcnt(2)\n\t"
            "v_mfma_f32_16x16x32_bf16 v[160:163], v[224:227], v[16:19], v[160:163]\n\t"
            "v_cvt_pk_bf16_f32 v222, v156, v157\n\t"
            "v_cvt_pk_bf16_f32 v223, v158, v159\n\t"
            "v_mfma_f32_16x16x32_bf16 v[160:163], v[228:231], v[20:23], v[160:163]\n\t"
            "v_add_f32 v168, 0x3727c5ac, v168\n\t"
            "v_rsq_f32 v168, v168\n\t"
            "v_mfma_f32_16x16x32_bf16 v[164:167], v[228:231], v[44:47], v[164:167]\n\t"
            "v_add_u32 v232, s67, v242\n\t"
            "v_xor_b32 v233, 64, v232\n\t"
            "v_mfma_f32_16x16x32_bf16 v[164:167], v[224:227], v[40:43], v[164:167]\n\t"
            "s_xor_b32 s48, s48, s71\n\t"
            "s_xor_b32 s67, s67, s71\n\t"
            "s_add_u32 s76, s76, 4\n\t"
            "s_add_u32 s44, s44, 8\n\t"
            "v_mfma_f32_16x16x32_bf16 v[176:179], v[224:227], v[64:67], v[176:179]\n\t"
            "s_lshl_b32 s56, s56, 2\n\t"
            "s_lshr_b32 s49, s26, 30\n\t"
            "s_or_b32 s56, s56, s49\n\t"
            "v_mfma_f32_16x16x32_bf16 v[176:179], v[228:231], v[68:71], v[176:179]\n\t"
            "ds_read_b128 v[192:195], v232\n\t"
            "ds_read_b128 v[196:199], v233\n\t"
            "v_mfma_f32_16x16x32_bf16 v[180:183], v[228:231], v[92:95], v[180:183]\n\t"
            "v_mul_f32 v160, v236, v160\n\t"
            "v_mul_f32 v161, v237, v161\n\t"
            "v_mfma_f32_16x16x32_bf16 v[180:183], v[224:227], v[88:91], v[180:183]\n\t"
            "v_mul_f32 v164, v236, v164\n\t"
            "v_mul_f32 v165, v237, v165\n\t"
            "v_mfma_f32_16x16x32_bf16 v[184:187], v[224:227], v[112:115], v[184:187]\n\t"
            "v_mul_f32 v176, v236, v176\n\t"
            "v_mul_f32 v177, v237, v177\n\t"
            "v_mfma_f32_16x16x32_bf16 v[184:187], v[228:231], v[116:119], v[184:187]\n\t"
            "ds_write_b128 v243, v[200:203]\n\t"
            "v_mfma_f32_16x16x32_bf16 v[188:191], v[228:231], v[140:143], v[188:191]\n\t"
            "ds_read_b128 v[228:231], v233 offset:128\n\t"
            "v_mul_f32 v180, v236, v180\n\t"
            "v_mfma_f32_16x16x32_bf16 v[188:191], v[224:227], v[136:139], v[188:191]\n\t"
            "ds_read_b128 v[224:227], v232 offset:128\n\t"
            "v_mul_f32 v181, v237, v181\n\t"
            "s_waitcnt lgkmcnt(3)\n\t"
            "v_mfma_f32_16x16x32_bf16 v[200:203], v[192:195], v[0:3], 0\n\t"
            "v_mul_f32 v184, v236, v184\n\t"
            "v_mul_f32 v185, v237, v185\n\t"
            "v_mfma_f32_16x16x32_bf16 v[200:203], v[196:199], v[4:7], v[200:203]\n\t"
            "ds_write_b128 v243, v[204:207] offset:1536\n\t"
            "v_mfma_f32_16x16x32_bf16 v[204:207], v[196:199], v[28:31], 0\n\t"
            "v_mul_f32 v188, v236, v188\n\t"
            "v_mul_f32 v189, v237, v189\n\t"
            "v_mfma_f32_16x16x32_bf16 v[204:207], v[192:195], v[24:27], v[204:207]\n\t"
            "ds_write_b128 v243, v[208:211] offset:3072\n\t"
            "v_mfma_f32_16x16x32_bf16 v[208:211], v[192:195], v[48:51], 0\n\t"
            "v_mul_f32 v162, v248, v162\n\t"
            "v_mul_f32 v163, v249, v163\n\t"
            "v_mfma_f32_16x16x32_bf16 v[208:211], v[196:199], v[52:55], v[208:211]\n\t"
            "ds_write_b128 v243, v[212:215] offset:4608\n\t"
            "v_mfma_f32_16x16x32_bf16 v[212:215], v[196:199], v[76:79], 0\n\t"
            "v_cvt_pk_bf16_f32 v160, v160, v161\n\t"
            "v_mul_f32 v166, v248, v166\n\t"
            "v_mfma_f32_16x16x32_bf16 v[212:215], v[192:195], v[72:75], v[212:215]\n\t"
            "ds_write_b128 v243, v[216:219] offset:6144\n\t"
            "v_mfma_f32_16x16x32_bf16 v[216:219], v[192:195], v[96:99], 0\n\t"
            "v_mul_f32 v167, v249, v167\n\t"
            "v_cvt_pk_bf16_f32 v164, v164, v165\n\t"
            "v_mfma_f32_16x16x32_bf16 v[216:219], v[196:199], v[100:103], v[216:219]\n\t"
            "ds_write_b128 v243, v[220:223] offset:7680\n\t"
            "v_mfma_f32_16x16x32_bf16 v[220:223], v[196:199], v[124:127], 0\n\t"
            "v_mul_f32 v178, v248, v178\n\t"
            "v_mul_f32 v179, v249, v179\n\t"
            "v_mfma_f32_16x16x32_bf16 v[220:223], v[192:195], v[120:123], v[220:223]\n\t"
            "v_cvt_pk_bf16_f32 v176, v176, v177\n\t"
            "v_mul_f32 v182, v248, v182\n\t"
            "s_waitcnt lgkmcnt(5)\n\t"
            "v_mfma_f32_16x16x32_bf16 v[200:203], v[224:227], v[8:11], v[200:203]\n\t"
            "s_waitcnt vmcnt(0)\n\t"
            "ds_read_b64 v[144:145], v253\n\t"
            "ds_read_b64 v[146:147], v253 offset:96\n\t"
            "v_mfma_f32_16x16x32_bf16 v[200:203], v[228:231], v[12:15], v[200:203]\n\t"
            "ds_read_b64 v[148:149], v253 offset:1744\n\t"
            "ds_read_b64 v[150:151], v253 offset:1840\n\t"
            "v_mfma_f32_16x16x32_bf16 v[204:207], v[228:231], v[36:39], v[204:207]\n\t"
            "ds_read_b64 v[152:153], v253 offset:8\n\t"
            "ds_read_b64 v[154:155], v253 offset:104\n\t"
            "v_mfma_f32_16x16x32_bf16 v[204:207], v[224:227], v[32:35], v[204:207]\n\t"
            "ds_read_b64 v[156:157], v253 offset:1752\n\t"
            "ds_read_b64 v[158:159], v253 offset:1848\n\t"
            "v_mfma_f32_16x16x32_bf16 v[208:211], v[224:227], v[56:59], v[208:211]\n\t"
            "v_mul_f32 v183, v249, v183\n\t"
            "v_cvt_pk_bf16_f32 v180, v180, v181\n\t"
            "v_mfma_f32_16x16x32_bf16 v[208:211], v[228:231], v[60:63], v[208:211]\n\t"
            "v_mul_f32 v186, v248, v186\n\t"
            "v_mul_f32 v187, v249, v187\n\t"
            "v_mfma_f32_16x16x32_bf16 v[212:215], v[228:231], v[84:87], v[212:215]\n\t"
            "v_cvt_pk_bf16_f32 v184, v184, v185\n\t"
            "v_mul_f32 v190, v248, v190\n\t"
            "v_mfma_f32_16x16x32_bf16 v[212:215], v[224:227], v[80:83], v[212:215]\n\t"
            "v_mul_f32 v191, v249, v191\n\t"
            "v_cvt_pk_bf16_f32 v188, v188, v189\n\t"
            "v_mfma_f32_16x16x32_bf16 v[216:219], v[224:227], v[104:107], v[216:219]\n\t"
            "v_cvt_pk_bf16_f32 v161, v162, v163\n\t"
            "v_cvt_pk_bf16_f32 v165, v166, v167\n\t"
            "v_mfma_f32_16x16x32_bf16 v[216:219], v[228:231], v[108:111], v[216:219]\n\t"
            "ds_read_b64 v[162:163], v253 offset:112\n\t"
            "ds_read_b64 v[166:167], v253 offset:1856\n\t"
            "v_mfma_f32_16x16x32_bf16 v[220:223], v[228:231], v[132:135], v[220:223]\n\t"
            "s_waitcnt lgkmcnt(14)\n\t"
            "ds_read_b128 v[228:231], v233 offset:256\n\t"
            "v_cvt_pk_bf16_f32 v177, v178, v179\n\t"
            "v_mfma_f32_16x16x32_bf16 v[220:223], v[224:227], v[128:131], v[220:223]\n\t"
            "s_waitcnt lgkmcnt(14)\n\t"
            "ds_read_b128 v[224:227], v232 offset:256\n\t"
            "v_cvt_pk_bf16_f32 v181, v182, v183\n\t"
            "v_cvt_pk_bf16_f32 v185, v186, v187\n\t"
            "v_cvt_pk_bf16_f32 v189, v190, v191\n\t"
            "v_xor_b32 v247, 0x100, v247\n\t"
            "s_waitcnt lgkmcnt(14)\n\t"
            "ds_write_b64 v244, v[160:161]\n\t"
            "s_waitcnt lgkmcnt(14)\n\t"
            "ds_read_b64 v[160:161], v253 offset:16\n\t"
            "s_waitcnt lgkmcnt(14)\n\t"
            "ds_write_b64 v244, v[164:165] offset:1536\n\t"
            "s_waitcnt lgkmcnt(14)\n\t"
            "ds_read_b64 v[164:165], v253 offset:1760\n\t"
            "s_waitcnt lgkmcnt(14)\n\t"
            "ds_write_b64 v244, v[176:177] offset:3072\n\t"
            "s_waitcnt lgkmcnt(14)\n\t"
            "ds_write_b64 v244, v[180:181] offset:4608\n\t"
            "s_waitcnt lgkmcnt(14)\n\t"
            "ds_write_b64 v244, v[184:185] offset:6144\n\t"
            "s_waitcnt lgkmcnt(14)\n\t"
            "ds_write_b64 v244, v[188:189] offset:7680\n\t"
            "s_waitcnt lgkmcnt(14)\n\t"
            "ds_write_b32 v246, v168\n\t"
            "s_waitcnt lgkmcnt(14)\n\t"
            "ds_read_b128 v[168:171], v254\n\t"
            "s_waitcnt lgkmcnt(1)\n\t"
            "s_barrier\n\t"
            "s_cmp_lt_u32 s46, 0x7000000\n\t"
            "s_cbranch_scc1 .Lwc_loop%=\n\t"
            "s_branch .Lwc_end%=\n\t"
            ".Lwc_spin%=:\n\t"
            "s_cmp_eq_u32 s83, 0\n\t"
            "s_cbranch_scc1 .Lwc_go%=\n\t"
            "s_sub_u32 s83, s83, 1\n\t"
            "s_sleep 2\n\t"
            "v_mov_b32 v234, 0\n\t"
            "ds_read_b64 v[234:235], v234 offset:7856\n\t"
            "s_waitcnt lgkmcnt(0)\n\t"
            "v_readfirstlane_b32 s77, v234\n\t"
            "v_readfirstlane_b32 s38, v235\n\t"
            "s_add_u32 s77, s77, s38\n\t"
            "s_cmp_ge_u32 s77, s44\n\t"
            "s_cbranch_scc0 .Lwc_spin%=\n\t"
            "s_branch .Lwc_go%=\n\t"
            ".Lwc_end%=:\n\t"
            "s_waitcnt lgkmcnt(0)\n\t"
            "s_nop 7\n\t"
            : "+{v246}"(aRSw_), "+{v247}"(aRSr_), "+{s44}"(s44), B_SIO
            : "{v242}"(pE_), "{v243}"(aVAw_), "{v244}"(aVBw_), "{v245}"(pST_), "{v250}"(pXw_), "{v251}"(pSTw_), "{v252}"(aCA_), "{v253}"(aB_), "{v254}"(aA_), "{v255}"(idx_), B_SIN, "{s82}"(touch)
            : "v144", "v145", "v146", "v147", "v148", "v149", "v150", "v151", "v152", "v153", "v154", "v155", "v156", "v157", "v158", "v159", "v160", "v161", "v162", "v163", "v164", "v165", "v166", "v167", "v168", "v169", "v170", "v171", "v172", "v173", "v174", "v175", "v176", "v177", "v178", "v179", "v180", "v181", "v182", "v183", "v184", "v185", "v186", "v187", "v188", "v189", "v190", "v191", "v192", "v193", "v194", "v195", "v196", "v197", "v198", "v199", "v236", "v237", "v248", "v249", "v0", "v1", "v2", "v3", "v4", "v5", "v6", "v7", "v8", "v9", "v10", "v11", "v12", "v13", "v14", "v15", "v16", "v17", "v18", "v19", "v20", "v21", "v22", "v23", "v24", "v25", "v26", "v27", "v28", "v29", "v30", "v31", "v32", "v33", "v34", "v35", "v36", "v37", "v38", "v39", "v40", "v41", "v42", "v43", "v44", "v45", "v46", "v47", "v48", "v49", "v50", "v51", "v52", "v53", "v54", "v55", "v56", "v57", "v58", "v59", "v60", "v61", "v62", "v63", "v64", "v65", "v66", "v67", "v68", "v69", "v70", "v71", "v72", "v73", "v74", "v75", "v76", "v77", "v78", "v79", "v80", "v81", "v82", "v83", "v84", "v85", "v86", "v87", "v88", "v89", "v90", "v91", "v92", "v93", "v94", "v95", "v96", "v97", "v98", "v99", "v100", "v101", "v102", "v103", "v104", "v105", "v106", "v107", "v108", "v109", "v110", "v111", "v112", "v113", "v114", "v115", "v116", "v117", "v118", "v119", "v120", "v121", "v122", "v123", "v124", "v125", "v126", "v127", "v128", "v129", "v130", "v131", "v132", "v133", "v134", "v135", "v136", "v137", "v138", "v139", "v140", "v141", "v142", "v143", "v200", "v201", "v202", "v203", "v204", "v205", "v206", "v207", "v208", "v209", "v210", "v211", "v212", "v213", "v214", "v215", "v216", "v217", "v218", "v219", "v220", "v221", "v222", "v223", "v224", "v225", "v226", "v227", "v228", "v229", "v230", "v231", "v232", "v233", "v234", "v235", B_SCLOB);
        }
        {
            __builtin_amdgcn_s_setprio(3);
            int ln_;
            asm volatile("v_mbcnt_lo_u32_b32 %0, -1, 0\n\tv_mbcnt_hi_u32_b32 %0, -1, %0" : "=v"(ln_));
            const int l15_ = ln_ & 15, g_ = ln_ >> 4;
            KARGS
            const auto orsrc = __builtin_amdgcn_make_buffer_rsrc((void*)ob, 0, IMG * IMG * CH * 2, 0x00020000);
            const auto wq = __builtin_amdgcn_make_buffer_rsrc((void*)wb, 0, WS_BYTES, 0x00020000);
            bf16x8 W[36];
#pragma unroll
            for (int ks = 0; ks < 6; ++ks)
#pragma unroll
                for (int j = 0; j < 6; ++j) W[j * 6 + ks] = BC8(__builtin_amdgcn_raw_buffer_load_b128(wq, ln_ * 16, (wave + 2) * 36864 + (j * 6 + ks) * 1024, SC1));
            SB();
            int lE[2], nw;
            unsigned o[2];
            f32x4 acc[2][6];
#pragma unroll
            for (int lt = 0; lt < 2; ++lt) {
                int i = 16 * lt + l15_;
                const unsigned en = *(const unsigned*)(smem + WL_OFF + i * 4);
                const bool ok = en != 0x7ffffffu;
                if (lt == 1) nw = 16 + __builtin_popcount((unsigned)__builtin_amdgcn_ballot_w64(ok) & 0xffffu);
                i = ok ? i : 0;
                lE[lt] = OL_OFF + i * 384 + ((g_ ^ ((i >> 1) & 7)) << 4);
                o[lt] = ok ? (en & 0x7ffffffu) + LPOS(en >> 31) * ROWBYTES + LPOS(en >> 30) * (CH * 2) + 192 * w + 16 * g_ : OOB;
#pragma unroll
                for (int k = 0; k < 3; ++k) {
                    const int c8 = 12 * w + 4 * k + g_, ws = (c8 * 43) >> 8, r6 = c8 - 6 * ws;
                    const u32x4 r = *(const u32x4*)(smem + LX_OFF(i) + (r6 >> 1) * 128 + ((2 * ws + (r6 & 1)) << 4));
                    acc[lt][2 * k] = (f32x4){bflo(r[0]), bfhi(r[0]), bflo(r[1]), bfhi(r[1])};
                    acc[lt][2 * k + 1] = (f32x4){bflo(r[2]), bfhi(r[2]), bflo(r[3]), bfhi(r[3])};
                }
            }
            SB();
            for (int it_ = 0; it_ < 100000; ++it_) {
                unsigned n_;
                asm volatile("ds_read_b32 %0, %1\n\ts_waitcnt lgkmcnt(0)" : "=v"(n_) : "v"(ldsb + FLAG_OFF) : "memory");
                if (__builtin_amdgcn_readfirstlane(n_) >= 4 * nw) break;
                __builtin_amdgcn_s_sleep(1);
            }
            bf16x8 of[2][6];
#pragma unroll
            for (int ks = 0; ks < 6; ++ks)
#pragma unroll
                for (int lt = 0; lt < 2; ++lt) of[lt][ks] = *(const bf16x8*)(smem + ((ks & 1) ? (lE[lt] ^ 64) : lE[lt]) + (ks >> 1) * 128);
            SB();
#pragma unroll
            for (int lt = 0; lt < 2; ++lt) {
#pragma unroll
                for (int ks = 0; ks < 6; ++ks)
#pragma unroll
                    for (int j = 0; j < 6; ++j) acc[lt][j] = MFMA32(W[j * 6 + ks], of[lt][ks], acc[lt][j]);
#pragma unroll
                for (int k = 0; k < 3; ++k) {
                    const f32x4 a = acc[lt][2 * k], b = acc[lt][2 * k + 1];
                    const u32x4 ov = {pk2(a[0], a[1]), pk2(a[2], a[3]), pk2(b[0], b[1]), pk2(b[2], b[3])};
                    __builtin_amdgcn_raw_buffer_store_b128(ov, orsrc, o[lt] + 64 * k, 0, SC1);
                }
            }
        }
        __builtin_amdgcn_endpgm();
    } else {
        LANE_SETUP
        {
            const unsigned oE_ = ldsb + fE + (O_OFF - X_OFF), oO_ = oE_ ^ 64;
            unsigned l16_ = lane * 16;
            unsigned pr[3];
#pragma unroll
            for (int k = 0; k < 3; ++k) {
                const int c8 = 12 * (w - 2) + 4 * k + g;
                const int ws = c8 / 6, r6 = c8 - 6 * ws;
                pr[k] = l15 * 384 + (r6 >> 1) * 128 + (((2 * ws + (r6 & 1)) ^ sw) << 4);
            }
            const unsigned pR0_ = pr[0], pR1_ = pr[1], pR2_ = pr[2];
            unsigned q0 = ldsb + PF_OFF + 1024 + lane * 4, q1 = ldsb + O_OFF + lane * 16, q2 = oE_, q3 = ldsb + PF_OFF + 1024 + l15 * 4;
            unsigned q4 = ldsb + OL_OFF + l15 * 384 + (((4 * (w - 2) + (g >> 1)) ^ sw) << 4) + 8 * (g & 1);
            B_SCALARS
            asm volatile(
            "s_waitcnt lgkmcnt(0)\n\t"
            "s_mov_b32 s83, 0x10000\n\t"
            "s_mov_b32 s84, -47104\n\t"
            "s_mov_b64 s[88:89], s[72:73]\n\t"
            "s_mov_b64 s[90:91], s[74:75]\n\t"
            "s_nop 7\n\t"
            "s_barrier\n\t"
            "ds_read_b64 v[236:237], v252 offset:2560\n\t"
            "s_waitcnt lgkmcnt(0)\n\t"
            "s_mov_b64 exec, 1\n\t"
            "buffer_store_dwordx2 v[236:237], off, s[28:31], s44 sc1\n\t"
            "s_mov_b64 exec, -1\n\t"
            "s_sleep 30\n\t"
            "ds_read_b32 v249, v252 offset:12\n\t"
            "s_waitcnt lgkmcnt(0)\n\t"
            "v_and_b32 v249, s69, v249\n\t"
            "s_mov_b32 exec_hi, 0x3fffff\n\t"
            "s_add_u32 m0, s66, 0\n\t"
            "s_add_u32 s49, s79, 0\n\t"
            "buffer_load_dwordx4 v249, s[72:75], s49 offen lds\n\t"
            "s_mov_b32 exec_hi, -1\n\t"
            "s_mov_b32 exec_hi, 0x3fffff\n\t"
            "s_add_u32 m0, s66, 872\n\t"
            "s_add_u32 s49, s47, 196224\n\t"
            "buffer_load_dwordx4 v249, s[72:75], s49 offen lds\n\t"
            "s_mov_b32 exec_hi, -1\n\t"
            "s_mov_b32 exec_hi, 0x3fffff\n\t"
            "s_add_u32 m0, s66, 1744\n\t"
            "s_add_u32 s49, s47, 392448\n\t"
            "buffer_load_dwordx4 v249, s[72:75], s49 offen lds\n\t"
            "s_mov_b32 exec_hi, -1\n\t"
            "s_mov_b32 exec_hi, 0x3fffff\n\t"
            "s_add_u32 m0, s66, 2616\n\t"
            "s_add_u32 s49, s47, 588672\n\t"
            "buffer_load_dwordx4 v249, s[72:75], s49 offen lds\n\t"
            "s_mov_b32 exec_hi, -1\n\t"
            "s_mov_b32 exec_hi, 0x3fffff\n\t"
            "s_add_u32 m0, s66, 3488\n\t"
            "s_add_u32 s49, s47, 784896\n\t"
            "buffer_load_dwordx4 v249, s[72:75], s49 offen lds\n\t"
            "s_mov_b32 exec_hi, -1\n\t"
            "s_mov_b32 exec_hi, 0x3fffff\n\t"
            "s_add_u32 m0, s66, 4360\n\t"
            "s_add_u32 s49, s47, 981120\n\t"
            "buffer_load_dwordx4 v249, s[72:75], s49 offen lds\n\t"
            "s_mov_b32 exec_hi, -1\n\t"
            "s_mov_b32 exec_hi, 0x3fffff\n\t"
            "s_add_u32 m0, s66, 5232\n\t"
            "s_add_u32 s49, s47, 1177344\n\t"
            "buffer_load_dwordx4 v249, s[72:75], s49 offen lds\n\t"
            "s_mov_b32 exec_hi, -1\n\t"
            "s_mov_b32 exec_hi, 0x3fffff\n\t"
            "s_add_u32 m0, s66, 6104\n\t"
            "s_add_u32 s49, s47, 1373568\n\t"
            "buffer_load_dwordx4 v249, s[72:75], s49 offen lds\n\t"
            "s_mov_b32 exec_hi, -1\n\t"
            "s_mov_b32 exec_hi, 0x3fffff\n\t"
            "s_add_u32 m0, s66, 6976\n\t"
            "s_add_u32 s49, s47, 1569792\n\t"
            "buffer_load_dwordx4 v249, s[72:75], s49 offen lds\n\t"
            "s_mov_b32 exec_hi, -1\n\t"
            "s_add_u32 s49, s33, -73728\n\t"
            "buffer_load_dwordx4 v[0:3], v247, s[28:31], s49 offen sc1\n\t"
            "s_add_u32 s49, s33, -67584\n\t"
            "buffer_load_dwordx4 v[24:27], v247, s[28:31], s49 offen sc1\n\t"
            "s_add_u32 s49, s33, -61440\n\t"
            "buffer_load_dwordx4 v[48:51], v247, s[28:31], s49 offen sc1\n\t"
            "s_add_u32 s49, s33, -55296\n\t"
            "buffer_load_dwordx4 v[72:75], v247, s[28:31], s49 offen sc1\n\t"
            "s_add_u32 s49, s33, -49152\n\t"
            "buffer_load_dwordx4 v[96:99], v247, s[28:31], s49 offen sc1\n\t"
            "s_add_u32 s49, s33, -43008\n\t"
            "buffer_load_dwordx4 v[120:123], v247, s[28:31], s49 offen sc1\n\t"
            "s_add_u32 s49, s33, -72704\n\t"
            "buffer_load_dwordx4 v[4:7], v247, s[28:31], s49 offen sc1\n\t"
            "s_add_u32 s49, s33, -66560\n\t"
            "buffer_load_dwordx4 v[28:31], v247, s[28:31], s49 offen sc1\n\t"
            "s_add_u32 s49, s33, -60416\n\t"
            "buffer_load_dwordx4 v[52:55], v247, s[28:31], s49 offen sc1\n\t"
            "s_add_u32 s49, s33, -54272\n\t"
            "buffer_load_dwordx4 v[76:79], v247, s[28:31], s49 offen sc1\n\t"
            "s_add_u32 s49, s33, -48128\n\t"
            "buffer_load_dwordx4 v[100:103], v247, s[28:31], s49 offen sc1\n\t"
            "s_add_u32 s49, s33, -41984\n\t"
            "buffer_load_dwordx4 v[124:127], v247, s[28:31], s49 offen sc1\n\t"
            "s_add_u32 s49, s33, -71680\n\t"
            "buffer_load_dwordx4 v[8:11], v247, s[28:31], s49 offen sc1\n\t"
            "s_add_u32 s49, s33, -65536\n\t"
            "buffer_load_dwordx4 v[32:35], v247, s[28:31], s49 offen sc1\n\t"
            "s_add_u32 s49, s33, -59392\n\t"
            "buffer_load_dwordx4 v[56:59], v247, s[28:31], s49 offen sc1\n\t"
            "s_add_u32 s49, s33, -53248\n\t"
            "buffer_load_dwordx4 v[80:83], v247, s[28:31], s49 offen sc1\n\t"
            "s_add_u32 s49, s33, -47104\n\t"
            "buffer_load_dwordx4 v[104:107], v247, s[28:31], s49 offen sc1\n\t"
            "s_add_u32 s49, s33, -40960\n\t"
            "buffer_load_dwordx4 v[128:131], v247, s[28:31], s49 offen sc1\n\t"
            "s_add_u32 s49, s33, -70656\n\t"
            "buffer_load_dwordx4 v[12:15], v247, s[28:31], s49 offen sc1\n\t"
            "s_add_u32 s49, s33, -64512\n\t"
            "buffer_load_dwordx4 v[36:39], v247, s[28:31], s49 offen sc1\n\t"
            "s_add_u32 s49, s33, -58368\n\t"
            "buffer_load_dwordx4 v[60:63], v247, s[28:31], s49 offen sc1\n\t"
            "s_add_u32 s49, s33, -52224\n\t"
            "buffer_load_dwordx4 v[84:87], v247, s[28:31], s49 offen sc1\n\t"
            "s_add_u32 s49, s33, -46080\n\t"
            "buffer_load_dwordx4 v[108:111], v247, s[28:31], s49 offen sc1\n\t"
            "s_add_u32 s49, s33, -39936\n\t"
            "buffer_load_dwordx4 v[132:135], v247, s[28:31], s49 offen sc1\n\t"
            "s_add_u32 s49, s33, -69632\n\t"
            "buffer_load_dwordx4 v[16:19], v247, s[28:31], s49 offen sc1\n\t"
            "s_add_u32 s49, s33, -63488\n\t"
            "buffer_load_dwordx4 v[40:43], v247, s[28:31], s49 offen sc1\n\t"
            "s_add_u32 s49, s33, -57344\n\t"
            "buffer_load_dwordx4 v[64:67], v247, s[28:31], s49 offen sc1\n\t"
            "s_add_u32 s49, s33, -51200\n\t"
            "buffer_load_dwordx4 v[88:91], v247, s[28:31], s49 offen sc1\n\t"
            "s_add_u32 s49, s33, -45056\n\t"
            "buffer_load_dwordx4 v[112:115], v247, s[28:31], s49 offen sc1\n\t"
            "s_add_u32 s49, s33, -38912\n\t"
            "buffer_load_dwordx4 v[136:139], v247, s[28:31], s49 offen sc1\n\t"
            "s_add_u32 s49, s33, -68608\n\t"
            "buffer_load_dwordx4 v[20:23], v247, s[28:31], s49 offen sc1\n\t"
            "s_add_u32 s49, s33, -62464\n\t"
            "buffer_load_dwordx4 v[44:47], v247, s[28:31], s49 offen sc1\n\t"
            "s_add_u32 s49, s33, -56320\n\t"
            "buffer_load_dwordx4 v[68:71], v247, s[28:31], s49 offen sc1\n\t"
            "s_add_u32 s49, s33, -50176\n\t"
            "buffer_load_dwordx4 v[92:95], v247, s[28:31], s49 offen sc1\n\t"
            "s_add_u32 s49, s33, -44032\n\t"
            "buffer_load_dwordx4 v[116:119], v247, s[28:31], s49 offen sc1\n\t"
            "s_add_u32 s49, s33, -37888\n\t"
            "buffer_load_dwordx4 v[140:143], v247, s[28:31], s49 offen sc1\n\t"
            "s_setprio 3\n\t"
            "ds_read_b128 v[168:171], v254\n\t"
            "ds_read_b128 v[172:175], v254 offset:64\n\t"
            "ds_read_b128 v[220:223], v229 offset:12288\n\t"
            "ds_read_b128 v[224:227], v229 offset:13312\n\t"
            "ds_read_b128 v[200:203], v232\n\t"
            "ds_read_b128 v[208:211], v232 offset:128\n\t"
            "ds_read_b128 v[216:219], v232 offset:256\n\t"
            "s_waitcnt vmcnt(36)\n\t"
            "ds_read_b64 v[144:145], v253\n\t"
            "ds_read_b64 v[146:147], v253 offset:96\n\t"
            "ds_read_b64 v[148:149], v253 offset:1744\n\t"
            "ds_read_b64 v[150:151], v253 offset:1840\n\t"
            "ds_read_b64 v[152:153], v253 offset:8\n\t"
            "ds_read_b64 v[154:155], v253 offset:104\n\t"
            "ds_read_b64 v[156:157], v253 offset:1752\n\t"
            "ds_read_b64 v[158:159], v253 offset:1848\n\t"
            "s_waitcnt lgkmcnt(14)\n\t"
            "ds_read_b64 v[160:161], v253 offset:16\n\t"
            "s_waitcnt lgkmcnt(14)\n\t"
            "ds_read_b64 v[162:163], v253 offset:112\n\t"
            "s_waitcnt lgkmcnt(14)\n\t"
            "ds_read_b64 v[164:165], v253 offset:1760\n\t"
            "s_waitcnt lgkmcnt(14)\n\t"
            "ds_read_b64 v[166:167], v253 offset:1856\n\t"
            "v_mov_b64 v[176:177], 0\n\t"
            "v_mov_b64 v[178:179], 0\n\t"
            "v_mov_b64 v[180:181], 0\n\t"
            "v_mov_b64 v[182:183], 0\n\t"
            "v_mov_b32 v249, s76\n\t"
            "v_mov_b64 v[184:185], 0\n\t"
            "v_mov_b64 v[186:187], 0\n\t"
            "v_mov_b64 v[188:189], 0\n\t"
            "v_mov_b64 v[190:191], 0\n\t"
            "v_add_u32 v236, s67, v250\n\t"
            "v_add_u32 v248, s67, v251\n\t"
            "v_xor_b32 v235, 64, v232\n\t"
            "s_nop 0\n\t"
            "s_waitcnt lgkmcnt(14)\n\t"
            "ds_read_b32 v249, v249\n\t"
            "s_waitcnt lgkmcnt(14)\n\t"
            "ds_read_b128 v[204:207], v235\n\t"
            "s_waitcnt lgkmcnt(14)\n\t"
            "ds_read_b128 v[212:215], v235 offset:128\n\t"
            "v_add_f32 v220, v220, v222\n\t"
            "v_add_f32 v224, v224, v226\n\t"
            "v_add_f32 v220, v220, v224\n\t"
            "v_add_f32 v221, v221, v223\n\t"
            "v_add_f32 v225, v225, v227\n\t"
            "v_mul_f32 v220, 0x3baaaaab, v220\n\t"
            "v_add_f32 v221, v221, v225\n\t"
            "v_mul_f32_e64 v220, v220, -v220\n\t"
            "v_fmac_f32 v220, 0x3baaaaab, v221\n\t"
            "s_waitcnt lgkmcnt(11)\n\t"
            "v_smfmac_f32_16x16x64_bf16 v[176:179], v[168:171], v[144:151], v255\n\t"
            "ds_read_b128 v[168:171], v254 offset:128\n\t"
            "s_waitcnt lgkmcnt(8)\n\t"
            "v_smfmac_f32_16x16x64_bf16 v[180:183], v[172:175], v[152:159], v255\n\t"
            "ds_read_b64 v[144:145], v253 offset:24\n\t"
            "ds_read_b64 v[146:147], v253 offset:120\n\t"
            "ds_read_b64 v[148:149], v253 offset:1768\n\t"
            "ds_read_b64 v[150:151], v253 offset:1864\n\t"
            "ds_read_b128 v[172:175], v254 offset:192\n\t"
            "ds_read_b64 v[152:153], v253 offset:32\n\t"
            "ds_read_b64 v[154:155], v253 offset:128\n\t"
            "s_waitcnt lgkmcnt(14)\n\t"
            "ds_read_b64 v[156:157], v253 offset:1776\n\t"
            "s_waitcnt lgkmcnt(14)\n\t"
            "ds_read_b64 v[158:159], v253 offset:1872\n\t"
            "s_waitcnt lgkmcnt(14)\n\t"
            "ds_read_b128 v[224:227], v232 offset:6144\n\t"
            "v_cvt_pk_bf16_f32 v176, v176, v177\n\t"
            "v_cvt_pk_bf16_f32 v177, v178, v179\n\t"
            "v_cvt_pk_bf16_f32 v178, v180, v181\n\t"
            "v_cvt_pk_bf16_f32 v179, v182, v183\n\t"
            "v_mfma_f32_4x4x4_16b_bf16 v[196:199], v[176:177], v[176:177], 0\n\t"
            "v_mov_b64 v[180:181], 0\n\t"
            "v_mfma_f32_16x16x32_bf16 v[192:195], v[238:241], v[176:179], 0\n\t"
            "s_waitcnt lgkmcnt(13)\n\t"
            "v_readfirstlane_b32 s26, v249\n\t"
            "v_mov_b64 v[182:183], 0\n\t"
            "v_mfma_f32_4x4x4_16b_bf16 v[196:199], v[178:179], v[178:179], v[196:199]\n\t"
            "ds_read_b32 v249, v252 offset:12\n\t"
            "s_and_b32 s68, s26, 0x7ffffff\n\t"
            "s_add_u32 s68, s68, s78\n\t"
            "s_and_b32 s69, s26, 0x38000000\n\t"
            "s_or_b32 s69, s69, s39\n\t"
            "s_bitcmp1_b32 s26, 27\n\t"
            "s_cselect_b32 s79, 2.0, s68\n\t"
            "s_and_b32 s57, s56, 3\n\t"
            "s_lshl_b32 s58, s57, 3\n\t"
            "s_lshr_b32 m0, s54, s58\n\t"
            "s_lshl_b32 s58, s57, 4\n\t"
            "s_lshr_b64 s[36:37], s[50:51], s58\n\t"
            "s_and_b32 s57, s36, 0xffff\n\t"
            "s_add_u32 s57, s57, s67\n\t"
            "v_writelane_b32 v236, s57, 63\n\t"
            "v_writelane_b32 v236, s59, m0\n\t"
            "v_xor_b32 v237, 16, v236\n\t"
            "s_lshr_b64 s[36:37], s[52:53], s58\n\t"
            "s_and_b32 s58, s36, 0xffff\n\t"
            "s_add_u32 s58, s58, s67\n\t"
            "s_waitcnt lgkmcnt(11)\n\t"
            "v_smfmac_f32_16x16x64_bf16 v[184:187], v[168:171], v[160:167], v255\n\t"
            "s_and_b32 s38, s52, 0xffff\n\t"
            "s_add_u32 s38, s38, s67\n\t"
            "ds_read_b128 v[168:171], v254 offset:256\n\t"
            "ds_read_b64 v[160:161], v253 offset:40\n\t"
            "ds_read_b64 v[162:163], v253 offset:136\n\t"
            "ds_read_b64 v[164:165], v253 offset:1784\n\t"
            "s_waitcnt lgkmcnt(14)\n\t"
            "ds_read_b64 v[166:167], v253 offset:1880\n\t"
            "s_waitcnt lgkmcnt(11)\n\t"
            "v_smfmac_f32_16x16x64_bf16 v[188:191], v[172:175], v[144:151], v255\n\t"
            "ds_read_b128 v[172:175], v254 offset:320\n\t"
            "ds_read_b64 v[144:145], v253 offset:48\n\t"
            "ds_read_b64 v[146:147], v253 offset:144\n\t"
            "ds_read_b64 v[148:149], v253 offset:1792\n\t"
            "s_waitcnt lgkmcnt(14)\n\t"
            "ds_read_b64 v[150:151], v253 offset:1888\n\t"
            "s_waitcnt lgkmcnt(14)\n\t"
            "ds_write_b128 v236, v[176:179]\n\t"
            "v_mov_b64 v[176:177], 0\n\t"
            "v_mov_b64 v[178:179], 0\n\t"
            "v_cvt_pk_bf16_f32 v184, v184, v185\n\t"
            "v_cvt_pk_bf16_f32 v185, v186, v187\n\t"
            "v_cvt_pk_bf16_f32 v186, v188, v189\n\t"
            "v_cvt_pk_bf16_f32 v187, v190, v191\n\t"
            "v_mfma_f32_4x4x4_16b_bf16 v[196:199], v[184:185], v[184:185], v[196:199]\n\t"
            "s_waitcnt lgkmcnt(14)\n\t"
            "ds_write_b128 v237, v[184:187]\n\t"
            "v_mfma_f32_16x16x32_bf16 v[192:195], v[238:241], v[184:187], v[192:195]\n\t"
            "v_mov_b64 v[184:185], 0\n\t"
            "v_mov_b64 v[188:189], 0\n\t"
            "v_mfma_f32_4x4x4_16b_bf16 v[196:199], v[186:187], v[186:187], v[196:199]\n\t"
            "v_mov_b64 v[186:187], 0\n\t"
            "v_mov_b64 v[190:191], 0\n\t"
            "v_writelane_b32 v248, s58, 63\n\t"
            "v_writelane_b32 v248, s38, m0\n\t"
            "s_waitcnt lgkmcnt(12)\n\t"
            "v_and_b32 v249, s69, v249\n\t"
            "v_max_f32 v220, 0, v220\n\t"
            "v_add_f32 v220, 0x3727c5ac, v220\n\t"
            "v_rsq_f32 v220, v220\n\t"
            "s_waitcnt lgkmcnt(11)\n\t"
            "v_smfmac_f32_16x16x64_bf16 v[176:179], v[168:171], v[152:159], v255\n\t"
            "ds_read_b128 v[168:171], v254 offset:384\n\t"
            "ds_read_b64 v[152:153], v253 offset:56\n\t"
            "ds_read_b64 v[154:155], v253 offset:152\n\t"
            "ds_read_b64 v[156:157], v253 offset:1800\n\t"
            "s_waitcnt lgkmcnt(14)\n\t"
            "ds_read_b64 v[158:159], v253 offset:1896\n\t"
            "s_waitcnt lgkmcnt(11)\n\t"
            "v_smfmac_f32_16x16x64_bf16 v[180:183], v[172:175], v[160:167], v255\n\t"
            "ds_read_b128 v[172:175], v254 offset:448\n\t"
            "ds_read_b64 v[160:161], v253 offset:64\n\t"
            "ds_read_b64 v[162:163], v253 offset:160\n\t"
            "ds_read_b64 v[164:165], v253 offset:1808\n\t"
            "s_waitcnt lgkmcnt(14)\n\t"
            "ds_read_b64 v[166:167], v253 offset:1904\n\t"
            "v_cvt_pk_bf16_f32 v176, v176, v177\n\t"
            "v_cvt_pk_bf16_f32 v177, v178, v179\n\t"
            "v_cvt_pk_bf16_f32 v178, v180, v181\n\t"
            "v_cvt_pk_bf16_f32 v179, v182, v183\n\t"
            "v_mfma_f32_4x4x4_16b_bf16 v[196:199], v[176:177], v[176:177], v[196:199]\n\t"
            "s_waitcnt lgkmcnt(14)\n\t"
            "ds_write_b128 v236, v[176:179] offset:128\n\t"
            "v_mfma_f32_16x16x32_bf16 v[192:195], v[238:241], v[176:179], v[192:195]\n\t"
            "v_mov_b64 v[176:177], 0\n\t"
            "v_mov_b64 v[180:181], 0\n\t"
            "v_mfma_f32_4x4x4_16b_bf16 v[196:199], v[178:179], v[178:179], v[196:199]\n\t"
            "v_mov_b64 v[178:179], 0\n\t"
            "v_mov_b64 v[182:183], 0\n\t"
            "s_waitcnt lgkmcnt(14)\n\t"
            "ds_write_b32 v228, v220\n\t"
            "s_waitcnt lgkmcnt(14)\n\t"
            "ds_read_b128 v[220:223], v235 offset:256\n\t"
            "s_waitcnt lgkmcnt(14)\n\t"
            "ds_read_b32 v230, v233\n\t"
            "s_waitcnt lgkmcnt(14)\n\t"
            "ds_read_b32 v231, v233 offset:64\n\t"
            "v_xor_b32 v228, 32, v234\n\t"
            "s_waitcnt lgkmcnt(14)\n\t"
            "v_smfmac_f32_16x16x64_bf16 v[184:187], v[168:171], v[144:151], v255\n\t"
            "ds_read_b128 v[168:171], v254 offset:512\n\t"
            "s_waitcnt lgkmcnt(14)\n\t"
            "ds_read_b64 v[144:145], v253 offset:72\n\t"
            "s_waitcnt lgkmcnt(14)\n\t"
            "ds_read_b64 v[146:147], v253 offset:168\n\t"
            "s_waitcnt lgkmcnt(14)\n\t"
            "ds_read_b64 v[148:149], v253 offset:1816\n\t"
            "s_waitcnt lgkmcnt(14)\n\t"
            "ds_read_b64 v[150:151], v253 offset:1912\n\t"
            "s_waitcnt lgkmcnt(14)\n\t"
            "v_smfmac_f32_16x16x64_bf16 v[188:191], v[172:175], v[152:159], v255\n\t"
            "ds_read_b128 v[172:175], v254 offset:576\n\t"
            "s_waitcnt lgkmcnt(14)\n\t"
            "ds_read_b64 v[152:153], v253 offset:80\n\t"
            "s_waitcnt lgkmcnt(14)\n\t"
            "ds_read_b64 v[154:155], v253 offset:176\n\t"
            "s_waitcnt lgkmcnt(14)\n\t"
            "ds_read_b64 v[156:157], v253 offset:1824\n\t"
            "s_waitcnt lgkmcnt(14)\n\t"
            "ds_read_b64 v[158:159], v253 offset:1920\n\t"
            "v_cvt_pk_bf16_f32 v184, v184, v185\n\t"
            "v_cvt_pk_bf16_f32 v185, v186, v187\n\t"
            "v_cvt_pk_bf16_f32 v186, v188, v189\n\t"
            "v_cvt_pk_bf16_f32 v187, v190, v191\n\t"
            "v_mfma_f32_4x4x4_16b_bf16 v[196:199], v[184:185], v[184:185], v[196:199]\n\t"
            "s_waitcnt lgkmcnt(14)\n\t"
            "ds_write_b128 v237, v[184:187] offset:128\n\t"
            "v_mfma_f32_16x16x32_bf16 v[192:195], v[238:241], v[184:187], v[192:195]\n\t"
            "v_mov_b64 v[184:185], 0\n\t"
            "v_mov_b64 v[188:189], 0\n\t"
            "v_mfma_f32_4x4x4_16b_bf16 v[196:199], v[186:187], v[186:187], v[196:199]\n\t"
            "v_mov_b64 v[186:187], 0\n\t"
            "v_mov_b64 v[190:191], 0\n\t"
            "s_waitcnt lgkmcnt(10)\n\t"
            "v_smfmac_f32_16x16x64_bf16 v[176:179], v[168:171], v[160:167], v255\n\t"
            "ds_read_b128 v[168:171], v254 offset:640\n\t"
            "ds_read_b64 v[160:161], v253 offset:88\n\t"
            "ds_read_b64 v[162:163], v253 offset:184\n\t"
            "ds_read_b64 v[164:165], v253 offset:1832\n\t"
            "ds_read_b64 v[166:167], v253 offset:1928\n\t"
            "s_waitcnt lgkmcnt(10)\n\t"
            "v_smfmac_f32_16x16x64_bf16 v[180:183], v[172:175], v[144:151], v255\n\t"
            "ds_read_b128 v[172:175], v254 offset:704\n\t"
            "v_cvt_pk_bf16_f32 v176, v176, v177\n\t"
            "v_cvt_pk_bf16_f32 v177, v178, v179\n\t"
            "s_nop 1\n\t"
            "v_mfma_f32_4x4x4_16b_bf16 v[196:199], v[176:177], v[176:177], v[196:199]\n\t"
            "s_nop 1\n\t"
            "v_cvt_pk_bf16_f32 v178, v180, v181\n\t"
            "v_cvt_pk_bf16_f32 v179, v182, v183\n\t"
            "s_nop 1\n\t"
            "v_mfma_f32_4x4x4_16b_bf16 v[196:199], v[178:179], v[178:179], v[196:199]\n\t"
            "ds_write_b128 v236, v[176:179] offset:256\n\t"
            "v_mfma_f32_16x16x32_bf16 v[192:195], v[238:241], v[176:179], v[192:195]\n\t"
            "v_add_u32 v236, s48, v244\n\t"
            "s_waitcnt lgkmcnt(6)\n\t"
            "v_smfmac_f32_16x16x64_bf16 v[184:187], v[168:171], v[152:159], v255\n\t"
            "s_waitcnt lgkmcnt(1)\n\t"
            "v_smfmac_f32_16x16x64_bf16 v[188:191], v[172:175], v[160:167], v255\n\t"
            "s_mov_b32 exec_hi, 0x3fffff\n\t"
            "s_add_u32 m0, s66, 0\n\t"
            "s_add_u32 s49, s79, 0\n\t"
            "buffer_load_dwordx4 v249, s[88:91], s49 offen lds\n\t"
            "s_mov_b32 exec_hi, -1\n\t"
            "s_mov_b32 exec_hi, 0x3fffff\n\t"
            "s_add_u32 m0, s66, 872\n\t"
            "s_add_u32 s49, s68, 196224\n\t"
            "buffer_load_dwordx4 v249, s[72:75], s49 offen lds\n\t"
            "s_mov_b32 exec_hi, -1\n\t"
            "s_mov_b32 exec_hi, 0x3fffff\n\t"
            "s_add_u32 m0, s66, 1744\n\t"
            "s_add_u32 s49, s68, 392448\n\t"
            "buffer_load_dwordx4 v249, s[72:75], s49 offen lds\n\t"
            "s_mov_b32 exec_hi, -1\n\t"
            "s_mov_b32 exec_hi, 0x3fffff\n\t"
            "s_add_u32 m0, s66, 2616\n\t"
            "s_add_u32 s49, s68, 588672\n\t"
            "buffer_load_dwordx4 v249, s[72:75], s49 offen lds\n\t"
            "s_mov_b32 exec_hi, -1\n\t"
            "s_mov_b32 exec_hi, 0x3fffff\n\t"
            "s_add_u32 m0, s66, 3488\n\t"
            "s_add_u32 s49, s68, 784896\n\t"
            "buffer_load_dwordx4 v249, s[72:75], s49 offen lds\n\t"
            "s_mov_b32 exec_hi, -1\n\t"
            "s_mov_b32 exec_hi, 0x3fffff\n\t"
            "s_add_u32 m0, s66, 4360\n\t"
            "s_add_u32 s49, s68, 981120\n\t"
            "buffer_load_dwordx4 v249, s[72:75], s49 offen lds\n\t"
            "s_mov_b32 exec_hi, -1\n\t"
            "s_mov_b32 exec_hi, 0x3fffff\n\t"
            "s_add_u32 m0, s66, 5232\n\t"
            "s_add_u32 s49, s68, 1177344\n\t"
            "buffer_load_dwordx4 v249, s[72:75], s49 offen lds\n\t"
            "s_mov_b32 exec_hi, -1\n\t"
            "v_cvt_pk_bf16_f32 v184, v184, v185\n\t"
            "v_cvt_pk_bf16_f32 v185, v186, v187\n\t"
            "v_cvt_pk_bf16_f32 v186, v188, v189\n\t"
            "v_cvt_pk_bf16_f32 v187, v190, v191\n\t"
            "v_mfma_f32_4x4x4_16b_bf16 v[196:199], v[184:185], v[184:185], v[196:199]\n\t"
            "s_mov_b32 exec_hi, 0x3fffff\n\t"
            "s_add_u32 m0, s66, 6104\n\t"
            "s_add_u32 s49, s68, 1373568\n\t"
            "buffer_load_dwordx4 v249, s[72:75], s49 offen lds\n\t"
            "s_mov_b32 exec_hi, -1\n\t"
            "v_mfma_f32_4x4x4_16b_bf16 v[196:199], v[186:187], v[186:187], v[196:199]\n\t"
            "s_mov_b32 exec_hi, 0x3fffff\n\t"
            "s_add_u32 m0, s66, 6976\n\t"
            "s_add_u32 s49, s68, 1569792\n\t"
            "buffer_load_dwordx4 v249, s[72:75], s49 offen lds\n\t"
            "s_mov_b32 exec_hi, -1\n\t"
            "v_mfma_f32_16x16x32_bf16 v[192:195], v[238:241], v[184:187], v[192:195]\n\t"
            "s_waitcnt vmcnt(9)\n\t"
            "ds_write_b128 v237, v[184:187] offset:256\n\t"
            "v_mfma_f32_16x16x32_bf16 v[144:147], v[0:3], v[200:203], 0\n\t"
            "v_add_u32 v237, s48, v245\n\t"
            "v_mfma_f32_16x16x32_bf16 v[144:147], v[4:7], v[204:207], v[144:147]\n\t"
            "v_mfma_f32_16x16x32_bf16 v[148:151], v[24:27], v[200:203], 0\n\t"
            "s_nop 1\n\t"
            "v_cndmask_b32_e64 v193, v196, v197, s[60:61]\n\t"
            "v_mfma_f32_16x16x32_bf16 v[148:151], v[28:31], v[204:207], v[148:151]\n\t"
            "v_cndmask_b32_e64 v193, v193, v198, s[62:63]\n\t"
            "v_cndmask_b32_e64 v193, v193, v199, s[64:65]\n\t"
            "v_mfma_f32_16x16x32_bf16 v[168:171], v[48:51], v[200:203], 0\n\t"
            "ds_read_b128 v[196:199], v232 offset:6272\n\t"
            "v_mfma_f32_16x16x32_bf16 v[168:171], v[52:55], v[204:207], v[168:171]\n\t"
            "ds_write_b64 v248, v[192:193] offset:18816\n\t"
            "v_mfma_f32_16x16x32_bf16 v[172:175], v[72:75], v[200:203], 0\n\t"
            "ds_read_b128 v[192:195], v235 offset:6144\n\t"
            "v_add_u32 v248, s48, v246\n\t"
            "v_mfma_f32_16x16x32_bf16 v[172:175], v[76:79], v[204:207], v[172:175]\n\t"
            "s_mov_b64 exec, 1\n\t"
            "ds_inc_u32 v255, v238 offset:7860\n\t"
            "s_mov_b64 exec, -1\n\t"
            "v_mfma_f32_16x16x32_bf16 v[152:155], v[96:99], v[200:203], 0\n\t"
            "v_mfma_f32_16x16x32_bf16 v[152:155], v[100:103], v[204:207], v[152:155]\n\t"
            "v_mfma_f32_16x16x32_bf16 v[156:159], v[120:123], v[200:203], 0\n\t"
            "ds_read_b128 v[200:203], v235 offset:6272\n\t"
            "v_mfma_f32_16x16x32_bf16 v[156:159], v[124:127], v[204:207], v[156:159]\n\t"
            "ds_read_b128 v[204:207], v232 offset:6400\n\t"
            "v_mfma_f32_16x16x32_bf16 v[144:147], v[8:11], v[208:211], v[144:147]\n\t"
            "v_mfma_f32_16x16x32_bf16 v[144:147], v[12:15], v[212:215], v[144:147]\n\t"
            "v_mfma_f32_16x16x32_bf16 v[148:151], v[32:35], v[208:211], v[148:151]\n\t"
            "v_mfma_f32_16x16x32_bf16 v[148:151], v[36:39], v[212:215], v[148:151]\n\t"
            "v_mfma_f32_16x16x32_bf16 v[168:171], v[56:59], v[208:211], v[168:171]\n\t"
            "v_mfma_f32_16x16x32_bf16 v[168:171], v[60:63], v[212:215], v[168:171]\n\t"
            "v_mfma_f32_16x16x32_bf16 v[172:175], v[80:83], v[208:211], v[172:175]\n\t"
            "v_mfma_f32_16x16x32_bf16 v[172:175], v[84:87], v[212:215], v[172:175]\n\t"
            "v_mfma_f32_16x16x32_bf16 v[152:155], v[104:107], v[208:211], v[152:155]\n\t"
            "v_mfma_f32_16x16x32_bf16 v[152:155], v[108:111], v[212:215], v[152:155]\n\t"
            "v_mfma_f32_16x16x32_bf16 v[156:159], v[128:131], v[208:211], v[156:159]\n\t"
            "ds_read_b128 v[208:211], v235 offset:6400\n\t"
            "v_mfma_f32_16x16x32_bf16 v[156:159], v[132:135], v[212:215], v[156:159]\n\t"
            "ds_read_b128 v[212:215], v236 offset:6144\n\t"
            "v_mfma_f32_16x16x32_bf16 v[144:147], v[16:19], v[216:219], v[144:147]\n\t"
            "v_mfma_f32_16x16x32_bf16 v[144:147], v[20:23], v[220:223], v[144:147]\n\t"
            "v_mfma_f32_16x16x32_bf16 v[148:151], v[40:43], v[216:219], v[148:151]\n\t"
            "v_mfma_f32_16x16x32_bf16 v[148:151], v[44:47], v[220:223], v[148:151]\n\t"
            "v_mfma_f32_16x16x32_bf16 v[168:171], v[64:67], v[216:219], v[168:171]\n\t"
            "v_mfma_f32_16x16x32_bf16 v[168:171], v[68:71], v[220:223], v[168:171]\n\t"
            "v_mfma_f32_16x16x32_bf16 v[172:175], v[88:91], v[216:219], v[172:175]\n\t"
            "v_mfma_f32_16x16x32_bf16 v[172:175], v[92:95], v[220:223], v[172:175]\n\t"
            "s_nop 1\n\t"
            "v_mul_f32 v144, v230, v144\n\t"
            "v_mfma_f32_16x16x32_bf16 v[152:155], v[112:115], v[216:219], v[152:155]\n\t"
            "v_mul_f32 v145, v230, v145\n\t"
            "v_mul_f32 v146, v230, v146\n\t"
            "v_mfma_f32_16x16x32_bf16 v[152:155], v[116:119], v[220:223], v[152:155]\n\t"
            "v_mul_f32 v148, v230, v148\n\t"
            "v_mul_f32 v149, v230, v149\n\t"
            "v_mfma_f32_16x16x32_bf16 v[156:159], v[136:139], v[216:219], v[156:159]\n\t"
            "ds_read_b128 v[216:219], v237 offset:6144\n\t"
            "v_mul_f32 v168, v230, v168\n\t"
            "v_mfma_f32_16x16x32_bf16 v[156:159], v[140:143], v[220:223], v[156:159]\n\t"
            "ds_read_b128 v[220:223], v248 offset:6144\n\t"
            "v_mul_f32 v169, v230, v169\n\t"
            "s_waitcnt lgkmcnt(7)\n\t"
            "v_mfma_f32_16x16x32_bf16 v[160:163], v[0:3], v[224:227], 0\n\t"
            "v_mul_f32 v172, v230, v172\n\t"
            "v_mul_f32 v173, v230, v173\n\t"
            "v_mfma_f32_16x16x32_bf16 v[160:163], v[4:7], v[192:195], v[160:163]\n\t"
            "v_mul_f32 v152, v230, v152\n\t"
            "v_mul_f32 v153, v230, v153\n\t"
            "v_mfma_f32_16x16x32_bf16 v[164:167], v[24:27], v[224:227], 0\n\t"
            "v_mul_f32 v156, v230, v156\n\t"
            "v_mul_f32 v157, v230, v157\n\t"
            "v_mfma_f32_16x16x32_bf16 v[164:167], v[28:31], v[192:195], v[164:167]\n\t"
            "v_mul_f32 v147, v230, v147\n\t"
            "v_cvt_pk_bf16_f32 v144, v144, v145\n\t"
            "v_mfma_f32_16x16x32_bf16 v[176:179], v[48:51], v[224:227], 0\n\t"
            "v_mul_f32 v150, v230, v150\n\t"
            "v_mul_f32 v151, v230, v151\n\t"
            "v_mfma_f32_16x16x32_bf16 v[176:179], v[52:55], v[192:195], v[176:179]\n\t"
            "v_cvt_pk_bf16_f32 v148, v148, v149\n\t"
            "v_mul_f32 v170, v230, v170\n\t"
            "v_mfma_f32_16x16x32_bf16 v[180:183], v[72:75], v[224:227], 0\n\t"
            "v_mul_f32 v171, v230, v171\n\t"
            "v_cvt_pk_bf16_f32 v168, v168, v169\n\t"
            "v_mfma_f32_16x16x32_bf16 v[180:183], v[76:79], v[192:195], v[180:183]\n\t"
            "v_mul_f32 v174, v230, v174\n\t"
            "v_mul_f32 v175, v230, v175\n\t"
            "v_mfma_f32_16x16x32_bf16 v[184:187], v[96:99], v[224:227], 0\n\t"
            "v_cvt_pk_bf16_f32 v172, v172, v173\n\t"
            "v_mul_f32 v154, v230, v154\n\t"
            "v_mfma_f32_16x16x32_bf16 v[184:187], v[100:103], v[192:195], v[184:187]\n\t"
            "v_mul_f32 v155, v230, v155\n\t"
            "v_cvt_pk_bf16_f32 v152, v152, v153\n\t"
            "v_mfma_f32_16x16x32_bf16 v[188:191], v[120:123], v[224:227], 0\n\t"
            "ds_read_b128 v[224:227], v236 offset:12288\n\t"
            "v_mul_f32 v158, v230, v158\n\t"
            "v_mfma_f32_16x16x32_bf16 v[188:191], v[124:127], v[192:195], v[188:191]\n\t"
            "v_mul_f32 v159, v230, v159\n\t"
            "v_cvt_pk_bf16_f32 v156, v156, v157\n\t"
            "s_waitcnt lgkmcnt(6)\n\t"
            "v_mfma_f32_16x16x32_bf16 v[160:163], v[8:11], v[196:199], v[160:163]\n\t"
            "v_cvt_pk_bf16_f32 v145, v146, v147\n\t"
            "v_cvt_pk_bf16_f32 v149, v150, v151\n\t"
            "v_mfma_f32_16x16x32_bf16 v[160:163], v[12:15], v[200:203], v[160:163]\n\t"
            "v_cvt_pk_bf16_f32 v169, v170, v171\n\t"
            "v_cvt_pk_bf16_f32 v173, v174, v175\n\t"
            "v_mfma_f32_16x16x32_bf16 v[164:167], v[32:35], v[196:199], v[164:167]\n\t"
            "v_cvt_pk_bf16_f32 v153, v154, v155\n\t"
            "v_cvt_pk_bf16_f32 v157, v158, v159\n\t"
            "v_mfma_f32_16x16x32_bf16 v[164:167], v[36:39], v[200:203], v[164:167]\n\t"
            "s_mov_b64 exec, -1\n\t"
            "ds_write_b64 v234, v[144:145]\n\t"
            "ds_write_b64 v234, v[148:149] offset:128\n\t"
            "ds_write_b64 v234, v[168:169] offset:256\n\t"
            "ds_write_b64 v228, v[172:173]\n\t"
            "ds_write_b64 v228, v[152:153] offset:128\n\t"
            "ds_write_b64 v228, v[156:157] offset:256\n\t"
            "s_mov_b64 exec, -1\n\t"
            "v_mfma_f32_16x16x32_bf16 v[176:179], v[56:59], v[196:199], v[176:179]\n\t"
            "v_mfma_f32_16x16x32_bf16 v[176:179], v[60:63], v[200:203], v[176:179]\n\t"
            "s_add_u32 s49, s33, 0\n\t"
            "buffer_load_dwordx4 v[0:3], v247, s[28:31], s49 offen sc1\n\t"
            "v_mfma_f32_16x16x32_bf16 v[180:183], v[80:83], v[196:199], v[180:183]\n\t"
            "v_mfma_f32_16x16x32_bf16 v[180:183], v[84:87], v[200:203], v[180:183]\n\t"
            "s_add_u32 s49, s33, 6144\n\t"
            "buffer_load_dwordx4 v[24:27], v247, s[28:31], s49 offen sc1\n\t"
            "v_mfma_f32_16x16x32_bf16 v[184:187], v[104:107], v[196:199], v[184:187]\n\t"
            "v_mfma_f32_16x16x32_bf16 v[184:187], v[108:111], v[200:203], v[184:187]\n\t"
            "s_add_u32 s49, s33, 12288\n\t"
            "buffer_load_dwordx4 v[48:51], v247, s[28:31], s49 offen sc1\n\t"
            "v_mfma_f32_16x16x32_bf16 v[188:191], v[128:131], v[196:199], v[188:191]\n\t"
            "v_mfma_f32_16x16x32_bf16 v[188:191], v[132:135], v[200:203], v[188:191]\n\t"
            "ds_read_b128 v[200:203], v236\n\t"
            "s_add_u32 s49, s33, 18432\n\t"
            "buffer_load_dwordx4 v[72:75], v247, s[28:31], s49 offen sc1\n\t"
            "s_waitcnt lgkmcnt(11)\n\t"
            "v_mfma_f32_16x16x32_bf16 v[160:163], v[16:19], v[204:207], v[160:163]\n\t"
            "v_mfma_f32_16x16x32_bf16 v[160:163], v[20:23], v[208:211], v[160:163]\n\t"
            "s_add_u32 s49, s33, 24576\n\t"
            "buffer_load_dwordx4 v[96:99], v247, s[28:31], s49 offen sc1\n\t"
            "v_mfma_f32_16x16x32_bf16 v[164:167], v[40:43], v[204:207], v[164:167]\n\t"
            "v_mfma_f32_16x16x32_bf16 v[164:167], v[44:47], v[208:211], v[164:167]\n\t"
            "s_add_u32 s49, s33, 30720\n\t"
            "buffer_load_dwordx4 v[120:123], v247, s[28:31], s49 offen sc1\n\t"
            "v_mfma_f32_16x16x32_bf16 v[176:179], v[64:67], v[204:207], v[176:179]\n\t"
            "s_nop 0\n\t"
            "v_mul_f32 v160, v231, v160\n\t"
            "v_mul_f32 v161, v231, v161\n\t"
            "v_mfma_f32_16x16x32_bf16 v[176:179], v[68:71], v[208:211], v[176:179]\n\t"
            "v_mul_f32 v162, v231, v162\n\t"
            "v_mul_f32 v163, v231, v163\n\t"
            "v_mfma_f32_16x16x32_bf16 v[180:183], v[88:91], v[204:207], v[180:183]\n\t"
            "v_mul_f32 v164, v231, v164\n\t"
            "v_mul_f32 v165, v231, v165\n\t"
            "v_mfma_f32_16x16x32_bf16 v[180:183], v[92:95], v[208:211], v[180:183]\n\t"
            "v_cvt_pk_bf16_f32 v160, v160, v161\n\t"
            "v_mul_f32 v166, v231, v166\n\t"
            "v_mfma_f32_16x16x32_bf16 v[184:187], v[112:115], v[204:207], v[184:187]\n\t"
            "v_mul_f32 v176, v231, v176\n\t"
            "v_mul_f32 v177, v231, v177\n\t"
            "v_mfma_f32_16x16x32_bf16 v[184:187], v[116:119], v[208:211], v[184:187]\n\t"
            "v_mul_f32 v167, v231, v167\n\t"
            "v_cvt_pk_bf16_f32 v164, v164, v165\n\t"
            "v_mfma_f32_16x16x32_bf16 v[188:191], v[136:139], v[204:207], v[188:191]\n\t"
            "ds_read_b128 v[204:207], v237\n\t"
            "v_mul_f32 v180, v231, v180\n\t"
            "v_mfma_f32_16x16x32_bf16 v[188:191], v[140:143], v[208:211], v[188:191]\n\t"
            "ds_read_b128 v[208:211], v248\n\t"
            "v_mul_f32 v181, v231, v181\n\t"
            "v_mul_f32 v184, v231, v184\n\t"
            "v_mul_f32 v185, v231, v185\n\t"
            "v_mul_f32 v178, v231, v178\n\t"
            "v_mul_f32 v179, v231, v179\n\t"
            "v_cvt_pk_bf16_f32 v176, v176, v177\n\t"
            "v_mul_f32 v182, v231, v182\n\t"
            "v_mul_f32 v188, v231, v188\n\t"
            "v_mul_f32 v189, v231, v189\n\t"
            "v_mul_f32 v183, v231, v183\n\t"
            "v_cvt_pk_bf16_f32 v180, v180, v181\n\t"
            "v_mul_f32 v186, v231, v186\n\t"
            "v_mul_f32 v187, v231, v187\n\t"
            "v_cvt_pk_bf16_f32 v184, v184, v185\n\t"
            "v_mul_f32 v190, v231, v190\n\t"
            "v_mul_f32 v191, v231, v191\n\t"
            "v_cvt_pk_bf16_f32 v188, v188, v189\n\t"
            "v_cvt_pk_bf16_f32 v161, v162, v163\n\t"
            "v_cvt_pk_bf16_f32 v165, v166, v167\n\t"
            "v_cvt_pk_bf16_f32 v177, v178, v179\n\t"
            "v_cvt_pk_bf16_f32 v181, v182, v183\n\t"
            "v_cvt_pk_bf16_f32 v185, v186, v187\n\t"
            "v_cvt_pk_bf16_f32 v189, v190, v191\n\t"
            "s_waitcnt lgkmcnt(9)\n\t"
            "s_mov_b64 exec, s[80:81]\n\t"
            "ds_write_b64 v234, v[160:161] offset:6144\n\t"
            "ds_write_b64 v234, v[164:165] offset:6272\n\t"
            "ds_write_b64 v234, v[176:177] offset:6400\n\t"
            "ds_write_b64 v228, v[180:181] offset:6144\n\t"
            "ds_write_b64 v228, v[184:185] offset:6272\n\t"
            "ds_write_b64 v228, v[188:189] offset:6400\n\t"
            "s_mov_b64 exec, -1\n\t"
            "s_waitcnt lgkmcnt(14)\n\t"
            "ds_read_b128 v[228:231], v237 offset:12288\n\t"
            "s_waitcnt lgkmcnt(14)\n\t"
            "ds_read_b128 v[232:235], v248 offset:12288\n\t"
            "s_add_u32 s49, s33, 1024\n\t"
            "buffer_load_dwordx4 v[4:7], v247, s[28:31], s49 offen sc1\n\t"
            "s_add_u32 s49, s33, 7168\n\t"
            "buffer_load_dwordx4 v[28:31], v247, s[28:31], s49 offen sc1\n\t"
            "s_waitcnt lgkmcnt(0)\n\t"
            "s_barrier\n\t"
            "s_setprio 3\n\t"
            "s_add_u32 s49, s33, 13312\n\t"
            "buffer_load_dwordx4 v[52:55], v247, s[28:31], s49 offen sc1\n\t"
            "s_add_u32 s49, s33, 19456\n\t"
            "buffer_load_dwordx4 v[76:79], v247, s[28:31], s49 offen sc1\n\t"
            "s_add_u32 s49, s33, 25600\n\t"
            "buffer_load_dwordx4 v[100:103], v247, s[28:31], s49 offen sc1\n\t"
            "s_add_u32 s49, s33, 31744\n\t"
            "buffer_load_dwordx4 v[124:127], v247, s[28:31], s49 offen sc1\n\t"
            "s_add_u32 s49, s33, 2048\n\t"
            "buffer_load_dwordx4 v[8:11], v247, s[28:31], s49 offen sc1\n\t"
            "s_add_u32 s49, s33, 8192\n\t"
            "buffer_load_dwordx4 v[32:35], v247, s[28:31], s49 offen sc1\n\t"
            "s_add_u32 s49, s33, 14336\n\t"
            "buffer_load_dwordx4 v[56:59], v247, s[28:31], s49 offen sc1\n\t"
            "s_add_u32 s49, s33, 20480\n\t"
            "buffer_load_dwordx4 v[80:83], v247, s[28:31], s49 offen sc1\n\t"
            "s_add_u32 s49, s33, 26624\n\t"
            "buffer_load_dwordx4 v[104:107], v247, s[28:31], s49 offen sc1\n\t"
            "s_add_u32 s49, s33, 32768\n\t"
            "buffer_load_dwordx4 v[128:131], v247, s[28:31], s49 offen sc1\n\t"
            "s_add_u32 s49, s33, 3072\n\t"
            "buffer_load_dwordx4 v[12:15], v247, s[28:31], s49 offen sc1\n\t"
            "s_add_u32 s49, s33, 9216\n\t"
            "buffer_load_dwordx4 v[36:39], v247, s[28:31], s49 offen sc1\n\t"
            "s_add_u32 s49, s33, 15360\n\t"
            "buffer_load_dwordx4 v[60:63], v247, s[28:31], s49 offen sc1\n\t"
            "s_add_u32 s49, s33, 21504\n\t"
            "buffer_load_dwordx4 v[84:87], v247, s[28:31], s49 offen sc1\n\t"
            "s_add_u32 s49, s33, 27648\n\t"
            "buffer_load_dwordx4 v[108:111], v247, s[28:31], s49 offen sc1\n\t"
            "s_add_u32 s49, s33, 33792\n\t"
            "buffer_load_dwordx4 v[132:135], v247, s[28:31], s49 offen sc1\n\t"
            "s_add_u32 s49, s33, 4096\n\t"
            "buffer_load_dwordx4 v[16:19], v247, s[28:31], s49 offen sc1\n\t"
            "s_add_u32 s49, s33, 10240\n\t"
            "buffer_load_dwordx4 v[40:43], v247, s[28:31], s49 offen sc1\n\t"
            "s_add_u32 s49, s33, 16384\n\t"
            "buffer_load_dwordx4 v[64:67], v247, s[28:31], s49 offen sc1\n\t"
            "s_add_u32 s49, s33, 22528\n\t"
            "buffer_load_dwordx4 v[88:91], v247, s[28:31], s49 offen sc1\n\t"
            "s_add_u32 s49, s33, 28672\n\t"
            "buffer_load_dwordx4 v[112:115], v247, s[28:31], s49 offen sc1\n\t"
            "s_add_u32 s49, s33, 34816\n\t"
            "buffer_load_dwordx4 v[136:139], v247, s[28:31], s49 offen sc1\n\t"
            "s_add_u32 s49, s33, 5120\n\t"
            "buffer_load_dwordx4 v[20:23], v247, s[28:31], s49 offen sc1\n\t"
            "s_add_u32 s49, s33, 11264\n\t"
            "buffer_load_dwordx4 v[44:47], v247, s[28:31], s49 offen sc1\n\t"
            "s_add_u32 s49, s33, 17408\n\t"
            "buffer_load_dwordx4 v[68:71], v247, s[28:31], s49 offen sc1\n\t"
            "s_add_u32 s49, s33, 23552\n\t"
            "buffer_load_dwordx4 v[92:95], v247, s[28:31], s49 offen sc1\n\t"
            "s_add_u32 s49, s33, 29696\n\t"
            "buffer_load_dwordx4 v[116:119], v247, s[28:31], s49 offen sc1\n\t"
            "s_add_u32 s49, s33, 35840\n\t"
            "buffer_load_dwordx4 v[140:143], v247, s[28:31], s49 offen sc1\n\t"
            "s_add_u32 s59, s59, s84\n\t"
            "s_movk_i32 s84, 800\n\t"
            "s_add_u32 s45, s46, s27\n\t"
            "s_mov_b32 s46, s47\n\t"
            "s_mov_b32 s47, s68\n\t"
            "s_xor_b32 s48, s48, s71\n\t"
            "s_xor_b32 s67, s67, s71\n\t"
            "s_add_u32 s76, s76, 4\n\t"
            "s_add_u32 s70, s70, 4\n\t"
            "s_lshl_b32 s56, s56, 2\n\t"
            "s_lshr_b32 s49, s26, 30\n\t"
            "s_or_b32 s56, s56, s49\n\t"
            "s_cmp_eq_u32 s82, 0\n\t"
            "s_cbranch_scc1 .Lwk_nr%=\n\t"
            "s_waitcnt lgkmcnt(0)\n\t"
            "s_nop 7\n\t"
            "v_add_u32 v236, 127360, v247\n\t"
            "v_add_u32 v237, 136704, v247\n\t"
            "ds_read_b128 v[144:147], v236\n\t"
            "ds_read_b128 v[148:151], v237\n\t"
            "s_waitcnt lgkmcnt(0)\n\t"
            "s_mov_b32 exec_hi, 0x3ffff\n\t"
            "buffer_store_dwordx4 v[144:147], v247, s[28:31], s82 offen sc1\n\t"
            "s_mov_b64 exec, 0xffffff\n\t"
            "buffer_store_dwordx4 v[148:151], v247, s[28:31], s82 offen offset:800 sc1\n\t"
            "s_mov_b64 exec, -1\n\t"
            "ds_read_b128 v[152:155], v252 offset:2560\n\t"
            "s_waitcnt vmcnt(0) lgkmcnt(0)\n\t"
            "s_mov_b64 exec, 1\n\t"
            "buffer_store_dwordx4 v[152:155], off, s[28:31], s82 offset:1216 sc1\n\t"
            "s_mov_b64 exec, -1\n\t"
            "s_branch .Lwk_n2%=\n\t"
            ".Lwk_nr%=:\n\t"
            "s_waitcnt vmcnt(0)\n\t"
            ".Lwk_n2%=:\n\t"
            "s_nop 1\n\t"
            "s_nop 7\n\t"
            ".p2align 6\n\t"
            "s_nop 0\n\t"
            "s_nop 0\n\t"
            "s_nop 0\n\t"
            "s_nop 0\n\t"
            "s_nop 0\n\t"
            "s_nop 0\n\t"
            "s_nop 0\n\t"
            ".Lwb_loop%=:\n\t"
            "s_setprio 1\n\t"
            "ds_read_b128 v[168:171], v254\n\t"
            "ds_read_b128 v[172:175], v254 offset:64\n\t"
            "s_waitcnt vmcnt(9)\n\t"
            "ds_read_b64 v[144:145], v253\n\t"
            "ds_read_b64 v[146:147], v253 offset:96\n\t"
            "ds_read_b64 v[148:149], v253 offset:1744\n\t"
            "ds_read_b64 v[150:151], v253 offset:1840\n\t"
            "ds_read_b64 v[152:153], v253 offset:8\n\t"
            "ds_read_b64 v[154:155], v253 offset:104\n\t"
            "ds_read_b64 v[156:157], v253 offset:1752\n\t"
            "ds_read_b64 v[158:159], v253 offset:1848\n\t"
            "ds_read_b64 v[160:161], v253 offset:16\n\t"
            "ds_read_b64 v[162:163], v253 offset:112\n\t"
            "ds_read_b64 v[164:165], v253 offset:1760\n\t"
            "ds_read_b64 v[166:167], v253 offset:1856\n\t"
            "v_mov_b64 v[176:177], 0\n\t"
            "v_mov_b64 v[178:179], 0\n\t"
            "v_mov_b64 v[180:181], 0\n\t"
            "v_mov_b64 v[182:183], 0\n\t"
            "v_mov_b32 v249, s76\n\t"
            "v_mov_b64 v[184:185], 0\n\t"
            "v_mov_b64 v[186:187], 0\n\t"
            "v_mov_b64 v[188:189], 0\n\t"
            "v_mov_b64 v[190:191], 0\n\t"
            "v_add_u32 v236, s67, v250\n\t"
            "v_add_u32 v248, s67, v251\n\t"
            "v_mov_b32 v247, 0\n\t"
            "ds_read_b32 v249, v249\n\t"
            "s_setprio 3\n\t"
            "s_waitcnt lgkmcnt(9)\n\t"
            "v_smfmac_f32_16x16x64_bf16 v[176:179], v[168:171], v[144:151], v255\n\t"
            "ds_read_b128 v[168:171], v254 offset:128\n\t"
            "s_waitcnt lgkmcnt(6)\n\t"
            "v_smfmac_f32_16x16x64_bf16 v[180:183], v[172:175], v[152:159], v255\n\t"
            "s_setprio 1\n\t"
            "ds_read_b64 v[144:145], v253 offset:24\n\t"
            "ds_read_b64 v[146:147], v253 offset:120\n\t"
            "ds_read_b64 v[148:149], v253 offset:1768\n\t"
            "ds_read_b64 v[150:151], v253 offset:1864\n\t"
            "ds_read_b128 v[172:175], v254 offset:192\n\t"
            "ds_read_b64 v[152:153], v253 offset:32\n\t"
            "ds_read_b64 v[154:155], v253 offset:128\n\t"
            "ds_read_b64 v[156:157], v253 offset:1776\n\t"
            "ds_read_b64 v[158:159], v253 offset:1872\n\t"
            "v_cvt_pk_bf16_f32 v176, v176, v177\n\t"
            "v_cvt_pk_bf16_f32 v177, v178, v179\n\t"
            "v_cvt_pk_bf16_f32 v178, v180, v181\n\t"
            "v_cvt_pk_bf16_f32 v179, v182, v183\n\t"
            "s_setprio 3\n\t"
            "v_mfma_f32_4x4x4_16b_bf16 v[196:199], v[176:177], v[176:177], 0\n\t"
            "v_mov_b64 v[180:181], 0\n\t"
            "v_mfma_f32_16x16x32_bf16 v[192:195], v[238:241], v[176:179], 0\n\t"
            "s_waitcnt lgkmcnt(10)\n\t"
            "v_readfirstlane_b32 s26, v249\n\t"
            "v_mov_b64 v[182:183], 0\n\t"
            "v_mfma_f32_4x4x4_16b_bf16 v[196:199], v[178:179], v[178:179], v[196:199]\n\t"
            "ds_read_b32 v249, v252 offset:12\n\t"
            "s_and_b32 s68, s26, 0x7ffffff\n\t"
            "s_add_u32 s68, s68, s78\n\t"
            "s_and_b32 s69, s26, 0x38000000\n\t"
            "s_or_b32 s69, s69, s39\n\t"
            "s_bitcmp1_b32 s26, 27\n\t"
            "s_cselect_b32 s79, 2.0, s68\n\t"
            "s_and_b32 s57, s56, 3\n\t"
            "s_lshl_b32 s58, s57, 3\n\t"
            "s_lshr_b32 m0, s54, s58\n\t"
            "s_lshl_b32 s58, s57, 4\n\t"
            "s_lshr_b64 s[36:37], s[50:51], s58\n\t"
            "s_and_b32 s57, s36, 0xffff\n\t"
            "s_add_u32 s57, s57, s67\n\t"
            "v_writelane_b32 v236, s57, 63\n\t"
            "v_writelane_b32 v236, s59, m0\n\t"
            "v_xor_b32 v237, 16, v236\n\t"
            "s_lshr_b64 s[36:37], s[52:53], s58\n\t"
            "s_and_b32 s58, s36, 0xffff\n\t"
            "s_add_u32 s58, s58, s67\n\t"
            "ds_write_b128 v236, v[176:179]\n\t"
            "s_waitcnt lgkmcnt(11)\n\t"
            "v_smfmac_f32_16x16x64_bf16 v[184:187], v[168:171], v[160:167], v255\n\t"
            "s_and_b32 s38, s52, 0xffff\n\t"
            "s_add_u32 s38, s38, s67\n\t"
            "ds_read_b128 v[168:171], v254 offset:256\n\t"
            "s_waitcnt lgkmcnt(7)\n\t"
            "v_smfmac_f32_16x16x64_bf16 v[188:191], v[172:175], v[144:151], v255\n\t"
            "s_setprio 1\n\t"
            "ds_read_b64 v[160:161], v253 offset:40\n\t"
            "ds_read_b64 v[162:163], v253 offset:136\n\t"
            "ds_read_b64 v[164:165], v253 offset:1784\n\t"
            "ds_read_b64 v[166:167], v253 offset:1880\n\t"
            "ds_read_b128 v[172:175], v254 offset:320\n\t"
            "ds_read_b64 v[144:145], v253 offset:48\n\t"
            "ds_read_b64 v[146:147], v253 offset:144\n\t"
            "ds_read_b64 v[148:149], v253 offset:1792\n\t"
            "s_waitcnt lgkmcnt(14)\n\t"
            "ds_read_b64 v[150:151], v253 offset:1888\n\t"
            "v_mov_b64 v[176:177], 0\n\t"
            "v_mov_b64 v[178:179], 0\n\t"
            "v_cvt_pk_bf16_f32 v184, v184, v185\n\t"
            "v_cvt_pk_bf16_f32 v185, v186, v187\n\t"
            "v_cvt_pk_bf16_f32 v186, v188, v189\n\t"
            "v_cvt_pk_bf16_f32 v187, v190, v191\n\t"
            "s_setprio 3\n\t"
            "v_mfma_f32_4x4x4_16b_bf16 v[196:199], v[184:185], v[184:185], v[196:199]\n\t"
            "s_waitcnt lgkmcnt(14)\n\t"
            "ds_write_b128 v237, v[184:187]\n\t"
            "v_mfma_f32_16x16x32_bf16 v[192:195], v[238:241], v[184:187], v[192:195]\n\t"
            "v_mov_b64 v[184:185], 0\n\t"
            "v_mov_b64 v[188:189], 0\n\t"
            "v_mfma_f32_4x4x4_16b_bf16 v[196:199], v[186:187], v[186:187], v[196:199]\n\t"
            "v_mov_b64 v[186:187], 0\n\t"
            "v_mov_b64 v[190:191], 0\n\t"
            "v_writelane_b32 v248, s58, 63\n\t"
            "v_writelane_b32 v248, s38, m0\n\t"
            "s_waitcnt lgkmcnt(12)\n\t"
            "v_and_b32 v249, s69, v249\n\t"
            "s_waitcnt lgkmcnt(10)\n\t"
            "v_smfmac_f32_16x16x64_bf16 v[176:179], v[168:171], v[152:159], v255\n\t"
            "ds_read_b128 v[168:171], v254 offset:384\n\t"
            "ds_read_b64 v[152:153], v253 offset:56\n\t"
            "ds_read_b64 v[154:155], v253 offset:152\n\t"
            "ds_read_b64 v[156:157], v253 offset:1800\n\t"
            "ds_read_b64 v[158:159], v253 offset:1896\n\t"
            "s_waitcnt lgkmcnt(10)\n\t"
            "v_smfmac_f32_16x16x64_bf16 v[180:183], v[172:175], v[160:167], v255\n\t"
            "s_setprio 1\n\t"
            "ds_read_b128 v[172:175], v254 offset:448\n\t"
            "ds_read_b64 v[160:161], v253 offset:64\n\t"
            "ds_read_b64 v[162:163], v253 offset:160\n\t"
            "ds_read_b64 v[164:165], v253 offset:1808\n\t"
            "ds_read_b64 v[166:167], v253 offset:1904\n\t"
            "v_cvt_pk_bf16_f32 v176, v176, v177\n\t"
            "v_cvt_pk_bf16_f32 v177, v178, v179\n\t"
            "v_cvt_pk_bf16_f32 v178, v180, v181\n\t"
            "v_cvt_pk_bf16_f32 v179, v182, v183\n\t"
            "s_setprio 3\n\t"
            "v_mfma_f32_4x4x4_16b_bf16 v[196:199], v[176:177], v[176:177], v[196:199]\n\t"
            "s_waitcnt lgkmcnt(14)\n\t"
            "ds_write_b128 v236, v[176:179] offset:128\n\t"
            "v_mfma_f32_16x16x32_bf16 v[192:195], v[238:241], v[176:179], v[192:195]\n\t"
            "v_mov_b64 v[176:177], 0\n\t"
            "v_mov_b64 v[180:181], 0\n\t"
            "v_mfma_f32_4x4x4_16b_bf16 v[196:199], v[178:179], v[178:179], v[196:199]\n\t"
            "v_mov_b64 v[178:179], 0\n\t"
            "v_mov_b64 v[182:183], 0\n\t"
            "s_waitcnt lgkmcnt(10)\n\t"
            "v_smfmac_f32_16x16x64_bf16 v[184:187], v[168:171], v[144:151], v255\n\t"
            "ds_read_b128 v[168:171], v254 offset:512\n\t"
            "ds_read_b64 v[144:145], v253 offset:72\n\t"
            "ds_read_b64 v[146:147], v253 offset:168\n\t"
            "ds_read_b64 v[148:149], v253 offset:1816\n\t"
            "ds_read_b64 v[150:151], v253 offset:1912\n\t"
            "s_waitcnt lgkmcnt(10)\n\t"
            "v_smfmac_f32_16x16x64_bf16 v[188:191], v[172:175], v[152:159], v255\n\t"
            "s_setprio 1\n\t"
            "ds_read_b128 v[172:175], v254 offset:576\n\t"
            "ds_read_b64 v[152:153], v253 offset:80\n\t"
            "ds_read_b64 v[154:155], v253 offset:176\n\t"
            "ds_read_b64 v[156:157], v253 offset:1824\n\t"
            "ds_read_b64 v[158:159], v253 offset:1920\n\t"
            "v_cvt_pk_bf16_f32 v184, v184, v185\n\t"
            "v_cvt_pk_bf16_f32 v185, v186, v187\n\t"
            "v_cvt_pk_bf16_f32 v186, v188, v189\n\t"
            "v_cvt_pk_bf16_f32 v187, v190, v191\n\t"
            "s_setprio 3\n\t"
            "v_mfma_f32_4x4x4_16b_bf16 v[196:199], v[184:185], v[184:185], v[196:199]\n\t"
            "s_waitcnt lgkmcnt(14)\n\t"
            "ds_write_b128 v237, v[184:187] offset:128\n\t"
            "v_mfma_f32_16x16x32_bf16 v[192:195], v[238:241], v[184:187], v[192:195]\n\t"
            "v_mov_b64 v[184:185], 0\n\t"
            "v_mov_b64 v[188:189], 0\n\t"
            "v_mfma_f32_4x4x4_16b_bf16 v[196:199], v[186:187], v[186:187], v[196:199]\n\t"
            "v_mov_b64 v[186:187], 0\n\t"
            "v_mov_b64 v[190:191], 0\n\t"
            "s_waitcnt lgkmcnt(10)\n\t"
            "v_smfmac_f32_16x16x64_bf16 v[176:179], v[168:171], v[160:167], v255\n\t"
            "ds_read_b128 v[168:171], v254 offset:640\n\t"
            "ds_read_b64 v[160:161], v253 offset:88\n\t"
            "ds_read_b64 v[162:163], v253 offset:184\n\t"
            "ds_read_b64 v[164:165], v253 offset:1832\n\t"
            "ds_read_b64 v[166:167], v253 offset:1928\n\t"
            "s_waitcnt lgkmcnt(10)\n\t"
            "v_smfmac_f32_16x16x64_bf16 v[180:183], v[172:175], v[144:151], v255\n\t"
            "s_setprio 1\n\t"
            "ds_read_b128 v[172:175], v254 offset:704\n\t"
            "v_lshlrev_b32 v144, 16, v212\n\t"
            "v_cvt_pk_bf16_f32 v176, v176, v177\n\t"
            "v_cvt_pk_bf16_f32 v177, v178, v179\n\t"
            "v_and_b32 v145, 0xffff0000, v212\n\t"
            "v_lshlrev_b32 v146, 16, v213\n\t"
            "s_setprio 3\n\t"
            "v_mfma_f32_4x4x4_16b_bf16 v[196:199], v[176:177], v[176:177], v[196:199]\n\t"
            "v_and_b32 v147, 0xffff0000, v213\n\t"
            "v_cvt_pk_bf16_f32 v178, v180, v181\n\t"
            "v_cvt_pk_bf16_f32 v179, v182, v183\n\t"
            "v_lshlrev_b32 v180, 16, v210\n\t"
            "v_and_b32 v181, 0xffff0000, v210\n\t"
            "v_mfma_f32_4x4x4_16b_bf16 v[196:199], v[178:179], v[178:179], v[196:199]\n\t"
            "ds_write_b128 v236, v[176:179] offset:256\n\t"
            "v_mfma_f32_16x16x32_bf16 v[192:195], v[238:241], v[176:179], v[192:195]\n\t"
            "s_setprio 1\n\t"
            "v_lshlrev_b32 v176, 16, v208\n\t"
            "v_and_b32 v177, 0xffff0000, v208\n\t"
            "v_lshlrev_b32 v178, 16, v209\n\t"
            "v_and_b32 v179, 0xffff0000, v209\n\t"
            "v_lshlrev_b32 v182, 16, v211\n\t"
            "v_and_b32 v183, 0xffff0000, v211\n\t"
            "v_lshlrev_b32 v148, 16, v214\n\t"
            "v_and_b32 v149, 0xffff0000, v214\n\t"
            "v_lshlrev_b32 v150, 16, v215\n\t"
            "v_and_b32 v151, 0xffff0000, v215\n\t"
            "v_add_u32 v236, s48, v244\n\t"
            "ds_read_b128 v[212:215], v236 offset:6144\n\t"
            "s_setprio 3\n\t"
            "s_waitcnt lgkmcnt(7)\n\t"
            "v_smfmac_f32_16x16x64_bf16 v[184:187], v[168:171], v[152:159], v255\n\t"
            "v_lshlrev_b32 v152, 16, v200\n\t"
            "v_and_b32 v153, 0xffff0000, v200\n\t"
            "v_lshlrev_b32 v154, 16, v201\n\t"
            "v_and_b32 v155, 0xffff0000, v201\n\t"
            "v_lshlrev_b32 v156, 16, v202\n\t"
            "s_waitcnt lgkmcnt(2)\n\t"
            "v_smfmac_f32_16x16x64_bf16 v[188:191], v[172:175], v[160:167], v255\n\t"
            "s_setprio 1\n\t"
            "s_mov_b32 exec_hi, 0x3fffff\n\t"
            "s_add_u32 m0, s66, 0\n\t"
            "s_add_u32 s49, s79, 0\n\t"
            "buffer_load_dwordx4 v249, s[88:91], s49 offen lds\n\t"
            "s_mov_b32 exec_hi, -1\n\t"
            "s_mov_b32 exec_hi, 0x3fffff\n\t"
            "s_add_u32 m0, s66, 872\n\t"
            "s_add_u32 s49, s68, 196224\n\t"
            "buffer_load_dwordx4 v249, s[72:75], s49 offen lds\n\t"
            "s_mov_b32 exec_hi, -1\n\t"
            "s_mov_b32 exec_hi, 0x3fffff\n\t"
            "s_add_u32 m0, s66, 1744\n\t"
            "s_add_u32 s49, s68, 392448\n\t"
            "buffer_load_dwordx4 v249, s[72:75], s49 offen lds\n\t"
            "s_mov_b32 exec_hi, -1\n\t"
            "s_mov_b32 exec_hi, 0x3fffff\n\t"
            "s_add_u32 m0, s66, 2616\n\t"
            "s_add_u32 s49, s68, 588672\n\t"
            "buffer_load_dwordx4 v249, s[72:75], s49 offen lds\n\t"
            "s_mov_b32 exec_hi, -1\n\t"
            "ds_read_b32 v247, v247 offset:7856\n\t"
            "s_mov_b32 exec_hi, 0x3fffff\n\t"
            "s_add_u32 m0, s66, 3488\n\t"
            "s_add_u32 s49, s68, 784896\n\t"
            "buffer_load_dwordx4 v249, s[72:75], s49 offen lds\n\t"
            "s_mov_b32 exec_hi, -1\n\t"
            "s_mov_b32 exec_hi, 0x3fffff\n\t"
            "s_add_u32 m0, s66, 4360\n\t"
            "s_add_u32 s49, s68, 981120\n\t"
            "buffer_load_dwordx4 v249, s[72:75], s49 offen lds\n\t"
            "s_mov_b32 exec_hi, -1\n\t"
            "s_mov_b32 exec_hi, 0x3fffff\n\t"
            "s_add_u32 m0, s66, 5232\n\t"
            "s_add_u32 s49, s68, 1177344\n\t"
            "buffer_load_dwordx4 v249, s[72:75], s49 offen lds\n\t"
            "s_mov_b32 exec_hi, -1\n\t"
            "s_mov_b32 exec_hi, 0x3fffff\n\t"
            "s_add_u32 m0, s66, 6104\n\t"
            "s_add_u32 s49, s68, 1373568\n\t"
            "buffer_load_dwordx4 v249, s[72:75], s49 offen lds\n\t"
            "s_mov_b32 exec_hi, -1\n\t"
            "s_mov_b32 exec_hi, 0x3fffff\n\t"
            "s_add_u32 m0, s66, 6976\n\t"
            "s_add_u32 s49, s68, 1569792\n\t"
            "buffer_load_dwordx4 v249, s[72:75], s49 offen lds\n\t"
            "s_mov_b32 exec_hi, -1\n\t"
            "v_cvt_pk_bf16_f32 v184, v184, v185\n\t"
            "v_cvt_pk_bf16_f32 v185, v186, v187\n\t"
            "v_cvt_pk_bf16_f32 v186, v188, v189\n\t"
            "v_cvt_pk_bf16_f32 v187, v190, v191\n\t"
            "s_setprio 3\n\t"
            "v_mfma_f32_4x4x4_16b_bf16 v[196:199], v[184:185], v[184:185], v[196:199]\n\t"
            "ds_write_b128 v237, v[184:187] offset:256\n\t"
            "v_mfma_f32_16x16x32_bf16 v[192:195], v[238:241], v[184:187], v[192:195]\n\t"
            "s_waitcnt lgkmcnt(1)\n\t"
            "v_readfirstlane_b32 s77, v247\n\t"
            "s_cmp_ge_u32 s77, s70\n\t"
            "s_cbranch_scc0 .Lwb_spin%=\n\t"
            ".Lwb_go%=:\n\t"
            "v_mfma_f32_4x4x4_16b_bf16 v[196:199], v[186:187], v[186:187], v[196:199]\n\t"
            "s_setprio 1\n\t"
            "ds_read_b128 v[184:187], v242\n\t"
            "ds_read_b128 v[188:191], v243\n\t"
            "ds_read_b128 v[172:175], v243 offset:128\n\t"
            "v_and_b32 v157, 0xffff0000, v202\n\t"
            "v_lshlrev_b32 v158, 16, v203\n\t"
            "v_and_b32 v159, 0xffff0000, v203\n\t"
            "v_lshlrev_b32 v160, 16, v204\n\t"
            "v_and_b32 v161, 0xffff0000, v204\n\t"
            "v_lshlrev_b32 v162, 16, v205\n\t"
            "v_cndmask_b32_e64 v193, v196, v197, s[60:61]\n\t"
            "v_cndmask_b32_e64 v193, v193, v198, s[62:63]\n\t"
            "v_cndmask_b32_e64 v193, v193, v199, s[64:65]\n\t"
            "v_and_b32 v163, 0xffff0000, v205\n\t"
            "v_lshlrev_b32 v164, 16, v206\n\t"
            "v_and_b32 v165, 0xffff0000, v206\n\t"
            "v_lshlrev_b32 v166, 16, v207\n\t"
            "v_and_b32 v167, 0xffff0000, v207\n\t"
            "v_lshlrev_b32 v168, 16, v216\n\t"
            "v_and_b32 v169, 0xffff0000, v216\n\t"
            "v_lshlrev_b32 v170, 16, v217\n\t"
            "v_and_b32 v171, 0xffff0000, v217\n\t"
            "s_bfe_u32 s57, s56, 0x20004\n\t"
            "s_lshl_b32 s58, s57, 3\n\t"
            "s_lshr_b32 s58, s55, s58\n\t"
            "s_lshl_b64 s[36:37], s[34:35], s58\n\t"
            "ds_read_b128 v[196:199], v252\n\t"
            "ds_read_b128 v[200:203], v236\n\t"
            "ds_write_b64 v248, v[192:193] offset:18816\n\t"
            "ds_read_b128 v[192:195], v242 offset:128\n\t"
            "s_bitcmp1_b32 s57, 1\n\t"
            "s_cselect_b32 s58, 1177344, 0\n\t"
            "s_bitcmp1_b32 s57, 0\n\t"
            "s_cselect_b32 s57, 2304, 0\n\t"
            "s_add_u32 s57, s57, s58\n\t"
            "s_mov_b64 exec, 1\n\t"
            "ds_inc_u32 v255, v238 offset:7860\n\t"
            "s_mov_b64 exec, -1\n\t"
            "v_add_u32 v237, s48, v245\n\t"
            "v_add_u32 v248, s48, v246\n\t"
            "s_add_u32 s59, s59, s84\n\t"
            "s_movk_i32 s84, 800\n\t"
            "ds_read_b128 v[204:207], v237\n\t"
            "s_setprio 3\n\t"
            "s_waitcnt lgkmcnt(7)\n\t"
            "v_mfma_f32_16x16x32_bf16 v[152:155], v[0:3], v[184:187], v[152:155]\n\t"
            "s_setprio 1\n\t"
            "s_setprio 1\n\t"
            "ds_read_b128 v[208:211], v248\n\t"
            "v_mfma_f32_16x16x32_bf16 v[152:155], v[4:7], v[188:191], v[152:155]\n\t"
            "v_mfma_f32_16x16x32_bf16 v[156:159], v[28:31], v[188:191], v[156:159]\n\t"
            "v_mfma_f32_16x16x32_bf16 v[156:159], v[24:27], v[184:187], v[156:159]\n\t"
            "v_mfma_f32_16x16x32_bf16 v[160:163], v[48:51], v[184:187], v[160:163]\n\t"
            "v_mfma_f32_16x16x32_bf16 v[160:163], v[52:55], v[188:191], v[160:163]\n\t"
            "v_mfma_f32_16x16x32_bf16 v[164:167], v[76:79], v[188:191], v[164:167]\n\t"
            "s_waitcnt lgkmcnt(6)\n\t"
            "s_mov_b64 exec, s[36:37]\n\t"
            "v_add_u32 v196, s57, v196\n\t"
            "s_mov_b64 exec, -1\n\t"
            "v_mfma_f32_16x16x32_bf16 v[164:167], v[72:75], v[184:187], v[164:167]\n\t"
            "v_mfma_f32_16x16x32_bf16 v[176:179], v[96:99], v[184:187], v[176:179]\n\t"
            "v_mfma_f32_16x16x32_bf16 v[176:179], v[100:103], v[188:191], v[176:179]\n\t"
            "v_mfma_f32_16x16x32_bf16 v[180:183], v[124:127], v[188:191], v[180:183]\n\t"
            "ds_read_b128 v[188:191], v243 offset:256\n\t"
            "v_mfma_f32_16x16x32_bf16 v[180:183], v[120:123], v[184:187], v[180:183]\n\t"
            "ds_read_b128 v[184:187], v242 offset:256\n\t"
            "s_waitcnt lgkmcnt(5)\n\t"
            "v_mfma_f32_16x16x32_bf16 v[152:155], v[8:11], v[192:195], v[152:155]\n\t"
            "v_mfma_f32_16x16x32_bf16 v[152:155], v[12:15], v[172:175], v[152:155]\n\t"
            "v_mfma_f32_16x16x32_bf16 v[156:159], v[36:39], v[172:175], v[156:159]\n\t"
            "v_mfma_f32_16x16x32_bf16 v[156:159], v[32:35], v[192:195], v[156:159]\n\t"
            "v_mfma_f32_16x16x32_bf16 v[160:163], v[56:59], v[192:195], v[160:163]\n\t"
            "v_mfma_f32_16x16x32_bf16 v[160:163], v[60:63], v[172:175], v[160:163]\n\t"
            "v_mfma_f32_16x16x32_bf16 v[164:167], v[84:87], v[172:175], v[164:167]\n\t"
            "v_mfma_f32_16x16x32_bf16 v[164:167], v[80:83], v[192:195], v[164:167]\n\t"
            "v_mfma_f32_16x16x32_bf16 v[176:179], v[104:107], v[192:195], v[176:179]\n\t"
            "v_mfma_f32_16x16x32_bf16 v[176:179], v[108:111], v[172:175], v[176:179]\n\t"
            "v_mfma_f32_16x16x32_bf16 v[180:183], v[132:135], v[172:175], v[180:183]\n\t"
            "ds_read_b128 v[172:175], v243 offset:6144\n\t"
            "v_mfma_f32_16x16x32_bf16 v[180:183], v[128:131], v[192:195], v[180:183]\n\t"
            "ds_read_b128 v[192:195], v242 offset:6144\n\t"
            "s_waitcnt lgkmcnt(2)\n\t"
            "v_mfma_f32_16x16x32_bf16 v[152:155], v[16:19], v[184:187], v[152:155]\n\t"
            "v_mfma_f32_16x16x32_bf16 v[152:155], v[20:23], v[188:191], v[152:155]\n\t"
            "v_mfma_f32_16x16x32_bf16 v[156:159], v[44:47], v[188:191], v[156:159]\n\t"
            "v_mfma_f32_16x16x32_bf16 v[156:159], v[40:43], v[184:187], v[156:159]\n\t"
            "v_mfma_f32_16x16x32_bf16 v[160:163], v[64:67], v[184:187], v[160:163]\n\t"
            "v_mfma_f32_16x16x32_bf16 v[160:163], v[68:71], v[188:191], v[160:163]\n\t"
            "v_mfma_f32_16x16x32_bf16 v[164:167], v[92:95], v[188:191], v[164:167]\n\t"
            "v_mfma_f32_16x16x32_bf16 v[164:167], v[88:91], v[184:187], v[164:167]\n\t"
            "s_nop 1\n\t"
            "v_cvt_pk_bf16_f32 v152, v152, v153\n\t"
            "v_mfma_f32_16x16x32_bf16 v[176:179], v[112:115], v[184:187], v[176:179]\n\t"
            "v_cvt_pk_bf16_f32 v153, v154, v155\n\t"
            "v_mfma_f32_16x16x32_bf16 v[176:179], v[116:119], v[188:191], v[176:179]\n\t"
            "v_cvt_pk_bf16_f32 v154, v156, v157\n\t"
            "v_cvt_pk_bf16_f32 v155, v158, v159\n\t"
            "v_mfma_f32_16x16x32_bf16 v[180:183], v[140:143], v[188:191], v[180:183]\n\t"
            "ds_read_b128 v[188:191], v243 offset:6272\n\t"
            "v_cvt_pk_bf16_f32 v160, v160, v161\n\t"
            "v_mfma_f32_16x16x32_bf16 v[180:183], v[136:139], v[184:187], v[180:183]\n\t"
            "ds_read_b128 v[184:187], v242 offset:6272\n\t"
            "v_cvt_pk_bf16_f32 v161, v162, v163\n\t"
            "s_waitcnt lgkmcnt(2)\n\t"
            "v_mfma_f32_16x16x32_bf16 v[144:147], v[0:3], v[192:195], v[144:147]\n\t"
            "v_cvt_pk_bf16_f32 v176, v176, v177\n\t"
            "v_cvt_pk_bf16_f32 v177, v178, v179\n\t"
            "v_mfma_f32_16x16x32_bf16 v[144:147], v[4:7], v[172:175], v[144:147]\n\t"
            "v_cvt_pk_bf16_f32 v162, v164, v165\n\t"
            "v_cvt_pk_bf16_f32 v163, v166, v167\n\t"
            "v_mfma_f32_16x16x32_bf16 v[148:151], v[28:31], v[172:175], v[148:151]\n\t"
            "v_cvt_pk_bf16_f32 v178, v180, v181\n\t"
            "v_cvt_pk_bf16_f32 v179, v182, v183\n\t"
            "v_mfma_f32_16x16x32_bf16 v[148:151], v[24:27], v[192:195], v[148:151]\n\t"
            "v_lshlrev_b32 v156, 16, v218\n\t"
            "v_and_b32 v157, 0xffff0000, v218\n\t"
            "v_mfma_f32_16x16x32_bf16 v[168:171], v[48:51], v[192:195], v[168:171]\n\t"
            "v_lshlrev_b32 v158, 16, v219\n\t"
            "v_and_b32 v159, 0xffff0000, v219\n\t"
            "v_mfma_f32_16x16x32_bf16 v[168:171], v[52:55], v[172:175], v[168:171]\n\t"
            "ds_read_b128 v[216:219], v237 offset:6144\n\t"
            "v_lshlrev_b32 v164, 16, v220\n\t"
            "v_mfma_f32_16x16x32_bf16 v[156:159], v[76:79], v[172:175], v[156:159]\n\t"
            "v_and_b32 v165, 0xffff0000, v220\n\t"
            "v_lshlrev_b32 v166, 16, v221\n\t"
            "v_mfma_f32_16x16x32_bf16 v[156:159], v[72:75], v[192:195], v[156:159]\n\t"
            "v_and_b32 v167, 0xffff0000, v221\n\t"
            "v_lshlrev_b32 v180, 16, v222\n\t"
            "v_and_b32 v181, 0xffff0000, v222\n\t"
            "v_mfma_f32_16x16x32_bf16 v[164:167], v[96:99], v[192:195], v[164:167]\n\t"
            "v_lshlrev_b32 v182, 16, v223\n\t"
            "v_and_b32 v183, 0xffff0000, v223\n\t"
            "v_mfma_f32_16x16x32_bf16 v[164:167], v[100:103], v[172:175], v[164:167]\n\t"
            "ds_read_b128 v[220:223], v248 offset:6144\n\t"
            "buffer_store_dwordx4 v[152:155], v196, s[40:43], s45 offen sc1\n\t"
            "v_mfma_f32_16x16x32_bf16 v[180:183], v[124:127], v[172:175], v[180:183]\n\t"
            "ds_read_b128 v[172:175], v243 offset:6400\n\t"
            "v_lshlrev_b32 v152, 16, v224\n\t"
            "v_mfma_f32_16x16x32_bf16 v[180:183], v[120:123], v[192:195], v[180:183]\n\t"
            "ds_read_b128 v[192:195], v242 offset:6400\n\t"
            "buffer_store_dwordx4 v[160:163], v196, s[40:43], s45 offen offset:64 sc1\n\t"
            "s_waitcnt lgkmcnt(4)\n\t"
            "v_mfma_f32_16x16x32_bf16 v[144:147], v[8:11], v[184:187], v[144:147]\n\t"
            "v_and_b32 v153, 0xffff0000, v224\n\t"
            "v_lshlrev_b32 v154, 16, v225\n\t"
            "v_mfma_f32_16x16x32_bf16 v[144:147], v[12:15], v[188:191], v[144:147]\n\t"
            "buffer_store_dwordx4 v[176:179], v196, s[40:43], s45 offen offset:128 sc1\n\t"
            "v_mfma_f32_16x16x32_bf16 v[148:151], v[36:39], v[188:191], v[148:151]\n\t"
            "v_and_b32 v155, 0xffff0000, v225\n\t"
            "v_lshlrev_b32 v160, 16, v226\n\t"
            "v_mfma_f32_16x16x32_bf16 v[148:151], v[32:35], v[184:187], v[148:151]\n\t"
            "v_and_b32 v161, 0xffff0000, v226\n\t"
            "v_lshlrev_b32 v162, 16, v227\n\t"
            "v_mfma_f32_16x16x32_bf16 v[168:171], v[56:59], v[184:187], v[168:171]\n\t"
            "v_and_b32 v163, 0xffff0000, v227\n\t"
            "v_lshlrev_b32 v176, 16, v228\n\t"
            "v_mfma_f32_16x16x32_bf16 v[168:171], v[60:63], v[188:191], v[168:171]\n\t"
            "ds_read_b128 v[224:227], v236 offset:12288\n\t"
            "v_and_b32 v177, 0xffff0000, v228\n\t"
            "v_mfma_f32_16x16x32_bf16 v[156:159], v[84:87], v[188:191], v[156:159]\n\t"
            "v_lshlrev_b32 v178, 16, v229\n\t"
            "v_and_b32 v179, 0xffff0000, v229\n\t"
            "v_mfma_f32_16x16x32_bf16 v[156:159], v[80:83], v[184:187], v[156:159]\n\t"
            "v_mfma_f32_16x16x32_bf16 v[164:167], v[104:107], v[184:187], v[164:167]\n\t"
            "v_mfma_f32_16x16x32_bf16 v[164:167], v[108:111], v[188:191], v[164:167]\n\t"
            "v_mfma_f32_16x16x32_bf16 v[180:183], v[132:135], v[188:191], v[180:183]\n\t"
            "ds_read_b128 v[188:191], v243 offset:12288\n\t"
            "v_mfma_f32_16x16x32_bf16 v[180:183], v[128:131], v[184:187], v[180:183]\n\t"
            "ds_read_b128 v[184:187], v242 offset:12288\n\t"
            "s_waitcnt lgkmcnt(3)\n\t"
            "v_mfma_f32_16x16x32_bf16 v[144:147], v[16:19], v[192:195], v[144:147]\n\t"
            "v_mfma_f32_16x16x32_bf16 v[144:147], v[20:23], v[172:175], v[144:147]\n\t"
            "v_mfma_f32_16x16x32_bf16 v[168:171], v[64:67], v[192:195], v[168:171]\n\t"
            "v_mfma_f32_16x16x32_bf16 v[168:171], v[68:71], v[172:175], v[168:171]\n\t"
            "v_mfma_f32_16x16x32_bf16 v[164:167], v[112:115], v[192:195], v[164:167]\n\t"
            "v_mfma_f32_16x16x32_bf16 v[164:167], v[116:119], v[172:175], v[164:167]\n\t"
            "v_mfma_f32_16x16x32_bf16 v[148:151], v[44:47], v[172:175], v[148:151]\n\t"
            "v_mfma_f32_16x16x32_bf16 v[148:151], v[40:43], v[192:195], v[148:151]\n\t"
            "s_nop 1\n\t"
            "v_cvt_pk_bf16_f32 v144, v144, v145\n\t"
            "v_mfma_f32_16x16x32_bf16 v[156:159], v[92:95], v[172:175], v[156:159]\n\t"
            "v_cvt_pk_bf16_f32 v145, v146, v147\n\t"
            "v_mfma_f32_16x16x32_bf16 v[156:159], v[88:91], v[192:195], v[156:159]\n\t"
            "v_cvt_pk_bf16_f32 v168, v168, v169\n\t"
            "v_cvt_pk_bf16_f32 v169, v170, v171\n\t"
            "v_mfma_f32_16x16x32_bf16 v[180:183], v[140:143], v[172:175], v[180:183]\n\t"
            "ds_read_b128 v[172:175], v243 offset:12416\n\t"
            "v_cvt_pk_bf16_f32 v164, v164, v165\n\t"
            "v_mfma_f32_16x16x32_bf16 v[180:183], v[136:139], v[192:195], v[180:183]\n\t"
            "ds_read_b128 v[192:195], v242 offset:12416\n\t"
            "v_cvt_pk_bf16_f32 v165, v166, v167\n\t"
            "s_waitcnt lgkmcnt(2)\n\t"
            "v_mfma_f32_16x16x32_bf16 v[152:155], v[0:3], v[184:187], v[152:155]\n\t"
            "v_cvt_pk_bf16_f32 v146, v148, v149\n\t"
            "v_cvt_pk_bf16_f32 v147, v150, v151\n\t"
            "v_mfma_f32_16x16x32_bf16 v[152:155], v[4:7], v[188:191], v[152:155]\n\t"
            "v_cvt_pk_bf16_f32 v170, v156, v157\n\t"
            "v_cvt_pk_bf16_f32 v171, v158, v159\n\t"
            "v_mfma_f32_16x16x32_bf16 v[160:163], v[28:31], v[188:191], v[160:163]\n\t"
            "v_cvt_pk_bf16_f32 v166, v180, v181\n\t"
            "v_cvt_pk_bf16_f32 v167, v182, v183\n\t"
            "v_mfma_f32_16x16x32_bf16 v[160:163], v[24:27], v[184:187], v[160:163]\n\t"
            "v_lshlrev_b32 v148, 16, v230\n\t"
            "v_and_b32 v149, 0xffff0000, v230\n\t"
            "v_mfma_f32_16x16x32_bf16 v[176:179], v[48:51], v[184:187], v[176:179]\n\t"
            "v_lshlrev_b32 v150, 16, v231\n\t"
            "v_and_b32 v151, 0xffff0000, v231\n\t"
            "v_mfma_f32_16x16x32_bf16 v[176:179], v[52:55], v[188:191], v[176:179]\n\t"
            "ds_read_b128 v[228:231], v237 offset:12288\n\t"
            "v_lshlrev_b32 v156, 16, v232\n\t"
            "v_mfma_f32_16x16x32_bf16 v[148:151], v[76:79], v[188:191], v[148:151]\n\t"
            "v_and_b32 v157, 0xffff0000, v232\n\t"
            "v_lshlrev_b32 v158, 16, v233\n\t"
            "v_mfma_f32_16x16x32_bf16 v[148:151], v[72:75], v[184:187], v[148:151]\n\t"
            "v_and_b32 v159, 0xffff0000, v233\n\t"
            "v_lshlrev_b32 v180, 16, v234\n\t"
            "v_and_b32 v181, 0xffff0000, v234\n\t"
            "v_mfma_f32_16x16x32_bf16 v[156:159], v[96:99], v[184:187], v[156:159]\n\t"
            "v_lshlrev_b32 v182, 16, v235\n\t"
            "v_and_b32 v183, 0xffff0000, v235\n\t"
            "v_mfma_f32_16x16x32_bf16 v[156:159], v[100:103], v[188:191], v[156:159]\n\t"
            "ds_read_b128 v[232:235], v248 offset:12288\n\t"
            "buffer_store_dwordx4 v[144:147], v197, s[40:43], s45 offen sc1\n\t"
            "v_mfma_f32_16x16x32_bf16 v[180:183], v[124:127], v[188:191], v[180:183]\n\t"
            "ds_read_b128 v[188:191], v243 offset:12544\n\t"
            "v_mfma_f32_16x16x32_bf16 v[180:183], v[120:123], v[184:187], v[180:183]\n\t"
            "ds_read_b128 v[184:187], v242 offset:12544\n\t"
            "buffer_store_dwordx4 v[168:171], v197, s[40:43], s45 offen offset:64 sc1\n\t"
            "s_waitcnt lgkmcnt(4)\n\t"
            "v_mfma_f32_16x16x32_bf16 v[152:155], v[8:11], v[192:195], v[152:155]\n\t"
            "v_mfma_f32_16x16x32_bf16 v[152:155], v[12:15], v[172:175], v[152:155]\n\t"
            "buffer_store_dwordx4 v[164:167], v197, s[40:43], s45 offen offset:128 sc1\n\t"
            "v_mfma_f32_16x16x32_bf16 v[160:163], v[36:39], v[172:175], v[160:163]\n\t"
            "v_mfma_f32_16x16x32_bf16 v[160:163], v[32:35], v[192:195], v[160:163]\n\t"
            "s_waitcnt lgkmcnt(0)\n\t"
            "s_barrier\n\t"
            "s_setprio 3\n\t"
            "v_mfma_f32_16x16x32_bf16 v[176:179], v[56:59], v[192:195], v[176:179]\n\t"
            "v_mfma_f32_16x16x32_bf16 v[176:179], v[60:63], v[172:175], v[176:179]\n\t"
            "v_mfma_f32_16x16x32_bf16 v[148:151], v[84:87], v[172:175], v[148:151]\n\t"
            "v_mfma_f32_16x16x32_bf16 v[148:151], v[80:83], v[192:195], v[148:151]\n\t"
            "v_mfma_f32_16x16x32_bf16 v[156:159], v[104:107], v[192:195], v[156:159]\n\t"
            "v_mfma_f32_16x16x32_bf16 v[156:159], v[108:111], v[172:175], v[156:159]\n\t"
            "v_mfma_f32_16x16x32_bf16 v[152:155], v[16:19], v[184:187], v[152:155]\n\t"
            "v_mfma_f32_16x16x32_bf16 v[152:155], v[20:23], v[188:191], v[152:155]\n\t"
            "v_mfma_f32_16x16x32_bf16 v[160:163], v[44:47], v[188:191], v[160:163]\n\t"
            "v_mfma_f32_16x16x32_bf16 v[160:163], v[40:43], v[184:187], v[160:163]\n\t"
            "v_mfma_f32_16x16x32_bf16 v[180:183], v[132:135], v[172:175], v[180:183]\n\t"
            "v_mfma_f32_16x16x32_bf16 v[180:183], v[128:131], v[192:195], v[180:183]\n\t"
            "v_mfma_f32_16x16x32_bf16 v[176:179], v[64:67], v[184:187], v[176:179]\n\t"
            "v_mfma_f32_16x16x32_bf16 v[176:179], v[68:71], v[188:191], v[176:179]\n\t"
            "s_nop 1\n\t"
            "v_cvt_pk_bf16_f32 v152, v152, v153\n\t"
            "v_mfma_f32_16x16x32_bf16 v[148:151], v[92:95], v[188:191], v[148:151]\n\t"
            "v_cvt_pk_bf16_f32 v153, v154, v155\n\t"
            "v_mfma_f32_16x16x32_bf16 v[148:151], v[88:91], v[184:187], v[148:151]\n\t"
            "v_cvt_pk_bf16_f32 v154, v160, v161\n\t"
            "v_cvt_pk_bf16_f32 v155, v162, v163\n\t"
            "v_mfma_f32_16x16x32_bf16 v[156:159], v[112:115], v[184:187], v[156:159]\n\t"
            "v_cvt_pk_bf16_f32 v176, v176, v177\n\t"
            "v_cvt_pk_bf16_f32 v177, v178, v179\n\t"
            "v_mfma_f32_16x16x32_bf16 v[156:159], v[116:119], v[188:191], v[156:159]\n\t"
            "buffer_store_dwordx4 v[152:155], v198, s[40:43], s45 offen sc1\n\t"
            "v_mfma_f32_16x16x32_bf16 v[180:183], v[140:143], v[188:191], v[180:183]\n\t"
            "v_cvt_pk_bf16_f32 v178, v148, v149\n\t"
            "v_cvt_pk_bf16_f32 v179, v150, v151\n\t"
            "v_mfma_f32_16x16x32_bf16 v[180:183], v[136:139], v[184:187], v[180:183]\n\t"
            "buffer_store_dwordx4 v[176:179], v198, s[40:43], s45 offen offset:64 sc1\n\t"
            "s_nop 1\n\t"
            "v_cvt_pk_bf16_f32 v156, v156, v157\n\t"
            "v_cvt_pk_bf16_f32 v157, v158, v159\n\t"
            "s_nop 2\n\t"
            "v_cvt_pk_bf16_f32 v158, v180, v181\n\t"
            "v_cvt_pk_bf16_f32 v159, v182, v183\n\t"
            "buffer_store_dwordx4 v[156:159], v198, s[40:43], s45 offen offset:128 sc1\n\t"
            "s_add_u32 s45, s46, s27\n\t"
            "s_mov_b32 s46, s47\n\t"
            "s_mov_b32 s47, s68\n\t"
            "s_xor_b32 s48, s48, s71\n\t"
            "s_xor_b32 s67, s67, s71\n\t"
            "s_add_u32 s76, s76, 4\n\t"
            "s_add_u32 s70, s70, 4\n\t"
            "s_lshl_b32 s56, s56, 2\n\t"
            "s_lshr_b32 s49, s26, 30\n\t"
            "s_or_b32 s56, s56, s49\n\t"
            "s_cmp_lt_u32 s46, 0x7000000\n\t"
            "s_cbranch_scc1 .Lwb_loop%=\n\t"
            "s_branch .Lwb_end%=\n\t"
            ".Lwb_spin%=:\n\t"
            "s_cmp_eq_u32 s83, 0\n\t"
            "s_cbranch_scc1 .Lwb_go%=\n\t"
            "s_sub_u32 s83, s83, 1\n\t"
            "s_sleep 2\n\t"
            "v_mov_b32 v247, 0\n\t"
            "ds_read_b32 v247, v247 offset:7856\n\t"
            "s_waitcnt lgkmcnt(0)\n\t"
            "v_readfirstlane_b32 s77, v247\n\t"
            "s_cmp_ge_u32 s77, s70\n\t"
            "s_cbranch_scc0 .Lwb_spin%=\n\t"
            "s_branch .Lwb_go%=\n\t"
            ".Lwb_end%=:\n\t"
            "s_waitcnt lgkmcnt(0)\n\t"
            "s_nop 7\n\t"
            "ds_read_b128 v[196:199], v252\n\t"
            "s_bfe_u32 s57, s56, 0x20004\n\t"
            "s_lshl_b32 s58, s57, 3\n\t"
            "s_lshr_b32 s58, s55, s58\n\t"
            "s_lshl_b64 s[36:37], s[34:35], s58\n\t"
            "s_bitcmp1_b32 s57, 1\n\t"
            "s_cselect_b32 s58, 1177344, 0\n\t"
            "s_bitcmp1_b32 s57, 0\n\t"
            "s_cselect_b32 s57, 2304, 0\n\t"
            "s_add_u32 s57, s57, s58\n\t"
            "v_lshlrev_b32 v152, 16, v200\n\t"
            "v_and_b32 v153, 0xffff0000, v200\n\t"
            "v_lshlrev_b32 v154, 16, v201\n\t"
            "v_and_b32 v155, 0xffff0000, v201\n\t"
            "v_lshlrev_b32 v156, 16, v202\n\t"
            "v_and_b32 v157, 0xffff0000, v202\n\t"
            "v_lshlrev_b32 v158, 16, v203\n\t"
            "v_and_b32 v159, 0xffff0000, v203\n\t"
            "v_lshlrev_b32 v160, 16, v204\n\t"
            "v_and_b32 v161, 0xffff0000, v204\n\t"
            "v_lshlrev_b32 v162, 16, v205\n\t"
            "v_and_b32 v163, 0xffff0000, v205\n\t"
            "v_lshlrev_b32 v164, 16, v206\n\t"
            "v_and_b32 v165, 0xffff0000, v206\n\t"
            "v_lshlrev_b32 v166, 16, v207\n\t"
            "v_and_b32 v167, 0xffff0000, v207\n\t"
            "v_lshlrev_b32 v176, 16, v208\n\t"
            "v_and_b32 v177, 0xffff0000, v208\n\t"
            "v_lshlrev_b32 v178, 16, v209\n\t"
            "v_and_b32 v179, 0xffff0000, v209\n\t"
            "v_lshlrev_b32 v180, 16, v210\n\t"
            "v_and_b32 v181, 0xffff0000, v210\n\t"
            "v_lshlrev_b32 v182, 16, v211\n\t"
            "v_and_b32 v183, 0xffff0000, v211\n\t"
            "v_lshlrev_b32 v144, 16, v212\n\t"
            "v_and_b32 v145, 0xffff0000, v212\n\t"
            "v_lshlrev_b32 v146, 16, v213\n\t"
            "v_and_b32 v147, 0xffff0000, v213\n\t"
            "v_lshlrev_b32 v148, 16, v214\n\t"
            "v_and_b32 v149, 0xffff0000, v214\n\t"
            "v_lshlrev_b32 v150, 16, v215\n\t"
            "v_and_b32 v151, 0xffff0000, v215\n\t"
            "v_lshlrev_b32 v168, 16, v216\n\t"
            "v_and_b32 v169, 0xffff0000, v216\n\t"
            "v_lshlrev_b32 v170, 16, v217\n\t"
            "s_waitcnt lgkmcnt(0)\n\t"
            "s_mov_b64 exec, s[36:37]\n\t"
            "v_add_u32 v196, s57, v196\n\t"
            "s_mov_b64 exec, -1\n\t"
            ".Lwk_t1%=:\n\t"
            "v_mov_b32 v247, 0\n\t"
            "ds_read_b32 v247, v247 offset:7856\n\t"
            "s_waitcnt lgkmcnt(0)\n\t"
            "v_readfirstlane_b32 s77, v247\n\t"
            "s_cmp_ge_u32 s77, s70\n\t"
            "s_cbranch_scc1 .Lwk_t2%=\n\t"
            "s_cmp_eq_u32 s83, 0\n\t"
            "s_cbranch_scc1 .Lwk_t2%=\n\t"
            "s_sub_u32 s83, s83, 1\n\t"
            "s_sleep 1\n\t"
            "s_branch .Lwk_t1%=\n\t"
            ".Lwk_t2%=:\n\t"
            "v_and_b32 v171, 0xffff0000, v217\n\t"
            "ds_read_b128 v[184:187], v242\n\t"
            "ds_read_b128 v[188:191], v243\n\t"
            "ds_read_b128 v[192:195], v242 offset:128\n\t"
            "ds_read_b128 v[172:175], v243 offset:128\n\t"
            "s_waitcnt lgkmcnt(2)\n\t"
            "v_mfma_f32_16x16x32_bf16 v[152:155], v[0:3], v[184:187], v[152:155]\n\t"
            "v_mfma_f32_16x16x32_bf16 v[152:155], v[4:7], v[188:191], v[152:155]\n\t"
            "v_mfma_f32_16x16x32_bf16 v[156:159], v[28:31], v[188:191], v[156:159]\n\t"
            "v_mfma_f32_16x16x32_bf16 v[156:159], v[24:27], v[184:187], v[156:159]\n\t"
            "v_mfma_f32_16x16x32_bf16 v[160:163], v[48:51], v[184:187], v[160:163]\n\t"
            "v_mfma_f32_16x16x32_bf16 v[160:163], v[52:55], v[188:191], v[160:163]\n\t"
            "v_mfma_f32_16x16x32_bf16 v[164:167], v[76:79], v[188:191], v[164:167]\n\t"
            "v_mfma_f32_16x16x32_bf16 v[164:167], v[72:75], v[184:187], v[164:167]\n\t"
            "v_mfma_f32_16x16x32_bf16 v[176:179], v[96:99], v[184:187], v[176:179]\n\t"
            "v_mfma_f32_16x16x32_bf16 v[176:179], v[100:103], v[188:191], v[176:179]\n\t"
            "v_mfma_f32_16x16x32_bf16 v[180:183], v[124:127], v[188:191], v[180:183]\n\t"
            "ds_read_b128 v[188:191], v243 offset:256\n\t"
            "v_mfma_f32_16x16x32_bf16 v[180:183], v[120:123], v[184:187], v[180:183]\n\t"
            "ds_read_b128 v[184:187], v242 offset:256\n\t"
            "s_waitcnt lgkmcnt(2)\n\t"
            "v_mfma_f32_16x16x32_bf16 v[152:155], v[8:11], v[192:195], v[152:155]\n\t"
            "v_mfma_f32_16x16x32_bf16 v[152:155], v[12:15], v[172:175], v[152:155]\n\t"
            "v_mfma_f32_16x16x32_bf16 v[156:159], v[36:39], v[172:175], v[156:159]\n\t"
            "v_mfma_f32_16x16x32_bf16 v[156:159], v[32:35], v[192:195], v[156:159]\n\t"
            "v_mfma_f32_16x16x32_bf16 v[160:163], v[56:59], v[192:195], v[160:163]\n\t"
            "v_mfma_f32_16x16x32_bf16 v[160:163], v[60:63], v[172:175], v[160:163]\n\t"
            "v_mfma_f32_16x16x32_bf16 v[164:167], v[84:87], v[172:175], v[164:167]\n\t"
            "v_mfma_f32_16x16x32_bf16 v[164:167], v[80:83], v[192:195], v[164:167]\n\t"
            "v_mfma_f32_16x16x32_bf16 v[176:179], v[104:107], v[192:195], v[176:179]\n\t"
            "v_mfma_f32_16x16x32_bf16 v[176:179], v[108:111], v[172:175], v[176:179]\n\t"
            "v_mfma_f32_16x16x32_bf16 v[180:183], v[132:135], v[172:175], v[180:183]\n\t"
            "ds_read_b128 v[172:175], v243 offset:6144\n\t"
            "v_mfma_f32_16x16x32_bf16 v[180:183], v[128:131], v[192:195], v[180:183]\n\t"
            "ds_read_b128 v[192:195], v242 offset:6144\n\t"
            "s_waitcnt lgkmcnt(2)\n\t"
            "v_mfma_f32_16x16x32_bf16 v[152:155], v[16:19], v[184:187], v[152:155]\n\t"
            "v_mfma_f32_16x16x32_bf16 v[152:155], v[20:23], v[188:191], v[152:155]\n\t"
            "v_mfma_f32_16x16x32_bf16 v[156:159], v[44:47], v[188:191], v[156:159]\n\t"
            "v_mfma_f32_16x16x32_bf16 v[156:159], v[40:43], v[184:187], v[156:159]\n\t"
            "v_mfma_f32_16x16x32_bf16 v[160:163], v[64:67], v[184:187], v[160:163]\n\t"
            "v_mfma_f32_16x16x32_bf16 v[160:163], v[68:71], v[188:191], v[160:163]\n\t"
            "v_mfma_f32_16x16x32_bf16 v[164:167], v[92:95], v[188:191], v[164:167]\n\t"
            "v_mfma_f32_16x16x32_bf16 v[164:167], v[88:91], v[184:187], v[164:167]\n\t"
            "s_nop 1\n\t"
            "v_cvt_pk_bf16_f32 v152, v152, v153\n\t"
            "v_mfma_f32_16x16x32_bf16 v[176:179], v[112:115], v[184:187], v[176:179]\n\t"
            "v_cvt_pk_bf16_f32 v153, v154, v155\n\t"
            "v_mfma_f32_16x16x32_bf16 v[176:179], v[116:119], v[188:191], v[176:179]\n\t"
            "v_cvt_pk_bf16_f32 v154, v156, v157\n\t"
            "v_cvt_pk_bf16_f32 v155, v158, v159\n\t"
            "v_mfma_f32_16x16x32_bf16 v[180:183], v[140:143], v[188:191], v[180:183]\n\t"
            "ds_read_b128 v[188:191], v243 offset:6272\n\t"
            "v_cvt_pk_bf16_f32 v160, v160, v161\n\t"
            "v_mfma_f32_16x16x32_bf16 v[180:183], v[136:139], v[184:187], v[180:183]\n\t"
            "ds_read_b128 v[184:187], v242 offset:6272\n\t"
            "v_cvt_pk_bf16_f32 v161, v162, v163\n\t"
            "s_waitcnt lgkmcnt(2)\n\t"
            "v_mfma_f32_16x16x32_bf16 v[144:147], v[0:3], v[192:195], v[144:147]\n\t"
            "v_cvt_pk_bf16_f32 v176, v176, v177\n\t"
            "v_cvt_pk_bf16_f32 v177, v178, v179\n\t"
            "v_mfma_f32_16x16x32_bf16 v[144:147], v[4:7], v[172:175], v[144:147]\n\t"
            "v_cvt_pk_bf16_f32 v162, v164, v165\n\t"
            "v_cvt_pk_bf16_f32 v163, v166, v167\n\t"
            "v_mfma_f32_16x16x32_bf16 v[148:151], v[28:31], v[172:175], v[148:151]\n\t"
            "v_cvt_pk_bf16_f32 v178, v180, v181\n\t"
            "v_cvt_pk_bf16_f32 v179, v182, v183\n\t"
            "v_mfma_f32_16x16x32_bf16 v[148:151], v[24:27], v[192:195], v[148:151]\n\t"
            "v_lshlrev_b32 v156, 16, v218\n\t"
            "v_and_b32 v157, 0xffff0000, v218\n\t"
            "v_mfma_f32_16x16x32_bf16 v[168:171], v[48:51], v[192:195], v[168:171]\n\t"
            "v_lshlrev_b32 v158, 16, v219\n\t"
            "v_and_b32 v159, 0xffff0000, v219\n\t"
            "v_mfma_f32_16x16x32_bf16 v[168:171], v[52:55], v[172:175], v[168:171]\n\t"
            "v_lshlrev_b32 v164, 16, v220\n\t"
            "v_and_b32 v165, 0xffff0000, v220\n\t"
            "v_mfma_f32_16x16x32_bf16 v[156:159], v[76:79], v[172:175], v[156:159]\n\t"
            "v_lshlrev_b32 v166, 16, v221\n\t"
            "v_and_b32 v167, 0xffff0000, v221\n\t"
            "v_mfma_f32_16x16x32_bf16 v[156:159], v[72:75], v[192:195], v[156:159]\n\t"
            "v_lshlrev_b32 v180, 16, v222\n\t"
            "v_and_b32 v181, 0xffff0000, v222\n\t"
            "v_mfma_f32_16x16x32_bf16 v[164:167], v[96:99], v[192:195], v[164:167]\n\t"
            "v_lshlrev_b32 v182, 16, v223\n\t"
            "v_and_b32 v183, 0xffff0000, v223\n\t"
            "v_mfma_f32_16x16x32_bf16 v[164:167], v[100:103], v[172:175], v[164:167]\n\t"
            "buffer_store_dwordx4 v[152:155], v196, s[40:43], s45 offen sc1\n\t"
            "v_mfma_f32_16x16x32_bf16 v[180:183], v[124:127], v[172:175], v[180:183]\n\t"
            "ds_read_b128 v[172:175], v243 offset:6400\n\t"
            "v_lshlrev_b32 v152, 16, v224\n\t"
            "v_mfma_f32_16x16x32_bf16 v[180:183], v[120:123], v[192:195], v[180:183]\n\t"
            "ds_read_b128 v[192:195], v242 offset:6400\n\t"
            "buffer_store_dwordx4 v[160:163], v196, s[40:43], s45 offen offset:64 sc1\n\t"
            "s_waitcnt lgkmcnt(2)\n\t"
            "v_mfma_f32_16x16x32_bf16 v[144:147], v[8:11], v[184:187], v[144:147]\n\t"
            "v_and_b32 v153, 0xffff0000, v224\n\t"
            "v_lshlrev_b32 v154, 16, v225\n\t"
            "v_mfma_f32_16x16x32_bf16 v[144:147], v[12:15], v[188:191], v[144:147]\n\t"
            "buffer_store_dwordx4 v[176:179], v196, s[40:43], s45 offen offset:128 sc1\n\t"
            "v_mfma_f32_16x16x32_bf16 v[148:151], v[36:39], v[188:191], v[148:151]\n\t"
            "v_and_b32 v155, 0xffff0000, v225\n\t"
            "v_lshlrev_b32 v160, 16, v226\n\t"
            "v_mfma_f32_16x16x32_bf16 v[148:151], v[32:35], v[184:187], v[148:151]\n\t"
            "v_and_b32 v161, 0xffff0000, v226\n\t"
            "v_lshlrev_b32 v162, 16, v227\n\t"
            "v_mfma_f32_16x16x32_bf16 v[168:171], v[56:59], v[184:187], v[168:171]\n\t"
            "v_and_b32 v163, 0xffff0000, v227\n\t"
            "v_lshlrev_b32 v176, 16, v228\n\t"
            "v_mfma_f32_16x16x32_bf16 v[168:171], v[60:63], v[188:191], v[168:171]\n\t"
            "v_and_b32 v177, 0xffff0000, v228\n\t"
            "v_lshlrev_b32 v178, 16, v229\n\t"
            "v_mfma_f32_16x16x32_bf16 v[156:159], v[84:87], v[188:191], v[156:159]\n\t"
            "v_and_b32 v179, 0xffff0000, v229\n\t"
            "v_mfma_f32_16x16x32_bf16 v[156:159], v[80:83], v[184:187], v[156:159]\n\t"
            "v_mfma_f32_16x16x32_bf16 v[164:167], v[104:107], v[184:187], v[164:167]\n\t"
            "v_mfma_f32_16x16x32_bf16 v[164:167], v[108:111], v[188:191], v[164:167]\n\t"
            "v_mfma_f32_16x16x32_bf16 v[180:183], v[132:135], v[188:191], v[180:183]\n\t"
            "ds_read_b128 v[188:191], v243 offset:12288\n\t"
            "v_mfma_f32_16x16x32_bf16 v[180:183], v[128:131], v[184:187], v[180:183]\n\t"
            "ds_read_b128 v[184:187], v242 offset:12288\n\t"
            "s_waitcnt lgkmcnt(2)\n\t"
            "v_mfma_f32_16x16x32_bf16 v[144:147], v[16:19], v[192:195], v[144:147]\n\t"
            "v_mfma_f32_16x16x32_bf16 v[144:147], v[20:23], v[172:175], v[144:147]\n\t"
            "v_mfma_f32_16x16x32_bf16 v[168:171], v[64:67], v[192:195], v[168:171]\n\t"
            "v_mfma_f32_16x16x32_bf16 v[168:171], v[68:71], v[172:175], v[168:171]\n\t"
            "v_mfma_f32_16x16x32_bf16 v[164:167], v[112:115], v[192:195], v[164:167]\n\t"
            "v_mfma_f32_16x16x32_bf16 v[164:167], v[116:119], v[172:175], v[164:167]\n\t"
            "v_mfma_f32_16x16x32_bf16 v[148:151], v[44:47], v[172:175], v[148:151]\n\t"
            "v_mfma_f32_16x16x32_bf16 v[148:151], v[40:43], v[192:195], v[148:151]\n\t"
            "s_nop 1\n\t"
            "v_cvt_pk_bf16_f32 v144, v144, v145\n\t"
            "v_mfma_f32_16x16x32_bf16 v[156:159], v[92:95], v[172:175], v[156:159]\n\t"
            "v_cvt_pk_bf16_f32 v145, v146, v147\n\t"
            "v_mfma_f32_16x16x32_bf16 v[156:159], v[88:91], v[192:195], v[156:159]\n\t"
            "v_cvt_pk_bf16_f32 v168, v168, v169\n\t"
            "v_cvt_pk_bf16_f32 v169, v170, v171\n\t"
            "v_mfma_f32_16x16x32_bf16 v[180:183], v[140:143], v[172:175], v[180:183]\n\t"
            "ds_read_b128 v[172:175], v243 offset:12416\n\t"
            "v_cvt_pk_bf16_f32 v164, v164, v165\n\t"
            "v_mfma_f32_16x16x32_bf16 v[180:183], v[136:139], v[192:195], v[180:183]\n\t"
            "ds_read_b128 v[192:195], v242 offset:12416\n\t"
            "v_cvt_pk_bf16_f32 v165, v166, v167\n\t"
            "s_waitcnt lgkmcnt(2)\n\t"
            "v_mfma_f32_16x16x32_bf16 v[152:155], v[0:3], v[184:187], v[152:155]\n\t"
            "v_cvt_pk_bf16_f32 v146, v148, v149\n\t"
            "v_cvt_pk_bf16_f32 v147, v150, v151\n\t"
            "v_mfma_f32_16x16x32_bf16 v[152:155], v[4:7], v[188:191], v[152:155]\n\t"
            "v_cvt_pk_bf16_f32 v170, v156, v157\n\t"
            "v_cvt_pk_bf16_f32 v171, v158, v159\n\t"
            "v_mfma_f32_16x16x32_bf16 v[160:163], v[28:31], v[188:191], v[160:163]\n\t"
            "v_cvt_pk_bf16_f32 v166, v180, v181\n\t"
            "v_cvt_pk_bf16_f32 v167, v182, v183\n\t"
            "v_mfma_f32_16x16x32_bf16 v[160:163], v[24:27], v[184:187], v[160:163]\n\t"
            "v_lshlrev_b32 v148, 16, v230\n\t"
            "v_and_b32 v149, 0xffff0000, v230\n\t"
            "v_mfma_f32_16x16x32_bf16 v[176:179], v[48:51], v[184:187], v[176:179]\n\t"
            "v_lshlrev_b32 v150, 16, v231\n\t"
            "v_and_b32 v151, 0xffff0000, v231\n\t"
            "v_mfma_f32_16x16x32_bf16 v[176:179], v[52:55], v[188:191], v[176:179]\n\t"
            "v_lshlrev_b32 v156, 16, v232\n\t"
            "v_and_b32 v157, 0xffff0000, v232\n\t"
            "v_mfma_f32_16x16x32_bf16 v[148:151], v[76:79], v[188:191], v[148:151]\n\t"
            "v_lshlrev_b32 v158, 16, v233\n\t"
            "v_and_b32 v159, 0xffff0000, v233\n\t"
            "v_mfma_f32_16x16x32_bf16 v[148:151], v[72:75], v[184:187], v[148:151]\n\t"
            "v_lshlrev_b32 v180, 16, v234\n\t"
            "v_and_b32 v181, 0xffff0000, v234\n\t"
            "v_mfma_f32_16x16x32_bf16 v[156:159], v[96:99], v[184:187], v[156:159]\n\t"
            "v_lshlrev_b32 v182, 16, v235\n\t"
            "v_and_b32 v183, 0xffff0000, v235\n\t"
            "v_mfma_f32_16x16x32_bf16 v[156:159], v[100:103], v[188:191], v[156:159]\n\t"
            "buffer_store_dwordx4 v[144:147], v197, s[40:43], s45 offen sc1\n\t"
            "v_mfma_f32_16x16x32_bf16 v[180:183], v[124:127], v[188:191], v[180:183]\n\t"
            "ds_read_b128 v[188:191], v243 offset:12544\n\t"
            "v_mfma_f32_16x16x32_bf16 v[180:183], v[120:123], v[184:187], v[180:183]\n\t"
            "ds_read_b128 v[184:187], v242 offset:12544\n\t"
            "buffer_store_dwordx4 v[168:171], v197, s[40:43], s45 offen offset:64 sc1\n\t"
            "s_waitcnt lgkmcnt(2)\n\t"
            "v_mfma_f32_16x16x32_bf16 v[152:155], v[8:11], v[192:195], v[152:155]\n\t"
            "v_mfma_f32_16x16x32_bf16 v[152:155], v[12:15], v[172:175], v[152:155]\n\t"
            "buffer_store_dwordx4 v[164:167], v197, s[40:43], s45 offen offset:128 sc1\n\t"
            "v_mfma_f32_16x16x32_bf16 v[160:163], v[36:39], v[172:175], v[160:163]\n\t"
            "v_mfma_f32_16x16x32_bf16 v[160:163], v[32:35], v[192:195], v[160:163]\n\t"
            "v_mfma_f32_16x16x32_bf16 v[176:179], v[56:59], v[192:195], v[176:179]\n\t"
            "v_mfma_f32_16x16x32_bf16 v[176:179], v[60:63], v[172:175], v[176:179]\n\t"
            "v_mfma_f32_16x16x32_bf16 v[148:151], v[84:87], v[172:175], v[148:151]\n\t"
            "v_mfma_f32_16x16x32_bf16 v[148:151], v[80:83], v[192:195], v[148:151]\n\t"
            "v_mfma_f32_16x16x32_bf16 v[156:159], v[104:107], v[192:195], v[156:159]\n\t"
            "v_mfma_f32_16x16x32_bf16 v[156:159], v[108:111], v[172:175], v[156:159]\n\t"
            "s_waitcnt lgkmcnt(0)\n\t"
            "v_mfma_f32_16x16x32_bf16 v[152:155], v[16:19], v[184:187], v[152:155]\n\t"
            "v_mfma_f32_16x16x32_bf16 v[152:155], v[20:23], v[188:191], v[152:155]\n\t"
            "v_mfma_f32_16x16x32_bf16 v[160:163], v[44:47], v[188:191], v[160:163]\n\t"
            "v_mfma_f32_16x16x32_bf16 v[160:163], v[40:43], v[184:187], v[160:163]\n\t"
            "v_mfma_f32_16x16x32_bf16 v[180:183], v[132:135], v[172:175], v[180:183]\n\t"
            "v_mfma_f32_16x16x32_bf16 v[180:183], v[128:131], v[192:195], v[180:183]\n\t"
            "v_mfma_f32_16x16x32_bf16 v[176:179], v[64:67], v[184:187], v[176:179]\n\t"
            "v_mfma_f32_16x16x32_bf16 v[176:179], v[68:71], v[188:191], v[176:179]\n\t"
            "s_nop 1\n\t"
            "v_cvt_pk_bf16_f32 v152, v152, v153\n\t"
            "v_mfma_f32_16x16x32_bf16 v[148:151], v[92:95], v[188:191], v[148:151]\n\t"
            "v_cvt_pk_bf16_f32 v153, v154, v155\n\t"
            "v_mfma_f32_16x16x32_bf16 v[148:151], v[88:91], v[184:187], v[148:151]\n\t"
            "v_cvt_pk_bf16_f32 v154, v160, v161\n\t"
            "v_cvt_pk_bf16_f32 v155, v162, v163\n\t"
            "v_mfma_f32_16x16x32_bf16 v[156:159], v[112:115], v[184:187], v[156:159]\n\t"
            "v_cvt_pk_bf16_f32 v176, v176, v177\n\t"
            "v_cvt_pk_bf16_f32 v177, v178, v179\n\t"
            "v_mfma_f32_16x16x32_bf16 v[156:159], v[116:119], v[188:191], v[156:159]\n\t"
            "buffer_store_dwordx4 v[152:155], v198, s[40:43], s45 offen sc1\n\t"
            "v_mfma_f32_16x16x32_bf16 v[180:183], v[140:143], v[188:191], v[180:183]\n\t"
            "v_cvt_pk_bf16_f32 v178, v148, v149\n\t"
            "v_cvt_pk_bf16_f32 v179, v150, v151\n\t"
            "v_mfma_f32_16x16x32_bf16 v[180:183], v[136:139], v[184:187], v[180:183]\n\t"
            "buffer_store_dwordx4 v[176:179], v198, s[40:43], s45 offen offset:64 sc1\n\t"
            "s_nop 1\n\t"
            "v_cvt_pk_bf16_f32 v156, v156, v157\n\t"
            "v_cvt_pk_bf16_f32 v157, v158, v159\n\t"
            "s_nop 2\n\t"
            "v_cvt_pk_bf16_f32 v158, v180, v181\n\t"
            "v_cvt_pk_bf16_f32 v159, v182, v183\n\t"
            "buffer_store_dwordx4 v[156:159], v198, s[40:43], s45 offen offset:128 sc1\n\t"
            "s_nop 7\n\t"
            : B_SIO, "+{v247}"(l16_), "+{v228}"(q0), "+{v229}"(q1), "+{v232}"(q2), "+{v233}"(q3), "+{v234}"(q4)
            : "{v242}"(oE_), "{v243}"(oO_), "{v244}"(pR0_), "{v245}"(pR1_), "{v246}"(pR2_), "{v250}"(pXw_), "{v251}"(pSTw_), "{v252}"(aCA_), "{v253}"(aB_), "{v254}"(aA_), "{v255}"(idx_), B_SIN, "{s44}"(ARR_OFF + blockIdx.x * 8), "{s[80:81]}"(mrow), "{s82}"(wave == 6 && cnt == 21 ? REC_OFF + (start - 20 * ((blockIdx.x & 7) * 32 + xi)) * 1280 : 0)
            : "v144", "v145", "v146", "v147", "v148", "v149", "v150", "v151", "v152", "v153", "v154", "v155", "v156", "v157", "v158", "v159", "v160", "v161", "v162", "v163", "v164", "v165", "v166", "v167", "v168", "v169", "v170", "v171", "v172", "v173", "v174", "v175", "v176", "v177", "v178", "v179", "v180", "v181", "v182", "v183", "v184", "v185", "v186", "v187", "v188", "v189", "v190", "v191", "v192", "v193", "v194", "v195", "v196", "v197", "v198", "v199", "v236", "v237", "v248", "v249", "v0", "v1", "v2", "v3", "v4", "v5", "v6", "v7", "v8", "v9", "v10", "v11", "v12", "v13", "v14", "v15", "v16", "v17", "v18", "v19", "v20", "v21", "v22", "v23", "v24", "v25", "v26", "v27", "v28", "v29", "v30", "v31", "v32", "v33", "v34", "v35", "v36", "v37", "v38", "v39", "v40", "v41", "v42", "v43", "v44", "v45", "v46", "v47", "v48", "v49", "v50", "v51", "v52", "v53", "v54", "v55", "v56", "v57", "v58", "v59", "v60", "v61", "v62", "v63", "v64", "v65", "v66", "v67", "v68", "v69", "v70", "v71", "v72", "v73", "v74", "v75", "v76", "v77", "v78", "v79", "v80", "v81", "v82", "v83", "v84", "v85", "v86", "v87", "v88", "v89", "v90", "v91", "v92", "v93", "v94", "v95", "v96", "v97", "v98", "v99", "v100", "v101", "v102", "v103", "v104", "v105", "v106", "v107", "v108", "v109", "v110", "v111", "v112", "v113", "v114", "v115", "v116", "v117", "v118", "v119", "v120", "v121", "v122", "v123", "v124", "v125", "v126", "v127", "v128", "v129", "v130", "v131", "v132", "v133", "v134", "v135", "v136", "v137", "v138", "v139", "v140", "v141", "v142", "v143", "v200", "v201", "v202", "v203", "v204", "v205", "v206", "v207", "v208", "v209", "v210", "v211", "v212", "v213", "v214", "v215", "v216", "v217", "v218", "v219", "v220", "v221", "v222", "v223", "v224", "v225", "v226", "v227", "v230", "v231", "v235", B_SCLOB);
        }
        __builtin_amdgcn_endpgm();
    }
}

extern "C" void kernel_launch(void* const* d_in, const int* in_sizes, int n_in,
                              void* d_out, int out_size, void* d_ws, size_t ws_size,
                              hipStream_t stream) {
    const unsigned short* x = (const unsigned short*)d_in[0];
    const unsigned short* dw = (const unsigned short*)d_in[1];
    const unsigned short* wqkv = (const unsigned short*)d_in[2];
    const unsigned short* wo = (const unsigned short*)d_in[3];
    unsigned short* out = (unsigned short*)d_out;
    u32x4* wf = (u32x4*)d_ws;
    win_attn_kernel<<<GRID, 512, 0, stream>>>(x, dw, wqkv, wo, wf, out);
}
```

```cpp
#include <hip/hip_runtime.h>
#include <hip/hip_bf16.h>
#include <stdint.h>

typedef short bf16x8 __attribute__((ext_vector_type(8)));
typedef float f32x4 __attribute__((ext_vector_type(4)));
typedef float f32x2 __attribute__((ext_vector_type(2)));
typedef unsigned u32x2 __attribute__((ext_vector_type(2)));
typedef unsigned u32x4 __attribute__((ext_vector_type(4)));

constexpr int IMG = 511;
constexpr int CH = 192;
constexpr int NWX = 73;
constexpr int NWIN = NWX * NWX;
constexpr int GRID = 256;
constexpr int ROWBYTES = IMG * CH * 2;
constexpr int XBIAS = (IMG + 1) * CH * 2;
constexpr unsigned OOB = 0x40000000u;

constexpr int FLAG_OFF = 7856;
constexpr int X_OFF = 32768;
constexpr int X1_OFF = 53632;
constexpr int O_OFF = 74496;
constexpr int V_OFF = 92928;
constexpr int KQL_OFF = 111360;
constexpr int OL_OFF = 129024;
constexpr int AT_OFF = 137472;
constexpr int CT_OFF = AT_OFF + 19584;
constexpr int RS_OFF = CT_OFF + 4096;
constexpr int RSX_OFF = 162816;
constexpr int WL_OFF = RS_OFF + 2304 + 64;
constexpr int LDS_BYTES = WL_OFF + 128;
#define RS_BASE (RS_OFF + wave * 384)
constexpr int STL_OFF = O_OFF + 12288;
constexpr int DW_OFF = O_OFF + 8448;
#define LX_OFF(i) ((i) < 2 ? CT_OFF + 1024 + (i) * 384 : KQL_OFF + ((i) - 2) * 800)
constexpr int PF_OFF = CT_OFF + 2048;
constexpr int PSLEEP = 4;
#define LB_OFF (w < 3 ? V_OFF + w * 13952 : X1_OFF)
#define LPOS(wc) (((wc) & 1) ? 0 : 6)
constexpr unsigned CROW = 0x060a0230u;
constexpr unsigned CLANE = 0x00132C3Fu;

constexpr unsigned long long TOKMAP[6] = {0x20001c190f2a2202ull, 0x052517121f061a13ull, 0x181e08151d0b260cull, 0x1b2b2903102d0901ull, 0x04162e0e2824142cull, 0x110a072127232f0dull};
constexpr unsigned long long ROWMAP[8] = {0x08321b240f1c0006ull, 0x23310e2a09170d14ull, 0x210c2f030a273418ull, 0x1d2b11221f043739ull, 0x3234363a103e192dull, 0x291a20022c3f1605ull, 0x303a3c2628122e15ull, 0x39253d1e01070b13ull};

constexpr int WF_BYTES = 8 * 36 * 1024;
constexpr int FLG_OFF = WF_BYTES;
constexpr int ARR_OFF = FLG_OFF + 4096;
constexpr int EPO_OFF = ARR_OFF + 2048;
constexpr int CTR_OFF = EPO_OFF + 128;
constexpr int REC_OFF = WF_BYTES + 8192;
constexpr int WS_BYTES = REC_OFF + 209 * 1280;
constexpr int SC1 = 16;

#define MFMA32(a, b, c) __builtin_amdgcn_mfma_f32_16x16x32_bf16((a), (b), (c), 0, 0, 0)
#define BC8(v) __builtin_bit_cast(bf16x8, (v))
#define BAR() asm volatile("s_waitcnt lgkmcnt(0)\n\ts_barrier" ::: "memory")
#define SB() __builtin_amdgcn_sched_barrier(0)
#define OPQ(x) asm volatile("" : "+v"(x))
#define ZERO4 ((f32x4){0.f, 0.f, 0.f, 0.f})

typedef __bf16 nbf16x2 __attribute__((ext_vector_type(2)));
__device__ __forceinline__ unsigned pk2(float a, float b) {
    f32x2 v = {a, b};
    nbf16x2 r = __builtin_convertvector(v, nbf16x2);
    return __builtin_bit_cast(unsigned, r);
}
__device__ __forceinline__ float bflo(unsigned u) { return __uint_as_float(u << 16); }
__device__ __forceinline__ float bfhi(unsigned u) { return __uint_as_float(u & 0xffff0000u); }

__device__ __forceinline__ float bf2f(unsigned short v) { return __uint_as_float((unsigned)v << 16); }
__device__ __forceinline__ unsigned byte_of(unsigned long long a, unsigned long long b, int i) {
    return (unsigned)(((i & 8) ? b : a) >> (8 * (i & 7))) & 0xFFu;
}
#define M24(a, b) ((unsigned)__mul24((int)(a), (int)(b)))
__device__ __forceinline__ void seq_to_win(int seq, int& wy, int& wx) {
    const bool last = seq >= 5110;
    const int s = last ? 35 : M24(seq, 28729) >> 22, r = seq - M24(146, s);
    const int kx = last ? M24(r, 171) >> 10 : r >> 2, pos = r - M24(last ? 6 : 4, kx);
    const bool low = last && pos >= (kx == 36 ? 2 : 4);
    wy = low ? 72 : 2 * s + (pos & 1);
    wx = 2 * kx + (low ? (kx == 36 ? 0 : pos - 4) : pos >> 1);
}
__device__ __forceinline__ int win_to_seq(int wy, int wx) {
    const int kx = wx >> 1;
    return M24(146, wy == 72 ? 35 : wy >> 1) + M24(wy >= 70 ? 6 : 4, kx) + (wy == 72 ? (kx == 36 ? 2 : 4 + (wx & 1)) : 2 * (wx & 1) + (wy & 1));
}
__device__ __forceinline__ int block_of(int wy, int wx) { return wy >= 70 ? 1295 + (wx & ~1) + (wy == 72) : M24(37, wy >> 1) + (wx >> 1); }
__device__ __forceinline__ void block_pos(int b, int& ky, int& kx) {
    const int s = M24(b, 1772) >> 16, l = b - 1295;
    kx = l >= 0 ? l >> 1 : b - M24(37, s);
    ky = l >= 0 ? 70 + 2 * (l & 1) : 2 * s;
}
__device__ __forceinline__ unsigned win_org(int wy, int wx) { return M24(wy, 7 * ROWBYTES) + M24(wx, 7 * CH * 2); }
__device__ __forceinline__ unsigned wl_entry(int wy, int wx) {
    return win_org(wy, wx) | (wy == 0 ? 0x08000000u : 0u) | (wx == 0 ? 0x10000000u : 0u) |
           (wx == NWX - 1 ? 0x20000000u : 0u) | ((unsigned)(wx & 1) << 30) | ((unsigned)(wy & 1) << 31);
}
__device__ __forceinline__ unsigned dma_off(int lane) {
    const int col = (lane * 43) >> 8;
    return (unsigned)(col * (CH * 2 - 96) + 16 * lane) | (col == 0 ? 0x10000000u : 0u) | (col == 8 ? 0x20000000u : 0u);
}

#if defined(__HIP_DEVICE_COMPILE__)
#define NOPK __attribute__((target("no-packed-fp32-ops")))
#else
#define NOPK
#endif
__global__ __launch_bounds__(512, 2) NOPK void win_attn_kernel(const unsigned short* __restrict__ x,
                                                          const unsigned short* __restrict__ dw, const unsigned short* __restrict__ wqkv,
                                                          const unsigned short* __restrict__ wo, u32x4* wf,
                                                          unsigned short* __restrict__ out) {
    __shared__ __attribute__((aligned(16))) char smem[LDS_BYTES];

    asm volatile("wk_code_begin:");
    const int tid = threadIdx.x;
    const int wave = __builtin_amdgcn_readfirstlane(tid >> 6);
    const int w = wave & 3;
    const unsigned ldsb = (unsigned)(size_t)smem;
    {
        unsigned long long pc;
        unsigned len, rel;
        asm volatile("s_getpc_b64 %0\n.Lwk_here%=:\n\ts_mov_b32 %1, wk_code_end-wk_code_begin\n\ts_mov_b32 %2, .Lwk_here%=-wk_code_begin" : "=s"(pc), "=s"(len), "=s"(rel));
        pc -= rel;
        const u32x4 cr = {(unsigned)pc, (unsigned)(pc >> 32) & 0xffffu, len, 0x00020000u};
        asm volatile("s_mov_b32 m0, %0\n\ts_nop 0\n\tbuffer_load_dwordx4 %1, %2, %3 offen lds"
                     :: "s"(ldsb + X1_OFF + 14336 + w * 1024), "v"((tid & 63) * 16), "s"(cr), "s"((wave * 32 + (blockIdx.x >> 3)) * 1024) : "m0", "memory");
    }
    *(unsigned*)(smem + PF_OFF + tid * 4) = 0x9E3779B9u * (tid + 1);
    *(unsigned*)(smem + (tid < 192 ? AT_OFF + 18816 + tid * 4 : FLAG_OFF + (tid & 3) * 4)) = 0u;
    BAR();
    const unsigned flg = FLG_OFF + (blockIdx.x & 7) * 512;
    asm volatile("" :: "s"(x), "s"(dw), "s"(wqkv), "s"(wo), "s"(wf));
    const auto wrsrc = __builtin_amdgcn_make_buffer_rsrc((void*)wf, 0, WS_BYTES, 0x00020000);
    unsigned tag_lo = 0, tag_hi = 0;
#define TAG_READY                                                                                  \
    {                                                                                              \
        unsigned long long tag_;                                                                   \
        asm volatile("s_load_dwordx2 %0, %1, %2\n\ts_waitcnt lgkmcnt(0)" : "=s"(tag_) : "s"(wf), "n"(EPO_OFF) : "memory"); \
        tag_lo = (unsigned)tag_ + 1u; tag_hi = (unsigned)(tag_ >> 32);                             \
    }
    const int pb = blockIdx.x;
    if (pb < 36) {
        const int lane = tid & 63, n = lane & 15, g = lane >> 4;
        const bool isO = wave >= 6;
        const int fr = isO ? 216 + 2 * pb + (wave - 6) : 6 * pb + wave;
        const int tile = fr / 6, ks = fr - 6 * tile, tw = tile / 6, mdt = tile - 6 * tw;
        const int col = isO ? 96 * (tw - 6) + 32 * (mdt >> 1) + 8 * (n >> 2) + 4 * (mdt & 1) + (n & 3)
                            : (tw < 4 ? (mdt < 3 ? 192 : -48) + 48 * tw : 96 * tw) + 16 * mdt + n;
        const int rb = isO ? 384 : 1152;
        const auto rr = __builtin_amdgcn_make_buffer_rsrc((void*)(isO ? wo : wqkv), 0, 192 * rb, 0x00020000);
        const int vo = (48 * (g >> 1) + 8 * (g & 1)) * rb + 2 * col;
        float v[8];
#pragma unroll
        for (int j = 0; j < 8; ++j) v[j] = bf2f(__builtin_amdgcn_raw_buffer_load_b16(rr, vo, (96 * (ks & 1) + 16 * (ks >> 1) + j) * rb, 0));
        TAG_READY
        float* red = (float*)smem;
        red[n * 36 + wave * 4 + g] = ((v[0] + v[1]) + (v[2] + v[3])) + ((v[4] + v[5]) + (v[6] + v[7]));
        BAR();
        float mean = 0.f;
#pragma unroll
        for (int i = 0; i < 6; ++i) {
            const f32x4 p = *(const f32x4*)(red + n * 36 + 4 * i);
            mean += (p[0] + p[1]) + (p[2] + p[3]);
        }
        mean = isO ? 0.f : mean * (1.f / 192.f);
        u32x4 o;
#pragma unroll
        for (int j = 0; j < 4; ++j) o[j] = pk2(v[2 * j] - mean, v[2 * j + 1] - mean);
        __builtin_amdgcn_raw_buffer_store_b128(o, wrsrc, lane * 16, fr * 1024, SC1);
        if (pb == 0 && tid == 448) __builtin_amdgcn_raw_buffer_store_b32(0u, wrsrc, CTR_OFF, 0, SC1);
        asm volatile("s_waitcnt vmcnt(0)" ::: "memory");
        BAR();
        if (tid < 8) __builtin_amdgcn_raw_buffer_store_b64((u32x2){tag_lo, tag_hi}, wrsrc, FLG_OFF + tid * 512 + pb * 8, 0, SC1);
    }
    const int xi = blockIdx.x >> 3, nshort = (int)(0x0506060606060606ull >> (8 * (blockIdx.x & 7))) & 63;
    const int start = 672 * (blockIdx.x & 7) - ((int)(0x2a241e18120c0600ull >> (8 * (blockIdx.x & 7))) & 63) + 21 * xi - (xi < nshort ? xi : nshort);
    const int cnt = xi < nshort ? 20 : 21;

    const auto xrsrc = __builtin_amdgcn_make_buffer_rsrc((void*)((const char*)x - XBIAS), 0, IMG * IMG * CH * 2 + XBIAS, 0x00020000);
#define POLL_ISSUE                                                                                  \
    asm volatile("s_mov_b32 m0, %0\n\ts_mov_b64 exec, 0x3ffff\n\tbuffer_load_dwordx4 %1, %2, %3 offen sc1 lds\n\ts_mov_b64 exec, -1" \
                 :: "s"(ldsb + PF_OFF), "v"(lane * 16), "s"(wrsrc), "s"(flg) : "m0", "memory");
#define POLL_W                                                                                      \
    {                                                                                               \
        const int fi_ = lane < 36 ? lane : 0;                                                       \
        for (int it_ = 0; it_ < 100000; ++it_) {                                                    \
            u32x2 f_;                                                                               \
            if (it_ < 48) asm volatile("ds_read_b64 %0, %1\n\ts_waitcnt lgkmcnt(0)" : "=v"(f_) : "v"(ldsb + PF_OFF + fi_ * 8) : "memory"); \
            else f_ = __builtin_amdgcn_raw_buffer_load_b64(wrsrc, fi_ * 8, flg, SC1);        \
            if (__builtin_amdgcn_ballot_w64(f_[0] != tag_lo || f_[1] != tag_hi) == 0ull) break;     \
            if (it_ < 47 && wave == 0) POLL_ISSUE                                                              \
            __builtin_amdgcn_s_sleep(PSLEEP);                                                       \
        }                                                                                           \
        *(u32x2*)(smem + PF_OFF + 512) = (u32x2){tag_lo, tag_hi};                                   \
        __builtin_amdgcn_fence(__ATOMIC_ACQUIRE, "wavefront");                                      \
        SB();                                                                                       \
    }
#define LANE_SETUP                                                                                 \
    int lane;                                                                                      \
    asm volatile("v_mbcnt_lo_u32_b32 %0, -1, 0\n\tv_mbcnt_hi_u32_b32 %0, -1, %0" : "=v"(lane));    \
    __builtin_assume(lane >= 0 && lane < 64);                                                      \
    const int g = lane >> 4, l15 = lane & 15, sw = l15 >> 1;                                       \
    int fE = X_OFF + l15 * 384 + ((g ^ sw) << 4);                                                  \
    OPQ(fE);

#define DMA_SLICE(en)                                                                              \
        {                                                                                          \
            const unsigned vo_ = dma_off(lane) & (0x401fffffu | ((en) & 0x38000000u));             \
            const unsigned so_ = ((en) & 0x7ffffffu) + 96 * w;                                     \
            _Pragma("unroll") for (int r = 0; r < 9; ++r) {                                        \
                unsigned m0_ = ldsb + w * 8192 + r * 872;                                          \
                unsigned sr_ = (r == 0 && ((en) & 0x08000000u)) ? OOB : so_ + r * ROWBYTES;        \
                asm volatile("s_mov_b32 m0, %0\n\ts_mov_b32 exec_hi, 0x3fffff\n\tbuffer_load_dwordx4 %1, %2, %3 offen lds\n\ts_mov_b32 exec_hi, -1" \
                             :: "s"(m0_), "v"(vo_), "s"(xrsrc), "s"(sr_) : "m0", "memory");        \
            }                                                                                      \
        }
#define XPAT(r) (((r) < 48 ? (r) : 48) * 384 + (((2 * w) ^ (((r) >> 1) & 7)) << 4))
#define SPAT(r) ((w >> 1) * 1024 + (w & 1) * 8 + (r) * 16)
#define CONV_CONSTS(var) CONV_ROWS(var) CONV_OPS
#define CONV_ROWS(var) \
            const unsigned long long rlo = g == 0 ? ROWMAP[0] : (g == 1 ? ROWMAP[2] : (g == 2 ? ROWMAP[4] : ROWMAP[6])); \
            const unsigned long long rhi = g == 0 ? ROWMAP[1] : (g == 1 ? ROWMAP[3] : (g == 2 ? ROWMAP[5] : ROWMAP[7])); \
            int xrow = byte_of(rlo, rhi, l15); \
            if (lane == (int)((CLANE >> (8 * (var))) & 0xFF)) xrow = 48; \
            if (lane == 63) xrow = (CROW >> (8 * (var))) & 0xFF; \
            const unsigned pXw_ = XPAT(xrow); \
            const unsigned pSTw_ = (w >> 1) * 1024 + (w & 1) * 8 + xrow * 16; \
            const int oy = (0x5320 >> (4 * (l15 >> 2))) & 15, ox = (0x5320 >> (4 * (l15 & 3))) & 15; \
            const unsigned aB_ = ldsb + w * 8192 + (oy + (g >> 1)) * 872 + (ox + 2 * (g & 1)) * 96;
#define CONV_OPS \
            const int dyi = g - (l15 >> 3); \
            const unsigned aA_ = ldsb + AT_OFF + (l15 & 3) * 16 + \
                                 ((dyi < 0 || dyi > 2) ? 18816 : (dyi == 0 ? 0 : (dyi == 1 ? 6208 : 12544)) + ((l15 >> 2) & 1) * 3072 + w * 768); \
            const unsigned idx_ = (l15 & 3) * 0x5555u; \
            const unsigned se = (l15 == 4 * g) ? 0x3F803F80u : 0u; \
            const u32x4 sel4 = {se, se, se, se};

    unsigned en0, en1;
    {
        LANE_SETUP
        const int px = (lane * 43) >> 8;

        int wy, wx;
        seq_to_win(start + (lane < cnt ? lane : cnt - 1), wy, wx);
        const unsigned en = wl_entry(wy, wx);
        en0 = __builtin_amdgcn_readlane(en, 0);
        en1 = __builtin_amdgcn_readlane(en, 1);
        unsigned cB, cX, cS;
        if (wave >= 4) {
            const int bk = block_of(wy, wx);
            const int key0 = __builtin_amdgcn_readlane(bk, 0), nblk = __builtin_amdgcn_readlane(bk, 63) - key0 + 1;
            const int b = l15 < nblk ? l15 : 0;
            int ky, kx;
            block_pos(key0 + b, ky, kx);
            const unsigned org = l15 < nblk ? (win_org(ky, 2 * kx) + (5 * ROWBYTES + 5 * CH * 2 + XBIAS)) | (kx == 36 ? OOB : 0u) : OOB + 0x8000000u;
            const unsigned lo = (px & 3) * (CH * 2) + (lane - 6 * px) * 16, keep = (px & 2) ? ~0u : ~OOB;
#pragma unroll
            for (int p = 0; p < 4; ++p) {
                if (p < 3 || nblk > 6) {
                    const unsigned oa = __builtin_amdgcn_readlane(org, 2 * p), ob = __builtin_amdgcn_readlane(org, 2 * p + 1);
                    const unsigned vo_ = ((lane < 24 ? oa : ob) & keep) + lo;
#pragma unroll
                    for (int r = 0; r < 4; ++r) {
                        unsigned m0_ = ldsb + LB_OFF + (4 * p + r) * 872;
                        asm volatile("s_mov_b32 m0, %0\n\ts_mov_b32 exec_hi, 0xffff\n\tbuffer_load_dwordx4 %1, %2, %3 offen lds\n\ts_mov_b32 exec_hi, -1"
                                     :: "s"(m0_), "v"(vo_), "s"(xrsrc), "s"(96 * w + r * ROWBYTES) : "m0", "memory");
                    }
                }
            }
            SB();
            const int ly = ky + (g >> 1), lx = 2 * kx + (g & 1), li = win_to_seq(ly, lx) - start;
            const bool ok = ly < NWX && lx < NWX && li >= 0 && li < cnt && l15 < nblk;
            const int row = ok ? li : 47;
            cB = ldsb + LB_OFF + (4 * (b >> 1) + (g >> 1)) * 872 + (b & 1) * 384 + (g & 1) * 192;
            cX = XPAT(row);
            cS = SPAT(row) + (unsigned)(STL_OFF - O_OFF - 18816);
            for (int it_ = 0; it_ < 100000; ++it_) {
                unsigned n_;
                asm volatile("ds_read_b32 %0, %1\n\ts_waitcnt lgkmcnt(0)" : "=v"(n_) : "v"(ldsb + FLAG_OFF + 8) : "memory");
                if (__builtin_amdgcn_readfirstlane(n_) >= 4) break;
                __builtin_amdgcn_s_sleep(1);
            }
        } else {
            {
                const auto dr = __builtin_amdgcn_make_buffer_rsrc((void*)dw, 0, 9 * 384, 0x00020000);
                unsigned m0_ = ldsb + DW_OFF + w * 864, vo_ = px * 288 + lane * 16;
                asm volatile("s_mov_b32 m0, %0\n\ts_mov_b32 exec_hi, 0x3fffff\n\tbuffer_load_dwordx4 %1, %2, %3 offen lds\n\ts_mov_b32 exec_hi, -1"
                             :: "s"(m0_), "v"(vo_), "s"(dr), "s"(96 * w) : "m0", "memory");
            }
            DMA_SLICE(en0)
            SB();
            if (wave == 2) {
                u32x4 so;
#pragma unroll
                for (int e = 0; e < 3; ++e) {
                    const int tk = byte_of(TOKMAP[2 * e], TOKMAP[2 * e + 1], l15);
                    const int ty = (tk * 37) >> 8, tx = tk - 7 * ty;
                    so[e] = (unsigned)(ty * ROWBYTES + tx * (CH * 2)) + 16 * g;
                }
                so[3] = dma_off(lane);
                *(u32x4*)(smem + CT_OFF + lane * 16) = so;
            }
            if (wave == 1 && lane < 32) *(unsigned*)(smem + WL_OFF + lane * 4) = lane < 20 ? en : 0x7ffffffu;
            if (wave == 1 && lane == 20) *(unsigned*)(smem + PF_OFF + 520) = en;
            CONV_ROWS(en0 >> 30)
            cB = aB_;
            cX = lane == (int)((CLANE >> (8 * (en0 >> 30))) & 0xFF) ? LX_OFF(0) - X_OFF + 32 * w : pXw_;
            cS = pSTw_;
            TAG_READY
            asm volatile("s_waitcnt vmcnt(9)" ::: "memory");
            if (lane < 48) {
                unsigned short dv[9];
#pragma unroll
                for (int i = 0; i < 9; ++i) dv[i] = *(const unsigned short*)(smem + DW_OFF + w * 864 + i * 96 + lane * 2);
#pragma unroll
                for (int dy = 0; dy < 3; ++dy) {
                    const unsigned a = dv[3 * dy], b2 = dv[3 * dy + 1] | (dy == 1 ? 0x3F800000u : 0u), c = dv[3 * dy + 2];
                    char* p = smem + AT_OFF + (dy == 0 ? 0 : (dy == 1 ? 6208 : 12544)) + w * 768 + lane * 16;
                    *(u32x4*)p = (u32x4){a, b2, c, 0u};
                    *(u32x4*)(p + 3072) = (u32x4){0u, a, b2, c};
                }
            }
            asm volatile("s_mov_b64 exec, 1\n\tds_add_u32 %0, %1\n\ts_mov_b64 exec, -1" :: "v"(ldsb + FLAG_OFF + 8), "v"(1) : "memory");
        }
        CONV_OPS
        asm volatile("s_waitcnt vmcnt(0) lgkmcnt(0)" ::: "memory");
        if (wave == 0) POLL_ISSUE
        asm volatile(
            "s_mov_b32 m0, s73\n\t"
            "s_cmp_eq_u32 s73, 0\n\t"
            "s_nop 7\n\t"
            "ds_read_b128 v[168:171], v254\n\t"
            "ds_read_b64 v[144:145], v253\n\t"
            "ds_read_b64 v[146:147], v253 offset:96\n\t"
            "ds_read_b64 v[148:149], v253 offset:1744\n\t"
            "ds_read_b64 v[150:151], v253 offset:1840\n\t"
            "ds_read_b128 v[172:175], v254 offset:64\n\t"
            "ds_read_b64 v[152:153], v253 offset:8\n\t"
            "ds_read_b64 v[154:155], v253 offset:104\n\t"
            "ds_read_b64 v[156:157], v253 offset:1752\n\t"
            "ds_read_b64 v[158:159], v253 offset:1848\n\t"
            "ds_read_b64 v[160:161], v253 offset:16\n\t"
            "ds_read_b64 v[162:163], v253 offset:112\n\t"
            "ds_read_b64 v[164:165], v253 offset:1760\n\t"
            "ds_read_b64 v[166:167], v253 offset:1856\n\t"
            "v_mov_b64 v[176:177], 0\n\t"
            "v_mov_b64 v[178:179], 0\n\t"
            "v_mov_b64 v[180:181], 0\n\t"
            "v_mov_b64 v[182:183], 0\n\t"
            "v_mov_b64 v[184:185], 0\n\t"
            "v_mov_b64 v[186:187], 0\n\t"
            "v_mov_b64 v[188:189], 0\n\t"
            "v_mov_b64 v[190:191], 0\n\t"
            "v_add_u32 v236, s67, v250\n\t"
            "v_xor_b32 v237, 16, v236\n\t"
            "v_add_u32 v248, s67, v251\n\t"
            "s_waitcnt lgkmcnt(9)\n\t"
            "v_smfmac_f32_16x16x64_bf16 v[176:179], v[168:171], v[144:151], v255\n\t"
            "ds_read_b128 v[168:171], v254 offset:128\n\t"
            "ds_read_b64 v[144:145], v253 offset:24\n\t"
            "s_waitcnt lgkmcnt(6)\n\t"
            "v_smfmac_f32_16x16x64_bf16 v[180:183], v[172:175], v[152:159], v255\n\t"
            "ds_read_b64 v[146:147], v253 offset:120\n\t"
            "ds_read_b64 v[148:149], v253 offset:1768\n\t"
            "ds_read_b64 v[150:151], v253 offset:1864\n\t"
            "ds_read_b128 v[172:175], v254 offset:192\n\t"
            "ds_read_b64 v[152:153], v253 offset:32\n\t"
            "ds_read_b64 v[154:155], v253 offset:128\n\t"
            "ds_read_b64 v[156:157], v253 offset:1776\n\t"
            "ds_read_b64 v[158:159], v253 offset:1872\n\t"
            "v_cvt_pk_bf16_f32 v176, v176, v177\n\t"
            "v_cvt_pk_bf16_f32 v177, v178, v179\n\t"
            "v_cvt_pk_bf16_f32 v178, v180, v181\n\t"
            "v_cvt_pk_bf16_f32 v179, v182, v183\n\t"
            "v_mfma_f32_4x4x4_16b_bf16 v[196:199], v[176:177], v[176:177], 0\n\t"
            "ds_write_b128 v236, v[176:179]\n\t"
            "v_mfma_f32_16x16x32_bf16 v[192:195], v[238:241], v[176:179], 0\n\t"
            "v_mov_b64 v[176:177], 0\n\t"
            "v_mov_b64 v[180:181], 0\n\t"
            "v_mfma_f32_4x4x4_16b_bf16 v[196:199], v[178:179], v[178:179], v[196:199]\n\t"
            "v_mov_b64 v[178:179], 0\n\t"
            "v_mov_b64 v[182:183], 0\n\t"
            "s_waitcnt lgkmcnt(10)\n\t"
            "v_smfmac_f32_16x16x64_bf16 v[184:187], v[168:171], v[160:167], v255\n\t"
            "ds_read_b128 v[168:171], v254 offset:256\n\t"
            "ds_read_b64 v[160:161], v253 offset:40\n\t"
            "ds_read_b64 v[162:163], v253 offset:136\n\t"
            "ds_read_b64 v[164:165], v253 offset:1784\n\t"
            "ds_read_b64 v[166:167], v253 offset:1880\n\t"
            "s_waitcnt lgkmcnt(10)\n\t"
            "v_smfmac_f32_16x16x64_bf16 v[188:191], v[172:175], v[144:151], v255\n\t"
            "ds_read_b128 v[172:175], v254 offset:320\n\t"
            "ds_read_b64 v[144:145], v253 offset:48\n\t"
            "ds_read_b64 v[146:147], v253 offset:144\n\t"
            "s_cbranch_scc1 .Lwk_np1%=\n\t"
            "s_mov_b64 exec, 0x3ffff\n\t"
            "buffer_load_dwordx4 v252, s[68:71], s72 offen sc1 lds\n\t"
            "s_mov_b64 exec, -1\n\t"
            ".Lwk_np1%=:\n\t"
            "ds_read_b64 v[148:149], v253 offset:1792\n\t"
            "ds_read_b64 v[150:151], v253 offset:1888\n\t"
            "v_cvt_pk_bf16_f32 v184, v184, v185\n\t"
            "v_cvt_pk_bf16_f32 v185, v186, v187\n\t"
            "s_nop 0\n\t"
            "v_cvt_pk_bf16_f32 v186, v188, v189\n\t"
            "v_cvt_pk_bf16_f32 v187, v190, v191\n\t"
            "v_mfma_f32_4x4x4_16b_bf16 v[196:199], v[184:185], v[184:185], v[196:199]\n\t"
            "s_waitcnt lgkmcnt(14)\n\t"
            "ds_write_b128 v237, v[184:187]\n\t"
            "v_mfma_f32_16x16x32_bf16 v[192:195], v[238:241], v[184:187], v[192:195]\n\t"
            "v_mov_b64 v[184:185], 0\n\t"
            "v_mov_b64 v[188:189], 0\n\t"
            "v_mfma_f32_4x4x4_16b_bf16 v[196:199], v[186:187], v[186:187], v[196:199]\n\t"
            "v_mov_b64 v[186:187], 0\n\t"
            "v_mov_b64 v[190:191], 0\n\t"
            "s_waitcnt lgkmcnt(10)\n\t"
            "v_smfmac_f32_16x16x64_bf16 v[176:179], v[168:171], v[152:159], v255\n\t"
            "ds_read_b128 v[168:171], v254 offset:384\n\t"
            "ds_read_b64 v[152:153], v253 offset:56\n\t"
            "ds_read_b64 v[154:155], v253 offset:152\n\t"
            "ds_read_b64 v[156:157], v253 offset:1800\n\t"
            "ds_read_b64 v[158:159], v253 offset:1896\n\t"
            "s_waitcnt lgkmcnt(10)\n\t"
            "v_smfmac_f32_16x16x64_bf16 v[180:183], v[172:175], v[160:167], v255\n\t"
            "ds_read_b128 v[172:175], v254 offset:448\n\t"
            "ds_read_b64 v[160:161], v253 offset:64\n\t"
            "ds_read_b64 v[162:163], v253 offset:160\n\t"
            "ds_read_b64 v[164:165], v253 offset:1808\n\t"
            "ds_read_b64 v[166:167], v253 offset:1904\n\t"
            "v_cvt_pk_bf16_f32 v176, v176, v177\n\t"
            "v_cvt_pk_bf16_f32 v177, v178, v179\n\t"
            "s_nop 0\n\t"
            "v_cvt_pk_bf16_f32 v178, v180, v181\n\t"
            "v_cvt_pk_bf16_f32 v179, v182, v183\n\t"
            "v_mfma_f32_4x4x4_16b_bf16 v[196:199], v[176:177], v[176:177], v[196:199]\n\t"
            "s_waitcnt lgkmcnt(14)\n\t"
            "ds_write_b128 v236, v[176:179] offset:128\n\t"
            "v_mfma_f32_16x16x32_bf16 v[192:195], v[238:241], v[176:179], v[192:195]\n\t"
            "v_mov_b64 v[176:177], 0\n\t"
            "v_mov_b64 v[180:181], 0\n\t"
            "v_mfma_f32_4x4x4_16b_bf16 v[196:199], v[178:179], v[178:179], v[196:199]\n\t"
            "v_mov_b64 v[178:179], 0\n\t"
            "v_mov_b64 v[182:183], 0\n\t"
            "s_waitcnt lgkmcnt(10)\n\t"
            "v_smfmac_f32_16x16x64_bf16 v[184:187], v[168:171], v[144:151], v255\n\t"
            "ds_read_b128 v[168:171], v254 offset:512\n\t"
            "ds_read_b64 v[144:145], v253 offset:72\n\t"
            "ds_read_b64 v[146:147], v253 offset:168\n\t"
            "ds_read_b64 v[148:149], v253 offset:1816\n\t"
            "ds_read_b64 v[150:151], v253 offset:1912\n\t"
            "s_waitcnt lgkmcnt(10)\n\t"
            "v_smfmac_f32_16x16x64_bf16 v[188:191], v[172:175], v[152:159], v255\n\t"
            "ds_read_b128 v[172:175], v254 offset:576\n\t"
            "ds_read_b64 v[152:153], v253 offset:80\n\t"
            "ds_read_b64 v[154:155], v253 offset:176\n\t"
            "ds_read_b64 v[156:157], v253 offset:1824\n\t"
            "ds_read_b64 v[158:159], v253 offset:1920\n\t"
            "v_cvt_pk_bf16_f32 v184, v184, v185\n\t"
            "v_cvt_pk_bf16_f32 v185, v186, v187\n\t"
            "s_cbranch_scc1 .Lwk_np0%=\n\t"
            "s_mov_b64 exec, 0x3ffff\n\t"
            "buffer_load_dwordx4 v252, s[68:71], s72 offen sc1 lds\n\t"
            "s_mov_b64 exec, -1\n\t"
            ".Lwk_np0%=:\n\t"
            "s_nop 0\n\t"
            "v_cvt_pk_bf16_f32 v186, v188, v189\n\t"
            "v_cvt_pk_bf16_f32 v187, v190, v191\n\t"
            "v_mfma_f32_4x4x4_16b_bf16 v[196:199], v[184:185], v[184:185], v[196:199]\n\t"
            "s_waitcnt lgkmcnt(14)\n\t"
            "ds_write_b128 v237, v[184:187] offset:128\n\t"
            "v_mfma_f32_16x16x32_bf16 v[192:195], v[238:241], v[184:187], v[192:195]\n\t"
            "v_mov_b64 v[184:185], 0\n\t"
            "v_mov_b64 v[188:189], 0\n\t"
            "v_mfma_f32_4x4x4_16b_bf16 v[196:199], v[186:187], v[186:187], v[196:199]\n\t"
            "v_mov_b64 v[186:187], 0\n\t"
            "v_mov_b64 v[190:191], 0\n\t"
            "s_waitcnt lgkmcnt(10)\n\t"
            "v_smfmac_f32_16x16x64_bf16 v[176:179], v[168:171], v[160:167], v255\n\t"
            "ds_read_b128 v[168:171], v254 offset:640\n\t"
            "ds_read_b64 v[160:161], v253 offset:88\n\t"
            "ds_read_b64 v[162:163], v253 offset:184\n\t"
            "ds_read_b64 v[164:165], v253 offset:1832\n\t"
            "ds_read_b64 v[166:167], v253 offset:1928\n\t"
            "s_waitcnt lgkmcnt(10)\n\t"
            "v_smfmac_f32_16x16x64_bf16 v[180:183], v[172:175], v[144:151], v255\n\t"
            "ds_read_b128 v[172:175], v254 offset:704\n\t"
            "v_cvt_pk_bf16_f32 v176, v176, v177\n\t"
            "v_cvt_pk_bf16_f32 v177, v178, v179\n\t"
            "s_nop 1\n\t"
            "v_mfma_f32_4x4x4_16b_bf16 v[196:199], v[176:177], v[176:177], v[196:199]\n\t"
            "s_nop 1\n\t"
            "v_cvt_pk_bf16_f32 v178, v180, v181\n\t"
            "v_cvt_pk_bf16_f32 v179, v182, v183\n\t"
            "s_nop 1\n\t"
            "v_mfma_f32_4x4x4_16b_bf16 v[196:199], v[178:179], v[178:179], v[196:199]\n\t"
            "ds_write_b128 v236, v[176:179] offset:256\n\t"
            "v_mfma_f32_16x16x32_bf16 v[192:195], v[238:241], v[176:179], v[192:195]\n\t"
            "s_waitcnt lgkmcnt(6)\n\t"
            "v_smfmac_f32_16x16x64_bf16 v[184:187], v[168:171], v[152:159], v255\n\t"
            "s_waitcnt lgkmcnt(1)\n\t"
            "v_smfmac_f32_16x16x64_bf16 v[188:191], v[172:175], v[160:167], v255\n\t"
            "s_nop 5\n\t"
            "v_cvt_pk_bf16_f32 v184, v184, v185\n\t"
            "v_cvt_pk_bf16_f32 v185, v186, v187\n\t"
            "s_nop 1\n\t"
            "v_mfma_f32_4x4x4_16b_bf16 v[196:199], v[184:185], v[184:185], v[196:199]\n\t"
            "v_cvt_pk_bf16_f32 v186, v188, v189\n\t"
            "v_cvt_pk_bf16_f32 v187, v190, v191\n\t"
            "s_nop 1\n\t"
            "v_mfma_f32_4x4x4_16b_bf16 v[196:199], v[186:187], v[186:187], v[196:199]\n\t"
            "v_mfma_f32_16x16x32_bf16 v[192:195], v[238:241], v[184:187], v[192:195]\n\t"
            "ds_write_b128 v237, v[184:187] offset:256\n\t"
            "s_nop 6\n\t"
            "v_cndmask_b32_e64 v193, v196, v197, s[60:61]\n\t"
            "v_cndmask_b32_e64 v193, v193, v198, s[62:63]\n\t"
            "v_cndmask_b32_e64 v193, v193, v199, s[64:65]\n\t"
            "ds_write_b64 v248, v[192:193] offset:18816\n\t"
            "s_waitcnt lgkmcnt(0)\n\t"
            "s_nop 7\n\t"
            :
            : "{v250}"(cX), "{v251}"(cS), "{v253}"(cB), "{v254}"(aA_), "{v255}"(idx_), "{v[238:241]}"(sel4), "{s67}"(ldsb + (wave < 4 ? X_OFF : O_OFF)),
              "{v252}"(lane * 16), "{s[68:71]}"(wrsrc), "{s72}"(flg), "{s73}"(wave == 0 ? ldsb + PF_OFF : 0u),
              "{s[60:61]}"(0x2222222222222222ull), "{s[62:63]}"(0x4444444444444444ull), "{s[64:65]}"(0x8888888888888888ull)
            : "v144", "v145", "v146", "v147", "v148", "v149", "v150", "v151", "v152", "v153", "v154", "v155", "v156", "v157", "v158", "v159", "v160", "v161", "v162", "v163", "v164", "v165", "v166", "v167", "v168", "v169", "v170", "v171", "v172", "v173", "v174", "v175", "v176", "v177", "v178", "v179", "v180", "v181", "v182", "v183", "v184", "v185", "v186", "v187", "v188", "v189", "v190", "v191", "v192", "v193", "v194", "v195", "v196", "v197", "v198", "v199", "v236", "v237", "v248", "v249", "m0", "scc", "memory");
        if (wave < 4) {
            POLL_W
        }
    }
    const unsigned long long mrow = 0x0001000100010001ull * ((1u << (cnt - 16)) - 1u);
#define KARGS                                                                                      \
    unsigned long long xb, wb, ob;                                                                 \
    asm volatile("s_load_dwordx2 %0, %3, 0x0\n\ts_load_dwordx2 %1, %3, 0x20\n\ts_load_dwordx2 %2, %3, 0x28\n\ts_waitcnt lgkmcnt(0)" \
                 : "=&s"(xb), "=&s"(wb), "=&s"(ob) : "s"(__builtin_amdgcn_kernarg_segment_ptr()) : "memory"); \
    xb -= XBIAS;                                                                                   \
    const u32x4 wr = {(unsigned)wb, (unsigned)(wb >> 32) & 0xffffu, (unsigned)WS_BYTES, 0x00020000u}; \
    const u32x4 orr = {(unsigned)ob, (unsigned)(ob >> 32) & 0xffffu, (unsigned)(IMG * IMG * CH * 2), 0x00020000u}; \
    const u32x4 xr = {(unsigned)xb, (unsigned)(xb >> 32) & 0xffffu, (unsigned)(IMG * IMG * CH * 2 + XBIAS), 0x00020000u};
    const int tk_ = (xi + 4) / 6;
    const unsigned touch = wave * 36864 + ((xi + 4 - 6 * tk_) * 6 + tk_) * 1024;
#define B_SCALARS \
            CONV_CONSTS(0) \
            const unsigned aCA_ = ldsb + CT_OFF + lane * 16; \
            KARGS \
            unsigned s45 = OOB, s46 = (en0 & 0x7ffffffu) + 96 * w, s47 = (en1 & 0x7ffffffu) + 96 * w; \
            unsigned s48 = ldsb + X_OFF, s67 = ldsb + X1_OFF, s76 = ldsb + WL_OFF + 8, s70 = 0; \
            unsigned s56 = __builtin_amdgcn_readfirstlane((en0 >> 30) * 4 + (en1 >> 30)); \
            unsigned s69 = 0x401fffffu | (en1 & 0x38000000u), s79 = (en1 & 0x08000000u) ? OOB : s47, s59 = ldsb + LX_OFF(1) + 32 * w; \
            unsigned long long wbase = wb + 6 * 36864; \
            unsigned wlen = 2 * 36864; \
            if (wave >= 6) { \
                unsigned rel; \
                asm volatile("s_getpc_b64 %0\n.Lwk_h2%=:\n\ts_mov_b32 %1, wk_code_end-wk_code_begin\n\ts_mov_b32 %2, .Lwk_h2%=-wk_code_begin" : "=s"(wbase), "=s"(wlen), "=s"(rel)); \
                wbase -= rel; \
            } \
            const unsigned wo_ = xi * 3456 + 96 + 96 * (w & 1); \
            wbase = (wbase + wo_) & 0xffffffffffffull; \
            wlen = __builtin_amdgcn_readfirstlane(wlen > wo_ ? wlen - wo_ : 0u); \
            unsigned long long xp = 0, sp = 0; \
            _Pragma("unroll") for (int v = 0; v < 4; ++v) { \
                const unsigned r = (CROW >> (8 * v)) & 0xFF; \
                xp |= (unsigned long long)XPAT(r) << (16 * v); \
                sp |= (unsigned long long)SPAT(r) << (16 * v); \
            }
#define B_SIO "+{s45}"(s45), "+{s46}"(s46), "+{s47}"(s47), "+{s48}"(s48), "+{s67}"(s67), "+{s76}"(s76), "+{s56}"(s56), "+{s70}"(s70), "+{s69}"(s69), "+{s79}"(s79), "+{s59}"(s59)
#define B_SIN "{v[238:241]}"(sel4), "{s[40:43]}"(orr), "{s[72:75]}"(xr), "{s39}"(0x401fffffu), "{s[28:31]}"(wr), "{s33}"(wave * 36864), "{s78}"(96 * w), \
              "{s27}"(96 * w - 384), "{s[50:51]}"(xp), "{s[52:53]}"(sp), "{s54}"(CLANE), "{s55}"(CROW & 0x0F0F0F0Fu), "{s[34:35]}"(0x0001000100010001ull), \
              "{s[60:61]}"(0x2222222222222222ull), "{s[62:63]}"(0x4444444444444444ull), "{s[64:65]}"(0x8888888888888888ull), \
              "{s66}"(ldsb + w * 8192), "{s71}"((ldsb + X_OFF) ^ (ldsb + X1_OFF)), "{s[86:87]}"(wbase), "{s85}"(wlen)
#define B_SCLOB "s83", "s84", "s88", "s89", "s90", "s91", "s26", "s77", "s68", "s36", "s37", "s38", "s57", "s58", "s49", "vcc", "scc", "m0", "memory"

    if (wave < 4) {
        LANE_SETUP
        const int dW = X_OFF + l15 * 384 + (((2 * w + (g >> 1)) ^ sw) << 4) + 8 * (g & 1);
        const int vB = V_OFF + w * 4608 + 1024 + lane * 8, rsR = RS_BASE + l15 * 4;
        {
            const unsigned rsb = ldsb + RS_BASE;
            const unsigned pE_ = fE - X_OFF, aO_ = ldsb + dW + (O_OFF - X_OFF), sel_ = (l15 & 3) ? 0u : 1u, aVB_ = ldsb + vB;
            const unsigned pST_ = lane * 16, aPLw_ = rsb + lane * 2, aRSr_ = ldsb + rsR;
            unsigned aRSw_ = rsb + lane * 4;
            const unsigned g16_ = (l15 & 3) ? ldsb + AT_OFF + 18816 : g * 16, g8_ = (l15 & 3) ? ldsb + AT_OFF + 18816 : g * 8;
            const unsigned loVL_ = ((2 * w + (l15 >> 3)) << 4) + (l15 & 7) * 2, loOL_ = g * 128;
            const unsigned on = (l15 & 3) ? 0u : 0x3F803F80u;
            const u32x4 ones4 = {on, on, on, on};
            unsigned s48 = ldsb + X_OFF, s70 = 0;
            unsigned s72 = ldsb + KQL_OFF + w * 192, s73 = ldsb + OL_OFF, s74 = ldsb + KQL_OFF + 784, s77 = ldsb + OL_OFF, s76 = ldsb + KQL_OFF + 768 + w * 4;
            KARGS
            asm volatile(
            "s_nop 4\n\t"
            "buffer_load_dwordx4 v[140:143], v248, s[84:87], s88 offen sc1\n\t"
            "s_add_u32 s49, s46, 0\n\t"
            "buffer_load_dwordx4 v[0:3], v248, s[84:87], s49 offen sc1\n\t"
            "s_add_u32 s49, s46, 6144\n\t"
            "buffer_load_dwordx4 v[24:27], v248, s[84:87], s49 offen sc1\n\t"
            "s_add_u32 s49, s46, 12288\n\t"
            "buffer_load_dwordx4 v[48:51], v248, s[84:87], s49 offen sc1\n\t"
            "s_add_u32 s49, s46, 18432\n\t"
            "buffer_load_dwordx4 v[72:75], v248, s[84:87], s49 offen sc1\n\t"
            "s_add_u32 s49, s46, 24576\n\t"
            "buffer_load_dwordx4 v[96:99], v248, s[84:87], s49 offen sc1\n\t"
            "s_add_u32 s49, s46, 30720\n\t"
            "buffer_load_dwordx4 v[120:123], v248, s[84:87], s49 offen sc1\n\t"
            "s_add_u32 s49, s46, 1024\n\t"
            "buffer_load_dwordx4 v[4:7], v248, s[84:87], s49 offen sc1\n\t"
            "s_add_u32 s49, s46, 7168\n\t"
            "buffer_load_dwordx4 v[28:31], v248, s[84:87], s49 offen sc1\n\t"
            "s_add_u32 s49, s46, 13312\n\t"
            "buffer_load_dwordx4 v[52:55], v248, s[84:87], s49 offen sc1\n\t"
            "s_add_u32 s49, s46, 19456\n\t"
            "buffer_load_dwordx4 v[76:79], v248, s[84:87], s49 offen sc1\n\t"
            "s_add_u32 s49, s46, 25600\n\t"
            "buffer_load_dwordx4 v[100:103], v248, s[84:87], s49 offen sc1\n\t"
            "s_add_u32 s49, s46, 31744\n\t"
            "buffer_load_dwordx4 v[124:127], v248, s[84:87], s49 offen sc1\n\t"
            "s_add_u32 s49, s46, 2048\n\t"
            "buffer_load_dwordx4 v[8:11], v248, s[84:87], s49 offen sc1\n\t"
            "s_add_u32 s49, s46, 8192\n\t"
            "buffer_load_dwordx4 v[32:35], v248, s[84:87], s49 offen sc1\n\t"
            "s_add_u32 s49, s46, 14336\n\t"
            "buffer_load_dwordx4 v[56:59], v248, s[84:87], s49 offen sc1\n\t"
            "s_add_u32 s49, s46, 20480\n\t"
            "buffer_load_dwordx4 v[80:83], v248, s[84:87], s49 offen sc1\n\t"
            "s_add_u32 s49, s46, 26624\n\t"
            "buffer_load_dwordx4 v[104:107], v248, s[84:87], s49 offen sc1\n\t"
            "s_add_u32 s49, s46, 32768\n\t"
            "buffer_load_dwordx4 v[128:131], v248, s[84:87], s49 offen sc1\n\t"
            "s_add_u32 s49, s46, 3072\n\t"
            "buffer_load_dwordx4 v[12:15], v248, s[84:87], s49 offen sc1\n\t"
            "s_add_u32 s49, s46, 9216\n\t"
            "buffer_load_dwordx4 v[36:39], v248, s[84:87], s49 offen sc1\n\t"
            "s_add_u32 s49, s46, 15360\n\t"
            "buffer_load_dwordx4 v[60:63], v248, s[84:87], s49 offen sc1\n\t"
            "s_add_u32 s49, s46, 21504\n\t"
            "buffer_load_dwordx4 v[84:87], v248, s[84:87], s49 offen sc1\n\t"
            "s_add_u32 s49, s46, 27648\n\t"
            "buffer_load_dwordx4 v[108:111], v248, s[84:87], s49 offen sc1\n\t"
            "s_add_u32 s49, s46, 33792\n\t"
            "buffer_load_dwordx4 v[132:135], v248, s[84:87], s49 offen sc1\n\t"
            "s_add_u32 s49, s46, 4096\n\t"
            "buffer_load_dwordx4 v[16:19], v248, s[84:87], s49 offen sc1\n\t"
            "s_add_u32 s49, s46, 10240\n\t"
            "buffer_load_dwordx4 v[40:43], v248, s[84:87], s49 offen sc1\n\t"
            "s_add_u32 s49, s46, 16384\n\t"
            "buffer_load_dwordx4 v[64:67], v248, s[84:87], s49 offen sc1\n\t"
            "s_add_u32 s49, s46, 22528\n\t"
            "buffer_load_dwordx4 v[88:91], v248, s[84:87], s49 offen sc1\n\t"
            "s_add_u32 s49, s46, 28672\n\t"
            "buffer_load_dwordx4 v[112:115], v248, s[84:87], s49 offen sc1\n\t"
            "s_add_u32 s49, s46, 34816\n\t"
            "buffer_load_dwordx4 v[136:139], v248, s[84:87], s49 offen sc1\n\t"
            "s_add_u32 s49, s46, 5120\n\t"
            "buffer_load_dwordx4 v[20:23], v248, s[84:87], s49 offen sc1\n\t"
            "s_add_u32 s49, s46, 11264\n\t"
            "buffer_load_dwordx4 v[44:47], v248, s[84:87], s49 offen sc1\n\t"
            "s_add_u32 s49, s46, 17408\n\t"
            "buffer_load_dwordx4 v[68:71], v248, s[84:87], s49 offen sc1\n\t"
            "s_add_u32 s49, s46, 23552\n\t"
            "buffer_load_dwordx4 v[92:95], v248, s[84:87], s49 offen sc1\n\t"
            "s_add_u32 s49, s46, 29696\n\t"
            "buffer_load_dwordx4 v[116:119], v248, s[84:87], s49 offen sc1\n\t"
            "s_add_u32 s49, s46, 35840\n\t"
            "buffer_load_dwordx4 v[140:143], v248, s[84:87], s49 offen sc1\n\t"
            "s_waitcnt lgkmcnt(0)\n\t"
            "s_barrier\n\t"
            "s_setprio 2\n\t"
            "v_add_u32 v235, s71, v248\n\t"
            "v_add_u32 v232, s71, v244\n\t"
            "v_xor_b32 v233, 64, v232\n\t"
            "v_subrev_u32 v239, s75, v251\n\t"
            "v_mul_u32_u24 v239, 200, v239\n\t"
            "v_lshrrev_b32 v234, 3, v255\n\t"
            "v_add3_u32 v234, v234, v239, s72\n\t"
            "ds_read_b128 v[228:231], v235 offset:12288\n\t"
            "ds_read_b128 v[144:147], v232\n\t"
            "ds_read_b128 v[148:151], v233\n\t"
            "ds_read_b128 v[204:207], v232 offset:6144\n\t"
            "ds_read_b128 v[208:211], v233 offset:6144\n\t"
            "ds_read_b128 v[152:155], v232 offset:128\n\t"
            "ds_read_b128 v[156:159], v233 offset:128\n\t"
            "ds_read_b128 v[212:215], v232 offset:6272\n\t"
            "ds_read_b128 v[224:227], v233 offset:6272\n\t"
            "ds_read_b128 v[160:163], v232 offset:256\n\t"
            "ds_read_b128 v[164:167], v233 offset:256\n\t"
            "s_waitcnt vmcnt(24)\n\t"
            "s_waitcnt lgkmcnt(10)\n\t"
            "v_add_f32 v236, v228, v230\n\t"
            "v_add_f32 v237, v229, v231\n\t"
            "s_waitcnt lgkmcnt(8)\n\t"
            "v_mfma_f32_16x16x32_bf16 v[168:171], v[0:3], v[144:147], 0\n\t"
            "ds_read_b128 v[228:231], v235 offset:13312\n\t"
            "v_lshrrev_b32 v235, 4, v255\n\t"
            "v_mfma_f32_16x16x32_bf16 v[168:171], v[4:7], v[148:151], v[168:171]\n\t"
            "v_add3_u32 v235, v235, v239, s72\n\t"
            "v_mfma_f32_16x16x32_bf16 v[176:179], v[48:51], v[144:147], 0\n\t"
            "v_mfma_f32_16x16x32_bf16 v[176:179], v[52:55], v[148:151], v[176:179]\n\t"
            "v_mfma_f32_16x16x32_bf16 v[172:175], v[24:27], v[144:147], 0\n\t"
            "v_mfma_f32_16x16x32_bf16 v[172:175], v[28:31], v[148:151], v[172:175]\n\t"
            "v_mfma_f32_16x16x32_bf16 v[180:183], v[72:75], v[144:147], 0\n\t"
            "v_mfma_f32_16x16x32_bf16 v[180:183], v[76:79], v[148:151], v[180:183]\n\t"
            "s_waitcnt lgkmcnt(0)\n\t"
            "v_add_f32 v228, v228, v230\n\t"
            "v_add_f32 v236, v236, v228\n\t"
            "v_mfma_f32_16x16x32_bf16 v[184:187], v[96:99], v[144:147], 0\n\t"
            "v_add_f32 v229, v229, v231\n\t"
            "v_mul_f32 v236, 0x3baaaaab, v236\n\t"
            "v_mfma_f32_16x16x32_bf16 v[184:187], v[100:103], v[148:151], v[184:187]\n\t"
            "v_add_f32 v237, v237, v229\n\t"
            "v_mul_f32_e64 v236, v236, -v236\n\t"
            "v_mfma_f32_16x16x32_bf16 v[188:191], v[120:123], v[144:147], 0\n\t"
            "v_fmac_f32 v236, 0x3baaaaab, v237\n\t"
            "v_max_f32 v236, 0, v236\n\t"
            "v_mfma_f32_16x16x32_bf16 v[188:191], v[124:127], v[148:151], v[188:191]\n\t"
            "v_add_f32 v236, 0x3727c5ac, v236\n\t"
            "v_rsq_f32 v236, v236\n\t"
            "v_mfma_f32_16x16x32_bf16 v[192:195], v[0:3], v[204:207], 0\n\t"
            "v_mfma_f32_16x16x32_bf16 v[192:195], v[4:7], v[208:211], v[192:195]\n\t"
            "ds_write_b32 v249, v236\n\t"
            "v_mfma_f32_16x16x32_bf16 v[196:199], v[24:27], v[204:207], 0\n\t"
            "ds_read_b32 v236, v251\n\t"
            "ds_read_b32 v237, v251 offset:64\n\t"
            "v_mfma_f32_16x16x32_bf16 v[196:199], v[28:31], v[208:211], v[196:199]\n\t"
            "v_mfma_f32_16x16x32_bf16 v[200:203], v[48:51], v[204:207], 0\n\t"
            "v_mfma_f32_16x16x32_bf16 v[200:203], v[52:55], v[208:211], v[200:203]\n\t"
            "v_mfma_f32_16x16x32_bf16 v[216:219], v[72:75], v[204:207], 0\n\t"
            "v_mfma_f32_16x16x32_bf16 v[216:219], v[76:79], v[208:211], v[216:219]\n\t"
            "v_mfma_f32_16x16x32_bf16 v[220:223], v[96:99], v[204:207], 0\n\t"
            "v_mfma_f32_16x16x32_bf16 v[220:223], v[100:103], v[208:211], v[220:223]\n\t"
            "v_mfma_f32_16x16x32_bf16 v[228:231], v[120:123], v[204:207], 0\n\t"
            "ds_read_b128 v[204:207], v232 offset:6400\n\t"
            "v_add_u32 v232, s48, v244\n\t"
            "v_mfma_f32_16x16x32_bf16 v[228:231], v[124:127], v[208:211], v[228:231]\n\t"
            "ds_read_b128 v[208:211], v233 offset:6400\n\t"
            "s_waitcnt vmcnt(12)\n\t"
            "v_mfma_f32_16x16x32_bf16 v[168:171], v[8:11], v[152:155], v[168:171]\n\t"
            "v_xor_b32 v233, 64, v232\n\t"
            "v_mfma_f32_16x16x32_bf16 v[168:171], v[12:15], v[156:159], v[168:171]\n\t"
            "v_mfma_f32_16x16x32_bf16 v[176:179], v[56:59], v[152:155], v[176:179]\n\t"
            "v_mfma_f32_16x16x32_bf16 v[176:179], v[60:63], v[156:159], v[176:179]\n\t"
            "v_mfma_f32_16x16x32_bf16 v[172:175], v[32:35], v[152:155], v[172:175]\n\t"
            "v_mfma_f32_16x16x32_bf16 v[172:175], v[36:39], v[156:159], v[172:175]\n\t"
            "v_mfma_f32_16x16x32_bf16 v[180:183], v[80:83], v[152:155], v[180:183]\n\t"
            "v_mfma_f32_16x16x32_bf16 v[180:183], v[84:87], v[156:159], v[180:183]\n\t"
            "v_mfma_f32_16x16x32_bf16 v[184:187], v[104:107], v[152:155], v[184:187]\n\t"
            "v_mfma_f32_16x16x32_bf16 v[184:187], v[108:111], v[156:159], v[184:187]\n\t"
            "v_mfma_f32_16x16x32_bf16 v[188:191], v[128:131], v[152:155], v[188:191]\n\t"
            "v_mfma_f32_16x16x32_bf16 v[188:191], v[132:135], v[156:159], v[188:191]\n\t"
            "v_mfma_f32_16x16x32_bf16 v[192:195], v[8:11], v[212:215], v[192:195]\n\t"
            "v_mfma_f32_16x16x32_bf16 v[192:195], v[12:15], v[224:227], v[192:195]\n\t"
            "v_mfma_f32_16x16x32_bf16 v[196:199], v[32:35], v[212:215], v[196:199]\n\t"
            "v_mfma_f32_16x16x32_bf16 v[196:199], v[36:39], v[224:227], v[196:199]\n\t"
            "v_mfma_f32_16x16x32_bf16 v[200:203], v[56:59], v[212:215], v[200:203]\n\t"
            "v_mfma_f32_16x16x32_bf16 v[200:203], v[60:63], v[224:227], v[200:203]\n\t"
            "v_mfma_f32_16x16x32_bf16 v[216:219], v[80:83], v[212:215], v[216:219]\n\t"
            "v_mfma_f32_16x16x32_bf16 v[216:219], v[84:87], v[224:227], v[216:219]\n\t"
            "v_mfma_f32_16x16x32_bf16 v[220:223], v[104:107], v[212:215], v[220:223]\n\t"
            "v_mfma_f32_16x16x32_bf16 v[220:223], v[108:111], v[224:227], v[220:223]\n\t"
            "v_mfma_f32_16x16x32_bf16 v[228:231], v[128:131], v[212:215], v[228:231]\n\t"
            "ds_read_b128 v[212:215], v232 offset:256\n\t"
            "v_mfma_f32_16x16x32_bf16 v[228:231], v[132:135], v[224:227], v[228:231]\n\t"
            "ds_read_b128 v[224:227], v233 offset:256\n\t"
            "s_waitcnt vmcnt(0)\n\t"
            "v_mfma_f32_16x16x32_bf16 v[168:171], v[16:19], v[160:163], v[168:171]\n\t"
            "v_mfma_f32_16x16x32_bf16 v[168:171], v[20:23], v[164:167], v[168:171]\n\t"
            "v_mfma_f32_16x16x32_bf16 v[176:179], v[64:67], v[160:163], v[176:179]\n\t"
            "v_mfma_f32_16x16x32_bf16 v[176:179], v[68:71], v[164:167], v[176:179]\n\t"
            "v_mfma_f32_16x16x32_bf16 v[180:183], v[88:91], v[160:163], v[180:183]\n\t"
            "v_mfma_f32_16x16x32_bf16 v[180:183], v[92:95], v[164:167], v[180:183]\n\t"
            "v_mfma_f32_16x16x32_bf16 v[172:175], v[40:43], v[160:163], v[172:175]\n\t"
            "v_mfma_f32_16x16x32_bf16 v[172:175], v[44:47], v[164:167], v[172:175]\n\t"
            "s_waitcnt lgkmcnt(5)\n\t"
            "s_nop 0\n\t"
            "v_mul_f32 v168, v236, v168\n\t"
            "v_mfma_f32_16x16x32_bf16 v[184:187], v[112:115], v[160:163], v[184:187]\n\t"
            "v_mul_f32 v169, v236, v169\n\t"
            "v_mul_f32 v170, v236, v170\n\t"
            "v_mfma_f32_16x16x32_bf16 v[184:187], v[116:119], v[164:167], v[184:187]\n\t"
            "v_mul_f32 v176, v236, v176\n\t"
            "v_mul_f32 v177, v236, v177\n\t"
            "v_mfma_f32_16x16x32_bf16 v[188:191], v[136:139], v[160:163], v[188:191]\n\t"
            "v_mul_f32 v171, v236, v171\n\t"
            "v_mul_f32 v178, v236, v178\n\t"
            "v_mfma_f32_16x16x32_bf16 v[188:191], v[140:143], v[164:167], v[188:191]\n\t"
            "v_mul_f32 v179, v236, v179\n\t"
            "v_cvt_pk_bf16_f32 v168, v168, v169\n\t"
            "s_waitcnt lgkmcnt(2)\n\t"
            "v_mfma_f32_16x16x32_bf16 v[192:195], v[16:19], v[204:207], v[192:195]\n\t"
            "v_cvt_pk_bf16_f32 v176, v176, v177\n\t"
            "v_cvt_pk_bf16_f32 v180, v180, v181\n\t"
            "v_mfma_f32_16x16x32_bf16 v[192:195], v[20:23], v[208:211], v[192:195]\n\t"
            "v_mul_f32 v172, v236, v172\n\t"
            "v_mul_f32 v173, v236, v173\n\t"
            "v_mfma_f32_16x16x32_bf16 v[196:199], v[40:43], v[204:207], v[196:199]\n\t"
            "v_mul_f32 v174, v236, v174\n\t"
            "v_mul_f32 v175, v236, v175\n\t"
            "v_mfma_f32_16x16x32_bf16 v[196:199], v[44:47], v[208:211], v[196:199]\n\t"
            "v_cvt_pk_bf16_f32 v169, v170, v171\n\t"
            "v_cvt_pk_bf16_f32 v177, v178, v179\n\t"
            "v_mfma_f32_16x16x32_bf16 v[200:203], v[64:67], v[204:207], v[200:203]\n\t"
            "v_cvt_pk_bf16_f32 v181, v182, v183\n\t"
            "v_cvt_pk_bf16_f32 v170, v172, v173\n\t"
            "v_mfma_f32_16x16x32_bf16 v[200:203], v[68:71], v[208:211], v[200:203]\n\t"
            "v_cvt_pk_bf16_f32 v171, v174, v175\n\t"
            "v_cvt_pk_bf16_f32 v182, v184, v185\n\t"
            "v_mfma_f32_16x16x32_bf16 v[216:219], v[88:91], v[204:207], v[216:219]\n\t"
            "v_cvt_pk_bf16_f32 v183, v186, v187\n\t"
            "v_cvt_pk_bf16_f32 v178, v188, v189\n\t"
            "v_mfma_f32_16x16x32_bf16 v[216:219], v[92:95], v[208:211], v[216:219]\n\t"
            "v_cvt_pk_bf16_f32 v179, v190, v191\n\t"
            "v_mul_f32 v236, 0x3e553b94, v236\n\t"
            "v_mfma_f32_16x16x32_bf16 v[220:223], v[112:115], v[204:207], v[220:223]\n\t"
            "v_add_u32 v184, s76, v239\n\t"
            "v_add_u32 v185, s74, v239\n\t"
            "v_mfma_f32_16x16x32_bf16 v[220:223], v[116:119], v[208:211], v[220:223]\n\t"
            "s_mov_b64 exec, -1\n\t"
            "ds_write_b128 v234, v[168:171]\n\t"
            "ds_write_b128 v234, v[180:183] offset:64\n\t"
            "ds_write_b64 v235, v[176:177] offset:128\n\t"
            "ds_write_b64 v235, v[178:179] offset:160\n\t"
            "s_mov_b64 exec, -1\n\t"
            "v_mfma_f32_16x16x16_bf16 v[172:175], v[176:177], v[178:179], 0\n\t"
            "v_mul_f32 v192, v237, v192\n\t"
            "v_mul_f32 v193, v237, v193\n\t"
            "v_mul_f32 v200, v237, v200\n\t"
            "v_mfma_f32_16x16x32_bf16 v[228:231], v[136:139], v[204:207], v[228:231]\n\t"
            "ds_read_b128 v[204:207], v232\n\t"
            "v_mul_f32 v201, v237, v201\n\t"
            "v_mfma_f32_16x16x32_bf16 v[228:231], v[140:143], v[208:211], v[228:231]\n\t"
            "ds_read_b128 v[208:211], v233\n\t"
            "v_mul_f32 v194, v237, v194\n\t"
            "v_mfma_f32_16x16x32_bf16 v[172:175], v[168:171], v[180:183], v[172:175]\n\t"
            "v_mul_f32 v195, v237, v195\n\t"
            "v_mul_f32 v202, v237, v202\n\t"
            "v_mul_f32 v203, v237, v203\n\t"
            "v_cvt_pk_bf16_f32 v192, v192, v193\n\t"
            "v_cvt_pk_bf16_f32 v200, v200, v201\n\t"
            "v_cvt_pk_bf16_f32 v216, v216, v217\n\t"
            "v_mul_f32 v196, v237, v196\n\t"
            "v_mul_f32 v197, v237, v197\n\t"
            "v_mul_f32 v198, v237, v198\n\t"
            "v_mul_f32 v199, v237, v199\n\t"
            "v_cvt_pk_bf16_f32 v193, v194, v195\n\t"
            "v_cvt_pk_bf16_f32 v201, v202, v203\n\t"
            "v_cvt_pk_bf16_f32 v217, v218, v219\n\t"
            "v_cvt_pk_bf16_f32 v194, v196, v197\n\t"
            "v_cvt_pk_bf16_f32 v195, v198, v199\n\t"
            "v_cvt_pk_bf16_f32 v218, v220, v221\n\t"
            "v_cvt_pk_bf16_f32 v219, v222, v223\n\t"
            "v_cvt_pk_bf16_f32 v202, v228, v229\n\t"
            "v_cvt_pk_bf16_f32 v203, v230, v231\n\t"
            "v_mul_f32 v237, 0x3e553b94, v237\n\t"
            "v_add_u32 v220, s76, v239\n\t"
            "v_mfma_f32_16x16x16_bf16 v[196:199], v[200:201], v[202:203], 0\n\t"
            "s_and_b32 exec_lo, s56, 0x11111111\n\t"
            "s_and_b32 exec_hi, s57, 0x11111111\n\t"
            "ds_write_b32 v184, v172 offset:0\n\t"
            "s_and_b32 exec_lo, s56, 0x22222222\n\t"
            "s_and_b32 exec_hi, s57, 0x22222222\n\t"
            "ds_write_b32 v184, v173 offset:0\n\t"
            "s_and_b32 exec_lo, s56, 0x44444444\n\t"
            "s_and_b32 exec_hi, s57, 0x44444444\n\t"
            "ds_write_b32 v184, v174 offset:0\n\t"
            "s_and_b32 exec_lo, s56, 0x88888888\n\t"
            "s_and_b32 exec_hi, s57, 0x88888888\n\t"
            "ds_write_b32 v184, v175 offset:0\n\t"
            "s_mov_b64 exec, s[56:57]\n\t"
            "ds_write_b32 v185, v236 offset:0\n\t"
            "s_mov_b64 exec, -1\n\t"
            "s_waitcnt lgkmcnt(5)\n\t"
            "v_mfma_f32_16x16x32_bf16 v[144:147], v[0:3], v[204:207], 0\n\t"
            "v_add_u32 v221, s74, v239\n\t"
            "v_mfma_f32_16x16x32_bf16 v[144:147], v[4:7], v[208:211], v[144:147]\n\t"
            "s_mov_b64 exec, s[54:55]\n\t"
            "ds_write_b128 v234, v[192:195] offset:12800\n\t"
            "ds_write_b128 v234, v[216:219] offset:12864\n\t"
            "ds_write_b64 v235, v[200:201] offset:12928\n\t"
            "ds_write_b64 v235, v[202:203] offset:12960\n\t"
            "s_mov_b64 exec, -1\n\t"
            "v_mfma_f32_16x16x32_bf16 v[148:151], v[28:31], v[208:211], 0\n\t"
            "v_add_u32 v235, s48, v248\n\t"
            "v_mfma_f32_16x16x32_bf16 v[148:151], v[24:27], v[204:207], v[148:151]\n\t"
            "ds_read_b128 v[228:231], v235 offset:18816\n\t"
            "v_mfma_f32_16x16x32_bf16 v[152:155], v[48:51], v[204:207], 0\n\t"
            "v_mfma_f32_16x16x32_bf16 v[152:155], v[52:55], v[208:211], v[152:155]\n\t"
            "v_mfma_f32_16x16x32_bf16 v[156:159], v[76:79], v[208:211], 0\n\t"
            "v_mfma_f32_16x16x32_bf16 v[156:159], v[72:75], v[204:207], v[156:159]\n\t"
            "v_mfma_f32_16x16x32_bf16 v[160:163], v[96:99], v[204:207], 0\n\t"
            "v_mfma_f32_16x16x32_bf16 v[160:163], v[100:103], v[208:211], v[160:163]\n\t"
            "v_mfma_f32_16x16x32_bf16 v[164:167], v[124:127], v[208:211], 0\n\t"
            "ds_read_b128 v[208:211], v233 offset:128\n\t"
            "s_waitcnt lgkmcnt(1)\n\t"
            "v_add_f32 v236, v228, v230\n\t"
            "v_mfma_f32_16x16x32_bf16 v[164:167], v[120:123], v[204:207], v[164:167]\n\t"
            "ds_read_b128 v[204:207], v232 offset:128\n\t"
            "v_mfma_f32_16x16x32_bf16 v[196:199], v[192:195], v[216:219], v[196:199]\n\t"
            "s_nop 7\n\t"
            "s_and_b32 exec_lo, s78, 0x11111111\n\t"
            "s_and_b32 exec_hi, s79, 0x11111111\n\t"
            "ds_write_b32 v220, v196 offset:12800\n\t"
            "s_and_b32 exec_lo, s78, 0x22222222\n\t"
            "s_and_b32 exec_hi, s79, 0x22222222\n\t"
            "ds_write_b32 v220, v197 offset:12800\n\t"
            "s_and_b32 exec_lo, s78, 0x44444444\n\t"
            "s_and_b32 exec_hi, s79, 0x44444444\n\t"
            "ds_write_b32 v220, v198 offset:12800\n\t"
            "s_and_b32 exec_lo, s78, 0x88888888\n\t"
            "s_and_b32 exec_hi, s79, 0x88888888\n\t"
            "ds_write_b32 v220, v199 offset:12800\n\t"
            "s_mov_b64 exec, s[78:79]\n\t"
            "ds_write_b32 v221, v237 offset:12800\n\t"
            "s_mov_b64 exec, -1\n\t"
            "s_waitcnt lgkmcnt(5)\n\t"
            "v_mfma_f32_16x16x32_bf16 v[144:147], v[8:11], v[204:207], v[144:147]\n\t"
            "v_add_f32 v237, v229, v231\n\t"
            "v_mfma_f32_16x16x32_bf16 v[144:147], v[12:15], v[208:211], v[144:147]\n\t"
            "ds_read_b128 v[228:231], v235 offset:19840\n\t"
            "v_mfma_f32_16x16x32_bf16 v[148:151], v[36:39], v[208:211], v[148:151]\n\t"
            "v_mfma_f32_16x16x32_bf16 v[148:151], v[32:35], v[204:207], v[148:151]\n\t"
            "v_mfma_f32_16x16x32_bf16 v[152:155], v[56:59], v[204:207], v[152:155]\n\t"
            "v_mfma_f32_16x16x32_bf16 v[152:155], v[60:63], v[208:211], v[152:155]\n\t"
            "v_mfma_f32_16x16x32_bf16 v[156:159], v[84:87], v[208:211], v[156:159]\n\t"
            "v_mfma_f32_16x16x32_bf16 v[156:159], v[80:83], v[204:207], v[156:159]\n\t"
            "v_mfma_f32_16x16x32_bf16 v[160:163], v[104:107], v[204:207], v[160:163]\n\t"
            "s_waitcnt lgkmcnt(0)\n\t"
            "v_add_f32 v228, v228, v230\n\t"
            "v_add_f32 v236, v236, v228\n\t"
            "v_mfma_f32_16x16x32_bf16 v[160:163], v[108:111], v[208:211], v[160:163]\n\t"
            "v_add_f32 v229, v229, v231\n\t"
            "v_mul_f32 v236, 0x3baaaaab, v236\n\t"
            "v_mfma_f32_16x16x32_bf16 v[164:167], v[132:135], v[208:211], v[164:167]\n\t"
            "ds_read_b128 v[208:211], v233 offset:6144\n\t"
            "v_add_f32 v237, v237, v229\n\t"
            "v_mfma_f32_16x16x32_bf16 v[164:167], v[128:131], v[204:207], v[164:167]\n\t"
            "ds_read_b128 v[204:207], v232 offset:6144\n\t"
            "v_mul_f32_e64 v236, v236, -v236\n\t"
            "v_mfma_f32_16x16x32_bf16 v[144:147], v[16:19], v[212:215], v[144:147]\n\t"
            "v_fmac_f32 v236, 0x3baaaaab, v237\n\t"
            "v_max_f32 v236, 0, v236\n\t"
            "v_mfma_f32_16x16x32_bf16 v[144:147], v[20:23], v[224:227], v[144:147]\n\t"
            "v_add_f32 v236, 0x3727c5ac, v236\n\t"
            "v_rsq_f32 v236, v236\n\t"
            "v_mfma_f32_16x16x32_bf16 v[148:151], v[44:47], v[224:227], v[148:151]\n\t"
            "v_mfma_f32_16x16x32_bf16 v[148:151], v[40:43], v[212:215], v[148:151]\n\t"
            "ds_write_b32 v249, v236\n\t"
            "v_mfma_f32_16x16x32_bf16 v[152:155], v[64:67], v[212:215], v[152:155]\n\t"
            "ds_read_b32 v236, v251\n\t"
            "ds_read_b32 v237, v251 offset:64\n\t"
            "v_mfma_f32_16x16x32_bf16 v[152:155], v[68:71], v[224:227], v[152:155]\n\t"
            "ds_read_b32 v238, v251 offset:128\n\t"
            "v_mfma_f32_16x16x32_bf16 v[156:159], v[92:95], v[224:227], v[156:159]\n\t"
            "v_mfma_f32_16x16x32_bf16 v[156:159], v[88:91], v[212:215], v[156:159]\n\t"
            "v_mfma_f32_16x16x32_bf16 v[160:163], v[112:115], v[212:215], v[160:163]\n\t"
            "v_mfma_f32_16x16x32_bf16 v[160:163], v[116:119], v[224:227], v[160:163]\n\t"
            "v_mfma_f32_16x16x32_bf16 v[164:167], v[140:143], v[224:227], v[164:167]\n\t"
            "ds_read_b128 v[224:227], v233 offset:6272\n\t"
            "v_mfma_f32_16x16x32_bf16 v[164:167], v[136:139], v[212:215], v[164:167]\n\t"
            "ds_read_b128 v[212:215], v232 offset:6272\n\t"
            "s_waitcnt lgkmcnt(4)\n\t"
            "v_mul_f32 v144, v236, v144\n\t"
            "v_mfma_f32_16x16x32_bf16 v[168:171], v[0:3], v[204:207], 0\n\t"
            "v_mul_f32 v145, v236, v145\n\t"
            "v_mul_f32 v152, v236, v152\n\t"
            "v_mfma_f32_16x16x32_bf16 v[168:171], v[4:7], v[208:211], v[168:171]\n\t"
            "v_mul_f32 v153, v236, v153\n\t"
            "v_mul_f32 v146, v236, v146\n\t"
            "v_mfma_f32_16x16x32_bf16 v[172:175], v[28:31], v[208:211], 0\n\t"
            "v_mul_f32 v147, v236, v147\n\t"
            "v_mul_f32 v154, v236, v154\n\t"
            "v_mfma_f32_16x16x32_bf16 v[172:175], v[24:27], v[204:207], v[172:175]\n\t"
            "v_mul_f32 v155, v236, v155\n\t"
            "v_cvt_pk_bf16_f32 v144, v144, v145\n\t"
            "v_mfma_f32_16x16x32_bf16 v[176:179], v[48:51], v[204:207], 0\n\t"
            "v_cvt_pk_bf16_f32 v152, v152, v153\n\t"
            "v_cvt_pk_bf16_f32 v156, v156, v157\n\t"
            "v_mfma_f32_16x16x32_bf16 v[176:179], v[52:55], v[208:211], v[176:179]\n\t"
            "v_mul_f32 v148, v236, v148\n\t"
            "v_mul_f32 v149, v236, v149\n\t"
            "v_mfma_f32_16x16x32_bf16 v[180:183], v[76:79], v[208:211], 0\n\t"
            "v_mul_f32 v150, v236, v150\n\t"
            "v_mul_f32 v151, v236, v151\n\t"
            "v_mfma_f32_16x16x32_bf16 v[180:183], v[72:75], v[204:207], v[180:183]\n\t"
            "v_cvt_pk_bf16_f32 v145, v146, v147\n\t"
            "v_cvt_pk_bf16_f32 v153, v154, v155\n\t"
            "v_mfma_f32_16x16x32_bf16 v[184:187], v[96:99], v[204:207], 0\n\t"
            "v_cvt_pk_bf16_f32 v157, v158, v159\n\t"
            "v_cvt_pk_bf16_f32 v146, v148, v149\n\t"
            "v_mfma_f32_16x16x32_bf16 v[184:187], v[100:103], v[208:211], v[184:187]\n\t"
            "v_cvt_pk_bf16_f32 v147, v150, v151\n\t"
            "v_cvt_pk_bf16_f32 v158, v160, v161\n\t"
            "v_mfma_f32_16x16x32_bf16 v[188:191], v[124:127], v[208:211], 0\n\t"
            "ds_read_b128 v[208:211], v233 offset:6400\n\t"
            "v_cvt_pk_bf16_f32 v159, v162, v163\n\t"
            "v_mfma_f32_16x16x32_bf16 v[188:191], v[120:123], v[204:207], v[188:191]\n\t"
            "ds_read_b128 v[204:207], v232 offset:6400\n\t"
            "v_cvt_pk_bf16_f32 v154, v164, v165\n\t"
            "s_waitcnt lgkmcnt(2)\n\t"
            "v_mfma_f32_16x16x32_bf16 v[168:171], v[8:11], v[212:215], v[168:171]\n\t"
            "v_cvt_pk_bf16_f32 v155, v166, v167\n\t"
            "v_mul_f32 v236, 0x3e553b94, v236\n\t"
            "v_mfma_f32_16x16x32_bf16 v[168:171], v[12:15], v[224:227], v[168:171]\n\t"
            "v_mfma_f32_16x16x32_bf16 v[172:175], v[36:39], v[224:227], v[172:175]\n\t"
            "v_mfma_f32_16x16x32_bf16 v[172:175], v[32:35], v[212:215], v[172:175]\n\t"
            "v_mfma_f32_16x16x32_bf16 v[176:179], v[56:59], v[212:215], v[176:179]\n\t"
            "v_mfma_f32_16x16x32_bf16 v[176:179], v[60:63], v[224:227], v[176:179]\n\t"
            "v_mfma_f32_16x16x32_bf16 v[180:183], v[84:87], v[224:227], v[180:183]\n\t"
            "v_mfma_f32_16x16x32_bf16 v[180:183], v[80:83], v[212:215], v[180:183]\n\t"
            "v_mfma_f32_16x16x32_bf16 v[184:187], v[104:107], v[212:215], v[184:187]\n\t"
            "v_mfma_f32_16x16x32_bf16 v[184:187], v[108:111], v[224:227], v[184:187]\n\t"
            "v_mfma_f32_16x16x32_bf16 v[188:191], v[132:135], v[224:227], v[188:191]\n\t"
            "ds_read_b128 v[224:227], v233 offset:12288\n\t"
            "v_mfma_f32_16x16x32_bf16 v[188:191], v[128:131], v[212:215], v[188:191]\n\t"
            "ds_read_b128 v[212:215], v232 offset:12288\n\t"
            "s_waitcnt lgkmcnt(2)\n\t"
            "v_mfma_f32_16x16x32_bf16 v[168:171], v[16:19], v[204:207], v[168:171]\n\t"
            "v_mfma_f32_16x16x32_bf16 v[168:171], v[20:23], v[208:211], v[168:171]\n\t"
            "v_mfma_f32_16x16x32_bf16 v[172:175], v[44:47], v[208:211], v[172:175]\n\t"
            "v_mfma_f32_16x16x32_bf16 v[172:175], v[40:43], v[204:207], v[172:175]\n\t"
            "v_mfma_f32_16x16x32_bf16 v[176:179], v[64:67], v[204:207], v[176:179]\n\t"
            "v_mfma_f32_16x16x32_bf16 v[176:179], v[68:71], v[208:211], v[176:179]\n\t"
            "v_mfma_f32_16x16x32_bf16 v[180:183], v[92:95], v[208:211], v[180:183]\n\t"
            "v_mfma_f32_16x16x32_bf16 v[180:183], v[88:91], v[204:207], v[180:183]\n\t"
            "v_mfma_f32_16x16x32_bf16 v[184:187], v[112:115], v[204:207], v[184:187]\n\t"
            "v_mfma_f32_16x16x32_bf16 v[184:187], v[116:119], v[208:211], v[184:187]\n\t"
            "v_mfma_f32_16x16x32_bf16 v[188:191], v[140:143], v[208:211], v[188:191]\n\t"
            "ds_read_b128 v[208:211], v233 offset:12416\n\t"
            "v_mfma_f32_16x16x32_bf16 v[188:191], v[136:139], v[204:207], v[188:191]\n\t"
            "ds_read_b128 v[204:207], v232 offset:12416\n\t"
            "s_waitcnt lgkmcnt(0)\n\t"
            "v_subrev_u32 v249, s75, v251\n\t"
            "v_add_u32 v249, 162816, v249\n\t"
            "s_mov_b32 s90, 2\n\t"
            "s_mov_b32 s81, 0\n\t"
            "s_mov_b32 s91, 0\n\t"
            "s_nop 7\n\t"
            ".p2align 6\n\t"
            "s_nop 0\n\t"
            "s_nop 0\n\t"
            "s_nop 0\n\t"
            ".Lwa_loop%=:\n\t"
            "s_waitcnt lgkmcnt(2)\n\t"
            "v_mfma_f32_16x16x32_bf16 v[192:195], v[0:3], v[212:215], 0\n\t"
            "s_bfe_u32 s49, s70, 0x30001\n\t"
            "s_lshl_b32 s49, s49, 4\n\t"
            "s_or_b32 s73, s77, s49\n\t"
            "v_mfma_f32_16x16x32_bf16 v[192:195], v[4:7], v[224:227], v[192:195]\n\t"
            "v_mad_u32_u24 v220, v246, s72, v253\n\t"
            "v_mad_u32_u24 v216, v246, s72, v252\n\t"
            "v_mfma_f32_16x16x32_bf16 v[148:151], v[28:31], v[224:227], 0\n\t"
            "ds_read_b64 v[222:223], v220 offset:128\n\t"
            "v_mov_b32_dpp v221, v220 quad_perm:[1,0,2,3] row_mask:0xf bank_mask:0xf\n\t"
            "v_mfma_f32_16x16x32_bf16 v[148:151], v[24:27], v[212:215], v[148:151]\n\t"
            "ds_read_b64 v[220:221], v221 offset:160\n\t"
            "v_mov_b32_dpp v217, v216 quad_perm:[1,0,2,3] row_mask:0xf bank_mask:0xf\n\t"
            "v_mfma_f32_16x16x32_bf16 v[196:199], v[48:51], v[212:215], 0\n\t"
            "v_mul_f32 v176, v237, v176\n\t"
            "v_mul_f32 v177, v237, v177\n\t"
            "v_mfma_f32_16x16x32_bf16 v[196:199], v[52:55], v[224:227], v[196:199]\n\t"
            "v_mul_f32 v178, v237, v178\n\t"
            "v_mul_f32 v179, v237, v179\n\t"
            "v_mfma_f32_16x16x32_bf16 v[200:203], v[76:79], v[224:227], 0\n\t"
            "v_cvt_pk_bf16_f32 v176, v176, v177\n\t"
            "v_mul_f32 v168, v237, v168\n\t"
            "v_mfma_f32_16x16x32_bf16 v[200:203], v[72:75], v[212:215], v[200:203]\n\t"
            "v_mul_f32 v169, v237, v169\n\t"
            "v_cvt_pk_bf16_f32 v177, v178, v179\n\t"
            "v_mfma_f32_16x16x32_bf16 v[160:163], v[96:99], v[212:215], 0\n\t"
            "v_mul_f32 v170, v237, v170\n\t"
            "v_mul_f32 v171, v237, v171\n\t"
            "v_mfma_f32_16x16x32_bf16 v[160:163], v[100:103], v[224:227], v[160:163]\n\t"
            "v_cvt_pk_bf16_f32 v168, v168, v169\n\t"
            "v_cvt_pk_bf16_f32 v180, v180, v181\n\t"
            "v_mfma_f32_16x16x32_bf16 v[164:167], v[124:127], v[224:227], 0\n\t"
            "ds_read_b128 v[224:227], v233 offset:12544\n\t"
            "v_cvt_pk_bf16_f32 v178, v188, v189\n\t"
            "v_mfma_f32_16x16x32_bf16 v[164:167], v[120:123], v[212:215], v[164:167]\n\t"
            "ds_read_b128 v[212:215], v232 offset:12544\n\t"
            "v_cvt_pk_bf16_f32 v179, v190, v191\n\t"
            "s_waitcnt lgkmcnt(4)\n\t"
            "v_mfma_f32_16x16x32_bf16 v[192:195], v[8:11], v[204:207], v[192:195]\n\t"
            "v_mul_f32 v172, v237, v172\n\t"
            "v_mul_f32 v173, v237, v173\n\t"
            "v_mfma_f32_16x16x32_bf16 v[192:195], v[12:15], v[208:211], v[192:195]\n\t"
            "v_mul_f32 v174, v237, v174\n\t"
            "v_mul_f32 v175, v237, v175\n\t"
            "v_mfma_f32_16x16x32_bf16 v[196:199], v[56:59], v[204:207], v[196:199]\n\t"
            "v_cvt_pk_bf16_f32 v169, v170, v171\n\t"
            "v_cvt_pk_bf16_f32 v181, v182, v183\n\t"
            "v_mfma_f32_16x16x32_bf16 v[196:199], v[60:63], v[208:211], v[196:199]\n\t"
            "v_cvt_pk_bf16_f32 v170, v172, v173\n\t"
            "v_cvt_pk_bf16_f32 v171, v174, v175\n\t"
            "v_mfma_f32_16x16x32_bf16 v[160:163], v[104:107], v[204:207], v[160:163]\n\t"
            "v_cvt_pk_bf16_f32 v182, v184, v185\n\t"
            "v_cvt_pk_bf16_f32 v183, v186, v187\n\t"
            "v_mfma_f32_16x16x32_bf16 v[160:163], v[108:111], v[208:211], v[160:163]\n\t"
            "v_mov_b32 v230, s76\n\t"
            "v_mov_b32 v239, s74\n\t"
            "v_mfma_f32_16x16x32_bf16 v[148:151], v[36:39], v[208:211], v[148:151]\n\t"
            "s_add_u32 s70, s70, 1\n\t"
            "s_add_u32 s72, s72, 800\n\t"
            "s_add_u32 s74, s74, 800\n\t"
            "s_add_u32 s76, s76, 800\n\t"
            "v_mfma_f32_16x16x32_bf16 v[148:151], v[32:35], v[204:207], v[148:151]\n\t"
            "s_add_u32 s77, s77, 384\n\t"
            "s_xor_b32 s48, s48, s47\n\t"
            "ds_read_b32 v230, v230\n\t"
            "v_mfma_f32_16x16x32_bf16 v[200:203], v[84:87], v[208:211], v[200:203]\n\t"
            "ds_read_b32 v239, v239\n\t"
            "v_mul_f32 v237, 0x3e553b94, v237\n\t"
            "v_mfma_f32_16x16x32_bf16 v[200:203], v[80:83], v[204:207], v[200:203]\n\t"
            "v_mfma_f32_16x16x32_bf16 v[164:167], v[132:135], v[208:211], v[164:167]\n\t"
            "v_mfma_f32_16x16x32_bf16 v[164:167], v[128:131], v[204:207], v[164:167]\n\t"
            "s_waitcnt lgkmcnt(2)\n\t"
            "v_mfma_f32_16x16x32_bf16 v[192:195], v[16:19], v[212:215], v[192:195]\n\t"
            "v_mfma_f32_16x16x32_bf16 v[192:195], v[20:23], v[224:227], v[192:195]\n\t"
            "v_mfma_f32_16x16x32_bf16 v[196:199], v[64:67], v[212:215], v[196:199]\n\t"
            "v_mfma_f32_16x16x32_bf16 v[196:199], v[68:71], v[224:227], v[196:199]\n\t"
            "v_mfma_f32_16x16x32_bf16 v[160:163], v[112:115], v[212:215], v[160:163]\n\t"
            "v_mfma_f32_16x16x32_bf16 v[160:163], v[116:119], v[224:227], v[160:163]\n\t"
            "v_mfma_f32_16x16x32_bf16 v[148:151], v[44:47], v[224:227], v[148:151]\n\t"
            "v_mfma_f32_16x16x32_bf16 v[148:151], v[40:43], v[212:215], v[148:151]\n\t"
            "s_nop 1\n\t"
            "v_mul_f32 v192, v238, v192\n\t"
            "v_mfma_f32_16x16x32_bf16 v[200:203], v[92:95], v[224:227], v[200:203]\n\t"
            "v_mul_f32 v193, v238, v193\n\t"
            "v_mul_f32 v194, v238, v194\n\t"
            "v_mfma_f32_16x16x32_bf16 v[200:203], v[88:91], v[212:215], v[200:203]\n\t"
            "v_mul_f32 v196, v238, v196\n\t"
            "v_mul_f32 v197, v238, v197\n\t"
            "v_mfma_f32_16x16x32_bf16 v[164:167], v[140:143], v[224:227], v[164:167]\n\t"
            "ds_read_b128 v[224:227], v217 offset:64\n\t"
            "ds_read_b128 v[216:219], v216\n\t"
            "v_mfma_f32_16x16x32_bf16 v[164:167], v[136:139], v[212:215], v[164:167]\n\t"
            "v_mul_f32 v198, v238, v198\n\t"
            "v_mul_f32 v199, v238, v199\n\t"
            "v_mfma_f32_16x16x32_bf16 v[172:175], v[220:223], v[152:155], 0\n\t"
            "s_setprio 0\n\t"
            "v_cvt_pk_bf16_f32 v196, v196, v197\n\t"
            "v_mul_f32 v195, v238, v195\n\t"
            "s_setprio 3\n\t"
            "v_mfma_f32_16x16x32_bf16 v[212:215], v[220:223], v[176:179], 0\n\t"
            "v_cvt_pk_bf16_f32 v192, v192, v193\n\t"
            "v_cvt_pk_bf16_f32 v197, v198, v199\n\t"
            "v_cvt_pk_bf16_f32 v200, v200, v201\n\t"
            "v_mul_f32 v148, v238, v148\n\t"
            "v_mfma_f32_16x16x16_bf16 v[184:187], v[152:153], v[178:179], 0\n\t"
            "v_mul_f32 v149, v238, v149\n\t"
            "v_mul_f32 v150, v238, v150\n\t"
            "v_mul_f32 v151, v238, v151\n\t"
            "v_mfma_f32_16x16x16_bf16 v[204:207], v[176:177], v[178:179], 0\n\t"
            "v_cvt_pk_bf16_f32 v193, v194, v195\n\t"
            "v_cvt_pk_bf16_f32 v201, v202, v203\n\t"
            "v_cvt_pk_bf16_f32 v194, v148, v149\n\t"
            "v_mfma_f32_16x16x16_bf16 v[188:191], v[196:197], v[178:179], 0\n\t"
            "v_cvt_pk_bf16_f32 v195, v150, v151\n\t"
            "v_cvt_pk_bf16_f32 v202, v160, v161\n\t"
            "v_cvt_pk_bf16_f32 v203, v162, v163\n\t"
            "s_waitcnt lgkmcnt(0)\n\t"
            "v_mfma_f32_16x16x32_bf16 v[172:175], v[216:219], v[156:159], v[172:175]\n\t"
            "v_cvt_pk_bf16_f32 v198, v164, v165\n\t"
            "v_cvt_pk_bf16_f32 v199, v166, v167\n\t"
            "v_mfma_f32_16x16x32_bf16 v[172:175], v[224:227], v[144:147], v[172:175]\n\t"
            "v_mul_f32 v238, 0x3e553b94, v238\n\t"
            "v_mfma_f32_16x16x16_bf16 v[148:151], v[152:153], v[154:155], 0\n\t"
            "v_mfma_f32_16x16x16_bf16 v[160:163], v[176:177], v[154:155], 0\n\t"
            "v_mfma_f32_16x16x16_bf16 v[164:167], v[196:197], v[154:155], 0\n\t"
            "v_mfma_f32_16x16x32_bf16 v[212:215], v[216:219], v[180:183], v[212:215]\n\t"
            "v_mfma_f32_16x16x32_bf16 v[212:215], v[224:227], v[168:171], v[212:215]\n\t"
            "s_nop 1\n\t"
            "v_mfma_f32_16x16x32_bf16 v[148:151], v[144:147], v[156:159], v[148:151]\n\t"
            "v_mfma_f32_16x16x32_bf16 v[184:187], v[144:147], v[180:183], v[184:187]\n\t"
            "v_mfma_f32_16x16x32_bf16 v[160:163], v[168:171], v[156:159], v[160:163]\n\t"
            "v_mfma_f32_16x16x32_bf16 v[204:207], v[168:171], v[180:183], v[204:207]\n\t"
            "v_mfma_f32_16x16x32_bf16 v[164:167], v[192:195], v[156:159], v[164:167]\n\t"
            "v_mfma_f32_16x16x32_bf16 v[188:191], v[192:195], v[180:183], v[188:191]\n\t"
            "v_cndmask_b32_e64 v235, v173, v213, s[60:61]\n\t"
            "v_mfma_f32_16x16x32_bf16 v[208:211], v[220:223], v[196:199], 0\n\t"
            "s_nop 0\n\t"
            "v_max3_f32 v228, v172, v148, v149\n\t"
            "v_max3_f32 v228, v228, v150, v151\n\t"
            "v_mfma_f32_16x16x32_bf16 v[208:211], v[216:219], v[200:203], v[208:211]\n\t"
            "v_max3_f32 v229, v212, v184, v185\n\t"
            "v_max3_f32 v229, v229, v186, v187\n\t"
            "v_mfma_f32_16x16x32_bf16 v[208:211], v[224:227], v[192:195], v[208:211]\n\t"
            "s_setprio 0\n\t"
            "v_max3_f32 v228, v228, v160, v161\n\t"
            "v_max3_f32 v229, v229, v204, v205\n\t"
            "v_max3_f32 v228, v228, v162, v163\n\t"
            "v_max3_f32 v229, v229, v206, v207\n\t"
            "v_max3_f32 v228, v228, v164, v165\n\t"
            "v_max3_f32 v229, v229, v188, v189\n\t"
            "v_max3_f32 v228, v228, v166, v167\n\t"
            "v_max3_f32 v229, v229, v190, v191\n\t"
            "v_max3_f32 v234, v173, v213, v209\n\t"
            "v_max_f32 v234, v234, v230\n\t"
            "v_permlane32_swap_b32 v228, v229\n\t"
            "v_max_f32 v228, v228, v229\n\t"
            "v_mov_b32 v231, v228\n\t"
            "v_max_f32_dpp v234, v234, v234 row_ror:8 row_mask:0xf bank_mask:0xf\n\t"
            "v_cndmask_b32_e64 v235, v235, v209, s[62:63]\n\t"
            "v_permlane16_swap_b32 v228, v231\n\t"
            "v_max_f32 v228, v228, v231\n\t"
            "v_mov_b32 v229, v228\n\t"
            "v_max_f32_dpp v234, v234, v234 row_ror:4 row_mask:0xf bank_mask:0xf\n\t"
            "v_cndmask_b32_e64 v235, v235, v230, s[64:65]\n\t"
            "v_permlane32_swap_b32 v228, v229\n\t"
            "v_mul_f32_e64 v228, -v228, v236\n\t"
            "v_mul_f32_e64 v229, -v229, v237\n\t"
            "v_fma_f32 v164, v164, v236, v228\n\t"
            "v_fma_f32 v165, v165, v236, v228\n\t"
            "v_fma_f32 v184, v184, v237, v229\n\t"
            "v_fma_f32 v185, v185, v237, v229\n\t"
            "v_exp_f32 v164, v164\n\t"
            "v_exp_f32 v165, v165\n\t"
            "v_fma_f32 v166, v166, v236, v228\n\t"
            "v_fma_f32 v167, v167, v236, v228\n\t"
            "v_fma_f32 v172, v172, v236, v228\n\t"
            "v_exp_f32 v184, v184\n\t"
            "v_exp_f32 v185, v185\n\t"
            "v_fma_f32 v186, v186, v237, v229\n\t"
            "v_fma_f32 v187, v187, v237, v229\n\t"
            "v_exp_f32 v166, v166\n\t"
            "v_exp_f32 v167, v167\n\t"
            "v_exp_f32 v172, v172\n\t"
            "v_exp_f32 v186, v186\n\t"
            "v_exp_f32 v187, v187\n\t"
            "v_fma_f32 v204, v204, v237, v229\n\t"
            "v_fma_f32 v205, v205, v237, v229\n\t"
            "v_fma_f32 v206, v206, v237, v229\n\t"
            "v_fma_f32 v207, v207, v237, v229\n\t"
            "v_exp_f32 v204, v204\n\t"
            "v_exp_f32 v205, v205\n\t"
            "v_exp_f32 v206, v206\n\t"
            "v_exp_f32 v207, v207\n\t"
            "v_cvt_pk_bf16_f32 v164, v164, v165\n\t"
            "v_cvt_pk_bf16_f32 v184, v184, v185\n\t"
            "v_cvt_pk_bf16_f32 v165, v166, v167\n\t"
            "v_cvt_pk_bf16_f32 v172, v172, 0\n\t"
            "v_cvt_pk_bf16_f32 v185, v186, v187\n\t"
            "v_max_f32_dpp v234, v234, v234 row_ror:2 row_mask:0xf bank_mask:0xf\n\t"
            "v_cndmask_b32_e64 v166, 0, v172, s[58:59]\n\t"
            "v_cvt_pk_bf16_f32 v186, v204, v205\n\t"
            "v_cvt_pk_bf16_f32 v187, v206, v207\n\t"
            "s_setprio 3\n\t"
            "v_mfma_f32_16x16x16_bf16 v[180:183], v[152:153], v[198:199], 0\n\t"
            "v_max_f32_dpp v234, v234, v234 row_ror:1 row_mask:0xf bank_mask:0xf\n\t"
            "v_sub_f32 v235, v235, v234\n\t"
            "v_mul_f32 v235, v239, v235\n\t"
            "v_mfma_f32_16x16x16_bf16 v[204:207], v[176:177], v[198:199], 0\n\t"
            "v_exp_f32 v235, v235\n\t"
            "v_fma_f32 v148, v148, v236, v228\n\t"
            "v_mfma_f32_16x16x16_bf16 v[172:175], v[196:197], v[198:199], 0\n\t"
            "v_cvt_pk_bf16_f32 v235, v235, 0\n\t"
            "v_cndmask_b32_e64 v235, 0, v235, s[66:67]\n\t"
            "v_fma_f32 v149, v149, v236, v228\n\t"
            "v_mfma_f32_16x16x32_bf16 v[204:207], v[168:171], v[200:203], v[204:207]\n\t"
            "ds_write_b16 v250, v235 offset:256\n\t"
            "v_mfma_f32_16x16x32_bf16 v[180:183], v[144:147], v[200:203], v[180:183]\n\t"
            "v_exp_f32 v148, v148\n\t"
            "v_mfma_f32_16x16x32_bf16 v[172:175], v[192:195], v[200:203], v[172:175]\n\t"
            "s_setprio 0\n\t"
            "v_exp_f32 v149, v149\n\t"
            "v_fma_f32 v150, v150, v236, v228\n\t"
            "v_fma_f32 v151, v151, v236, v228\n\t"
            "v_exp_f32 v150, v150\n\t"
            "v_exp_f32 v151, v151\n\t"
            "v_fma_f32 v160, v160, v236, v228\n\t"
            "v_max3_f32 v230, v208, v180, v181\n\t"
            "v_max3_f32 v230, v230, v182, v183\n\t"
            "v_max3_f32 v230, v230, v204, v205\n\t"
            "v_max3_f32 v230, v230, v206, v207\n\t"
            "v_max3_f32 v230, v230, v172, v173\n\t"
            "v_max3_f32 v230, v230, v174, v175\n\t"
            "v_mov_b32 v231, v230\n\t"
            "v_fma_f32 v161, v161, v236, v228\n\t"
            "v_fma_f32 v162, v162, v236, v228\n\t"
            "v_fma_f32 v163, v163, v236, v228\n\t"
            "v_exp_f32 v160, v160\n\t"
            "v_exp_f32 v161, v161\n\t"
            "v_exp_f32 v162, v162\n\t"
            "v_exp_f32 v163, v163\n\t"
            "v_permlane32_swap_b32 v230, v231\n\t"
            "v_cvt_pk_bf16_f32 v148, v148, v149\n\t"
            "v_cvt_pk_bf16_f32 v149, v150, v151\n\t"
            "v_fma_f32 v188, v188, v237, v229\n\t"
            "v_fma_f32 v189, v189, v237, v229\n\t"
            "v_max_f32 v230, v230, v231\n\t"
            "v_cvt_pk_bf16_f32 v150, v160, v161\n\t"
            "v_cvt_pk_bf16_f32 v151, v162, v163\n\t"
            "v_mov_b32 v167, 0\n\t"
            "v_exp_f32 v188, v188\n\t"
            "v_exp_f32 v189, v189\n\t"
            "v_fma_f32 v190, v190, v237, v229\n\t"
            "v_fma_f32 v191, v191, v237, v229\n\t"
            "v_fma_f32 v212, v212, v237, v229\n\t"
            "v_mov_b32 v231, v230\n\t"
            "v_exp_f32 v190, v190\n\t"
            "v_exp_f32 v191, v191\n\t"
            "v_exp_f32 v212, v212\n\t"
            "v_cvt_pk_bf16_f32 v188, v188, v189\n\t"
            "v_permlane16_swap_b32 v230, v231\n\t"
            "v_cvt_pk_bf16_f32 v189, v190, v191\n\t"
            "v_cvt_pk_bf16_f32 v212, v212, 0\n\t"
            "v_cndmask_b32_e64 v190, 0, v212, s[58:59]\n\t"
            "v_max_f32 v230, v230, v231\n\t"
            "v_mul_f32_e64 v230, -v230, v238\n\t"
            "v_fma_f32 v180, v180, v238, v230\n\t"
            "v_fma_f32 v181, v181, v238, v230\n\t"
            "v_fma_f32 v172, v172, v238, v230\n\t"
            "v_fma_f32 v173, v173, v238, v230\n\t"
            "v_exp_f32 v180, v180\n\t"
            "v_exp_f32 v181, v181\n\t"
            "v_fma_f32 v182, v182, v238, v230\n\t"
            "v_fma_f32 v183, v183, v238, v230\n\t"
            "v_exp_f32 v172, v172\n\t"
            "v_exp_f32 v173, v173\n\t"
            "v_fma_f32 v174, v174, v238, v230\n\t"
            "v_fma_f32 v175, v175, v238, v230\n\t"
            "v_fma_f32 v208, v208, v238, v230\n\t"
            "v_exp_f32 v182, v182\n\t"
            "v_exp_f32 v183, v183\n\t"
            "v_fma_f32 v204, v204, v238, v230\n\t"
            "v_fma_f32 v205, v205, v238, v230\n\t"
            "v_fma_f32 v206, v206, v238, v230\n\t"
            "v_fma_f32 v207, v207, v238, v230\n\t"
            "v_exp_f32 v174, v174\n\t"
            "v_exp_f32 v175, v175\n\t"
            "v_exp_f32 v208, v208\n\t"
            "v_exp_f32 v204, v204\n\t"
            "v_exp_f32 v205, v205\n\t"
            "v_exp_f32 v206, v206\n\t"
            "v_exp_f32 v207, v207\n\t"
            "v_cvt_pk_bf16_f32 v180, v180, v181\n\t"
            "v_cvt_pk_bf16_f32 v172, v172, v173\n\t"
            "v_cvt_pk_bf16_f32 v181, v182, v183\n\t"
            "v_cvt_pk_bf16_f32 v173, v174, v175\n\t"
            "v_cvt_pk_bf16_f32 v208, v208, 0\n\t"
            "v_cvt_pk_bf16_f32 v182, v204, v205\n\t"
            "v_cvt_pk_bf16_f32 v183, v206, v207\n\t"
            "v_cndmask_b32_e64 v174, 0, v208, s[58:59]\n\t"
            "v_mov_b32 v191, 0\n\t"
            "v_mov_b32 v175, 0\n\t"
            "s_cmp_ge_u32 s70, s80\n\t"
            "s_barrier\n\t"
            "s_setprio 2\n\t"
            "s_cbranch_scc1 .Lwa_h1%=\n\t"
            ".Lwa_r1%=:\n\t"
            "v_mfma_f32_16x16x32_bf16 v[200:203], v[240:243], v[148:151], 0\n\t"
            "ds_read_b64 v[176:177], v247\n\t"
            "ds_read_b64 v[196:197], v247 offset:1536\n\t"
            "v_mfma_f32_16x16x32_bf16 v[200:203], v[240:243], v[164:167], v[200:203]\n\t"
            "ds_read_b64 v[220:221], v247 offset:3072\n\t"
            "v_xor_b32 v234, s73, v254\n\t"
            "v_add_u32 v235, s45, v248\n\t"
            "v_mov_b32 v179, 0\n\t"
            "v_mov_b32 v199, 0\n\t"
            "v_mov_b32 v223, 0\n\t"
            "v_add_u32 v232, s48, v244\n\t"
            "v_xor_b32 v233, 64, v232\n\t"
            "v_rcp_f32 v201, v200\n\t"
            "v_xor_b32 v249, 0x100, v249\n\t"
            "ds_read_b128 v[168:171], v235\n\t"
            "ds_read_u16 v178, v234\n\t"
            "ds_read_b128 v[192:195], v235 offset:1536\n\t"
            "ds_read_u16 v198, v234 offset:128\n\t"
            "ds_read_b128 v[216:219], v235 offset:3072\n\t"
            "ds_read_u16 v222, v234 offset:256\n\t"
            "ds_read_b128 v[204:207], v232\n\t"
            "ds_read_b128 v[208:211], v233\n\t"
            "ds_read_b32 v236, v249\n\t"
            "ds_read_b32 v237, v249 offset:64\n\t"
            "ds_read_b32 v238, v249 offset:128\n\t"
            "v_mad_u32_u24 v234, v246, s75, v253\n\t"
            "s_waitcnt lgkmcnt(9)\n\t"
            "v_mfma_f32_16x16x32_bf16 v[224:227], v[168:171], v[148:151], 0\n\t"
            "v_mfma_f32_16x16x32_bf16 v[224:227], v[176:179], v[164:167], v[224:227]\n\t"
            "s_waitcnt lgkmcnt(7)\n\t"
            "v_mfma_f32_16x16x32_bf16 v[212:215], v[192:195], v[148:151], 0\n\t"
            "v_mfma_f32_16x16x32_bf16 v[212:215], v[196:199], v[164:167], v[212:215]\n\t"
            "s_waitcnt lgkmcnt(5)\n\t"
            "v_mfma_f32_16x16x32_bf16 v[148:151], v[216:219], v[148:151], 0\n\t"
            "v_mfma_f32_16x16x32_bf16 v[148:151], v[220:223], v[164:167], v[148:151]\n\t"
            "s_waitcnt lgkmcnt(3)\n\t"
            "v_mfma_f32_16x16x32_bf16 v[144:147], v[0:3], v[204:207], 0\n\t"
            "v_mfma_f32_16x16x32_bf16 v[144:147], v[4:7], v[208:211], v[144:147]\n\t"
            "v_mul_f32 v224, v201, v224\n\t"
            "v_mfma_f32_16x16x32_bf16 v[152:155], v[48:51], v[204:207], 0\n\t"
            "v_mul_f32 v225, v201, v225\n\t"
            "v_mul_f32 v226, v201, v226\n\t"
            "v_mfma_f32_16x16x32_bf16 v[152:155], v[52:55], v[208:211], v[152:155]\n\t"
            "v_mul_f32 v212, v201, v212\n\t"
            "v_mul_f32 v213, v201, v213\n\t"
            "v_mfma_f32_16x16x32_bf16 v[156:159], v[76:79], v[208:211], 0\n\t"
            "v_mul_f32 v227, v201, v227\n\t"
            "v_cvt_pk_bf16_f32 v224, v224, v225\n\t"
            "v_mfma_f32_16x16x32_bf16 v[156:159], v[72:75], v[204:207], v[156:159]\n\t"
            "v_mul_f32 v214, v201, v214\n\t"
            "v_mul_f32 v215, v201, v215\n\t"
            "v_mfma_f32_16x16x32_bf16 v[160:163], v[96:99], v[204:207], 0\n\t"
            "v_cvt_pk_bf16_f32 v212, v212, v213\n\t"
            "v_cvt_pk_bf16_f32 v225, v226, v227\n\t"
            "v_mfma_f32_16x16x32_bf16 v[160:163], v[100:103], v[208:211], v[160:163]\n\t"
            "v_cvt_pk_bf16_f32 v213, v214, v215\n\t"
            "v_mul_f32 v148, v201, v148\n\t"
            "v_mfma_f32_16x16x32_bf16 v[164:167], v[124:127], v[208:211], 0\n\t"
            "v_mul_f32 v149, v201, v149\n\t"
            "v_mul_f32 v150, v201, v150\n\t"
            "v_mfma_f32_16x16x32_bf16 v[164:167], v[120:123], v[204:207], v[164:167]\n\t"
            "ds_write_b64 v245, v[224:225]\n\t"
            "v_mfma_f32_16x16x32_bf16 v[224:227], v[168:171], v[184:187], 0\n\t"
            "v_mul_f32 v151, v201, v151\n\t"
            "v_cvt_pk_bf16_f32 v148, v148, v149\n\t"
            "v_mfma_f32_16x16x32_bf16 v[224:227], v[176:179], v[188:191], v[224:227]\n\t"
            "ds_write_b64 v245, v[212:213] offset:128\n\t"
            "v_mfma_f32_16x16x32_bf16 v[200:203], v[240:243], v[184:187], 0\n\t"
            "v_cvt_pk_bf16_f32 v149, v150, v151\n\t"
            "v_mfma_f32_16x16x32_bf16 v[200:203], v[240:243], v[188:191], v[200:203]\n\t"
            "ds_write_b64 v245, v[148:149] offset:256\n\t"
            "v_mfma_f32_16x16x32_bf16 v[212:215], v[192:195], v[184:187], 0\n\t"
            "v_mfma_f32_16x16x32_bf16 v[212:215], v[196:199], v[188:191], v[212:215]\n\t"
            "v_mfma_f32_16x16x32_bf16 v[184:187], v[216:219], v[184:187], 0\n\t"
            "v_mfma_f32_16x16x32_bf16 v[184:187], v[220:223], v[188:191], v[184:187]\n\t"
            "v_mfma_f32_16x16x32_bf16 v[148:151], v[28:31], v[208:211], 0\n\t"
            "ds_read_b128 v[208:211], v233 offset:128\n\t"
            "v_mfma_f32_16x16x32_bf16 v[148:151], v[24:27], v[204:207], v[148:151]\n\t"
            "ds_read_b128 v[204:207], v232 offset:128\n\t"
            "v_rcp_f32 v201, v200\n\t"
            "v_mfma_f32_16x16x32_bf16 v[188:191], v[192:195], v[180:183], 0\n\t"
            "v_mul_f32 v224, v201, v224\n\t"
            "v_mul_f32 v225, v201, v225\n\t"
            "v_mfma_f32_16x16x32_bf16 v[188:191], v[196:199], v[172:175], v[188:191]\n\t"
            "v_mul_f32 v226, v201, v226\n\t"
            "v_mul_f32 v227, v201, v227\n\t"
            "v_mul_f32 v212, v201, v212\n\t"
            "v_mul_f32 v213, v201, v213\n\t"
            "v_mul_f32 v214, v201, v214\n\t"
            "v_mul_f32 v215, v201, v215\n\t"
            "v_mul_f32 v184, v201, v184\n\t"
            "v_mul_f32 v185, v201, v185\n\t"
            "v_mul_f32 v186, v201, v186\n\t"
            "v_mul_f32 v187, v201, v187\n\t"
            "v_mfma_f32_16x16x32_bf16 v[200:203], v[240:243], v[180:183], 0\n\t"
            "v_cvt_pk_bf16_f32 v224, v224, v225\n\t"
            "v_cvt_pk_bf16_f32 v225, v226, v227\n\t"
            "v_mfma_f32_16x16x32_bf16 v[200:203], v[240:243], v[172:175], v[200:203]\n\t"
            "ds_write_b64 v245, v[224:225] offset:6144\n\t"
            "v_mfma_f32_16x16x32_bf16 v[224:227], v[168:171], v[180:183], 0\n\t"
            "v_cvt_pk_bf16_f32 v184, v184, v185\n\t"
            "v_cvt_pk_bf16_f32 v185, v186, v187\n\t"
            "v_mfma_f32_16x16x32_bf16 v[224:227], v[176:179], v[172:175], v[224:227]\n\t"
            "ds_read_b64 v[186:187], v234 offset:288\n\t"
            "ds_write_b64 v245, v[184:185] offset:6400\n\t"
            "s_waitcnt lgkmcnt(3)\n\t"
            "v_mfma_f32_16x16x32_bf16 v[144:147], v[8:11], v[204:207], v[144:147]\n\t"
            "ds_read_b64 v[184:185], v234 offset:256\n\t"
            "v_rcp_f32 v201, v200\n\t"
            "v_mfma_f32_16x16x32_bf16 v[144:147], v[12:15], v[208:211], v[144:147]\n\t"
            "v_mul_f32 v188, v201, v188\n\t"
            "v_mul_f32 v189, v201, v189\n\t"
            "v_mfma_f32_16x16x32_bf16 v[148:151], v[36:39], v[208:211], v[148:151]\n\t"
            "v_mul_f32 v190, v201, v190\n\t"
            "v_mul_f32 v191, v201, v191\n\t"
            "v_mfma_f32_16x16x32_bf16 v[148:151], v[32:35], v[204:207], v[148:151]\n\t"
            "v_cvt_pk_bf16_f32 v188, v188, v189\n\t"
            "v_cvt_pk_bf16_f32 v189, v190, v191\n\t"
            "v_mfma_f32_16x16x32_bf16 v[152:155], v[56:59], v[204:207], v[152:155]\n\t"
            "ds_read_b64 v[190:191], v234 offset:352\n\t"
            "v_mul_f32 v224, v201, v224\n\t"
            "v_mfma_f32_16x16x32_bf16 v[152:155], v[60:63], v[208:211], v[152:155]\n\t"
            "ds_write_b64 v245, v[188:189] offset:12416\n\t"
            "v_mfma_f32_16x16x32_bf16 v[156:159], v[84:87], v[208:211], v[156:159]\n\t"
            "ds_read_b64 v[188:189], v234 offset:320\n\t"
            "v_mul_f32 v225, v201, v225\n\t"
            "v_mfma_f32_16x16x32_bf16 v[156:159], v[80:83], v[204:207], v[156:159]\n\t"
            "v_cvt_pk_bf16_f32 v212, v212, v213\n\t"
            "v_mul_f32 v226, v201, v226\n\t"
            "v_mfma_f32_16x16x32_bf16 v[160:163], v[104:107], v[204:207], v[160:163]\n\t"
            "v_mul_f32 v227, v201, v227\n\t"
            "v_cvt_pk_bf16_f32 v224, v224, v225\n\t"
            "v_mfma_f32_16x16x32_bf16 v[160:163], v[108:111], v[208:211], v[160:163]\n\t"
            "v_cvt_pk_bf16_f32 v213, v214, v215\n\t"
            "v_cvt_pk_bf16_f32 v225, v226, v227\n\t"
            "v_mfma_f32_16x16x32_bf16 v[164:167], v[132:135], v[208:211], v[164:167]\n\t"
            "ds_read_b128 v[208:211], v233 offset:6144\n\t"
            "v_mfma_f32_16x16x32_bf16 v[164:167], v[128:131], v[204:207], v[164:167]\n\t"
            "ds_read_b128 v[204:207], v232 offset:6144\n\t"
            "ds_write_b64 v245, v[212:213] offset:6272\n\t"
            "v_mfma_f32_16x16x32_bf16 v[180:183], v[216:219], v[180:183], 0\n\t"
            "ds_read_b128 v[212:215], v232 offset:256\n\t"
            "v_mfma_f32_16x16x32_bf16 v[180:183], v[220:223], v[172:175], v[180:183]\n\t"
            "ds_write_b64 v245, v[224:225] offset:12288\n\t"
            "s_waitcnt lgkmcnt(5)\n\t"
            "v_mfma_f32_16x16x32_bf16 v[168:171], v[184:187], v[168:171], 0\n\t"
            "ds_read_b128 v[224:227], v233 offset:256\n\t"
            "v_mfma_f32_16x16x32_bf16 v[168:171], v[188:191], v[176:179], v[168:171]\n\t"
            "v_mfma_f32_16x16x32_bf16 v[192:195], v[184:187], v[192:195], 0\n\t"
            "v_mfma_f32_16x16x32_bf16 v[192:195], v[188:191], v[196:199], v[192:195]\n\t"
            "s_nop 0\n\t"
            "v_mul_f32 v180, v201, v180\n\t"
            "v_mfma_f32_16x16x32_bf16 v[216:219], v[184:187], v[216:219], 0\n\t"
            "v_mul_f32 v181, v201, v181\n\t"
            "v_mul_f32 v182, v201, v182\n\t"
            "v_mfma_f32_16x16x32_bf16 v[216:219], v[188:191], v[220:223], v[216:219]\n\t"
            "v_mul_f32 v183, v201, v183\n\t"
            "v_cvt_pk_bf16_f32 v180, v180, v181\n\t"
            "v_mfma_f32_16x16x32_bf16 v[200:203], v[240:243], v[184:187], 0\n\t"
            "v_cvt_pk_bf16_f32 v181, v182, v183\n\t"
            "v_mfma_f32_16x16x32_bf16 v[200:203], v[240:243], v[188:191], v[200:203]\n\t"
            "ds_write_b64 v245, v[180:181] offset:12544\n\t"
            "s_waitcnt lgkmcnt(1)\n\t"
            "v_mfma_f32_16x16x32_bf16 v[144:147], v[16:19], v[212:215], v[144:147]\n\t"
            "v_mfma_f32_16x16x32_bf16 v[144:147], v[20:23], v[224:227], v[144:147]\n\t"
            "v_mfma_f32_16x16x32_bf16 v[148:151], v[44:47], v[224:227], v[148:151]\n\t"
            "v_mfma_f32_16x16x32_bf16 v[148:151], v[40:43], v[212:215], v[148:151]\n\t"
            "v_mfma_f32_16x16x32_bf16 v[152:155], v[64:67], v[212:215], v[152:155]\n\t"
            "s_nop 0\n\t"
            "v_cndmask_b32_e64 v203, v168, v192, s[60:61]\n\t"
            "v_mfma_f32_16x16x32_bf16 v[152:155], v[68:71], v[224:227], v[152:155]\n\t"
            "v_rcp_f32 v201, v200\n\t"
            "v_mfma_f32_16x16x32_bf16 v[156:159], v[92:95], v[224:227], v[156:159]\n\t"
            "v_cndmask_b32_e64 v203, v203, v216, s[62:63]\n\t"
            "v_xad_u32 v202, s73, v254, v255\n\t"
            "v_mfma_f32_16x16x32_bf16 v[156:159], v[88:91], v[212:215], v[156:159]\n\t"
            "v_mul_f32_dpp v203, v201, v203 quad_perm:[0,0,0,0] row_mask:0xf bank_mask:0xf\n\t"
            "v_cvt_pk_bf16_f32 v203, v203, 0\n\t"
            "v_mfma_f32_16x16x32_bf16 v[160:163], v[112:115], v[212:215], v[160:163]\n\t"
            "v_mul_f32 v144, v236, v144\n\t"
            "v_mul_f32 v145, v236, v145\n\t"
            "v_mfma_f32_16x16x32_bf16 v[160:163], v[116:119], v[224:227], v[160:163]\n\t"
            "s_mov_b64 exec, s[68:69]\n\t"
            "ds_write_b16 v202, v203\n\t"
            "s_mov_b64 exec, 1\n\t"
            "ds_add_u32 v253, v246 offset:7856\n\t"
            "s_mov_b64 exec, -1\n\t"
            "v_mfma_f32_16x16x32_bf16 v[164:167], v[140:143], v[224:227], v[164:167]\n\t"
            "ds_read_b128 v[224:227], v233 offset:6272\n\t"
            "v_mul_f32 v152, v236, v152\n\t"
            "v_mfma_f32_16x16x32_bf16 v[164:167], v[136:139], v[212:215], v[164:167]\n\t"
            "ds_read_b128 v[212:215], v232 offset:6272\n\t"
            "v_mul_f32 v153, v236, v153\n\t"
            "v_mfma_f32_16x16x32_bf16 v[168:171], v[0:3], v[204:207], 0\n\t"
            "v_mul_f32 v146, v236, v146\n\t"
            "v_mul_f32 v147, v236, v147\n\t"
            "v_mfma_f32_16x16x32_bf16 v[168:171], v[4:7], v[208:211], v[168:171]\n\t"
            "v_mul_f32 v154, v236, v154\n\t"
            "v_mul_f32 v155, v236, v155\n\t"
            "v_mfma_f32_16x16x32_bf16 v[172:175], v[28:31], v[208:211], 0\n\t"
            "v_cvt_pk_bf16_f32 v144, v144, v145\n\t"
            "v_cvt_pk_bf16_f32 v152, v152, v153\n\t"
            "v_mfma_f32_16x16x32_bf16 v[172:175], v[24:27], v[204:207], v[172:175]\n\t"
            "v_cvt_pk_bf16_f32 v156, v156, v157\n\t"
            "v_mul_f32 v148, v236, v148\n\t"
            "v_mfma_f32_16x16x32_bf16 v[176:179], v[48:51], v[204:207], 0\n\t"
            "v_mul_f32 v149, v236, v149\n\t"
            "v_mul_f32 v150, v236, v150\n\t"
            "v_mfma_f32_16x16x32_bf16 v[176:179], v[52:55], v[208:211], v[176:179]\n\t"
            "v_mul_f32 v151, v236, v151\n\t"
            "v_cvt_pk_bf16_f32 v145, v146, v147\n\t"
            "v_mfma_f32_16x16x32_bf16 v[180:183], v[76:79], v[208:211], 0\n\t"
            "v_cvt_pk_bf16_f32 v153, v154, v155\n\t"
            "v_cvt_pk_bf16_f32 v157, v158, v159\n\t"
            "v_mfma_f32_16x16x32_bf16 v[180:183], v[72:75], v[204:207], v[180:183]\n\t"
            "v_cvt_pk_bf16_f32 v146, v148, v149\n\t"
            "v_cvt_pk_bf16_f32 v147, v150, v151\n\t"
            "v_mfma_f32_16x16x32_bf16 v[184:187], v[96:99], v[204:207], 0\n\t"
            "v_cvt_pk_bf16_f32 v158, v160, v161\n\t"
            "v_cvt_pk_bf16_f32 v159, v162, v163\n\t"
            "v_mfma_f32_16x16x32_bf16 v[184:187], v[100:103], v[208:211], v[184:187]\n\t"
            "v_cvt_pk_bf16_f32 v154, v164, v165\n\t"
            "v_cvt_pk_bf16_f32 v155, v166, v167\n\t"
            "v_mfma_f32_16x16x32_bf16 v[188:191], v[124:127], v[208:211], 0\n\t"
            "ds_read_b128 v[208:211], v233 offset:6400\n\t"
            "v_mul_f32 v236, 0x3e553b94, v236\n\t"
            "v_mfma_f32_16x16x32_bf16 v[188:191], v[120:123], v[204:207], v[188:191]\n\t"
            "ds_read_b128 v[204:207], v232 offset:6400\n\t"
            "s_waitcnt lgkmcnt(2)\n\t"
            "v_mfma_f32_16x16x32_bf16 v[168:171], v[8:11], v[212:215], v[168:171]\n\t"
            "v_mfma_f32_16x16x32_bf16 v[168:171], v[12:15], v[224:227], v[168:171]\n\t"
            "v_mfma_f32_16x16x32_bf16 v[172:175], v[36:39], v[224:227], v[172:175]\n\t"
            "v_mfma_f32_16x16x32_bf16 v[172:175], v[32:35], v[212:215], v[172:175]\n\t"
            "v_mfma_f32_16x16x32_bf16 v[176:179], v[56:59], v[212:215], v[176:179]\n\t"
            "v_mfma_f32_16x16x32_bf16 v[176:179], v[60:63], v[224:227], v[176:179]\n\t"
            "v_mfma_f32_16x16x32_bf16 v[180:183], v[84:87], v[224:227], v[180:183]\n\t"
            "v_mfma_f32_16x16x32_bf16 v[180:183], v[80:83], v[212:215], v[180:183]\n\t"
            "v_mfma_f32_16x16x32_bf16 v[184:187], v[104:107], v[212:215], v[184:187]\n\t"
            "v_mfma_f32_16x16x32_bf16 v[184:187], v[108:111], v[224:227], v[184:187]\n\t"
            "v_mfma_f32_16x16x32_bf16 v[188:191], v[132:135], v[224:227], v[188:191]\n\t"
            "ds_read_b128 v[224:227], v233 offset:12288\n\t"
            "v_mfma_f32_16x16x32_bf16 v[188:191], v[128:131], v[212:215], v[188:191]\n\t"
            "ds_read_b128 v[212:215], v232 offset:12288\n\t"
            "s_waitcnt lgkmcnt(2)\n\t"
            "v_mfma_f32_16x16x32_bf16 v[168:171], v[16:19], v[204:207], v[168:171]\n\t"
            "v_mfma_f32_16x16x32_bf16 v[168:171], v[20:23], v[208:211], v[168:171]\n\t"
            "v_mfma_f32_16x16x32_bf16 v[172:175], v[44:47], v[208:211], v[172:175]\n\t"
            "v_mfma_f32_16x16x32_bf16 v[172:175], v[40:43], v[204:207], v[172:175]\n\t"
            "v_mfma_f32_16x16x32_bf16 v[176:179], v[64:67], v[204:207], v[176:179]\n\t"
            "v_mfma_f32_16x16x32_bf16 v[176:179], v[68:71], v[208:211], v[176:179]\n\t"
            "v_mfma_f32_16x16x32_bf16 v[180:183], v[92:95], v[208:211], v[180:183]\n\t"
            "v_mfma_f32_16x16x32_bf16 v[180:183], v[88:91], v[204:207], v[180:183]\n\t"
            "v_mfma_f32_16x16x32_bf16 v[184:187], v[112:115], v[204:207], v[184:187]\n\t"
            "v_mfma_f32_16x16x32_bf16 v[184:187], v[116:119], v[208:211], v[184:187]\n\t"
            "v_mfma_f32_16x16x32_bf16 v[188:191], v[140:143], v[208:211], v[188:191]\n\t"
            "ds_read_b128 v[208:211], v233 offset:12416\n\t"
            "v_mfma_f32_16x16x32_bf16 v[188:191], v[136:139], v[204:207], v[188:191]\n\t"
            "ds_read_b128 v[204:207], v232 offset:12416\n\t"
            "s_cmp_eq_u32 s91, 0\n\t"
            "s_cbranch_scc1 .Lwa_loop%=\n\t"
            "s_mov_b32 s91, 0\n\t"
            "s_waitcnt vmcnt(0)\n\t"
            "s_cmp_eq_u32 s81, 0\n\t"
            "s_cbranch_scc1 .Lwa_h6%=\n\t"
            "s_mov_b32 s89, 0x10000\n\t"
            ".Lwa_h4%=:\n\t"
            "v_readfirstlane_b32 s49, v228\n\t"
            "v_readfirstlane_b32 vcc_lo, v229\n\t"
            "s_xor_b32 s49, s49, s82\n\t"
            "s_xor_b32 vcc_lo, vcc_lo, s83\n\t"
            "s_or_b32 s49, s49, vcc_lo\n\t"
            "s_cbranch_scc0 .Lwa_h5%=\n\t"
            "s_sub_u32 s89, s89, 1\n\t"
            "s_cbranch_scc1 .Lwa_h5%=\n\t"
            "s_sleep 2\n\t"
            "buffer_load_dwordx3 v[228:230], off, s[84:87], s81 offset:1216 sc1\n\t"
            "s_waitcnt vmcnt(0)\n\t"
            "s_branch .Lwa_h4%=\n\t"
            ".Lwa_h5%=:\n\t"
            "s_lshl_b32 s49, s70, 2\n\t"
            "s_add_u32 s49, s49, 163520\n\t"
            "v_mov_b32 v228, s49\n\t"
            "s_mul_i32 s49, s70, 800\n\t"
            "s_add_u32 m0, s49, 113760\n\t"
            "ds_write_b32 v228, v230 offset:12\n\t"
            "s_mov_b32 exec_hi, 0x3ffff\n\t"
            "buffer_load_dwordx4 v248, s[84:87], s81 offen sc1 lds\n\t"
            "s_mul_i32 s49, s70, 384\n\t"
            "s_add_u32 m0, s49, 130176\n\t"
            "s_add_u32 s81, s81, 800\n\t"
            "s_mov_b64 exec, 0xffffff\n\t"
            "buffer_load_dwordx4 v248, s[84:87], s81 offen sc1 lds\n\t"
            "s_mov_b64 exec, -1\n\t"
            "s_mov_b32 s81, 0\n\t"
            "s_waitcnt lgkmcnt(0)\n\t"
            ".Lwa_h6%=:\n\t"
            "s_cmp_eq_u32 s90, 0\n\t"
            "s_cbranch_scc1 .Lwa_loop%=\n\t"
            "v_readfirstlane_b32 s49, v231\n\t"
            "s_sub_u32 s90, s90, 1\n\t"
            "s_cmp_lt_u32 s49, 209\n\t"
            "s_cselect_b32 s90, s90, 0\n\t"
            "s_cbranch_scc0 .Lwa_loop%=\n\t"
            "s_mul_i32 s81, s49, 1280\n\t"
            "s_add_u32 s81, s81, 303104\n\t"
            "s_branch .Lwa_loop%=\n\t"
            ".Lwa_h1%=:\n\t"
            "s_cmp_lt_u32 s70, 20\n\t"
            "s_cbranch_scc1 .Lwa_h2%=\n\t"
            "s_lshl_b32 s49, s70, 2\n\t"
            "s_add_u32 s49, s49, 163520\n\t"
            "v_mov_b32 v228, s49\n\t"
            "ds_read_b32 v228, v228\n\t"
            "s_waitcnt lgkmcnt(0)\n\t"
            "v_readfirstlane_b32 s49, v228\n\t"
            "s_cmp_eq_u32 s49, 0x7ffffff\n\t"
            "s_cbranch_scc1 .Lwa_last%=\n\t"
            "s_branch .Lwa_r1%=\n\t"
            ".Lwa_h2%=:\n\t"
            "s_waitcnt vmcnt(0)\n\t"
            "s_or_b32 s91, s81, s90\n\t"
            "s_cmp_eq_u32 s81, 0\n\t"
            "s_cbranch_scc1 .Lwa_h3%=\n\t"
            "buffer_load_dwordx3 v[228:230], off, s[84:87], s81 offset:1216 sc1\n\t"
            ".Lwa_h3%=:\n\t"
            "s_cmp_eq_u32 s90, 0\n\t"
            "s_cbranch_scc1 .Lwa_r1%=\n\t"
            "v_mov_b32 v231, 1\n\t"
            "s_mov_b32 s49, 301184\n\t"
            "s_mov_b64 exec, 1\n\t"
            "buffer_atomic_add v231, off, s[84:87], s49 sc0\n\t"
            "s_mov_b64 exec, -1\n\t"
            "s_branch .Lwa_r1%=\n\t"
            ".Lwa_last%=:\n\t"
            "s_setprio 2\n\t"
            "s_waitcnt lgkmcnt(0)\n\t"
            "s_nop 7\n\t"
            "v_mfma_f32_16x16x32_bf16 v[156:159], v[240:243], v[184:187], 0\n\t"
            "ds_read_b64 v[176:177], v247\n\t"
            "ds_read_b64 v[196:197], v247 offset:1536\n\t"
            "v_mfma_f32_16x16x32_bf16 v[156:159], v[240:243], v[188:191], v[156:159]\n\t"
            "ds_read_b64 v[220:221], v247 offset:3072\n\t"
            "v_xor_b32 v234, s73, v254\n\t"
            "v_mfma_f32_16x16x32_bf16 v[228:231], v[240:243], v[148:151], 0\n\t"
            "ds_read_u16 v178, v234\n\t"
            "ds_read_u16 v198, v234 offset:128\n\t"
            "v_mfma_f32_16x16x32_bf16 v[228:231], v[240:243], v[164:167], v[228:231]\n\t"
            "ds_read_u16 v222, v234 offset:256\n\t"
            "v_add_u32 v235, s45, v248\n\t"
            "v_mfma_f32_16x16x32_bf16 v[208:211], v[240:243], v[180:183], 0\n\t"
            "ds_read_b128 v[168:171], v235\n\t"
            "ds_read_b128 v[192:195], v235 offset:1536\n\t"
            "v_mfma_f32_16x16x32_bf16 v[208:211], v[240:243], v[172:175], v[208:211]\n\t"
            "ds_read_b128 v[216:219], v235 offset:3072\n\t"
            "v_mov_b32 v179, 0\n\t"
            "v_mov_b32 v199, 0\n\t"
            "v_mov_b32 v223, 0\n\t"
            "v_rcp_f32 v157, v156\n\t"
            "v_mad_u32_u24 v234, v246, s75, v253\n\t"
            "v_rcp_f32 v229, v228\n\t"
            "s_nop 0\n\t"
            "v_rcp_f32 v209, v208\n\t"
            "s_waitcnt lgkmcnt(2)\n\t"
            "v_mfma_f32_16x16x32_bf16 v[144:147], v[168:171], v[184:187], 0\n\t"
            "v_mfma_f32_16x16x32_bf16 v[144:147], v[176:179], v[188:191], v[144:147]\n\t"
            "s_waitcnt lgkmcnt(1)\n\t"
            "v_mfma_f32_16x16x32_bf16 v[152:155], v[192:195], v[184:187], 0\n\t"
            "v_mfma_f32_16x16x32_bf16 v[152:155], v[196:199], v[188:191], v[152:155]\n\t"
            "s_waitcnt lgkmcnt(0)\n\t"
            "v_mfma_f32_16x16x32_bf16 v[184:187], v[216:219], v[184:187], 0\n\t"
            "v_mfma_f32_16x16x32_bf16 v[184:187], v[220:223], v[188:191], v[184:187]\n\t"
            "ds_read_b64 v[188:189], v234 offset:320\n\t"
            "ds_read_b64 v[190:191], v234 offset:352\n\t"
            "v_mfma_f32_16x16x32_bf16 v[224:227], v[168:171], v[148:151], 0\n\t"
            "v_mul_f32 v144, v157, v144\n\t"
            "v_mul_f32 v145, v157, v145\n\t"
            "v_mfma_f32_16x16x32_bf16 v[224:227], v[176:179], v[164:167], v[224:227]\n\t"
            "v_mul_f32 v152, v157, v152\n\t"
            "v_mul_f32 v153, v157, v153\n\t"
            "v_mfma_f32_16x16x32_bf16 v[212:215], v[192:195], v[148:151], 0\n\t"
            "v_mul_f32 v184, v157, v184\n\t"
            "v_mul_f32 v185, v157, v185\n\t"
            "v_mfma_f32_16x16x32_bf16 v[212:215], v[196:199], v[164:167], v[212:215]\n\t"
            "v_mul_f32 v186, v157, v186\n\t"
            "v_mul_f32 v187, v157, v187\n\t"
            "v_mfma_f32_16x16x32_bf16 v[160:163], v[168:171], v[180:183], 0\n\t"
            "v_cvt_pk_bf16_f32 v184, v184, v185\n\t"
            "v_cvt_pk_bf16_f32 v185, v186, v187\n\t"
            "v_mfma_f32_16x16x32_bf16 v[160:163], v[176:179], v[172:175], v[160:163]\n\t"
            "ds_read_b64 v[186:187], v234 offset:288\n\t"
            "ds_write_b64 v245, v[184:185] offset:6400\n\t"
            "v_mfma_f32_16x16x32_bf16 v[204:207], v[192:195], v[180:183], 0\n\t"
            "ds_read_b64 v[184:185], v234 offset:256\n\t"
            "v_mul_f32 v224, v229, v224\n\t"
            "v_mfma_f32_16x16x32_bf16 v[204:207], v[196:199], v[172:175], v[204:207]\n\t"
            "v_mul_f32 v225, v229, v225\n\t"
            "v_mul_f32 v212, v229, v212\n\t"
            "v_mfma_f32_16x16x32_bf16 v[148:151], v[216:219], v[148:151], 0\n\t"
            "v_mul_f32 v213, v229, v213\n\t"
            "v_mul_f32 v160, v209, v160\n\t"
            "v_mfma_f32_16x16x32_bf16 v[148:151], v[220:223], v[164:167], v[148:151]\n\t"
            "v_mul_f32 v161, v209, v161\n\t"
            "v_mul_f32 v226, v229, v226\n\t"
            "v_mfma_f32_16x16x32_bf16 v[180:183], v[216:219], v[180:183], 0\n\t"
            "v_mul_f32 v204, v209, v204\n\t"
            "v_mul_f32 v205, v209, v205\n\t"
            "v_mfma_f32_16x16x32_bf16 v[180:183], v[220:223], v[172:175], v[180:183]\n\t"
            "v_mul_f32 v227, v229, v227\n\t"
            "v_cvt_pk_bf16_f32 v224, v224, v225\n\t"
            "v_mul_f32 v148, v229, v148\n\t"
            "v_mul_f32 v149, v229, v149\n\t"
            "v_mul_f32 v214, v229, v214\n\t"
            "v_mul_f32 v215, v229, v215\n\t"
            "v_cvt_pk_bf16_f32 v212, v212, v213\n\t"
            "s_waitcnt lgkmcnt(0)\n\t"
            "v_mfma_f32_16x16x32_bf16 v[200:203], v[240:243], v[184:187], 0\n\t"
            "v_mul_f32 v180, v209, v180\n\t"
            "v_mul_f32 v181, v209, v181\n\t"
            "v_mfma_f32_16x16x32_bf16 v[200:203], v[240:243], v[188:191], v[200:203]\n\t"
            "v_mul_f32 v150, v229, v150\n\t"
            "v_mul_f32 v151, v229, v151\n\t"
            "v_mfma_f32_16x16x32_bf16 v[168:171], v[184:187], v[168:171], 0\n\t"
            "v_cvt_pk_bf16_f32 v148, v148, v149\n\t"
            "v_mul_f32 v146, v157, v146\n\t"
            "v_mfma_f32_16x16x32_bf16 v[168:171], v[188:191], v[176:179], v[168:171]\n\t"
            "v_mul_f32 v147, v157, v147\n\t"
            "v_cvt_pk_bf16_f32 v144, v144, v145\n\t"
            "v_mfma_f32_16x16x32_bf16 v[192:195], v[184:187], v[192:195], 0\n\t"
            "v_rcp_f32 v201, v200\n\t"
            "v_mfma_f32_16x16x32_bf16 v[192:195], v[188:191], v[196:199], v[192:195]\n\t"
            "v_mul_f32 v154, v157, v154\n\t"
            "v_mul_f32 v155, v157, v155\n\t"
            "v_mfma_f32_16x16x32_bf16 v[216:219], v[184:187], v[216:219], 0\n\t"
            "v_cvt_pk_bf16_f32 v152, v152, v153\n\t"
            "v_mul_f32 v162, v209, v162\n\t"
            "v_mfma_f32_16x16x32_bf16 v[216:219], v[188:191], v[220:223], v[216:219]\n\t"
            "v_mul_f32 v163, v209, v163\n\t"
            "v_cvt_pk_bf16_f32 v160, v160, v161\n\t"
            "v_cndmask_b32_e64 v203, v168, v192, s[60:61]\n\t"
            "v_mul_f32 v206, v209, v206\n\t"
            "v_mul_f32 v207, v209, v207\n\t"
            "v_cvt_pk_bf16_f32 v204, v204, v205\n\t"
            "v_mul_f32 v182, v209, v182\n\t"
            "v_mul_f32 v183, v209, v183\n\t"
            "v_cvt_pk_bf16_f32 v180, v180, v181\n\t"
            "v_cndmask_b32_e64 v203, v203, v216, s[62:63]\n\t"
            "v_cvt_pk_bf16_f32 v225, v226, v227\n\t"
            "v_cvt_pk_bf16_f32 v213, v214, v215\n\t"
            "v_cvt_pk_bf16_f32 v149, v150, v151\n\t"
            "v_cvt_pk_bf16_f32 v145, v146, v147\n\t"
            "v_cvt_pk_bf16_f32 v153, v154, v155\n\t"
            "v_cvt_pk_bf16_f32 v161, v162, v163\n\t"
            "v_cvt_pk_bf16_f32 v205, v206, v207\n\t"
            "v_cvt_pk_bf16_f32 v181, v182, v183\n\t"
            "v_mul_f32_dpp v203, v201, v203 quad_perm:[0,0,0,0] row_mask:0xf bank_mask:0xf\n\t"
            "v_xad_u32 v202, s73, v254, v255\n\t"
            "v_cvt_pk_bf16_f32 v203, v203, 0\n\t"
            "ds_write_b64 v245, v[224:225]\n\t"
            "ds_write_b64 v245, v[212:213] offset:128\n\t"
            "ds_write_b64 v245, v[148:149] offset:256\n\t"
            "ds_write_b64 v245, v[144:145] offset:6144\n\t"
            "ds_write_b64 v245, v[152:153] offset:6272\n\t"
            "ds_write_b64 v245, v[160:161] offset:12288\n\t"
            "ds_write_b64 v245, v[204:205] offset:12416\n\t"
            "ds_write_b64 v245, v[180:181] offset:12544\n\t"
            "s_mov_b64 exec, s[68:69]\n\t"
            "ds_write_b16 v202, v203\n\t"
            "s_mov_b64 exec, 1\n\t"
            "ds_add_u32 v253, v246 offset:7856\n\t"
            "s_mov_b64 exec, -1\n\t"
            ".Lwa_done%=:\n\t"
            "s_waitcnt lgkmcnt(0)\n\t"
            "s_nop 7\n\t"
            : "+{v249}"(aRSw_), "+{s48}"(s48), "+{s70}"(s70), "+{s72}"(s72), "+{s73}"(s73), "+{s74}"(s74), "+{s76}"(s76), "+{s77}"(s77)
            : "{v244}"(pE_), "{v245}"(aO_), "{v246}"(sel_), "{v247}"(aVB_), "{v248}"(pST_), "{v250}"(aPLw_), "{v251}"(aRSr_), "{v252}"(g16_), "{v253}"(g8_), "{v254}"(loVL_), "{v255}"(loOL_), "{v[240:243]}"(ones4), "{s47}"((ldsb + X_OFF) ^ (ldsb + X1_OFF)),
              "{s[58:59]}"(0xFFFFull), "{s[60:61]}"(0xFFFF0000ull), "{s[62:63]}"(0xFFFF00000000ull), "{s[64:65]}"(0xFFFF000000000000ull),
              "{s[66:67]}"(0x1FFFFFFFFFFFFull), "{s75}"(rsb), "{s45}"(ldsb + V_OFF + w * 4608), "{s[68:69]}"(0xFFFFFFFFFFFFull),
              "{s[84:87]}"(wr), "{s46}"(wave * 36864), "{s88}"(touch), "{s80}"(wave == 0 ? 16 : 20), "{s82}"(tag_lo), "{s83}"(tag_hi), "{s71}"(ldsb + O_OFF), "{s[54:55]}"(mrow), "{s[56:57]}"(0xF0000F0000F0000Full),
              "{s[78:79]}"(mrow & 0xF0000F0000F0000Full)
            : "v144", "v145", "v146", "v147", "v148", "v149", "v150", "v151", "v152", "v153", "v154", "v155", "v156", "v157", "v158", "v159", "v160", "v161", "v162", "v163", "v164", "v165", "v166", "v167", "v168", "v169", "v170", "v171", "v172", "v173", "v174", "v175", "v176", "v177", "v178", "v179", "v180", "v181", "v182", "v183", "v184", "v185", "v186", "v187", "v188", "v189", "v190", "v191", "v192", "v193", "v194", "v195", "v196", "v197", "v198", "v199", "v200", "v201", "v202", "v203", "v204", "v205", "v206", "v207", "v208", "v209", "v210", "v211", "v212", "v213", "v214", "v215", "v216", "v217", "v218", "v219", "v220", "v221", "v222", "v223", "v224", "v225", "v226", "v227", "v228", "v229", "v230", "v231", "v232", "v233", "v234", "v235", "v236", "v237", "v238", "v239", "v0", "v1", "v2", "v3", "v4", "v5", "v6", "v7", "v8", "v9", "v10", "v11", "v12", "v13", "v14", "v15", "v16", "v17", "v18", "v19", "v20", "v21", "v22", "v23", "v24", "v25", "v26", "v27", "v28", "v29", "v30", "v31", "v32", "v33", "v34", "v35", "v36", "v37", "v38", "v39", "v40", "v41", "v42", "v43", "v44", "v45", "v46", "v47", "v48", "v49", "v50", "v51", "v52", "v53", "v54", "v55", "v56", "v57", "v58", "v59", "v60", "v61", "v62", "v63", "v64", "v65", "v66", "v67", "v68", "v69", "v70", "v71", "v72", "v73", "v74", "v75", "v76", "v77", "v78", "v79", "v80", "v81", "v82", "v83", "v84", "v85", "v86", "v87", "v88", "v89", "v90", "v91", "v92", "v93", "v94", "v95", "v96", "v97", "v98", "v99", "v100", "v101", "v102", "v103", "v104", "v105", "v106", "v107", "v108", "v109", "v110", "v111", "v112", "v113", "v114", "v115", "v116", "v117", "v118", "v119", "v120", "v121", "v122", "v123", "v124", "v125", "v126", "v127", "v128", "v129", "v130", "v131", "v132", "v133", "v134", "v135", "v136", "v137", "v138", "v139", "v140", "v141", "v142", "v143",  "m0", "s81", "s89", "s90", "s91", "s49", "vcc", "scc", "memory");

        }
        if (wave == 0 && blockIdx.x == 0) {
            int ln_;
            asm volatile("v_mbcnt_lo_u32_b32 %0, -1, 0\n\tv_mbcnt_hi_u32_b32 %0, -1, %0" : "=v"(ln_));
            KARGS
            unsigned long long tg;
            asm volatile("s_load_dwordx2 %0, %1, %2\n\ts_waitcnt lgkmcnt(0)" : "=s"(tg) : "s"(wb), "n"(EPO_OFF) : "memory");
            const unsigned lo = (unsigned)tg + 1u, hi = (unsigned)(tg >> 32);
            const auto wq = __builtin_amdgcn_make_buffer_rsrc((void*)wb, 0, WS_BYTES, 0x00020000);
            for (int it_ = 0; it_ < 100000; ++it_) {
                bool bad = false;
#pragma unroll
                for (int k = 0; k < 2; ++k) {
                    const u32x4 a_ = __builtin_amdgcn_raw_buffer_load_b128(wq, ARR_OFF + ln_ * 16, k * 1024, SC1);
                    bad |= a_[0] != lo || a_[1] != hi || a_[2] != lo || a_[3] != hi;
                }
                if (__builtin_amdgcn_ballot_w64(bad) == 0ull) break;
                __builtin_amdgcn_s_sleep(2);
            }
            if (ln_ == 0) __builtin_amdgcn_raw_buffer_store_b64((u32x2){lo, hi}, wq, EPO_OFF, 0, SC1);
        }
        asm volatile("wk_code_end:");
        __builtin_amdgcn_endpgm();
    } else if (wave < 6) {
        LANE_SETUP
        {
            const unsigned rsb = ldsb + RSX_OFF;
            const unsigned pE_ = fE - X_OFF;
            const unsigned aVAw_ = ldsb + V_OFF + w * 9216 + lane * 16, aVBw_ = ldsb + V_OFF + w * 9216 + 1024 + lane * 8;
            const unsigned pST_ = lane * 16;
            unsigned aRSw_ = rsb + 256 + lane * 4, aRSr_ = rsb + (lane & 48);
            B_SCALARS
            unsigned s44 = 4;
            asm volatile(
            "s_waitcnt lgkmcnt(0)\n\t"
            "s_mov_b32 s83, 0x10000\n\t"
            "s_mov_b32 s84, -47104\n\t"
            "s_mov_b64 s[88:89], s[72:73]\n\t"
            "s_mov_b64 s[90:91], s[74:75]\n\t"
            "s_nop 7\n\t"
            "s_setprio 3\n\t"
            "s_barrier\n\t"
            "buffer_load_dwordx4 v[140:143], v245, s[28:31], s82 offen sc1\n\t"
            "s_add_u32 s49, s33, 0\n\t"
            "buffer_load_dwordx4 v[0:3], v245, s[28:31], s49 offen sc1\n\t"
            "s_add_u32 s49, s33, 6144\n\t"
            "buffer_load_dwordx4 v[24:27], v245, s[28:31], s49 offen sc1\n\t"
            "s_add_u32 s49, s33, 12288\n\t"
            "buffer_load_dwordx4 v[48:51], v245, s[28:31], s49 offen sc1\n\t"
            "s_add_u32 s49, s33, 18432\n\t"
            "buffer_load_dwordx4 v[72:75], v245, s[28:31], s49 offen sc1\n\t"
            "s_add_u32 s49, s33, 24576\n\t"
            "buffer_load_dwordx4 v[96:99], v245, s[28:31], s49 offen sc1\n\t"
            "s_add_u32 s49, s33, 30720\n\t"
            "buffer_load_dwordx4 v[120:123], v245, s[28:31], s49 offen sc1\n\t"
            "s_add_u32 s49, s33, 1024\n\t"
            "buffer_load_dwordx4 v[4:7], v245, s[28:31], s49 offen sc1\n\t"
            "s_add_u32 s49, s33, 7168\n\t"
            "buffer_load_dwordx4 v[28:31], v245, s[28:31], s49 offen sc1\n\t"
            "s_add_u32 s49, s33, 13312\n\t"
            "buffer_load_dwordx4 v[52:55], v245, s[28:31], s49 offen sc1\n\t"
            "s_add_u32 s49, s33, 19456\n\t"
            "buffer_load_dwordx4 v[76:79], v245, s[28:31], s49 offen sc1\n\t"
            "s_add_u32 s49, s33, 25600\n\t"
            "buffer_load_dwordx4 v[100:103], v245, s[28:31], s49 offen sc1\n\t"
            "s_add_u32 s49, s33, 31744\n\t"
            "buffer_load_dwordx4 v[124:127], v245, s[28:31], s49 offen sc1\n\t"
            "s_add_u32 s49, s33, 2048\n\t"
            "buffer_load_dwordx4 v[8:11], v245, s[28:31], s49 offen sc1\n\t"
            "s_add_u32 s49, s33, 8192\n\t"
            "buffer_load_dwordx4 v[32:35], v245, s[28:31], s49 offen sc1\n\t"
            "s_add_u32 s49, s33, 14336\n\t"
            "buffer_load_dwordx4 v[56:59], v245, s[28:31], s49 offen sc1\n\t"
            "s_add_u32 s49, s33, 20480\n\t"
            "buffer_load_dwordx4 v[80:83], v245, s[28:31], s49 offen sc1\n\t"
            "s_add_u32 s49, s33, 26624\n\t"
            "buffer_load_dwordx4 v[104:107], v245, s[28:31], s49 offen sc1\n\t"
            "s_add_u32 s49, s33, 32768\n\t"
            "buffer_load_dwordx4 v[128:131], v245, s[28:31], s49 offen sc1\n\t"
            "s_add_u32 s49, s33, 3072\n\t"
            "buffer_load_dwordx4 v[12:15], v245, s[28:31], s49 offen sc1\n\t"
            "s_add_u32 s49, s33, 9216\n\t"
            "buffer_load_dwordx4 v[36:39], v245, s[28:31], s49 offen sc1\n\t"
            "s_add_u32 s49, s33, 15360\n\t"
            "buffer_load_dwordx4 v[60:63], v245, s[28:31], s49 offen sc1\n\t"
            "s_add_u32 s49, s33, 21504\n\t"
            "buffer_load_dwordx4 v[84:87], v245, s[28:31], s49 offen sc1\n\t"
            "s_add_u32 s49, s33, 27648\n\t"
            "buffer_load_dwordx4 v[108:111], v245, s[28:31], s49 offen sc1\n\t"
            "s_add_u32 s49, s33, 33792\n\t"
            "buffer_load_dwordx4 v[132:135], v245, s[28:31], s49 offen sc1\n\t"
            "s_add_u32 s49, s33, 4096\n\t"
            "buffer_load_dwordx4 v[16:19], v245, s[28:31], s49 offen sc1\n\t"
            "s_add_u32 s49, s33, 10240\n\t"
            "buffer_load_dwordx4 v[40:43], v245, s[28:31], s49 offen sc1\n\t"
            "s_add_u32 s49, s33, 16384\n\t"
            "buffer_load_dwordx4 v[64:67], v245, s[28:31], s49 offen sc1\n\t"
            "s_add_u32 s49, s33, 22528\n\t"
            "buffer_load_dwordx4 v[88:91], v245, s[28:31], s49 offen sc1\n\t"
            "s_add_u32 s49, s33, 28672\n\t"
            "buffer_load_dwordx4 v[112:115], v245, s[28:31], s49 offen sc1\n\t"
            "s_add_u32 s49, s33, 34816\n\t"
            "buffer_load_dwordx4 v[136:139], v245, s[28:31], s49 offen sc1\n\t"
            "s_add_u32 s49, s33, 5120\n\t"
            "buffer_load_dwordx4 v[20:23], v245, s[28:31], s49 offen sc1\n\t"
            "s_add_u32 s49, s33, 11264\n\t"
            "buffer_load_dwordx4 v[44:47], v245, s[28:31], s49 offen sc1\n\t"
            "s_add_u32 s49, s33, 17408\n\t"
            "buffer_load_dwordx4 v[68:71], v245, s[28:31], s49 offen sc1\n\t"
            "s_add_u32 s49, s33, 23552\n\t"
            "buffer_load_dwordx4 v[92:95], v245, s[28:31], s49 offen sc1\n\t"
            "s_add_u32 s49, s33, 29696\n\t"
            "buffer_load_dwordx4 v[116:119], v245, s[28:31], s49 offen sc1\n\t"
            "s_add_u32 s49, s33, 35840\n\t"
            "buffer_load_dwordx4 v[140:143], v245, s[28:31], s49 offen sc1\n\t"
            "ds_read_b32 v249, v252 offset:12\n\t"
            "s_waitcnt lgkmcnt(0)\n\t"
            "v_and_b32 v249, s69, v249\n\t"
            "s_mov_b32 exec_hi, 0x3fffff\n\t"
            "s_add_u32 m0, s66, 0\n\t"
            "s_add_u32 s49, s79, 0\n\t"
            "buffer_load_dwordx4 v249, s[72:75], s49 offen lds\n\t"
            "s_mov_b32 exec_hi, -1\n\t"
            "s_mov_b32 exec_hi, 0x3fffff\n\t"
            "s_add_u32 m0, s66, 872\n\t"
            "s_add_u32 s49, s47, 196224\n\t"
            "buffer_load_dwordx4 v249, s[72:75], s49 offen lds\n\t"
            "s_mov_b32 exec_hi, -1\n\t"
            "s_mov_b32 exec_hi, 0x3fffff\n\t"
            "s_add_u32 m0, s66, 1744\n\t"
            "s_add_u32 s49, s47, 392448\n\t"
            "buffer_load_dwordx4 v249, s[72:75], s49 offen lds\n\t"
            "s_mov_b32 exec_hi, -1\n\t"
            "s_mov_b32 exec_hi, 0x3fffff\n\t"
            "s_add_u32 m0, s66, 2616\n\t"
            "s_add_u32 s49, s47, 588672\n\t"
            "buffer_load_dwordx4 v249, s[72:75], s49 offen lds\n\t"
            "s_mov_b32 exec_hi, -1\n\t"
            "s_mov_b32 exec_hi, 0x3fffff\n\t"
            "s_add_u32 m0, s66, 3488\n\t"
            "s_add_u32 s49, s47, 784896\n\t"
            "buffer_load_dwordx4 v249, s[72:75], s49 offen lds\n\t"
            "s_mov_b32 exec_hi, -1\n\t"
            "s_mov_b32 exec_hi, 0x3fffff\n\t"
            "s_add_u32 m0, s66, 4360\n\t"
            "s_add_u32 s49, s47, 981120\n\t"
            "buffer_load_dwordx4 v249, s[72:75], s49 offen lds\n\t"
            "s_mov_b32 exec_hi, -1\n\t"
            "s_mov_b32 exec_hi, 0x3fffff\n\t"
            "s_add_u32 m0, s66, 5232\n\t"
            "s_add_u32 s49, s47, 1177344\n\t"
            "buffer_load_dwordx4 v249, s[72:75], s49 offen lds\n\t"
            "s_mov_b32 exec_hi, -1\n\t"
            "s_mov_b32 exec_hi, 0x3fffff\n\t"
            "s_add_u32 m0, s66, 6104\n\t"
            "s_add_u32 s49, s47, 1373568\n\t"
            "buffer_load_dwordx4 v249, s[72:75], s49 offen lds\n\t"
            "s_mov_b32 exec_hi, -1\n\t"
            "s_mov_b32 exec_hi, 0x3fffff\n\t"
            "s_add_u32 m0, s66, 6976\n\t"
            "s_add_u32 s49, s47, 1569792\n\t"
            "buffer_load_dwordx4 v249, s[72:75], s49 offen lds\n\t"
            "s_mov_b32 exec_hi, -1\n\t"
            "ds_read_b128 v[168:171], v254\n\t"
            "ds_read_b128 v[172:175], v254 offset:64\n\t"
            "v_add_u32 v180, s48, v245\n\t"
            "v_add_u32 v232, s48, v242\n\t"
            "v_xor_b32 v233, 64, v232\n\t"
            "v_xor_b32 v246, 0x100, v246\n\t"
            "ds_read_b128 v[176:179], v180 offset:18816\n\t"
            "ds_read_b128 v[180:183], v180 offset:19840\n\t"
            "ds_read_b128 v[192:195], v232\n\t"
            "ds_read_b128 v[196:199], v233\n\t"
            "ds_read_b128 v[224:227], v232 offset:128\n\t"
            "ds_read_b128 v[228:231], v233 offset:128\n\t"
            "s_waitcnt lgkmcnt(5)\n\t"
            "v_add_f32 v176, v176, v178\n\t"
            "s_waitcnt lgkmcnt(4)\n\t"
            "v_add_f32 v180, v180, v182\n\t"
            "v_add_f32 v176, v176, v180\n\t"
            "v_add_f32 v177, v177, v179\n\t"
            "v_add_f32 v181, v181, v183\n\t"
            "v_mul_f32 v176, 0x3baaaaab, v176\n\t"
            "v_add_f32 v177, v177, v181\n\t"
            "v_mul_f32_e64 v176, v176, -v176\n\t"
            "v_fmac_f32 v176, 0x3baaaaab, v177\n\t"
            "v_max_f32 v176, 0, v176\n\t"
            "v_add_f32 v176, 0x3727c5ac, v176\n\t"
            "v_rsq_f32 v176, v176\n\t"
            "s_nop 0\n\t"
            "ds_write_b32 v246, v176\n\t"
            "s_setprio 3\n\t"
            "s_waitcnt vmcnt(9)\n\t"
            "s_waitcnt lgkmcnt(3)\n\t"
            "v_mfma_f32_16x16x32_bf16 v[200:203], v[192:195], v[0:3], 0\n\t"
            "v_mfma_f32_16x16x32_bf16 v[200:203], v[196:199], v[4:7], v[200:203]\n\t"
            "v_mfma_f32_16x16x32_bf16 v[204:207], v[196:199], v[28:31], 0\n\t"
            "v_mfma_f32_16x16x32_bf16 v[204:207], v[192:195], v[24:27], v[204:207]\n\t"
            "v_mfma_f32_16x16x32_bf16 v[208:211], v[192:195], v[48:51], 0\n\t"
            "v_mfma_f32_16x16x32_bf16 v[208:211], v[196:199], v[52:55], v[208:211]\n\t"
            "v_mfma_f32_16x16x32_bf16 v[212:215], v[196:199], v[76:79], 0\n\t"
            "v_mfma_f32_16x16x32_bf16 v[212:215], v[192:195], v[72:75], v[212:215]\n\t"
            "v_mfma_f32_16x16x32_bf16 v[216:219], v[192:195], v[96:99], 0\n\t"
            "v_mfma_f32_16x16x32_bf16 v[216:219], v[196:199], v[100:103], v[216:219]\n\t"
            "v_mfma_f32_16x16x32_bf16 v[220:223], v[196:199], v[124:127], 0\n\t"
            "v_mfma_f32_16x16x32_bf16 v[220:223], v[192:195], v[120:123], v[220:223]\n\t"
            "s_waitcnt lgkmcnt(1)\n\t"
            "v_mfma_f32_16x16x32_bf16 v[200:203], v[224:227], v[8:11], v[200:203]\n\t"
            "v_mfma_f32_16x16x32_bf16 v[200:203], v[228:231], v[12:15], v[200:203]\n\t"
            "v_mfma_f32_16x16x32_bf16 v[204:207], v[228:231], v[36:39], v[204:207]\n\t"
            "v_mfma_f32_16x16x32_bf16 v[204:207], v[224:227], v[32:35], v[204:207]\n\t"
            "v_mfma_f32_16x16x32_bf16 v[208:211], v[224:227], v[56:59], v[208:211]\n\t"
            "v_mfma_f32_16x16x32_bf16 v[208:211], v[228:231], v[60:63], v[208:211]\n\t"
            "v_mfma_f32_16x16x32_bf16 v[212:215], v[228:231], v[84:87], v[212:215]\n\t"
            "v_mfma_f32_16x16x32_bf16 v[212:215], v[224:227], v[80:83], v[212:215]\n\t"
            "v_mfma_f32_16x16x32_bf16 v[216:219], v[224:227], v[104:107], v[216:219]\n\t"
            "s_waitcnt vmcnt(0)\n\t"
            "ds_read_b64 v[144:145], v253\n\t"
            "v_mfma_f32_16x16x32_bf16 v[216:219], v[228:231], v[108:111], v[216:219]\n\t"
            "ds_read_b64 v[146:147], v253 offset:96\n\t"
            "ds_read_b64 v[148:149], v253 offset:1744\n\t"
            "v_mfma_f32_16x16x32_bf16 v[220:223], v[228:231], v[132:135], v[220:223]\n\t"
            "ds_read_b128 v[228:231], v233 offset:256\n\t"
            "ds_read_b64 v[150:151], v253 offset:1840\n\t"
            "v_mfma_f32_16x16x32_bf16 v[220:223], v[224:227], v[128:131], v[220:223]\n\t"
            "ds_read_b128 v[224:227], v232 offset:256\n\t"
            "ds_read_b64 v[152:153], v253 offset:8\n\t"
            "ds_read_b64 v[154:155], v253 offset:104\n\t"
            "ds_read_b64 v[156:157], v253 offset:1752\n\t"
            "ds_read_b64 v[158:159], v253 offset:1848\n\t"
            "ds_read_b64 v[160:161], v253 offset:16\n\t"
            "ds_read_b64 v[162:163], v253 offset:112\n\t"
            "ds_read_b64 v[164:165], v253 offset:1760\n\t"
            "ds_read_b64 v[166:167], v253 offset:1856\n\t"
            "s_waitcnt lgkmcnt(0)\n\t"
            "s_nop 7\n\t"
            ".p2align 6\n\t"
            "s_nop 0\n\t"
            "s_nop 0\n\t"
            "s_nop 0\n\t"
            "s_nop 0\n\t"
            ".Lwc_loop%=:\n\t"
            "s_setprio 1\n\t"
            "s_setprio 3\n\t"
            "v_mfma_f32_16x16x32_bf16 v[200:203], v[224:227], v[16:19], v[200:203]\n\t"
            "v_mov_b64 v[176:177], 0\n\t"
            "v_mov_b64 v[178:179], 0\n\t"
            "v_mfma_f32_16x16x32_bf16 v[200:203], v[228:231], v[20:23], v[200:203]\n\t"
            "v_mov_b64 v[180:181], 0\n\t"
            "v_mov_b64 v[182:183], 0\n\t"
            "s_waitcnt lgkmcnt(0)\n\t"
            "v_smfmac_f32_16x16x64_bf16 v[176:179], v[168:171], v[144:151], v255\n\t"
            "ds_read_b128 v[168:171], v254 offset:128\n\t"
            "v_smfmac_f32_16x16x64_bf16 v[180:183], v[172:175], v[152:159], v255\n\t"
            "ds_read_b64 v[144:145], v253 offset:24\n\t"
            "v_mfma_f32_16x16x32_bf16 v[204:207], v[228:231], v[44:47], v[204:207]\n\t"
            "ds_read_b64 v[146:147], v253 offset:120\n\t"
            "ds_read_b64 v[148:149], v253 offset:1768\n\t"
            "v_mfma_f32_16x16x32_bf16 v[204:207], v[224:227], v[40:43], v[204:207]\n\t"
            "ds_read_b64 v[150:151], v253 offset:1864\n\t"
            "ds_read_b128 v[172:175], v254 offset:192\n\t"
            "v_mfma_f32_16x16x32_bf16 v[208:211], v[224:227], v[64:67], v[208:211]\n\t"
            "ds_read_b64 v[152:153], v253 offset:32\n\t"
            "ds_read_b64 v[154:155], v253 offset:128\n\t"
            "v_mfma_f32_16x16x32_bf16 v[208:211], v[228:231], v[68:71], v[208:211]\n\t"
            "ds_read_b64 v[156:157], v253 offset:1776\n\t"
            "ds_read_b64 v[158:159], v253 offset:1872\n\t"
            "v_mfma_f32_16x16x32_bf16 v[212:215], v[228:231], v[92:95], v[212:215]\n\t"
            "v_mov_b32 v249, s76\n\t"
            "v_mov_b64 v[184:185], 0\n\t"
            "v_mfma_f32_16x16x32_bf16 v[212:215], v[224:227], v[88:91], v[212:215]\n\t"
            "ds_read_b32 v249, v249\n\t"
            "v_mov_b64 v[186:187], 0\n\t"
            "v_mfma_f32_16x16x32_bf16 v[216:219], v[224:227], v[112:115], v[216:219]\n\t"
            "v_mov_b64 v[188:189], 0\n\t"
            "v_mov_b64 v[190:191], 0\n\t"
            "v_mfma_f32_16x16x32_bf16 v[216:219], v[228:231], v[116:119], v[216:219]\n\t"
            "v_cvt_pk_bf16_f32 v176, v176, v177\n\t"
            "v_add_u32 v236, s67, v250\n\t"
            "s_waitcnt lgkmcnt(10)\n\t"
            "v_smfmac_f32_16x16x64_bf16 v[184:187], v[168:171], v[160:167], v255\n\t"
            "ds_read_b128 v[168:171], v254 offset:256\n\t"
            "s_waitcnt lgkmcnt(6)\n\t"
            "v_smfmac_f32_16x16x64_bf16 v[188:191], v[172:175], v[144:151], v255\n\t"
            "ds_read_b64 v[160:161], v253 offset:40\n\t"
            "v_mfma_f32_16x16x32_bf16 v[220:223], v[228:231], v[140:143], v[220:223]\n\t"
            "ds_read_b64 v[162:163], v253 offset:136\n\t"
            "ds_read_b64 v[164:165], v253 offset:1784\n\t"
            "v_mfma_f32_16x16x32_bf16 v[220:223], v[224:227], v[136:139], v[220:223]\n\t"
            "s_setprio 1\n\t"
            "ds_read_b64 v[166:167], v253 offset:1880\n\t"
            "ds_read_b128 v[172:175], v254 offset:320\n\t"
            "ds_read_b64 v[144:145], v253 offset:48\n\t"
            "ds_read_b64 v[146:147], v253 offset:144\n\t"
            "ds_read_b64 v[148:149], v253 offset:1792\n\t"
            "ds_read_b64 v[150:151], v253 offset:1888\n\t"
            "s_waitcnt lgkmcnt(14)\n\t"
            "ds_read_b128 v[224:227], v232 offset:6272\n\t"
            "s_waitcnt lgkmcnt(14)\n\t"
            "ds_read_b128 v[228:231], v233 offset:6272\n\t"
            "s_waitcnt lgkmcnt(12)\n\t"
            "v_readfirstlane_b32 s26, v249\n\t"
            "v_cvt_pk_bf16_f32 v177, v178, v179\n\t"
            "v_cvt_pk_bf16_f32 v178, v180, v181\n\t"
            "s_and_b32 s68, s26, 0x7ffffff\n\t"
            "s_add_u32 s68, s68, s78\n\t"
            "s_and_b32 s69, s26, 0x38000000\n\t"
            "s_or_b32 s69, s69, s39\n\t"
            "s_bitcmp1_b32 s26, 27\n\t"
            "s_cselect_b32 s79, 2.0, s68\n\t"
            "s_setprio 3\n\t"
            "v_mfma_f32_4x4x4_16b_bf16 v[196:199], v[176:177], v[176:177], 0\n\t"
            "s_setprio 1\n\t"
            "ds_read_b32 v249, v252 offset:12\n\t"
            "s_and_b32 s57, s56, 3\n\t"
            "s_lshl_b32 s58, s57, 3\n\t"
            "s_lshr_b32 m0, s54, s58\n\t"
            "s_lshl_b32 s58, s57, 4\n\t"
            "s_lshr_b64 s[36:37], s[50:51], s58\n\t"
            "s_and_b32 s57, s36, 0xffff\n\t"
            "s_add_u32 s57, s57, s67\n\t"
            "v_writelane_b32 v236, s57, 63\n\t"
            "v_writelane_b32 v236, s59, m0\n\t"
            "v_cvt_pk_bf16_f32 v179, v182, v183\n\t"
            "v_mov_b64 v[180:181], 0\n\t"
            "v_mov_b64 v[182:183], 0\n\t"
            "s_setprio 3\n\t"
            "v_mfma_f32_16x16x32_bf16 v[192:195], v[238:241], v[176:179], 0\n\t"
            "s_lshr_b64 s[36:37], s[52:53], s58\n\t"
            "s_and_b32 s58, s36, 0xffff\n\t"
            "s_add_u32 s58, s58, s67\n\t"
            "v_mfma_f32_4x4x4_16b_bf16 v[196:199], v[178:179], v[178:179], v[196:199]\n\t"
            "ds_write_b128 v236, v[176:179]\n\t"
            "s_waitcnt lgkmcnt(8)\n\t"
            "v_smfmac_f32_16x16x64_bf16 v[180:183], v[172:175], v[160:167], v255\n\t"
            "s_setprio 1\n\t"
            "s_and_b32 s38, s52, 0xffff\n\t"
            "s_add_u32 s38, s38, s67\n\t"
            "ds_read_b128 v[172:175], v254 offset:448\n\t"
            "ds_read_b64 v[160:161], v253 offset:64\n\t"
            "ds_read_b64 v[162:163], v253 offset:160\n\t"
            "ds_read_b64 v[164:165], v253 offset:1808\n\t"
            "ds_read_b64 v[166:167], v253 offset:1904\n\t"
            "v_mov_b64 v[176:177], 0\n\t"
            "v_mov_b64 v[178:179], 0\n\t"
            "v_cvt_pk_bf16_f32 v184, v184, v185\n\t"
            "v_cvt_pk_bf16_f32 v185, v186, v187\n\t"
            "s_setprio 3\n\t"
            "v_smfmac_f32_16x16x64_bf16 v[176:179], v[168:171], v[152:159], v255\n\t"
            "ds_read_b128 v[168:171], v254 offset:384\n\t"
            "v_mfma_f32_4x4x4_16b_bf16 v[196:199], v[184:185], v[184:185], v[196:199]\n\t"
            "s_setprio 1\n\t"
            "ds_read_b64 v[152:153], v253 offset:56\n\t"
            "s_waitcnt lgkmcnt(14)\n\t"
            "ds_read_b64 v[154:155], v253 offset:152\n\t"
            "s_waitcnt lgkmcnt(14)\n\t"
            "ds_read_b64 v[156:157], v253 offset:1800\n\t"
            "s_waitcnt lgkmcnt(14)\n\t"
            "ds_read_b64 v[158:159], v253 offset:1896\n\t"
            "v_xor_b32 v237, 16, v236\n\t"
            "v_cvt_pk_bf16_f32 v186, v188, v189\n\t"
            "v_cvt_pk_bf16_f32 v187, v190, v191\n\t"
            "v_mov_b64 v[188:189], 0\n\t"
            "v_mov_b64 v[190:191], 0\n\t"
            "s_setprio 3\n\t"
            "v_mfma_f32_16x16x32_bf16 v[192:195], v[238:241], v[184:187], v[192:195]\n\t"
            "s_waitcnt lgkmcnt(14)\n\t"
            "ds_write_b128 v237, v[184:187]\n\t"
            "v_mfma_f32_4x4x4_16b_bf16 v[196:199], v[186:187], v[186:187], v[196:199]\n\t"
            "s_setprio 1\n\t"
            "v_mov_b64 v[184:185], 0\n\t"
            "v_mov_b64 v[186:187], 0\n\t"
            "v_cvt_pk_bf16_f32 v176, v176, v177\n\t"
            "v_cvt_pk_bf16_f32 v177, v178, v179\n\t"
            "v_cvt_pk_bf16_f32 v178, v180, v181\n\t"
            "v_cvt_pk_bf16_f32 v179, v182, v183\n\t"
            "s_setprio 3\n\t"
            "v_mfma_f32_4x4x4_16b_bf16 v[196:199], v[176:177], v[176:177], v[196:199]\n\t"
            "s_waitcnt lgkmcnt(14)\n\t"
            "ds_write_b128 v236, v[176:179] offset:128\n\t"
            "s_waitcnt lgkmcnt(6)\n\t"
            "v_smfmac_f32_16x16x64_bf16 v[184:187], v[168:171], v[144:151], v255\n\t"
            "ds_read_b128 v[168:171], v254 offset:512\n\t"
            "s_waitcnt lgkmcnt(3)\n\t"
            "v_smfmac_f32_16x16x64_bf16 v[188:191], v[172:175], v[152:159], v255\n\t"
            "ds_read_b64 v[144:145], v253 offset:72\n\t"
            "v_mfma_f32_16x16x32_bf16 v[192:195], v[238:241], v[176:179], v[192:195]\n\t"
            "ds_read_b64 v[146:147], v253 offset:168\n\t"
            "ds_read_b64 v[148:149], v253 offset:1816\n\t"
            "v_mfma_f32_4x4x4_16b_bf16 v[196:199], v[178:179], v[178:179], v[196:199]\n\t"
            "s_setprio 1\n\t"
            "ds_read_b64 v[150:151], v253 offset:1912\n\t"
            "ds_read_b128 v[172:175], v254 offset:576\n\t"
            "ds_read_b64 v[152:153], v253 offset:80\n\t"
            "ds_read_b64 v[154:155], v253 offset:176\n\t"
            "ds_read_b64 v[156:157], v253 offset:1824\n\t"
            "ds_read_b64 v[158:159], v253 offset:1920\n\t"
            "v_mov_b64 v[176:177], 0\n\t"
            "v_mov_b64 v[178:179], 0\n\t"
            "v_mov_b64 v[180:181], 0\n\t"
            "v_mov_b64 v[182:183], 0\n\t"
            "v_cvt_pk_bf16_f32 v184, v184, v185\n\t"
            "v_cvt_pk_bf16_f32 v185, v186, v187\n\t"
            "v_cvt_pk_bf16_f32 v186, v188, v189\n\t"
            "v_cvt_pk_bf16_f32 v187, v190, v191\n\t"
            "s_setprio 3\n\t"
            "v_mfma_f32_4x4x4_16b_bf16 v[196:199], v[184:185], v[184:185], v[196:199]\n\t"
            "ds_write_b128 v237, v[184:187] offset:128\n\t"
            "s_waitcnt lgkmcnt(10)\n\t"
            "v_smfmac_f32_16x16x64_bf16 v[176:179], v[168:171], v[160:167], v255\n\t"
            "ds_read_b128 v[168:171], v254 offset:640\n\t"
            "v_mfma_f32_16x16x32_bf16 v[192:195], v[238:241], v[184:187], v[192:195]\n\t"
            "ds_read_b64 v[160:161], v253 offset:88\n\t"
            "ds_read_b64 v[162:163], v253 offset:184\n\t"
            "s_waitcnt lgkmcnt(8)\n\t"
            "v_smfmac_f32_16x16x64_bf16 v[180:183], v[172:175], v[144:151], v255\n\t"
            "ds_read_b64 v[164:165], v253 offset:1832\n\t"
            "v_mfma_f32_4x4x4_16b_bf16 v[196:199], v[186:187], v[186:187], v[196:199]\n\t"
            "s_setprio 1\n\t"
            "ds_read_b64 v[166:167], v253 offset:1928\n\t"
            "ds_read_b128 v[172:175], v254 offset:704\n\t"
            "v_mov_b64 v[184:185], 0\n\t"
            "v_mov_b64 v[186:187], 0\n\t"
            "v_mov_b64 v[188:189], 0\n\t"
            "v_mov_b64 v[190:191], 0\n\t"
            "v_add_u32 v248, s67, v251\n\t"
            "v_writelane_b32 v248, s58, 63\n\t"
            "v_writelane_b32 v248, s38, m0\n\t"
            "v_and_b32 v249, s69, v249\n\t"
            "v_cvt_pk_bf16_f32 v176, v176, v177\n\t"
            "v_cvt_pk_bf16_f32 v177, v178, v179\n\t"
            "v_cvt_pk_bf16_f32 v178, v180, v181\n\t"
            "v_cvt_pk_bf16_f32 v179, v182, v183\n\t"
            "s_setprio 3\n\t"
            "v_mfma_f32_4x4x4_16b_bf16 v[196:199], v[176:177], v[176:177], v[196:199]\n\t"
            "ds_write_b128 v236, v[176:179] offset:256\n\t"
            "s_waitcnt lgkmcnt(6)\n\t"
            "v_smfmac_f32_16x16x64_bf16 v[184:187], v[168:171], v[152:159], v255\n\t"
            "v_mov_b32 v234, 0\n\t"
            "v_mfma_f32_4x4x4_16b_bf16 v[196:199], v[178:179], v[178:179], v[196:199]\n\t"
            "v_xor_b32 v246, 0x100, v246\n\t"
            "v_mfma_f32_16x16x32_bf16 v[192:195], v[238:241], v[176:179], v[192:195]\n\t"
            "s_waitcnt lgkmcnt(1)\n\t"
            "v_smfmac_f32_16x16x64_bf16 v[188:191], v[172:175], v[160:167], v255\n\t"
            "s_setprio 1\n\t"
            "s_mov_b32 exec_hi, 0x3fffff\n\t"
            "s_add_u32 m0, s66, 0\n\t"
            "s_add_u32 s49, s79, 0\n\t"
            "buffer_load_dwordx4 v249, s[88:91], s49 offen lds\n\t"
            "s_mov_b32 exec_hi, -1\n\t"
            "s_mov_b32 exec_hi, 0x3fffff\n\t"
            "s_add_u32 m0, s66, 872\n\t"
            "s_add_u32 s49, s68, 196224\n\t"
            "buffer_load_dwordx4 v249, s[72:75], s49 offen lds\n\t"
            "s_mov_b32 exec_hi, -1\n\t"
            "s_mov_b32 exec_hi, 0x3fffff\n\t"
            "s_add_u32 m0, s66, 1744\n\t"
            "s_add_u32 s49, s68, 392448\n\t"
            "buffer_load_dwordx4 v249, s[72:75], s49 offen lds\n\t"
            "s_mov_b32 exec_hi, -1\n\t"
            "s_mov_b32 exec_hi, 0x3fffff\n\t"
            "s_add_u32 m0, s66, 2616\n\t"
            "s_add_u32 s49, s68, 588672\n\t"
            "buffer_load_dwordx4 v249, s[72:75], s49 offen lds\n\t"
            "s_mov_b32 exec_hi, -1\n\t"
            "s_mov_b32 exec_hi, 0x3fffff\n\t"
            "s_add_u32 m0, s66, 3488\n\t"
            "s_add_u32 s49, s68, 784896\n\t"
            "buffer_load_dwordx4 v249, s[72:75], s49 offen lds\n\t"
            "s_mov_b32 exec_hi, -1\n\t"
            "v_cvt_pk_bf16_f32 v184, v184, v185\n\t"
            "v_cvt_pk_bf16_f32 v185, v186, v187\n\t"
            "v_cvt_pk_bf16_f32 v186, v188, v189\n\t"
            "v_cvt_pk_bf16_f32 v187, v190, v191\n\t"
            "s_setprio 3\n\t"
            "v_mfma_f32_4x4x4_16b_bf16 v[196:199], v[184:185], v[184:185], v[196:199]\n\t"
            "s_mov_b32 exec_hi, 0x3fffff\n\t"
            "s_add_u32 m0, s66, 4360\n\t"
            "s_add_u32 s49, s68, 981120\n\t"
            "buffer_load_dwordx4 v249, s[72:75], s49 offen lds\n\t"
            "s_mov_b32 exec_hi, -1\n\t"
            "v_mfma_f32_4x4x4_16b_bf16 v[196:199], v[186:187], v[186:187], v[196:199]\n\t"
            "s_mov_b32 exec_hi, 0x3fffff\n\t"
            "s_add_u32 m0, s66, 5232\n\t"
            "s_add_u32 s49, s68, 1177344\n\t"
            "buffer_load_dwordx4 v249, s[72:75], s49 offen lds\n\t"
            "s_mov_b32 exec_hi, -1\n\t"
            "v_mfma_f32_16x16x32_bf16 v[192:195], v[238:241], v[184:187], v[192:195]\n\t"
            "s_setprio 1\n\t"
            "s_mov_b32 exec_hi, 0x3fffff\n\t"
            "s_add_u32 m0, s66, 6104\n\t"
            "s_add_u32 s49, s68, 1373568\n\t"
            "buffer_load_dwordx4 v249, s[72:75], s49 offen lds\n\t"
            "s_mov_b32 exec_hi, -1\n\t"
            "s_mov_b32 exec_hi, 0x3fffff\n\t"
            "s_add_u32 m0, s66, 6976\n\t"
            "s_add_u32 s49, s68, 1569792\n\t"
            "buffer_load_dwordx4 v249, s[72:75], s49 offen lds\n\t"
            "s_mov_b32 exec_hi, -1\n\t"
            "v_cndmask_b32_e64 v193, v196, v197, s[60:61]\n\t"
            "v_cndmask_b32_e64 v193, v193, v198, s[62:63]\n\t"
            "v_cndmask_b32_e64 v193, v193, v199, s[64:65]\n\t"
            "s_setprio 1\n\t"
            "ds_read_b128 v[196:199], v233 offset:6144\n\t"
            "ds_write_b64 v248, v[192:193] offset:18816\n\t"
            "ds_read_b128 v[192:195], v232 offset:6144\n\t"
            "ds_read_b64 v[248:249], v247 offset:8\n\t"
            "ds_write_b128 v237, v[184:187] offset:256\n\t"
            "ds_read_b64 v[236:237], v247\n\t"
            "s_mov_b64 exec, 1\n\t"
            "ds_inc_u32 v255, v238 offset:7860\n\t"
            "s_mov_b64 exec, -1\n\t"
            "s_waitcnt lgkmcnt(4)\n\t"
            "v_mfma_f32_16x16x32_bf16 v[144:147], v[192:195], v[0:3], 0\n\t"
            "s_waitcnt lgkmcnt(3)\n\t"
            "v_mul_f32 v202, v248, v202\n\t"
            "v_mul_f32 v203, v249, v203\n\t"
            "v_mfma_f32_16x16x32_bf16 v[144:147], v[196:199], v[4:7], v[144:147]\n\t"
            "v_mul_f32 v206, v248, v206\n\t"
            "s_waitcnt lgkmcnt(1)\n\t"
            "v_mul_f32 v200, v236, v200\n\t"
            "v_mfma_f32_16x16x32_bf16 v[148:151], v[196:199], v[28:31], 0\n\t"
            "v_mul_f32 v201, v237, v201\n\t"
            "v_mul_f32 v204, v236, v204\n\t"
            "v_mfma_f32_16x16x32_bf16 v[148:151], v[192:195], v[24:27], v[148:151]\n\t"
            "v_mul_f32 v205, v237, v205\n\t"
            "v_mul_f32 v208, v236, v208\n\t"
            "v_mfma_f32_16x16x32_bf16 v[168:171], v[192:195], v[48:51], 0\n\t"
            "v_mul_f32 v209, v237, v209\n\t"
            "v_mul_f32 v212, v236, v212\n\t"
            "v_mfma_f32_16x16x32_bf16 v[168:171], v[196:199], v[52:55], v[168:171]\n\t"
            "v_mul_f32 v213, v237, v213\n\t"
            "v_mul_f32 v216, v236, v216\n\t"
            "v_mfma_f32_16x16x32_bf16 v[172:175], v[196:199], v[76:79], 0\n\t"
            "v_mul_f32 v217, v237, v217\n\t"
            "v_mul_f32 v220, v236, v220\n\t"
            "v_mfma_f32_16x16x32_bf16 v[172:175], v[192:195], v[72:75], v[172:175]\n\t"
            "v_mul_f32 v221, v237, v221\n\t"
            "v_mul_f32 v207, v249, v207\n\t"
            "v_mfma_f32_16x16x32_bf16 v[152:155], v[192:195], v[96:99], 0\n\t"
            "ds_read_b64 v[236:237], v247 offset:64\n\t"
            "v_mul_f32 v210, v248, v210\n\t"
            "v_mfma_f32_16x16x32_bf16 v[152:155], v[196:199], v[100:103], v[152:155]\n\t"
            "v_mul_f32 v211, v249, v211\n\t"
            "v_mul_f32 v214, v248, v214\n\t"
            "v_mfma_f32_16x16x32_bf16 v[156:159], v[196:199], v[124:127], 0\n\t"
            "ds_read_b128 v[196:199], v233 offset:6400\n\t"
            "v_mul_f32 v215, v249, v215\n\t"
            "v_mfma_f32_16x16x32_bf16 v[156:159], v[192:195], v[120:123], v[156:159]\n\t"
            "ds_read_b128 v[192:195], v232 offset:6400\n\t"
            "v_mul_f32 v218, v248, v218\n\t"
            "v_mfma_f32_16x16x32_bf16 v[144:147], v[224:227], v[8:11], v[144:147]\n\t"
            "v_mul_f32 v219, v249, v219\n\t"
            "v_mul_f32 v222, v248, v222\n\t"
            "v_mfma_f32_16x16x32_bf16 v[144:147], v[228:231], v[12:15], v[144:147]\n\t"
            "v_mul_f32 v223, v249, v223\n\t"
            "v_cvt_pk_bf16_f32 v212, v212, v213\n\t"
            "v_mfma_f32_16x16x32_bf16 v[148:151], v[228:231], v[36:39], v[148:151]\n\t"
            "ds_read_b64 v[248:249], v247 offset:72\n\t"
            "v_cvt_pk_bf16_f32 v208, v208, v209\n\t"
            "v_mfma_f32_16x16x32_bf16 v[148:151], v[224:227], v[32:35], v[148:151]\n\t"
            "v_cvt_pk_bf16_f32 v213, v214, v215\n\t"
            "v_cvt_pk_bf16_f32 v209, v210, v211\n\t"
            "v_mfma_f32_16x16x32_bf16 v[168:171], v[224:227], v[56:59], v[168:171]\n\t"
            "v_cvt_pk_bf16_f32 v200, v200, v201\n\t"
            "v_cvt_pk_bf16_f32 v204, v204, v205\n\t"
            "v_mfma_f32_16x16x32_bf16 v[168:171], v[228:231], v[60:63], v[168:171]\n\t"
            "v_cvt_pk_bf16_f32 v216, v216, v217\n\t"
            "v_cvt_pk_bf16_f32 v220, v220, v221\n\t"
            "v_mfma_f32_16x16x32_bf16 v[172:175], v[228:231], v[84:87], v[172:175]\n\t"
            "v_cvt_pk_bf16_f32 v201, v202, v203\n\t"
            "v_cvt_pk_bf16_f32 v205, v206, v207\n\t"
            "v_mfma_f32_16x16x32_bf16 v[172:175], v[224:227], v[80:83], v[172:175]\n\t"
            "v_cvt_pk_bf16_f32 v217, v218, v219\n\t"
            "v_cvt_pk_bf16_f32 v221, v222, v223\n\t"
            "v_mfma_f32_16x16x32_bf16 v[152:155], v[224:227], v[104:107], v[152:155]\n\t"
            "v_mfma_f32_16x16x32_bf16 v[152:155], v[228:231], v[108:111], v[152:155]\n\t"
            "v_mfma_f32_16x16x32_bf16 v[156:159], v[228:231], v[132:135], v[156:159]\n\t"
            "ds_read_b128 v[228:231], v233 offset:12288\n\t"
            "v_mfma_f32_16x16x32_bf16 v[156:159], v[224:227], v[128:131], v[156:159]\n\t"
            "ds_read_b128 v[224:227], v232 offset:12288\n\t"
            "s_waitcnt lgkmcnt(3)\n\t"
            "v_mfma_f32_16x16x32_bf16 v[144:147], v[192:195], v[16:19], v[144:147]\n\t"
            "v_mfma_f32_16x16x32_bf16 v[144:147], v[196:199], v[20:23], v[144:147]\n\t"
            "v_mfma_f32_16x16x32_bf16 v[148:151], v[196:199], v[44:47], v[148:151]\n\t"
            "v_mfma_f32_16x16x32_bf16 v[148:151], v[192:195], v[40:43], v[148:151]\n\t"
            "v_mfma_f32_16x16x32_bf16 v[168:171], v[192:195], v[64:67], v[168:171]\n\t"
            "v_mfma_f32_16x16x32_bf16 v[168:171], v[196:199], v[68:71], v[168:171]\n\t"
            "ds_read_b64 v[234:235], v234 offset:7856\n\t"
            "v_mfma_f32_16x16x32_bf16 v[172:175], v[196:199], v[92:95], v[172:175]\n\t"
            "s_nop 1\n\t"
            "v_mul_f32 v144, v236, v144\n\t"
            "v_mfma_f32_16x16x32_bf16 v[172:175], v[192:195], v[88:91], v[172:175]\n\t"
            "v_mul_f32 v145, v237, v145\n\t"
            "s_waitcnt lgkmcnt(3)\n\t"
            "v_mul_f32 v146, v248, v146\n\t"
            "v_mfma_f32_16x16x32_bf16 v[152:155], v[192:195], v[112:115], v[152:155]\n\t"
            "v_mul_f32 v168, v236, v168\n\t"
            "v_mul_f32 v169, v237, v169\n\t"
            "v_mfma_f32_16x16x32_bf16 v[152:155], v[196:199], v[116:119], v[152:155]\n\t"
            "v_mul_f32 v170, v248, v170\n\t"
            "v_mul_f32 v171, v249, v171\n\t"
            "v_mfma_f32_16x16x32_bf16 v[156:159], v[196:199], v[140:143], v[156:159]\n\t"
            "ds_read_b128 v[196:199], v233 offset:12416\n\t"
            "v_mul_f32 v172, v236, v172\n\t"
            "v_mfma_f32_16x16x32_bf16 v[156:159], v[192:195], v[136:139], v[156:159]\n\t"
            "ds_read_b128 v[192:195], v232 offset:12416\n\t"
            "v_mul_f32 v173, v237, v173\n\t"
            "s_waitcnt lgkmcnt(3)\n\t"
            "v_mfma_f32_16x16x32_bf16 v[160:163], v[224:227], v[0:3], 0\n\t"
            "v_cvt_pk_bf16_f32 v214, v172, v173\n\t"
            "v_cvt_pk_bf16_f32 v210, v168, v169\n\t"
            "v_mfma_f32_16x16x32_bf16 v[160:163], v[228:231], v[4:7], v[160:163]\n\t"
            "s_waitcnt lgkmcnt(2)\n\t"
            "v_readfirstlane_b32 s77, v234\n\t"
            "v_readfirstlane_b32 s38, v235\n\t"
            "s_add_u32 s77, s77, s38\n\t"
            "s_cmp_ge_u32 s77, s44\n\t"
            "s_cbranch_scc0 .Lwc_spin%=\n\t"
            ".Lwc_go%=:\n\t"
            "v_mfma_f32_16x16x32_bf16 v[164:167], v[228:231], v[28:31], 0\n\t"
            "s_add_u32 s59, s59, s84\n\t"
            "s_movk_i32 s84, 800\n\t"
            "v_cvt_pk_bf16_f32 v211, v170, v171\n\t"
            "v_mfma_f32_16x16x32_bf16 v[164:167], v[224:227], v[24:27], v[164:167]\n\t"
            "s_add_u32 s45, s46, s27\n\t"
            "s_mov_b32 s46, s47\n\t"
            "s_mov_b32 s47, s68\n\t"
            "v_mfma_f32_16x16x32_bf16 v[176:179], v[224:227], v[48:51], 0\n\t"
            "v_mul_f32 v174, v248, v174\n\t"
            "v_mul_f32 v175, v249, v175\n\t"
            "v_mfma_f32_16x16x32_bf16 v[176:179], v[228:231], v[52:55], v[176:179]\n\t"
            "v_add_u32 v172, s67, v245\n\t"
            "v_cvt_pk_bf16_f32 v215, v174, v175\n\t"
            "v_mfma_f32_16x16x32_bf16 v[180:183], v[228:231], v[76:79], 0\n\t"
            "ds_read_b128 v[168:171], v172 offset:18816\n\t"
            "ds_read_b128 v[172:175], v172 offset:19840\n\t"
            "v_mfma_f32_16x16x32_bf16 v[180:183], v[224:227], v[72:75], v[180:183]\n\t"
            "v_mul_f32 v148, v236, v148\n\t"
            "v_mul_f32 v149, v237, v149\n\t"
            "v_mfma_f32_16x16x32_bf16 v[184:187], v[224:227], v[96:99], 0\n\t"
            "v_mul_f32 v152, v236, v152\n\t"
            "v_mul_f32 v153, v237, v153\n\t"
            "v_mfma_f32_16x16x32_bf16 v[184:187], v[228:231], v[100:103], v[184:187]\n\t"
            "v_mul_f32 v156, v236, v156\n\t"
            "v_mul_f32 v157, v237, v157\n\t"
            "v_mfma_f32_16x16x32_bf16 v[188:191], v[228:231], v[124:127], 0\n\t"
            "ds_read_b128 v[228:231], v233 offset:12544\n\t"
            "ds_read_b64 v[236:237], v247 offset:128\n\t"
            "v_mfma_f32_16x16x32_bf16 v[188:191], v[224:227], v[120:123], v[188:191]\n\t"
            "ds_read_b128 v[224:227], v232 offset:12544\n\t"
            "v_mul_f32 v147, v249, v147\n\t"
            "s_waitcnt lgkmcnt(5)\n\t"
            "v_mfma_f32_16x16x32_bf16 v[160:163], v[192:195], v[8:11], v[160:163]\n\t"
            "v_mul_f32 v150, v248, v150\n\t"
            "v_mul_f32 v151, v249, v151\n\t"
            "v_mfma_f32_16x16x32_bf16 v[160:163], v[196:199], v[12:15], v[160:163]\n\t"
            "v_mul_f32 v154, v248, v154\n\t"
            "v_mul_f32 v155, v249, v155\n\t"
            "v_mfma_f32_16x16x32_bf16 v[164:167], v[196:199], v[36:39], v[164:167]\n\t"
            "v_mul_f32 v158, v248, v158\n\t"
            "v_mul_f32 v159, v249, v159\n\t"
            "v_mfma_f32_16x16x32_bf16 v[164:167], v[192:195], v[32:35], v[164:167]\n\t"
            "ds_read_b64 v[248:249], v247 offset:136\n\t"
            "s_waitcnt lgkmcnt(5)\n\t"
            "v_add_f32 v168, v168, v170\n\t"
            "v_mfma_f32_16x16x32_bf16 v[176:179], v[192:195], v[56:59], v[176:179]\n\t"
            "s_waitcnt lgkmcnt(4)\n\t"
            "v_add_f32 v172, v172, v174\n\t"
            "v_add_f32 v168, v168, v172\n\t"
            "v_mfma_f32_16x16x32_bf16 v[176:179], v[196:199], v[60:63], v[176:179]\n\t"
            "v_add_f32 v169, v169, v171\n\t"
            "v_add_f32 v173, v173, v175\n\t"
            "v_mfma_f32_16x16x32_bf16 v[180:183], v[196:199], v[84:87], v[180:183]\n\t"
            "v_mul_f32 v168, 0x3baaaaab, v168\n\t"
            "v_add_f32 v169, v169, v173\n\t"
            "v_mfma_f32_16x16x32_bf16 v[180:183], v[192:195], v[80:83], v[180:183]\n\t"
            "ds_read_b128 v[172:175], v254 offset:64\n\t"
            "v_mul_f32_e64 v168, v168, -v168\n\t"
            "v_mfma_f32_16x16x32_bf16 v[184:187], v[192:195], v[104:107], v[184:187]\n\t"
            "v_fmac_f32 v168, 0x3baaaaab, v169\n\t"
            "v_max_f32 v168, 0, v168\n\t"
            "v_mfma_f32_16x16x32_bf16 v[184:187], v[196:199], v[108:111], v[184:187]\n\t"
            "v_cvt_pk_bf16_f32 v202, v144, v145\n\t"
            "v_cvt_pk_bf16_f32 v203, v146, v147\n\t"
            "v_mfma_f32_16x16x32_bf16 v[188:191], v[196:199], v[132:135], v[188:191]\n\t"
            "v_cvt_pk_bf16_f32 v206, v148, v149\n\t"
            "v_cvt_pk_bf16_f32 v207, v150, v151\n\t"
            "v_mfma_f32_16x16x32_bf16 v[188:191], v[192:195], v[128:131], v[188:191]\n\t"
            "v_cvt_pk_bf16_f32 v218, v152, v153\n\t"
            "v_cvt_pk_bf16_f32 v219, v154, v155\n\t"
            "s_waitcnt lgkmcnt(2)\n\t"
            "v_mfma_f32_16x16x32_bf16 v[160:163], v[224:227], v[16:19], v[160:163]\n\t"
            "v_cvt_pk_bf16_f32 v222, v156, v157\n\t"
            "v_cvt_pk_bf16_f32 v223, v158, v159\n\t"
            "v_mfma_f32_16x16x32_bf16 v[160:163], v[228:231], v[20:23], v[160:163]\n\t"
            "v_add_f32 v168, 0x3727c5ac, v168\n\t"
            "v_rsq_f32 v168, v168\n\t"
            "v_mfma_f32_16x16x32_bf16 v[164:167], v[228:231], v[44:47], v[164:167]\n\t"
            "v_add_u32 v232, s67, v242\n\t"
            "v_xor_b32 v233, 64, v232\n\t"
            "v_mfma_f32_16x16x32_bf16 v[164:167], v[224:227], v[40:43], v[164:167]\n\t"
            "s_xor_b32 s48, s48, s71\n\t"
            "s_xor_b32 s67, s67, s71\n\t"
            "s_add_u32 s76, s76, 4\n\t"
            "s_add_u32 s44, s44, 8\n\t"
            "v_mfma_f32_16x16x32_bf16 v[176:179], v[224:227], v[64:67], v[176:179]\n\t"
            "s_lshl_b32 s56, s56, 2\n\t"
            "s_lshr_b32 s49, s26, 30\n\t"
            "s_or_b32 s56, s56, s49\n\t"
            "v_mfma_f32_16x16x32_bf16 v[176:179], v[228:231], v[68:71], v[176:179]\n\t"
            "ds_read_b128 v[192:195], v232\n\t"
            "ds_read_b128 v[196:199], v233\n\t"
            "v_mfma_f32_16x16x32_bf16 v[180:183], v[228:231], v[92:95], v[180:183]\n\t"
            "v_mul_f32 v160, v236, v160\n\t"
            "v_mul_f32 v161, v237, v161\n\t"
            "v_mfma_f32_16x16x32_bf16 v[180:183], v[224:227], v[88:91], v[180:183]\n\t"
            "v_mul_f32 v164, v236, v164\n\t"
            "v_mul_f32 v165, v237, v165\n\t"
            "v_mfma_f32_16x16x32_bf16 v[184:187], v[224:227], v[112:115], v[184:187]\n\t"
            "v_mul_f32 v176, v236, v176\n\t"
            "v_mul_f32 v177, v237, v177\n\t"
            "v_mfma_f32_16x16x32_bf16 v[184:187], v[228:231], v[116:119], v[184:187]\n\t"
            "ds_write_b128 v243, v[200:203]\n\t"
            "v_mfma_f32_16x16x32_bf16 v[188:191], v[228:231], v[140:143], v[188:191]\n\t"
            "ds_read_b128 v[228:231], v233 offset:128\n\t"
            "v_mul_f32 v180, v236, v180\n\t"
            "v_mfma_f32_16x16x32_bf16 v[188:191], v[224:227], v[136:139], v[188:191]\n\t"
            "ds_read_b128 v[224:227], v232 offset:128\n\t"
            "v_mul_f32 v181, v237, v181\n\t"
            "s_waitcnt lgkmcnt(3)\n\t"
            "v_mfma_f32_16x16x32_bf16 v[200:203], v[192:195], v[0:3], 0\n\t"
            "v_mul_f32 v184, v236, v184\n\t"
            "v_mul_f32 v185, v237, v185\n\t"
            "v_mfma_f32_16x16x32_bf16 v[200:203], v[196:199], v[4:7], v[200:203]\n\t"
            "ds_write_b128 v243, v[204:207] offset:1536\n\t"
            "v_mfma_f32_16x16x32_bf16 v[204:207], v[196:199], v[28:31], 0\n\t"
            "v_mul_f32 v188, v236, v188\n\t"
            "v_mul_f32 v189, v237, v189\n\t"
            "v_mfma_f32_16x16x32_bf16 v[204:207], v[192:195], v[24:27], v[204:207]\n\t"
            "ds_write_b128 v243, v[208:211] offset:3072\n\t"
            "v_mfma_f32_16x16x32_bf16 v[208:211], v[192:195], v[48:51], 0\n\t"
            "v_mul_f32 v162, v248, v162\n\t"
            "v_mul_f32 v163, v249, v163\n\t"
            "v_mfma_f32_16x16x32_bf16 v[208:211], v[196:199], v[52:55], v[208:211]\n\t"
            "ds_write_b128 v243, v[212:215] offset:4608\n\t"
            "v_mfma_f32_16x16x32_bf16 v[212:215], v[196:199], v[76:79], 0\n\t"
            "v_cvt_pk_bf16_f32 v160, v160, v161\n\t"
            "v_mul_f32 v166, v248, v166\n\t"
            "v_mfma_f32_16x16x32_bf16 v[212:215], v[192:195], v[72:75], v[212:215]\n\t"
            "ds_write_b128 v243, v[216:219] offset:6144\n\t"
            "v_mfma_f32_16x16x32_bf16 v[216:219], v[192:195], v[96:99], 0\n\t"
            "v_mul_f32 v167, v249, v167\n\t"
            "v_cvt_pk_bf16_f32 v164, v164, v165\n\t"
            "v_mfma_f32_16x16x32_bf16 v[216:219], v[196:199], v[100:103], v[216:219]\n\t"
            "ds_write_b128 v243, v[220:223] offset:7680\n\t"
            "v_mfma_f32_16x16x32_bf16 v[220:223], v[196:199], v[124:127], 0\n\t"
            "v_mul_f32 v178, v248, v178\n\t"
            "v_mul_f32 v179, v249, v179\n\t"
            "v_mfma_f32_16x16x32_bf16 v[220:223], v[192:195], v[120:123], v[220:223]\n\t"
            "v_cvt_pk_bf16_f32 v176, v176, v177\n\t"
            "v_mul_f32 v182, v248, v182\n\t"
            "s_waitcnt lgkmcnt(5)\n\t"
            "v_mfma_f32_16x16x32_bf16 v[200:203], v[224:227], v[8:11], v[200:203]\n\t"
            "s_waitcnt vmcnt(0)\n\t"
            "ds_read_b64 v[144:145], v253\n\t"
            "ds_read_b64 v[146:147], v253 offset:96\n\t"
            "v_mfma_f32_16x16x32_bf16 v[200:203], v[228:231], v[12:15], v[200:203]\n\t"
            "ds_read_b64 v[148:149], v253 offset:1744\n\t"
            "ds_read_b64 v[150:151], v253 offset:1840\n\t"
            "v_mfma_f32_16x16x32_bf16 v[204:207], v[228:231], v[36:39], v[204:207]\n\t"
            "ds_read_b64 v[152:153], v253 offset:8\n\t"
            "ds_read_b64 v[154:155], v253 offset:104\n\t"
            "v_mfma_f32_16x16x32_bf16 v[204:207], v[224:227], v[32:35], v[204:207]\n\t"
            "ds_read_b64 v[156:157], v253 offset:1752\n\t"
            "ds_read_b64 v[158:159], v253 offset:1848\n\t"
            "v_mfma_f32_16x16x32_bf16 v[208:211], v[224:227], v[56:59], v[208:211]\n\t"
            "v_mul_f32 v183, v249, v183\n\t"
            "v_cvt_pk_bf16_f32 v180, v180, v181\n\t"
            "v_mfma_f32_16x16x32_bf16 v[208:211], v[228:231], v[60:63], v[208:211]\n\t"
            "v_mul_f32 v186, v248, v186\n\t"
            "v_mul_f32 v187, v249, v187\n\t"
            "v_mfma_f32_16x16x32_bf16 v[212:215], v[228:231], v[84:87], v[212:215]\n\t"
            "v_cvt_pk_bf16_f32 v184, v184, v185\n\t"
            "v_mul_f32 v190, v248, v190\n\t"
            "v_mfma_f32_16x16x32_bf16 v[212:215], v[224:227], v[80:83], v[212:215]\n\t"
            "v_mul_f32 v191, v249, v191\n\t"
            "v_cvt_pk_bf16_f32 v188, v188, v189\n\t"
            "v_mfma_f32_16x16x32_bf16 v[216:219], v[224:227], v[104:107], v[216:219]\n\t"
            "v_cvt_pk_bf16_f32 v161, v162, v163\n\t"
            "v_cvt_pk_bf16_f32 v165, v166, v167\n\t"
            "v_mfma_f32_16x16x32_bf16 v[216:219], v[228:231], v[108:111], v[216:219]\n\t"
            "ds_read_b64 v[162:163], v253 offset:112\n\t"
            "ds_read_b64 v[166:167], v253 offset:1856\n\t"
            "v_mfma_f32_16x16x32_bf16 v[220:223], v[228:231], v[132:135], v[220:223]\n\t"
            "s_waitcnt lgkmcnt(14)\n\t"
            "ds_read_b128 v[228:231], v233 offset:256\n\t"
            "v_cvt_pk_bf16_f32 v177, v178, v179\n\t"
            "v_mfma_f32_16x16x32_bf16 v[220:223], v[224:227], v[128:131], v[220:223]\n\t"
            "s_waitcnt lgkmcnt(14)\n\t"
            "ds_read_b128 v[224:227], v232 offset:256\n\t"
            "v_cvt_pk_bf16_f32 v181, v182, v183\n\t"
            "v_cvt_pk_bf16_f32 v185, v186, v187\n\t"
            "v_cvt_pk_bf16_f32 v189, v190, v191\n\t"
            "v_xor_b32 v247, 0x100, v247\n\t"
            "s_waitcnt lgkmcnt(14)\n\t"
            "ds_write_b64 v244, v[160:161]\n\t"
            "s_waitcnt lgkmcnt(14)\n\t"
            "ds_read_b64 v[160:161], v253 offset:16\n\t"
            "s_waitcnt lgkmcnt(14)\n\t"
            "ds_write_b64 v244, v[164:165] offset:1536\n\t"
            "s_waitcnt lgkmcnt(14)\n\t"
            "ds_read_b64 v[164:165], v253 offset:1760\n\t"
            "s_waitcnt lgkmcnt(14)\n\t"
            "ds_write_b64 v244, v[176:177] offset:3072\n\t"
            "s_waitcnt lgkmcnt(14)\n\t"
            "ds_write_b64 v244, v[180:181] offset:4608\n\t"
            "s_waitcnt lgkmcnt(14)\n\t"
            "ds_write_b64 v244, v[184:185] offset:6144\n\t"
            "s_waitcnt lgkmcnt(14)\n\t"
            "ds_write_b64 v244, v[188:189] offset:7680\n\t"
            "s_waitcnt lgkmcnt(14)\n\t"
            "ds_write_b32 v246, v168\n\t"
            "s_waitcnt lgkmcnt(14)\n\t"
            "ds_read_b128 v[168:171], v254\n\t"
            "s_waitcnt lgkmcnt(1)\n\t"
            "s_barrier\n\t"
            "s_cmp_lt_u32 s46, 0x7000000\n\t"
            "s_cbranch_scc1 .Lwc_loop%=\n\t"
            "s_branch .Lwc_end%=\n\t"
            ".Lwc_spin%=:\n\t"
            "s_cmp_eq_u32 s83, 0\n\t"
            "s_cbranch_scc1 .Lwc_go%=\n\t"
            "s_sub_u32 s83, s83, 1\n\t"
            "s_sleep 2\n\t"
            "v_mov_b32 v234, 0\n\t"
            "ds_read_b64 v[234:235], v234 offset:7856\n\t"
            "s_waitcnt lgkmcnt(0)\n\t"
            "v_readfirstlane_b32 s77, v234\n\t"
            "v_readfirstlane_b32 s38, v235\n\t"
            "s_add_u32 s77, s77, s38\n\t"
            "s_cmp_ge_u32 s77, s44\n\t"
            "s_cbranch_scc0 .Lwc_spin%=\n\t"
            "s_branch .Lwc_go%=\n\t"
            ".Lwc_end%=:\n\t"
            "s_waitcnt lgkmcnt(0)\n\t"
            "s_nop 7\n\t"
            : "+{v246}"(aRSw_), "+{v247}"(aRSr_), "+{s44}"(s44), B_SIO
            : "{v242}"(pE_), "{v243}"(aVAw_), "{v244}"(aVBw_), "{v245}"(pST_), "{v250}"(pXw_), "{v251}"(pSTw_), "{v252}"(aCA_), "{v253}"(aB_), "{v254}"(aA_), "{v255}"(idx_), B_SIN, "{s82}"(touch)
            : "v144", "v145", "v146", "v147", "v148", "v149", "v150", "v151", "v152", "v153", "v154", "v155", "v156", "v157", "v158", "v159", "v160", "v161", "v162", "v163", "v164", "v165", "v166", "v167", "v168", "v169", "v170", "v171", "v172", "v173", "v174", "v175", "v176", "v177", "v178", "v179", "v180", "v181", "v182", "v183", "v184", "v185", "v186", "v187", "v188", "v189", "v190", "v191", "v192", "v193", "v194", "v195", "v196", "v197", "v198", "v199", "v236", "v237", "v248", "v249", "v0", "v1", "v2", "v3", "v4", "v5", "v6", "v7", "v8", "v9", "v10", "v11", "v12", "v13", "v14", "v15", "v16", "v17", "v18", "v19", "v20", "v21", "v22", "v23", "v24", "v25", "v26", "v27", "v28", "v29", "v30", "v31", "v32", "v33", "v34", "v35", "v36", "v37", "v38", "v39", "v40", "v41", "v42", "v43", "v44", "v45", "v46", "v47", "v48", "v49", "v50", "v51", "v52", "v53", "v54", "v55", "v56", "v57", "v58", "v59", "v60", "v61", "v62", "v63", "v64", "v65", "v66", "v67", "v68", "v69", "v70", "v71", "v72", "v73", "v74", "v75", "v76", "v77", "v78", "v79", "v80", "v81", "v82", "v83", "v84", "v85", "v86", "v87", "v88", "v89", "v90", "v91", "v92", "v93", "v94", "v95", "v96", "v97", "v98", "v99", "v100", "v101", "v102", "v103", "v104", "v105", "v106", "v107", "v108", "v109", "v110", "v111", "v112", "v113", "v114", "v115", "v116", "v117", "v118", "v119", "v120", "v121", "v122", "v123", "v124", "v125", "v126", "v127", "v128", "v129", "v130", "v131", "v132", "v133", "v134", "v135", "v136", "v137", "v138", "v139", "v140", "v141", "v142", "v143", "v200", "v201", "v202", "v203", "v204", "v205", "v206", "v207", "v208", "v209", "v210", "v211", "v212", "v213", "v214", "v215", "v216", "v217", "v218", "v219", "v220", "v221", "v222", "v223", "v224", "v225", "v226", "v227", "v228", "v229", "v230", "v231", "v232", "v233", "v234", "v235", B_SCLOB);
        }
        {
            __builtin_amdgcn_s_setprio(3);
            int ln_;
            asm volatile("v_mbcnt_lo_u32_b32 %0, -1, 0\n\tv_mbcnt_hi_u32_b32 %0, -1, %0" : "=v"(ln_));
            const int l15_ = ln_ & 15, g_ = ln_ >> 4;
            KARGS
            const auto orsrc = __builtin_amdgcn_make_buffer_rsrc((void*)ob, 0, IMG * IMG * CH * 2, 0x00020000);
            const auto wq = __builtin_amdgcn_make_buffer_rsrc((void*)wb, 0, WS_BYTES, 0x00020000);
            bf16x8 W[36];
#pragma unroll
            for (int ks = 0; ks < 6; ++ks)
#pragma unroll
                for (int j = 0; j < 6; ++j) W[j * 6 + ks] = BC8(__builtin_amdgcn_raw_buffer_load_b128(wq, ln_ * 16, (wave + 2) * 36864 + (j * 6 + ks) * 1024, SC1));
            SB();
            int lE[2], nw;
            unsigned o[2];
            f32x4 acc[2][6];
#pragma unroll
            for (int lt = 0; lt < 2; ++lt) {
                int i = 16 * lt + l15_;
                const unsigned en = *(const unsigned*)(smem + WL_OFF + i * 4);
                const bool ok = en != 0x7ffffffu;
                if (lt == 1) nw = 16 + __builtin_popcount((unsigned)__builtin_amdgcn_ballot_w64(ok) & 0xffffu);
                i = ok ? i : 0;
                lE[lt] = OL_OFF + i * 384 + ((g_ ^ ((i >> 1) & 7)) << 4);
                o[lt] = ok ? (en & 0x7ffffffu) + LPOS(en >> 31) * ROWBYTES + LPOS(en >> 30) * (CH * 2) + 192 * w + 16 * g_ : OOB;
#pragma unroll
                for (int k = 0; k < 3; ++k) {
                    const int c8 = 12 * w + 4 * k + g_, ws = (c8 * 43) >> 8, r6 = c8 - 6 * ws;
                    const u32x4 r = *(const u32x4*)(smem + LX_OFF(i) + (r6 >> 1) * 128 + ((2 * ws + (r6 & 1)) << 4));
                    acc[lt][2 * k] = (f32x4){bflo(r[0]), bfhi(r[0]), bflo(r[1]), bfhi(r[1])};
                    acc[lt][2 * k + 1] = (f32x4){bflo(r[2]), bfhi(r[2]), bflo(r[3]), bfhi(r[3])};
                }
            }
            SB();
            for (int it_ = 0; it_ < 100000; ++it_) {
                unsigned n_;
                asm volatile("ds_read_b32 %0, %1\n\ts_waitcnt lgkmcnt(0)" : "=v"(n_) : "v"(ldsb + FLAG_OFF) : "memory");
                if (__builtin_amdgcn_readfirstlane(n_) >= 4 * nw) break;
                __builtin_amdgcn_s_sleep(1);
            }
            bf16x8 of[2][6];
#pragma unroll
            for (int ks = 0; ks < 6; ++ks)
#pragma unroll
                for (int lt = 0; lt < 2; ++lt) of[lt][ks] = *(const bf16x8*)(smem + ((ks & 1) ? (lE[lt] ^ 64) : lE[lt]) + (ks >> 1) * 128);
            SB();
#pragma unroll
            for (int lt = 0; lt < 2; ++lt) {
#pragma unroll
                for (int ks = 0; ks < 6; ++ks)
#pragma unroll
                    for (int j = 0; j < 6; ++j) acc[lt][j] = MFMA32(W[j * 6 + ks], of[lt][ks], acc[lt][j]);
#pragma unroll
                for (int k = 0; k < 3; ++k) {
                    const f32x4 a = acc[lt][2 * k], b = acc[lt][2 * k + 1];
                    const u32x4 ov = {pk2(a[0], a[1]), pk2(a[2], a[3]), pk2(b[0], b[1]), pk2(b[2], b[3])};
                    __builtin_amdgcn_raw_buffer_store_b128(ov, orsrc, o[lt] + 64 * k, 0, SC1);
                }
            }
        }
        __builtin_amdgcn_endpgm();
    } else {
        LANE_SETUP
        {
            const unsigned oE_ = ldsb + fE + (O_OFF - X_OFF), oO_ = oE_ ^ 64;
            unsigned l16_ = lane * 16;
            unsigned pr[3];
#pragma unroll
            for (int k = 0; k < 3; ++k) {
                const int c8 = 12 * (w - 2) + 4 * k + g;
                const int ws = c8 / 6, r6 = c8 - 6 * ws;
                pr[k] = l15 * 384 + (r6 >> 1) * 128 + (((2 * ws + (r6 & 1)) ^ sw) << 4);
            }
            const unsigned pR0_ = pr[0], pR1_ = pr[1], pR2_ = pr[2];
            unsigned q0 = ldsb + PF_OFF + 1024 + lane * 4, q1 = ldsb + O_OFF + lane * 16, q2 = oE_, q3 = ldsb + PF_OFF + 1024 + l15 * 4;
            unsigned q4 = ldsb + OL_OFF + l15 * 384 + (((4 * (w - 2) + (g >> 1)) ^ sw) << 4) + 8 * (g & 1);
            B_SCALARS
            asm volatile(
            "s_waitcnt lgkmcnt(0)\n\t"
            "s_mov_b32 s83, 0x10000\n\t"
            "s_mov_b32 s84, -47104\n\t"
            "s_mov_b64 s[88:89], s[72:73]\n\t"
            "s_mov_b64 s[90:91], s[74:75]\n\t"
            "s_nop 7\n\t"
            "s_barrier\n\t"
            "ds_read_b64 v[236:237], v252 offset:2560\n\t"
            "s_waitcnt lgkmcnt(0)\n\t"
            "s_mov_b64 exec, 1\n\t"
            "buffer_store_dwordx2 v[236:237], off, s[28:31], s44 sc1\n\t"
            "s_mov_b64 exec, -1\n\t"
            "s_sleep 30\n\t"
            "ds_read_b32 v249, v252 offset:12\n\t"
            "s_waitcnt lgkmcnt(0)\n\t"
            "v_and_b32 v249, s69, v249\n\t"
            "s_mov_b32 exec_hi, 0x3fffff\n\t"
            "s_add_u32 m0, s66, 0\n\t"
            "s_add_u32 s49, s79, 0\n\t"
            "buffer_load_dwordx4 v249, s[72:75], s49 offen lds\n\t"
            "s_mov_b32 exec_hi, -1\n\t"
            "s_mov_b32 exec_hi, 0x3fffff\n\t"
            "s_add_u32 m0, s66, 872\n\t"
            "s_add_u32 s49, s47, 196224\n\t"
            "buffer_load_dwordx4 v249, s[72:75], s49 offen lds\n\t"
            "s_mov_b32 exec_hi, -1\n\t"
            "s_mov_b32 exec_hi, 0x3fffff\n\t"
            "s_add_u32 m0, s66, 1744\n\t"
            "s_add_u32 s49, s47, 392448\n\t"
            "buffer_load_dwordx4 v249, s[72:75], s49 offen lds\n\t"
            "s_mov_b32 exec_hi, -1\n\t"
            "s_mov_b32 exec_hi, 0x3fffff\n\t"
            "s_add_u32 m0, s66, 2616\n\t"
            "s_add_u32 s49, s47, 588672\n\t"
            "buffer_load_dwordx4 v249, s[72:75], s49 offen lds\n\t"
            "s_mov_b32 exec_hi, -1\n\t"
            "s_mov_b32 exec_hi, 0x3fffff\n\t"
            "s_add_u32 m0, s66, 3488\n\t"
            "s_add_u32 s49, s47, 784896\n\t"
            "buffer_load_dwordx4 v249, s[72:75], s49 offen lds\n\t"
            "s_mov_b32 exec_hi, -1\n\t"
            "s_mov_b32 exec_hi, 0x3fffff\n\t"
            "s_add_u32 m0, s66, 4360\n\t"
            "s_add_u32 s49, s47, 981120\n\t"
            "buffer_load_dwordx4 v249, s[72:75], s49 offen lds\n\t"
            "s_mov_b32 exec_hi, -1\n\t"
            "s_mov_b32 exec_hi, 0x3fffff\n\t"
            "s_add_u32 m0, s66, 5232\n\t"
            "s_add_u32 s49, s47, 1177344\n\t"
            "buffer_load_dwordx4 v249, s[72:75], s49 offen lds\n\t"
            "s_mov_b32 exec_hi, -1\n\t"
            "s_mov_b32 exec_hi, 0x3fffff\n\t"
            "s_add_u32 m0, s66, 6104\n\t"
            "s_add_u32 s49, s47, 1373568\n\t"
            "buffer_load_dwordx4 v249, s[72:75], s49 offen lds\n\t"
            "s_mov_b32 exec_hi, -1\n\t"
            "s_mov_b32 exec_hi, 0x3fffff\n\t"
            "s_add_u32 m0, s66, 6976\n\t"
            "s_add_u32 s49, s47, 1569792\n\t"
            "buffer_load_dwordx4 v249, s[72:75], s49 offen lds\n\t"
            "s_mov_b32 exec_hi, -1\n\t"
            "s_add_u32 s49, s33, -73728\n\t"
            "buffer_load_dwordx4 v[0:3], v247, s[28:31], s49 offen sc1\n\t"
            "s_add_u32 s49, s33, -67584\n\t"
            "buffer_load_dwordx4 v[24:27], v247, s[28:31], s49 offen sc1\n\t"
            "s_add_u32 s49, s33, -61440\n\t"
            "buffer_load_dwordx4 v[48:51], v247, s[28:31], s49 offen sc1\n\t"
            "s_add_u32 s49, s33, -55296\n\t"
            "buffer_load_dwordx4 v[72:75], v247, s[28:31], s49 offen sc1\n\t"
            "s_add_u32 s49, s33, -49152\n\t"
            "buffer_load_dwordx4 v[96:99], v247, s[28:31], s49 offen sc1\n\t"
            "s_add_u32 s49, s33, -43008\n\t"
            "buffer_load_dwordx4 v[120:123], v247, s[28:31], s49 offen sc1\n\t"
            "s_add_u32 s49, s33, -72704\n\t"
            "buffer_load_dwordx4 v[4:7], v247, s[28:31], s49 offen sc1\n\t"
            "s_add_u32 s49, s33, -66560\n\t"
            "buffer_load_dwordx4 v[28:31], v247, s[28:31], s49 offen sc1\n\t"
            "s_add_u32 s49, s33, -60416\n\t"
            "buffer_load_dwordx4 v[52:55], v247, s[28:31], s49 offen sc1\n\t"
            "s_add_u32 s49, s33, -54272\n\t"
            "buffer_load_dwordx4 v[76:79], v247, s[28:31], s49 offen sc1\n\t"
            "s_add_u32 s49, s33, -48128\n\t"
            "buffer_load_dwordx4 v[100:103], v247, s[28:31], s49 offen sc1\n\t"
            "s_add_u32 s49, s33, -41984\n\t"
            "buffer_load_dwordx4 v[124:127], v247, s[28:31], s49 offen sc1\n\t"
            "s_add_u32 s49, s33, -71680\n\t"
            "buffer_load_dwordx4 v[8:11], v247, s[28:31], s49 offen sc1\n\t"
            "s_add_u32 s49, s33, -65536\n\t"
            "buffer_load_dwordx4 v[32:35], v247, s[28:31], s49 offen sc1\n\t"
            "s_add_u32 s49, s33, -59392\n\t"
            "buffer_load_dwordx4 v[56:59], v247, s[28:31], s49 offen sc1\n\t"
            "s_add_u32 s49, s33, -53248\n\t"
            "buffer_load_dwordx4 v[80:83], v247, s[28:31], s49 offen sc1\n\t"
            "s_add_u32 s49, s33, -47104\n\t"
            "buffer_load_dwordx4 v[104:107], v247, s[28:31], s49 offen sc1\n\t"
            "s_add_u32 s49, s33, -40960\n\t"
            "buffer_load_dwordx4 v[128:131], v247, s[28:31], s49 offen sc1\n\t"
            "s_add_u32 s49, s33, -70656\n\t"
            "buffer_load_dwordx4 v[12:15], v247, s[28:31], s49 offen sc1\n\t"
            "s_add_u32 s49, s33, -64512\n\t"
            "buffer_load_dwordx4 v[36:39], v247, s[28:31], s49 offen sc1\n\t"
            "s_add_u32 s49, s33, -58368\n\t"
            "buffer_load_dwordx4 v[60:63], v247, s[28:31], s49 offen sc1\n\t"
            "s_add_u32 s49, s33, -52224\n\t"
            "buffer_load_dwordx4 v[84:87], v247, s[28:31], s49 offen sc1\n\t"
            "s_add_u32 s49, s33, -46080\n\t"
            "buffer_load_dwordx4 v[108:111], v247, s[28:31], s49 offen sc1\n\t"
            "s_add_u32 s49, s33, -39936\n\t"
            "buffer_load_dwordx4 v[132:135], v247, s[28:31], s49 offen sc1\n\t"
            "s_add_u32 s49, s33, -69632\n\t"
            "buffer_load_dwordx4 v[16:19], v247, s[28:31], s49 offen sc1\n\t"
            "s_add_u32 s49, s33, -63488\n\t"
            "buffer_load_dwordx4 v[40:43], v247, s[28:31], s49 offen sc1\n\t"
            "s_add_u32 s49, s33, -57344\n\t"
            "buffer_load_dwordx4 v[64:67], v247, s[28:31], s49 offen sc1\n\t"
            "s_add_u32 s49, s33, -51200\n\t"
            "buffer_load_dwordx4 v[88:91], v247, s[28:31], s49 offen sc1\n\t"
            "s_add_u32 s49, s33, -45056\n\t"
            "buffer_load_dwordx4 v[112:115], v247, s[28:31], s49 offen sc1\n\t"
            "s_add_u32 s49, s33, -38912\n\t"
            "buffer_load_dwordx4 v[136:139], v247, s[28:31], s49 offen sc1\n\t"
            "s_add_u32 s49, s33, -68608\n\t"
            "buffer_load_dwordx4 v[20:23], v247, s[28:31], s49 offen sc1\n\t"
            "s_add_u32 s49, s33, -62464\n\t"
            "buffer_load_dwordx4 v[44:47], v247, s[28:31], s49 offen sc1\n\t"
            "s_add_u32 s49, s33, -56320\n\t"
            "buffer_load_dwordx4 v[68:71], v247, s[28:31], s49 offen sc1\n\t"
            "s_add_u32 s49, s33, -50176\n\t"
            "buffer_load_dwordx4 v[92:95], v247, s[28:31], s49 offen sc1\n\t"
            "s_add_u32 s49, s33, -44032\n\t"
            "buffer_load_dwordx4 v[116:119], v247, s[28:31], s49 offen sc1\n\t"
            "s_add_u32 s49, s33, -37888\n\t"
            "buffer_load_dwordx4 v[140:143], v247, s[28:31], s49 offen sc1\n\t"
            "s_setprio 3\n\t"
            "ds_read_b128 v[168:171], v254\n\t"
            "ds_read_b128 v[172:175], v254 offset:64\n\t"
            "ds_read_b128 v[220:223], v229 offset:12288\n\t"
            "ds_read_b128 v[224:227], v229 offset:13312\n\t"
            "ds_read_b128 v[200:203], v232\n\t"
            "ds_read_b128 v[208:211], v232 offset:128\n\t"
            "ds_read_b128 v[216:219], v232 offset:256\n\t"
            "s_waitcnt vmcnt(36)\n\t"
            "ds_read_b64 v[144:145], v253\n\t"
            "ds_read_b64 v[146:147], v253 offset:96\n\t"
            "ds_read_b64 v[148:149], v253 offset:1744\n\t"
            "ds_read_b64 v[150:151], v253 offset:1840\n\t"
            "ds_read_b64 v[152:153], v253 offset:8\n\t"
            "ds_read_b64 v[154:155], v253 offset:104\n\t"
            "ds_read_b64 v[156:157], v253 offset:1752\n\t"
            "ds_read_b64 v[158:159], v253 offset:1848\n\t"
            "s_waitcnt lgkmcnt(14)\n\t"
            "ds_read_b64 v[160:161], v253 offset:16\n\t"
            "s_waitcnt lgkmcnt(14)\n\t"
            "ds_read_b64 v[162:163], v253 offset:112\n\t"
            "s_waitcnt lgkmcnt(14)\n\t"
            "ds_read_b64 v[164:165], v253 offset:1760\n\t"
            "s_waitcnt lgkmcnt(14)\n\t"
            "ds_read_b64 v[166:167], v253 offset:1856\n\t"
            "v_mov_b64 v[176:177], 0\n\t"
            "v_mov_b64 v[178:179], 0\n\t"
            "v_mov_b64 v[180:181], 0\n\t"
            "v_mov_b64 v[182:183], 0\n\t"
            "v_mov_b32 v249, s76\n\t"
            "v_mov_b64 v[184:185], 0\n\t"
            "v_mov_b64 v[186:187], 0\n\t"
            "v_mov_b64 v[188:189], 0\n\t"
            "v_mov_b64 v[190:191], 0\n\t"
            "v_add_u32 v236, s67, v250\n\t"
            "v_add_u32 v248, s67, v251\n\t"
            "v_xor_b32 v235, 64, v232\n\t"
            "s_nop 0\n\t"
            "s_waitcnt lgkmcnt(14)\n\t"
            "ds_read_b32 v249, v249\n\t"
            "s_waitcnt lgkmcnt(14)\n\t"
            "ds_read_b128 v[204:207], v235\n\t"
            "s_waitcnt lgkmcnt(14)\n\t"
            "ds_read_b128 v[212:215], v235 offset:128\n\t"
            "v_add_f32 v220, v220, v222\n\t"
            "v_add_f32 v224, v224, v226\n\t"
            "v_add_f32 v220, v220, v224\n\t"
            "v_add_f32 v221, v221, v223\n\t"
            "v_add_f32 v225, v225, v227\n\t"
            "v_mul_f32 v220, 0x3baaaaab, v220\n\t"
            "v_add_f32 v221, v221, v225\n\t"
            "v_mul_f32_e64 v220, v220, -v220\n\t"
            "v_fmac_f32 v220, 0x3baaaaab, v221\n\t"
            "s_waitcnt lgkmcnt(11)\n\t"
            "v_smfmac_f32_16x16x64_bf16 v[176:179], v[168:171], v[144:151], v255\n\t"
            "ds_read_b128 v[168:171], v254 offset:128\n\t"
            "s_waitcnt lgkmcnt(8)\n\t"
            "v_smfmac_f32_16x16x64_bf16 v[180:183], v[172:175], v[152:159], v255\n\t"
            "ds_read_b64 v[144:145], v253 offset:24\n\t"
            "ds_read_b64 v[146:147], v253 offset:120\n\t"
            "ds_read_b64 v[148:149], v253 offset:1768\n\t"
            "ds_read_b64 v[150:151], v253 offset:1864\n\t"
            "ds_read_b128 v[172:175], v254 offset:192\n\t"
            "ds_read_b64 v[152:153], v253 offset:32\n\t"
            "ds_read_b64 v[154:155], v253 offset:128\n\t"
            "s_waitcnt lgkmcnt(14)\n\t"
            "ds_read_b64 v[156:157], v253 offset:1776\n\t"
            "s_waitcnt lgkmcnt(14)\n\t"
            "ds_read_b64 v[158:159], v253 offset:1872\n\t"
            "s_waitcnt lgkmcnt(14)\n\t"
            "ds_read_b128 v[224:227], v232 offset:6144\n\t"
            "v_cvt_pk_bf16_f32 v176, v176, v177\n\t"
            "v_cvt_pk_bf16_f32 v177, v178, v179\n\t"
            "v_cvt_pk_bf16_f32 v178, v180, v181\n\t"
            "v_cvt_pk_bf16_f32 v179, v182, v183\n\t"
            "v_mfma_f32_4x4x4_16b_bf16 v[196:199], v[176:177], v[176:177], 0\n\t"
            "v_mov_b64 v[180:181], 0\n\t"
            "v_mfma_f32_16x16x32_bf16 v[192:195], v[238:241], v[176:179], 0\n\t"
            "s_waitcnt lgkmcnt(13)\n\t"
            "v_readfirstlane_b32 s26, v249\n\t"
            "v_mov_b64 v[182:183], 0\n\t"
            "v_mfma_f32_4x4x4_16b_bf16 v[196:199], v[178:179], v[178:179], v[196:199]\n\t"
            "ds_read_b32 v249, v252 offset:12\n\t"
            "s_and_b32 s68, s26, 0x7ffffff\n\t"
            "s_add_u32 s68, s68, s78\n\t"
            "s_and_b32 s69, s26, 0x38000000\n\t"
            "s_or_b32 s69, s69, s39\n\t"
            "s_bitcmp1_b32 s26, 27\n\t"
            "s_cselect_b32 s79, 2.0, s68\n\t"
            "s_and_b32 s57, s56, 3\n\t"
            "s_lshl_b32 s58, s57, 3\n\t"
            "s_lshr_b32 m0, s54, s58\n\t"
            "s_lshl_b32 s58, s57, 4\n\t"
            "s_lshr_b64 s[36:37], s[50:51], s58\n\t"
            "s_and_b32 s57, s36, 0xffff\n\t"
            "s_add_u32 s57, s57, s67\n\t"
            "v_writelane_b32 v236, s57, 63\n\t"
            "v_writelane_b32 v236, s59, m0\n\t"
            "v_xor_b32 v237, 16, v236\n\t"
            "s_lshr_b64 s[36:37], s[52:53], s58\n\t"
            "s_and_b32 s58, s36, 0xffff\n\t"
            "s_add_u32 s58, s58, s67\n\t"
            "s_waitcnt lgkmcnt(11)\n\t"
            "v_smfmac_f32_16x16x64_bf16 v[184:187], v[168:171], v[160:167], v255\n\t"
            "s_and_b32 s38, s52, 0xffff\n\t"
            "s_add_u32 s38, s38, s67\n\t"
            "ds_read_b128 v[168:171], v254 offset:256\n\t"
            "ds_read_b64 v[160:161], v253 offset:40\n\t"
            "ds_read_b64 v[162:163], v253 offset:136\n\t"
            "ds_read_b64 v[164:165], v253 offset:1784\n\t"
            "s_waitcnt lgkmcnt(14)\n\t"
            "ds_read_b64 v[166:167], v253 offset:1880\n\t"
            "s_waitcnt lgkmcnt(11)\n\t"
            "v_smfmac_f32_16x16x64_bf16 v[188:191], v[172:175], v[144:151], v255\n\t"
            "ds_read_b128 v[172:175], v254 offset:320\n\t"
            "ds_read_b64 v[144:145], v253 offset:48\n\t"
            "ds_read_b64 v[146:147], v253 offset:144\n\t"
            "ds_read_b64 v[148:149], v253 offset:1792\n\t"
            "s_waitcnt lgkmcnt(14)\n\t"
            "ds_read_b64 v[150:151], v253 offset:1888\n\t"
            "s_waitcnt lgkmcnt(14)\n\t"
            "ds_write_b128 v236, v[176:179]\n\t"
            "v_mov_b64 v[176:177], 0\n\t"
            "v_mov_b64 v[178:179], 0\n\t"
            "v_cvt_pk_bf16_f32 v184, v184, v185\n\t"
            "v_cvt_pk_bf16_f32 v185, v186, v187\n\t"
            "v_cvt_pk_bf16_f32 v186, v188, v189\n\t"
            "v_cvt_pk_bf16_f32 v187, v190, v191\n\t"
            "v_mfma_f32_4x4x4_16b_bf16 v[196:199], v[184:185], v[184:185], v[196:199]\n\t"
            "s_waitcnt lgkmcnt(14)\n\t"
            "ds_write_b128 v237, v[184:187]\n\t"
            "v_mfma_f32_16x16x32_bf16 v[192:195], v[238:241], v[184:187], v[192:195]\n\t"
            "v_mov_b64 v[184:185], 0\n\t"
            "v_mov_b64 v[188:189], 0\n\t"
            "v_mfma_f32_4x4x4_16b_bf16 v[196:199], v[186:187], v[186:187], v[196:199]\n\t"
            "v_mov_b64 v[186:187], 0\n\t"
            "v_mov_b64 v[190:191], 0\n\t"
            "v_writelane_b32 v248, s58, 63\n\t"
            "v_writelane_b32 v248, s38, m0\n\t"
            "s_waitcnt lgkmcnt(12)\n\t"
            "v_and_b32 v249, s69, v249\n\t"
            "v_max_f32 v220, 0, v220\n\t"
            "v_add_f32 v220, 0x3727c5ac, v220\n\t"
            "v_rsq_f32 v220, v220\n\t"
            "s_waitcnt lgkmcnt(11)\n\t"
            "v_smfmac_f32_16x16x64_bf16 v[176:179], v[168:171], v[152:159], v255\n\t"
            "ds_read_b128 v[168:171], v254 offset:384\n\t"
            "ds_read_b64 v[152:153], v253 offset:56\n\t"
            "ds_read_b64 v[154:155], v253 offset:152\n\t"
            "ds_read_b64 v[156:157], v253 offset:1800\n\t"
            "s_waitcnt lgkmcnt(14)\n\t"
            "ds_read_b64 v[158:159], v253 offset:1896\n\t"
            "s_waitcnt lgkmcnt(11)\n\t"
            "v_smfmac_f32_16x16x64_bf16 v[180:183], v[172:175], v[160:167], v255\n\t"
            "ds_read_b128 v[172:175], v254 offset:448\n\t"
            "ds_read_b64 v[160:161], v253 offset:64\n\t"
            "ds_read_b64 v[162:163], v253 offset:160\n\t"
            "ds_read_b64 v[164:165], v253 offset:1808\n\t"
            "s_waitcnt lgkmcnt(14)\n\t"
            "ds_read_b64 v[166:167], v253 offset:1904\n\t"
            "v_cvt_pk_bf16_f32 v176, v176, v177\n\t"
            "v_cvt_pk_bf16_f32 v177, v178, v179\n\t"
            "v_cvt_pk_bf16_f32 v178, v180, v181\n\t"
            "v_cvt_pk_bf16_f32 v179, v182, v183\n\t"
            "v_mfma_f32_4x4x4_16b_bf16 v[196:199], v[176:177], v[176:177], v[196:199]\n\t"
            "s_waitcnt lgkmcnt(14)\n\t"
            "ds_write_b128 v236, v[176:179] offset:128\n\t"
            "v_mfma_f32_16x16x32_bf16 v[192:195], v[238:241], v[176:179], v[192:195]\n\t"
            "v_mov_b64 v[176:177], 0\n\t"
            "v_mov_b64 v[180:181], 0\n\t"
            "v_mfma_f32_4x4x4_16b_bf16 v[196:199], v[178:179], v[178:179], v[196:199]\n\t"
            "v_mov_b64 v[178:179], 0\n\t"
            "v_mov_b64 v[182:183], 0\n\t"
            "s_waitcnt lgkmcnt(14)\n\t"
            "ds_write_b32 v228, v220\n\t"
            "s_waitcnt lgkmcnt(14)\n\t"
            "ds_read_b128 v[220:223], v235 offset:256\n\t"
            "s_waitcnt lgkmcnt(14)\n\t"
            "ds_read_b32 v230, v233\n\t"
            "s_waitcnt lgkmcnt(14)\n\t"
            "ds_read_b32 v231, v233 offset:64\n\t"
            "v_xor_b32 v228, 32, v234\n\t"
            "s_waitcnt lgkmcnt(14)\n\t"
            "v_smfmac_f32_16x16x64_bf16 v[184:187], v[168:171], v[144:151], v255\n\t"
            "ds_read_b128 v[168:171], v254 offset:512\n\t"
            "s_waitcnt lgkmcnt(14)\n\t"
            "ds_read_b64 v[144:145], v253 offset:72\n\t"
            "s_waitcnt lgkmcnt(14)\n\t"
            "ds_read_b64 v[146:147], v253 offset:168\n\t"
            "s_waitcnt lgkmcnt(14)\n\t"
            "ds_read_b64 v[148:149], v253 offset:1816\n\t"
            "s_waitcnt lgkmcnt(14)\n\t"
            "ds_read_b64 v[150:151], v253 offset:1912\n\t"
            "s_waitcnt lgkmcnt(14)\n\t"
            "v_smfmac_f32_16x16x64_bf16 v[188:191], v[172:175], v[152:159], v255\n\t"
            "ds_read_b128 v[172:175], v254 offset:576\n\t"
            "s_waitcnt lgkmcnt(14)\n\t"
            "ds_read_b64 v[152:153], v253 offset:80\n\t"
            "s_waitcnt lgkmcnt(14)\n\t"
            "ds_read_b64 v[154:155], v253 offset:176\n\t"
            "s_waitcnt lgkmcnt(14)\n\t"
            "ds_read_b64 v[156:157], v253 offset:1824\n\t"
            "s_waitcnt lgkmcnt(14)\n\t"
            "ds_read_b64 v[158:159], v253 offset:1920\n\t"
            "v_cvt_pk_bf16_f32 v184, v184, v185\n\t"
            "v_cvt_pk_bf16_f32 v185, v186, v187\n\t"
            "v_cvt_pk_bf16_f32 v186, v188, v189\n\t"
            "v_cvt_pk_bf16_f32 v187, v190, v191\n\t"
            "v_mfma_f32_4x4x4_16b_bf16 v[196:199], v[184:185], v[184:185], v[196:199]\n\t"
            "s_waitcnt lgkmcnt(14)\n\t"
            "ds_write_b128 v237, v[184:187] offset:128\n\t"
            "v_mfma_f32_16x16x32_bf16 v[192:195], v[238:241], v[184:187], v[192:195]\n\t"
            "v_mov_b64 v[184:185], 0\n\t"
            "v_mov_b64 v[188:189], 0\n\t"
            "v_mfma_f32_4x4x4_16b_bf16 v[196:199], v[186:187], v[186:187], v[196:199]\n\t"
            "v_mov_b64 v[186:187], 0\n\t"
            "v_mov_b64 v[190:191], 0\n\t"
            "s_waitcnt lgkmcnt(10)\n\t"
            "v_smfmac_f32_16x16x64_bf16 v[176:179], v[168:171], v[160:167], v255\n\t"
            "ds_read_b128 v[168:171], v254 offset:640\n\t"
            "ds_read_b64 v[160:161], v253 offset:88\n\t"
            "ds_read_b64 v[162:163], v253 offset:184\n\t"
            "ds_read_b64 v[164:165], v253 offset:1832\n\t"
            "ds_read_b64 v[166:167], v253 offset:1928\n\t"
            "s_waitcnt lgkmcnt(10)\n\t"
            "v_smfmac_f32_16x16x64_bf16 v[180:183], v[172:175], v[144:151], v255\n\t"
            "ds_read_b128 v[172:175], v254 offset:704\n\t"
            "v_cvt_pk_bf16_f32 v176, v176, v177\n\t"
            "v_cvt_pk_bf16_f32 v177, v178, v179\n\t"
            "s_nop 1\n\t"
            "v_mfma_f32_4x4x4_16b_bf16 v[196:199], v[176:177], v[176:177], v[196:199]\n\t"
            "s_nop 1\n\t"
            "v_cvt_pk_bf16_f32 v178, v180, v181\n\t"
            "v_cvt_pk_bf16_f32 v179, v182, v183\n\t"
            "s_nop 1\n\t"
            "v_mfma_f32_4x4x4_16b_bf16 v[196:199], v[178:179], v[178:179], v[196:199]\n\t"
            "ds_write_b128 v236, v[176:179] offset:256\n\t"
            "v_mfma_f32_16x16x32_bf16 v[192:195], v[238:241], v[176:179], v[192:195]\n\t"
            "v_add_u32 v236, s48, v244\n\t"
            "s_waitcnt lgkmcnt(6)\n\t"
            "v_smfmac_f32_16x16x64_bf16 v[184:187], v[168:171], v[152:159], v255\n\t"
            "s_waitcnt lgkmcnt(1)\n\t"
            "v_smfmac_f32_16x16x64_bf16 v[188:191], v[172:175], v[160:167], v255\n\t"
            "s_mov_b32 exec_hi, 0x3fffff\n\t"
            "s_add_u32 m0, s66, 0\n\t"
            "s_add_u32 s49, s79, 0\n\t"
            "buffer_load_dwordx4 v249, s[88:91], s49 offen lds\n\t"
            "s_mov_b32 exec_hi, -1\n\t"
            "s_mov_b32 exec_hi, 0x3fffff\n\t"
            "s_add_u32 m0, s66, 872\n\t"
            "s_add_u32 s49, s68, 196224\n\t"
            "buffer_load_dwordx4 v249, s[72:75], s49 offen lds\n\t"
            "s_mov_b32 exec_hi, -1\n\t"
            "s_mov_b32 exec_hi, 0x3fffff\n\t"
            "s_add_u32 m0, s66, 1744\n\t"
            "s_add_u32 s49, s68, 392448\n\t"
            "buffer_load_dwordx4 v249, s[72:75], s49 offen lds\n\t"
            "s_mov_b32 exec_hi, -1\n\t"
            "s_mov_b32 exec_hi, 0x3fffff\n\t"
            "s_add_u32 m0, s66, 2616\n\t"
            "s_add_u32 s49, s68, 588672\n\t"
            "buffer_load_dwordx4 v249, s[72:75], s49 offen lds\n\t"
            "s_mov_b32 exec_hi, -1\n\t"
            "s_mov_b32 exec_hi, 0x3fffff\n\t"
            "s_add_u32 m0, s66, 3488\n\t"
            "s_add_u32 s49, s68, 784896\n\t"
            "buffer_load_dwordx4 v249, s[72:75], s49 offen lds\n\t"
            "s_mov_b32 exec_hi, -1\n\t"
            "s_mov_b32 exec_hi, 0x3fffff\n\t"
            "s_add_u32 m0, s66, 4360\n\t"
            "s_add_u32 s49, s68, 981120\n\t"
            "buffer_load_dwordx4 v249, s[72:75], s49 offen lds\n\t"
            "s_mov_b32 exec_hi, -1\n\t"
            "s_mov_b32 exec_hi, 0x3fffff\n\t"
            "s_add_u32 m0, s66, 5232\n\t"
            "s_add_u32 s49, s68, 1177344\n\t"
            "buffer_load_dwordx4 v249, s[72:75], s49 offen lds\n\t"
            "s_mov_b32 exec_hi, -1\n\t"
            "v_cvt_pk_bf16_f32 v184, v184, v185\n\t"
            "v_cvt_pk_bf16_f32 v185, v186, v187\n\t"
            "v_cvt_pk_bf16_f32 v186, v188, v189\n\t"
            "v_cvt_pk_bf16_f32 v187, v190, v191\n\t"
            "v_mfma_f32_4x4x4_16b_bf16 v[196:199], v[184:185], v[184:185], v[196:199]\n\t"
            "s_mov_b32 exec_hi, 0x3fffff\n\t"
            "s_add_u32 m0, s66, 6104\n\t"
            "s_add_u32 s49, s68, 1373568\n\t"
            "buffer_load_dwordx4 v249, s[72:75], s49 offen lds\n\t"
            "s_mov_b32 exec_hi, -1\n\t"
            "v_mfma_f32_4x4x4_16b_bf16 v[196:199], v[186:187], v[186:187], v[196:199]\n\t"
            "s_mov_b32 exec_hi, 0x3fffff\n\t"
            "s_add_u32 m0, s66, 6976\n\t"
            "s_add_u32 s49, s68, 1569792\n\t"
            "buffer_load_dwordx4 v249, s[72:75], s49 offen lds\n\t"
            "s_mov_b32 exec_hi, -1\n\t"
            "v_mfma_f32_16x16x32_bf16 v[192:195], v[238:241], v[184:187], v[192:195]\n\t"
            "s_waitcnt vmcnt(9)\n\t"
            "ds_write_b128 v237, v[184:187] offset:256\n\t"
            "v_mfma_f32_16x16x32_bf16 v[144:147], v[0:3], v[200:203], 0\n\t"
            "v_add_u32 v237, s48, v245\n\t"
            "v_mfma_f32_16x16x32_bf16 v[144:147], v[4:7], v[204:207], v[144:147]\n\t"
            "v_mfma_f32_16x16x32_bf16 v[148:151], v[24:27], v[200:203], 0\n\t"
            "s_nop 1\n\t"
            "v_cndmask_b32_e64 v193, v196, v197, s[60:61]\n\t"
            "v_mfma_f32_16x16x32_bf16 v[148:151], v[28:31], v[204:207], v[148:151]\n\t"
            "v_cndmask_b32_e64 v193, v193, v198, s[62:63]\n\t"
            "v_cndmask_b32_e64 v193, v193, v199, s[64:65]\n\t"
            "v_mfma_f32_16x16x32_bf16 v[168:171], v[48:51], v[200:203], 0\n\t"
            "ds_read_b128 v[196:199], v232 offset:6272\n\t"
            "v_mfma_f32_16x16x32_bf16 v[168:171], v[52:55], v[204:207], v[168:171]\n\t"
            "ds_write_b64 v248, v[192:193] offset:18816\n\t"
            "v_mfma_f32_16x16x32_bf16 v[172:175], v[72:75], v[200:203], 0\n\t"
            "ds_read_b128 v[192:195], v235 offset:6144\n\t"
            "v_add_u32 v248, s48, v246\n\t"
            "v_mfma_f32_16x16x32_bf16 v[172:175], v[76:79], v[204:207], v[172:175]\n\t"
            "s_mov_b64 exec, 1\n\t"
            "ds_inc_u32 v255, v238 offset:7860\n\t"
            "s_mov_b64 exec, -1\n\t"
            "v_mfma_f32_16x16x32_bf16 v[152:155], v[96:99], v[200:203], 0\n\t"
            "v_mfma_f32_16x16x32_bf16 v[152:155], v[100:103], v[204:207], v[152:155]\n\t"
            "v_mfma_f32_16x16x32_bf16 v[156:159], v[120:123], v[200:203], 0\n\t"
            "ds_read_b128 v[200:203], v235 offset:6272\n\t"
            "v_mfma_f32_16x16x32_bf16 v[156:159], v[124:127], v[204:207], v[156:159]\n\t"
            "ds_read_b128 v[204:207], v232 offset:6400\n\t"
            "v_mfma_f32_16x16x32_bf16 v[144:147], v[8:11], v[208:211], v[144:147]\n\t"
            "v_mfma_f32_16x16x32_bf16 v[144:147], v[12:15], v[212:215], v[144:147]\n\t"
            "v_mfma_f32_16x16x32_bf16 v[148:151], v[32:35], v[208:211], v[148:151]\n\t"
            "v_mfma_f32_16x16x32_bf16 v[148:151], v[36:39], v[212:215], v[148:151]\n\t"
            "v_mfma_f32_16x16x32_bf16 v[168:171], v[56:59], v[208:211], v[168:171]\n\t"
            "v_mfma_f32_16x16x32_bf16 v[168:171], v[60:63], v[212:215], v[168:171]\n\t"
            "v_mfma_f32_16x16x32_bf16 v[172:175], v[80:83], v[208:211], v[172:175]\n\t"
            "v_mfma_f32_16x16x32_bf16 v[172:175], v[84:87], v[212:215], v[172:175]\n\t"
            "v_mfma_f32_16x16x32_bf16 v[152:155], v[104:107], v[208:211], v[152:155]\n\t"
            "v_mfma_f32_16x16x32_bf16 v[152:155], v[108:111], v[212:215], v[152:155]\n\t"
            "v_mfma_f32_16x16x32_bf16 v[156:159], v[128:131], v[208:211], v[156:159]\n\t"
            "ds_read_b128 v[208:211], v235 offset:6400\n\t"
            "v_mfma_f32_16x16x32_bf16 v[156:159], v[132:135], v[212:215], v[156:159]\n\t"
            "ds_read_b128 v[212:215], v236 offset:6144\n\t"
            "v_mfma_f32_16x16x32_bf16 v[144:147], v[16:19], v[216:219], v[144:147]\n\t"
            "v_mfma_f32_16x16x32_bf16 v[144:147], v[20:23], v[220:223], v[144:147]\n\t"
            "v_mfma_f32_16x16x32_bf16 v[148:151], v[40:43], v[216:219], v[148:151]\n\t"
            "v_mfma_f32_16x16x32_bf16 v[148:151], v[44:47], v[220:223], v[148:151]\n\t"
            "v_mfma_f32_16x16x32_bf16 v[168:171], v[64:67], v[216:219], v[168:171]\n\t"
            "v_mfma_f32_16x16x32_bf16 v[168:171], v[68:71], v[220:223], v[168:171]\n\t"
            "v_mfma_f32_16x16x32_bf16 v[172:175], v[88:91], v[216:219], v[172:175]\n\t"
            "v_mfma_f32_16x16x32_bf16 v[172:175], v[92:95], v[220:223], v[172:175]\n\t"
            "s_nop 1\n\t"
            "v_mul_f32 v144, v230, v144\n\t"
            "v_mfma_f32_16x16x32_bf16 v[152:155], v[112:115], v[216:219], v[152:155]\n\t"
            "v_mul_f32 v145, v230, v145\n\t"
            "v_mul_f32 v146, v230, v146\n\t"
            "v_mfma_f32_16x16x32_bf16 v[152:155], v[116:119], v[220:223], v[152:155]\n\t"
            "v_mul_f32 v148, v230, v148\n\t"
            "v_mul_f32 v149, v230, v149\n\t"
            "v_mfma_f32_16x16x32_bf16 v[156:159], v[136:139], v[216:219], v[156:159]\n\t"
            "ds_read_b128 v[216:219], v237 offset:6144\n\t"
            "v_mul_f32 v168, v230, v168\n\t"
            "v_mfma_f32_16x16x32_bf16 v[156:159], v[140:143], v[220:223], v[156:159]\n\t"
            "ds_read_b128 v[220:223], v248 offset:6144\n\t"
            "v_mul_f32 v169, v230, v169\n\t"
            "s_waitcnt lgkmcnt(7)\n\t"
            "v_mfma_f32_16x16x32_bf16 v[160:163], v[0:3], v[224:227], 0\n\t"
            "v_mul_f32 v172, v230, v172\n\t"
            "v_mul_f32 v173, v230, v173\n\t"
            "v_mfma_f32_16x16x32_bf16 v[160:163], v[4:7], v[192:195], v[160:163]\n\t"
            "v_mul_f32 v152, v230, v152\n\t"
            "v_mul_f32 v153, v230, v153\n\t"
            "v_mfma_f32_16x16x32_bf16 v[164:167], v[24:27], v[224:227], 0\n\t"
            "v_mul_f32 v156, v230, v156\n\t"
            "v_mul_f32 v157, v230, v157\n\t"
            "v_mfma_f32_16x16x32_bf16 v[164:167], v[28:31], v[192:195], v[164:167]\n\t"
            "v_mul_f32 v147, v230, v147\n\t"
            "v_cvt_pk_bf16_f32 v144, v144, v145\n\t"
            "v_mfma_f32_16x16x32_bf16 v[176:179], v[48:51], v[224:227], 0\n\t"
            "v_mul_f32 v150, v230, v150\n\t"
            "v_mul_f32 v151, v230, v151\n\t"
            "v_mfma_f32_16x16x32_bf16 v[176:179], v[52:55], v[192:195], v[176:179]\n\t"
            "v_cvt_pk_bf16_f32 v148, v148, v149\n\t"
            "v_mul_f32 v170, v230, v170\n\t"
            "v_mfma_f32_16x16x32_bf16 v[180:183], v[72:75], v[224:227], 0\n\t"
            "v_mul_f32 v171, v230, v171\n\t"
            "v_cvt_pk_bf16_f32 v168, v168, v169\n\t"
            "v_mfma_f32_16x16x32_bf16 v[180:183], v[76:79], v[192:195], v[180:183]\n\t"
            "v_mul_f32 v174, v230, v174\n\t"
            "v_mul_f32 v175, v230, v175\n\t"
            "v_mfma_f32_16x16x32_bf16 v[184:187], v[96:99], v[224:227], 0\n\t"
            "v_cvt_pk_bf16_f32 v172, v172, v173\n\t"
            "v_mul_f32 v154, v230, v154\n\t"
            "v_mfma_f32_16x16x32_bf16 v[184:187], v[100:103], v[192:195], v[184:187]\n\t"
            "v_mul_f32 v155, v230, v155\n\t"
            "v_cvt_pk_bf16_f32 v152, v152, v153\n\t"
            "v_mfma_f32_16x16x32_bf16 v[188:191], v[120:123], v[224:227], 0\n\t"
            "ds_read_b128 v[224:227], v236 offset:12288\n\t"
            "v_mul_f32 v158, v230, v158\n\t"
            "v_mfma_f32_16x16x32_bf16 v[188:191], v[124:127], v[192:195], v[188:191]\n\t"
            "v_mul_f32 v159, v230, v159\n\t"
            "v_cvt_pk_bf16_f32 v156, v156, v157\n\t"
            "s_waitcnt lgkmcnt(6)\n\t"
            "v_mfma_f32_16x16x32_bf16 v[160:163], v[8:11], v[196:199], v[160:163]\n\t"
            "v_cvt_pk_bf16_f32 v145, v146, v147\n\t"
            "v_cvt_pk_bf16_f32 v149, v150, v151\n\t"
            "v_mfma_f32_16x16x32_bf16 v[160:163], v[12:15], v[200:203], v[160:163]\n\t"
            "v_cvt_pk_bf16_f32 v169, v170, v171\n\t"
            "v_cvt_pk_bf16_f32 v173, v174, v175\n\t"
            "v_mfma_f32_16x16x32_bf16 v[164:167], v[32:35], v[196:199], v[164:167]\n\t"
            "v_cvt_pk_bf16_f32 v153, v154, v155\n\t"
            "v_cvt_pk_bf16_f32 v157, v158, v159\n\t"
            "v_mfma_f32_16x16x32_bf16 v[164:167], v[36:39], v[200:203], v[164:167]\n\t"
            "s_mov_b64 exec, -1\n\t"
            "ds_write_b64 v234, v[144:145]\n\t"
            "ds_write_b64 v234, v[148:149] offset:128\n\t"
            "ds_write_b64 v234, v[168:169] offset:256\n\t"
            "ds_write_b64 v228, v[172:173]\n\t"
            "ds_write_b64 v228, v[152:153] offset:128\n\t"
            "ds_write_b64 v228, v[156:157] offset:256\n\t"
            "s_mov_b64 exec, -1\n\t"
            "v_mfma_f32_16x16x32_bf16 v[176:179], v[56:59], v[196:199], v[176:179]\n\t"
            "v_mfma_f32_16x16x32_bf16 v[176:179], v[60:63], v[200:203], v[176:179]\n\t"
            "s_add_u32 s49, s33, 0\n\t"
            "buffer_load_dwordx4 v[0:3], v247, s[28:31], s49 offen sc1\n\t"
            "v_mfma_f32_16x16x32_bf16 v[180:183], v[80:83], v[196:199], v[180:183]\n\t"
            "v_mfma_f32_16x16x32_bf16 v[180:183], v[84:87], v[200:203], v[180:183]\n\t"
            "s_add_u32 s49, s33, 6144\n\t"
            "buffer_load_dwordx4 v[24:27], v247, s[28:31], s49 offen sc1\n\t"
            "v_mfma_f32_16x16x32_bf16 v[184:187], v[104:107], v[196:199], v[184:187]\n\t"
            "v_mfma_f32_16x16x32_bf16 v[184:187], v[108:111], v[200:203], v[184:187]\n\t"
            "s_add_u32 s49, s33, 12288\n\t"
            "buffer_load_dwordx4 v[48:51], v247, s[28:31], s49 offen sc1\n\t"
            "v_mfma_f32_16x16x32_bf16 v[188:191], v[128:131], v[196:199], v[188:191]\n\t"
            "v_mfma_f32_16x16x32_bf16 v[188:191], v[132:135], v[200:203], v[188:191]\n\t"
            "ds_read_b128 v[200:203], v236\n\t"
            "s_add_u32 s49, s33, 18432\n\t"
            "buffer_load_dwordx4 v[72:75], v247, s[28:31], s49 offen sc1\n\t"
            "s_waitcnt lgkmcnt(11)\n\t"
            "v_mfma_f32_16x16x32_bf16 v[160:163], v[16:19], v[204:207], v[160:163]\n\t"
            "v_mfma_f32_16x16x32_bf16 v[160:163], v[20:23], v[208:211], v[160:163]\n\t"
            "s_add_u32 s49, s33, 24576\n\t"
            "buffer_load_dwordx4 v[96:99], v247, s[28:31], s49 offen sc1\n\t"
            "v_mfma_f32_16x16x32_bf16 v[164:167], v[40:43], v[204:207], v[164:167]\n\t"
            "v_mfma_f32_16x16x32_bf16 v[164:167], v[44:47], v[208:211], v[164:167]\n\t"
            "s_add_u32 s49, s33, 30720\n\t"
            "buffer_load_dwordx4 v[120:123], v247, s[28:31], s49 offen sc1\n\t"
            "v_mfma_f32_16x16x32_bf16 v[176:179], v[64:67], v[204:207], v[176:179]\n\t"
            "s_nop 0\n\t"
            "v_mul_f32 v160, v231, v160\n\t"
            "v_mul_f32 v161, v231, v161\n\t"
            "v_mfma_f32_16x16x32_bf16 v[176:179], v[68:71], v[208:211], v[176:179]\n\t"
            "v_mul_f32 v162, v231, v162\n\t"
            "v_mul_f32 v163, v231, v163\n\t"
            "v_mfma_f32_16x16x32_bf16 v[180:183], v[88:91], v[204:207], v[180:183]\n\t"
            "v_mul_f32 v164, v231, v164\n\t"
            "v_mul_f32 v165, v231, v165\n\t"
            "v_mfma_f32_16x16x32_bf16 v[180:183], v[92:95], v[208:211], v[180:183]\n\t"
            "v_cvt_pk_bf16_f32 v160, v160, v161\n\t"
            "v_mul_f32 v166, v231, v166\n\t"
            "v_mfma_f32_16x16x32_bf16 v[184:187], v[112:115], v[204:207], v[184:187]\n\t"
            "v_mul_f32 v176, v231, v176\n\t"
            "v_mul_f32 v177, v231, v177\n\t"
            "v_mfma_f32_16x16x32_bf16 v[184:187], v[116:119], v[208:211], v[184:187]\n\t"
            "v_mul_f32 v167, v231, v167\n\t"
            "v_cvt_pk_bf16_f32 v164, v164, v165\n\t"
            "v_mfma_f32_16x16x32_bf16 v[188:191], v[136:139], v[204:207], v[188:191]\n\t"
            "ds_read_b128 v[204:207], v237\n\t"
            "v_mul_f32 v180, v231, v180\n\t"
            "v_mfma_f32_16x16x32_bf16 v[188:191], v[140:143], v[208:211], v[188:191]\n\t"
            "ds_read_b128 v[208:211], v248\n\t"
            "v_mul_f32 v181, v231, v181\n\t"
            "v_mul_f32 v184, v231, v184\n\t"
            "v_mul_f32 v185, v231, v185\n\t"
            "v_mul_f32 v178, v231, v178\n\t"
            "v_mul_f32 v179, v231, v179\n\t"
            "v_cvt_pk_bf16_f32 v176, v176, v177\n\t"
            "v_mul_f32 v182, v231, v182\n\t"
            "v_mul_f32 v188, v231, v188\n\t"
            "v_mul_f32 v189, v231, v189\n\t"
            "v_mul_f32 v183, v231, v183\n\t"
            "v_cvt_pk_bf16_f32 v180, v180, v181\n\t"
            "v_mul_f32 v186, v231, v186\n\t"
            "v_mul_f32 v187, v231, v187\n\t"
            "v_cvt_pk_bf16_f32 v184, v184, v185\n\t"
            "v_mul_f32 v190, v231, v190\n\t"
            "v_mul_f32 v191, v231, v191\n\t"
            "v_cvt_pk_bf16_f32 v188, v188, v189\n\t"
            "v_cvt_pk_bf16_f32 v161, v162, v163\n\t"
            "v_cvt_pk_bf16_f32 v165, v166, v167\n\t"
            "v_cvt_pk_bf16_f32 v177, v178, v179\n\t"
            "v_cvt_pk_bf16_f32 v181, v182, v183\n\t"
            "v_cvt_pk_bf16_f32 v185, v186, v187\n\t"
            "v_cvt_pk_bf16_f32 v189, v190, v191\n\t"
            "s_waitcnt lgkmcnt(9)\n\t"
            "s_mov_b64 exec, s[80:81]\n\t"
            "ds_write_b64 v234, v[160:161] offset:6144\n\t"
            "ds_write_b64 v234, v[164:165] offset:6272\n\t"
            "ds_write_b64 v234, v[176:177] offset:6400\n\t"
            "ds_write_b64 v228, v[180:181] offset:6144\n\t"
            "ds_write_b64 v228, v[184:185] offset:6272\n\t"
            "ds_write_b64 v228, v[188:189] offset:6400\n\t"
            "s_mov_b64 exec, -1\n\t"
            "s_waitcnt lgkmcnt(14)\n\t"
            "ds_read_b128 v[228:231], v237 offset:12288\n\t"
            "s_waitcnt lgkmcnt(14)\n\t"
            "ds_read_b128 v[232:235], v248 offset:12288\n\t"
            "s_add_u32 s49, s33, 1024\n\t"
            "buffer_load_dwordx4 v[4:7], v247, s[28:31], s49 offen sc1\n\t"
            "s_add_u32 s49, s33, 7168\n\t"
            "buffer_load_dwordx4 v[28:31], v247, s[28:31], s49 offen sc1\n\t"
            "s_waitcnt lgkmcnt(0)\n\t"
            "s_barrier\n\t"
            "s_setprio 3\n\t"
            "s_add_u32 s49, s33, 13312\n\t"
            "buffer_load_dwordx4 v[52:55], v247, s[28:31], s49 offen sc1\n\t"
            "s_add_u32 s49, s33, 19456\n\t"
            "buffer_load_dwordx4 v[76:79], v247, s[28:31], s49 offen sc1\n\t"
            "s_add_u32 s49, s33, 25600\n\t"
            "buffer_load_dwordx4 v[100:103], v247, s[28:31], s49 offen sc1\n\t"
            "s_add_u32 s49, s33, 31744\n\t"
            "buffer_load_dwordx4 v[124:127], v247, s[28:31], s49 offen sc1\n\t"
            "s_add_u32 s49, s33, 2048\n\t"
            "buffer_load_dwordx4 v[8:11], v247, s[28:31], s49 offen sc1\n\t"
            "s_add_u32 s49, s33, 8192\n\t"
            "buffer_load_dwordx4 v[32:35], v247, s[28:31], s49 offen sc1\n\t"
            "s_add_u32 s49, s33, 14336\n\t"
            "buffer_load_dwordx4 v[56:59], v247, s[28:31], s49 offen sc1\n\t"
            "s_add_u32 s49, s33, 20480\n\t"
            "buffer_load_dwordx4 v[80:83], v247, s[28:31], s49 offen sc1\n\t"
            "s_add_u32 s49, s33, 26624\n\t"
            "buffer_load_dwordx4 v[104:107], v247, s[28:31], s49 offen sc1\n\t"
            "s_add_u32 s49, s33, 32768\n\t"
            "buffer_load_dwordx4 v[128:131], v247, s[28:31], s49 offen sc1\n\t"
            "s_add_u32 s49, s33, 3072\n\t"
            "buffer_load_dwordx4 v[12:15], v247, s[28:31], s49 offen sc1\n\t"
            "s_add_u32 s49, s33, 9216\n\t"
            "buffer_load_dwordx4 v[36:39], v247, s[28:31], s49 offen sc1\n\t"
            "s_add_u32 s49, s33, 15360\n\t"
            "buffer_load_dwordx4 v[60:63], v247, s[28:31], s49 offen sc1\n\t"
            "s_add_u32 s49, s33, 21504\n\t"
            "buffer_load_dwordx4 v[84:87], v247, s[28:31], s49 offen sc1\n\t"
            "s_add_u32 s49, s33, 27648\n\t"
            "buffer_load_dwordx4 v[108:111], v247, s[28:31], s49 offen sc1\n\t"
            "s_add_u32 s49, s33, 33792\n\t"
            "buffer_load_dwordx4 v[132:135], v247, s[28:31], s49 offen sc1\n\t"
            "s_add_u32 s49, s33, 4096\n\t"
            "buffer_load_dwordx4 v[16:19], v247, s[28:31], s49 offen sc1\n\t"
            "s_add_u32 s49, s33, 10240\n\t"
            "buffer_load_dwordx4 v[40:43], v247, s[28:31], s49 offen sc1\n\t"
            "s_add_u32 s49, s33, 16384\n\t"
            "buffer_load_dwordx4 v[64:67], v247, s[28:31], s49 offen sc1\n\t"
            "s_add_u32 s49, s33, 22528\n\t"
            "buffer_load_dwordx4 v[88:91], v247, s[28:31], s49 offen sc1\n\t"
            "s_add_u32 s49, s33, 28672\n\t"
            "buffer_load_dwordx4 v[112:115], v247, s[28:31], s49 offen sc1\n\t"
            "s_add_u32 s49, s33, 34816\n\t"
            "buffer_load_dwordx4 v[136:139], v247, s[28:31], s49 offen sc1\n\t"
            "s_add_u32 s49, s33, 5120\n\t"
            "buffer_load_dwordx4 v[20:23], v247, s[28:31], s49 offen sc1\n\t"
            "s_add_u32 s49, s33, 11264\n\t"
            "buffer_load_dwordx4 v[44:47], v247, s[28:31], s49 offen sc1\n\t"
            "s_add_u32 s49, s33, 17408\n\t"
            "buffer_load_dwordx4 v[68:71], v247, s[28:31], s49 offen sc1\n\t"
            "s_add_u32 s49, s33, 23552\n\t"
            "buffer_load_dwordx4 v[92:95], v247, s[28:31], s49 offen sc1\n\t"
            "s_add_u32 s49, s33, 29696\n\t"
            "buffer_load_dwordx4 v[116:119], v247, s[28:31], s49 offen sc1\n\t"
            "s_add_u32 s49, s33, 35840\n\t"
            "buffer_load_dwordx4 v[140:143], v247, s[28:31], s49 offen sc1\n\t"
            "s_add_u32 s59, s59, s84\n\t"
            "s_movk_i32 s84, 800\n\t"
            "s_add_u32 s45, s46, s27\n\t"
            "s_mov_b32 s46, s47\n\t"
            "s_mov_b32 s47, s68\n\t"
            "s_xor_b32 s48, s48, s71\n\t"
            "s_xor_b32 s67, s67, s71\n\t"
            "s_add_u32 s76, s76, 4\n\t"
            "s_add_u32 s70, s70, 4\n\t"
            "s_lshl_b32 s56, s56, 2\n\t"
            "s_lshr_b32 s49, s26, 30\n\t"
            "s_or_b32 s56, s56, s49\n\t"
            "s_cmp_eq_u32 s82, 0\n\t"
            "s_cbranch_scc1 .Lwk_nr%=\n\t"
            "s_waitcnt lgkmcnt(0)\n\t"
            "s_nop 7\n\t"
            "v_add_u32 v236, 127360, v247\n\t"
            "v_add_u32 v237, 136704, v247\n\t"
            "ds_read_b128 v[144:147], v236\n\t"
            "ds_read_b128 v[148:151], v237\n\t"
            "s_waitcnt lgkmcnt(0)\n\t"
            "s_mov_b32 exec_hi, 0x3ffff\n\t"
            "buffer_store_dwordx4 v[144:147], v247, s[28:31], s82 offen sc1\n\t"
            "s_mov_b64 exec, 0xffffff\n\t"
            "buffer_store_dwordx4 v[148:151], v247, s[28:31], s82 offen offset:800 sc1\n\t"
            "s_mov_b64 exec, -1\n\t"
            "ds_read_b128 v[152:155], v252 offset:2560\n\t"
            "s_waitcnt vmcnt(0) lgkmcnt(0)\n\t"
            "s_mov_b64 exec, 1\n\t"
            "buffer_store_dwordx4 v[152:155], off, s[28:31], s82 offset:1216 sc1\n\t"
            "s_mov_b64 exec, -1\n\t"
            "s_branch .Lwk_n2%=\n\t"
            ".Lwk_nr%=:\n\t"
            "s_waitcnt vmcnt(0)\n\t"
            ".Lwk_n2%=:\n\t"
            "s_nop 1\n\t"
            "s_nop 7\n\t"
            ".p2align 6\n\t"
            "s_nop 0\n\t"
            "s_nop 0\n\t"
            "s_nop 0\n\t"
            "s_nop 0\n\t"
            "s_nop 0\n\t"
            "s_nop 0\n\t"
            "s_nop 0\n\t"
            ".Lwb_loop%=:\n\t"
            "s_setprio 1\n\t"
            "ds_read_b128 v[168:171], v254\n\t"
            "ds_read_b128 v[172:175], v254 offset:64\n\t"
            "s_waitcnt vmcnt(9)\n\t"
            "ds_read_b64 v[144:145], v253\n\t"
            "ds_read_b64 v[146:147], v253 offset:96\n\t"
            "ds_read_b64 v[148:149], v253 offset:1744\n\t"
            "ds_read_b64 v[150:151], v253 offset:1840\n\t"
            "ds_read_b64 v[152:153], v253 offset:8\n\t"
            "ds_read_b64 v[154:155], v253 offset:104\n\t"
            "ds_read_b64 v[156:157], v253 offset:1752\n\t"
            "ds_read_b64 v[158:159], v253 offset:1848\n\t"
            "ds_read_b64 v[160:161], v253 offset:16\n\t"
            "ds_read_b64 v[162:163], v253 offset:112\n\t"
            "ds_read_b64 v[164:165], v253 offset:1760\n\t"
            "ds_read_b64 v[166:167], v253 offset:1856\n\t"
            "v_mov_b64 v[176:177], 0\n\t"
            "v_mov_b64 v[178:179], 0\n\t"
            "v_mov_b64 v[180:181], 0\n\t"
            "v_mov_b64 v[182:183], 0\n\t"
            "v_mov_b32 v249, s76\n\t"
            "v_mov_b64 v[184:185], 0\n\t"
            "v_mov_b64 v[186:187], 0\n\t"
            "v_mov_b64 v[188:189], 0\n\t"
            "v_mov_b64 v[190:191], 0\n\t"
            "v_add_u32 v236, s67, v250\n\t"
            "v_add_u32 v248, s67, v251\n\t"
            "v_mov_b32 v247, 0\n\t"
            "ds_read_b32 v249, v249\n\t"
            "s_setprio 3\n\t"
            "s_waitcnt lgkmcnt(9)\n\t"
            "v_smfmac_f32_16x16x64_bf16 v[176:179], v[168:171], v[144:151], v255\n\t"
            "ds_read_b128 v[168:171], v254 offset:128\n\t"
            "s_waitcnt lgkmcnt(6)\n\t"
            "v_smfmac_f32_16x16x64_bf16 v[180:183], v[172:175], v[152:159], v255\n\t"
            "s_setprio 1\n\t"
            "ds_read_b64 v[144:145], v253 offset:24\n\t"
            "ds_read_b64 v[146:147], v253 offset:120\n\t"
            "ds_read_b64 v[148:149], v253 offset:1768\n\t"
            "ds_read_b64 v[150:151], v253 offset:1864\n\t"
            "ds_read_b128 v[172:175], v254 offset:192\n\t"
            "ds_read_b64 v[152:153], v253 offset:32\n\t"
            "ds_read_b64 v[154:155], v253 offset:128\n\t"
            "ds_read_b64 v[156:157], v253 offset:1776\n\t"
            "ds_read_b64 v[158:159], v253 offset:1872\n\t"
            "v_cvt_pk_bf16_f32 v176, v176, v177\n\t"
            "v_cvt_pk_bf16_f32 v177, v178, v179\n\t"
            "v_cvt_pk_bf16_f32 v178, v180, v181\n\t"
            "v_cvt_pk_bf16_f32 v179, v182, v183\n\t"
            "s_setprio 3\n\t"
            "v_mfma_f32_4x4x4_16b_bf16 v[196:199], v[176:177], v[176:177], 0\n\t"
            "v_mov_b64 v[180:181], 0\n\t"
            "v_mfma_f32_16x16x32_bf16 v[192:195], v[238:241], v[176:179], 0\n\t"
            "s_waitcnt lgkmcnt(10)\n\t"
            "v_readfirstlane_b32 s26, v249\n\t"
            "v_mov_b64 v[182:183], 0\n\t"
            "v_mfma_f32_4x4x4_16b_bf16 v[196:199], v[178:179], v[178:179], v[196:199]\n\t"
            "ds_read_b32 v249, v252 offset:12\n\t"
            "s_and_b32 s68, s26, 0x7ffffff\n\t"
            "s_add_u32 s68, s68, s78\n\t"
            "s_and_b32 s69, s26, 0x38000000\n\t"
            "s_or_b32 s69, s69, s39\n\t"
            "s_bitcmp1_b32 s26, 27\n\t"
            "s_cselect_b32 s79, 2.0, s68\n\t"
            "s_and_b32 s57, s56, 3\n\t"
            "s_lshl_b32 s58, s57, 3\n\t"
            "s_lshr_b32 m0, s54, s58\n\t"
            "s_lshl_b32 s58, s57, 4\n\t"
            "s_lshr_b64 s[36:37], s[50:51], s58\n\t"
            "s_and_b32 s57, s36, 0xffff\n\t"
            "s_add_u32 s57, s57, s67\n\t"
            "v_writelane_b32 v236, s57, 63\n\t"
            "v_writelane_b32 v236, s59, m0\n\t"
            "v_xor_b32 v237, 16, v236\n\t"
            "s_lshr_b64 s[36:37], s[52:53], s58\n\t"
            "s_and_b32 s58, s36, 0xffff\n\t"
            "s_add_u32 s58, s58, s67\n\t"
            "ds_write_b128 v236, v[176:179]\n\t"
            "s_waitcnt lgkmcnt(11)\n\t"
            "v_smfmac_f32_16x16x64_bf16 v[184:187], v[168:171], v[160:167], v255\n\t"
            "s_and_b32 s38, s52, 0xffff\n\t"
            "s_add_u32 s38, s38, s67\n\t"
            "ds_read_b128 v[168:171], v254 offset:256\n\t"
            "s_waitcnt lgkmcnt(7)\n\t"
            "v_smfmac_f32_16x16x64_bf16 v[188:191], v[172:175], v[144:151], v255\n\t"
            "s_setprio 1\n\t"
            "ds_read_b64 v[160:161], v253 offset:40\n\t"
            "ds_read_b64 v[162:163], v253 offset:136\n\t"
            "ds_read_b64 v[164:165], v253 offset:1784\n\t"
            "ds_read_b64 v[166:167], v253 offset:1880\n\t"
            "ds_read_b128 v[172:175], v254 offset:320\n\t"
            "ds_read_b64 v[144:145], v253 offset:48\n\t"
            "ds_read_b64 v[146:147], v253 offset:144\n\t"
            "ds_read_b64 v[148:149], v253 offset:1792\n\t"
            "s_waitcnt lgkmcnt(14)\n\t"
            "ds_read_b64 v[150:151], v253 offset:1888\n\t"
            "v_mov_b64 v[176:177], 0\n\t"
            "v_mov_b64 v[178:179], 0\n\t"
            "v_cvt_pk_bf16_f32 v184, v184, v185\n\t"
            "v_cvt_pk_bf16_f32 v185, v186, v187\n\t"
            "v_cvt_pk_bf16_f32 v186, v188, v189\n\t"
            "v_cvt_pk_bf16_f32 v187, v190, v191\n\t"
            "s_setprio 3\n\t"
            "v_mfma_f32_4x4x4_16b_bf16 v[196:199], v[184:185], v[184:185], v[196:199]\n\t"
            "s_waitcnt lgkmcnt(14)\n\t"
            "ds_write_b128 v237, v[184:187]\n\t"
            "v_mfma_f32_16x16x32_bf16 v[192:195], v[238:241], v[184:187], v[192:195]\n\t"
            "v_mov_b64 v[184:185], 0\n\t"
            "v_mov_b64 v[188:189], 0\n\t"
            "v_mfma_f32_4x4x4_16b_bf16 v[196:199], v[186:187], v[186:187], v[196:199]\n\t"
            "v_mov_b64 v[186:187], 0\n\t"
            "v_mov_b64 v[190:191], 0\n\t"
            "v_writelane_b32 v248, s58, 63\n\t"
            "v_writelane_b32 v248, s38, m0\n\t"
            "s_waitcnt lgkmcnt(12)\n\t"
            "v_and_b32 v249, s69, v249\n\t"
            "s_waitcnt lgkmcnt(10)\n\t"
            "v_smfmac_f32_16x16x64_bf16 v[176:179], v[168:171], v[152:159], v255\n\t"
            "ds_read_b128 v[168:171], v254 offset:384\n\t"
            "ds_read_b64 v[152:153], v253 offset:56\n\t"
            "ds_read_b64 v[154:155], v253 offset:152\n\t"
            "ds_read_b64 v[156:157], v253 offset:1800\n\t"
            "ds_read_b64 v[158:159], v253 offset:1896\n\t"
            "s_waitcnt lgkmcnt(10)\n\t"
            "v_smfmac_f32_16x16x64_bf16 v[180:183], v[172:175], v[160:167], v255\n\t"
            "s_setprio 1\n\t"
            "ds_read_b128 v[172:175], v254 offset:448\n\t"
            "ds_read_b64 v[160:161], v253 offset:64\n\t"
            "ds_read_b64 v[162:163], v253 offset:160\n\t"
            "ds_read_b64 v[164:165], v253 offset:1808\n\t"
            "ds_read_b64 v[166:167], v253 offset:1904\n\t"
            "v_cvt_pk_bf16_f32 v176, v176, v177\n\t"
            "v_cvt_pk_bf16_f32 v177, v178, v179\n\t"
            "v_cvt_pk_bf16_f32 v178, v180, v181\n\t"
            "v_cvt_pk_bf16_f32 v179, v182, v183\n\t"
            "s_setprio 3\n\t"
            "v_mfma_f32_4x4x4_16b_bf16 v[196:199], v[176:177], v[176:177], v[196:199]\n\t"
            "s_waitcnt lgkmcnt(14)\n\t"
            "ds_write_b128 v236, v[176:179] offset:128\n\t"
            "v_mfma_f32_16x16x32_bf16 v[192:195], v[238:241], v[176:179], v[192:195]\n\t"
            "v_mov_b64 v[176:177], 0\n\t"
            "v_mov_b64 v[180:181], 0\n\t"
            "v_mfma_f32_4x4x4_16b_bf16 v[196:199], v[178:179], v[178:179], v[196:199]\n\t"
            "v_mov_b64 v[178:179], 0\n\t"
            "v_mov_b64 v[182:183], 0\n\t"
            "s_waitcnt lgkmcnt(10)\n\t"
            "v_smfmac_f32_16x16x64_bf16 v[184:187], v[168:171], v[144:151], v255\n\t"
            "ds_read_b128 v[168:171], v254 offset:512\n\t"
            "ds_read_b64 v[144:145], v253 offset:72\n\t"
            "ds_read_b64 v[146:147], v253 offset:168\n\t"
            "ds_read_b64 v[148:149], v253 offset:1816\n\t"
            "ds_read_b64 v[150:151], v253 offset:1912\n\t"
            "s_waitcnt lgkmcnt(10)\n\t"
            "v_smfmac_f32_16x16x64_bf16 v[188:191], v[172:175], v[152:159], v255\n\t"
            "s_setprio 1\n\t"
            "ds_read_b128 v[172:175], v254 offset:576\n\t"
            "ds_read_b64 v[152:153], v253 offset:80\n\t"
            "ds_read_b64 v[154:155], v253 offset:176\n\t"
            "ds_read_b64 v[156:157], v253 offset:1824\n\t"
            "ds_read_b64 v[158:159], v253 offset:1920\n\t"
            "v_cvt_pk_bf16_f32 v184, v184, v185\n\t"
            "v_cvt_pk_bf16_f32 v185, v186, v187\n\t"
            "v_cvt_pk_bf16_f32 v186, v188, v189\n\t"
            "v_cvt_pk_bf16_f32 v187, v190, v191\n\t"
            "s_setprio 3\n\t"
            "v_mfma_f32_4x4x4_16b_bf16 v[196:199], v[184:185], v[184:185], v[196:199]\n\t"
            "s_waitcnt lgkmcnt(14)\n\t"
            "ds_write_b128 v237, v[184:187] offset:128\n\t"
            "v_mfma_f32_16x16x32_bf16 v[192:195], v[238:241], v[184:187], v[192:195]\n\t"
            "v_mov_b64 v[184:185], 0\n\t"
            "v_mov_b64 v[188:189], 0\n\t"
            "v_mfma_f32_4x4x4_16b_bf16 v[196:199], v[186:187], v[186:187], v[196:199]\n\t"
            "v_mov_b64 v[186:187], 0\n\t"
            "v_mov_b64 v[190:191], 0\n\t"
            "s_waitcnt lgkmcnt(10)\n\t"
            "v_smfmac_f32_16x16x64_bf16 v[176:179], v[168:171], v[160:167], v255\n\t"
            "ds_read_b128 v[168:171], v254 offset:640\n\t"
            "ds_read_b64 v[160:161], v253 offset:88\n\t"
            "ds_read_b64 v[162:163], v253 offset:184\n\t"
            "ds_read_b64 v[164:165], v253 offset:1832\n\t"
            "ds_read_b64 v[166:167], v253 offset:1928\n\t"
            "s_waitcnt lgkmcnt(10)\n\t"
            "v_smfmac_f32_16x16x64_bf16 v[180:183], v[172:175], v[144:151], v255\n\t"
            "s_setprio 1\n\t"
            "ds_read_b128 v[172:175], v254 offset:704\n\t"
            "v_lshlrev_b32 v144, 16, v212\n\t"
            "v_cvt_pk_bf16_f32 v176, v176, v177\n\t"
            "v_cvt_pk_bf16_f32 v177, v178, v179\n\t"
            "v_and_b32 v145, 0xffff0000, v212\n\t"
            "v_lshlrev_b32 v146, 16, v213\n\t"
            "s_setprio 3\n\t"
            "v_mfma_f32_4x4x4_16b_bf16 v[196:199], v[176:177], v[176:177], v[196:199]\n\t"
            "v_and_b32 v147, 0xffff0000, v213\n\t"
            "v_cvt_pk_bf16_f32 v178, v180, v181\n\t"
            "v_cvt_pk_bf16_f32 v179, v182, v183\n\t"
            "v_lshlrev_b32 v180, 16, v210\n\t"
            "v_and_b32 v181, 0xffff0000, v210\n\t"
            "v_mfma_f32_4x4x4_16b_bf16 v[196:199], v[178:179], v[178:179], v[196:199]\n\t"
            "ds_write_b128 v236, v[176:179] offset:256\n\t"
            "v_mfma_f32_16x16x32_bf16 v[192:195], v[238:241], v[176:179], v[192:195]\n\t"
            "s_setprio 1\n\t"
            "v_lshlrev_b32 v176, 16, v208\n\t"
            "v_and_b32 v177, 0xffff0000, v208\n\t"
            "v_lshlrev_b32 v178, 16, v209\n\t"
            "v_and_b32 v179, 0xffff0000, v209\n\t"
            "v_lshlrev_b32 v182, 16, v211\n\t"
            "v_and_b32 v183, 0xffff0000, v211\n\t"
            "v_lshlrev_b32 v148, 16, v214\n\t"
            "v_and_b32 v149, 0xffff0000, v214\n\t"
            "v_lshlrev_b32 v150, 16, v215\n\t"
            "v_and_b32 v151, 0xffff0000, v215\n\t"
            "v_add_u32 v236, s48, v244\n\t"
            "ds_read_b128 v[212:215], v236 offset:6144\n\t"
            "s_setprio 3\n\t"
            "s_waitcnt lgkmcnt(7)\n\t"
            "v_smfmac_f32_16x16x64_bf16 v[184:187], v[168:171], v[152:159], v255\n\t"
            "v_lshlrev_b32 v152, 16, v200\n\t"
            "v_and_b32 v153, 0xffff0000, v200\n\t"
            "v_lshlrev_b32 v154, 16, v201\n\t"
            "v_and_b32 v155, 0xffff0000, v201\n\t"
            "v_lshlrev_b32 v156, 16, v202\n\t"
            "s_waitcnt lgkmcnt(2)\n\t"
            "v_smfmac_f32_16x16x64_bf16 v[188:191], v[172:175], v[160:167], v255\n\t"
            "s_setprio 1\n\t"
            "s_mov_b32 exec_hi, 0x3fffff\n\t"
            "s_add_u32 m0, s66, 0\n\t"
            "s_add_u32 s49, s79, 0\n\t"
            "buffer_load_dwordx4 v249, s[88:91], s49 offen lds\n\t"
            "s_mov_b32 exec_hi, -1\n\t"
            "s_mov_b32 exec_hi, 0x3fffff\n\t"
            "s_add_u32 m0, s66, 872\n\t"
            "s_add_u32 s49, s68, 196224\n\t"
            "buffer_load_dwordx4 v249, s[72:75], s49 offen lds\n\t"
            "s_mov_b32 exec_hi, -1\n\t"
            "s_mov_b32 exec_hi, 0x3fffff\n\t"
            "s_add_u32 m0, s66, 1744\n\t"
            "s_add_u32 s49, s68, 392448\n\t"
            "buffer_load_dwordx4 v249, s[72:75], s49 offen lds\n\t"
            "s_mov_b32 exec_hi, -1\n\t"
            "s_mov_b32 exec_hi, 0x3fffff\n\t"
            "s_add_u32 m0, s66, 2616\n\t"
            "s_add_u32 s49, s68, 588672\n\t"
            "buffer_load_dwordx4 v249, s[72:75], s49 offen lds\n\t"
            "s_mov_b32 exec_hi, -1\n\t"
            "ds_read_b32 v247, v247 offset:7856\n\t"
            "s_mov_b32 exec_hi, 0x3fffff\n\t"
            "s_add_u32 m0, s66, 3488\n\t"
            "s_add_u32 s49, s68, 784896\n\t"
            "buffer_load_dwordx4 v249, s[72:75], s49 offen lds\n\t"
            "s_mov_b32 exec_hi, -1\n\t"
            "s_mov_b32 exec_hi, 0x3fffff\n\t"
            "s_add_u32 m0, s66, 4360\n\t"
            "s_add_u32 s49, s68, 981120\n\t"
            "buffer_load_dwordx4 v249, s[72:75], s49 offen lds\n\t"
            "s_mov_b32 exec_hi, -1\n\t"
            "s_mov_b32 exec_hi, 0x3fffff\n\t"
            "s_add_u32 m0, s66, 5232\n\t"
            "s_add_u32 s49, s68, 1177344\n\t"
            "buffer_load_dwordx4 v249, s[72:75], s49 offen lds\n\t"
            "s_mov_b32 exec_hi, -1\n\t"
            "s_mov_b32 exec_hi, 0x3fffff\n\t"
            "s_add_u32 m0, s66, 6104\n\t"
            "s_add_u32 s49, s68, 1373568\n\t"
            "buffer_load_dwordx4 v249, s[72:75], s49 offen lds\n\t"
            "s_mov_b32 exec_hi, -1\n\t"
            "s_mov_b32 exec_hi, 0x3fffff\n\t"
            "s_add_u32 m0, s66, 6976\n\t"
            "s_add_u32 s49, s68, 1569792\n\t"
            "buffer_load_dwordx4 v249, s[72:75], s49 offen lds\n\t"
            "s_mov_b32 exec_hi, -1\n\t"
            "v_cvt_pk_bf16_f32 v184, v184, v185\n\t"
            "v_cvt_pk_bf16_f32 v185, v186, v187\n\t"
            "v_cvt_pk_bf16_f32 v186, v188, v189\n\t"
            "v_cvt_pk_bf16_f32 v187, v190, v191\n\t"
            "s_setprio 3\n\t"
            "v_mfma_f32_4x4x4_16b_bf16 v[196:199], v[184:185], v[184:185], v[196:199]\n\t"
            "ds_write_b128 v237, v[184:187] offset:256\n\t"
            "v_mfma_f32_16x16x32_bf16 v[192:195], v[238:241], v[184:187], v[192:195]\n\t"
            "s_waitcnt lgkmcnt(1)\n\t"
            "v_readfirstlane_b32 s77, v247\n\t"
            "s_cmp_ge_u32 s77, s70\n\t"
            "s_cbranch_scc0 .Lwb_spin%=\n\t"
            ".Lwb_go%=:\n\t"
            "v_mfma_f32_4x4x4_16b_bf16 v[196:199], v[186:187], v[186:187], v[196:199]\n\t"
            "s_setprio 1\n\t"
            "ds_read_b128 v[184:187], v242\n\t"
            "ds_read_b128 v[188:191], v243\n\t"
            "ds_read_b128 v[172:175], v243 offset:128\n\t"
            "v_and_b32 v157, 0xffff0000, v202\n\t"
            "v_lshlrev_b32 v158, 16, v203\n\t"
            "v_and_b32 v159, 0xffff0000, v203\n\t"
            "v_lshlrev_b32 v160, 16, v204\n\t"
            "v_and_b32 v161, 0xffff0000, v204\n\t"
            "v_lshlrev_b32 v162, 16, v205\n\t"
            "v_cndmask_b32_e64 v193, v196, v197, s[60:61]\n\t"
            "v_cndmask_b32_e64 v193, v193, v198, s[62:63]\n\t"
            "v_cndmask_b32_e64 v193, v193, v199, s[64:65]\n\t"
            "v_and_b32 v163, 0xffff0000, v205\n\t"
            "v_lshlrev_b32 v164, 16, v206\n\t"
            "v_and_b32 v165, 0xffff0000, v206\n\t"
            "v_lshlrev_b32 v166, 16, v207\n\t"
            "v_and_b32 v167, 0xffff0000, v207\n\t"
            "v_lshlrev_b32 v168, 16, v216\n\t"
            "v_and_b32 v169, 0xffff0000, v216\n\t"
            "v_lshlrev_b32 v170, 16, v217\n\t"
            "v_and_b32 v171, 0xffff0000, v217\n\t"
            "s_bfe_u32 s57, s56, 0x20004\n\t"
            "s_lshl_b32 s58, s57, 3\n\t"
            "s_lshr_b32 s58, s55, s58\n\t"
            "s_lshl_b64 s[36:37], s[34:35], s58\n\t"
            "ds_read_b128 v[196:199], v252\n\t"
            "ds_read_b128 v[200:203], v236\n\t"
            "ds_write_b64 v248, v[192:193] offset:18816\n\t"
            "ds_read_b128 v[192:195], v242 offset:128\n\t"
            "s_bitcmp1_b32 s57, 1\n\t"
            "s_cselect_b32 s58, 1177344, 0\n\t"
            "s_bitcmp1_b32 s57, 0\n\t"
            "s_cselect_b32 s57, 2304, 0\n\t"
            "s_add_u32 s57, s57, s58\n\t"
            "s_mov_b64 exec, 1\n\t"
            "ds_inc_u32 v255, v238 offset:7860\n\t"
            "s_mov_b64 exec, -1\n\t"
            "v_add_u32 v237, s48, v245\n\t"
            "v_add_u32 v248, s48, v246\n\t"
            "s_add_u32 s59, s59, s84\n\t"
            "s_movk_i32 s84, 800\n\t"
            "ds_read_b128 v[204:207], v237\n\t"
            "s_setprio 3\n\t"
            "s_waitcnt lgkmcnt(7)\n\t"
            "v_mfma_f32_16x16x32_bf16 v[152:155], v[0:3], v[184:187], v[152:155]\n\t"
            "s_setprio 1\n\t"
            "s_setprio 1\n\t"
            "ds_read_b128 v[208:211], v248\n\t"
            "v_mfma_f32_16x16x32_bf16 v[152:155], v[4:7], v[188:191], v[152:155]\n\t"
            "v_mfma_f32_16x16x32_bf16 v[156:159], v[28:31], v[188:191], v[156:159]\n\t"
            "v_mfma_f32_16x16x32_bf16 v[156:159], v[24:27], v[184:187], v[156:159]\n\t"
            "v_mfma_f32_16x16x32_bf16 v[160:163], v[48:51], v[184:187], v[160:163]\n\t"
            "v_mfma_f32_16x16x32_bf16 v[160:163], v[52:55], v[188:191], v[160:163]\n\t"
            "v_mfma_f32_16x16x32_bf16 v[164:167], v[76:79], v[188:191], v[164:167]\n\t"
            "s_waitcnt lgkmcnt(6)\n\t"
            "s_mov_b64 exec, s[36:37]\n\t"
            "v_add_u32 v196, s57, v196\n\t"
            "s_mov_b64 exec, -1\n\t"
            "v_mfma_f32_16x16x32_bf16 v[164:167], v[72:75], v[184:187], v[164:167]\n\t"
            "v_mfma_f32_16x16x32_bf16 v[176:179], v[96:99], v[184:187], v[176:179]\n\t"
            "v_mfma_f32_16x16x32_bf16 v[176:179], v[100:103], v[188:191], v[176:179]\n\t"
            "v_mfma_f32_16x16x32_bf16 v[180:183], v[124:127], v[188:191], v[180:183]\n\t"
            "ds_read_b128 v[188:191], v243 offset:256\n\t"
            "v_mfma_f32_16x16x32_bf16 v[180:183], v[120:123], v[184:187], v[180:183]\n\t"
            "ds_read_b128 v[184:187], v242 offset:256\n\t"
            "s_waitcnt lgkmcnt(5)\n\t"
            "v_mfma_f32_16x16x32_bf16 v[152:155], v[8:11], v[192:195], v[152:155]\n\t"
            "v_mfma_f32_16x16x32_bf16 v[152:155], v[12:15], v[172:175], v[152:155]\n\t"
            "v_mfma_f32_16x16x32_bf16 v[156:159], v[36:39], v[172:175], v[156:159]\n\t"
            "v_mfma_f32_16x16x32_bf16 v[156:159], v[32:35], v[192:195], v[156:159]\n\t"
            "v_mfma_f32_16x16x32_bf16 v[160:163], v[56:59], v[192:195], v[160:163]\n\t"
            "v_mfma_f32_16x16x32_bf16 v[160:163], v[60:63], v[172:175], v[160:163]\n\t"
            "v_mfma_f32_16x16x32_bf16 v[164:167], v[84:87], v[172:175], v[164:167]\n\t"
            "v_mfma_f32_16x16x32_bf16 v[164:167], v[80:83], v[192:195], v[164:167]\n\t"
            "v_mfma_f32_16x16x32_bf16 v[176:179], v[104:107], v[192:195], v[176:179]\n\t"
            "v_mfma_f32_16x16x32_bf16 v[176:179], v[108:111], v[172:175], v[176:179]\n\t"
            "v_mfma_f32_16x16x32_bf16 v[180:183], v[132:135], v[172:175], v[180:183]\n\t"
            "ds_read_b128 v[172:175], v243 offset:6144\n\t"
            "v_mfma_f32_16x16x32_bf16 v[180:183], v[128:131], v[192:195], v[180:183]\n\t"
            "ds_read_b128 v[192:195], v242 offset:6144\n\t"
            "s_waitcnt lgkmcnt(2)\n\t"
            "v_mfma_f32_16x16x32_bf16 v[152:155], v[16:19], v[184:187], v[152:155]\n\t"
            "v_mfma_f32_16x16x32_bf16 v[152:155], v[20:23], v[188:191], v[152:155]\n\t"
            "v_mfma_f32_16x16x32_bf16 v[156:159], v[44:47], v[188:191], v[156:159]\n\t"
            "v_mfma_f32_16x16x32_bf16 v[156:159], v[40:43], v[184:187], v[156:159]\n\t"
            "v_mfma_f32_16x16x32_bf16 v[160:163], v[64:67], v[184:187], v[160:163]\n\t"
            "v_mfma_f32_16x16x32_bf16 v[160:163], v[68:71], v[188:191], v[160:163]\n\t"
            "v_mfma_f32_16x16x32_bf16 v[164:167], v[92:95], v[188:191], v[164:167]\n\t"
            "v_mfma_f32_16x16x32_bf16 v[164:167], v[88:91], v[184:187], v[164:167]\n\t"
            "s_nop 1\n\t"
            "v_cvt_pk_bf16_f32 v152, v152, v153\n\t"
            "v_mfma_f32_16x16x32_bf16 v[176:179], v[112:115], v[184:187], v[176:179]\n\t"
            "v_cvt_pk_bf16_f32 v153, v154, v155\n\t"
            "v_mfma_f32_16x16x32_bf16 v[176:179], v[116:119], v[188:191], v[176:179]\n\t"
            "v_cvt_pk_bf16_f32 v154, v156, v157\n\t"
            "v_cvt_pk_bf16_f32 v155, v158, v159\n\t"
            "v_mfma_f32_16x16x32_bf16 v[180:183], v[140:143], v[188:191], v[180:183]\n\t"
            "ds_read_b128 v[188:191], v243 offset:6272\n\t"
            "v_cvt_pk_bf16_f32 v160, v160, v161\n\t"
            "v_mfma_f32_16x16x32_bf16 v[180:183], v[136:139], v[184:187], v[180:183]\n\t"
            "ds_read_b128 v[184:187], v242 offset:6272\n\t"
            "v_cvt_pk_bf16_f32 v161, v162, v163\n\t"
            "s_waitcnt lgkmcnt(2)\n\t"
            "v_mfma_f32_16x16x32_bf16 v[144:147], v[0:3], v[192:195], v[144:147]\n\t"
            "v_cvt_pk_bf16_f32 v176, v176, v177\n\t"
            "v_cvt_pk_bf16_f32 v177, v178, v179\n\t"
            "v_mfma_f32_16x16x32_bf16 v[144:147], v[4:7], v[172:175], v[144:147]\n\t"
            "v_cvt_pk_bf16_f32 v162, v164, v165\n\t"
            "v_cvt_pk_bf16_f32 v163, v166, v167\n\t"
            "v_mfma_f32_16x16x32_bf16 v[148:151], v[28:31], v[172:175], v[148:151]\n\t"
            "v_cvt_pk_bf16_f32 v178, v180, v181\n\t"
            "v_cvt_pk_bf16_f32 v179, v182, v183\n\t"
            "v_mfma_f32_16x16x32_bf16 v[148:151], v[24:27], v[192:195], v[148:151]\n\t"
            "v_lshlrev_b32 v156, 16, v218\n\t"
            "v_and_b32 v157, 0xffff0000, v218\n\t"
            "v_mfma_f32_16x16x32_bf16 v[168:171], v[48:51], v[192:195], v[168:171]\n\t"
            "v_lshlrev_b32 v158, 16, v219\n\t"
            "v_and_b32 v159, 0xffff0000, v219\n\t"
            "v_mfma_f32_16x16x32_bf16 v[168:171], v[52:55], v[172:175], v[168:171]\n\t"
            "ds_read_b128 v[216:219], v237 offset:6144\n\t"
            "v_lshlrev_b32 v164, 16, v220\n\t"
            "v_mfma_f32_16x16x32_bf16 v[156:159], v[76:79], v[172:175], v[156:159]\n\t"
            "v_and_b32 v165, 0xffff0000, v220\n\t"
            "v_lshlrev_b32 v166, 16, v221\n\t"
            "v_mfma_f32_16x16x32_bf16 v[156:159], v[72:75], v[192:195], v[156:159]\n\t"
            "v_and_b32 v167, 0xffff0000, v221\n\t"
            "v_lshlrev_b32 v180, 16, v222\n\t"
            "v_and_b32 v181, 0xffff0000, v222\n\t"
            "v_mfma_f32_16x16x32_bf16 v[164:167], v[96:99], v[192:195], v[164:167]\n\t"
            "v_lshlrev_b32 v182, 16, v223\n\t"
            "v_and_b32 v183, 0xffff0000, v223\n\t"
            "v_mfma_f32_16x16x32_bf16 v[164:167], v[100:103], v[172:175], v[164:167]\n\t"
            "ds_read_b128 v[220:223], v248 offset:6144\n\t"
            "buffer_store_dwordx4 v[152:155], v196, s[40:43], s45 offen sc1\n\t"
            "v_mfma_f32_16x16x32_bf16 v[180:183], v[124:127], v[172:175], v[180:183]\n\t"
            "ds_read_b128 v[172:175], v243 offset:6400\n\t"
            "v_lshlrev_b32 v152, 16, v224\n\t"
            "v_mfma_f32_16x16x32_bf16 v[180:183], v[120:123], v[192:195], v[180:183]\n\t"
            "ds_read_b128 v[192:195], v242 offset:6400\n\t"
            "buffer_store_dwordx4 v[160:163], v196, s[40:43], s45 offen offset:64 sc1\n\t"
            "s_waitcnt lgkmcnt(4)\n\t"
            "v_mfma_f32_16x16x32_bf16 v[144:147], v[8:11], v[184:187], v[144:147]\n\t"
            "v_and_b32 v153, 0xffff0000, v224\n\t"
            "v_lshlrev_b32 v154, 16, v225\n\t"
            "v_mfma_f32_16x16x32_bf16 v[144:147], v[12:15], v[188:191], v[144:147]\n\t"
            "buffer_store_dwordx4 v[176:179], v196, s[40:43], s45 offen offset:128 sc1\n\t"
            "v_mfma_f32_16x16x32_bf16 v[148:151], v[36:39], v[188:191], v[148:151]\n\t"
            "v_and_b32 v155, 0xffff0000, v225\n\t"
            "v_lshlrev_b32 v160, 16, v226\n\t"
            "v_mfma_f32_16x16x32_bf16 v[148:151], v[32:35], v[184:187], v[148:151]\n\t"
            "v_and_b32 v161, 0xffff0000, v226\n\t"
            "v_lshlrev_b32 v162, 16, v227\n\t"
            "v_mfma_f32_16x16x32_bf16 v[168:171], v[56:59], v[184:187], v[168:171]\n\t"
            "v_and_b32 v163, 0xffff0000, v227\n\t"
            "v_lshlrev_b32 v176, 16, v228\n\t"
            "v_mfma_f32_16x16x32_bf16 v[168:171], v[60:63], v[188:191], v[168:171]\n\t"
            "ds_read_b128 v[224:227], v236 offset:12288\n\t"
            "v_and_b32 v177, 0xffff0000, v228\n\t"
            "v_mfma_f32_16x16x32_bf16 v[156:159], v[84:87], v[188:191], v[156:159]\n\t"
            "v_lshlrev_b32 v178, 16, v229\n\t"
            "v_and_b32 v179, 0xffff0000, v229\n\t"
            "v_mfma_f32_16x16x32_bf16 v[156:159], v[80:83], v[184:187], v[156:159]\n\t"
            "v_mfma_f32_16x16x32_bf16 v[164:167], v[104:107], v[184:187], v[164:167]\n\t"
            "v_mfma_f32_16x16x32_bf16 v[164:167], v[108:111], v[188:191], v[164:167]\n\t"
            "v_mfma_f32_16x16x32_bf16 v[180:183], v[132:135], v[188:191], v[180:183]\n\t"
            "ds_read_b128 v[188:191], v243 offset:12288\n\t"
            "v_mfma_f32_16x16x32_bf16 v[180:183], v[128:131], v[184:187], v[180:183]\n\t"
            "ds_read_b128 v[184:187], v242 offset:12288\n\t"
            "s_waitcnt lgkmcnt(3)\n\t"
            "v_mfma_f32_16x16x32_bf16 v[144:147], v[16:19], v[192:195], v[144:147]\n\t"
            "v_mfma_f32_16x16x32_bf16 v[144:147], v[20:23], v[172:175], v[144:147]\n\t"
            "v_mfma_f32_16x16x32_bf16 v[168:171], v[64:67], v[192:195], v[168:171]\n\t"
            "v_mfma_f32_16x16x32_bf16 v[168:171], v[68:71], v[172:175], v[168:171]\n\t"
            "v_mfma_f32_16x16x32_bf16 v[164:167], v[112:115], v[192:195], v[164:167]\n\t"
            "v_mfma_f32_16x16x32_bf16 v[164:167], v[116:119], v[172:175], v[164:167]\n\t"
            "v_mfma_f32_16x16x32_bf16 v[148:151], v[44:47], v[172:175], v[148:151]\n\t"
            "v_mfma_f32_16x16x32_bf16 v[148:151], v[40:43], v[192:195], v[148:151]\n\t"
            "s_nop 1\n\t"
            "v_cvt_pk_bf16_f32 v144, v144, v145\n\t"
            "v_mfma_f32_16x16x32_bf16 v[156:159], v[92:95], v[172:175], v[156:159]\n\t"
            "v_cvt_pk_bf16_f32 v145, v146, v147\n\t"
            "v_mfma_f32_16x16x32_bf16 v[156:159], v[88:91], v[192:195], v[156:159]\n\t"
            "v_cvt_pk_bf16_f32 v168, v168, v169\n\t"
            "v_cvt_pk_bf16_f32 v169, v170, v171\n\t"
            "v_mfma_f32_16x16x32_bf16 v[180:183], v[140:143], v[172:175], v[180:183]\n\t"
            "ds_read_b128 v[172:175], v243 offset:12416\n\t"
            "v_cvt_pk_bf16_f32 v164, v164, v165\n\t"
            "v_mfma_f32_16x16x32_bf16 v[180:183], v[136:139], v[192:195], v[180:183]\n\t"
            "ds_read_b128 v[192:195], v242 offset:12416\n\t"
            "v_cvt_pk_bf16_f32 v165, v166, v167\n\t"
            "s_waitcnt lgkmcnt(2)\n\t"
            "v_mfma_f32_16x16x32_bf16 v[152:155], v[0:3], v[184:187], v[152:155]\n\t"
            "v_cvt_pk_bf16_f32 v146, v148, v149\n\t"
            "v_cvt_pk_bf16_f32 v147, v150, v151\n\t"
            "v_mfma_f32_16x16x32_bf16 v[152:155], v[4:7], v[188:191], v[152:155]\n\t"
            "v_cvt_pk_bf16_f32 v170, v156, v157\n\t"
            "v_cvt_pk_bf16_f32 v171, v158, v159\n\t"
            "v_mfma_f32_16x16x32_bf16 v[160:163], v[28:31], v[188:191], v[160:163]\n\t"
            "v_cvt_pk_bf16_f32 v166, v180, v181\n\t"
            "v_cvt_pk_bf16_f32 v167, v182, v183\n\t"
            "v_mfma_f32_16x16x32_bf16 v[160:163], v[24:27], v[184:187], v[160:163]\n\t"
            "v_lshlrev_b32 v148, 16, v230\n\t"
            "v_and_b32 v149, 0xffff0000, v230\n\t"
            "v_mfma_f32_16x16x32_bf16 v[176:179], v[48:51], v[184:187], v[176:179]\n\t"
            "v_lshlrev_b32 v150, 16, v231\n\t"
            "v_and_b32 v151, 0xffff0000, v231\n\t"
            "v_mfma_f32_16x16x32_bf16 v[176:179], v[52:55], v[188:191], v[176:179]\n\t"
            "ds_read_b128 v[228:231], v237 offset:12288\n\t"
            "v_lshlrev_b32 v156, 16, v232\n\t"
            "v_mfma_f32_16x16x32_bf16 v[148:151], v[76:79], v[188:191], v[148:151]\n\t"
            "v_and_b32 v157, 0xffff0000, v232\n\t"
            "v_lshlrev_b32 v158, 16, v233\n\t"
            "v_mfma_f32_16x16x32_bf16 v[148:151], v[72:75], v[184:187], v[148:151]\n\t"
            "v_and_b32 v159, 0xffff0000, v233\n\t"
            "v_lshlrev_b32 v180, 16, v234\n\t"
            "v_and_b32 v181, 0xffff0000, v234\n\t"
            "v_mfma_f32_16x16x32_bf16 v[156:159], v[96:99], v[184:187], v[156:159]\n\t"
            "v_lshlrev_b32 v182, 16, v235\n\t"
            "v_and_b32 v183, 0xffff0000, v235\n\t"
            "v_mfma_f32_16x16x32_bf16 v[156:159], v[100:103], v[188:191], v[156:159]\n\t"
            "ds_read_b128 v[232:235], v248 offset:12288\n\t"
            "buffer_store_dwordx4 v[144:147], v197, s[40:43], s45 offen sc1\n\t"
            "v_mfma_f32_16x16x32_bf16 v[180:183], v[124:127], v[188:191], v[180:183]\n\t"
            "ds_read_b128 v[188:191], v243 offset:12544\n\t"
            "v_mfma_f32_16x16x32_bf16 v[180:183], v[120:123], v[184:187], v[180:183]\n\t"
            "ds_read_b128 v[184:187], v242 offset:12544\n\t"
            "buffer_store_dwordx4 v[168:171], v197, s[40:43], s45 offen offset:64 sc1\n\t"
            "s_waitcnt lgkmcnt(4)\n\t"
            "v_mfma_f32_16x16x32_bf16 v[152:155], v[8:11], v[192:195], v[152:155]\n\t"
            "v_mfma_f32_16x16x32_bf16 v[152:155], v[12:15], v[172:175], v[152:155]\n\t"
            "buffer_store_dwordx4 v[164:167], v197, s[40:43], s45 offen offset:128 sc1\n\t"
            "v_mfma_f32_16x16x32_bf16 v[160:163], v[36:39], v[172:175], v[160:163]\n\t"
            "v_mfma_f32_16x16x32_bf16 v[160:163], v[32:35], v[192:195], v[160:163]\n\t"
            "s_waitcnt lgkmcnt(0)\n\t"
            "s_barrier\n\t"
            "s_setprio 3\n\t"
            "v_mfma_f32_16x16x32_bf16 v[176:179], v[56:59], v[192:195], v[176:179]\n\t"
            "v_mfma_f32_16x16x32_bf16 v[176:179], v[60:63], v[172:175], v[176:179]\n\t"
            "v_mfma_f32_16x16x32_bf16 v[148:151], v[84:87], v[172:175], v[148:151]\n\t"
            "v_mfma_f32_16x16x32_bf16 v[148:151], v[80:83], v[192:195], v[148:151]\n\t"
            "v_mfma_f32_16x16x32_bf16 v[156:159], v[104:107], v[192:195], v[156:159]\n\t"
            "v_mfma_f32_16x16x32_bf16 v[156:159], v[108:111], v[172:175], v[156:159]\n\t"
            "v_mfma_f32_16x16x32_bf16 v[152:155], v[16:19], v[184:187], v[152:155]\n\t"
            "v_mfma_f32_16x16x32_bf16 v[152:155], v[20:23], v[188:191], v[152:155]\n\t"
            "v_mfma_f32_16x16x32_bf16 v[160:163], v[44:47], v[188:191], v[160:163]\n\t"
            "v_mfma_f32_16x16x32_bf16 v[160:163], v[40:43], v[184:187], v[160:163]\n\t"
            "v_mfma_f32_16x16x32_bf16 v[180:183], v[132:135], v[172:175], v[180:183]\n\t"
            "v_mfma_f32_16x16x32_bf16 v[180:183], v[128:131], v[192:195], v[180:183]\n\t"
            "v_mfma_f32_16x16x32_bf16 v[176:179], v[64:67], v[184:187], v[176:179]\n\t"
            "v_mfma_f32_16x16x32_bf16 v[176:179], v[68:71], v[188:191], v[176:179]\n\t"
            "s_nop 1\n\t"
            "v_cvt_pk_bf16_f32 v152, v152, v153\n\t"
            "v_mfma_f32_16x16x32_bf16 v[148:151], v[92:95], v[188:191], v[148:151]\n\t"
            "v_cvt_pk_bf16_f32 v153, v154, v155\n\t"
            "v_mfma_f32_16x16x32_bf16 v[148:151], v[88:91], v[184:187], v[148:151]\n\t"
            "v_cvt_pk_bf16_f32 v154, v160, v161\n\t"
            "v_cvt_pk_bf16_f32 v155, v162, v163\n\t"
            "v_mfma_f32_16x16x32_bf16 v[156:159], v[112:115], v[184:187], v[156:159]\n\t"
            "v_cvt_pk_bf16_f32 v176, v176, v177\n\t"
            "v_cvt_pk_bf16_f32 v177, v178, v179\n\t"
            "v_mfma_f32_16x16x32_bf16 v[156:159], v[116:119], v[188:191], v[156:159]\n\t"
            "buffer_store_dwordx4 v[152:155], v198, s[40:43], s45 offen sc1\n\t"
            "v_mfma_f32_16x16x32_bf16 v[180:183], v[140:143], v[188:191], v[180:183]\n\t"
            "v_cvt_pk_bf16_f32 v178, v148, v149\n\t"
            "v_cvt_pk_bf16_f32 v179, v150, v151\n\t"
            "v_mfma_f32_16x16x32_bf16 v[180:183], v[136:139], v[184:187], v[180:183]\n\t"
            "buffer_store_dwordx4 v[176:179], v198, s[40:43], s45 offen offset:64 sc1\n\t"
            "s_nop 1\n\t"
            "v_cvt_pk_bf16_f32 v156, v156, v157\n\t"
            "v_cvt_pk_bf16_f32 v157, v158, v159\n\t"
            "s_nop 2\n\t"
            "v_cvt_pk_bf16_f32 v158, v180, v181\n\t"
            "v_cvt_pk_bf16_f32 v159, v182, v183\n\t"
            "buffer_store_dwordx4 v[156:159], v198, s[40:43], s45 offen offset:128 sc1\n\t"
            "s_add_u32 s45, s46, s27\n\t"
            "s_mov_b32 s46, s47\n\t"
            "s_mov_b32 s47, s68\n\t"
            "s_xor_b32 s48, s48, s71\n\t"
            "s_xor_b32 s67, s67, s71\n\t"
            "s_add_u32 s76, s76, 4\n\t"
            "s_add_u32 s70, s70, 4\n\t"
            "s_lshl_b32 s56, s56, 2\n\t"
            "s_lshr_b32 s49, s26, 30\n\t"
            "s_or_b32 s56, s56, s49\n\t"
            "s_cmp_lt_u32 s46, 0x7000000\n\t"
            "s_cbranch_scc1 .Lwb_loop%=\n\t"
            "s_branch .Lwb_end%=\n\t"
            ".Lwb_spin%=:\n\t"
            "s_cmp_eq_u32 s83, 0\n\t"
            "s_cbranch_scc1 .Lwb_go%=\n\t"
            "s_sub_u32 s83, s83, 1\n\t"
            "s_sleep 2\n\t"
            "v_mov_b32 v247, 0\n\t"
            "ds_read_b32 v247, v247 offset:7856\n\t"
            "s_waitcnt lgkmcnt(0)\n\t"
            "v_readfirstlane_b32 s77, v247\n\t"
            "s_cmp_ge_u32 s77, s70\n\t"
            "s_cbranch_scc0 .Lwb_spin%=\n\t"
            "s_branch .Lwb_go%=\n\t"
            ".Lwb_end%=:\n\t"
            "s_waitcnt lgkmcnt(0)\n\t"
            "s_nop 7\n\t"
            "ds_read_b128 v[196:199], v252\n\t"
            "s_bfe_u32 s57, s56, 0x20004\n\t"
            "s_lshl_b32 s58, s57, 3\n\t"
            "s_lshr_b32 s58, s55, s58\n\t"
            "s_lshl_b64 s[36:37], s[34:35], s58\n\t"
            "s_bitcmp1_b32 s57, 1\n\t"
            "s_cselect_b32 s58, 1177344, 0\n\t"
            "s_bitcmp1_b32 s57, 0\n\t"
            "s_cselect_b32 s57, 2304, 0\n\t"
            "s_add_u32 s57, s57, s58\n\t"
            "v_lshlrev_b32 v152, 16, v200\n\t"
            "v_and_b32 v153, 0xffff0000, v200\n\t"
            "v_lshlrev_b32 v154, 16, v201\n\t"
            "v_and_b32 v155, 0xffff0000, v201\n\t"
            "v_lshlrev_b32 v156, 16, v202\n\t"
            "v_and_b32 v157, 0xffff0000, v202\n\t"
            "v_lshlrev_b32 v158, 16, v203\n\t"
            "v_and_b32 v159, 0xffff0000, v203\n\t"
            "v_lshlrev_b32 v160, 16, v204\n\t"
            "v_and_b32 v161, 0xffff0000, v204\n\t"
            "v_lshlrev_b32 v162, 16, v205\n\t"
            "v_and_b32 v163, 0xffff0000, v205\n\t"
            "v_lshlrev_b32 v164, 16, v206\n\t"
            "v_and_b32 v165, 0xffff0000, v206\n\t"
            "v_lshlrev_b32 v166, 16, v207\n\t"
            "v_and_b32 v167, 0xffff0000, v207\n\t"
            "v_lshlrev_b32 v176, 16, v208\n\t"
            "v_and_b32 v177, 0xffff0000, v208\n\t"
            "v_lshlrev_b32 v178, 16, v209\n\t"
            "v_and_b32 v179, 0xffff0000, v209\n\t"
            "v_lshlrev_b32 v180, 16, v210\n\t"
            "v_and_b32 v181, 0xffff0000, v210\n\t"
            "v_lshlrev_b32 v182, 16, v211\n\t"
            "v_and_b32 v183, 0xffff0000, v211\n\t"
            "v_lshlrev_b32 v144, 16, v212\n\t"
            "v_and_b32 v145, 0xffff0000, v212\n\t"
            "v_lshlrev_b32 v146, 16, v213\n\t"
            "v_and_b32 v147, 0xffff0000, v213\n\t"
            "v_lshlrev_b32 v148, 16, v214\n\t"
            "v_and_b32 v149, 0xffff0000, v214\n\t"
            "v_lshlrev_b32 v150, 16, v215\n\t"
            "v_and_b32 v151, 0xffff0000, v215\n\t"
            "v_lshlrev_b32 v168, 16, v216\n\t"
            "v_and_b32 v169, 0xffff0000, v216\n\t"
            "v_lshlrev_b32 v170, 16, v217\n\t"
            "s_waitcnt lgkmcnt(0)\n\t"
            "s_mov_b64 exec, s[36:37]\n\t"
            "v_add_u32 v196, s57, v196\n\t"
            "s_mov_b64 exec, -1\n\t"
            ".Lwk_t1%=:\n\t"
            "v_mov_b32 v247, 0\n\t"
            "ds_read_b32 v247, v247 offset:7856\n\t"
            "s_waitcnt lgkmcnt(0)\n\t"
            "v_readfirstlane_b32 s77, v247\n\t"
            "s_cmp_ge_u32 s77, s70\n\t"
            "s_cbranch_scc1 .Lwk_t2%=\n\t"
            "s_cmp_eq_u32 s83, 0\n\t"
            "s_cbranch_scc1 .Lwk_t2%=\n\t"
            "s_sub_u32 s83, s83, 1\n\t"
            "s_sleep 1\n\t"
            "s_branch .Lwk_t1%=\n\t"
            ".Lwk_t2%=:\n\t"
            "v_and_b32 v171, 0xffff0000, v217\n\t"
            "ds_read_b128 v[184:187], v242\n\t"
            "ds_read_b128 v[188:191], v243\n\t"
            "ds_read_b128 v[192:195], v242 offset:128\n\t"
            "ds_read_b128 v[172:175], v243 offset:128\n\t"
            "s_waitcnt lgkmcnt(2)\n\t"
            "v_mfma_f32_16x16x32_bf16 v[152:155], v[0:3], v[184:187], v[152:155]\n\t"
            "v_mfma_f32_16x16x32_bf16 v[152:155], v[4:7], v[188:191], v[152:155]\n\t"
            "v_mfma_f32_16x16x32_bf16 v[156:159], v[28:31], v[188:191], v[156:159]\n\t"
            "v_mfma_f32_16x16x32_bf16 v[156:159], v[24:27], v[184:187], v[156:159]\n\t"
            "v_mfma_f32_16x16x32_bf16 v[160:163], v[48:51], v[184:187], v[160:163]\n\t"
            "v_mfma_f32_16x16x32_bf16 v[160:163], v[52:55], v[188:191], v[160:163]\n\t"
            "v_mfma_f32_16x16x32_bf16 v[164:167], v[76:79], v[188:191], v[164:167]\n\t"
            "v_mfma_f32_16x16x32_bf16 v[164:167], v[72:75], v[184:187], v[164:167]\n\t"
            "v_mfma_f32_16x16x32_bf16 v[176:179], v[96:99], v[184:187], v[176:179]\n\t"
            "v_mfma_f32_16x16x32_bf16 v[176:179], v[100:103], v[188:191], v[176:179]\n\t"
            "v_mfma_f32_16x16x32_bf16 v[180:183], v[124:127], v[188:191], v[180:183]\n\t"
            "ds_read_b128 v[188:191], v243 offset:256\n\t"
            "v_mfma_f32_16x16x32_bf16 v[180:183], v[120:123], v[184:187], v[180:183]\n\t"
            "ds_read_b128 v[184:187], v242 offset:256\n\t"
            "s_waitcnt lgkmcnt(2)\n\t"
            "v_mfma_f32_16x16x32_bf16 v[152:155], v[8:11], v[192:195], v[152:155]\n\t"
            "v_mfma_f32_16x16x32_bf16 v[152:155], v[12:15], v[172:175], v[152:155]\n\t"
            "v_mfma_f32_16x16x32_bf16 v[156:159], v[36:39], v[172:175], v[156:159]\n\t"
            "v_mfma_f32_16x16x32_bf16 v[156:159], v[32:35], v[192:195], v[156:159]\n\t"
            "v_mfma_f32_16x16x32_bf16 v[160:163], v[56:59], v[192:195], v[160:163]\n\t"
            "v_mfma_f32_16x16x32_bf16 v[160:163], v[60:63], v[172:175], v[160:163]\n\t"
            "v_mfma_f32_16x16x32_bf16 v[164:167], v[84:87], v[172:175], v[164:167]\n\t"
            "v_mfma_f32_16x16x32_bf16 v[164:167], v[80:83], v[192:195], v[164:167]\n\t"
            "v_mfma_f32_16x16x32_bf16 v[176:179], v[104:107], v[192:195], v[176:179]\n\t"
            "v_mfma_f32_16x16x32_bf16 v[176:179], v[108:111], v[172:175], v[176:179]\n\t"
            "v_mfma_f32_16x16x32_bf16 v[180:183], v[132:135], v[172:175], v[180:183]\n\t"
            "ds_read_b128 v[172:175], v243 offset:6144\n\t"
            "v_mfma_f32_16x16x32_bf16 v[180:183], v[128:131], v[192:195], v[180:183]\n\t"
            "ds_read_b128 v[192:195], v242 offset:6144\n\t"
            "s_waitcnt lgkmcnt(2)\n\t"
            "v_mfma_f32_16x16x32_bf16 v[152:155], v[16:19], v[184:187], v[152:155]\n\t"
            "v_mfma_f32_16x16x32_bf16 v[152:155], v[20:23], v[188:191], v[152:155]\n\t"
            "v_mfma_f32_16x16x32_bf16 v[156:159], v[44:47], v[188:191], v[156:159]\n\t"
            "v_mfma_f32_16x16x32_bf16 v[156:159], v[40:43], v[184:187], v[156:159]\n\t"
            "v_mfma_f32_16x16x32_bf16 v[160:163], v[64:67], v[184:187], v[160:163]\n\t"
            "v_mfma_f32_16x16x32_bf16 v[160:163], v[68:71], v[188:191], v[160:163]\n\t"
            "v_mfma_f32_16x16x32_bf16 v[164:167], v[92:95], v[188:191], v[164:167]\n\t"
            "v_mfma_f32_16x16x32_bf16 v[164:167], v[88:91], v[184:187], v[164:167]\n\t"
            "s_nop 1\n\t"
            "v_cvt_pk_bf16_f32 v152, v152, v153\n\t"
            "v_mfma_f32_16x16x32_bf16 v[176:179], v[112:115], v[184:187], v[176:179]\n\t"
            "v_cvt_pk_bf16_f32 v153, v154, v155\n\t"
            "v_mfma_f32_16x16x32_bf16 v[176:179], v[116:119], v[188:191], v[176:179]\n\t"
            "v_cvt_pk_bf16_f32 v154, v156, v157\n\t"
            "v_cvt_pk_bf16_f32 v155, v158, v159\n\t"
            "v_mfma_f32_16x16x32_bf16 v[180:183], v[140:143], v[188:191], v[180:183]\n\t"
            "ds_read_b128 v[188:191], v243 offset:6272\n\t"
            "v_cvt_pk_bf16_f32 v160, v160, v161\n\t"
            "v_mfma_f32_16x16x32_bf16 v[180:183], v[136:139], v[184:187], v[180:183]\n\t"
            "ds_read_b128 v[184:187], v242 offset:6272\n\t"
            "v_cvt_pk_bf16_f32 v161, v162, v163\n\t"
            "s_waitcnt lgkmcnt(2)\n\t"
            "v_mfma_f32_16x16x32_bf16 v[144:147], v[0:3], v[192:195], v[144:147]\n\t"
            "v_cvt_pk_bf16_f32 v176, v176, v177\n\t"
            "v_cvt_pk_bf16_f32 v177, v178, v179\n\t"
            "v_mfma_f32_16x16x32_bf16 v[144:147], v[4:7], v[172:175], v[144:147]\n\t"
            "v_cvt_pk_bf16_f32 v162, v164, v165\n\t"
            "v_cvt_pk_bf16_f32 v163, v166, v167\n\t"
            "v_mfma_f32_16x16x32_bf16 v[148:151], v[28:31], v[172:175], v[148:151]\n\t"
            "v_cvt_pk_bf16_f32 v178, v180, v181\n\t"
            "v_cvt_pk_bf16_f32 v179, v182, v183\n\t"
            "v_mfma_f32_16x16x32_bf16 v[148:151], v[24:27], v[192:195], v[148:151]\n\t"
            "v_lshlrev_b32 v156, 16, v218\n\t"
            "v_and_b32 v157, 0xffff0000, v218\n\t"
            "v_mfma_f32_16x16x32_bf16 v[168:171], v[48:51], v[192:195], v[168:171]\n\t"
            "v_lshlrev_b32 v158, 16, v219\n\t"
            "v_and_b32 v159, 0xffff0000, v219\n\t"
            "v_mfma_f32_16x16x32_bf16 v[168:171], v[52:55], v[172:175], v[168:171]\n\t"
            "v_lshlrev_b32 v164, 16, v220\n\t"
            "v_and_b32 v165, 0xffff0000, v220\n\t"
            "v_mfma_f32_16x16x32_bf16 v[156:159], v[76:79], v[172:175], v[156:159]\n\t"
            "v_lshlrev_b32 v166, 16, v221\n\t"
            "v_and_b32 v167, 0xffff0000, v221\n\t"
            "v_mfma_f32_16x16x32_bf16 v[156:159], v[72:75], v[192:195], v[156:159]\n\t"
            "v_lshlrev_b32 v180, 16, v222\n\t"
            "v_and_b32 v181, 0xffff0000, v222\n\t"
            "v_mfma_f32_16x16x32_bf16 v[164:167], v[96:99], v[192:195], v[164:167]\n\t"
            "v_lshlrev_b32 v182, 16, v223\n\t"
            "v_and_b32 v183, 0xffff0000, v223\n\t"
            "v_mfma_f32_16x16x32_bf16 v[164:167], v[100:103], v[172:175], v[164:167]\n\t"
            "buffer_store_dwordx4 v[152:155], v196, s[40:43], s45 offen sc1\n\t"
            "v_mfma_f32_16x16x32_bf16 v[180:183], v[124:127], v[172:175], v[180:183]\n\t"
            "ds_read_b128 v[172:175], v243 offset:6400\n\t"
            "v_lshlrev_b32 v152, 16, v224\n\t"
            "v_mfma_f32_16x16x32_bf16 v[180:183], v[120:123], v[192:195], v[180:183]\n\t"
            "ds_read_b128 v[192:195], v242 offset:6400\n\t"
            "buffer_store_dwordx4 v[160:163], v196, s[40:43], s45 offen offset:64 sc1\n\t"
            "s_waitcnt lgkmcnt(2)\n\t"
            "v_mfma_f32_16x16x32_bf16 v[144:147], v[8:11], v[184:187], v[144:147]\n\t"
            "v_and_b32 v153, 0xffff0000, v224\n\t"
            "v_lshlrev_b32 v154, 16, v225\n\t"
            "v_mfma_f32_16x16x32_bf16 v[144:147], v[12:15], v[188:191], v[144:147]\n\t"
            "buffer_store_dwordx4 v[176:179], v196, s[40:43], s45 offen offset:128 sc1\n\t"
            "v_mfma_f32_16x16x32_bf16 v[148:151], v[36:39], v[188:191], v[148:151]\n\t"
            "v_and_b32 v155, 0xffff0000, v225\n\t"
            "v_lshlrev_b32 v160, 16, v226\n\t"
            "v_mfma_f32_16x16x32_bf16 v[148:151], v[32:35], v[184:187], v[148:151]\n\t"
            "v_and_b32 v161, 0xffff0000, v226\n\t"
            "v_lshlrev_b32 v162, 16, v227\n\t"
            "v_mfma_f32_16x16x32_bf16 v[168:171], v[56:59], v[184:187], v[168:171]\n\t"
            "v_and_b32 v163, 0xffff0000, v227\n\t"
            "v_lshlrev_b32 v176, 16, v228\n\t"
            "v_mfma_f32_16x16x32_bf16 v[168:171], v[60:63], v[188:191], v[168:171]\n\t"
            "v_and_b32 v177, 0xffff0000, v228\n\t"
            "v_lshlrev_b32 v178, 16, v229\n\t"
            "v_mfma_f32_16x16x32_bf16 v[156:159], v[84:87], v[188:191], v[156:159]\n\t"
            "v_and_b32 v179, 0xffff0000, v229\n\t"
            "v_mfma_f32_16x16x32_bf16 v[156:159], v[80:83], v[184:187], v[156:159]\n\t"
            "v_mfma_f32_16x16x32_bf16 v[164:167], v[104:107], v[184:187], v[164:167]\n\t"
            "v_mfma_f32_16x16x32_bf16 v[164:167], v[108:111], v[188:191], v[164:167]\n\t"
            "v_mfma_f32_16x16x32_bf16 v[180:183], v[132:135], v[188:191], v[180:183]\n\t"
            "ds_read_b128 v[188:191], v243 offset:12288\n\t"
            "v_mfma_f32_16x16x32_bf16 v[180:183], v[128:131], v[184:187], v[180:183]\n\t"
            "ds_read_b128 v[184:187], v242 offset:12288\n\t"
            "s_waitcnt lgkmcnt(2)\n\t"
            "v_mfma_f32_16x16x32_bf16 v[144:147], v[16:19], v[192:195], v[144:147]\n\t"
            "v_mfma_f32_16x16x32_bf16 v[144:147], v[20:23], v[172:175], v[144:147]\n\t"
            "v_mfma_f32_16x16x32_bf16 v[168:171], v[64:67], v[192:195], v[168:171]\n\t"
            "v_mfma_f32_16x16x32_bf16 v[168:171], v[68:71], v[172:175], v[168:171]\n\t"
            "v_mfma_f32_16x16x32_bf16 v[164:167], v[112:115], v[192:195], v[164:167]\n\t"
            "v_mfma_f32_16x16x32_bf16 v[164:167], v[116:119], v[172:175], v[164:167]\n\t"
            "v_mfma_f32_16x16x32_bf16 v[148:151], v[44:47], v[172:175], v[148:151]\n\t"
            "v_mfma_f32_16x16x32_bf16 v[148:151], v[40:43], v[192:195], v[148:151]\n\t"
            "s_nop 1\n\t"
            "v_cvt_pk_bf16_f32 v144, v144, v145\n\t"
            "v_mfma_f32_16x16x32_bf16 v[156:159], v[92:95], v[172:175], v[156:159]\n\t"
            "v_cvt_pk_bf16_f32 v145, v146, v147\n\t"
            "v_mfma_f32_16x16x32_bf16 v[156:159], v[88:91], v[192:195], v[156:159]\n\t"
            "v_cvt_pk_bf16_f32 v168, v168, v169\n\t"
            "v_cvt_pk_bf16_f32 v169, v170, v171\n\t"
            "v_mfma_f32_16x16x32_bf16 v[180:183], v[140:143], v[172:175], v[180:183]\n\t"
            "ds_read_b128 v[172:175], v243 offset:12416\n\t"
            "v_cvt_pk_bf16_f32 v164, v164, v165\n\t"
            "v_mfma_f32_16x16x32_bf16 v[180:183], v[136:139], v[192:195], v[180:183]\n\t"
            "ds_read_b128 v[192:195], v242 offset:12416\n\t"
            "v_cvt_pk_bf16_f32 v165, v166, v167\n\t"
            "s_waitcnt lgkmcnt(2)\n\t"
            "v_mfma_f32_16x16x32_bf16 v[152:155], v[0:3], v[184:187], v[152:155]\n\t"
            "v_cvt_pk_bf16_f32 v146, v148, v149\n\t"
            "v_cvt_pk_bf16_f32 v147, v150, v151\n\t"
            "v_mfma_f32_16x16x32_bf16 v[152:155], v[4:7], v[188:191], v[152:155]\n\t"
            "v_cvt_pk_bf16_f32 v170, v156, v157\n\t"
            "v_cvt_pk_bf16_f32 v171, v158, v159\n\t"
            "v_mfma_f32_16x16x32_bf16 v[160:163], v[28:31], v[188:191], v[160:163]\n\t"
            "v_cvt_pk_bf16_f32 v166, v180, v181\n\t"
            "v_cvt_pk_bf16_f32 v167, v182, v183\n\t"
            "v_mfma_f32_16x16x32_bf16 v[160:163], v[24:27], v[184:187], v[160:163]\n\t"
            "v_lshlrev_b32 v148, 16, v230\n\t"
            "v_and_b32 v149, 0xffff0000, v230\n\t"
            "v_mfma_f32_16x16x32_bf16 v[176:179], v[48:51], v[184:187], v[176:179]\n\t"
            "v_lshlrev_b32 v150, 16, v231\n\t"
            "v_and_b32 v151, 0xffff0000, v231\n\t"
            "v_mfma_f32_16x16x32_bf16 v[176:179], v[52:55], v[188:191], v[176:179]\n\t"
            "v_lshlrev_b32 v156, 16, v232\n\t"
            "v_and_b32 v157, 0xffff0000, v232\n\t"
            "v_mfma_f32_16x16x32_bf16 v[148:151], v[76:79], v[188:191], v[148:151]\n\t"
            "v_lshlrev_b32 v158, 16, v233\n\t"
            "v_and_b32 v159, 0xffff0000, v233\n\t"
            "v_mfma_f32_16x16x32_bf16 v[148:151], v[72:75], v[184:187], v[148:151]\n\t"
            "v_lshlrev_b32 v180, 16, v234\n\t"
            "v_and_b32 v181, 0xffff0000, v234\n\t"
            "v_mfma_f32_16x16x32_bf16 v[156:159], v[96:99], v[184:187], v[156:159]\n\t"
            "v_lshlrev_b32 v182, 16, v235\n\t"
            "v_and_b32 v183, 0xffff0000, v235\n\t"
            "v_mfma_f32_16x16x32_bf16 v[156:159], v[100:103], v[188:191], v[156:159]\n\t"
            "buffer_store_dwordx4 v[144:147], v197, s[40:43], s45 offen sc1\n\t"
            "v_mfma_f32_16x16x32_bf16 v[180:183], v[124:127], v[188:191], v[180:183]\n\t"
            "ds_read_b128 v[188:191], v243 offset:12544\n\t"
            "v_mfma_f32_16x16x32_bf16 v[180:183], v[120:123], v[184:187], v[180:183]\n\t"
            "ds_read_b128 v[184:187], v242 offset:12544\n\t"
            "buffer_store_dwordx4 v[168:171], v197, s[40:43], s45 offen offset:64 sc1\n\t"
            "s_waitcnt lgkmcnt(2)\n\t"
            "v_mfma_f32_16x16x32_bf16 v[152:155], v[8:11], v[192:195], v[152:155]\n\t"
            "v_mfma_f32_16x16x32_bf16 v[152:155], v[12:15], v[172:175], v[152:155]\n\t"
            "buffer_store_dwordx4 v[164:167], v197, s[40:43], s45 offen offset:128 sc1\n\t"
            "v_mfma_f32_16x16x32_bf16 v[160:163], v[36:39], v[172:175], v[160:163]\n\t"
            "v_mfma_f32_16x16x32_bf16 v[160:163], v[32:35], v[192:195], v[160:163]\n\t"
            "v_mfma_f32_16x16x32_bf16 v[176:179], v[56:59], v[192:195], v[176:179]\n\t"
            "v_mfma_f32_16x16x32_bf16 v[176:179], v[60:63], v[172:175], v[176:179]\n\t"
            "v_mfma_f32_16x16x32_bf16 v[148:151], v[84:87], v[172:175], v[148:151]\n\t"
            "v_mfma_f32_16x16x32_bf16 v[148:151], v[80:83], v[192:195], v[148:151]\n\t"
            "v_mfma_f32_16x16x32_bf16 v[156:159], v[104:107], v[192:195], v[156:159]\n\t"
            "v_mfma_f32_16x16x32_bf16 v[156:159], v[108:111], v[172:175], v[156:159]\n\t"
            "s_waitcnt lgkmcnt(0)\n\t"
            "v_mfma_f32_16x16x32_bf16 v[152:155], v[16:19], v[184:187], v[152:155]\n\t"
            "v_mfma_f32_16x16x32_bf16 v[152:155], v[20:23], v[188:191], v[152:155]\n\t"
            "v_mfma_f32_16x16x32_bf16 v[160:163], v[44:47], v[188:191], v[160:163]\n\t"
            "v_mfma_f32_16x16x32_bf16 v[160:163], v[40:43], v[184:187], v[160:163]\n\t"
            "v_mfma_f32_16x16x32_bf16 v[180:183], v[132:135], v[172:175], v[180:183]\n\t"
            "v_mfma_f32_16x16x32_bf16 v[180:183], v[128:131], v[192:195], v[180:183]\n\t"
            "v_mfma_f32_16x16x32_bf16 v[176:179], v[64:67], v[184:187], v[176:179]\n\t"
            "v_mfma_f32_16x16x32_bf16 v[176:179], v[68:71], v[188:191], v[176:179]\n\t"
            "s_nop 1\n\t"
            "v_cvt_pk_bf16_f32 v152, v152, v153\n\t"
            "v_mfma_f32_16x16x32_bf16 v[148:151], v[92:95], v[188:191], v[148:151]\n\t"
            "v_cvt_pk_bf16_f32 v153, v154, v155\n\t"
            "v_mfma_f32_16x16x32_bf16 v[148:151], v[88:91], v[184:187], v[148:151]\n\t"
            "v_cvt_pk_bf16_f32 v154, v160, v161\n\t"
            "v_cvt_pk_bf16_f32 v155, v162, v163\n\t"
            "v_mfma_f32_16x16x32_bf16 v[156:159], v[112:115], v[184:187], v[156:159]\n\t"
            "v_cvt_pk_bf16_f32 v176, v176, v177\n\t"
            "v_cvt_pk_bf16_f32 v177, v178, v179\n\t"
            "v_mfma_f32_16x16x32_bf16 v[156:159], v[116:119], v[188:191], v[156:159]\n\t"
            "buffer_store_dwordx4 v[152:155], v198, s[40:43], s45 offen sc1\n\t"
            "v_mfma_f32_16x16x32_bf16 v[180:183], v[140:143], v[188:191], v[180:183]\n\t"
            "v_cvt_pk_bf16_f32 v178, v148, v149\n\t"
            "v_cvt_pk_bf16_f32 v179, v150, v151\n\t"
            "v_mfma_f32_16x16x32_bf16 v[180:183], v[136:139], v[184:187], v[180:183]\n\t"
            "buffer_store_dwordx4 v[176:179], v198, s[40:43], s45 offen offset:64 sc1\n\t"
            "s_nop 1\n\t"
            "v_cvt_pk_bf16_f32 v156, v156, v157\n\t"
            "v_cvt_pk_bf16_f32 v157, v158, v159\n\t"
            "s_nop 2\n\t"
            "v_cvt_pk_bf16_f32 v158, v180, v181\n\t"
            "v_cvt_pk_bf16_f32 v159, v182, v183\n\t"
            "buffer_store_dwordx4 v[156:159], v198, s[40:43], s45 offen offset:128 sc1\n\t"
            "s_nop 7\n\t"
            : B_SIO, "+{v247}"(l16_), "+{v228}"(q0), "+{v229}"(q1), "+{v232}"(q2), "+{v233}"(q3), "+{v234}"(q4)
            : "{v242}"(oE_), "{v243}"(oO_), "{v244}"(pR0_), "{v245}"(pR1_), "{v246}"(pR2_), "{v250}"(pXw_), "{v251}"(pSTw_), "{v252}"(aCA_), "{v253}"(aB_), "{v254}"(aA_), "{v255}"(idx_), B_SIN, "{s44}"(ARR_OFF + blockIdx.x * 8), "{s[80:81]}"(mrow), "{s82}"(wave == 6 && cnt == 21 ? REC_OFF + (start - 20 * ((blockIdx.x & 7) * 32 + xi)) * 1280 : 0)
            : "v144", "v145", "v146", "v147", "v148", "v149", "v150", "v151", "v152", "v153", "v154", "v155", "v156", "v157", "v158", "v159", "v160", "v161", "v162", "v163", "v164", "v165", "v166", "v167", "v168", "v169", "v170", "v171", "v172", "v173", "v174", "v175", "v176", "v177", "v178", "v179", "v180", "v181", "v182", "v183", "v184", "v185", "v186", "v187", "v188", "v189", "v190", "v191", "v192", "v193", "v194", "v195", "v196", "v197", "v198", "v199", "v236", "v237", "v248", "v249", "v0", "v1", "v2", "v3", "v4", "v5", "v6", "v7", "v8", "v9", "v10", "v11", "v12", "v13", "v14", "v15", "v16", "v17", "v18", "v19", "v20", "v21", "v22", "v23", "v24", "v25", "v26", "v27", "v28", "v29", "v30", "v31", "v32", "v33", "v34", "v35", "v36", "v37", "v38", "v39", "v40", "v41", "v42", "v43", "v44", "v45", "v46", "v47", "v48", "v49", "v50", "v51", "v52", "v53", "v54", "v55", "v56", "v57", "v58", "v59", "v60", "v61", "v62", "v63", "v64", "v65", "v66", "v67", "v68", "v69", "v70", "v71", "v72", "v73", "v74", "v75", "v76", "v77", "v78", "v79", "v80", "v81", "v82", "v83", "v84", "v85", "v86", "v87", "v88", "v89", "v90", "v91", "v92", "v93", "v94", "v95", "v96", "v97", "v98", "v99", "v100", "v101", "v102", "v103", "v104", "v105", "v106", "v107", "v108", "v109", "v110", "v111", "v112", "v113", "v114", "v115", "v116", "v117", "v118", "v119", "v120", "v121", "v122", "v123", "v124", "v125", "v126", "v127", "v128", "v129", "v130", "v131", "v132", "v133", "v134", "v135", "v136", "v137", "v138", "v139", "v140", "v141", "v142", "v143", "v200", "v201", "v202", "v203", "v204", "v205", "v206", "v207", "v208", "v209", "v210", "v211", "v212", "v213", "v214", "v215", "v216", "v217", "v218", "v219", "v220", "v221", "v222", "v223", "v224", "v225", "v226", "v227", "v230", "v231", "v235", B_SCLOB);
        }
        __builtin_amdgcn_endpgm();
    }
}

extern "C" void kernel_launch(void* const* d_in, const int* in_sizes, int n_in,
                              void* d_out, int out_size, void* d_ws, size_t ws_size,
                              hipStream_t stream) {
    const unsigned short* x = (const unsigned short*)d_in[0];
    const unsigned short* dw = (const unsigned short*)d_in[1];
    const unsigned short* wqkv = (const unsigned short*)d_in[2];
    const unsigned short* wo = (const unsigned short*)d_in[3];
    unsigned short* out = (unsigned short*)d_out;
    u32x4* wf = (u32x4*)d_ws;
    win_attn_kernel<<<GRID, 512, 0, stream>>>(x, dw, wqkv, wo, wf, out);
}
```

```cpp
#include <hip/hip_runtime.h>
#include <hip/hip_bf16.h>
#include <stdint.h>

typedef short bf16x8 __attribute__((ext_vector_type(8)));
typedef float f32x4 __attribute__((ext_vector_type(4)));
typedef float f32x2 __attribute__((ext_vector_type(2)));
typedef unsigned u32x2 __attribute__((ext_vector_type(2)));
typedef unsigned u32x4 __attribute__((ext_vector_type(4)));

constexpr int IMG = 511;
constexpr int CH = 192;
constexpr int NWX = 73;
constexpr int NWIN = NWX * NWX;
constexpr int GRID = 256;
constexpr int ROWBYTES = IMG * CH * 2;
constexpr int XBIAS = (IMG + 1) * CH * 2;
constexpr unsigned OOB = 0x40000000u;

constexpr int FLAG_OFF = 7856;
constexpr int X_OFF = 32768;
constexpr int X1_OFF = 53632;
constexpr int O_OFF = 74496;
constexpr int V_OFF = 92928;
constexpr int KQL_OFF = 111360;
constexpr int OL_OFF = 129024;
constexpr int AT_OFF = 137472;
constexpr int CT_OFF = AT_OFF + 19584;
constexpr int RS_OFF = CT_OFF + 4096;
constexpr int RSX_OFF = 162816;
constexpr int WL_OFF = RS_OFF + 2304 + 64;
constexpr int LDS_BYTES = WL_OFF + 128;
#define RS_BASE (RS_OFF + wave * 384)
constexpr int STL_OFF = O_OFF + 12288;
constexpr int DW_OFF = O_OFF + 8448;
constexpr int CP_OFF = X1_OFF + 16384;
#define LX_OFF(i) ((i) < 2 ? CT_OFF + 1024 + (i) * 384 : KQL_OFF + ((i) - 2) * 800)
constexpr int PF_OFF = CT_OFF + 2048;
#define LB_OFF (w < 3 ? V_OFF + w * 13952 : X1_OFF)
#define LPOS(wc) (((wc) & 1) ? 0 : 6)
constexpr unsigned CROW = 0x060a0230u;
constexpr unsigned CLANE = 0x00132C3Fu;

constexpr unsigned long long TOKMAP[6] = {0x20001c190f2a2202ull, 0x052517121f061a13ull, 0x181e08151d0b260cull, 0x1b2b2903102d0901ull, 0x04162e0e2824142cull, 0x110a072127232f0dull};
constexpr unsigned long long ROWMAP[8] = {0x08321b240f1c0006ull, 0x23310e2a09170d14ull, 0x210c2f030a273418ull, 0x1d2b11221f043739ull, 0x3234363a103e192dull, 0x291a20022c3f1605ull, 0x303a3c2628122e15ull, 0x39253d1e01070b13ull};

constexpr int WF_BYTES = 8 * 36 * 1024;
constexpr int FLG_OFF = WF_BYTES;
constexpr int ARR_OFF = FLG_OFF + 4096;
constexpr int EPO_OFF = ARR_OFF + 2048;
constexpr int CTR_OFF = EPO_OFF + 128;
constexpr int REC_OFF = WF_BYTES + 8192;
constexpr int WS_BYTES = REC_OFF + 209 * 1280;
constexpr int SC1 = 16;

#define MFMA32(a, b, c) __builtin_amdgcn_mfma_f32_16x16x32_bf16((a), (b), (c), 0, 0, 0)
#define BC8(v) __builtin_bit_cast(bf16x8, (v))
#define BAR() asm volatile("s_waitcnt lgkmcnt(0)\n\ts_barrier" ::: "memory")
#define SB() __builtin_amdgcn_sched_barrier(0)
#define OPQ(x) asm volatile("" : "+v"(x))
#define ZERO4 ((f32x4){0.f, 0.f, 0.f, 0.f})

typedef __bf16 nbf16x2 __attribute__((ext_vector_type(2)));
__device__ __forceinline__ unsigned pk2(float a, float b) {
    f32x2 v = {a, b};
    nbf16x2 r = __builtin_convertvector(v, nbf16x2);
    return __builtin_bit_cast(unsigned, r);
}
__device__ __forceinline__ float bflo(unsigned u) { return __uint_as_float(u << 16); }
__device__ __forceinline__ float bfhi(unsigned u) { return __uint_as_float(u & 0xffff0000u); }

__device__ __forceinline__ float bf2f(unsigned short v) { return __uint_as_float((unsigned)v << 16); }
__device__ __forceinline__ unsigned byte_of(unsigned long long a, unsigned long long b, int i) {
    return (unsigned)(((i & 8) ? b : a) >> (8 * (i & 7))) & 0xFFu;
}
#define M24(a, b) ((unsigned)__mul24((int)(a), (int)(b)))
__device__ __forceinline__ void seq_to_win(int seq, int& wy, int& wx) {
    const bool last = seq >= 5110;
    const int s = last ? 35 : M24(seq, 28729) >> 22, r = seq - M24(146, s);
    const int kx = last ? M24(r, 171) >> 10 : r >> 2, pos = r - M24(last ? 6 : 4, kx);
    const bool low = last && pos >= (kx == 36 ? 2 : 4);
    wy = low ? 72 : 2 * s + (pos & 1);
    wx = 2 * kx + (low ? (kx == 36 ? 0 : pos - 4) : pos >> 1);
}
__device__ __forceinline__ int win_to_seq(int wy, int wx) {
    const int kx = wx >> 1;
    return M24(146, wy == 72 ? 35 : wy >> 1) + M24(wy >= 70 ? 6 : 4, kx) + (wy == 72 ? (kx == 36 ? 2 : 4 + (wx & 1)) : 2 * (wx & 1) + (wy & 1));
}
__device__ __forceinline__ int block_of(int wy, int wx) { return wy >= 70 ? 1295 + (wx & ~1) + (wy == 72) : M24(37, wy >> 1) + (wx >> 1); }
__device__ __forceinline__ void block_pos(int b, int& ky, int& kx) {
    const int s = M24(b, 1772) >> 16, l = b - 1295;
    kx = l >= 0 ? l >> 1 : b - M24(37, s);
    ky = l >= 0 ? 70 + 2 * (l & 1) : 2 * s;
}
__device__ __forceinline__ unsigned win_org(int wy, int wx) { return M24(wy, 7 * ROWBYTES) + M24(wx, 7 * CH * 2); }
__device__ __forceinline__ unsigned wl_entry(int wy, int wx) {
    return win_org(wy, wx) | (wy == 0 ? 0x08000000u : 0u) | (wx == 0 ? 0x10000000u : 0u) |
           (wx == NWX - 1 ? 0x20000000u : 0u) | ((unsigned)(wx & 1) << 30) | ((unsigned)(wy & 1) << 31);
}
__device__ __forceinline__ unsigned dma_off(int lane) {
    const int col = (lane * 43) >> 8;
    return (unsigned)(col * (CH * 2 - 96) + 16 * lane) | (col == 0 ? 0x10000000u : 0u) | (col == 8 ? 0x20000000u : 0u);
}

#if defined(__HIP_DEVICE_COMPILE__)
#define NOPK __attribute__((target("no-packed-fp32-ops")))
#else
#define NOPK
#endif
__global__ __launch_bounds__(512, 2) NOPK void win_attn_kernel(const unsigned short* __restrict__ x,
                                                          const unsigned short* __restrict__ dw, const unsigned short* __restrict__ wqkv,
                                                          const unsigned short* __restrict__ wo, u32x4* wf,
                                                          unsigned short* __restrict__ out) {
    __shared__ __attribute__((aligned(16))) char smem[LDS_BYTES];

    asm volatile("wk_code_begin:");
    const int tid = threadIdx.x;
    const int wave = __builtin_amdgcn_readfirstlane(tid >> 6);
    const int w = wave & 3;
    const unsigned ldsb = (unsigned)(size_t)smem;
    {
        unsigned long long pc;
        unsigned len, rel;
        asm volatile("s_getpc_b64 %0\n.Lwk_here%=:\n\ts_mov_b32 %1, wk_code_end-wk_code_begin\n\ts_mov_b32 %2, .Lwk_here%=-wk_code_begin" : "=s"(pc), "=s"(len), "=s"(rel));
        pc -= rel;
        const u32x4 cr = {(unsigned)pc, (unsigned)(pc >> 32) & 0xffffu, len, 0x00020000u};
        asm volatile("s_mov_b32 m0, %0\n\ts_nop 0\n\tbuffer_load_dwordx4 %1, %2, %3 offen lds"
                     :: "s"(ldsb + X1_OFF + 14336 + (w & 1) * 1024), "v"((tid & 63) * 16), "s"(cr), "s"((wave * 32 + (blockIdx.x >> 3)) * 1024) : "m0", "memory");
    }
    *(unsigned*)(smem + PF_OFF + tid * 4) = 0x9E3779B9u * (tid + 1);
    *(unsigned*)(smem + (tid < 192 ? AT_OFF + 18816 + tid * 4 : FLAG_OFF + (tid & 3) * 4)) = 0u;
    BAR();
    asm volatile("" :: "s"(x), "s"(dw), "s"(wqkv), "s"(wo), "s"(wf));
    const auto wrsrc = __builtin_amdgcn_make_buffer_rsrc((void*)wf, 0, WS_BYTES, 0x00020000);
    unsigned tag_lo = 0, tag_hi = 0;
#define TAG_READY                                                                                  \
    {                                                                                              \
        unsigned long long tag_;                                                                   \
        asm volatile("s_load_dwordx2 %0, %1, %2\n\ts_waitcnt lgkmcnt(0)" : "=s"(tag_) : "s"(wf), "n"(EPO_OFF) : "memory"); \
        tag_lo = (unsigned)tag_ + 1u; tag_hi = (unsigned)(tag_ >> 32);                             \
    }
    const int pb = blockIdx.x;
    if (pb < 36) {
        const int lane = tid & 63, n = lane & 15, g = lane >> 4;
        const bool isO = wave >= 6;
        const int fr = isO ? 216 + 2 * pb + (wave - 6) : 6 * pb + wave;
        const int tile = fr / 6, ks = fr - 6 * tile, tw = tile / 6, mdt = tile - 6 * tw;
        const int col = isO ? 96 * (tw - 6) + 32 * (mdt >> 1) + 8 * (n >> 2) + 4 * (mdt & 1) + (n & 3)
                            : (tw < 4 ? (mdt < 3 ? 192 : -48) + 48 * tw : 96 * tw) + 16 * mdt + n;
        const int rb = isO ? 384 : 1152;
        const auto rr = __builtin_amdgcn_make_buffer_rsrc((void*)(isO ? wo : wqkv), 0, 192 * rb, 0x00020000);
        const int vo = (48 * (g >> 1) + 8 * (g & 1)) * rb + 2 * col;
        float v[8];
#pragma unroll
        for (int j = 0; j < 8; ++j) v[j] = bf2f(__builtin_amdgcn_raw_buffer_load_b16(rr, vo, (96 * (ks & 1) + 16 * (ks >> 1) + j) * rb, 0));
        TAG_READY
        float* red = (float*)smem;
        red[n * 36 + wave * 4 + g] = ((v[0] + v[1]) + (v[2] + v[3])) + ((v[4] + v[5]) + (v[6] + v[7]));
        BAR();
        float mean = 0.f;
#pragma unroll
        for (int i = 0; i < 6; ++i) {
            const f32x4 p = *(const f32x4*)(red + n * 36 + 4 * i);
            mean += (p[0] + p[1]) + (p[2] + p[3]);
        }
        mean = isO ? 0.f : mean * (1.f / 192.f);
        u32x4 o;
#pragma unroll
        for (int j = 0; j < 4; ++j) o[j] = pk2(v[2 * j] - mean, v[2 * j + 1] - mean);
        __builtin_amdgcn_raw_buffer_store_b128(o, wrsrc, lane * 16, fr * 1024, SC1);
        if (pb == 0 && tid == 448) __builtin_amdgcn_raw_buffer_store_b32(0u, wrsrc, CTR_OFF, 0, SC1);
        asm volatile("s_waitcnt vmcnt(0)" ::: "memory");
        BAR();
        if (tid < 8) __builtin_amdgcn_raw_buffer_store_b64((u32x2){tag_lo, tag_hi}, wrsrc, FLG_OFF + tid * 512 + pb * 8, 0, SC1);
    }
    const int xi = blockIdx.x >> 3, nshort = (int)(0x0506060606060606ull >> (8 * (blockIdx.x & 7))) & 63;
    const int start = 672 * (blockIdx.x & 7) - ((int)(0x2a241e18120c0600ull >> (8 * (blockIdx.x & 7))) & 63) + 21 * xi - (xi < nshort ? xi : nshort);
    const int cnt = xi < nshort ? 20 : 21;

    const auto xrsrc = __builtin_amdgcn_make_buffer_rsrc((void*)((const char*)x - XBIAS), 0, IMG * IMG * CH * 2 + XBIAS, 0x00020000);
#define LANE_SETUP                                                                                 \
    int lane;                                                                                      \
    asm volatile("v_mbcnt_lo_u32_b32 %0, -1, 0\n\tv_mbcnt_hi_u32_b32 %0, -1, %0" : "=v"(lane));    \
    __builtin_assume(lane >= 0 && lane < 64);                                                      \
    const int g = lane >> 4, l15 = lane & 15, sw = l15 >> 1;                                       \
    int fE = X_OFF + l15 * 384 + ((g ^ sw) << 4);                                                  \
    OPQ(fE);

#define DMA_SLICE(en)                                                                              \
        {                                                                                          \
            const unsigned vo_ = dma_off(lane) & (0x401fffffu | ((en) & 0x38000000u));             \
            const unsigned so_ = ((en) & 0x7ffffffu) + 96 * w;                                     \
            _Pragma("unroll") for (int r = 0; r < 9; ++r) {                                        \
                unsigned m0_ = ldsb + w * 8192 + r * 872;                                          \
                unsigned sr_ = (r == 0 && ((en) & 0x08000000u)) ? OOB : so_ + r * ROWBYTES;        \
                asm volatile("s_mov_b32 m0, %0\n\ts_mov_b32 exec_hi, 0x3fffff\n\tbuffer_load_dwordx4 %1, %2, %3 offen lds\n\ts_mov_b32 exec_hi, -1" \
                             :: "s"(m0_), "v"(vo_), "s"(xrsrc), "s"(sr_) : "m0", "memory");        \
            }                                                                                      \
        }
#define XPAT(r) (((r) < 48 ? (r) : 48) * 384 + (((2 * w) ^ (((r) >> 1) & 7)) << 4))
#define SPAT(r) ((w >> 1) * 1024 + (w & 1) * 8 + (r) * 16)
#define CONV_CONSTS(var) CONV_ROWS(var) CONV_OPS
#define CONV_ROWS(var) \
            const unsigned long long rlo = g == 0 ? ROWMAP[0] : (g == 1 ? ROWMAP[2] : (g == 2 ? ROWMAP[4] : ROWMAP[6])); \
            const unsigned long long rhi = g == 0 ? ROWMAP[1] : (g == 1 ? ROWMAP[3] : (g == 2 ? ROWMAP[5] : ROWMAP[7])); \
            int xrow = byte_of(rlo, rhi, l15); \
            if (lane == (int)((CLANE >> (8 * (var))) & 0xFF)) xrow = 48; \
            if (lane == 63) xrow = (CROW >> (8 * (var))) & 0xFF; \
            const unsigned pXw_ = XPAT(xrow); \
            const unsigned pSTw_ = (w >> 1) * 1024 + (w & 1) * 8 + xrow * 16; \
            const int oy = (0x5320 >> (4 * (l15 >> 2))) & 15, ox = (0x5320 >> (4 * (l15 & 3))) & 15; \
            const unsigned aB_ = ldsb + w * 8192 + (oy + (g >> 1)) * 872 + (ox + 2 * (g & 1)) * 96;
#define CONV_OPS \
            const int dyi = g - (l15 >> 3); \
            const unsigned aA_ = ldsb + AT_OFF + (l15 & 3) * 16 + \
                                 ((dyi < 0 || dyi > 2) ? 18816 : (dyi == 0 ? 0 : (dyi == 1 ? 6208 : 12544)) + ((l15 >> 2) & 1) * 3072 + w * 768); \
            const unsigned idx_ = (l15 & 3) * 0x5555u; \
            const unsigned se = (l15 == 4 * g) ? 0x3F803F80u : 0u; \
            const u32x4 sel4 = {se, se, se, se};

    unsigned en0, en1;
    {
        LANE_SETUP
        const int px = (lane * 43) >> 8;

        int wy, wx;
        seq_to_win(start + (lane < cnt ? lane : cnt - 1), wy, wx);
        const unsigned en = wl_entry(wy, wx);
        en0 = __builtin_amdgcn_readlane(en, 0);
        en1 = __builtin_amdgcn_readlane(en, 1);
        if (wave >= 4) {
            const int bk = block_of(wy, wx);
            const int key0 = __builtin_amdgcn_readlane(bk, 0), nblk = __builtin_amdgcn_readlane(bk, 63) - key0 + 1;
            const int b = l15 < nblk ? l15 : 0;
            int ky, kx;
            block_pos(key0 + b, ky, kx);
            const unsigned org = l15 < nblk ? (win_org(ky, 2 * kx) + (5 * ROWBYTES + 5 * CH * 2 + XBIAS)) | (kx == 36 ? OOB : 0u) : OOB + 0x8000000u;
            const unsigned lo = (px & 3) * (CH * 2) + (lane - 6 * px) * 16, keep = (px & 2) ? ~0u : ~OOB;
#pragma unroll
            for (int p = 0; p < 4; ++p) {
                if (p < 3 || nblk > 6) {
                    const unsigned oa = __builtin_amdgcn_readlane(org, 2 * p), ob = __builtin_amdgcn_readlane(org, 2 * p + 1);
                    const unsigned vo_ = ((lane < 24 ? oa : ob) & keep) + lo;
#pragma unroll
                    for (int r = 0; r < 4; ++r) {
                        unsigned m0_ = ldsb + LB_OFF + (4 * p + r) * 872;
                        asm volatile("s_mov_b32 m0, %0\n\ts_mov_b32 exec_hi, 0xffff\n\tbuffer_load_dwordx4 %1, %2, %3 offen lds\n\ts_mov_b32 exec_hi, -1"
                                     :: "s"(m0_), "v"(vo_), "s"(xrsrc), "s"(96 * w + r * ROWBYTES) : "m0", "memory");
                    }
                }
            }
            SB();
            const int ly = ky + (g >> 1), lx = 2 * kx + (g & 1), li = win_to_seq(ly, lx) - start;
            const bool ok = ly < NWX && lx < NWX && li >= 0 && li < cnt && l15 < nblk;
            const int row = ok ? li : 47;
            unsigned* cp = (unsigned*)(smem + CP_OFF + (w * 64 + lane) * 16);
            *(u32x2*)cp = (u32x2){(unsigned)XPAT(row), SPAT(row) + (unsigned)(STL_OFF - O_OFF - 18816)};
            cp[2] = ldsb + LB_OFF + (4 * (b >> 1) + (g >> 1)) * 872 + (b & 1) * 384 + (g & 1) * 192;
        } else {
            {
                const auto dr = __builtin_amdgcn_make_buffer_rsrc((void*)dw, 0, 9 * 384, 0x00020000);
                unsigned m0_ = ldsb + DW_OFF + w * 864, vo_ = px * 288 + lane * 16;
                asm volatile("s_mov_b32 m0, %0\n\ts_mov_b32 exec_hi, 0x3fffff\n\tbuffer_load_dwordx4 %1, %2, %3 offen lds\n\ts_mov_b32 exec_hi, -1"
                             :: "s"(m0_), "v"(vo_), "s"(dr), "s"(96 * w) : "m0", "memory");
            }
            DMA_SLICE(en0)
            SB();
            if (wave == 2) {
                u32x4 so;
#pragma unroll
                for (int e = 0; e < 3; ++e) {
                    const int tk = byte_of(TOKMAP[2 * e], TOKMAP[2 * e + 1], l15);
                    const int ty = (tk * 37) >> 8, tx = tk - 7 * ty;
                    so[e] = (unsigned)(ty * ROWBYTES + tx * (CH * 2)) + 16 * g;
                }
                so[3] = dma_off(lane);
                *(u32x4*)(smem + CT_OFF + lane * 16) = so;
            }
            if (wave == 1 && lane < 32) *(unsigned*)(smem + WL_OFF + lane * 4) = lane < 20 ? en : 0x7ffffffu;
            if (wave == 1 && lane == 20) *(unsigned*)(smem + PF_OFF + 520) = en;
            asm volatile("s_waitcnt vmcnt(9)" ::: "memory");
            if (lane < 48) {
                unsigned short dv[9];
#pragma unroll
                for (int i = 0; i < 9; ++i) dv[i] = *(const unsigned short*)(smem + DW_OFF + w * 864 + i * 96 + lane * 2);
#pragma unroll
                for (int dy = 0; dy < 3; ++dy) {
                    const unsigned a = dv[3 * dy], b2 = dv[3 * dy + 1] | (dy == 1 ? 0x3F800000u : 0u), c = dv[3 * dy + 2];
                    char* p = smem + AT_OFF + (dy == 0 ? 0 : (dy == 1 ? 6208 : 12544)) + w * 768 + lane * 16;
                    *(u32x4*)p = (u32x4){a, b2, c, 0u};
                    *(u32x4*)(p + 3072) = (u32x4){0u, a, b2, c};
                }
            }
            asm volatile("s_mov_b64 exec, 1\n\tds_add_u32 %0, %1\n\ts_mov_b64 exec, -1" :: "v"(ldsb + FLAG_OFF + 8), "v"(1) : "memory");
        }
    }
    const unsigned long long mrow = 0x0001000100010001ull * ((1u << (cnt - 16)) - 1u);
#define KARGS                                                                                      \
    unsigned long long xb, wb, ob;                                                                 \
    asm volatile("s_load_dwordx2 %0, %3, 0x0\n\ts_load_dwordx2 %1, %3, 0x20\n\ts_load_dwordx2 %2, %3, 0x28\n\ts_waitcnt lgkmcnt(0)" \
                 : "=&s"(xb), "=&s"(wb), "=&s"(ob) : "s"(__builtin_amdgcn_kernarg_segment_ptr()) : "memory"); \
    xb -= XBIAS;                                                                                   \
    const u32x4 wr = {(unsigned)wb, (unsigned)(wb >> 32) & 0xffffu, (unsigned)WS_BYTES, 0x00020000u}; \
    const u32x4 orr = {(unsigned)ob, (unsigned)(ob >> 32) & 0xffffu, (unsigned)(IMG * IMG * CH * 2), 0x00020000u}; \
    const u32x4 xr = {(unsigned)xb, (unsigned)(xb >> 32) & 0xffffu, (unsigned)(IMG * IMG * CH * 2 + XBIAS), 0x00020000u};
    const int tk_ = (xi + 4) / 6;
    const unsigned touch = wave * 36864 + ((xi + 4 - 6 * tk_) * 6 + tk_) * 1024;
#define B_SCALARS \
            CONV_CONSTS(0) \
            const unsigned aCA_ = ldsb + CT_OFF + lane * 16; \
            KARGS \
            unsigned s45 = OOB, s46 = (en0 & 0x7ffffffu) + 96 * w, s47 = (en1 & 0x7ffffffu) + 96 * w; \
            unsigned s48 = ldsb + X_OFF, s67 = ldsb + X1_OFF, s76 = ldsb + WL_OFF + 8, s70 = 0; \
            unsigned s56 = __builtin_amdgcn_readfirstlane((en0 >> 30) * 4 + (en1 >> 30)); \
            unsigned s69 = 0x401fffffu | (en1 & 0x38000000u), s79 = (en1 & 0x08000000u) ? OOB : s47, s59 = ldsb + LX_OFF(1) + 32 * w; \
            unsigned cp_ = ldsb + CP_OFF + (w * 64 + lane) * 16; \
            unsigned long long xp = 0, sp = 0; \
            _Pragma("unroll") for (int v = 0; v < 4; ++v) { \
                const unsigned r = (CROW >> (8 * v)) & 0xFF; \
                xp |= (unsigned long long)XPAT(r) << (16 * v); \
                sp |= (unsigned long long)SPAT(r) << (16 * v); \
            }
#define B_SIO "+{s45}"(s45), "+{s46}"(s46), "+{s47}"(s47), "+{s48}"(s48), "+{s67}"(s67), "+{s76}"(s76), "+{s56}"(s56), "+{s70}"(s70), "+{s69}"(s69), "+{s79}"(s79), "+{s59}"(s59), "+{v203}"(cp_)
#define B_SIN "{v[238:241]}"(sel4), "{s[40:43]}"(orr), "{s[72:75]}"(xr), "{s39}"(0x401fffffu), "{s[28:31]}"(wr), "{s33}"(wave * 36864), "{s78}"(96 * w), \
              "{s27}"(96 * w - 384), "{s[50:51]}"(xp), "{s[52:53]}"(sp), "{s54}"(CLANE), "{s55}"(CROW & 0x0F0F0F0Fu), "{s[34:35]}"(0x0001000100010001ull), \
              "{s[60:61]}"(0x2222222222222222ull), "{s[62:63]}"(0x4444444444444444ull), "{s[64:65]}"(0x8888888888888888ull), \
              "{s66}"(ldsb + w * 8192), "{s71}"((ldsb + X_OFF) ^ (ldsb + X1_OFF))
#define B_SCLOB "s83", "s84", "s26", "s77", "s68", "s36", "s37", "s38", "s57", "s58", "s49", "vcc", "scc", "m0", "memory"

    if (wave < 4) {
        LANE_SETUP
        const int dW = X_OFF + l15 * 384 + (((2 * w + (g >> 1)) ^ sw) << 4) + 8 * (g & 1);
        const int vB = V_OFF + w * 4608 + 1024 + lane * 8, rsR = RS_BASE + l15 * 4;
        {
            const unsigned rsb = ldsb + RS_BASE;
            const unsigned pE_ = fE - X_OFF, aO_ = ldsb + dW + (O_OFF - X_OFF), sel_ = (l15 & 3) ? 0u : 1u, aVB_ = ldsb + vB;
            unsigned pST_ = lane * 16, aPLw_ = rsb + lane * 2, aRSr_ = ldsb + rsR, aRSw_ = rsb + lane * 4;
            unsigned g16_ = (l15 & 3) ? ldsb + AT_OFF + 18816 : g * 16, g8_ = (l15 & 3) ? ldsb + AT_OFF + 18816 : g * 8;
            unsigned loVL_ = ((2 * w + (l15 >> 3)) << 4) + (l15 & 7) * 2, loOL_ = g * 128;
            unsigned on = (l15 & 3) ? 0u : 0x3F803F80u;
            CONV_CONSTS(en0 >> 30)
            unsigned c0 = lane == (int)((CLANE >> (8 * (en0 >> 30))) & 0xFF) ? LX_OFF(0) - X_OFF + 32 * w : pXw_, c1 = pSTw_, c2 = lane * 16, c3 = aB_, c4 = aA_, c5 = idx_;
            unsigned c6 = se, c7 = se, c8 = se, c9 = se, s89 = FLG_OFF + (blockIdx.x & 7) * 512;
            unsigned s48 = ldsb + X_OFF, s70 = 0;
            unsigned s72 = ldsb + KQL_OFF + w * 192, s73 = ldsb + OL_OFF, s74 = ldsb + KQL_OFF + 784, s77 = ldsb + OL_OFF, s76 = ldsb + KQL_OFF + 768 + w * 4;
            KARGS
            asm volatile(
            "s_load_dwordx2 s[82:83], s[84:85], 301056\n\t"
            "s_mov_b32 m0, 159104\n\t"
            "s_mov_b32 s67, 32768\n\t"
            "s_mov_b32 s60, 0x22222222\n\t"
            "s_mov_b32 s61, 0x22222222\n\t"
            "s_mov_b32 s62, 0x44444444\n\t"
            "s_mov_b32 s63, 0x44444444\n\t"
            "s_mov_b32 s64, 0x88888888\n\t"
            "s_mov_b32 s65, 0x88888888\n\t"
            "s_waitcnt vmcnt(0) lgkmcnt(0)\n\t"
            "s_add_u32 s82, s82, 1\n\t"
            "s_cmp_lg_u32 s80, 16\n\t"
            "s_cbranch_scc1 .Lwa_p0%=\n\t"
            "s_mov_b64 exec, 0x3ffff\n\t"
            "buffer_load_dwordx4 v252, s[84:87], s89 offen sc1 lds\n\t"
            "s_mov_b64 exec, -1\n\t"
            ".Lwa_p0%=:\n\t"
            "s_call_b64 vcc, wk_conv\n\t"
            "v_min_u32 v146, 0x230, v252\n\t"
            "v_lshrrev_b32 v146, 1, v146\n\t"
            "v_add_u32 v146, 159104, v146\n\t"
            "s_mov_b32 s81, 0x10000\n\t"
            "v_mov_b32 v240, v200\n\t"
            "v_mov_b32 v241, v200\n\t"
            "v_mov_b32 v242, v200\n\t"
            "v_mov_b32 v243, v200\n\t"
            "v_mov_b32 v248, v204\n\t"
            "v_mov_b32 v249, v205\n\t"
            "v_mov_b32 v250, v206\n\t"
            "v_mov_b32 v251, v207\n\t"
            "v_mov_b32 v253, v209\n\t"
            "v_mov_b32 v254, v210\n\t"
            "v_mov_b32 v255, v211\n\t"
            ".Lwa_p1%=:\n\t"
            "ds_read_b64 v[144:145], v146\n\t"
            "s_waitcnt lgkmcnt(0)\n\t"
            "v_cmp_ne_u32 vcc, s82, v144\n\t"
            "v_cmp_ne_u32 s[60:61], s83, v145\n\t"
            "s_or_b64 vcc, vcc, s[60:61]\n\t"
            "s_cbranch_scc0 .Lwa_p3%=\n\t"
            "s_sub_u32 s81, s81, 1\n\t"
            "s_cbranch_scc1 .Lwa_p3%=\n\t"
            "s_cmp_lg_u32 s80, 16\n\t"
            "s_cbranch_scc1 .Lwa_p2%=\n\t"
            "s_mov_b64 exec, 0x3ffff\n\t"
            "buffer_load_dwordx4 v252, s[84:87], s89 offen sc1 lds\n\t"
            "s_mov_b64 exec, -1\n\t"
            "s_waitcnt vmcnt(24)\n\t"
            ".Lwa_p2%=:\n\t"
            "s_sleep 3\n\t"
            "s_branch .Lwa_p1%=\n\t"
            ".Lwa_p3%=:\n\t"
            "v_mov_b32 v252, v208\n\t"
            "v_mov_b32 v144, s82\n\t"
            "v_mov_b32 v145, s83\n\t"
            "v_mov_b32 v146, 159616\n\t"
            "ds_write_b64 v146, v[144:145]\n\t"
            "s_mov_b32 s60, 0xffff0000\n\t"
            "s_mov_b32 s61, 0x0\n\t"
            "s_mov_b32 s62, 0x0\n\t"
            "s_mov_b32 s63, 0xffff\n\t"
            "s_mov_b32 s64, 0x0\n\t"
            "s_mov_b32 s65, 0xffff0000\n\t"
            "s_mov_b32 s66, 0xffffffff\n\t"
            "s_mov_b32 s67, 0x1ffff\n\t"
            "buffer_load_dwordx4 v[140:143], v248, s[84:87], s88 offen sc1\n\t"
            "s_add_u32 s49, s46, 0\n\t"
            "buffer_load_dwordx4 v[0:3], v248, s[84:87], s49 offen sc1\n\t"
            "s_add_u32 s49, s46, 6144\n\t"
            "buffer_load_dwordx4 v[24:27], v248, s[84:87], s49 offen sc1\n\t"
            "s_add_u32 s49, s46, 12288\n\t"
            "buffer_load_dwordx4 v[48:51], v248, s[84:87], s49 offen sc1\n\t"
            "s_add_u32 s49, s46, 18432\n\t"
            "buffer_load_dwordx4 v[72:75], v248, s[84:87], s49 offen sc1\n\t"
            "s_add_u32 s49, s46, 24576\n\t"
            "buffer_load_dwordx4 v[96:99], v248, s[84:87], s49 offen sc1\n\t"
            "s_add_u32 s49, s46, 30720\n\t"
            "buffer_load_dwordx4 v[120:123], v248, s[84:87], s49 offen sc1\n\t"
            "s_add_u32 s49, s46, 1024\n\t"
            "buffer_load_dwordx4 v[4:7], v248, s[84:87], s49 offen sc1\n\t"
            "s_add_u32 s49, s46, 7168\n\t"
            "buffer_load_dwordx4 v[28:31], v248, s[84:87], s49 offen sc1\n\t"
            "s_add_u32 s49, s46, 13312\n\t"
            "buffer_load_dwordx4 v[52:55], v248, s[84:87], s49 offen sc1\n\t"
            "s_add_u32 s49, s46, 19456\n\t"
            "buffer_load_dwordx4 v[76:79], v248, s[84:87], s49 offen sc1\n\t"
            "s_add_u32 s49, s46, 25600\n\t"
            "buffer_load_dwordx4 v[100:103], v248, s[84:87], s49 offen sc1\n\t"
            "s_add_u32 s49, s46, 31744\n\t"
            "buffer_load_dwordx4 v[124:127], v248, s[84:87], s49 offen sc1\n\t"
            "s_add_u32 s49, s46, 2048\n\t"
            "buffer_load_dwordx4 v[8:11], v248, s[84:87], s49 offen sc1\n\t"
            "s_add_u32 s49, s46, 8192\n\t"
            "buffer_load_dwordx4 v[32:35], v248, s[84:87], s49 offen sc1\n\t"
            "s_add_u32 s49, s46, 14336\n\t"
            "buffer_load_dwordx4 v[56:59], v248, s[84:87], s49 offen sc1\n\t"
            "s_add_u32 s49, s46, 20480\n\t"
            "buffer_load_dwordx4 v[80:83], v248, s[84:87], s49 offen sc1\n\t"
            "s_add_u32 s49, s46, 26624\n\t"
            "buffer_load_dwordx4 v[104:107], v248, s[84:87], s49 offen sc1\n\t"
            "s_add_u32 s49, s46, 32768\n\t"
            "buffer_load_dwordx4 v[128:131], v248, s[84:87], s49 offen sc1\n\t"
            "s_add_u32 s49, s46, 3072\n\t"
            "buffer_load_dwordx4 v[12:15], v248, s[84:87], s49 offen sc1\n\t"
            "s_add_u32 s49, s46, 9216\n\t"
            "buffer_load_dwordx4 v[36:39], v248, s[84:87], s49 offen sc1\n\t"
            "s_add_u32 s49, s46, 15360\n\t"
            "buffer_load_dwordx4 v[60:63], v248, s[84:87], s49 offen sc1\n\t"
            "s_add_u32 s49, s46, 21504\n\t"
            "buffer_load_dwordx4 v[84:87], v248, s[84:87], s49 offen sc1\n\t"
            "s_add_u32 s49, s46, 27648\n\t"
            "buffer_load_dwordx4 v[108:111], v248, s[84:87], s49 offen sc1\n\t"
            "s_add_u32 s49, s46, 33792\n\t"
            "buffer_load_dwordx4 v[132:135], v248, s[84:87], s49 offen sc1\n\t"
            "s_add_u32 s49, s46, 4096\n\t"
            "buffer_load_dwordx4 v[16:19], v248, s[84:87], s49 offen sc1\n\t"
            "s_add_u32 s49, s46, 10240\n\t"
            "buffer_load_dwordx4 v[40:43], v248, s[84:87], s49 offen sc1\n\t"
            "s_add_u32 s49, s46, 16384\n\t"
            "buffer_load_dwordx4 v[64:67], v248, s[84:87], s49 offen sc1\n\t"
            "s_add_u32 s49, s46, 22528\n\t"
            "buffer_load_dwordx4 v[88:91], v248, s[84:87], s49 offen sc1\n\t"
            "s_add_u32 s49, s46, 28672\n\t"
            "buffer_load_dwordx4 v[112:115], v248, s[84:87], s49 offen sc1\n\t"
            "s_add_u32 s49, s46, 34816\n\t"
            "buffer_load_dwordx4 v[136:139], v248, s[84:87], s49 offen sc1\n\t"
            "s_add_u32 s49, s46, 5120\n\t"
            "buffer_load_dwordx4 v[20:23], v248, s[84:87], s49 offen sc1\n\t"
            "s_add_u32 s49, s46, 11264\n\t"
            "buffer_load_dwordx4 v[44:47], v248, s[84:87], s49 offen sc1\n\t"
            "s_add_u32 s49, s46, 17408\n\t"
            "buffer_load_dwordx4 v[68:71], v248, s[84:87], s49 offen sc1\n\t"
            "s_add_u32 s49, s46, 23552\n\t"
            "buffer_load_dwordx4 v[92:95], v248, s[84:87], s49 offen sc1\n\t"
            "s_add_u32 s49, s46, 29696\n\t"
            "buffer_load_dwordx4 v[116:119], v248, s[84:87], s49 offen sc1\n\t"
            "s_add_u32 s49, s46, 35840\n\t"
            "buffer_load_dwordx4 v[140:143], v248, s[84:87], s49 offen sc1\n\t"
            "s_waitcnt lgkmcnt(0)\n\t"
            "s_barrier\n\t"
            "s_setprio 2\n\t"
            "v_add_u32 v235, s71, v248\n\t"
            "v_add_u32 v232, s71, v244\n\t"
            "v_xor_b32 v233, 64, v232\n\t"
            "v_subrev_u32 v239, s75, v251\n\t"
            "v_mul_u32_u24 v239, 200, v239\n\t"
            "v_lshrrev_b32 v234, 3, v255\n\t"
            "v_add3_u32 v234, v234, v239, s72\n\t"
            "ds_read_b128 v[228:231], v235 offset:12288\n\t"
            "ds_read_b128 v[144:147], v232\n\t"
            "ds_read_b128 v[148:151], v233\n\t"
            "ds_read_b128 v[204:207], v232 offset:6144\n\t"
            "ds_read_b128 v[208:211], v233 offset:6144\n\t"
            "ds_read_b128 v[152:155], v232 offset:128\n\t"
            "ds_read_b128 v[156:159], v233 offset:128\n\t"
            "ds_read_b128 v[212:215], v232 offset:6272\n\t"
            "ds_read_b128 v[224:227], v233 offset:6272\n\t"
            "ds_read_b128 v[160:163], v232 offset:256\n\t"
            "ds_read_b128 v[164:167], v233 offset:256\n\t"
            "s_waitcnt vmcnt(24)\n\t"
            "s_waitcnt lgkmcnt(10)\n\t"
            "v_add_f32 v236, v228, v230\n\t"
            "v_add_f32 v237, v229, v231\n\t"
            "s_waitcnt lgkmcnt(8)\n\t"
            "v_mfma_f32_16x16x32_bf16 v[168:171], v[0:3], v[144:147], 0\n\t"
            "ds_read_b128 v[228:231], v235 offset:13312\n\t"
            "v_lshrrev_b32 v235, 4, v255\n\t"
            "v_mfma_f32_16x16x32_bf16 v[168:171], v[4:7], v[148:151], v[168:171]\n\t"
            "v_add3_u32 v235, v235, v239, s72\n\t"
            "v_mfma_f32_16x16x32_bf16 v[176:179], v[48:51], v[144:147], 0\n\t"
            "v_mfma_f32_16x16x32_bf16 v[176:179], v[52:55], v[148:151], v[176:179]\n\t"
            "v_mfma_f32_16x16x32_bf16 v[172:175], v[24:27], v[144:147], 0\n\t"
            "v_mfma_f32_16x16x32_bf16 v[172:175], v[28:31], v[148:151], v[172:175]\n\t"
            "v_mfma_f32_16x16x32_bf16 v[180:183], v[72:75], v[144:147], 0\n\t"
            "v_mfma_f32_16x16x32_bf16 v[180:183], v[76:79], v[148:151], v[180:183]\n\t"
            "s_waitcnt lgkmcnt(0)\n\t"
            "v_add_f32 v228, v228, v230\n\t"
            "v_add_f32 v236, v236, v228\n\t"
            "v_mfma_f32_16x16x32_bf16 v[184:187], v[96:99], v[144:147], 0\n\t"
            "v_add_f32 v229, v229, v231\n\t"
            "v_mul_f32 v236, 0x3baaaaab, v236\n\t"
            "v_mfma_f32_16x16x32_bf16 v[184:187], v[100:103], v[148:151], v[184:187]\n\t"
            "v_add_f32 v237, v237, v229\n\t"
            "v_mul_f32_e64 v236, v236, -v236\n\t"
            "v_mfma_f32_16x16x32_bf16 v[188:191], v[120:123], v[144:147], 0\n\t"
            "v_fmac_f32 v236, 0x3baaaaab, v237\n\t"
            "v_max_f32 v236, 0, v236\n\t"
            "v_mfma_f32_16x16x32_bf16 v[188:191], v[124:127], v[148:151], v[188:191]\n\t"
            "v_add_f32 v236, 0x3727c5ac, v236\n\t"
            "v_rsq_f32 v236, v236\n\t"
            "v_mfma_f32_16x16x32_bf16 v[192:195], v[0:3], v[204:207], 0\n\t"
            "v_mfma_f32_16x16x32_bf16 v[192:195], v[4:7], v[208:211], v[192:195]\n\t"
            "ds_write_b32 v249, v236\n\t"
            "v_mfma_f32_16x16x32_bf16 v[196:199], v[24:27], v[204:207], 0\n\t"
            "ds_read_b32 v236, v251\n\t"
            "ds_read_b32 v237, v251 offset:64\n\t"
            "v_mfma_f32_16x16x32_bf16 v[196:199], v[28:31], v[208:211], v[196:199]\n\t"
            "v_mfma_f32_16x16x32_bf16 v[200:203], v[48:51], v[204:207], 0\n\t"
            "v_mfma_f32_16x16x32_bf16 v[200:203], v[52:55], v[208:211], v[200:203]\n\t"
            "v_mfma_f32_16x16x32_bf16 v[216:219], v[72:75], v[204:207], 0\n\t"
            "v_mfma_f32_16x16x32_bf16 v[216:219], v[76:79], v[208:211], v[216:219]\n\t"
            "v_mfma_f32_16x16x32_bf16 v[220:223], v[96:99], v[204:207], 0\n\t"
            "v_mfma_f32_16x16x32_bf16 v[220:223], v[100:103], v[208:211], v[220:223]\n\t"
            "v_mfma_f32_16x16x32_bf16 v[228:231], v[120:123], v[204:207], 0\n\t"
            "ds_read_b128 v[204:207], v232 offset:6400\n\t"
            "v_add_u32 v232, s48, v244\n\t"
            "v_mfma_f32_16x16x32_bf16 v[228:231], v[124:127], v[208:211], v[228:231]\n\t"
            "ds_read_b128 v[208:211], v233 offset:6400\n\t"
            "s_waitcnt vmcnt(12)\n\t"
            "v_mfma_f32_16x16x32_bf16 v[168:171], v[8:11], v[152:155], v[168:171]\n\t"
            "v_xor_b32 v233, 64, v232\n\t"
            "v_mfma_f32_16x16x32_bf16 v[168:171], v[12:15], v[156:159], v[168:171]\n\t"
            "v_mfma_f32_16x16x32_bf16 v[176:179], v[56:59], v[152:155], v[176:179]\n\t"
            "v_mfma_f32_16x16x32_bf16 v[176:179], v[60:63], v[156:159], v[176:179]\n\t"
            "v_mfma_f32_16x16x32_bf16 v[172:175], v[32:35], v[152:155], v[172:175]\n\t"
            "v_mfma_f32_16x16x32_bf16 v[172:175], v[36:39], v[156:159], v[172:175]\n\t"
            "v_mfma_f32_16x16x32_bf16 v[180:183], v[80:83], v[152:155], v[180:183]\n\t"
            "v_mfma_f32_16x16x32_bf16 v[180:183], v[84:87], v[156:159], v[180:183]\n\t"
            "v_mfma_f32_16x16x32_bf16 v[184:187], v[104:107], v[152:155], v[184:187]\n\t"
            "v_mfma_f32_16x16x32_bf16 v[184:187], v[108:111], v[156:159], v[184:187]\n\t"
            "v_mfma_f32_16x16x32_bf16 v[188:191], v[128:131], v[152:155], v[188:191]\n\t"
            "v_mfma_f32_16x16x32_bf16 v[188:191], v[132:135], v[156:159], v[188:191]\n\t"
            "v_mfma_f32_16x16x32_bf16 v[192:195], v[8:11], v[212:215], v[192:195]\n\t"
            "v_mfma_f32_16x16x32_bf16 v[192:195], v[12:15], v[224:227], v[192:195]\n\t"
            "v_mfma_f32_16x16x32_bf16 v[196:199], v[32:35], v[212:215], v[196:199]\n\t"
            "v_mfma_f32_16x16x32_bf16 v[196:199], v[36:39], v[224:227], v[196:199]\n\t"
            "v_mfma_f32_16x16x32_bf16 v[200:203], v[56:59], v[212:215], v[200:203]\n\t"
            "v_mfma_f32_16x16x32_bf16 v[200:203], v[60:63], v[224:227], v[200:203]\n\t"
            "v_mfma_f32_16x16x32_bf16 v[216:219], v[80:83], v[212:215], v[216:219]\n\t"
            "v_mfma_f32_16x16x32_bf16 v[216:219], v[84:87], v[224:227], v[216:219]\n\t"
            "v_mfma_f32_16x16x32_bf16 v[220:223], v[104:107], v[212:215], v[220:223]\n\t"
            "v_mfma_f32_16x16x32_bf16 v[220:223], v[108:111], v[224:227], v[220:223]\n\t"
            "v_mfma_f32_16x16x32_bf16 v[228:231], v[128:131], v[212:215], v[228:231]\n\t"
            "ds_read_b128 v[212:215], v232 offset:256\n\t"
            "v_mfma_f32_16x16x32_bf16 v[228:231], v[132:135], v[224:227], v[228:231]\n\t"
            "ds_read_b128 v[224:227], v233 offset:256\n\t"
            "s_waitcnt vmcnt(0)\n\t"
            "v_mfma_f32_16x16x32_bf16 v[168:171], v[16:19], v[160:163], v[168:171]\n\t"
            "v_mfma_f32_16x16x32_bf16 v[168:171], v[20:23], v[164:167], v[168:171]\n\t"
            "v_mfma_f32_16x16x32_bf16 v[176:179], v[64:67], v[160:163], v[176:179]\n\t"
            "v_mfma_f32_16x16x32_bf16 v[176:179], v[68:71], v[164:167], v[176:179]\n\t"
            "v_mfma_f32_16x16x32_bf16 v[180:183], v[88:91], v[160:163], v[180:183]\n\t"
            "v_mfma_f32_16x16x32_bf16 v[180:183], v[92:95], v[164:167], v[180:183]\n\t"
            "v_mfma_f32_16x16x32_bf16 v[172:175], v[40:43], v[160:163], v[172:175]\n\t"
            "v_mfma_f32_16x16x32_bf16 v[172:175], v[44:47], v[164:167], v[172:175]\n\t"
            "s_waitcnt lgkmcnt(5)\n\t"
            "s_nop 0\n\t"
            "v_mul_f32 v168, v236, v168\n\t"
            "v_mfma_f32_16x16x32_bf16 v[184:187], v[112:115], v[160:163], v[184:187]\n\t"
            "v_mul_f32 v169, v236, v169\n\t"
            "v_mul_f32 v170, v236, v170\n\t"
            "v_mfma_f32_16x16x32_bf16 v[184:187], v[116:119], v[164:167], v[184:187]\n\t"
            "v_mul_f32 v176, v236, v176\n\t"
            "v_mul_f32 v177, v236, v177\n\t"
            "v_mfma_f32_16x16x32_bf16 v[188:191], v[136:139], v[160:163], v[188:191]\n\t"
            "v_mul_f32 v171, v236, v171\n\t"
            "v_mul_f32 v178, v236, v178\n\t"
            "v_mfma_f32_16x16x32_bf16 v[188:191], v[140:143], v[164:167], v[188:191]\n\t"
            "v_mul_f32 v179, v236, v179\n\t"
            "v_cvt_pk_bf16_f32 v168, v168, v169\n\t"
            "s_waitcnt lgkmcnt(2)\n\t"
            "v_mfma_f32_16x16x32_bf16 v[192:195], v[16:19], v[204:207], v[192:195]\n\t"
            "v_cvt_pk_bf16_f32 v176, v176, v177\n\t"
            "v_cvt_pk_bf16_f32 v180, v180, v181\n\t"
            "v_mfma_f32_16x16x32_bf16 v[192:195], v[20:23], v[208:211], v[192:195]\n\t"
            "v_mul_f32 v172, v236, v172\n\t"
            "v_mul_f32 v173, v236, v173\n\t"
            "v_mfma_f32_16x16x32_bf16 v[196:199], v[40:43], v[204:207], v[196:199]\n\t"
            "v_mul_f32 v174, v236, v174\n\t"
            "v_mul_f32 v175, v236, v175\n\t"
            "v_mfma_f32_16x16x32_bf16 v[196:199], v[44:47], v[208:211], v[196:199]\n\t"
            "v_cvt_pk_bf16_f32 v169, v170, v171\n\t"
            "v_cvt_pk_bf16_f32 v177, v178, v179\n\t"
            "v_mfma_f32_16x16x32_bf16 v[200:203], v[64:67], v[204:207], v[200:203]\n\t"
            "v_cvt_pk_bf16_f32 v181, v182, v183\n\t"
            "v_cvt_pk_bf16_f32 v170, v172, v173\n\t"
            "v_mfma_f32_16x16x32_bf16 v[200:203], v[68:71], v[208:211], v[200:203]\n\t"
            "v_cvt_pk_bf16_f32 v171, v174, v175\n\t"
            "v_cvt_pk_bf16_f32 v182, v184, v185\n\t"
            "v_mfma_f32_16x16x32_bf16 v[216:219], v[88:91], v[204:207], v[216:219]\n\t"
            "v_cvt_pk_bf16_f32 v183, v186, v187\n\t"
            "v_cvt_pk_bf16_f32 v178, v188, v189\n\t"
            "v_mfma_f32_16x16x32_bf16 v[216:219], v[92:95], v[208:211], v[216:219]\n\t"
            "v_cvt_pk_bf16_f32 v179, v190, v191\n\t"
            "v_mul_f32 v236, 0x3e553b94, v236\n\t"
            "v_mfma_f32_16x16x32_bf16 v[220:223], v[112:115], v[204:207], v[220:223]\n\t"
            "v_add_u32 v184, s76, v239\n\t"
            "v_add_u32 v185, s74, v239\n\t"
            "v_mfma_f32_16x16x32_bf16 v[220:223], v[116:119], v[208:211], v[220:223]\n\t"
            "s_mov_b64 exec, -1\n\t"
            "ds_write_b128 v234, v[168:171]\n\t"
            "ds_write_b128 v234, v[180:183] offset:64\n\t"
            "ds_write_b64 v235, v[176:177] offset:128\n\t"
            "ds_write_b64 v235, v[178:179] offset:160\n\t"
            "s_mov_b64 exec, -1\n\t"
            "v_mfma_f32_16x16x16_bf16 v[172:175], v[176:177], v[178:179], 0\n\t"
            "v_mul_f32 v192, v237, v192\n\t"
            "v_mul_f32 v193, v237, v193\n\t"
            "v_mul_f32 v200, v237, v200\n\t"
            "v_mfma_f32_16x16x32_bf16 v[228:231], v[136:139], v[204:207], v[228:231]\n\t"
            "ds_read_b128 v[204:207], v232\n\t"
            "v_mul_f32 v201, v237, v201\n\t"
            "v_mfma_f32_16x16x32_bf16 v[228:231], v[140:143], v[208:211], v[228:231]\n\t"
            "ds_read_b128 v[208:211], v233\n\t"
            "v_mul_f32 v194, v237, v194\n\t"
            "v_mfma_f32_16x16x32_bf16 v[172:175], v[168:171], v[180:183], v[172:175]\n\t"
            "v_mul_f32 v195, v237, v195\n\t"
            "v_mul_f32 v202, v237, v202\n\t"
            "v_mul_f32 v203, v237, v203\n\t"
            "v_cvt_pk_bf16_f32 v192, v192, v193\n\t"
            "v_cvt_pk_bf16_f32 v200, v200, v201\n\t"
            "v_cvt_pk_bf16_f32 v216, v216, v217\n\t"
            "v_mul_f32 v196, v237, v196\n\t"
            "v_mul_f32 v197, v237, v197\n\t"
            "v_mul_f32 v198, v237, v198\n\t"
            "v_mul_f32 v199, v237, v199\n\t"
            "v_cvt_pk_bf16_f32 v193, v194, v195\n\t"
            "v_cvt_pk_bf16_f32 v201, v202, v203\n\t"
            "v_cvt_pk_bf16_f32 v217, v218, v219\n\t"
            "v_cvt_pk_bf16_f32 v194, v196, v197\n\t"
            "v_cvt_pk_bf16_f32 v195, v198, v199\n\t"
            "v_cvt_pk_bf16_f32 v218, v220, v221\n\t"
            "v_cvt_pk_bf16_f32 v219, v222, v223\n\t"
            "v_cvt_pk_bf16_f32 v202, v228, v229\n\t"
            "v_cvt_pk_bf16_f32 v203, v230, v231\n\t"
            "v_mul_f32 v237, 0x3e553b94, v237\n\t"
            "v_add_u32 v220, s76, v239\n\t"
            "v_mfma_f32_16x16x16_bf16 v[196:199], v[200:201], v[202:203], 0\n\t"
            "s_and_b32 exec_lo, s56, 0x11111111\n\t"
            "s_and_b32 exec_hi, s57, 0x11111111\n\t"
            "ds_write_b32 v184, v172 offset:0\n\t"
            "s_and_b32 exec_lo, s56, 0x22222222\n\t"
            "s_and_b32 exec_hi, s57, 0x22222222\n\t"
            "ds_write_b32 v184, v173 offset:0\n\t"
            "s_and_b32 exec_lo, s56, 0x44444444\n\t"
            "s_and_b32 exec_hi, s57, 0x44444444\n\t"
            "ds_write_b32 v184, v174 offset:0\n\t"
            "s_and_b32 exec_lo, s56, 0x88888888\n\t"
            "s_and_b32 exec_hi, s57, 0x88888888\n\t"
            "ds_write_b32 v184, v175 offset:0\n\t"
            "s_mov_b64 exec, s[56:57]\n\t"
            "ds_write_b32 v185, v236 offset:0\n\t"
            "s_mov_b64 exec, -1\n\t"
            "s_waitcnt lgkmcnt(5)\n\t"
            "v_mfma_f32_16x16x32_bf16 v[144:147], v[0:3], v[204:207], 0\n\t"
            "v_add_u32 v221, s74, v239\n\t"
            "v_mfma_f32_16x16x32_bf16 v[144:147], v[4:7], v[208:211], v[144:147]\n\t"
            "s_mov_b64 exec, s[54:55]\n\t"
            "ds_write_b128 v234, v[192:195] offset:12800\n\t"
            "ds_write_b128 v234, v[216:219] offset:12864\n\t"
            "ds_write_b64 v235, v[200:201] offset:12928\n\t"
            "ds_write_b64 v235, v[202:203] offset:12960\n\t"
            "s_mov_b64 exec, -1\n\t"
            "v_mfma_f32_16x16x32_bf16 v[148:151], v[28:31], v[208:211], 0\n\t"
            "v_add_u32 v235, s48, v248\n\t"
            "v_mfma_f32_16x16x32_bf16 v[148:151], v[24:27], v[204:207], v[148:151]\n\t"
            "ds_read_b128 v[228:231], v235 offset:18816\n\t"
            "v_mfma_f32_16x16x32_bf16 v[152:155], v[48:51], v[204:207], 0\n\t"
            "v_mfma_f32_16x16x32_bf16 v[152:155], v[52:55], v[208:211], v[152:155]\n\t"
            "v_mfma_f32_16x16x32_bf16 v[156:159], v[76:79], v[208:211], 0\n\t"
            "v_mfma_f32_16x16x32_bf16 v[156:159], v[72:75], v[204:207], v[156:159]\n\t"
            "v_mfma_f32_16x16x32_bf16 v[160:163], v[96:99], v[204:207], 0\n\t"
            "v_mfma_f32_16x16x32_bf16 v[160:163], v[100:103], v[208:211], v[160:163]\n\t"
            "v_mfma_f32_16x16x32_bf16 v[164:167], v[124:127], v[208:211], 0\n\t"
            "ds_read_b128 v[208:211], v233 offset:128\n\t"
            "s_waitcnt lgkmcnt(1)\n\t"
            "v_add_f32 v236, v228, v230\n\t"
            "v_mfma_f32_16x16x32_bf16 v[164:167], v[120:123], v[204:207], v[164:167]\n\t"
            "ds_read_b128 v[204:207], v232 offset:128\n\t"
            "v_mfma_f32_16x16x32_bf16 v[196:199], v[192:195], v[216:219], v[196:199]\n\t"
            "s_nop 7\n\t"
            "s_and_b32 exec_lo, s78, 0x11111111\n\t"
            "s_and_b32 exec_hi, s79, 0x11111111\n\t"
            "ds_write_b32 v220, v196 offset:12800\n\t"
            "s_and_b32 exec_lo, s78, 0x22222222\n\t"
            "s_and_b32 exec_hi, s79, 0x22222222\n\t"
            "ds_write_b32 v220, v197 offset:12800\n\t"
            "s_and_b32 exec_lo, s78, 0x44444444\n\t"
            "s_and_b32 exec_hi, s79, 0x44444444\n\t"
            "ds_write_b32 v220, v198 offset:12800\n\t"
            "s_and_b32 exec_lo, s78, 0x88888888\n\t"
            "s_and_b32 exec_hi, s79, 0x88888888\n\t"
            "ds_write_b32 v220, v199 offset:12800\n\t"
            "s_mov_b64 exec, s[78:79]\n\t"
            "ds_write_b32 v221, v237 offset:12800\n\t"
            "s_mov_b64 exec, -1\n\t"
            "s_waitcnt lgkmcnt(5)\n\t"
            "v_mfma_f32_16x16x32_bf16 v[144:147], v[8:11], v[204:207], v[144:147]\n\t"
            "v_add_f32 v237, v229, v231\n\t"
            "v_mfma_f32_16x16x32_bf16 v[144:147], v[12:15], v[208:211], v[144:147]\n\t"
            "ds_read_b128 v[228:231], v235 offset:19840\n\t"
            "v_mfma_f32_16x16x32_bf16 v[148:151], v[36:39], v[208:211], v[148:151]\n\t"
            "v_mfma_f32_16x16x32_bf16 v[148:151], v[32:35], v[204:207], v[148:151]\n\t"
            "v_mfma_f32_16x16x32_bf16 v[152:155], v[56:59], v[204:207], v[152:155]\n\t"
            "v_mfma_f32_16x16x32_bf16 v[152:155], v[60:63], v[208:211], v[152:155]\n\t"
            "v_mfma_f32_16x16x32_bf16 v[156:159], v[84:87], v[208:211], v[156:159]\n\t"
            "v_mfma_f32_16x16x32_bf16 v[156:159], v[80:83], v[204:207], v[156:159]\n\t"
            "v_mfma_f32_16x16x32_bf16 v[160:163], v[104:107], v[204:207], v[160:163]\n\t"
            "s_waitcnt lgkmcnt(0)\n\t"
            "v_add_f32 v228, v228, v230\n\t"
            "v_add_f32 v236, v236, v228\n\t"
            "v_mfma_f32_16x16x32_bf16 v[160:163], v[108:111], v[208:211], v[160:163]\n\t"
            "v_add_f32 v229, v229, v231\n\t"
            "v_mul_f32 v236, 0x3baaaaab, v236\n\t"
            "v_mfma_f32_16x16x32_bf16 v[164:167], v[132:135], v[208:211], v[164:167]\n\t"
            "ds_read_b128 v[208:211], v233 offset:6144\n\t"
            "v_add_f32 v237, v237, v229\n\t"
            "v_mfma_f32_16x16x32_bf16 v[164:167], v[128:131], v[204:207], v[164:167]\n\t"
            "ds_read_b128 v[204:207], v232 offset:6144\n\t"
            "v_mul_f32_e64 v236, v236, -v236\n\t"
            "v_mfma_f32_16x16x32_bf16 v[144:147], v[16:19], v[212:215], v[144:147]\n\t"
            "v_fmac_f32 v236, 0x3baaaaab, v237\n\t"
            "v_max_f32 v236, 0, v236\n\t"
            "v_mfma_f32_16x16x32_bf16 v[144:147], v[20:23], v[224:227], v[144:147]\n\t"
            "v_add_f32 v236, 0x3727c5ac, v236\n\t"
            "v_rsq_f32 v236, v236\n\t"
            "v_mfma_f32_16x16x32_bf16 v[148:151], v[44:47], v[224:227], v[148:151]\n\t"
            "v_mfma_f32_16x16x32_bf16 v[148:151], v[40:43], v[212:215], v[148:151]\n\t"
            "ds_write_b32 v249, v236\n\t"
            "v_mfma_f32_16x16x32_bf16 v[152:155], v[64:67], v[212:215], v[152:155]\n\t"
            "ds_read_b32 v236, v251\n\t"
            "ds_read_b32 v237, v251 offset:64\n\t"
            "v_mfma_f32_16x16x32_bf16 v[152:155], v[68:71], v[224:227], v[152:155]\n\t"
            "ds_read_b32 v238, v251 offset:128\n\t"
            "v_mfma_f32_16x16x32_bf16 v[156:159], v[92:95], v[224:227], v[156:159]\n\t"
            "v_mfma_f32_16x16x32_bf16 v[156:159], v[88:91], v[212:215], v[156:159]\n\t"
            "v_mfma_f32_16x16x32_bf16 v[160:163], v[112:115], v[212:215], v[160:163]\n\t"
            "v_mfma_f32_16x16x32_bf16 v[160:163], v[116:119], v[224:227], v[160:163]\n\t"
            "v_mfma_f32_16x16x32_bf16 v[164:167], v[140:143], v[224:227], v[164:167]\n\t"
            "ds_read_b128 v[224:227], v233 offset:6272\n\t"
            "v_mfma_f32_16x16x32_bf16 v[164:167], v[136:139], v[212:215], v[164:167]\n\t"
            "ds_read_b128 v[212:215], v232 offset:6272\n\t"
            "s_waitcnt lgkmcnt(4)\n\t"
            "v_mul_f32 v144, v236, v144\n\t"
            "v_mfma_f32_16x16x32_bf16 v[168:171], v[0:3], v[204:207], 0\n\t"
            "v_mul_f32 v145, v236, v145\n\t"
            "v_mul_f32 v152, v236, v152\n\t"
            "v_mfma_f32_16x16x32_bf16 v[168:171], v[4:7], v[208:211], v[168:171]\n\t"
            "v_mul_f32 v153, v236, v153\n\t"
            "v_mul_f32 v146, v236, v146\n\t"
            "v_mfma_f32_16x16x32_bf16 v[172:175], v[28:31], v[208:211], 0\n\t"
            "v_mul_f32 v147, v236, v147\n\t"
            "v_mul_f32 v154, v236, v154\n\t"
            "v_mfma_f32_16x16x32_bf16 v[172:175], v[24:27], v[204:207], v[172:175]\n\t"
            "v_mul_f32 v155, v236, v155\n\t"
            "v_cvt_pk_bf16_f32 v144, v144, v145\n\t"
            "v_mfma_f32_16x16x32_bf16 v[176:179], v[48:51], v[204:207], 0\n\t"
            "v_cvt_pk_bf16_f32 v152, v152, v153\n\t"
            "v_cvt_pk_bf16_f32 v156, v156, v157\n\t"
            "v_mfma_f32_16x16x32_bf16 v[176:179], v[52:55], v[208:211], v[176:179]\n\t"
            "v_mul_f32 v148, v236, v148\n\t"
            "v_mul_f32 v149, v236, v149\n\t"
            "v_mfma_f32_16x16x32_bf16 v[180:183], v[76:79], v[208:211], 0\n\t"
            "v_mul_f32 v150, v236, v150\n\t"
            "v_mul_f32 v151, v236, v151\n\t"
            "v_mfma_f32_16x16x32_bf16 v[180:183], v[72:75], v[204:207], v[180:183]\n\t"
            "v_cvt_pk_bf16_f32 v145, v146, v147\n\t"
            "v_cvt_pk_bf16_f32 v153, v154, v155\n\t"
            "v_mfma_f32_16x16x32_bf16 v[184:187], v[96:99], v[204:207], 0\n\t"
            "v_cvt_pk_bf16_f32 v157, v158, v159\n\t"
            "v_cvt_pk_bf16_f32 v146, v148, v149\n\t"
            "v_mfma_f32_16x16x32_bf16 v[184:187], v[100:103], v[208:211], v[184:187]\n\t"
            "v_cvt_pk_bf16_f32 v147, v150, v151\n\t"
            "v_cvt_pk_bf16_f32 v158, v160, v161\n\t"
            "v_mfma_f32_16x16x32_bf16 v[188:191], v[124:127], v[208:211], 0\n\t"
            "ds_read_b128 v[208:211], v233 offset:6400\n\t"
            "v_cvt_pk_bf16_f32 v159, v162, v163\n\t"
            "v_mfma_f32_16x16x32_bf16 v[188:191], v[120:123], v[204:207], v[188:191]\n\t"
            "ds_read_b128 v[204:207], v232 offset:6400\n\t"
            "v_cvt_pk_bf16_f32 v154, v164, v165\n\t"
            "s_waitcnt lgkmcnt(2)\n\t"
            "v_mfma_f32_16x16x32_bf16 v[168:171], v[8:11], v[212:215], v[168:171]\n\t"
            "v_cvt_pk_bf16_f32 v155, v166, v167\n\t"
            "v_mul_f32 v236, 0x3e553b94, v236\n\t"
            "v_mfma_f32_16x16x32_bf16 v[168:171], v[12:15], v[224:227], v[168:171]\n\t"
            "v_mfma_f32_16x16x32_bf16 v[172:175], v[36:39], v[224:227], v[172:175]\n\t"
            "v_mfma_f32_16x16x32_bf16 v[172:175], v[32:35], v[212:215], v[172:175]\n\t"
            "v_mfma_f32_16x16x32_bf16 v[176:179], v[56:59], v[212:215], v[176:179]\n\t"
            "v_mfma_f32_16x16x32_bf16 v[176:179], v[60:63], v[224:227], v[176:179]\n\t"
            "v_mfma_f32_16x16x32_bf16 v[180:183], v[84:87], v[224:227], v[180:183]\n\t"
            "v_mfma_f32_16x16x32_bf16 v[180:183], v[80:83], v[212:215], v[180:183]\n\t"
            "v_mfma_f32_16x16x32_bf16 v[184:187], v[104:107], v[212:215], v[184:187]\n\t"
            "v_mfma_f32_16x16x32_bf16 v[184:187], v[108:111], v[224:227], v[184:187]\n\t"
            "v_mfma_f32_16x16x32_bf16 v[188:191], v[132:135], v[224:227], v[188:191]\n\t"
            "ds_read_b128 v[224:227], v233 offset:12288\n\t"
            "v_mfma_f32_16x16x32_bf16 v[188:191], v[128:131], v[212:215], v[188:191]\n\t"
            "ds_read_b128 v[212:215], v232 offset:12288\n\t"
            "s_waitcnt lgkmcnt(2)\n\t"
            "v_mfma_f32_16x16x32_bf16 v[168:171], v[16:19], v[204:207], v[168:171]\n\t"
            "v_mfma_f32_16x16x32_bf16 v[168:171], v[20:23], v[208:211], v[168:171]\n\t"
            "v_mfma_f32_16x16x32_bf16 v[172:175], v[44:47], v[208:211], v[172:175]\n\t"
            "v_mfma_f32_16x16x32_bf16 v[172:175], v[40:43], v[204:207], v[172:175]\n\t"
            "v_mfma_f32_16x16x32_bf16 v[176:179], v[64:67], v[204:207], v[176:179]\n\t"
            "v_mfma_f32_16x16x32_bf16 v[176:179], v[68:71], v[208:211], v[176:179]\n\t"
            "v_mfma_f32_16x16x32_bf16 v[180:183], v[92:95], v[208:211], v[180:183]\n\t"
            "v_mfma_f32_16x16x32_bf16 v[180:183], v[88:91], v[204:207], v[180:183]\n\t"
            "v_mfma_f32_16x16x32_bf16 v[184:187], v[112:115], v[204:207], v[184:187]\n\t"
            "v_mfma_f32_16x16x32_bf16 v[184:187], v[116:119], v[208:211], v[184:187]\n\t"
            "v_mfma_f32_16x16x32_bf16 v[188:191], v[140:143], v[208:211], v[188:191]\n\t"
            "ds_read_b128 v[208:211], v233 offset:12416\n\t"
            "v_mfma_f32_16x16x32_bf16 v[188:191], v[136:139], v[204:207], v[188:191]\n\t"
            "ds_read_b128 v[204:207], v232 offset:12416\n\t"
            "s_waitcnt lgkmcnt(0)\n\t"
            "v_subrev_u32 v249, s75, v251\n\t"
            "v_add_u32 v249, 162816, v249\n\t"
            "s_mov_b32 s90, 2\n\t"
            "s_mov_b32 s81, 0\n\t"
            "s_mov_b32 s91, 0\n\t"
            "s_nop 7\n\t"
            ".p2align 6\n\t"
            "s_nop 0\n\t"
            "s_nop 0\n\t"
            "s_nop 0\n\t"
            ".Lwa_loop%=:\n\t"
            "s_waitcnt lgkmcnt(2)\n\t"
            "v_mfma_f32_16x16x32_bf16 v[192:195], v[0:3], v[212:215], 0\n\t"
            "s_bfe_u32 s49, s70, 0x30001\n\t"
            "s_lshl_b32 s49, s49, 4\n\t"
            "s_or_b32 s73, s77, s49\n\t"
            "v_mfma_f32_16x16x32_bf16 v[192:195], v[4:7], v[224:227], v[192:195]\n\t"
            "v_mad_u32_u24 v220, v246, s72, v253\n\t"
            "v_mad_u32_u24 v216, v246, s72, v252\n\t"
            "v_mfma_f32_16x16x32_bf16 v[148:151], v[28:31], v[224:227], 0\n\t"
            "ds_read_b64 v[222:223], v220 offset:128\n\t"
            "v_mov_b32_dpp v221, v220 quad_perm:[1,0,2,3] row_mask:0xf bank_mask:0xf\n\t"
            "v_mfma_f32_16x16x32_bf16 v[148:151], v[24:27], v[212:215], v[148:151]\n\t"
            "ds_read_b64 v[220:221], v221 offset:160\n\t"
            "v_mov_b32_dpp v217, v216 quad_perm:[1,0,2,3] row_mask:0xf bank_mask:0xf\n\t"
            "v_mfma_f32_16x16x32_bf16 v[196:199], v[48:51], v[212:215], 0\n\t"
            "v_mul_f32 v176, v237, v176\n\t"
            "v_mul_f32 v177, v237, v177\n\t"
            "v_mfma_f32_16x16x32_bf16 v[196:199], v[52:55], v[224:227], v[196:199]\n\t"
            "v_mul_f32 v178, v237, v178\n\t"
            "v_mul_f32 v179, v237, v179\n\t"
            "v_mfma_f32_16x16x32_bf16 v[200:203], v[76:79], v[224:227], 0\n\t"
            "v_cvt_pk_bf16_f32 v176, v176, v177\n\t"
            "v_mul_f32 v168, v237, v168\n\t"
            "v_mfma_f32_16x16x32_bf16 v[200:203], v[72:75], v[212:215], v[200:203]\n\t"
            "v_mul_f32 v169, v237, v169\n\t"
            "v_cvt_pk_bf16_f32 v177, v178, v179\n\t"
            "v_mfma_f32_16x16x32_bf16 v[160:163], v[96:99], v[212:215], 0\n\t"
            "v_mul_f32 v170, v237, v170\n\t"
            "v_mul_f32 v171, v237, v171\n\t"
            "v_mfma_f32_16x16x32_bf16 v[160:163], v[100:103], v[224:227], v[160:163]\n\t"
            "v_cvt_pk_bf16_f32 v168, v168, v169\n\t"
            "v_cvt_pk_bf16_f32 v180, v180, v181\n\t"
            "v_mfma_f32_16x16x32_bf16 v[164:167], v[124:127], v[224:227], 0\n\t"
            "ds_read_b128 v[224:227], v233 offset:12544\n\t"
            "v_cvt_pk_bf16_f32 v178, v188, v189\n\t"
            "v_mfma_f32_16x16x32_bf16 v[164:167], v[120:123], v[212:215], v[164:167]\n\t"
            "ds_read_b128 v[212:215], v232 offset:12544\n\t"
            "v_cvt_pk_bf16_f32 v179, v190, v191\n\t"
            "s_waitcnt lgkmcnt(4)\n\t"
            "v_mfma_f32_16x16x32_bf16 v[192:195], v[8:11], v[204:207], v[192:195]\n\t"
            "v_mul_f32 v172, v237, v172\n\t"
            "v_mul_f32 v173, v237, v173\n\t"
            "v_mfma_f32_16x16x32_bf16 v[192:195], v[12:15], v[208:211], v[192:195]\n\t"
            "v_mul_f32 v174, v237, v174\n\t"
            "v_mul_f32 v175, v237, v175\n\t"
            "v_mfma_f32_16x16x32_bf16 v[196:199], v[56:59], v[204:207], v[196:199]\n\t"
            "v_cvt_pk_bf16_f32 v169, v170, v171\n\t"
            "v_cvt_pk_bf16_f32 v181, v182, v183\n\t"
            "v_mfma_f32_16x16x32_bf16 v[196:199], v[60:63], v[208:211], v[196:199]\n\t"
            "v_cvt_pk_bf16_f32 v170, v172, v173\n\t"
            "v_cvt_pk_bf16_f32 v171, v174, v175\n\t"
            "v_mfma_f32_16x16x32_bf16 v[160:163], v[104:107], v[204:207], v[160:163]\n\t"
            "v_cvt_pk_bf16_f32 v182, v184, v185\n\t"
            "v_cvt_pk_bf16_f32 v183, v186, v187\n\t"
            "v_mfma_f32_16x16x32_bf16 v[160:163], v[108:111], v[208:211], v[160:163]\n\t"
            "v_mov_b32 v230, s76\n\t"
            "v_mov_b32 v239, s74\n\t"
            "v_mfma_f32_16x16x32_bf16 v[148:151], v[36:39], v[208:211], v[148:151]\n\t"
            "s_add_u32 s70, s70, 1\n\t"
            "s_add_u32 s72, s72, 800\n\t"
            "s_add_u32 s74, s74, 800\n\t"
            "s_add_u32 s76, s76, 800\n\t"
            "v_mfma_f32_16x16x32_bf16 v[148:151], v[32:35], v[204:207], v[148:151]\n\t"
            "s_add_u32 s77, s77, 384\n\t"
            "s_xor_b32 s48, s48, s47\n\t"
            "ds_read_b32 v230, v230\n\t"
            "v_mfma_f32_16x16x32_bf16 v[200:203], v[84:87], v[208:211], v[200:203]\n\t"
            "ds_read_b32 v239, v239\n\t"
            "v_mul_f32 v237, 0x3e553b94, v237\n\t"
            "v_mfma_f32_16x16x32_bf16 v[200:203], v[80:83], v[204:207], v[200:203]\n\t"
            "v_mfma_f32_16x16x32_bf16 v[164:167], v[132:135], v[208:211], v[164:167]\n\t"
            "v_mfma_f32_16x16x32_bf16 v[164:167], v[128:131], v[204:207], v[164:167]\n\t"
            "s_waitcnt lgkmcnt(2)\n\t"
            "v_mfma_f32_16x16x32_bf16 v[192:195], v[16:19], v[212:215], v[192:195]\n\t"
            "v_mfma_f32_16x16x32_bf16 v[192:195], v[20:23], v[224:227], v[192:195]\n\t"
            "v_mfma_f32_16x16x32_bf16 v[196:199], v[64:67], v[212:215], v[196:199]\n\t"
            "v_mfma_f32_16x16x32_bf16 v[196:199], v[68:71], v[224:227], v[196:199]\n\t"
            "v_mfma_f32_16x16x32_bf16 v[160:163], v[112:115], v[212:215], v[160:163]\n\t"
            "v_mfma_f32_16x16x32_bf16 v[160:163], v[116:119], v[224:227], v[160:163]\n\t"
            "v_mfma_f32_16x16x32_bf16 v[148:151], v[44:47], v[224:227], v[148:151]\n\t"
            "v_mfma_f32_16x16x32_bf16 v[148:151], v[40:43], v[212:215], v[148:151]\n\t"
            "s_nop 1\n\t"
            "v_mul_f32 v192, v238, v192\n\t"
            "v_mfma_f32_16x16x32_bf16 v[200:203], v[92:95], v[224:227], v[200:203]\n\t"
            "v_mul_f32 v193, v238, v193\n\t"
            "v_mul_f32 v194, v238, v194\n\t"
            "v_mfma_f32_16x16x32_bf16 v[200:203], v[88:91], v[212:215], v[200:203]\n\t"
            "v_mul_f32 v196, v238, v196\n\t"
            "v_mul_f32 v197, v238, v197\n\t"
            "v_mfma_f32_16x16x32_bf16 v[164:167], v[140:143], v[224:227], v[164:167]\n\t"
            "ds_read_b128 v[224:227], v217 offset:64\n\t"
            "ds_read_b128 v[216:219], v216\n\t"
            "v_mfma_f32_16x16x32_bf16 v[164:167], v[136:139], v[212:215], v[164:167]\n\t"
            "v_mul_f32 v198, v238, v198\n\t"
            "v_mul_f32 v199, v238, v199\n\t"
            "v_mfma_f32_16x16x32_bf16 v[172:175], v[220:223], v[152:155], 0\n\t"
            "s_setprio 0\n\t"
            "v_cvt_pk_bf16_f32 v196, v196, v197\n\t"
            "v_mul_f32 v195, v238, v195\n\t"
            "s_setprio 3\n\t"
            "v_mfma_f32_16x16x32_bf16 v[212:215], v[220:223], v[176:179], 0\n\t"
            "v_cvt_pk_bf16_f32 v192, v192, v193\n\t"
            "v_cvt_pk_bf16_f32 v197, v198, v199\n\t"
            "v_cvt_pk_bf16_f32 v200, v200, v201\n\t"
            "v_mul_f32 v148, v238, v148\n\t"
            "v_mfma_f32_16x16x16_bf16 v[184:187], v[152:153], v[178:179], 0\n\t"
            "v_mul_f32 v149, v238, v149\n\t"
            "v_mul_f32 v150, v238, v150\n\t"
            "v_mul_f32 v151, v238, v151\n\t"
            "v_mfma_f32_16x16x16_bf16 v[204:207], v[176:177], v[178:179], 0\n\t"
            "v_cvt_pk_bf16_f32 v193, v194, v195\n\t"
            "v_cvt_pk_bf16_f32 v201, v202, v203\n\t"
            "v_cvt_pk_bf16_f32 v194, v148, v149\n\t"
            "v_mfma_f32_16x16x16_bf16 v[188:191], v[196:197], v[178:179], 0\n\t"
            "v_cvt_pk_bf16_f32 v195, v150, v151\n\t"
            "v_cvt_pk_bf16_f32 v202, v160, v161\n\t"
            "v_cvt_pk_bf16_f32 v203, v162, v163\n\t"
            "s_waitcnt lgkmcnt(0)\n\t"
            "v_mfma_f32_16x16x32_bf16 v[172:175], v[216:219], v[156:159], v[172:175]\n\t"
            "v_cvt_pk_bf16_f32 v198, v164, v165\n\t"
            "v_cvt_pk_bf16_f32 v199, v166, v167\n\t"
            "v_mfma_f32_16x16x32_bf16 v[172:175], v[224:227], v[144:147], v[172:175]\n\t"
            "v_mul_f32 v238, 0x3e553b94, v238\n\t"
            "v_mfma_f32_16x16x16_bf16 v[148:151], v[152:153], v[154:155], 0\n\t"
            "v_mfma_f32_16x16x16_bf16 v[160:163], v[176:177], v[154:155], 0\n\t"
            "v_mfma_f32_16x16x16_bf16 v[164:167], v[196:197], v[154:155], 0\n\t"
            "v_mfma_f32_16x16x32_bf16 v[212:215], v[216:219], v[180:183], v[212:215]\n\t"
            "v_mfma_f32_16x16x32_bf16 v[212:215], v[224:227], v[168:171], v[212:215]\n\t"
            "s_nop 1\n\t"
            "v_mfma_f32_16x16x32_bf16 v[148:151], v[144:147], v[156:159], v[148:151]\n\t"
            "v_mfma_f32_16x16x32_bf16 v[184:187], v[144:147], v[180:183], v[184:187]\n\t"
            "v_mfma_f32_16x16x32_bf16 v[160:163], v[168:171], v[156:159], v[160:163]\n\t"
            "v_mfma_f32_16x16x32_bf16 v[204:207], v[168:171], v[180:183], v[204:207]\n\t"
            "v_mfma_f32_16x16x32_bf16 v[164:167], v[192:195], v[156:159], v[164:167]\n\t"
            "v_mfma_f32_16x16x32_bf16 v[188:191], v[192:195], v[180:183], v[188:191]\n\t"
            "v_cndmask_b32_e64 v235, v173, v213, s[60:61]\n\t"
            "v_mfma_f32_16x16x32_bf16 v[208:211], v[220:223], v[196:199], 0\n\t"
            "s_nop 0\n\t"
            "v_max3_f32 v228, v172, v148, v149\n\t"
            "v_max3_f32 v228, v228, v150, v151\n\t"
            "v_mfma_f32_16x16x32_bf16 v[208:211], v[216:219], v[200:203], v[208:211]\n\t"
            "v_max3_f32 v229, v212, v184, v185\n\t"
            "v_max3_f32 v229, v229, v186, v187\n\t"
            "v_mfma_f32_16x16x32_bf16 v[208:211], v[224:227], v[192:195], v[208:211]\n\t"
            "s_setprio 0\n\t"
            "v_max3_f32 v228, v228, v160, v161\n\t"
            "v_max3_f32 v229, v229, v204, v205\n\t"
            "v_max3_f32 v228, v228, v162, v163\n\t"
            "v_max3_f32 v229, v229, v206, v207\n\t"
            "v_max3_f32 v228, v228, v164, v165\n\t"
            "v_max3_f32 v229, v229, v188, v189\n\t"
            "v_max3_f32 v228, v228, v166, v167\n\t"
            "v_max3_f32 v229, v229, v190, v191\n\t"
            "v_max3_f32 v234, v173, v213, v209\n\t"
            "v_max_f32 v234, v234, v230\n\t"
            "v_permlane32_swap_b32 v228, v229\n\t"
            "v_max_f32 v228, v228, v229\n\t"
            "v_mov_b32 v231, v228\n\t"
            "v_max_f32_dpp v234, v234, v234 row_ror:8 row_mask:0xf bank_mask:0xf\n\t"
            "v_cndmask_b32_e64 v235, v235, v209, s[62:63]\n\t"
            "v_permlane16_swap_b32 v228, v231\n\t"
            "v_max_f32 v228, v228, v231\n\t"
            "v_mov_b32 v229, v228\n\t"
            "v_max_f32_dpp v234, v234, v234 row_ror:4 row_mask:0xf bank_mask:0xf\n\t"
            "v_cndmask_b32_e64 v235, v235, v230, s[64:65]\n\t"
            "v_permlane32_swap_b32 v228, v229\n\t"
            "v_mul_f32_e64 v228, -v228, v236\n\t"
            "v_mul_f32_e64 v229, -v229, v237\n\t"
            "v_fma_f32 v164, v164, v236, v228\n\t"
            "v_fma_f32 v165, v165, v236, v228\n\t"
            "v_fma_f32 v184, v184, v237, v229\n\t"
            "v_fma_f32 v185, v185, v237, v229\n\t"
            "v_exp_f32 v164, v164\n\t"
            "v_exp_f32 v165, v165\n\t"
            "v_fma_f32 v166, v166, v236, v228\n\t"
            "v_fma_f32 v167, v167, v236, v228\n\t"
            "v_fma_f32 v172, v172, v236, v228\n\t"
            "v_exp_f32 v184, v184\n\t"
            "v_exp_f32 v185, v185\n\t"
            "v_fma_f32 v186, v186, v237, v229\n\t"
            "v_fma_f32 v187, v187, v237, v229\n\t"
            "v_exp_f32 v166, v166\n\t"
            "v_exp_f32 v167, v167\n\t"
            "v_exp_f32 v172, v172\n\t"
            "v_exp_f32 v186, v186\n\t"
            "v_exp_f32 v187, v187\n\t"
            "v_fma_f32 v204, v204, v237, v229\n\t"
            "v_fma_f32 v205, v205, v237, v229\n\t"
            "v_fma_f32 v206, v206, v237, v229\n\t"
            "v_fma_f32 v207, v207, v237, v229\n\t"
            "v_exp_f32 v204, v204\n\t"
            "v_exp_f32 v205, v205\n\t"
            "v_exp_f32 v206, v206\n\t"
            "v_exp_f32 v207, v207\n\t"
            "v_cvt_pk_bf16_f32 v164, v164, v165\n\t"
            "v_cvt_pk_bf16_f32 v184, v184, v185\n\t"
            "v_cvt_pk_bf16_f32 v165, v166, v167\n\t"
            "v_cvt_pk_bf16_f32 v172, v172, 0\n\t"
            "v_cvt_pk_bf16_f32 v185, v186, v187\n\t"
            "v_max_f32_dpp v234, v234, v234 row_ror:2 row_mask:0xf bank_mask:0xf\n\t"
            "v_cndmask_b32_e64 v166, 0, v172, s[58:59]\n\t"
            "v_cvt_pk_bf16_f32 v186, v204, v205\n\t"
            "v_cvt_pk_bf16_f32 v187, v206, v207\n\t"
            "s_setprio 3\n\t"
            "v_mfma_f32_16x16x16_bf16 v[180:183], v[152:153], v[198:199], 0\n\t"
            "v_max_f32_dpp v234, v234, v234 row_ror:1 row_mask:0xf bank_mask:0xf\n\t"
            "v_sub_f32 v235, v235, v234\n\t"
            "v_mul_f32 v235, v239, v235\n\t"
            "v_mfma_f32_16x16x16_bf16 v[204:207], v[176:177], v[198:199], 0\n\t"
            "v_exp_f32 v235, v235\n\t"
            "v_fma_f32 v148, v148, v236, v228\n\t"
            "v_mfma_f32_16x16x16_bf16 v[172:175], v[196:197], v[198:199], 0\n\t"
            "v_cvt_pk_bf16_f32 v235, v235, 0\n\t"
            "v_cndmask_b32_e64 v235, 0, v235, s[66:67]\n\t"
            "v_fma_f32 v149, v149, v236, v228\n\t"
            "v_mfma_f32_16x16x32_bf16 v[204:207], v[168:171], v[200:203], v[204:207]\n\t"
            "ds_write_b16 v250, v235 offset:256\n\t"
            "v_mfma_f32_16x16x32_bf16 v[180:183], v[144:147], v[200:203], v[180:183]\n\t"
            "v_exp_f32 v148, v148\n\t"
            "v_mfma_f32_16x16x32_bf16 v[172:175], v[192:195], v[200:203], v[172:175]\n\t"
            "s_setprio 0\n\t"
            "v_exp_f32 v149, v149\n\t"
            "v_fma_f32 v150, v150, v236, v228\n\t"
            "v_fma_f32 v151, v151, v236, v228\n\t"
            "v_exp_f32 v150, v150\n\t"
            "v_exp_f32 v151, v151\n\t"
            "v_fma_f32 v160, v160, v236, v228\n\t"
            "v_max3_f32 v230, v208, v180, v181\n\t"
            "v_max3_f32 v230, v230, v182, v183\n\t"
            "v_max3_f32 v230, v230, v204, v205\n\t"
            "v_max3_f32 v230, v230, v206, v207\n\t"
            "v_max3_f32 v230, v230, v172, v173\n\t"
            "v_max3_f32 v230, v230, v174, v175\n\t"
            "v_mov_b32 v231, v230\n\t"
            "v_fma_f32 v161, v161, v236, v228\n\t"
            "v_fma_f32 v162, v162, v236, v228\n\t"
            "v_fma_f32 v163, v163, v236, v228\n\t"
            "v_exp_f32 v160, v160\n\t"
            "v_exp_f32 v161, v161\n\t"
            "v_exp_f32 v162, v162\n\t"
            "v_exp_f32 v163, v163\n\t"
            "v_permlane32_swap_b32 v230, v231\n\t"
            "v_cvt_pk_bf16_f32 v148, v148, v149\n\t"
            "v_cvt_pk_bf16_f32 v149, v150, v151\n\t"
            "v_fma_f32 v188, v188, v237, v229\n\t"
            "v_fma_f32 v189, v189, v237, v229\n\t"
            "v_max_f32 v230, v230, v231\n\t"
            "v_cvt_pk_bf16_f32 v150, v160, v161\n\t"
            "v_cvt_pk_bf16_f32 v151, v162, v163\n\t"
            "v_mov_b32 v167, 0\n\t"
            "v_exp_f32 v188, v188\n\t"
            "v_exp_f32 v189, v189\n\t"
            "v_fma_f32 v190, v190, v237, v229\n\t"
            "v_fma_f32 v191, v191, v237, v229\n\t"
            "v_fma_f32 v212, v212, v237, v229\n\t"
            "v_mov_b32 v231, v230\n\t"
            "v_exp_f32 v190, v190\n\t"
            "v_exp_f32 v191, v191\n\t"
            "v_exp_f32 v212, v212\n\t"
            "v_cvt_pk_bf16_f32 v188, v188, v189\n\t"
            "v_permlane16_swap_b32 v230, v231\n\t"
            "v_cvt_pk_bf16_f32 v189, v190, v191\n\t"
            "v_cvt_pk_bf16_f32 v212, v212, 0\n\t"
            "v_cndmask_b32_e64 v190, 0, v212, s[58:59]\n\t"
            "v_max_f32 v230, v230, v231\n\t"
            "v_mul_f32_e64 v230, -v230, v238\n\t"
            "v_fma_f32 v180, v180, v238, v230\n\t"
            "v_fma_f32 v181, v181, v238, v230\n\t"
            "v_fma_f32 v172, v172, v238, v230\n\t"
            "v_fma_f32 v173, v173, v238, v230\n\t"
            "v_exp_f32 v180, v180\n\t"
            "v_exp_f32 v181, v181\n\t"
            "v_fma_f32 v182, v182, v238, v230\n\t"
            "v_fma_f32 v183, v183, v238, v230\n\t"
            "v_exp_f32 v172, v172\n\t"
            "v_exp_f32 v173, v173\n\t"
            "v_fma_f32 v174, v174, v238, v230\n\t"
            "v_fma_f32 v175, v175, v238, v230\n\t"
            "v_fma_f32 v208, v208, v238, v230\n\t"
            "v_exp_f32 v182, v182\n\t"
            "v_exp_f32 v183, v183\n\t"
            "v_fma_f32 v204, v204, v238, v230\n\t"
            "v_fma_f32 v205, v205, v238, v230\n\t"
            "v_fma_f32 v206, v206, v238, v230\n\t"
            "v_fma_f32 v207, v207, v238, v230\n\t"
            "v_exp_f32 v174, v174\n\t"
            "v_exp_f32 v175, v175\n\t"
            "v_exp_f32 v208, v208\n\t"
            "v_exp_f32 v204, v204\n\t"
            "v_exp_f32 v205, v205\n\t"
            "v_exp_f32 v206, v206\n\t"
            "v_exp_f32 v207, v207\n\t"
            "v_cvt_pk_bf16_f32 v180, v180, v181\n\t"
            "v_cvt_pk_bf16_f32 v172, v172, v173\n\t"
            "v_cvt_pk_bf16_f32 v181, v182, v183\n\t"
            "v_cvt_pk_bf16_f32 v173, v174, v175\n\t"
            "v_cvt_pk_bf16_f32 v208, v208, 0\n\t"
            "v_cvt_pk_bf16_f32 v182, v204, v205\n\t"
            "v_cvt_pk_bf16_f32 v183, v206, v207\n\t"
            "v_cndmask_b32_e64 v174, 0, v208, s[58:59]\n\t"
            "v_mov_b32 v191, 0\n\t"
            "v_mov_b32 v175, 0\n\t"
            "s_cmp_ge_u32 s70, s80\n\t"
            "s_barrier\n\t"
            "s_setprio 2\n\t"
            "s_cbranch_scc1 .Lwa_h1%=\n\t"
            ".Lwa_r1%=:\n\t"
            "v_mfma_f32_16x16x32_bf16 v[200:203], v[240:243], v[148:151], 0\n\t"
            "ds_read_b64 v[176:177], v247\n\t"
            "ds_read_b64 v[196:197], v247 offset:1536\n\t"
            "v_mfma_f32_16x16x32_bf16 v[200:203], v[240:243], v[164:167], v[200:203]\n\t"
            "ds_read_b64 v[220:221], v247 offset:3072\n\t"
            "v_xor_b32 v234, s73, v254\n\t"
            "v_add_u32 v235, s45, v248\n\t"
            "v_mov_b32 v179, 0\n\t"
            "v_mov_b32 v199, 0\n\t"
            "v_mov_b32 v223, 0\n\t"
            "v_add_u32 v232, s48, v244\n\t"
            "v_xor_b32 v233, 64, v232\n\t"
            "v_rcp_f32 v201, v200\n\t"
            "v_xor_b32 v249, 0x100, v249\n\t"
            "ds_read_b128 v[168:171], v235\n\t"
            "ds_read_u16 v178, v234\n\t"
            "ds_read_b128 v[192:195], v235 offset:1536\n\t"
            "ds_read_u16 v198, v234 offset:128\n\t"
            "ds_read_b128 v[216:219], v235 offset:3072\n\t"
            "ds_read_u16 v222, v234 offset:256\n\t"
            "ds_read_b128 v[204:207], v232\n\t"
            "ds_read_b128 v[208:211], v233\n\t"
            "ds_read_b32 v236, v249\n\t"
            "ds_read_b32 v237, v249 offset:64\n\t"
            "ds_read_b32 v238, v249 offset:128\n\t"
            "v_mad_u32_u24 v234, v246, s75, v253\n\t"
            "s_waitcnt lgkmcnt(9)\n\t"
            "v_mfma_f32_16x16x32_bf16 v[224:227], v[168:171], v[148:151], 0\n\t"
            "v_mfma_f32_16x16x32_bf16 v[224:227], v[176:179], v[164:167], v[224:227]\n\t"
            "s_waitcnt lgkmcnt(7)\n\t"
            "v_mfma_f32_16x16x32_bf16 v[212:215], v[192:195], v[148:151], 0\n\t"
            "v_mfma_f32_16x16x32_bf16 v[212:215], v[196:199], v[164:167], v[212:215]\n\t"
            "s_waitcnt lgkmcnt(5)\n\t"
            "v_mfma_f32_16x16x32_bf16 v[148:151], v[216:219], v[148:151], 0\n\t"
            "v_mfma_f32_16x16x32_bf16 v[148:151], v[220:223], v[164:167], v[148:151]\n\t"
            "s_waitcnt lgkmcnt(3)\n\t"
            "v_mfma_f32_16x16x32_bf16 v[144:147], v[0:3], v[204:207], 0\n\t"
            "v_mfma_f32_16x16x32_bf16 v[144:147], v[4:7], v[208:211], v[144:147]\n\t"
            "v_mul_f32 v224, v201, v224\n\t"
            "v_mfma_f32_16x16x32_bf16 v[152:155], v[48:51], v[204:207], 0\n\t"
            "v_mul_f32 v225, v201, v225\n\t"
            "v_mul_f32 v226, v201, v226\n\t"
            "v_mfma_f32_16x16x32_bf16 v[152:155], v[52:55], v[208:211], v[152:155]\n\t"
            "v_mul_f32 v212, v201, v212\n\t"
            "v_mul_f32 v213, v201, v213\n\t"
            "v_mfma_f32_16x16x32_bf16 v[156:159], v[76:79], v[208:211], 0\n\t"
            "v_mul_f32 v227, v201, v227\n\t"
            "v_cvt_pk_bf16_f32 v224, v224, v225\n\t"
            "v_mfma_f32_16x16x32_bf16 v[156:159], v[72:75], v[204:207], v[156:159]\n\t"
            "v_mul_f32 v214, v201, v214\n\t"
            "v_mul_f32 v215, v201, v215\n\t"
            "v_mfma_f32_16x16x32_bf16 v[160:163], v[96:99], v[204:207], 0\n\t"
            "v_cvt_pk_bf16_f32 v212, v212, v213\n\t"
            "v_cvt_pk_bf16_f32 v225, v226, v227\n\t"
            "v_mfma_f32_16x16x32_bf16 v[160:163], v[100:103], v[208:211], v[160:163]\n\t"
            "v_cvt_pk_bf16_f32 v213, v214, v215\n\t"
            "v_mul_f32 v148, v201, v148\n\t"
            "v_mfma_f32_16x16x32_bf16 v[164:167], v[124:127], v[208:211], 0\n\t"
            "v_mul_f32 v149, v201, v149\n\t"
            "v_mul_f32 v150, v201, v150\n\t"
            "v_mfma_f32_16x16x32_bf16 v[164:167], v[120:123], v[204:207], v[164:167]\n\t"
            "ds_write_b64 v245, v[224:225]\n\t"
            "v_mfma_f32_16x16x32_bf16 v[224:227], v[168:171], v[184:187], 0\n\t"
            "v_mul_f32 v151, v201, v151\n\t"
            "v_cvt_pk_bf16_f32 v148, v148, v149\n\t"
            "v_mfma_f32_16x16x32_bf16 v[224:227], v[176:179], v[188:191], v[224:227]\n\t"
            "ds_write_b64 v245, v[212:213] offset:128\n\t"
            "v_mfma_f32_16x16x32_bf16 v[200:203], v[240:243], v[184:187], 0\n\t"
            "v_cvt_pk_bf16_f32 v149, v150, v151\n\t"
            "v_mfma_f32_16x16x32_bf16 v[200:203], v[240:243], v[188:191], v[200:203]\n\t"
            "ds_write_b64 v245, v[148:149] offset:256\n\t"
            "v_mfma_f32_16x16x32_bf16 v[212:215], v[192:195], v[184:187], 0\n\t"
            "v_mfma_f32_16x16x32_bf16 v[212:215], v[196:199], v[188:191], v[212:215]\n\t"
            "v_mfma_f32_16x16x32_bf16 v[184:187], v[216:219], v[184:187], 0\n\t"
            "v_mfma_f32_16x16x32_bf16 v[184:187], v[220:223], v[188:191], v[184:187]\n\t"
            "v_mfma_f32_16x16x32_bf16 v[148:151], v[28:31], v[208:211], 0\n\t"
            "ds_read_b128 v[208:211], v233 offset:128\n\t"
            "v_mfma_f32_16x16x32_bf16 v[148:151], v[24:27], v[204:207], v[148:151]\n\t"
            "ds_read_b128 v[204:207], v232 offset:128\n\t"
            "v_rcp_f32 v201, v200\n\t"
            "v_mfma_f32_16x16x32_bf16 v[188:191], v[192:195], v[180:183], 0\n\t"
            "v_mul_f32 v224, v201, v224\n\t"
            "v_mul_f32 v225, v201, v225\n\t"
            "v_mfma_f32_16x16x32_bf16 v[188:191], v[196:199], v[172:175], v[188:191]\n\t"
            "v_mul_f32 v226, v201, v226\n\t"
            "v_mul_f32 v227, v201, v227\n\t"
            "v_mul_f32 v212, v201, v212\n\t"
            "v_mul_f32 v213, v201, v213\n\t"
            "v_mul_f32 v214, v201, v214\n\t"
            "v_mul_f32 v215, v201, v215\n\t"
            "v_mul_f32 v184, v201, v184\n\t"
            "v_mul_f32 v185, v201, v185\n\t"
            "v_mul_f32 v186, v201, v186\n\t"
            "v_mul_f32 v187, v201, v187\n\t"
            "v_mfma_f32_16x16x32_bf16 v[200:203], v[240:243], v[180:183], 0\n\t"
            "v_cvt_pk_bf16_f32 v224, v224, v225\n\t"
            "v_cvt_pk_bf16_f32 v225, v226, v227\n\t"
            "v_mfma_f32_16x16x32_bf16 v[200:203], v[240:243], v[172:175], v[200:203]\n\t"
            "ds_write_b64 v245, v[224:225] offset:6144\n\t"
            "v_mfma_f32_16x16x32_bf16 v[224:227], v[168:171], v[180:183], 0\n\t"
            "v_cvt_pk_bf16_f32 v184, v184, v185\n\t"
            "v_cvt_pk_bf16_f32 v185, v186, v187\n\t"
            "v_mfma_f32_16x16x32_bf16 v[224:227], v[176:179], v[172:175], v[224:227]\n\t"
            "ds_read_b64 v[186:187], v234 offset:288\n\t"
            "ds_write_b64 v245, v[184:185] offset:6400\n\t"
            "s_waitcnt lgkmcnt(3)\n\t"
            "v_mfma_f32_16x16x32_bf16 v[144:147], v[8:11], v[204:207], v[144:147]\n\t"
            "ds_read_b64 v[184:185], v234 offset:256\n\t"
            "v_rcp_f32 v201, v200\n\t"
            "v_mfma_f32_16x16x32_bf16 v[144:147], v[12:15], v[208:211], v[144:147]\n\t"
            "v_mul_f32 v188, v201, v188\n\t"
            "v_mul_f32 v189, v201, v189\n\t"
            "v_mfma_f32_16x16x32_bf16 v[148:151], v[36:39], v[208:211], v[148:151]\n\t"
            "v_mul_f32 v190, v201, v190\n\t"
            "v_mul_f32 v191, v201, v191\n\t"
            "v_mfma_f32_16x16x32_bf16 v[148:151], v[32:35], v[204:207], v[148:151]\n\t"
            "v_cvt_pk_bf16_f32 v188, v188, v189\n\t"
            "v_cvt_pk_bf16_f32 v189, v190, v191\n\t"
            "v_mfma_f32_16x16x32_bf16 v[152:155], v[56:59], v[204:207], v[152:155]\n\t"
            "ds_read_b64 v[190:191], v234 offset:352\n\t"
            "v_mul_f32 v224, v201, v224\n\t"
            "v_mfma_f32_16x16x32_bf16 v[152:155], v[60:63], v[208:211], v[152:155]\n\t"
            "ds_write_b64 v245, v[188:189] offset:12416\n\t"
            "v_mfma_f32_16x16x32_bf16 v[156:159], v[84:87], v[208:211], v[156:159]\n\t"
            "ds_read_b64 v[188:189], v234 offset:320\n\t"
            "v_mul_f32 v225, v201, v225\n\t"
            "v_mfma_f32_16x16x32_bf16 v[156:159], v[80:83], v[204:207], v[156:159]\n\t"
            "v_cvt_pk_bf16_f32 v212, v212, v213\n\t"
            "v_mul_f32 v226, v201, v226\n\t"
            "v_mfma_f32_16x16x32_bf16 v[160:163], v[104:107], v[204:207], v[160:163]\n\t"
            "v_mul_f32 v227, v201, v227\n\t"
            "v_cvt_pk_bf16_f32 v224, v224, v225\n\t"
            "v_mfma_f32_16x16x32_bf16 v[160:163], v[108:111], v[208:211], v[160:163]\n\t"
            "v_cvt_pk_bf16_f32 v213, v214, v215\n\t"
            "v_cvt_pk_bf16_f32 v225, v226, v227\n\t"
            "v_mfma_f32_16x16x32_bf16 v[164:167], v[132:135], v[208:211], v[164:167]\n\t"
            "ds_read_b128 v[208:211], v233 offset:6144\n\t"
            "v_mfma_f32_16x16x32_bf16 v[164:167], v[128:131], v[204:207], v[164:167]\n\t"
            "ds_read_b128 v[204:207], v232 offset:6144\n\t"
            "ds_write_b64 v245, v[212:213] offset:6272\n\t"
            "v_mfma_f32_16x16x32_bf16 v[180:183], v[216:219], v[180:183], 0\n\t"
            "ds_read_b128 v[212:215], v232 offset:256\n\t"
            "v_mfma_f32_16x16x32_bf16 v[180:183], v[220:223], v[172:175], v[180:183]\n\t"
            "ds_write_b64 v245, v[224:225] offset:12288\n\t"
            "s_waitcnt lgkmcnt(5)\n\t"
            "v_mfma_f32_16x16x32_bf16 v[168:171], v[184:187], v[168:171], 0\n\t"
            "ds_read_b128 v[224:227], v233 offset:256\n\t"
            "v_mfma_f32_16x16x32_bf16 v[168:171], v[188:191], v[176:179], v[168:171]\n\t"
            "v_mfma_f32_16x16x32_bf16 v[192:195], v[184:187], v[192:195], 0\n\t"
            "v_mfma_f32_16x16x32_bf16 v[192:195], v[188:191], v[196:199], v[192:195]\n\t"
            "s_nop 0\n\t"
            "v_mul_f32 v180, v201, v180\n\t"
            "v_mfma_f32_16x16x32_bf16 v[216:219], v[184:187], v[216:219], 0\n\t"
            "v_mul_f32 v181, v201, v181\n\t"
            "v_mul_f32 v182, v201, v182\n\t"
            "v_mfma_f32_16x16x32_bf16 v[216:219], v[188:191], v[220:223], v[216:219]\n\t"
            "v_mul_f32 v183, v201, v183\n\t"
            "v_cvt_pk_bf16_f32 v180, v180, v181\n\t"
            "v_mfma_f32_16x16x32_bf16 v[200:203], v[240:243], v[184:187], 0\n\t"
            "v_cvt_pk_bf16_f32 v181, v182, v183\n\t"
            "v_mfma_f32_16x16x32_bf16 v[200:203], v[240:243], v[188:191], v[200:203]\n\t"
            "ds_write_b64 v245, v[180:181] offset:12544\n\t"
            "s_waitcnt lgkmcnt(1)\n\t"
            "v_mfma_f32_16x16x32_bf16 v[144:147], v[16:19], v[212:215], v[144:147]\n\t"
            "v_mfma_f32_16x16x32_bf16 v[144:147], v[20:23], v[224:227], v[144:147]\n\t"
            "v_mfma_f32_16x16x32_bf16 v[148:151], v[44:47], v[224:227], v[148:151]\n\t"
            "v_mfma_f32_16x16x32_bf16 v[148:151], v[40:43], v[212:215], v[148:151]\n\t"
            "v_mfma_f32_16x16x32_bf16 v[152:155], v[64:67], v[212:215], v[152:155]\n\t"
            "s_nop 0\n\t"
            "v_cndmask_b32_e64 v203, v168, v192, s[60:61]\n\t"
            "v_mfma_f32_16x16x32_bf16 v[152:155], v[68:71], v[224:227], v[152:155]\n\t"
            "v_rcp_f32 v201, v200\n\t"
            "v_mfma_f32_16x16x32_bf16 v[156:159], v[92:95], v[224:227], v[156:159]\n\t"
            "v_cndmask_b32_e64 v203, v203, v216, s[62:63]\n\t"
            "v_xad_u32 v202, s73, v254, v255\n\t"
            "v_mfma_f32_16x16x32_bf16 v[156:159], v[88:91], v[212:215], v[156:159]\n\t"
            "v_mul_f32_dpp v203, v201, v203 quad_perm:[0,0,0,0] row_mask:0xf bank_mask:0xf\n\t"
            "v_cvt_pk_bf16_f32 v203, v203, 0\n\t"
            "v_mfma_f32_16x16x32_bf16 v[160:163], v[112:115], v[212:215], v[160:163]\n\t"
            "v_mul_f32 v144, v236, v144\n\t"
            "v_mul_f32 v145, v236, v145\n\t"
            "v_mfma_f32_16x16x32_bf16 v[160:163], v[116:119], v[224:227], v[160:163]\n\t"
            "s_mov_b64 exec, s[68:69]\n\t"
            "ds_write_b16 v202, v203\n\t"
            "s_mov_b64 exec, 1\n\t"
            "ds_add_u32 v253, v246 offset:7856\n\t"
            "s_mov_b64 exec, -1\n\t"
            "v_mfma_f32_16x16x32_bf16 v[164:167], v[140:143], v[224:227], v[164:167]\n\t"
            "ds_read_b128 v[224:227], v233 offset:6272\n\t"
            "v_mul_f32 v152, v236, v152\n\t"
            "v_mfma_f32_16x16x32_bf16 v[164:167], v[136:139], v[212:215], v[164:167]\n\t"
            "ds_read_b128 v[212:215], v232 offset:6272\n\t"
            "v_mul_f32 v153, v236, v153\n\t"
            "v_mfma_f32_16x16x32_bf16 v[168:171], v[0:3], v[204:207], 0\n\t"
            "v_mul_f32 v146, v236, v146\n\t"
            "v_mul_f32 v147, v236, v147\n\t"
            "v_mfma_f32_16x16x32_bf16 v[168:171], v[4:7], v[208:211], v[168:171]\n\t"
            "v_mul_f32 v154, v236, v154\n\t"
            "v_mul_f32 v155, v236, v155\n\t"
            "v_mfma_f32_16x16x32_bf16 v[172:175], v[28:31], v[208:211], 0\n\t"
            "v_cvt_pk_bf16_f32 v144, v144, v145\n\t"
            "v_cvt_pk_bf16_f32 v152, v152, v153\n\t"
            "v_mfma_f32_16x16x32_bf16 v[172:175], v[24:27], v[204:207], v[172:175]\n\t"
            "v_cvt_pk_bf16_f32 v156, v156, v157\n\t"
            "v_mul_f32 v148, v236, v148\n\t"
            "v_mfma_f32_16x16x32_bf16 v[176:179], v[48:51], v[204:207], 0\n\t"
            "v_mul_f32 v149, v236, v149\n\t"
            "v_mul_f32 v150, v236, v150\n\t"
            "v_mfma_f32_16x16x32_bf16 v[176:179], v[52:55], v[208:211], v[176:179]\n\t"
            "v_mul_f32 v151, v236, v151\n\t"
            "v_cvt_pk_bf16_f32 v145, v146, v147\n\t"
            "v_mfma_f32_16x16x32_bf16 v[180:183], v[76:79], v[208:211], 0\n\t"
            "v_cvt_pk_bf16_f32 v153, v154, v155\n\t"
            "v_cvt_pk_bf16_f32 v157, v158, v159\n\t"
            "v_mfma_f32_16x16x32_bf16 v[180:183], v[72:75], v[204:207], v[180:183]\n\t"
            "v_cvt_pk_bf16_f32 v146, v148, v149\n\t"
            "v_cvt_pk_bf16_f32 v147, v150, v151\n\t"
            "v_mfma_f32_16x16x32_bf16 v[184:187], v[96:99], v[204:207], 0\n\t"
            "v_cvt_pk_bf16_f32 v158, v160, v161\n\t"
            "v_cvt_pk_bf16_f32 v159, v162, v163\n\t"
            "v_mfma_f32_16x16x32_bf16 v[184:187], v[100:103], v[208:211], v[184:187]\n\t"
            "v_cvt_pk_bf16_f32 v154, v164, v165\n\t"
            "v_cvt_pk_bf16_f32 v155, v166, v167\n\t"
            "v_mfma_f32_16x16x32_bf16 v[188:191], v[124:127], v[208:211], 0\n\t"
            "ds_read_b128 v[208:211], v233 offset:6400\n\t"
            "v_mul_f32 v236, 0x3e553b94, v236\n\t"
            "v_mfma_f32_16x16x32_bf16 v[188:191], v[120:123], v[204:207], v[188:191]\n\t"
            "ds_read_b128 v[204:207], v232 offset:6400\n\t"
            "s_waitcnt lgkmcnt(2)\n\t"
            "v_mfma_f32_16x16x32_bf16 v[168:171], v[8:11], v[212:215], v[168:171]\n\t"
            "v_mfma_f32_16x16x32_bf16 v[168:171], v[12:15], v[224:227], v[168:171]\n\t"
            "v_mfma_f32_16x16x32_bf16 v[172:175], v[36:39], v[224:227], v[172:175]\n\t"
            "v_mfma_f32_16x16x32_bf16 v[172:175], v[32:35], v[212:215], v[172:175]\n\t"
            "v_mfma_f32_16x16x32_bf16 v[176:179], v[56:59], v[212:215], v[176:179]\n\t"
            "v_mfma_f32_16x16x32_bf16 v[176:179], v[60:63], v[224:227], v[176:179]\n\t"
            "v_mfma_f32_16x16x32_bf16 v[180:183], v[84:87], v[224:227], v[180:183]\n\t"
            "v_mfma_f32_16x16x32_bf16 v[180:183], v[80:83], v[212:215], v[180:183]\n\t"
            "v_mfma_f32_16x16x32_bf16 v[184:187], v[104:107], v[212:215], v[184:187]\n\t"
            "v_mfma_f32_16x16x32_bf16 v[184:187], v[108:111], v[224:227], v[184:187]\n\t"
            "v_mfma_f32_16x16x32_bf16 v[188:191], v[132:135], v[224:227], v[188:191]\n\t"
            "ds_read_b128 v[224:227], v233 offset:12288\n\t"
            "v_mfma_f32_16x16x32_bf16 v[188:191], v[128:131], v[212:215], v[188:191]\n\t"
            "ds_read_b128 v[212:215], v232 offset:12288\n\t"
            "s_waitcnt lgkmcnt(2)\n\t"
            "v_mfma_f32_16x16x32_bf16 v[168:171], v[16:19], v[204:207], v[168:171]\n\t"
            "v_mfma_f32_16x16x32_bf16 v[168:171], v[20:23], v[208:211], v[168:171]\n\t"
            "v_mfma_f32_16x16x32_bf16 v[172:175], v[44:47], v[208:211], v[172:175]\n\t"
            "v_mfma_f32_16x16x32_bf16 v[172:175], v[40:43], v[204:207], v[172:175]\n\t"
            "v_mfma_f32_16x16x32_bf16 v[176:179], v[64:67], v[204:207], v[176:179]\n\t"
            "v_mfma_f32_16x16x32_bf16 v[176:179], v[68:71], v[208:211], v[176:179]\n\t"
            "v_mfma_f32_16x16x32_bf16 v[180:183], v[92:95], v[208:211], v[180:183]\n\t"
            "v_mfma_f32_16x16x32_bf16 v[180:183], v[88:91], v[204:207], v[180:183]\n\t"
            "v_mfma_f32_16x16x32_bf16 v[184:187], v[112:115], v[204:207], v[184:187]\n\t"
            "v_mfma_f32_16x16x32_bf16 v[184:187], v[116:119], v[208:211], v[184:187]\n\t"
            "v_mfma_f32_16x16x32_bf16 v[188:191], v[140:143], v[208:211], v[188:191]\n\t"
            "ds_read_b128 v[208:211], v233 offset:12416\n\t"
            "v_mfma_f32_16x16x32_bf16 v[188:191], v[136:139], v[204:207], v[188:191]\n\t"
            "ds_read_b128 v[204:207], v232 offset:12416\n\t"
            "s_cmp_eq_u32 s91, 0\n\t"
            "s_cbranch_scc1 .Lwa_loop%=\n\t"
            "s_mov_b32 s91, 0\n\t"
            "s_waitcnt vmcnt(0)\n\t"
            "s_cmp_eq_u32 s81, 0\n\t"
            "s_cbranch_scc1 .Lwa_h6%=\n\t"
            "s_mov_b32 s89, 0x10000\n\t"
            ".Lwa_h4%=:\n\t"
            "v_readfirstlane_b32 s49, v228\n\t"
            "v_readfirstlane_b32 vcc_lo, v229\n\t"
            "s_xor_b32 s49, s49, s82\n\t"
            "s_xor_b32 vcc_lo, vcc_lo, s83\n\t"
            "s_or_b32 s49, s49, vcc_lo\n\t"
            "s_cbranch_scc0 .Lwa_h5%=\n\t"
            "s_sub_u32 s89, s89, 1\n\t"
            "s_cbranch_scc1 .Lwa_h5%=\n\t"
            "s_sleep 2\n\t"
            "buffer_load_dwordx3 v[228:230], off, s[84:87], s81 offset:1216 sc1\n\t"
            "s_waitcnt vmcnt(0)\n\t"
            "s_branch .Lwa_h4%=\n\t"
            ".Lwa_h5%=:\n\t"
            "s_lshl_b32 s49, s70, 2\n\t"
            "s_add_u32 s49, s49, 163520\n\t"
            "v_mov_b32 v228, s49\n\t"
            "s_mul_i32 s49, s70, 800\n\t"
            "s_add_u32 m0, s49, 113760\n\t"
            "ds_write_b32 v228, v230 offset:12\n\t"
            "s_mov_b32 exec_hi, 0x3ffff\n\t"
            "buffer_load_dwordx4 v248, s[84:87], s81 offen sc1 lds\n\t"
            "s_mul_i32 s49, s70, 384\n\t"
            "s_add_u32 m0, s49, 130176\n\t"
            "s_add_u32 s81, s81, 800\n\t"
            "s_mov_b64 exec, 0xffffff\n\t"
            "buffer_load_dwordx4 v248, s[84:87], s81 offen sc1 lds\n\t"
            "s_mov_b64 exec, -1\n\t"
            "s_mov_b32 s81, 0\n\t"
            "s_waitcnt lgkmcnt(0)\n\t"
            ".Lwa_h6%=:\n\t"
            "s_cmp_eq_u32 s90, 0\n\t"
            "s_cbranch_scc1 .Lwa_loop%=\n\t"
            "v_readfirstlane_b32 s49, v231\n\t"
            "s_sub_u32 s90, s90, 1\n\t"
            "s_cmp_lt_u32 s49, 209\n\t"
            "s_cselect_b32 s90, s90, 0\n\t"
            "s_cbranch_scc0 .Lwa_loop%=\n\t"
            "s_mul_i32 s81, s49, 1280\n\t"
            "s_add_u32 s81, s81, 303104\n\t"
            "s_branch .Lwa_loop%=\n\t"
            ".Lwa_h1%=:\n\t"
            "s_cmp_lt_u32 s70, 20\n\t"
            "s_cbranch_scc1 .Lwa_h2%=\n\t"
            "s_lshl_b32 s49, s70, 2\n\t"
            "s_add_u32 s49, s49, 163520\n\t"
            "v_mov_b32 v228, s49\n\t"
            "ds_read_b32 v228, v228\n\t"
            "s_waitcnt lgkmcnt(0)\n\t"
            "v_readfirstlane_b32 s49, v228\n\t"
            "s_cmp_eq_u32 s49, 0x7ffffff\n\t"
            "s_cbranch_scc1 .Lwa_last%=\n\t"
            "s_branch .Lwa_r1%=\n\t"
            ".Lwa_h2%=:\n\t"
            "s_waitcnt vmcnt(0)\n\t"
            "s_or_b32 s91, s81, s90\n\t"
            "s_cmp_eq_u32 s81, 0\n\t"
            "s_cbranch_scc1 .Lwa_h3%=\n\t"
            "buffer_load_dwordx3 v[228:230], off, s[84:87], s81 offset:1216 sc1\n\t"
            ".Lwa_h3%=:\n\t"
            "s_cmp_eq_u32 s90, 0\n\t"
            "s_cbranch_scc1 .Lwa_r1%=\n\t"
            "v_mov_b32 v231, 1\n\t"
            "s_mov_b32 s49, 301184\n\t"
            "s_mov_b64 exec, 1\n\t"
            "buffer_atomic_add v231, off, s[84:87], s49 sc0\n\t"
            "s_mov_b64 exec, -1\n\t"
            "s_branch .Lwa_r1%=\n\t"
            ".Lwa_last%=:\n\t"
            "s_setprio 2\n\t"
            "s_waitcnt lgkmcnt(0)\n\t"
            "s_nop 7\n\t"
            "v_mfma_f32_16x16x32_bf16 v[156:159], v[240:243], v[184:187], 0\n\t"
            "ds_read_b64 v[176:177], v247\n\t"
            "ds_read_b64 v[196:197], v247 offset:1536\n\t"
            "v_mfma_f32_16x16x32_bf16 v[156:159], v[240:243], v[188:191], v[156:159]\n\t"
            "ds_read_b64 v[220:221], v247 offset:3072\n\t"
            "v_xor_b32 v234, s73, v254\n\t"
            "v_mfma_f32_16x16x32_bf16 v[228:231], v[240:243], v[148:151], 0\n\t"
            "ds_read_u16 v178, v234\n\t"
            "ds_read_u16 v198, v234 offset:128\n\t"
            "v_mfma_f32_16x16x32_bf16 v[228:231], v[240:243], v[164:167], v[228:231]\n\t"
            "ds_read_u16 v222, v234 offset:256\n\t"
            "v_add_u32 v235, s45, v248\n\t"
            "v_mfma_f32_16x16x32_bf16 v[208:211], v[240:243], v[180:183], 0\n\t"
            "ds_read_b128 v[168:171], v235\n\t"
            "ds_read_b128 v[192:195], v235 offset:1536\n\t"
            "v_mfma_f32_16x16x32_bf16 v[208:211], v[240:243], v[172:175], v[208:211]\n\t"
            "ds_read_b128 v[216:219], v235 offset:3072\n\t"
            "v_mov_b32 v179, 0\n\t"
            "v_mov_b32 v199, 0\n\t"
            "v_mov_b32 v223, 0\n\t"
            "v_rcp_f32 v157, v156\n\t"
            "v_mad_u32_u24 v234, v246, s75, v253\n\t"
            "v_rcp_f32 v229, v228\n\t"
            "s_nop 0\n\t"
            "v_rcp_f32 v209, v208\n\t"
            "s_waitcnt lgkmcnt(2)\n\t"
            "v_mfma_f32_16x16x32_bf16 v[144:147], v[168:171], v[184:187], 0\n\t"
            "v_mfma_f32_16x16x32_bf16 v[144:147], v[176:179], v[188:191], v[144:147]\n\t"
            "s_waitcnt lgkmcnt(1)\n\t"
            "v_mfma_f32_16x16x32_bf16 v[152:155], v[192:195], v[184:187], 0\n\t"
            "v_mfma_f32_16x16x32_bf16 v[152:155], v[196:199], v[188:191], v[152:155]\n\t"
            "s_waitcnt lgkmcnt(0)\n\t"
            "v_mfma_f32_16x16x32_bf16 v[184:187], v[216:219], v[184:187], 0\n\t"
            "v_mfma_f32_16x16x32_bf16 v[184:187], v[220:223], v[188:191], v[184:187]\n\t"
            "ds_read_b64 v[188:189], v234 offset:320\n\t"
            "ds_read_b64 v[190:191], v234 offset:352\n\t"
            "v_mfma_f32_16x16x32_bf16 v[224:227], v[168:171], v[148:151], 0\n\t"
            "v_mul_f32 v144, v157, v144\n\t"
            "v_mul_f32 v145, v157, v145\n\t"
            "v_mfma_f32_16x16x32_bf16 v[224:227], v[176:179], v[164:167], v[224:227]\n\t"
            "v_mul_f32 v152, v157, v152\n\t"
            "v_mul_f32 v153, v157, v153\n\t"
            "v_mfma_f32_16x16x32_bf16 v[212:215], v[192:195], v[148:151], 0\n\t"
            "v_mul_f32 v184, v157, v184\n\t"
            "v_mul_f32 v185, v157, v185\n\t"
            "v_mfma_f32_16x16x32_bf16 v[212:215], v[196:199], v[164:167], v[212:215]\n\t"
            "v_mul_f32 v186, v157, v186\n\t"
            "v_mul_f32 v187, v157, v187\n\t"
            "v_mfma_f32_16x16x32_bf16 v[160:163], v[168:171], v[180:183], 0\n\t"
            "v_cvt_pk_bf16_f32 v184, v184, v185\n\t"
            "v_cvt_pk_bf16_f32 v185, v186, v187\n\t"
            "v_mfma_f32_16x16x32_bf16 v[160:163], v[176:179], v[172:175], v[160:163]\n\t"
            "ds_read_b64 v[186:187], v234 offset:288\n\t"
            "ds_write_b64 v245, v[184:185] offset:6400\n\t"
            "v_mfma_f32_16x16x32_bf16 v[204:207], v[192:195], v[180:183], 0\n\t"
            "ds_read_b64 v[184:185], v234 offset:256\n\t"
            "v_mul_f32 v224, v229, v224\n\t"
            "v_mfma_f32_16x16x32_bf16 v[204:207], v[196:199], v[172:175], v[204:207]\n\t"
            "v_mul_f32 v225, v229, v225\n\t"
            "v_mul_f32 v212, v229, v212\n\t"
            "v_mfma_f32_16x16x32_bf16 v[148:151], v[216:219], v[148:151], 0\n\t"
            "v_mul_f32 v213, v229, v213\n\t"
            "v_mul_f32 v160, v209, v160\n\t"
            "v_mfma_f32_16x16x32_bf16 v[148:151], v[220:223], v[164:167], v[148:151]\n\t"
            "v_mul_f32 v161, v209, v161\n\t"
            "v_mul_f32 v226, v229, v226\n\t"
            "v_mfma_f32_16x16x32_bf16 v[180:183], v[216:219], v[180:183], 0\n\t"
            "v_mul_f32 v204, v209, v204\n\t"
            "v_mul_f32 v205, v209, v205\n\t"
            "v_mfma_f32_16x16x32_bf16 v[180:183], v[220:223], v[172:175], v[180:183]\n\t"
            "v_mul_f32 v227, v229, v227\n\t"
            "v_cvt_pk_bf16_f32 v224, v224, v225\n\t"
            "v_mul_f32 v148, v229, v148\n\t"
            "v_mul_f32 v149, v229, v149\n\t"
            "v_mul_f32 v214, v229, v214\n\t"
            "v_mul_f32 v215, v229, v215\n\t"
            "v_cvt_pk_bf16_f32 v212, v212, v213\n\t"
            "s_waitcnt lgkmcnt(0)\n\t"
            "v_mfma_f32_16x16x32_bf16 v[200:203], v[240:243], v[184:187], 0\n\t"
            "v_mul_f32 v180, v209, v180\n\t"
            "v_mul_f32 v181, v209, v181\n\t"
            "v_mfma_f32_16x16x32_bf16 v[200:203], v[240:243], v[188:191], v[200:203]\n\t"
            "v_mul_f32 v150, v229, v150\n\t"
            "v_mul_f32 v151, v229, v151\n\t"
            "v_mfma_f32_16x16x32_bf16 v[168:171], v[184:187], v[168:171], 0\n\t"
            "v_cvt_pk_bf16_f32 v148, v148, v149\n\t"
            "v_mul_f32 v146, v157, v146\n\t"
            "v_mfma_f32_16x16x32_bf16 v[168:171], v[188:191], v[176:179], v[168:171]\n\t"
            "v_mul_f32 v147, v157, v147\n\t"
            "v_cvt_pk_bf16_f32 v144, v144, v145\n\t"
            "v_mfma_f32_16x16x32_bf16 v[192:195], v[184:187], v[192:195], 0\n\t"
            "v_rcp_f32 v201, v200\n\t"
            "v_mfma_f32_16x16x32_bf16 v[192:195], v[188:191], v[196:199], v[192:195]\n\t"
            "v_mul_f32 v154, v157, v154\n\t"
            "v_mul_f32 v155, v157, v155\n\t"
            "v_mfma_f32_16x16x32_bf16 v[216:219], v[184:187], v[216:219], 0\n\t"
            "v_cvt_pk_bf16_f32 v152, v152, v153\n\t"
            "v_mul_f32 v162, v209, v162\n\t"
            "v_mfma_f32_16x16x32_bf16 v[216:219], v[188:191], v[220:223], v[216:219]\n\t"
            "v_mul_f32 v163, v209, v163\n\t"
            "v_cvt_pk_bf16_f32 v160, v160, v161\n\t"
            "v_cndmask_b32_e64 v203, v168, v192, s[60:61]\n\t"
            "v_mul_f32 v206, v209, v206\n\t"
            "v_mul_f32 v207, v209, v207\n\t"
            "v_cvt_pk_bf16_f32 v204, v204, v205\n\t"
            "v_mul_f32 v182, v209, v182\n\t"
            "v_mul_f32 v183, v209, v183\n\t"
            "v_cvt_pk_bf16_f32 v180, v180, v181\n\t"
            "v_cndmask_b32_e64 v203, v203, v216, s[62:63]\n\t"
            "v_cvt_pk_bf16_f32 v225, v226, v227\n\t"
            "v_cvt_pk_bf16_f32 v213, v214, v215\n\t"
            "v_cvt_pk_bf16_f32 v149, v150, v151\n\t"
            "v_cvt_pk_bf16_f32 v145, v146, v147\n\t"
            "v_cvt_pk_bf16_f32 v153, v154, v155\n\t"
            "v_cvt_pk_bf16_f32 v161, v162, v163\n\t"
            "v_cvt_pk_bf16_f32 v205, v206, v207\n\t"
            "v_cvt_pk_bf16_f32 v181, v182, v183\n\t"
            "v_mul_f32_dpp v203, v201, v203 quad_perm:[0,0,0,0] row_mask:0xf bank_mask:0xf\n\t"
            "v_xad_u32 v202, s73, v254, v255\n\t"
            "v_cvt_pk_bf16_f32 v203, v203, 0\n\t"
            "ds_write_b64 v245, v[224:225]\n\t"
            "ds_write_b64 v245, v[212:213] offset:128\n\t"
            "ds_write_b64 v245, v[148:149] offset:256\n\t"
            "ds_write_b64 v245, v[144:145] offset:6144\n\t"
            "ds_write_b64 v245, v[152:153] offset:6272\n\t"
            "ds_write_b64 v245, v[160:161] offset:12288\n\t"
            "ds_write_b64 v245, v[204:205] offset:12416\n\t"
            "ds_write_b64 v245, v[180:181] offset:12544\n\t"
            "s_mov_b64 exec, s[68:69]\n\t"
            "ds_write_b16 v202, v203\n\t"
            "s_mov_b64 exec, 1\n\t"
            "ds_add_u32 v253, v246 offset:7856\n\t"
            "s_mov_b64 exec, -1\n\t"
            ".Lwa_done%=:\n\t"
            "s_waitcnt lgkmcnt(0)\n\t"
            "s_nop 7\n\t"
            : "+{v204}"(pST_), "+{v205}"(aRSw_), "+{v206}"(aPLw_), "+{v207}"(aRSr_), "+{v208}"(g16_), "+{v209}"(g8_), "+{v210}"(loVL_), "+{v211}"(loOL_), "+{v200}"(on), "+{v250}"(c0), "+{v251}"(c1), "+{v252}"(c2), "+{v253}"(c3), "+{v254}"(c4), "+{v255}"(c5),
              "+{v238}"(c6), "+{v239}"(c7), "+{v240}"(c8), "+{v241}"(c9), "+{s89}"(s89), "+{s48}"(s48), "+{s70}"(s70), "+{s72}"(s72), "+{s73}"(s73), "+{s74}"(s74), "+{s76}"(s76), "+{s77}"(s77)
            : "{v244}"(pE_), "{v245}"(aO_), "{v246}"(sel_), "{v247}"(aVB_), "{s47}"((ldsb + X_OFF) ^ (ldsb + X1_OFF)),
              "{s[58:59]}"(0xFFFFull), "{s75}"(rsb), "{s45}"(ldsb + V_OFF + w * 4608), "{s[68:69]}"(0xFFFFFFFFFFFFull),
              "{s[84:87]}"(wr), "{s46}"(wave * 36864), "{s88}"(touch), "{s80}"(wave == 0 ? 16 : 20), "{s71}"(ldsb + O_OFF), "{s[54:55]}"(mrow), "{s[56:57]}"(0xF0000F0000F0000Full),
              "{s[78:79]}"(mrow & 0xF0000F0000F0000Full)
            : "v144", "v145", "v146", "v147", "v148", "v149", "v150", "v151", "v152", "v153", "v154", "v155", "v156", "v157", "v158", "v159", "v160", "v161", "v162", "v163", "v164", "v165", "v166", "v167", "v168", "v169", "v170", "v171", "v172", "v173", "v174", "v175", "v176", "v177", "v178", "v179", "v180", "v181", "v182", "v183", "v184", "v185", "v186", "v187", "v188", "v189", "v190", "v191", "v192", "v193", "v194", "v195", "v196", "v197", "v198", "v199", "v201", "v202", "v203", "v212", "v213", "v214", "v215", "v216", "v217", "v218", "v219", "v220", "v221", "v222", "v223", "v224", "v225", "v226", "v227", "v228", "v229", "v230", "v231", "v232", "v233", "v234", "v235", "v236", "v237", "v242", "v243", "v248", "v249", "v0", "v1", "v2", "v3", "v4", "v5", "v6", "v7", "v8", "v9", "v10", "v11", "v12", "v13", "v14", "v15", "v16", "v17", "v18", "v19", "v20", "v21", "v22", "v23", "v24", "v25", "v26", "v27", "v28", "v29", "v30", "v31", "v32", "v33", "v34", "v35", "v36", "v37", "v38", "v39", "v40", "v41", "v42", "v43", "v44", "v45", "v46", "v47", "v48", "v49", "v50", "v51", "v52", "v53", "v54", "v55", "v56", "v57", "v58", "v59", "v60", "v61", "v62", "v63", "v64", "v65", "v66", "v67", "v68", "v69", "v70", "v71", "v72", "v73", "v74", "v75", "v76", "v77", "v78", "v79", "v80", "v81", "v82", "v83", "v84", "v85", "v86", "v87", "v88", "v89", "v90", "v91", "v92", "v93", "v94", "v95", "v96", "v97", "v98", "v99", "v100", "v101", "v102", "v103", "v104", "v105", "v106", "v107", "v108", "v109", "v110", "v111", "v112", "v113", "v114", "v115", "v116", "v117", "v118", "v119", "v120", "v121", "v122", "v123", "v124", "v125", "v126", "v127", "v128", "v129", "v130", "v131", "v132", "v133", "v134", "v135", "v136", "v137", "v138", "v139", "v140", "v141", "v142", "v143",  "m0", "s60", "s61", "s62", "s63", "s64", "s65", "s66", "s67", "s81", "s82", "s83", "s90", "s91", "s49", "vcc", "scc", "memory");

        }
        if (wave == 0 && blockIdx.x == 0) {
            int ln_;
            asm volatile("v_mbcnt_lo_u32_b32 %0, -1, 0\n\tv_mbcnt_hi_u32_b32 %0, -1, %0" : "=v"(ln_));
            KARGS
            unsigned long long tg;
            asm volatile("s_load_dwordx2 %0, %1, %2\n\ts_waitcnt lgkmcnt(0)" : "=s"(tg) : "s"(wb), "n"(EPO_OFF) : "memory");
            const unsigned lo = (unsigned)tg + 1u, hi = (unsigned)(tg >> 32);
            const auto wq = __builtin_amdgcn_make_buffer_rsrc((void*)wb, 0, WS_BYTES, 0x00020000);
            for (int it_ = 0; it_ < 100000; ++it_) {
                bool bad = false;
#pragma unroll
                for (int k = 0; k < 2; ++k) {
                    const u32x4 a_ = __builtin_amdgcn_raw_buffer_load_b128(wq, ARR_OFF + ln_ * 16, k * 1024, SC1);
                    bad |= a_[0] != lo || a_[1] != hi || a_[2] != lo || a_[3] != hi;
                }
                if (__builtin_amdgcn_ballot_w64(bad) == 0ull) break;
                __builtin_amdgcn_s_sleep(2);
            }
            if (ln_ == 0) __builtin_amdgcn_raw_buffer_store_b64((u32x2){lo, hi}, wq, EPO_OFF, 0, SC1);
        }
        asm volatile("wk_code_end:");
        __builtin_amdgcn_endpgm();
    } else if (wave < 6) {
        LANE_SETUP
        {
            const unsigned rsb = ldsb + RSX_OFF;
            const unsigned pE_ = fE - X_OFF;
            const unsigned aVAw_ = ldsb + V_OFF + w * 9216 + lane * 16, aVBw_ = ldsb + V_OFF + w * 9216 + 1024 + lane * 8;
            const unsigned pST_ = lane * 16;
            unsigned aRSw_ = rsb + 256 + lane * 4, aRSr_ = rsb + (lane & 48);
            B_SCALARS
            unsigned s44 = 4;
            asm volatile(
            "s_waitcnt lgkmcnt(0)\n\t"
            "s_mov_b32 s83, 0x10000\n\t"
            "s_mov_b32 s84, -47104\n\t"
            "s_nop 7\n\t"
            "v_mov_b32 v200, v250\n\t"
            "v_mov_b32 v201, v251\n\t"
            "v_mov_b32 v202, v253\n\t"
            "s_mov_b32 s38, s67\n\t"
            "s_mov_b32 s67, 74496\n\t"
            "ds_read_b64 v[250:251], v203\n\t"
            "ds_read_b32 v253, v203 offset:8\n\t"
            ".Lwk_b1%=:\n\t"
            "v_mov_b32 v236, 0\n\t"
            "ds_read_b32 v236, v236 offset:7864\n\t"
            "s_waitcnt lgkmcnt(0)\n\t"
            "v_readfirstlane_b32 s77, v236\n\t"
            "s_cmp_ge_u32 s77, 4\n\t"
            "s_cbranch_scc1 .Lwk_b2%=\n\t"
            "s_cmp_eq_u32 s83, 0\n\t"
            "s_cbranch_scc1 .Lwk_b2%=\n\t"
            "s_sub_u32 s83, s83, 1\n\t"
            "s_sleep 1\n\t"
            "s_branch .Lwk_b1%=\n\t"
            ".Lwk_b2%=:\n\t"
            "s_waitcnt vmcnt(0)\n\t"
            "s_call_b64 vcc, wk_conv\n\t"
            "v_mov_b32 v250, v200\n\t"
            "v_mov_b32 v251, v201\n\t"
            "v_mov_b32 v253, v202\n\t"
            "s_mov_b32 s67, s38\n\t"
            "s_setprio 3\n\t"
            "s_barrier\n\t"
            "buffer_load_dwordx4 v[140:143], v245, s[28:31], s82 offen sc1\n\t"
            "s_add_u32 s49, s33, 0\n\t"
            "buffer_load_dwordx4 v[0:3], v245, s[28:31], s49 offen sc1\n\t"
            "s_add_u32 s49, s33, 6144\n\t"
            "buffer_load_dwordx4 v[24:27], v245, s[28:31], s49 offen sc1\n\t"
            "s_add_u32 s49, s33, 12288\n\t"
            "buffer_load_dwordx4 v[48:51], v245, s[28:31], s49 offen sc1\n\t"
            "s_add_u32 s49, s33, 18432\n\t"
            "buffer_load_dwordx4 v[72:75], v245, s[28:31], s49 offen sc1\n\t"
            "s_add_u32 s49, s33, 24576\n\t"
            "buffer_load_dwordx4 v[96:99], v245, s[28:31], s49 offen sc1\n\t"
            "s_add_u32 s49, s33, 30720\n\t"
            "buffer_load_dwordx4 v[120:123], v245, s[28:31], s49 offen sc1\n\t"
            "s_add_u32 s49, s33, 1024\n\t"
            "buffer_load_dwordx4 v[4:7], v245, s[28:31], s49 offen sc1\n\t"
            "s_add_u32 s49, s33, 7168\n\t"
            "buffer_load_dwordx4 v[28:31], v245, s[28:31], s49 offen sc1\n\t"
            "s_add_u32 s49, s33, 13312\n\t"
            "buffer_load_dwordx4 v[52:55], v245, s[28:31], s49 offen sc1\n\t"
            "s_add_u32 s49, s33, 19456\n\t"
            "buffer_load_dwordx4 v[76:79], v245, s[28:31], s49 offen sc1\n\t"
            "s_add_u32 s49, s33, 25600\n\t"
            "buffer_load_dwordx4 v[100:103], v245, s[28:31], s49 offen sc1\n\t"
            "s_add_u32 s49, s33, 31744\n\t"
            "buffer_load_dwordx4 v[124:127], v245, s[28:31], s49 offen sc1\n\t"
            "s_add_u32 s49, s33, 2048\n\t"
            "buffer_load_dwordx4 v[8:11], v245, s[28:31], s49 offen sc1\n\t"
            "s_add_u32 s49, s33, 8192\n\t"
            "buffer_load_dwordx4 v[32:35], v245, s[28:31], s49 offen sc1\n\t"
            "s_add_u32 s49, s33, 14336\n\t"
            "buffer_load_dwordx4 v[56:59], v245, s[28:31], s49 offen sc1\n\t"
            "s_add_u32 s49, s33, 20480\n\t"
            "buffer_load_dwordx4 v[80:83], v245, s[28:31], s49 offen sc1\n\t"
            "s_add_u32 s49, s33, 26624\n\t"
            "buffer_load_dwordx4 v[104:107], v245, s[28:31], s49 offen sc1\n\t"
            "s_add_u32 s49, s33, 32768\n\t"
            "buffer_load_dwordx4 v[128:131], v245, s[28:31], s49 offen sc1\n\t"
            "s_add_u32 s49, s33, 3072\n\t"
            "buffer_load_dwordx4 v[12:15], v245, s[28:31], s49 offen sc1\n\t"
            "s_add_u32 s49, s33, 9216\n\t"
            "buffer_load_dwordx4 v[36:39], v245, s[28:31], s49 offen sc1\n\t"
            "s_add_u32 s49, s33, 15360\n\t"
            "buffer_load_dwordx4 v[60:63], v245, s[28:31], s49 offen sc1\n\t"
            "s_add_u32 s49, s33, 21504\n\t"
            "buffer_load_dwordx4 v[84:87], v245, s[28:31], s49 offen sc1\n\t"
            "s_add_u32 s49, s33, 27648\n\t"
            "buffer_load_dwordx4 v[108:111], v245, s[28:31], s49 offen sc1\n\t"
            "s_add_u32 s49, s33, 33792\n\t"
            "buffer_load_dwordx4 v[132:135], v245, s[28:31], s49 offen sc1\n\t"
            "s_add_u32 s49, s33, 4096\n\t"
            "buffer_load_dwordx4 v[16:19], v245, s[28:31], s49 offen sc1\n\t"
            "s_add_u32 s49, s33, 10240\n\t"
            "buffer_load_dwordx4 v[40:43], v245, s[28:31], s49 offen sc1\n\t"
            "s_add_u32 s49, s33, 16384\n\t"
            "buffer_load_dwordx4 v[64:67], v245, s[28:31], s49 offen sc1\n\t"
            "s_add_u32 s49, s33, 22528\n\t"
            "buffer_load_dwordx4 v[88:91], v245, s[28:31], s49 offen sc1\n\t"
            "s_add_u32 s49, s33, 28672\n\t"
            "buffer_load_dwordx4 v[112:115], v245, s[28:31], s49 offen sc1\n\t"
            "s_add_u32 s49, s33, 34816\n\t"
            "buffer_load_dwordx4 v[136:139], v245, s[28:31], s49 offen sc1\n\t"
            "s_add_u32 s49, s33, 5120\n\t"
            "buffer_load_dwordx4 v[20:23], v245, s[28:31], s49 offen sc1\n\t"
            "s_add_u32 s49, s33, 11264\n\t"
            "buffer_load_dwordx4 v[44:47], v245, s[28:31], s49 offen sc1\n\t"
            "s_add_u32 s49, s33, 17408\n\t"
            "buffer_load_dwordx4 v[68:71], v245, s[28:31], s49 offen sc1\n\t"
            "s_add_u32 s49, s33, 23552\n\t"
            "buffer_load_dwordx4 v[92:95], v245, s[28:31], s49 offen sc1\n\t"
            "s_add_u32 s49, s33, 29696\n\t"
            "buffer_load_dwordx4 v[116:119], v245, s[28:31], s49 offen sc1\n\t"
            "s_add_u32 s49, s33, 35840\n\t"
            "buffer_load_dwordx4 v[140:143], v245, s[28:31], s49 offen sc1\n\t"
            "ds_read_b32 v249, v252 offset:12\n\t"
            "s_waitcnt lgkmcnt(0)\n\t"
            "v_and_b32 v249, s69, v249\n\t"
            "s_mov_b32 exec_hi, 0x3fffff\n\t"
            "s_add_u32 m0, s66, 0\n\t"
            "s_add_u32 s49, s79, 0\n\t"
            "buffer_load_dwordx4 v249, s[72:75], s49 offen lds\n\t"
            "s_mov_b32 exec_hi, -1\n\t"
            "s_mov_b32 exec_hi, 0x3fffff\n\t"
            "s_add_u32 m0, s66, 872\n\t"
            "s_add_u32 s49, s47, 196224\n\t"
            "buffer_load_dwordx4 v249, s[72:75], s49 offen lds\n\t"
            "s_mov_b32 exec_hi, -1\n\t"
            "s_mov_b32 exec_hi, 0x3fffff\n\t"
            "s_add_u32 m0, s66, 1744\n\t"
            "s_add_u32 s49, s47, 392448\n\t"
            "buffer_load_dwordx4 v249, s[72:75], s49 offen lds\n\t"
            "s_mov_b32 exec_hi, -1\n\t"
            "s_mov_b32 exec_hi, 0x3fffff\n\t"
            "s_add_u32 m0, s66, 2616\n\t"
            "s_add_u32 s49, s47, 588672\n\t"
            "buffer_load_dwordx4 v249, s[72:75], s49 offen lds\n\t"
            "s_mov_b32 exec_hi, -1\n\t"
            "s_mov_b32 exec_hi, 0x3fffff\n\t"
            "s_add_u32 m0, s66, 3488\n\t"
            "s_add_u32 s49, s47, 784896\n\t"
            "buffer_load_dwordx4 v249, s[72:75], s49 offen lds\n\t"
            "s_mov_b32 exec_hi, -1\n\t"
            "s_mov_b32 exec_hi, 0x3fffff\n\t"
            "s_add_u32 m0, s66, 4360\n\t"
            "s_add_u32 s49, s47, 981120\n\t"
            "buffer_load_dwordx4 v249, s[72:75], s49 offen lds\n\t"
            "s_mov_b32 exec_hi, -1\n\t"
            "s_mov_b32 exec_hi, 0x3fffff\n\t"
            "s_add_u32 m0, s66, 5232\n\t"
            "s_add_u32 s49, s47, 1177344\n\t"
            "buffer_load_dwordx4 v249, s[72:75], s49 offen lds\n\t"
            "s_mov_b32 exec_hi, -1\n\t"
            "s_mov_b32 exec_hi, 0x3fffff\n\t"
            "s_add_u32 m0, s66, 6104\n\t"
            "s_add_u32 s49, s47, 1373568\n\t"
            "buffer_load_dwordx4 v249, s[72:75], s49 offen lds\n\t"
            "s_mov_b32 exec_hi, -1\n\t"
            "s_mov_b32 exec_hi, 0x3fffff\n\t"
            "s_add_u32 m0, s66, 6976\n\t"
            "s_add_u32 s49, s47, 1569792\n\t"
            "buffer_load_dwordx4 v249, s[72:75], s49 offen lds\n\t"
            "s_mov_b32 exec_hi, -1\n\t"
            "ds_read_b128 v[168:171], v254\n\t"
            "ds_read_b128 v[172:175], v254 offset:64\n\t"
            "v_add_u32 v180, s48, v245\n\t"
            "v_add_u32 v232, s48, v242\n\t"
            "v_xor_b32 v233, 64, v232\n\t"
            "v_xor_b32 v246, 0x100, v246\n\t"
            "ds_read_b128 v[176:179], v180 offset:18816\n\t"
            "ds_read_b128 v[180:183], v180 offset:19840\n\t"
            "ds_read_b128 v[192:195], v232\n\t"
            "ds_read_b128 v[196:199], v233\n\t"
            "ds_read_b128 v[224:227], v232 offset:128\n\t"
            "ds_read_b128 v[228:231], v233 offset:128\n\t"
            "s_waitcnt lgkmcnt(5)\n\t"
            "v_add_f32 v176, v176, v178\n\t"
            "s_waitcnt lgkmcnt(4)\n\t"
            "v_add_f32 v180, v180, v182\n\t"
            "v_add_f32 v176, v176, v180\n\t"
            "v_add_f32 v177, v177, v179\n\t"
            "v_add_f32 v181, v181, v183\n\t"
            "v_mul_f32 v176, 0x3baaaaab, v176\n\t"
            "v_add_f32 v177, v177, v181\n\t"
            "v_mul_f32_e64 v176, v176, -v176\n\t"
            "v_fmac_f32 v176, 0x3baaaaab, v177\n\t"
            "v_max_f32 v176, 0, v176\n\t"
            "v_add_f32 v176, 0x3727c5ac, v176\n\t"
            "v_rsq_f32 v176, v176\n\t"
            "s_nop 0\n\t"
            "ds_write_b32 v246, v176\n\t"
            "s_setprio 3\n\t"
            "s_waitcnt vmcnt(9)\n\t"
            "s_waitcnt lgkmcnt(3)\n\t"
            "v_mfma_f32_16x16x32_bf16 v[200:203], v[192:195], v[0:3], 0\n\t"
            "v_mfma_f32_16x16x32_bf16 v[200:203], v[196:199], v[4:7], v[200:203]\n\t"
            "v_mfma_f32_16x16x32_bf16 v[204:207], v[196:199], v[28:31], 0\n\t"
            "v_mfma_f32_16x16x32_bf16 v[204:207], v[192:195], v[24:27], v[204:207]\n\t"
            "v_mfma_f32_16x16x32_bf16 v[208:211], v[192:195], v[48:51], 0\n\t"
            "v_mfma_f32_16x16x32_bf16 v[208:211], v[196:199], v[52:55], v[208:211]\n\t"
            "v_mfma_f32_16x16x32_bf16 v[212:215], v[196:199], v[76:79], 0\n\t"
            "v_mfma_f32_16x16x32_bf16 v[212:215], v[192:195], v[72:75], v[212:215]\n\t"
            "v_mfma_f32_16x16x32_bf16 v[216:219], v[192:195], v[96:99], 0\n\t"
            "v_mfma_f32_16x16x32_bf16 v[216:219], v[196:199], v[100:103], v[216:219]\n\t"
            "v_mfma_f32_16x16x32_bf16 v[220:223], v[196:199], v[124:127], 0\n\t"
            "v_mfma_f32_16x16x32_bf16 v[220:223], v[192:195], v[120:123], v[220:223]\n\t"
            "s_waitcnt lgkmcnt(1)\n\t"
            "v_mfma_f32_16x16x32_bf16 v[200:203], v[224:227], v[8:11], v[200:203]\n\t"
            "v_mfma_f32_16x16x32_bf16 v[200:203], v[228:231], v[12:15], v[200:203]\n\t"
            "v_mfma_f32_16x16x32_bf16 v[204:207], v[228:231], v[36:39], v[204:207]\n\t"
            "v_mfma_f32_16x16x32_bf16 v[204:207], v[224:227], v[32:35], v[204:207]\n\t"
            "v_mfma_f32_16x16x32_bf16 v[208:211], v[224:227], v[56:59], v[208:211]\n\t"
            "v_mfma_f32_16x16x32_bf16 v[208:211], v[228:231], v[60:63], v[208:211]\n\t"
            "v_mfma_f32_16x16x32_bf16 v[212:215], v[228:231], v[84:87], v[212:215]\n\t"
            "v_mfma_f32_16x16x32_bf16 v[212:215], v[224:227], v[80:83], v[212:215]\n\t"
            "v_mfma_f32_16x16x32_bf16 v[216:219], v[224:227], v[104:107], v[216:219]\n\t"
            "s_waitcnt vmcnt(0)\n\t"
            "ds_read_b64 v[144:145], v253\n\t"
            "v_mfma_f32_16x16x32_bf16 v[216:219], v[228:231], v[108:111], v[216:219]\n\t"
            "ds_read_b64 v[146:147], v253 offset:96\n\t"
            "ds_read_b64 v[148:149], v253 offset:1744\n\t"
            "v_mfma_f32_16x16x32_bf16 v[220:223], v[228:231], v[132:135], v[220:223]\n\t"
            "ds_read_b128 v[228:231], v233 offset:256\n\t"
            "ds_read_b64 v[150:151], v253 offset:1840\n\t"
            "v_mfma_f32_16x16x32_bf16 v[220:223], v[224:227], v[128:131], v[220:223]\n\t"
            "ds_read_b128 v[224:227], v232 offset:256\n\t"
            "ds_read_b64 v[152:153], v253 offset:8\n\t"
            "ds_read_b64 v[154:155], v253 offset:104\n\t"
            "ds_read_b64 v[156:157], v253 offset:1752\n\t"
            "ds_read_b64 v[158:159], v253 offset:1848\n\t"
            "ds_read_b64 v[160:161], v253 offset:16\n\t"
            "ds_read_b64 v[162:163], v253 offset:112\n\t"
            "ds_read_b64 v[164:165], v253 offset:1760\n\t"
            "ds_read_b64 v[166:167], v253 offset:1856\n\t"
            "s_waitcnt lgkmcnt(0)\n\t"
            "s_nop 7\n\t"
            ".p2align 6\n\t"
            "s_nop 0\n\t"
            "s_nop 0\n\t"
            "s_nop 0\n\t"
            "s_nop 0\n\t"
            ".Lwc_loop%=:\n\t"
            "s_setprio 1\n\t"
            "s_setprio 3\n\t"
            "v_mfma_f32_16x16x32_bf16 v[200:203], v[224:227], v[16:19], v[200:203]\n\t"
            "v_mov_b64 v[176:177], 0\n\t"
            "v_mov_b64 v[178:179], 0\n\t"
            "v_mfma_f32_16x16x32_bf16 v[200:203], v[228:231], v[20:23], v[200:203]\n\t"
            "v_mov_b64 v[180:181], 0\n\t"
            "v_mov_b64 v[182:183], 0\n\t"
            "s_waitcnt lgkmcnt(0)\n\t"
            "v_smfmac_f32_16x16x64_bf16 v[176:179], v[168:171], v[144:151], v255\n\t"
            "ds_read_b128 v[168:171], v254 offset:128\n\t"
            "v_smfmac_f32_16x16x64_bf16 v[180:183], v[172:175], v[152:159], v255\n\t"
            "ds_read_b64 v[144:145], v253 offset:24\n\t"
            "v_mfma_f32_16x16x32_bf16 v[204:207], v[228:231], v[44:47], v[204:207]\n\t"
            "ds_read_b64 v[146:147], v253 offset:120\n\t"
            "ds_read_b64 v[148:149], v253 offset:1768\n\t"
            "v_mfma_f32_16x16x32_bf16 v[204:207], v[224:227], v[40:43], v[204:207]\n\t"
            "ds_read_b64 v[150:151], v253 offset:1864\n\t"
            "ds_read_b128 v[172:175], v254 offset:192\n\t"
            "v_mfma_f32_16x16x32_bf16 v[208:211], v[224:227], v[64:67], v[208:211]\n\t"
            "ds_read_b64 v[152:153], v253 offset:32\n\t"
            "ds_read_b64 v[154:155], v253 offset:128\n\t"
            "v_mfma_f32_16x16x32_bf16 v[208:211], v[228:231], v[68:71], v[208:211]\n\t"
            "ds_read_b64 v[156:157], v253 offset:1776\n\t"
            "ds_read_b64 v[158:159], v253 offset:1872\n\t"
            "v_mfma_f32_16x16x32_bf16 v[212:215], v[228:231], v[92:95], v[212:215]\n\t"
            "v_mov_b32 v249, s76\n\t"
            "v_mov_b64 v[184:185], 0\n\t"
            "v_mfma_f32_16x16x32_bf16 v[212:215], v[224:227], v[88:91], v[212:215]\n\t"
            "ds_read_b32 v249, v249\n\t"
            "v_mov_b64 v[186:187], 0\n\t"
            "v_mfma_f32_16x16x32_bf16 v[216:219], v[224:227], v[112:115], v[216:219]\n\t"
            "v_mov_b64 v[188:189], 0\n\t"
            "v_mov_b64 v[190:191], 0\n\t"
            "v_mfma_f32_16x16x32_bf16 v[216:219], v[228:231], v[116:119], v[216:219]\n\t"
            "v_cvt_pk_bf16_f32 v176, v176, v177\n\t"
            "v_add_u32 v236, s67, v250\n\t"
            "s_waitcnt lgkmcnt(10)\n\t"
            "v_smfmac_f32_16x16x64_bf16 v[184:187], v[168:171], v[160:167], v255\n\t"
            "ds_read_b128 v[168:171], v254 offset:256\n\t"
            "s_waitcnt lgkmcnt(6)\n\t"
            "v_smfmac_f32_16x16x64_bf16 v[188:191], v[172:175], v[144:151], v255\n\t"
            "ds_read_b64 v[160:161], v253 offset:40\n\t"
            "v_mfma_f32_16x16x32_bf16 v[220:223], v[228:231], v[140:143], v[220:223]\n\t"
            "ds_read_b64 v[162:163], v253 offset:136\n\t"
            "ds_read_b64 v[164:165], v253 offset:1784\n\t"
            "v_mfma_f32_16x16x32_bf16 v[220:223], v[224:227], v[136:139], v[220:223]\n\t"
            "s_setprio 1\n\t"
            "ds_read_b64 v[166:167], v253 offset:1880\n\t"
            "ds_read_b128 v[172:175], v254 offset:320\n\t"
            "ds_read_b64 v[144:145], v253 offset:48\n\t"
            "ds_read_b64 v[146:147], v253 offset:144\n\t"
            "ds_read_b64 v[148:149], v253 offset:1792\n\t"
            "ds_read_b64 v[150:151], v253 offset:1888\n\t"
            "s_waitcnt lgkmcnt(14)\n\t"
            "ds_read_b128 v[224:227], v232 offset:6272\n\t"
            "s_waitcnt lgkmcnt(14)\n\t"
            "ds_read_b128 v[228:231], v233 offset:6272\n\t"
            "s_waitcnt lgkmcnt(12)\n\t"
            "v_readfirstlane_b32 s26, v249\n\t"
            "v_cvt_pk_bf16_f32 v177, v178, v179\n\t"
            "v_cvt_pk_bf16_f32 v178, v180, v181\n\t"
            "s_and_b32 s68, s26, 0x7ffffff\n\t"
            "s_add_u32 s68, s68, s78\n\t"
            "s_and_b32 s69, s26, 0x38000000\n\t"
            "s_or_b32 s69, s69, s39\n\t"
            "s_bitcmp1_b32 s26, 27\n\t"
            "s_cselect_b32 s79, 2.0, s68\n\t"
            "s_setprio 3\n\t"
            "v_mfma_f32_4x4x4_16b_bf16 v[196:199], v[176:177], v[176:177], 0\n\t"
            "s_setprio 1\n\t"
            "ds_read_b32 v249, v252 offset:12\n\t"
            "s_and_b32 s57, s56, 3\n\t"
            "s_lshl_b32 s58, s57, 3\n\t"
            "s_lshr_b32 m0, s54, s58\n\t"
            "s_lshl_b32 s58, s57, 4\n\t"
            "s_lshr_b64 s[36:37], s[50:51], s58\n\t"
            "s_and_b32 s57, s36, 0xffff\n\t"
            "s_add_u32 s57, s57, s67\n\t"
            "v_writelane_b32 v236, s57, 63\n\t"
            "v_writelane_b32 v236, s59, m0\n\t"
            "v_cvt_pk_bf16_f32 v179, v182, v183\n\t"
            "v_mov_b64 v[180:181], 0\n\t"
            "v_mov_b64 v[182:183], 0\n\t"
            "s_setprio 3\n\t"
            "v_mfma_f32_16x16x32_bf16 v[192:195], v[238:241], v[176:179], 0\n\t"
            "s_lshr_b64 s[36:37], s[52:53], s58\n\t"
            "s_and_b32 s58, s36, 0xffff\n\t"
            "s_add_u32 s58, s58, s67\n\t"
            "v_mfma_f32_4x4x4_16b_bf16 v[196:199], v[178:179], v[178:179], v[196:199]\n\t"
            "ds_write_b128 v236, v[176:179]\n\t"
            "s_waitcnt lgkmcnt(8)\n\t"
            "v_smfmac_f32_16x16x64_bf16 v[180:183], v[172:175], v[160:167], v255\n\t"
            "s_setprio 1\n\t"
            "s_and_b32 s38, s52, 0xffff\n\t"
            "s_add_u32 s38, s38, s67\n\t"
            "ds_read_b128 v[172:175], v254 offset:448\n\t"
            "ds_read_b64 v[160:161], v253 offset:64\n\t"
            "ds_read_b64 v[162:163], v253 offset:160\n\t"
            "ds_read_b64 v[164:165], v253 offset:1808\n\t"
            "ds_read_b64 v[166:167], v253 offset:1904\n\t"
            "v_mov_b64 v[176:177], 0\n\t"
            "v_mov_b64 v[178:179], 0\n\t"
            "v_cvt_pk_bf16_f32 v184, v184, v185\n\t"
            "v_cvt_pk_bf16_f32 v185, v186, v187\n\t"
            "s_setprio 3\n\t"
            "v_smfmac_f32_16x16x64_bf16 v[176:179], v[168:171], v[152:159], v255\n\t"
            "ds_read_b128 v[168:171], v254 offset:384\n\t"
            "v_mfma_f32_4x4x4_16b_bf16 v[196:199], v[184:185], v[184:185], v[196:199]\n\t"
            "s_setprio 1\n\t"
            "ds_read_b64 v[152:153], v253 offset:56\n\t"
            "s_waitcnt lgkmcnt(14)\n\t"
            "ds_read_b64 v[154:155], v253 offset:152\n\t"
            "s_waitcnt lgkmcnt(14)\n\t"
            "ds_read_b64 v[156:157], v253 offset:1800\n\t"
            "s_waitcnt lgkmcnt(14)\n\t"
            "ds_read_b64 v[158:159], v253 offset:1896\n\t"
            "v_xor_b32 v237, 16, v236\n\t"
            "v_cvt_pk_bf16_f32 v186, v188, v189\n\t"
            "v_cvt_pk_bf16_f32 v187, v190, v191\n\t"
            "v_mov_b64 v[188:189], 0\n\t"
            "v_mov_b64 v[190:191], 0\n\t"
            "s_setprio 3\n\t"
            "v_mfma_f32_16x16x32_bf16 v[192:195], v[238:241], v[184:187], v[192:195]\n\t"
            "s_waitcnt lgkmcnt(14)\n\t"
            "ds_write_b128 v237, v[184:187]\n\t"
            "v_mfma_f32_4x4x4_16b_bf16 v[196:199], v[186:187], v[186:187], v[196:199]\n\t"
            "s_setprio 1\n\t"
            "v_mov_b64 v[184:185], 0\n\t"
            "v_mov_b64 v[186:187], 0\n\t"
            "v_cvt_pk_bf16_f32 v176, v176, v177\n\t"
            "v_cvt_pk_bf16_f32 v177, v178, v179\n\t"
            "v_cvt_pk_bf16_f32 v178, v180, v181\n\t"
            "v_cvt_pk_bf16_f32 v179, v182, v183\n\t"
            "s_setprio 3\n\t"
            "v_mfma_f32_4x4x4_16b_bf16 v[196:199], v[176:177], v[176:177], v[196:199]\n\t"
            "s_waitcnt lgkmcnt(14)\n\t"
            "ds_write_b128 v236, v[176:179] offset:128\n\t"
            "s_waitcnt lgkmcnt(6)\n\t"
            "v_smfmac_f32_16x16x64_bf16 v[184:187], v[168:171], v[144:151], v255\n\t"
            "ds_read_b128 v[168:171], v254 offset:512\n\t"
            "s_waitcnt lgkmcnt(3)\n\t"
            "v_smfmac_f32_16x16x64_bf16 v[188:191], v[172:175], v[152:159], v255\n\t"
            "ds_read_b64 v[144:145], v253 offset:72\n\t"
            "v_mfma_f32_16x16x32_bf16 v[192:195], v[238:241], v[176:179], v[192:195]\n\t"
            "ds_read_b64 v[146:147], v253 offset:168\n\t"
            "ds_read_b64 v[148:149], v253 offset:1816\n\t"
            "v_mfma_f32_4x4x4_16b_bf16 v[196:199], v[178:179], v[178:179], v[196:199]\n\t"
            "s_setprio 1\n\t"
            "ds_read_b64 v[150:151], v253 offset:1912\n\t"
            "ds_read_b128 v[172:175], v254 offset:576\n\t"
            "ds_read_b64 v[152:153], v253 offset:80\n\t"
            "ds_read_b64 v[154:155], v253 offset:176\n\t"
            "ds_read_b64 v[156:157], v253 offset:1824\n\t"
            "ds_read_b64 v[158:159], v253 offset:1920\n\t"
            "v_mov_b64 v[176:177], 0\n\t"
            "v_mov_b64 v[178:179], 0\n\t"
            "v_mov_b64 v[180:181], 0\n\t"
            "v_mov_b64 v[182:183], 0\n\t"
            "v_cvt_pk_bf16_f32 v184, v184, v185\n\t"
            "v_cvt_pk_bf16_f32 v185, v186, v187\n\t"
            "v_cvt_pk_bf16_f32 v186, v188, v189\n\t"
            "v_cvt_pk_bf16_f32 v187, v190, v191\n\t"
            "s_setprio 3\n\t"
            "v_mfma_f32_4x4x4_16b_bf16 v[196:199], v[184:185], v[184:185], v[196:199]\n\t"
            "ds_write_b128 v237, v[184:187] offset:128\n\t"
            "s_waitcnt lgkmcnt(10)\n\t"
            "v_smfmac_f32_16x16x64_bf16 v[176:179], v[168:171], v[160:167], v255\n\t"
            "ds_read_b128 v[168:171], v254 offset:640\n\t"
            "v_mfma_f32_16x16x32_bf16 v[192:195], v[238:241], v[184:187], v[192:195]\n\t"
            "ds_read_b64 v[160:161], v253 offset:88\n\t"
            "ds_read_b64 v[162:163], v253 offset:184\n\t"
            "s_waitcnt lgkmcnt(8)\n\t"
            "v_smfmac_f32_16x16x64_bf16 v[180:183], v[172:175], v[144:151], v255\n\t"
            "ds_read_b64 v[164:165], v253 offset:1832\n\t"
            "v_mfma_f32_4x4x4_16b_bf16 v[196:199], v[186:187], v[186:187], v[196:199]\n\t"
            "s_setprio 1\n\t"
            "ds_read_b64 v[166:167], v253 offset:1928\n\t"
            "ds_read_b128 v[172:175], v254 offset:704\n\t"
            "v_mov_b64 v[184:185], 0\n\t"
            "v_mov_b64 v[186:187], 0\n\t"
            "v_mov_b64 v[188:189], 0\n\t"
            "v_mov_b64 v[190:191], 0\n\t"
            "v_add_u32 v248, s67, v251\n\t"
            "v_writelane_b32 v248, s58, 63\n\t"
            "v_writelane_b32 v248, s38, m0\n\t"
            "v_and_b32 v249, s69, v249\n\t"
            "v_cvt_pk_bf16_f32 v176, v176, v177\n\t"
            "v_cvt_pk_bf16_f32 v177, v178, v179\n\t"
            "v_cvt_pk_bf16_f32 v178, v180, v181\n\t"
            "v_cvt_pk_bf16_f32 v179, v182, v183\n\t"
            "s_setprio 3\n\t"
            "v_mfma_f32_4x4x4_16b_bf16 v[196:199], v[176:177], v[176:177], v[196:199]\n\t"
            "ds_write_b128 v236, v[176:179] offset:256\n\t"
            "s_waitcnt lgkmcnt(6)\n\t"
            "v_smfmac_f32_16x16x64_bf16 v[184:187], v[168:171], v[152:159], v255\n\t"
            "v_mov_b32 v234, 0\n\t"
            "v_mfma_f32_4x4x4_16b_bf16 v[196:199], v[178:179], v[178:179], v[196:199]\n\t"
            "v_xor_b32 v246, 0x100, v246\n\t"
            "v_mfma_f32_16x16x32_bf16 v[192:195], v[238:241], v[176:179], v[192:195]\n\t"
            "s_waitcnt lgkmcnt(1)\n\t"
            "v_smfmac_f32_16x16x64_bf16 v[188:191], v[172:175], v[160:167], v255\n\t"
            "s_setprio 1\n\t"
            "s_mov_b32 exec_hi, 0x3fffff\n\t"
            "s_add_u32 m0, s66, 0\n\t"
            "s_add_u32 s49, s79, 0\n\t"
            "buffer_load_dwordx4 v249, s[72:75], s49 offen lds\n\t"
            "s_mov_b32 exec_hi, -1\n\t"
            "s_mov_b32 exec_hi, 0x3fffff\n\t"
            "s_add_u32 m0, s66, 872\n\t"
            "s_add_u32 s49, s68, 196224\n\t"
            "buffer_load_dwordx4 v249, s[72:75], s49 offen lds\n\t"
            "s_mov_b32 exec_hi, -1\n\t"
            "s_mov_b32 exec_hi, 0x3fffff\n\t"
            "s_add_u32 m0, s66, 1744\n\t"
            "s_add_u32 s49, s68, 392448\n\t"
            "buffer_load_dwordx4 v249, s[72:75], s49 offen lds\n\t"
            "s_mov_b32 exec_hi, -1\n\t"
            "s_mov_b32 exec_hi, 0x3fffff\n\t"
            "s_add_u32 m0, s66, 2616\n\t"
            "s_add_u32 s49, s68, 588672\n\t"
            "buffer_load_dwordx4 v249, s[72:75], s49 offen lds\n\t"
            "s_mov_b32 exec_hi, -1\n\t"
            "s_mov_b32 exec_hi, 0x3fffff\n\t"
            "s_add_u32 m0, s66, 3488\n\t"
            "s_add_u32 s49, s68, 784896\n\t"
            "buffer_load_dwordx4 v249, s[72:75], s49 offen lds\n\t"
            "s_mov_b32 exec_hi, -1\n\t"
            "v_cvt_pk_bf16_f32 v184, v184, v185\n\t"
            "v_cvt_pk_bf16_f32 v185, v186, v187\n\t"
            "v_cvt_pk_bf16_f32 v186, v188, v189\n\t"
            "v_cvt_pk_bf16_f32 v187, v190, v191\n\t"
            "s_setprio 3\n\t"
            "v_mfma_f32_4x4x4_16b_bf16 v[196:199], v[184:185], v[184:185], v[196:199]\n\t"
            "s_mov_b32 exec_hi, 0x3fffff\n\t"
            "s_add_u32 m0, s66, 4360\n\t"
            "s_add_u32 s49, s68, 981120\n\t"
            "buffer_load_dwordx4 v249, s[72:75], s49 offen lds\n\t"
            "s_mov_b32 exec_hi, -1\n\t"
            "v_mfma_f32_4x4x4_16b_bf16 v[196:199], v[186:187], v[186:187], v[196:199]\n\t"
            "s_mov_b32 exec_hi, 0x3fffff\n\t"
            "s_add_u32 m0, s66, 5232\n\t"
            "s_add_u32 s49, s68, 1177344\n\t"
            "buffer_load_dwordx4 v249, s[72:75], s49 offen lds\n\t"
            "s_mov_b32 exec_hi, -1\n\t"
            "v_mfma_f32_16x16x32_bf16 v[192:195], v[238:241], v[184:187], v[192:195]\n\t"
            "s_setprio 1\n\t"
            "s_mov_b32 exec_hi, 0x3fffff\n\t"
            "s_add_u32 m0, s66, 6104\n\t"
            "s_add_u32 s49, s68, 1373568\n\t"
            "buffer_load_dwordx4 v249, s[72:75], s49 offen lds\n\t"
            "s_mov_b32 exec_hi, -1\n\t"
            "s_mov_b32 exec_hi, 0x3fffff\n\t"
            "s_add_u32 m0, s66, 6976\n\t"
            "s_add_u32 s49, s68, 1569792\n\t"
            "buffer_load_dwordx4 v249, s[72:75], s49 offen lds\n\t"
            "s_mov_b32 exec_hi, -1\n\t"
            "v_cndmask_b32_e64 v193, v196, v197, s[60:61]\n\t"
            "v_cndmask_b32_e64 v193, v193, v198, s[62:63]\n\t"
            "v_cndmask_b32_e64 v193, v193, v199, s[64:65]\n\t"
            "s_setprio 1\n\t"
            "ds_read_b128 v[196:199], v233 offset:6144\n\t"
            "ds_write_b64 v248, v[192:193] offset:18816\n\t"
            "ds_read_b128 v[192:195], v232 offset:6144\n\t"
            "ds_read_b64 v[248:249], v247 offset:8\n\t"
            "ds_write_b128 v237, v[184:187] offset:256\n\t"
            "ds_read_b64 v[236:237], v247\n\t"
            "s_mov_b64 exec, 1\n\t"
            "ds_inc_u32 v255, v238 offset:7860\n\t"
            "s_mov_b64 exec, -1\n\t"
            "s_waitcnt lgkmcnt(4)\n\t"
            "v_mfma_f32_16x16x32_bf16 v[144:147], v[192:195], v[0:3], 0\n\t"
            "s_waitcnt lgkmcnt(3)\n\t"
            "v_mul_f32 v202, v248, v202\n\t"
            "v_mul_f32 v203, v249, v203\n\t"
            "v_mfma_f32_16x16x32_bf16 v[144:147], v[196:199], v[4:7], v[144:147]\n\t"
            "v_mul_f32 v206, v248, v206\n\t"
            "s_waitcnt lgkmcnt(1)\n\t"
            "v_mul_f32 v200, v236, v200\n\t"
            "v_mfma_f32_16x16x32_bf16 v[148:151], v[196:199], v[28:31], 0\n\t"
            "v_mul_f32 v201, v237, v201\n\t"
            "v_mul_f32 v204, v236, v204\n\t"
            "v_mfma_f32_16x16x32_bf16 v[148:151], v[192:195], v[24:27], v[148:151]\n\t"
            "v_mul_f32 v205, v237, v205\n\t"
            "v_mul_f32 v208, v236, v208\n\t"
            "v_mfma_f32_16x16x32_bf16 v[168:171], v[192:195], v[48:51], 0\n\t"
            "v_mul_f32 v209, v237, v209\n\t"
            "v_mul_f32 v212, v236, v212\n\t"
            "v_mfma_f32_16x16x32_bf16 v[168:171], v[196:199], v[52:55], v[168:171]\n\t"
            "v_mul_f32 v213, v237, v213\n\t"
            "v_mul_f32 v216, v236, v216\n\t"
            "v_mfma_f32_16x16x32_bf16 v[172:175], v[196:199], v[76:79], 0\n\t"
            "v_mul_f32 v217, v237, v217\n\t"
            "v_mul_f32 v220, v236, v220\n\t"
            "v_mfma_f32_16x16x32_bf16 v[172:175], v[192:195], v[72:75], v[172:175]\n\t"
            "v_mul_f32 v221, v237, v221\n\t"
            "v_mul_f32 v207, v249, v207\n\t"
            "v_mfma_f32_16x16x32_bf16 v[152:155], v[192:195], v[96:99], 0\n\t"
            "ds_read_b64 v[236:237], v247 offset:64\n\t"
            "v_mul_f32 v210, v248, v210\n\t"
            "v_mfma_f32_16x16x32_bf16 v[152:155], v[196:199], v[100:103], v[152:155]\n\t"
            "v_mul_f32 v211, v249, v211\n\t"
            "v_mul_f32 v214, v248, v214\n\t"
            "v_mfma_f32_16x16x32_bf16 v[156:159], v[196:199], v[124:127], 0\n\t"
            "ds_read_b128 v[196:199], v233 offset:6400\n\t"
            "v_mul_f32 v215, v249, v215\n\t"
            "v_mfma_f32_16x16x32_bf16 v[156:159], v[192:195], v[120:123], v[156:159]\n\t"
            "ds_read_b128 v[192:195], v232 offset:6400\n\t"
            "v_mul_f32 v218, v248, v218\n\t"
            "v_mfma_f32_16x16x32_bf16 v[144:147], v[224:227], v[8:11], v[144:147]\n\t"
            "v_mul_f32 v219, v249, v219\n\t"
            "v_mul_f32 v222, v248, v222\n\t"
            "v_mfma_f32_16x16x32_bf16 v[144:147], v[228:231], v[12:15], v[144:147]\n\t"
            "v_mul_f32 v223, v249, v223\n\t"
            "v_cvt_pk_bf16_f32 v212, v212, v213\n\t"
            "v_mfma_f32_16x16x32_bf16 v[148:151], v[228:231], v[36:39], v[148:151]\n\t"
            "ds_read_b64 v[248:249], v247 offset:72\n\t"
            "v_cvt_pk_bf16_f32 v208, v208, v209\n\t"
            "v_mfma_f32_16x16x32_bf16 v[148:151], v[224:227], v[32:35], v[148:151]\n\t"
            "v_cvt_pk_bf16_f32 v213, v214, v215\n\t"
            "v_cvt_pk_bf16_f32 v209, v210, v211\n\t"
            "v_mfma_f32_16x16x32_bf16 v[168:171], v[224:227], v[56:59], v[168:171]\n\t"
            "v_cvt_pk_bf16_f32 v200, v200, v201\n\t"
            "v_cvt_pk_bf16_f32 v204, v204, v205\n\t"
            "v_mfma_f32_16x16x32_bf16 v[168:171], v[228:231], v[60:63], v[168:171]\n\t"
            "v_cvt_pk_bf16_f32 v216, v216, v217\n\t"
            "v_cvt_pk_bf16_f32 v220, v220, v221\n\t"
            "v_mfma_f32_16x16x32_bf16 v[172:175], v[228:231], v[84:87], v[172:175]\n\t"
            "v_cvt_pk_bf16_f32 v201, v202, v203\n\t"
            "v_cvt_pk_bf16_f32 v205, v206, v207\n\t"
            "v_mfma_f32_16x16x32_bf16 v[172:175], v[224:227], v[80:83], v[172:175]\n\t"
            "v_cvt_pk_bf16_f32 v217, v218, v219\n\t"
            "v_cvt_pk_bf16_f32 v221, v222, v223\n\t"
            "v_mfma_f32_16x16x32_bf16 v[152:155], v[224:227], v[104:107], v[152:155]\n\t"
            "v_mfma_f32_16x16x32_bf16 v[152:155], v[228:231], v[108:111], v[152:155]\n\t"
            "v_mfma_f32_16x16x32_bf16 v[156:159], v[228:231], v[132:135], v[156:159]\n\t"
            "ds_read_b128 v[228:231], v233 offset:12288\n\t"
            "v_mfma_f32_16x16x32_bf16 v[156:159], v[224:227], v[128:131], v[156:159]\n\t"
            "ds_read_b128 v[224:227], v232 offset:12288\n\t"
            "s_waitcnt lgkmcnt(3)\n\t"
            "v_mfma_f32_16x16x32_bf16 v[144:147], v[192:195], v[16:19], v[144:147]\n\t"
            "v_mfma_f32_16x16x32_bf16 v[144:147], v[196:199], v[20:23], v[144:147]\n\t"
            "v_mfma_f32_16x16x32_bf16 v[148:151], v[196:199], v[44:47], v[148:151]\n\t"
            "v_mfma_f32_16x16x32_bf16 v[148:151], v[192:195], v[40:43], v[148:151]\n\t"
            "v_mfma_f32_16x16x32_bf16 v[168:171], v[192:195], v[64:67], v[168:171]\n\t"
            "v_mfma_f32_16x16x32_bf16 v[168:171], v[196:199], v[68:71], v[168:171]\n\t"
            "ds_read_b64 v[234:235], v234 offset:7856\n\t"
            "v_mfma_f32_16x16x32_bf16 v[172:175], v[196:199], v[92:95], v[172:175]\n\t"
            "s_nop 1\n\t"
            "v_mul_f32 v144, v236, v144\n\t"
            "v_mfma_f32_16x16x32_bf16 v[172:175], v[192:195], v[88:91], v[172:175]\n\t"
            "v_mul_f32 v145, v237, v145\n\t"
            "s_waitcnt lgkmcnt(3)\n\t"
            "v_mul_f32 v146, v248, v146\n\t"
            "v_mfma_f32_16x16x32_bf16 v[152:155], v[192:195], v[112:115], v[152:155]\n\t"
            "v_mul_f32 v168, v236, v168\n\t"
            "v_mul_f32 v169, v237, v169\n\t"
            "v_mfma_f32_16x16x32_bf16 v[152:155], v[196:199], v[116:119], v[152:155]\n\t"
            "v_mul_f32 v170, v248, v170\n\t"
            "v_mul_f32 v171, v249, v171\n\t"
            "v_mfma_f32_16x16x32_bf16 v[156:159], v[196:199], v[140:143], v[156:159]\n\t"
            "ds_read_b128 v[196:199], v233 offset:12416\n\t"
            "v_mul_f32 v172, v236, v172\n\t"
            "v_mfma_f32_16x16x32_bf16 v[156:159], v[192:195], v[136:139], v[156:159]\n\t"
            "ds_read_b128 v[192:195], v232 offset:12416\n\t"
            "v_mul_f32 v173, v237, v173\n\t"
            "s_waitcnt lgkmcnt(3)\n\t"
            "v_mfma_f32_16x16x32_bf16 v[160:163], v[224:227], v[0:3], 0\n\t"
            "v_cvt_pk_bf16_f32 v214, v172, v173\n\t"
            "v_cvt_pk_bf16_f32 v210, v168, v169\n\t"
            "v_mfma_f32_16x16x32_bf16 v[160:163], v[228:231], v[4:7], v[160:163]\n\t"
            "s_waitcnt lgkmcnt(2)\n\t"
            "v_readfirstlane_b32 s77, v234\n\t"
            "v_readfirstlane_b32 s38, v235\n\t"
            "s_add_u32 s77, s77, s38\n\t"
            "s_cmp_ge_u32 s77, s44\n\t"
            "s_cbranch_scc0 .Lwc_spin%=\n\t"
            ".Lwc_go%=:\n\t"
            "v_mfma_f32_16x16x32_bf16 v[164:167], v[228:231], v[28:31], 0\n\t"
            "s_add_u32 s59, s59, s84\n\t"
            "s_movk_i32 s84, 800\n\t"
            "v_cvt_pk_bf16_f32 v211, v170, v171\n\t"
            "v_mfma_f32_16x16x32_bf16 v[164:167], v[224:227], v[24:27], v[164:167]\n\t"
            "s_add_u32 s45, s46, s27\n\t"
            "s_mov_b32 s46, s47\n\t"
            "s_mov_b32 s47, s68\n\t"
            "v_mfma_f32_16x16x32_bf16 v[176:179], v[224:227], v[48:51], 0\n\t"
            "v_mul_f32 v174, v248, v174\n\t"
            "v_mul_f32 v175, v249, v175\n\t"
            "v_mfma_f32_16x16x32_bf16 v[176:179], v[228:231], v[52:55], v[176:179]\n\t"
            "v_add_u32 v172, s67, v245\n\t"
            "v_cvt_pk_bf16_f32 v215, v174, v175\n\t"
            "v_mfma_f32_16x16x32_bf16 v[180:183], v[228:231], v[76:79], 0\n\t"
            "ds_read_b128 v[168:171], v172 offset:18816\n\t"
            "ds_read_b128 v[172:175], v172 offset:19840\n\t"
            "v_mfma_f32_16x16x32_bf16 v[180:183], v[224:227], v[72:75], v[180:183]\n\t"
            "v_mul_f32 v148, v236, v148\n\t"
            "v_mul_f32 v149, v237, v149\n\t"
            "v_mfma_f32_16x16x32_bf16 v[184:187], v[224:227], v[96:99], 0\n\t"
            "v_mul_f32 v152, v236, v152\n\t"
            "v_mul_f32 v153, v237, v153\n\t"
            "v_mfma_f32_16x16x32_bf16 v[184:187], v[228:231], v[100:103], v[184:187]\n\t"
            "v_mul_f32 v156, v236, v156\n\t"
            "v_mul_f32 v157, v237, v157\n\t"
            "v_mfma_f32_16x16x32_bf16 v[188:191], v[228:231], v[124:127], 0\n\t"
            "ds_read_b128 v[228:231], v233 offset:12544\n\t"
            "ds_read_b64 v[236:237], v247 offset:128\n\t"
            "v_mfma_f32_16x16x32_bf16 v[188:191], v[224:227], v[120:123], v[188:191]\n\t"
            "ds_read_b128 v[224:227], v232 offset:12544\n\t"
            "v_mul_f32 v147, v249, v147\n\t"
            "s_waitcnt lgkmcnt(5)\n\t"
            "v_mfma_f32_16x16x32_bf16 v[160:163], v[192:195], v[8:11], v[160:163]\n\t"
            "v_mul_f32 v150, v248, v150\n\t"
            "v_mul_f32 v151, v249, v151\n\t"
            "v_mfma_f32_16x16x32_bf16 v[160:163], v[196:199], v[12:15], v[160:163]\n\t"
            "v_mul_f32 v154, v248, v154\n\t"
            "v_mul_f32 v155, v249, v155\n\t"
            "v_mfma_f32_16x16x32_bf16 v[164:167], v[196:199], v[36:39], v[164:167]\n\t"
            "v_mul_f32 v158, v248, v158\n\t"
            "v_mul_f32 v159, v249, v159\n\t"
            "v_mfma_f32_16x16x32_bf16 v[164:167], v[192:195], v[32:35], v[164:167]\n\t"
            "ds_read_b64 v[248:249], v247 offset:136\n\t"
            "s_waitcnt lgkmcnt(5)\n\t"
            "v_add_f32 v168, v168, v170\n\t"
            "v_mfma_f32_16x16x32_bf16 v[176:179], v[192:195], v[56:59], v[176:179]\n\t"
            "s_waitcnt lgkmcnt(4)\n\t"
            "v_add_f32 v172, v172, v174\n\t"
            "v_add_f32 v168, v168, v172\n\t"
            "v_mfma_f32_16x16x32_bf16 v[176:179], v[196:199], v[60:63], v[176:179]\n\t"
            "v_add_f32 v169, v169, v171\n\t"
            "v_add_f32 v173, v173, v175\n\t"
            "v_mfma_f32_16x16x32_bf16 v[180:183], v[196:199], v[84:87], v[180:183]\n\t"
            "v_mul_f32 v168, 0x3baaaaab, v168\n\t"
            "v_add_f32 v169, v169, v173\n\t"
            "v_mfma_f32_16x16x32_bf16 v[180:183], v[192:195], v[80:83], v[180:183]\n\t"
            "ds_read_b128 v[172:175], v254 offset:64\n\t"
            "v_mul_f32_e64 v168, v168, -v168\n\t"
            "v_mfma_f32_16x16x32_bf16 v[184:187], v[192:195], v[104:107], v[184:187]\n\t"
            "v_fmac_f32 v168, 0x3baaaaab, v169\n\t"
            "v_max_f32 v168, 0, v168\n\t"
            "v_mfma_f32_16x16x32_bf16 v[184:187], v[196:199], v[108:111], v[184:187]\n\t"
            "v_cvt_pk_bf16_f32 v202, v144, v145\n\t"
            "v_cvt_pk_bf16_f32 v203, v146, v147\n\t"
            "v_mfma_f32_16x16x32_bf16 v[188:191], v[196:199], v[132:135], v[188:191]\n\t"
            "v_cvt_pk_bf16_f32 v206, v148, v149\n\t"
            "v_cvt_pk_bf16_f32 v207, v150, v151\n\t"
            "v_mfma_f32_16x16x32_bf16 v[188:191], v[192:195], v[128:131], v[188:191]\n\t"
            "v_cvt_pk_bf16_f32 v218, v152, v153\n\t"
            "v_cvt_pk_bf16_f32 v219, v154, v155\n\t"
            "s_waitcnt lgkmcnt(2)\n\t"
            "v_mfma_f32_16x16x32_bf16 v[160:163], v[224:227], v[16:19], v[160:163]\n\t"
            "v_cvt_pk_bf16_f32 v222, v156, v157\n\t"
            "v_cvt_pk_bf16_f32 v223, v158, v159\n\t"
            "v_mfma_f32_16x16x32_bf16 v[160:163], v[228:231], v[20:23], v[160:163]\n\t"
            "v_add_f32 v168, 0x3727c5ac, v168\n\t"
            "v_rsq_f32 v168, v168\n\t"
            "v_mfma_f32_16x16x32_bf16 v[164:167], v[228:231], v[44:47], v[164:167]\n\t"
            "v_add_u32 v232, s67, v242\n\t"
            "v_xor_b32 v233, 64, v232\n\t"
            "v_mfma_f32_16x16x32_bf16 v[164:167], v[224:227], v[40:43], v[164:167]\n\t"
            "s_xor_b32 s48, s48, s71\n\t"
            "s_xor_b32 s67, s67, s71\n\t"
            "s_add_u32 s76, s76, 4\n\t"
            "s_add_u32 s44, s44, 8\n\t"
            "v_mfma_f32_16x16x32_bf16 v[176:179], v[224:227], v[64:67], v[176:179]\n\t"
            "s_lshl_b32 s56, s56, 2\n\t"
            "s_lshr_b32 s49, s26, 30\n\t"
            "s_or_b32 s56, s56, s49\n\t"
            "v_mfma_f32_16x16x32_bf16 v[176:179], v[228:231], v[68:71], v[176:179]\n\t"
            "ds_read_b128 v[192:195], v232\n\t"
            "ds_read_b128 v[196:199], v233\n\t"
            "v_mfma_f32_16x16x32_bf16 v[180:183], v[228:231], v[92:95], v[180:183]\n\t"
            "v_mul_f32 v160, v236, v160\n\t"
            "v_mul_f32 v161, v237, v161\n\t"
            "v_mfma_f32_16x16x32_bf16 v[180:183], v[224:227], v[88:91], v[180:183]\n\t"
            "v_mul_f32 v164, v236, v164\n\t"
            "v_mul_f32 v165, v237, v165\n\t"
            "v_mfma_f32_16x16x32_bf16 v[184:187], v[224:227], v[112:115], v[184:187]\n\t"
            "v_mul_f32 v176, v236, v176\n\t"
            "v_mul_f32 v177, v237, v177\n\t"
            "v_mfma_f32_16x16x32_bf16 v[184:187], v[228:231], v[116:119], v[184:187]\n\t"
            "ds_write_b128 v243, v[200:203]\n\t"
            "v_mfma_f32_16x16x32_bf16 v[188:191], v[228:231], v[140:143], v[188:191]\n\t"
            "ds_read_b128 v[228:231], v233 offset:128\n\t"
            "v_mul_f32 v180, v236, v180\n\t"
            "v_mfma_f32_16x16x32_bf16 v[188:191], v[224:227], v[136:139], v[188:191]\n\t"
            "ds_read_b128 v[224:227], v232 offset:128\n\t"
            "v_mul_f32 v181, v237, v181\n\t"
            "s_waitcnt lgkmcnt(3)\n\t"
            "v_mfma_f32_16x16x32_bf16 v[200:203], v[192:195], v[0:3], 0\n\t"
            "v_mul_f32 v184, v236, v184\n\t"
            "v_mul_f32 v185, v237, v185\n\t"
            "v_mfma_f32_16x16x32_bf16 v[200:203], v[196:199], v[4:7], v[200:203]\n\t"
            "ds_write_b128 v243, v[204:207] offset:1536\n\t"
            "v_mfma_f32_16x16x32_bf16 v[204:207], v[196:199], v[28:31], 0\n\t"
            "v_mul_f32 v188, v236, v188\n\t"
            "v_mul_f32 v189, v237, v189\n\t"
            "v_mfma_f32_16x16x32_bf16 v[204:207], v[192:195], v[24:27], v[204:207]\n\t"
            "ds_write_b128 v243, v[208:211] offset:3072\n\t"
            "v_mfma_f32_16x16x32_bf16 v[208:211], v[192:195], v[48:51], 0\n\t"
            "v_mul_f32 v162, v248, v162\n\t"
            "v_mul_f32 v163, v249, v163\n\t"
            "v_mfma_f32_16x16x32_bf16 v[208:211], v[196:199], v[52:55], v[208:211]\n\t"
            "ds_write_b128 v243, v[212:215] offset:4608\n\t"
            "v_mfma_f32_16x16x32_bf16 v[212:215], v[196:199], v[76:79], 0\n\t"
            "v_cvt_pk_bf16_f32 v160, v160, v161\n\t"
            "v_mul_f32 v166, v248, v166\n\t"
            "v_mfma_f32_16x16x32_bf16 v[212:215], v[192:195], v[72:75], v[212:215]\n\t"
            "ds_write_b128 v243, v[216:219] offset:6144\n\t"
            "v_mfma_f32_16x16x32_bf16 v[216:219], v[192:195], v[96:99], 0\n\t"
            "v_mul_f32 v167, v249, v167\n\t"
            "v_cvt_pk_bf16_f32 v164, v164, v165\n\t"
            "v_mfma_f32_16x16x32_bf16 v[216:219], v[196:199], v[100:103], v[216:219]\n\t"
            "ds_write_b128 v243, v[220:223] offset:7680\n\t"
            "v_mfma_f32_16x16x32_bf16 v[220:223], v[196:199], v[124:127], 0\n\t"
            "v_mul_f32 v178, v248, v178\n\t"
            "v_mul_f32 v179, v249, v179\n\t"
            "v_mfma_f32_16x16x32_bf16 v[220:223], v[192:195], v[120:123], v[220:223]\n\t"
            "v_cvt_pk_bf16_f32 v176, v176, v177\n\t"
            "v_mul_f32 v182, v248, v182\n\t"
            "s_waitcnt lgkmcnt(5)\n\t"
            "v_mfma_f32_16x16x32_bf16 v[200:203], v[224:227], v[8:11], v[200:203]\n\t"
            "s_waitcnt vmcnt(0)\n\t"
            "ds_read_b64 v[144:145], v253\n\t"
            "ds_read_b64 v[146:147], v253 offset:96\n\t"
            "v_mfma_f32_16x16x32_bf16 v[200:203], v[228:231], v[12:15], v[200:203]\n\t"
            "ds_read_b64 v[148:149], v253 offset:1744\n\t"
            "ds_read_b64 v[150:151], v253 offset:1840\n\t"
            "v_mfma_f32_16x16x32_bf16 v[204:207], v[228:231], v[36:39], v[204:207]\n\t"
            "ds_read_b64 v[152:153], v253 offset:8\n\t"
            "ds_read_b64 v[154:155], v253 offset:104\n\t"
            "v_mfma_f32_16x16x32_bf16 v[204:207], v[224:227], v[32:35], v[204:207]\n\t"
            "ds_read_b64 v[156:157], v253 offset:1752\n\t"
            "ds_read_b64 v[158:159], v253 offset:1848\n\t"
            "v_mfma_f32_16x16x32_bf16 v[208:211], v[224:227], v[56:59], v[208:211]\n\t"
            "v_mul_f32 v183, v249, v183\n\t"
            "v_cvt_pk_bf16_f32 v180, v180, v181\n\t"
            "v_mfma_f32_16x16x32_bf16 v[208:211], v[228:231], v[60:63], v[208:211]\n\t"
            "v_mul_f32 v186, v248, v186\n\t"
            "v_mul_f32 v187, v249, v187\n\t"
            "v_mfma_f32_16x16x32_bf16 v[212:215], v[228:231], v[84:87], v[212:215]\n\t"
            "v_cvt_pk_bf16_f32 v184, v184, v185\n\t"
            "v_mul_f32 v190, v248, v190\n\t"
            "v_mfma_f32_16x16x32_bf16 v[212:215], v[224:227], v[80:83], v[212:215]\n\t"
            "v_mul_f32 v191, v249, v191\n\t"
            "v_cvt_pk_bf16_f32 v188, v188, v189\n\t"
            "v_mfma_f32_16x16x32_bf16 v[216:219], v[224:227], v[104:107], v[216:219]\n\t"
            "v_cvt_pk_bf16_f32 v161, v162, v163\n\t"
            "v_cvt_pk_bf16_f32 v165, v166, v167\n\t"
            "v_mfma_f32_16x16x32_bf16 v[216:219], v[228:231], v[108:111], v[216:219]\n\t"
            "ds_read_b64 v[162:163], v253 offset:112\n\t"
            "ds_read_b64 v[166:167], v253 offset:1856\n\t"
            "v_mfma_f32_16x16x32_bf16 v[220:223], v[228:231], v[132:135], v[220:223]\n\t"
            "s_waitcnt lgkmcnt(14)\n\t"
            "ds_read_b128 v[228:231], v233 offset:256\n\t"
            "v_cvt_pk_bf16_f32 v177, v178, v179\n\t"
            "v_mfma_f32_16x16x32_bf16 v[220:223], v[224:227], v[128:131], v[220:223]\n\t"
            "s_waitcnt lgkmcnt(14)\n\t"
            "ds_read_b128 v[224:227], v232 offset:256\n\t"
            "v_cvt_pk_bf16_f32 v181, v182, v183\n\t"
            "v_cvt_pk_bf16_f32 v185, v186, v187\n\t"
            "v_cvt_pk_bf16_f32 v189, v190, v191\n\t"
            "v_xor_b32 v247, 0x100, v247\n\t"
            "s_waitcnt lgkmcnt(14)\n\t"
            "ds_write_b64 v244, v[160:161]\n\t"
            "s_waitcnt lgkmcnt(14)\n\t"
            "ds_read_b64 v[160:161], v253 offset:16\n\t"
            "s_waitcnt lgkmcnt(14)\n\t"
            "ds_write_b64 v244, v[164:165] offset:1536\n\t"
            "s_waitcnt lgkmcnt(14)\n\t"
            "ds_read_b64 v[164:165], v253 offset:1760\n\t"
            "s_waitcnt lgkmcnt(14)\n\t"
            "ds_write_b64 v244, v[176:177] offset:3072\n\t"
            "s_waitcnt lgkmcnt(14)\n\t"
            "ds_write_b64 v244, v[180:181] offset:4608\n\t"
            "s_waitcnt lgkmcnt(14)\n\t"
            "ds_write_b64 v244, v[184:185] offset:6144\n\t"
            "s_waitcnt lgkmcnt(14)\n\t"
            "ds_write_b64 v244, v[188:189] offset:7680\n\t"
            "s_waitcnt lgkmcnt(14)\n\t"
            "ds_write_b32 v246, v168\n\t"
            "s_waitcnt lgkmcnt(14)\n\t"
            "ds_read_b128 v[168:171], v254\n\t"
            "s_waitcnt lgkmcnt(1)\n\t"
            "s_barrier\n\t"
            "s_cmp_lt_u32 s46, 0x7000000\n\t"
            "s_cbranch_scc1 .Lwc_loop%=\n\t"
            "s_branch .Lwc_end%=\n\t"
            ".Lwc_spin%=:\n\t"
            "s_cmp_eq_u32 s83, 0\n\t"
            "s_cbranch_scc1 .Lwc_go%=\n\t"
            "s_sub_u32 s83, s83, 1\n\t"
            "s_sleep 2\n\t"
            "v_mov_b32 v234, 0\n\t"
            "ds_read_b64 v[234:235], v234 offset:7856\n\t"
            "s_waitcnt lgkmcnt(0)\n\t"
            "v_readfirstlane_b32 s77, v234\n\t"
            "v_readfirstlane_b32 s38, v235\n\t"
            "s_add_u32 s77, s77, s38\n\t"
            "s_cmp_ge_u32 s77, s44\n\t"
            "s_cbranch_scc0 .Lwc_spin%=\n\t"
            "s_branch .Lwc_go%=\n\t"
            ".Lwc_end%=:\n\t"
            "s_waitcnt lgkmcnt(0)\n\t"
            "s_nop 7\n\t"
            : "+{v246}"(aRSw_), "+{v247}"(aRSr_), "+{s44}"(s44), B_SIO
            : "{v242}"(pE_), "{v243}"(aVAw_), "{v244}"(aVBw_), "{v245}"(pST_), "{v250}"(pXw_), "{v251}"(pSTw_), "{v252}"(aCA_), "{v253}"(aB_), "{v254}"(aA_), "{v255}"(idx_), B_SIN, "{s82}"(touch)
            : "v144", "v145", "v146", "v147", "v148", "v149", "v150", "v151", "v152", "v153", "v154", "v155", "v156", "v157", "v158", "v159", "v160", "v161", "v162", "v163", "v164", "v165", "v166", "v167", "v168", "v169", "v170", "v171", "v172", "v173", "v174", "v175", "v176", "v177", "v178", "v179", "v180", "v181", "v182", "v183", "v184", "v185", "v186", "v187", "v188", "v189", "v190", "v191", "v192", "v193", "v194", "v195", "v196", "v197", "v198", "v199", "v236", "v237", "v248", "v249", "v0", "v1", "v2", "v3", "v4", "v5", "v6", "v7", "v8", "v9", "v10", "v11", "v12", "v13", "v14", "v15", "v16", "v17", "v18", "v19", "v20", "v21", "v22", "v23", "v24", "v25", "v26", "v27", "v28", "v29", "v30", "v31", "v32", "v33", "v34", "v35", "v36", "v37", "v38", "v39", "v40", "v41", "v42", "v43", "v44", "v45", "v46", "v47", "v48", "v49", "v50", "v51", "v52", "v53", "v54", "v55", "v56", "v57", "v58", "v59", "v60", "v61", "v62", "v63", "v64", "v65", "v66", "v67", "v68", "v69", "v70", "v71", "v72", "v73", "v74", "v75", "v76", "v77", "v78", "v79", "v80", "v81", "v82", "v83", "v84", "v85", "v86", "v87", "v88", "v89", "v90", "v91", "v92", "v93", "v94", "v95", "v96", "v97", "v98", "v99", "v100", "v101", "v102", "v103", "v104", "v105", "v106", "v107", "v108", "v109", "v110", "v111", "v112", "v113", "v114", "v115", "v116", "v117", "v118", "v119", "v120", "v121", "v122", "v123", "v124", "v125", "v126", "v127", "v128", "v129", "v130", "v131", "v132", "v133", "v134", "v135", "v136", "v137", "v138", "v139", "v140", "v141", "v142", "v143", "v200", "v201", "v202", "v204", "v205", "v206", "v207", "v208", "v209", "v210", "v211", "v212", "v213", "v214", "v215", "v216", "v217", "v218", "v219", "v220", "v221", "v222", "v223", "v224", "v225", "v226", "v227", "v228", "v229", "v230", "v231", "v232", "v233", "v234", "v235", B_SCLOB);
        }
        {
            __builtin_amdgcn_s_setprio(3);
            int ln_;
            asm volatile("v_mbcnt_lo_u32_b32 %0, -1, 0\n\tv_mbcnt_hi_u32_b32 %0, -1, %0" : "=v"(ln_));
            const int l15_ = ln_ & 15, g_ = ln_ >> 4;
            KARGS
            const auto orsrc = __builtin_amdgcn_make_buffer_rsrc((void*)ob, 0, IMG * IMG * CH * 2, 0x00020000);
            const auto wq = __builtin_amdgcn_make_buffer_rsrc((void*)wb, 0, WS_BYTES, 0x00020000);
            bf16x8 W[36];
#pragma unroll
            for (int ks = 0; ks < 6; ++ks)
#pragma unroll
                for (int j = 0; j < 6; ++j) W[j * 6 + ks] = BC8(__builtin_amdgcn_raw_buffer_load_b128(wq, ln_ * 16, (wave + 2) * 36864 + (j * 6 + ks) * 1024, SC1));
            SB();
            int lE[2], nw;
            unsigned o[2];
            f32x4 acc[2][6];
#pragma unroll
            for (int lt = 0; lt < 2; ++lt) {
                int i = 16 * lt + l15_;
                const unsigned en = *(const unsigned*)(smem + WL_OFF + i * 4);
                const bool ok = en != 0x7ffffffu;
                if (lt == 1) nw = 16 + __builtin_popcount((unsigned)__builtin_amdgcn_ballot_w64(ok) & 0xffffu);
                i = ok ? i : 0;
                lE[lt] = OL_OFF + i * 384 + ((g_ ^ ((i >> 1) & 7)) << 4);
                o[lt] = ok ? (en & 0x7ffffffu) + LPOS(en >> 31) * ROWBYTES + LPOS(en >> 30) * (CH * 2) + 192 * w + 16 * g_ : OOB;
#pragma unroll
                for (int k = 0; k < 3; ++k) {
                    const int c8 = 12 * w + 4 * k + g_, ws = (c8 * 43) >> 8, r6 = c8 - 6 * ws;
                    const u32x4 r = *(const u32x4*)(smem + LX_OFF(i) + (r6 >> 1) * 128 + ((2 * ws + (r6 & 1)) << 4));
                    acc[lt][2 * k] = (f32x4){bflo(r[0]), bfhi(r[0]), bflo(r[1]), bfhi(r[1])};
                    acc[lt][2 * k + 1] = (f32x4){bflo(r[2]), bfhi(r[2]), bflo(r[3]), bfhi(r[3])};
                }
            }
            SB();
            for (int it_ = 0; it_ < 100000; ++it_) {
                unsigned n_;
                asm volatile("ds_read_b32 %0, %1\n\ts_waitcnt lgkmcnt(0)" : "=v"(n_) : "v"(ldsb + FLAG_OFF) : "memory");
                if (__builtin_amdgcn_readfirstlane(n_) >= 4 * nw) break;
                __builtin_amdgcn_s_sleep(1);
            }
            bf16x8 of[2][6];
#pragma unroll
            for (int ks = 0; ks < 6; ++ks)
#pragma unroll
                for (int lt = 0; lt < 2; ++lt) of[lt][ks] = *(const bf16x8*)(smem + ((ks & 1) ? (lE[lt] ^ 64) : lE[lt]) + (ks >> 1) * 128);
            SB();
#pragma unroll
            for (int lt = 0; lt < 2; ++lt) {
#pragma unroll
                for (int ks = 0; ks < 6; ++ks)
#pragma unroll
                    for (int j = 0; j < 6; ++j) acc[lt][j] = MFMA32(W[j * 6 + ks], of[lt][ks], acc[lt][j]);
#pragma unroll
                for (int k = 0; k < 3; ++k) {
                    const f32x4 a = acc[lt][2 * k], b = acc[lt][2 * k + 1];
                    const u32x4 ov = {pk2(a[0], a[1]), pk2(a[2], a[3]), pk2(b[0], b[1]), pk2(b[2], b[3])};
                    __builtin_amdgcn_raw_buffer_store_b128(ov, orsrc, o[lt] + 64 * k, 0, SC1);
                }
            }
        }
        __builtin_amdgcn_endpgm();
    } else {
        LANE_SETUP
        {
            const unsigned oE_ = ldsb + fE + (O_OFF - X_OFF), oO_ = oE_ ^ 64;
            unsigned l16_ = lane * 16;
            unsigned pr[3];
#pragma unroll
            for (int k = 0; k < 3; ++k) {
                const int c8 = 12 * (w - 2) + 4 * k + g;
                const int ws = c8 / 6, r6 = c8 - 6 * ws;
                pr[k] = l15 * 384 + (r6 >> 1) * 128 + (((2 * ws + (r6 & 1)) ^ sw) << 4);
            }
            const unsigned pR0_ = pr[0], pR1_ = pr[1], pR2_ = pr[2];
            unsigned q0 = ldsb + PF_OFF + 1024 + lane * 4, q1 = ldsb + O_OFF + lane * 16, q2 = oE_, q3 = ldsb + PF_OFF + 1024 + l15 * 4;
            unsigned q4 = ldsb + OL_OFF + l15 * 384 + (((4 * (w - 2) + (g >> 1)) ^ sw) << 4) + 8 * (g & 1);
            B_SCALARS
            asm volatile(
            "s_waitcnt lgkmcnt(0)\n\t"
            "s_mov_b32 s83, 0x10000\n\t"
            "s_mov_b32 s84, -47104\n\t"
            "s_nop 7\n\t"
            "v_mov_b32 v200, v250\n\t"
            "v_mov_b32 v201, v251\n\t"
            "v_mov_b32 v202, v253\n\t"
            "s_mov_b32 s38, s67\n\t"
            "s_mov_b32 s67, 74496\n\t"
            "ds_read_b64 v[250:251], v203\n\t"
            "ds_read_b32 v253, v203 offset:8\n\t"
            ".Lwk_b1%=:\n\t"
            "v_mov_b32 v236, 0\n\t"
            "ds_read_b32 v236, v236 offset:7864\n\t"
            "s_waitcnt lgkmcnt(0)\n\t"
            "v_readfirstlane_b32 s77, v236\n\t"
            "s_cmp_ge_u32 s77, 4\n\t"
            "s_cbranch_scc1 .Lwk_b2%=\n\t"
            "s_cmp_eq_u32 s83, 0\n\t"
            "s_cbranch_scc1 .Lwk_b2%=\n\t"
            "s_sub_u32 s83, s83, 1\n\t"
            "s_sleep 1\n\t"
            "s_branch .Lwk_b1%=\n\t"
            ".Lwk_b2%=:\n\t"
            "s_waitcnt vmcnt(0)\n\t"
            "s_call_b64 vcc, wk_conv\n\t"
            "v_mov_b32 v250, v200\n\t"
            "v_mov_b32 v251, v201\n\t"
            "v_mov_b32 v253, v202\n\t"
            "s_mov_b32 s67, s38\n\t"
            "s_barrier\n\t"
            "ds_read_b64 v[236:237], v252 offset:2560\n\t"
            "s_waitcnt lgkmcnt(0)\n\t"
            "s_mov_b64 exec, 1\n\t"
            "buffer_store_dwordx2 v[236:237], off, s[28:31], s44 sc1\n\t"
            "s_mov_b64 exec, -1\n\t"
            "s_sleep 30\n\t"
            "ds_read_b32 v249, v252 offset:12\n\t"
            "s_waitcnt lgkmcnt(0)\n\t"
            "v_and_b32 v249, s69, v249\n\t"
            "s_mov_b32 exec_hi, 0x3fffff\n\t"
            "s_add_u32 m0, s66, 0\n\t"
            "s_add_u32 s49, s79, 0\n\t"
            "buffer_load_dwordx4 v249, s[72:75], s49 offen lds\n\t"
            "s_mov_b32 exec_hi, -1\n\t"
            "s_mov_b32 exec_hi, 0x3fffff\n\t"
            "s_add_u32 m0, s66, 872\n\t"
            "s_add_u32 s49, s47, 196224\n\t"
            "buffer_load_dwordx4 v249, s[72:75], s49 offen lds\n\t"
            "s_mov_b32 exec_hi, -1\n\t"
            "s_mov_b32 exec_hi, 0x3fffff\n\t"
            "s_add_u32 m0, s66, 1744\n\t"
            "s_add_u32 s49, s47, 392448\n\t"
            "buffer_load_dwordx4 v249, s[72:75], s49 offen lds\n\t"
            "s_mov_b32 exec_hi, -1\n\t"
            "s_mov_b32 exec_hi, 0x3fffff\n\t"
            "s_add_u32 m0, s66, 2616\n\t"
            "s_add_u32 s49, s47, 588672\n\t"
            "buffer_load_dwordx4 v249, s[72:75], s49 offen lds\n\t"
            "s_mov_b32 exec_hi, -1\n\t"
            "s_mov_b32 exec_hi, 0x3fffff\n\t"
            "s_add_u32 m0, s66, 3488\n\t"
            "s_add_u32 s49, s47, 784896\n\t"
            "buffer_load_dwordx4 v249, s[72:75], s49 offen lds\n\t"
            "s_mov_b32 exec_hi, -1\n\t"
            "s_mov_b32 exec_hi, 0x3fffff\n\t"
            "s_add_u32 m0, s66, 4360\n\t"
            "s_add_u32 s49, s47, 981120\n\t"
            "buffer_load_dwordx4 v249, s[72:75], s49 offen lds\n\t"
            "s_mov_b32 exec_hi, -1\n\t"
            "s_mov_b32 exec_hi, 0x3fffff\n\t"
            "s_add_u32 m0, s66, 5232\n\t"
            "s_add_u32 s49, s47, 1177344\n\t"
            "buffer_load_dwordx4 v249, s[72:75], s49 offen lds\n\t"
            "s_mov_b32 exec_hi, -1\n\t"
            "s_mov_b32 exec_hi, 0x3fffff\n\t"
            "s_add_u32 m0, s66, 6104\n\t"
            "s_add_u32 s49, s47, 1373568\n\t"
            "buffer_load_dwordx4 v249, s[72:75], s49 offen lds\n\t"
            "s_mov_b32 exec_hi, -1\n\t"
            "s_mov_b32 exec_hi, 0x3fffff\n\t"
            "s_add_u32 m0, s66, 6976\n\t"
            "s_add_u32 s49, s47, 1569792\n\t"
            "buffer_load_dwordx4 v249, s[72:75], s49 offen lds\n\t"
            "s_mov_b32 exec_hi, -1\n\t"
            "s_add_u32 s49, s33, -73728\n\t"
            "buffer_load_dwordx4 v[0:3], v247, s[28:31], s49 offen sc1\n\t"
            "s_add_u32 s49, s33, -67584\n\t"
            "buffer_load_dwordx4 v[24:27], v247, s[28:31], s49 offen sc1\n\t"
            "s_add_u32 s49, s33, -61440\n\t"
            "buffer_load_dwordx4 v[48:51], v247, s[28:31], s49 offen sc1\n\t"
            "s_add_u32 s49, s33, -55296\n\t"
            "buffer_load_dwordx4 v[72:75], v247, s[28:31], s49 offen sc1\n\t"
            "s_add_u32 s49, s33, -49152\n\t"
            "buffer_load_dwordx4 v[96:99], v247, s[28:31], s49 offen sc1\n\t"
            "s_add_u32 s49, s33, -43008\n\t"
            "buffer_load_dwordx4 v[120:123], v247, s[28:31], s49 offen sc1\n\t"
            "s_add_u32 s49, s33, -72704\n\t"
            "buffer_load_dwordx4 v[4:7], v247, s[28:31], s49 offen sc1\n\t"
            "s_add_u32 s49, s33, -66560\n\t"
            "buffer_load_dwordx4 v[28:31], v247, s[28:31], s49 offen sc1\n\t"
            "s_add_u32 s49, s33, -60416\n\t"
            "buffer_load_dwordx4 v[52:55], v247, s[28:31], s49 offen sc1\n\t"
            "s_add_u32 s49, s33, -54272\n\t"
            "buffer_load_dwordx4 v[76:79], v247, s[28:31], s49 offen sc1\n\t"
            "s_add_u32 s49, s33, -48128\n\t"
            "buffer_load_dwordx4 v[100:103], v247, s[28:31], s49 offen sc1\n\t"
            "s_add_u32 s49, s33, -41984\n\t"
            "buffer_load_dwordx4 v[124:127], v247, s[28:31], s49 offen sc1\n\t"
            "s_add_u32 s49, s33, -71680\n\t"
            "buffer_load_dwordx4 v[8:11], v247, s[28:31], s49 offen sc1\n\t"
            "s_add_u32 s49, s33, -65536\n\t"
            "buffer_load_dwordx4 v[32:35], v247, s[28:31], s49 offen sc1\n\t"
            "s_add_u32 s49, s33, -59392\n\t"
            "buffer_load_dwordx4 v[56:59], v247, s[28:31], s49 offen sc1\n\t"
            "s_add_u32 s49, s33, -53248\n\t"
            "buffer_load_dwordx4 v[80:83], v247, s[28:31], s49 offen sc1\n\t"
            "s_add_u32 s49, s33, -47104\n\t"
            "buffer_load_dwordx4 v[104:107], v247, s[28:31], s49 offen sc1\n\t"
            "s_add_u32 s49, s33, -40960\n\t"
            "buffer_load_dwordx4 v[128:131], v247, s[28:31], s49 offen sc1\n\t"
            "s_add_u32 s49, s33, -70656\n\t"
            "buffer_load_dwordx4 v[12:15], v247, s[28:31], s49 offen sc1\n\t"
            "s_add_u32 s49, s33, -64512\n\t"
            "buffer_load_dwordx4 v[36:39], v247, s[28:31], s49 offen sc1\n\t"
            "s_add_u32 s49, s33, -58368\n\t"
            "buffer_load_dwordx4 v[60:63], v247, s[28:31], s49 offen sc1\n\t"
            "s_add_u32 s49, s33, -52224\n\t"
            "buffer_load_dwordx4 v[84:87], v247, s[28:31], s49 offen sc1\n\t"
            "s_add_u32 s49, s33, -46080\n\t"
            "buffer_load_dwordx4 v[108:111], v247, s[28:31], s49 offen sc1\n\t"
            "s_add_u32 s49, s33, -39936\n\t"
            "buffer_load_dwordx4 v[132:135], v247, s[28:31], s49 offen sc1\n\t"
            "s_add_u32 s49, s33, -69632\n\t"
            "buffer_load_dwordx4 v[16:19], v247, s[28:31], s49 offen sc1\n\t"
            "s_add_u32 s49, s33, -63488\n\t"
            "buffer_load_dwordx4 v[40:43], v247, s[28:31], s49 offen sc1\n\t"
            "s_add_u32 s49, s33, -57344\n\t"
            "buffer_load_dwordx4 v[64:67], v247, s[28:31], s49 offen sc1\n\t"
            "s_add_u32 s49, s33, -51200\n\t"
            "buffer_load_dwordx4 v[88:91], v247, s[28:31], s49 offen sc1\n\t"
            "s_add_u32 s49, s33, -45056\n\t"
            "buffer_load_dwordx4 v[112:115], v247, s[28:31], s49 offen sc1\n\t"
            "s_add_u32 s49, s33, -38912\n\t"
            "buffer_load_dwordx4 v[136:139], v247, s[28:31], s49 offen sc1\n\t"
            "s_add_u32 s49, s33, -68608\n\t"
            "buffer_load_dwordx4 v[20:23], v247, s[28:31], s49 offen sc1\n\t"
            "s_add_u32 s49, s33, -62464\n\t"
            "buffer_load_dwordx4 v[44:47], v247, s[28:31], s49 offen sc1\n\t"
            "s_add_u32 s49, s33, -56320\n\t"
            "buffer_load_dwordx4 v[68:71], v247, s[28:31], s49 offen sc1\n\t"
            "s_add_u32 s49, s33, -50176\n\t"
            "buffer_load_dwordx4 v[92:95], v247, s[28:31], s49 offen sc1\n\t"
            "s_add_u32 s49, s33, -44032\n\t"
            "buffer_load_dwordx4 v[116:119], v247, s[28:31], s49 offen sc1\n\t"
            "s_add_u32 s49, s33, -37888\n\t"
            "buffer_load_dwordx4 v[140:143], v247, s[28:31], s49 offen sc1\n\t"
            "s_setprio 3\n\t"
            "ds_read_b128 v[168:171], v254\n\t"
            "ds_read_b128 v[172:175], v254 offset:64\n\t"
            "ds_read_b128 v[220:223], v229 offset:12288\n\t"
            "ds_read_b128 v[224:227], v229 offset:13312\n\t"
            "ds_read_b128 v[200:203], v232\n\t"
            "ds_read_b128 v[208:211], v232 offset:128\n\t"
            "ds_read_b128 v[216:219], v232 offset:256\n\t"
            "s_waitcnt vmcnt(36)\n\t"
            "ds_read_b64 v[144:145], v253\n\t"
            "ds_read_b64 v[146:147], v253 offset:96\n\t"
            "ds_read_b64 v[148:149], v253 offset:1744\n\t"
            "ds_read_b64 v[150:151], v253 offset:1840\n\t"
            "ds_read_b64 v[152:153], v253 offset:8\n\t"
            "ds_read_b64 v[154:155], v253 offset:104\n\t"
            "ds_read_b64 v[156:157], v253 offset:1752\n\t"
            "ds_read_b64 v[158:159], v253 offset:1848\n\t"
            "s_waitcnt lgkmcnt(14)\n\t"
            "ds_read_b64 v[160:161], v253 offset:16\n\t"
            "s_waitcnt lgkmcnt(14)\n\t"
            "ds_read_b64 v[162:163], v253 offset:112\n\t"
            "s_waitcnt lgkmcnt(14)\n\t"
            "ds_read_b64 v[164:165], v253 offset:1760\n\t"
            "s_waitcnt lgkmcnt(14)\n\t"
            "ds_read_b64 v[166:167], v253 offset:1856\n\t"
            "v_mov_b64 v[176:177], 0\n\t"
            "v_mov_b64 v[178:179], 0\n\t"
            "v_mov_b64 v[180:181], 0\n\t"
            "v_mov_b64 v[182:183], 0\n\t"
            "v_mov_b32 v249, s76\n\t"
            "v_mov_b64 v[184:185], 0\n\t"
            "v_mov_b64 v[186:187], 0\n\t"
            "v_mov_b64 v[188:189], 0\n\t"
            "v_mov_b64 v[190:191], 0\n\t"
            "v_add_u32 v236, s67, v250\n\t"
            "v_add_u32 v248, s67, v251\n\t"
            "v_xor_b32 v235, 64, v232\n\t"
            "s_nop 0\n\t"
            "s_waitcnt lgkmcnt(14)\n\t"
            "ds_read_b32 v249, v249\n\t"
            "s_waitcnt lgkmcnt(14)\n\t"
            "ds_read_b128 v[204:207], v235\n\t"
            "s_waitcnt lgkmcnt(14)\n\t"
            "ds_read_b128 v[212:215], v235 offset:128\n\t"
            "v_add_f32 v220, v220, v222\n\t"
            "v_add_f32 v224, v224, v226\n\t"
            "v_add_f32 v220, v220, v224\n\t"
            "v_add_f32 v221, v221, v223\n\t"
            "v_add_f32 v225, v225, v227\n\t"
            "v_mul_f32 v220, 0x3baaaaab, v220\n\t"
            "v_add_f32 v221, v221, v225\n\t"
            "v_mul_f32_e64 v220, v220, -v220\n\t"
            "v_fmac_f32 v220, 0x3baaaaab, v221\n\t"
            "s_waitcnt lgkmcnt(11)\n\t"
            "v_smfmac_f32_16x16x64_bf16 v[176:179], v[168:171], v[144:151], v255\n\t"
            "ds_read_b128 v[168:171], v254 offset:128\n\t"
            "s_waitcnt lgkmcnt(8)\n\t"
            "v_smfmac_f32_16x16x64_bf16 v[180:183], v[172:175], v[152:159], v255\n\t"
            "ds_read_b64 v[144:145], v253 offset:24\n\t"
            "ds_read_b64 v[146:147], v253 offset:120\n\t"
            "ds_read_b64 v[148:149], v253 offset:1768\n\t"
            "ds_read_b64 v[150:151], v253 offset:1864\n\t"
            "ds_read_b128 v[172:175], v254 offset:192\n\t"
            "ds_read_b64 v[152:153], v253 offset:32\n\t"
            "ds_read_b64 v[154:155], v253 offset:128\n\t"
            "s_waitcnt lgkmcnt(14)\n\t"
            "ds_read_b64 v[156:157], v253 offset:1776\n\t"
            "s_waitcnt lgkmcnt(14)\n\t"
            "ds_read_b64 v[158:159], v253 offset:1872\n\t"
            "s_waitcnt lgkmcnt(14)\n\t"
            "ds_read_b128 v[224:227], v232 offset:6144\n\t"
            "v_cvt_pk_bf16_f32 v176, v176, v177\n\t"
            "v_cvt_pk_bf16_f32 v177, v178, v179\n\t"
            "v_cvt_pk_bf16_f32 v178, v180, v181\n\t"
            "v_cvt_pk_bf16_f32 v179, v182, v183\n\t"
            "v_mfma_f32_4x4x4_16b_bf16 v[196:199], v[176:177], v[176:177], 0\n\t"
            "v_mov_b64 v[180:181], 0\n\t"
            "v_mfma_f32_16x16x32_bf16 v[192:195], v[238:241], v[176:179], 0\n\t"
            "s_waitcnt lgkmcnt(13)\n\t"
            "v_readfirstlane_b32 s26, v249\n\t"
            "v_mov_b64 v[182:183], 0\n\t"
            "v_mfma_f32_4x4x4_16b_bf16 v[196:199], v[178:179], v[178:179], v[196:199]\n\t"
            "ds_read_b32 v249, v252 offset:12\n\t"
            "s_and_b32 s68, s26, 0x7ffffff\n\t"
            "s_add_u32 s68, s68, s78\n\t"
            "s_and_b32 s69, s26, 0x38000000\n\t"
            "s_or_b32 s69, s69, s39\n\t"
            "s_bitcmp1_b32 s26, 27\n\t"
            "s_cselect_b32 s79, 2.0, s68\n\t"
            "s_and_b32 s57, s56, 3\n\t"
            "s_lshl_b32 s58, s57, 3\n\t"
            "s_lshr_b32 m0, s54, s58\n\t"
            "s_lshl_b32 s58, s57, 4\n\t"
            "s_lshr_b64 s[36:37], s[50:51], s58\n\t"
            "s_and_b32 s57, s36, 0xffff\n\t"
            "s_add_u32 s57, s57, s67\n\t"
            "v_writelane_b32 v236, s57, 63\n\t"
            "v_writelane_b32 v236, s59, m0\n\t"
            "v_xor_b32 v237, 16, v236\n\t"
            "s_lshr_b64 s[36:37], s[52:53], s58\n\t"
            "s_and_b32 s58, s36, 0xffff\n\t"
            "s_add_u32 s58, s58, s67\n\t"
            "s_waitcnt lgkmcnt(11)\n\t"
            "v_smfmac_f32_16x16x64_bf16 v[184:187], v[168:171], v[160:167], v255\n\t"
            "s_and_b32 s38, s52, 0xffff\n\t"
            "s_add_u32 s38, s38, s67\n\t"
            "ds_read_b128 v[168:171], v254 offset:256\n\t"
            "ds_read_b64 v[160:161], v253 offset:40\n\t"
            "ds_read_b64 v[162:163], v253 offset:136\n\t"
            "ds_read_b64 v[164:165], v253 offset:1784\n\t"
            "s_waitcnt lgkmcnt(14)\n\t"
            "ds_read_b64 v[166:167], v253 offset:1880\n\t"
            "s_waitcnt lgkmcnt(11)\n\t"
            "v_smfmac_f32_16x16x64_bf16 v[188:191], v[172:175], v[144:151], v255\n\t"
            "ds_read_b128 v[172:175], v254 offset:320\n\t"
            "ds_read_b64 v[144:145], v253 offset:48\n\t"
            "ds_read_b64 v[146:147], v253 offset:144\n\t"
            "ds_read_b64 v[148:149], v253 offset:1792\n\t"
            "s_waitcnt lgkmcnt(14)\n\t"
            "ds_read_b64 v[150:151], v253 offset:1888\n\t"
            "s_waitcnt lgkmcnt(14)\n\t"
            "ds_write_b128 v236, v[176:179]\n\t"
            "v_mov_b64 v[176:177], 0\n\t"
            "v_mov_b64 v[178:179], 0\n\t"
            "v_cvt_pk_bf16_f32 v184, v184, v185\n\t"
            "v_cvt_pk_bf16_f32 v185, v186, v187\n\t"
            "v_cvt_pk_bf16_f32 v186, v188, v189\n\t"
            "v_cvt_pk_bf16_f32 v187, v190, v191\n\t"
            "v_mfma_f32_4x4x4_16b_bf16 v[196:199], v[184:185], v[184:185], v[196:199]\n\t"
            "s_waitcnt lgkmcnt(14)\n\t"
            "ds_write_b128 v237, v[184:187]\n\t"
            "v_mfma_f32_16x16x32_bf16 v[192:195], v[238:241], v[184:187], v[192:195]\n\t"
            "v_mov_b64 v[184:185], 0\n\t"
            "v_mov_b64 v[188:189], 0\n\t"
            "v_mfma_f32_4x4x4_16b_bf16 v[196:199], v[186:187], v[186:187], v[196:199]\n\t"
            "v_mov_b64 v[186:187], 0\n\t"
            "v_mov_b64 v[190:191], 0\n\t"
            "v_writelane_b32 v248, s58, 63\n\t"
            "v_writelane_b32 v248, s38, m0\n\t"
            "s_waitcnt lgkmcnt(12)\n\t"
            "v_and_b32 v249, s69, v249\n\t"
            "v_max_f32 v220, 0, v220\n\t"
            "v_add_f32 v220, 0x3727c5ac, v220\n\t"
            "v_rsq_f32 v220, v220\n\t"
            "s_waitcnt lgkmcnt(11)\n\t"
            "v_smfmac_f32_16x16x64_bf16 v[176:179], v[168:171], v[152:159], v255\n\t"
            "ds_read_b128 v[168:171], v254 offset:384\n\t"
            "ds_read_b64 v[152:153], v253 offset:56\n\t"
            "ds_read_b64 v[154:155], v253 offset:152\n\t"
            "ds_read_b64 v[156:157], v253 offset:1800\n\t"
            "s_waitcnt lgkmcnt(14)\n\t"
            "ds_read_b64 v[158:159], v253 offset:1896\n\t"
            "s_waitcnt lgkmcnt(11)\n\t"
            "v_smfmac_f32_16x16x64_bf16 v[180:183], v[172:175], v[160:167], v255\n\t"
            "ds_read_b128 v[172:175], v254 offset:448\n\t"
            "ds_read_b64 v[160:161], v253 offset:64\n\t"
            "ds_read_b64 v[162:163], v253 offset:160\n\t"
            "ds_read_b64 v[164:165], v253 offset:1808\n\t"
            "s_waitcnt lgkmcnt(14)\n\t"
            "ds_read_b64 v[166:167], v253 offset:1904\n\t"
            "v_cvt_pk_bf16_f32 v176, v176, v177\n\t"
            "v_cvt_pk_bf16_f32 v177, v178, v179\n\t"
            "v_cvt_pk_bf16_f32 v178, v180, v181\n\t"
            "v_cvt_pk_bf16_f32 v179, v182, v183\n\t"
            "v_mfma_f32_4x4x4_16b_bf16 v[196:199], v[176:177], v[176:177], v[196:199]\n\t"
            "s_waitcnt lgkmcnt(14)\n\t"
            "ds_write_b128 v236, v[176:179] offset:128\n\t"
            "v_mfma_f32_16x16x32_bf16 v[192:195], v[238:241], v[176:179], v[192:195]\n\t"
            "v_mov_b64 v[176:177], 0\n\t"
            "v_mov_b64 v[180:181], 0\n\t"
            "v_mfma_f32_4x4x4_16b_bf16 v[196:199], v[178:179], v[178:179], v[196:199]\n\t"
            "v_mov_b64 v[178:179], 0\n\t"
            "v_mov_b64 v[182:183], 0\n\t"
            "s_waitcnt lgkmcnt(14)\n\t"
            "ds_write_b32 v228, v220\n\t"
            "s_waitcnt lgkmcnt(14)\n\t"
            "ds_read_b128 v[220:223], v235 offset:256\n\t"
            "s_waitcnt lgkmcnt(14)\n\t"
            "ds_read_b32 v230, v233\n\t"
            "s_waitcnt lgkmcnt(14)\n\t"
            "ds_read_b32 v231, v233 offset:64\n\t"
            "v_xor_b32 v228, 32, v234\n\t"
            "s_waitcnt lgkmcnt(14)\n\t"
            "v_smfmac_f32_16x16x64_bf16 v[184:187], v[168:171], v[144:151], v255\n\t"
            "ds_read_b128 v[168:171], v254 offset:512\n\t"
            "s_waitcnt lgkmcnt(14)\n\t"
            "ds_read_b64 v[144:145], v253 offset:72\n\t"
            "s_waitcnt lgkmcnt(14)\n\t"
            "ds_read_b64 v[146:147], v253 offset:168\n\t"
            "s_waitcnt lgkmcnt(14)\n\t"
            "ds_read_b64 v[148:149], v253 offset:1816\n\t"
            "s_waitcnt lgkmcnt(14)\n\t"
            "ds_read_b64 v[150:151], v253 offset:1912\n\t"
            "s_waitcnt lgkmcnt(14)\n\t"
            "v_smfmac_f32_16x16x64_bf16 v[188:191], v[172:175], v[152:159], v255\n\t"
            "ds_read_b128 v[172:175], v254 offset:576\n\t"
            "s_waitcnt lgkmcnt(14)\n\t"
            "ds_read_b64 v[152:153], v253 offset:80\n\t"
            "s_waitcnt lgkmcnt(14)\n\t"
            "ds_read_b64 v[154:155], v253 offset:176\n\t"
            "s_waitcnt lgkmcnt(14)\n\t"
            "ds_read_b64 v[156:157], v253 offset:1824\n\t"
            "s_waitcnt lgkmcnt(14)\n\t"
            "ds_read_b64 v[158:159], v253 offset:1920\n\t"
            "v_cvt_pk_bf16_f32 v184, v184, v185\n\t"
            "v_cvt_pk_bf16_f32 v185, v186, v187\n\t"
            "v_cvt_pk_bf16_f32 v186, v188, v189\n\t"
            "v_cvt_pk_bf16_f32 v187, v190, v191\n\t"
            "v_mfma_f32_4x4x4_16b_bf16 v[196:199], v[184:185], v[184:185], v[196:199]\n\t"
            "s_waitcnt lgkmcnt(14)\n\t"
            "ds_write_b128 v237, v[184:187] offset:128\n\t"
            "v_mfma_f32_16x16x32_bf16 v[192:195], v[238:241], v[184:187], v[192:195]\n\t"
            "v_mov_b64 v[184:185], 0\n\t"
            "v_mov_b64 v[188:189], 0\n\t"
            "v_mfma_f32_4x4x4_16b_bf16 v[196:199], v[186:187], v[186:187], v[196:199]\n\t"
            "v_mov_b64 v[186:187], 0\n\t"
            "v_mov_b64 v[190:191], 0\n\t"
            "s_waitcnt lgkmcnt(10)\n\t"
            "v_smfmac_f32_16x16x64_bf16 v[176:179], v[168:171], v[160:167], v255\n\t"
            "ds_read_b128 v[168:171], v254 offset:640\n\t"
            "ds_read_b64 v[160:161], v253 offset:88\n\t"
            "ds_read_b64 v[162:163], v253 offset:184\n\t"
            "ds_read_b64 v[164:165], v253 offset:1832\n\t"
            "ds_read_b64 v[166:167], v253 offset:1928\n\t"
            "s_waitcnt lgkmcnt(10)\n\t"
            "v_smfmac_f32_16x16x64_bf16 v[180:183], v[172:175], v[144:151], v255\n\t"
            "ds_read_b128 v[172:175], v254 offset:704\n\t"
            "v_cvt_pk_bf16_f32 v176, v176, v177\n\t"
            "v_cvt_pk_bf16_f32 v177, v178, v179\n\t"
            "s_nop 1\n\t"
            "v_mfma_f32_4x4x4_16b_bf16 v[196:199], v[176:177], v[176:177], v[196:199]\n\t"
            "s_nop 1\n\t"
            "v_cvt_pk_bf16_f32 v178, v180, v181\n\t"
            "v_cvt_pk_bf16_f32 v179, v182, v183\n\t"
            "s_nop 1\n\t"
            "v_mfma_f32_4x4x4_16b_bf16 v[196:199], v[178:179], v[178:179], v[196:199]\n\t"
            "ds_write_b128 v236, v[176:179] offset:256\n\t"
            "v_mfma_f32_16x16x32_bf16 v[192:195], v[238:241], v[176:179], v[192:195]\n\t"
            "v_add_u32 v236, s48, v244\n\t"
            "s_waitcnt lgkmcnt(6)\n\t"
            "v_smfmac_f32_16x16x64_bf16 v[184:187], v[168:171], v[152:159], v255\n\t"
            "s_waitcnt lgkmcnt(1)\n\t"
            "v_smfmac_f32_16x16x64_bf16 v[188:191], v[172:175], v[160:167], v255\n\t"
            "s_mov_b32 exec_hi, 0x3fffff\n\t"
            "s_add_u32 m0, s66, 0\n\t"
            "s_add_u32 s49, s79, 0\n\t"
            "buffer_load_dwordx4 v249, s[72:75], s49 offen lds\n\t"
            "s_mov_b32 exec_hi, -1\n\t"
            "s_mov_b32 exec_hi, 0x3fffff\n\t"
            "s_add_u32 m0, s66, 872\n\t"
            "s_add_u32 s49, s68, 196224\n\t"
            "buffer_load_dwordx4 v249, s[72:75], s49 offen lds\n\t"
            "s_mov_b32 exec_hi, -1\n\t"
            "s_mov_b32 exec_hi, 0x3fffff\n\t"
            "s_add_u32 m0, s66, 1744\n\t"
            "s_add_u32 s49, s68, 392448\n\t"
            "buffer_load_dwordx4 v249, s[72:75], s49 offen lds\n\t"
            "s_mov_b32 exec_hi, -1\n\t"
            "s_mov_b32 exec_hi, 0x3fffff\n\t"
            "s_add_u32 m0, s66, 2616\n\t"
            "s_add_u32 s49, s68, 588672\n\t"
            "buffer_load_dwordx4 v249, s[72:75], s49 offen lds\n\t"
            "s_mov_b32 exec_hi, -1\n\t"
            "s_mov_b32 exec_hi, 0x3fffff\n\t"
            "s_add_u32 m0, s66, 3488\n\t"
            "s_add_u32 s49, s68, 784896\n\t"
            "buffer_load_dwordx4 v249, s[72:75], s49 offen lds\n\t"
            "s_mov_b32 exec_hi, -1\n\t"
            "s_mov_b32 exec_hi, 0x3fffff\n\t"
            "s_add_u32 m0, s66, 4360\n\t"
            "s_add_u32 s49, s68, 981120\n\t"
            "buffer_load_dwordx4 v249, s[72:75], s49 offen lds\n\t"
            "s_mov_b32 exec_hi, -1\n\t"
            "s_mov_b32 exec_hi, 0x3fffff\n\t"
            "s_add_u32 m0, s66, 5232\n\t"
            "s_add_u32 s49, s68, 1177344\n\t"
            "buffer_load_dwordx4 v249, s[72:75], s49 offen lds\n\t"
            "s_mov_b32 exec_hi, -1\n\t"
            "v_cvt_pk_bf16_f32 v184, v184, v185\n\t"
            "v_cvt_pk_bf16_f32 v185, v186, v187\n\t"
            "v_cvt_pk_bf16_f32 v186, v188, v189\n\t"
            "v_cvt_pk_bf16_f32 v187, v190, v191\n\t"
            "v_mfma_f32_4x4x4_16b_bf16 v[196:199], v[184:185], v[184:185], v[196:199]\n\t"
            "s_mov_b32 exec_hi, 0x3fffff\n\t"
            "s_add_u32 m0, s66, 6104\n\t"
            "s_add_u32 s49, s68, 1373568\n\t"
            "buffer_load_dwordx4 v249, s[72:75], s49 offen lds\n\t"
            "s_mov_b32 exec_hi, -1\n\t"
            "v_mfma_f32_4x4x4_16b_bf16 v[196:199], v[186:187], v[186:187], v[196:199]\n\t"
            "s_mov_b32 exec_hi, 0x3fffff\n\t"
            "s_add_u32 m0, s66, 6976\n\t"
            "s_add_u32 s49, s68, 1569792\n\t"
            "buffer_load_dwordx4 v249, s[72:75], s49 offen lds\n\t"
            "s_mov_b32 exec_hi, -1\n\t"
            "v_mfma_f32_16x16x32_bf16 v[192:195], v[238:241], v[184:187], v[192:195]\n\t"
            "s_waitcnt vmcnt(9)\n\t"
            "ds_write_b128 v237, v[184:187] offset:256\n\t"
            "v_mfma_f32_16x16x32_bf16 v[144:147], v[0:3], v[200:203], 0\n\t"
            "v_add_u32 v237, s48, v245\n\t"
            "v_mfma_f32_16x16x32_bf16 v[144:147], v[4:7], v[204:207], v[144:147]\n\t"
            "v_mfma_f32_16x16x32_bf16 v[148:151], v[24:27], v[200:203], 0\n\t"
            "s_nop 1\n\t"
            "v_cndmask_b32_e64 v193, v196, v197, s[60:61]\n\t"
            "v_mfma_f32_16x16x32_bf16 v[148:151], v[28:31], v[204:207], v[148:151]\n\t"
            "v_cndmask_b32_e64 v193, v193, v198, s[62:63]\n\t"
            "v_cndmask_b32_e64 v193, v193, v199, s[64:65]\n\t"
            "v_mfma_f32_16x16x32_bf16 v[168:171], v[48:51], v[200:203], 0\n\t"
            "ds_read_b128 v[196:199], v232 offset:6272\n\t"
            "v_mfma_f32_16x16x32_bf16 v[168:171], v[52:55], v[204:207], v[168:171]\n\t"
            "ds_write_b64 v248, v[192:193] offset:18816\n\t"
            "v_mfma_f32_16x16x32_bf16 v[172:175], v[72:75], v[200:203], 0\n\t"
            "ds_read_b128 v[192:195], v235 offset:6144\n\t"
            "v_add_u32 v248, s48, v246\n\t"
            "v_mfma_f32_16x16x32_bf16 v[172:175], v[76:79], v[204:207], v[172:175]\n\t"
            "s_mov_b64 exec, 1\n\t"
            "ds_inc_u32 v255, v238 offset:7860\n\t"
            "s_mov_b64 exec, -1\n\t"
            "v_mfma_f32_16x16x32_bf16 v[152:155], v[96:99], v[200:203], 0\n\t"
            "v_mfma_f32_16x16x32_bf16 v[152:155], v[100:103], v[204:207], v[152:155]\n\t"
            "v_mfma_f32_16x16x32_bf16 v[156:159], v[120:123], v[200:203], 0\n\t"
            "ds_read_b128 v[200:203], v235 offset:6272\n\t"
            "v_mfma_f32_16x16x32_bf16 v[156:159], v[124:127], v[204:207], v[156:159]\n\t"
            "ds_read_b128 v[204:207], v232 offset:6400\n\t"
            "v_mfma_f32_16x16x32_bf16 v[144:147], v[8:11], v[208:211], v[144:147]\n\t"
            "v_mfma_f32_16x16x32_bf16 v[144:147], v[12:15], v[212:215], v[144:147]\n\t"
            "v_mfma_f32_16x16x32_bf16 v[148:151], v[32:35], v[208:211], v[148:151]\n\t"
            "v_mfma_f32_16x16x32_bf16 v[148:151], v[36:39], v[212:215], v[148:151]\n\t"
            "v_mfma_f32_16x16x32_bf16 v[168:171], v[56:59], v[208:211], v[168:171]\n\t"
            "v_mfma_f32_16x16x32_bf16 v[168:171], v[60:63], v[212:215], v[168:171]\n\t"
            "v_mfma_f32_16x16x32_bf16 v[172:175], v[80:83], v[208:211], v[172:175]\n\t"
            "v_mfma_f32_16x16x32_bf16 v[172:175], v[84:87], v[212:215], v[172:175]\n\t"
            "v_mfma_f32_16x16x32_bf16 v[152:155], v[104:107], v[208:211], v[152:155]\n\t"
            "v_mfma_f32_16x16x32_bf16 v[152:155], v[108:111], v[212:215], v[152:155]\n\t"
            "v_mfma_f32_16x16x32_bf16 v[156:159], v[128:131], v[208:211], v[156:159]\n\t"
            "ds_read_b128 v[208:211], v235 offset:6400\n\t"
            "v_mfma_f32_16x16x32_bf16 v[156:159], v[132:135], v[212:215], v[156:159]\n\t"
            "ds_read_b128 v[212:215], v236 offset:6144\n\t"
            "v_mfma_f32_16x16x32_bf16 v[144:147], v[16:19], v[216:219], v[144:147]\n\t"
            "v_mfma_f32_16x16x32_bf16 v[144:147], v[20:23], v[220:223], v[144:147]\n\t"
            "v_mfma_f32_16x16x32_bf16 v[148:151], v[40:43], v[216:219], v[148:151]\n\t"
            "v_mfma_f32_16x16x32_bf16 v[148:151], v[44:47], v[220:223], v[148:151]\n\t"
            "v_mfma_f32_16x16x32_bf16 v[168:171], v[64:67], v[216:219], v[168:171]\n\t"
            "v_mfma_f32_16x16x32_bf16 v[168:171], v[68:71], v[220:223], v[168:171]\n\t"
            "v_mfma_f32_16x16x32_bf16 v[172:175], v[88:91], v[216:219], v[172:175]\n\t"
            "v_mfma_f32_16x16x32_bf16 v[172:175], v[92:95], v[220:223], v[172:175]\n\t"
            "s_nop 1\n\t"
            "v_mul_f32 v144, v230, v144\n\t"
            "v_mfma_f32_16x16x32_bf16 v[152:155], v[112:115], v[216:219], v[152:155]\n\t"
            "v_mul_f32 v145, v230, v145\n\t"
            "v_mul_f32 v146, v230, v146\n\t"
            "v_mfma_f32_16x16x32_bf16 v[152:155], v[116:119], v[220:223], v[152:155]\n\t"
            "v_mul_f32 v148, v230, v148\n\t"
            "v_mul_f32 v149, v230, v149\n\t"
            "v_mfma_f32_16x16x32_bf16 v[156:159], v[136:139], v[216:219], v[156:159]\n\t"
            "ds_read_b128 v[216:219], v237 offset:6144\n\t"
            "v_mul_f32 v168, v230, v168\n\t"
            "v_mfma_f32_16x16x32_bf16 v[156:159], v[140:143], v[220:223], v[156:159]\n\t"
            "ds_read_b128 v[220:223], v248 offset:6144\n\t"
            "v_mul_f32 v169, v230, v169\n\t"
            "s_waitcnt lgkmcnt(7)\n\t"
            "v_mfma_f32_16x16x32_bf16 v[160:163], v[0:3], v[224:227], 0\n\t"
            "v_mul_f32 v172, v230, v172\n\t"
            "v_mul_f32 v173, v230, v173\n\t"
            "v_mfma_f32_16x16x32_bf16 v[160:163], v[4:7], v[192:195], v[160:163]\n\t"
            "v_mul_f32 v152, v230, v152\n\t"
            "v_mul_f32 v153, v230, v153\n\t"
            "v_mfma_f32_16x16x32_bf16 v[164:167], v[24:27], v[224:227], 0\n\t"
            "v_mul_f32 v156, v230, v156\n\t"
            "v_mul_f32 v157, v230, v157\n\t"
            "v_mfma_f32_16x16x32_bf16 v[164:167], v[28:31], v[192:195], v[164:167]\n\t"
            "v_mul_f32 v147, v230, v147\n\t"
            "v_cvt_pk_bf16_f32 v144, v144, v145\n\t"
            "v_mfma_f32_16x16x32_bf16 v[176:179], v[48:51], v[224:227], 0\n\t"
            "v_mul_f32 v150, v230, v150\n\t"
            "v_mul_f32 v151, v230, v151\n\t"
            "v_mfma_f32_16x16x32_bf16 v[176:179], v[52:55], v[192:195], v[176:179]\n\t"
            "v_cvt_pk_bf16_f32 v148, v148, v149\n\t"
            "v_mul_f32 v170, v230, v170\n\t"
            "v_mfma_f32_16x16x32_bf16 v[180:183], v[72:75], v[224:227], 0\n\t"
            "v_mul_f32 v171, v230, v171\n\t"
            "v_cvt_pk_bf16_f32 v168, v168, v169\n\t"
            "v_mfma_f32_16x16x32_bf16 v[180:183], v[76:79], v[192:195], v[180:183]\n\t"
            "v_mul_f32 v174, v230, v174\n\t"
            "v_mul_f32 v175, v230, v175\n\t"
            "v_mfma_f32_16x16x32_bf16 v[184:187], v[96:99], v[224:227], 0\n\t"
            "v_cvt_pk_bf16_f32 v172, v172, v173\n\t"
            "v_mul_f32 v154, v230, v154\n\t"
            "v_mfma_f32_16x16x32_bf16 v[184:187], v[100:103], v[192:195], v[184:187]\n\t"
            "v_mul_f32 v155, v230, v155\n\t"
            "v_cvt_pk_bf16_f32 v152, v152, v153\n\t"
            "v_mfma_f32_16x16x32_bf16 v[188:191], v[120:123], v[224:227], 0\n\t"
            "ds_read_b128 v[224:227], v236 offset:12288\n\t"
            "v_mul_f32 v158, v230, v158\n\t"
            "v_mfma_f32_16x16x32_bf16 v[188:191], v[124:127], v[192:195], v[188:191]\n\t"
            "v_mul_f32 v159, v230, v159\n\t"
            "v_cvt_pk_bf16_f32 v156, v156, v157\n\t"
            "s_waitcnt lgkmcnt(6)\n\t"
            "v_mfma_f32_16x16x32_bf16 v[160:163], v[8:11], v[196:199], v[160:163]\n\t"
            "v_cvt_pk_bf16_f32 v145, v146, v147\n\t"
            "v_cvt_pk_bf16_f32 v149, v150, v151\n\t"
            "v_mfma_f32_16x16x32_bf16 v[160:163], v[12:15], v[200:203], v[160:163]\n\t"
            "v_cvt_pk_bf16_f32 v169, v170, v171\n\t"
            "v_cvt_pk_bf16_f32 v173, v174, v175\n\t"
            "v_mfma_f32_16x16x32_bf16 v[164:167], v[32:35], v[196:199], v[164:167]\n\t"
            "v_cvt_pk_bf16_f32 v153, v154, v155\n\t"
            "v_cvt_pk_bf16_f32 v157, v158, v159\n\t"
            "v_mfma_f32_16x16x32_bf16 v[164:167], v[36:39], v[200:203], v[164:167]\n\t"
            "s_mov_b64 exec, -1\n\t"
            "ds_write_b64 v234, v[144:145]\n\t"
            "ds_write_b64 v234, v[148:149] offset:128\n\t"
            "ds_write_b64 v234, v[168:169] offset:256\n\t"
            "ds_write_b64 v228, v[172:173]\n\t"
            "ds_write_b64 v228, v[152:153] offset:128\n\t"
            "ds_write_b64 v228, v[156:157] offset:256\n\t"
            "s_mov_b64 exec, -1\n\t"
            "v_mfma_f32_16x16x32_bf16 v[176:179], v[56:59], v[196:199], v[176:179]\n\t"
            "v_mfma_f32_16x16x32_bf16 v[176:179], v[60:63], v[200:203], v[176:179]\n\t"
            "s_add_u32 s49, s33, 0\n\t"
            "buffer_load_dwordx4 v[0:3], v247, s[28:31], s49 offen sc1\n\t"
            "v_mfma_f32_16x16x32_bf16 v[180:183], v[80:83], v[196:199], v[180:183]\n\t"
            "v_mfma_f32_16x16x32_bf16 v[180:183], v[84:87], v[200:203], v[180:183]\n\t"
            "s_add_u32 s49, s33, 6144\n\t"
            "buffer_load_dwordx4 v[24:27], v247, s[28:31], s49 offen sc1\n\t"
            "v_mfma_f32_16x16x32_bf16 v[184:187], v[104:107], v[196:199], v[184:187]\n\t"
            "v_mfma_f32_16x16x32_bf16 v[184:187], v[108:111], v[200:203], v[184:187]\n\t"
            "s_add_u32 s49, s33, 12288\n\t"
            "buffer_load_dwordx4 v[48:51], v247, s[28:31], s49 offen sc1\n\t"
            "v_mfma_f32_16x16x32_bf16 v[188:191], v[128:131], v[196:199], v[188:191]\n\t"
            "v_mfma_f32_16x16x32_bf16 v[188:191], v[132:135], v[200:203], v[188:191]\n\t"
            "ds_read_b128 v[200:203], v236\n\t"
            "s_add_u32 s49, s33, 18432\n\t"
            "buffer_load_dwordx4 v[72:75], v247, s[28:31], s49 offen sc1\n\t"
            "s_waitcnt lgkmcnt(11)\n\t"
            "v_mfma_f32_16x16x32_bf16 v[160:163], v[16:19], v[204:207], v[160:163]\n\t"
            "v_mfma_f32_16x16x32_bf16 v[160:163], v[20:23], v[208:211], v[160:163]\n\t"
            "s_add_u32 s49, s33, 24576\n\t"
            "buffer_load_dwordx4 v[96:99], v247, s[28:31], s49 offen sc1\n\t"
            "v_mfma_f32_16x16x32_bf16 v[164:167], v[40:43], v[204:207], v[164:167]\n\t"
            "v_mfma_f32_16x16x32_bf16 v[164:167], v[44:47], v[208:211], v[164:167]\n\t"
            "s_add_u32 s49, s33, 30720\n\t"
            "buffer_load_dwordx4 v[120:123], v247, s[28:31], s49 offen sc1\n\t"
            "v_mfma_f32_16x16x32_bf16 v[176:179], v[64:67], v[204:207], v[176:179]\n\t"
            "s_nop 0\n\t"
            "v_mul_f32 v160, v231, v160\n\t"
            "v_mul_f32 v161, v231, v161\n\t"
            "v_mfma_f32_16x16x32_bf16 v[176:179], v[68:71], v[208:211], v[176:179]\n\t"
            "v_mul_f32 v162, v231, v162\n\t"
            "v_mul_f32 v163, v231, v163\n\t"
            "v_mfma_f32_16x16x32_bf16 v[180:183], v[88:91], v[204:207], v[180:183]\n\t"
            "v_mul_f32 v164, v231, v164\n\t"
            "v_mul_f32 v165, v231, v165\n\t"
            "v_mfma_f32_16x16x32_bf16 v[180:183], v[92:95], v[208:211], v[180:183]\n\t"
            "v_cvt_pk_bf16_f32 v160, v160, v161\n\t"
            "v_mul_f32 v166, v231, v166\n\t"
            "v_mfma_f32_16x16x32_bf16 v[184:187], v[112:115], v[204:207], v[184:187]\n\t"
            "v_mul_f32 v176, v231, v176\n\t"
            "v_mul_f32 v177, v231, v177\n\t"
            "v_mfma_f32_16x16x32_bf16 v[184:187], v[116:119], v[208:211], v[184:187]\n\t"
            "v_mul_f32 v167, v231, v167\n\t"
            "v_cvt_pk_bf16_f32 v164, v164, v165\n\t"
            "v_mfma_f32_16x16x32_bf16 v[188:191], v[136:139], v[204:207], v[188:191]\n\t"
            "ds_read_b128 v[204:207], v237\n\t"
            "v_mul_f32 v180, v231, v180\n\t"
            "v_mfma_f32_16x16x32_bf16 v[188:191], v[140:143], v[208:211], v[188:191]\n\t"
            "ds_read_b128 v[208:211], v248\n\t"
            "v_mul_f32 v181, v231, v181\n\t"
            "v_mul_f32 v184, v231, v184\n\t"
            "v_mul_f32 v185, v231, v185\n\t"
            "v_mul_f32 v178, v231, v178\n\t"
            "v_mul_f32 v179, v231, v179\n\t"
            "v_cvt_pk_bf16_f32 v176, v176, v177\n\t"
            "v_mul_f32 v182, v231, v182\n\t"
            "v_mul_f32 v188, v231, v188\n\t"
            "v_mul_f32 v189, v231, v189\n\t"
            "v_mul_f32 v183, v231, v183\n\t"
            "v_cvt_pk_bf16_f32 v180, v180, v181\n\t"
            "v_mul_f32 v186, v231, v186\n\t"
            "v_mul_f32 v187, v231, v187\n\t"
            "v_cvt_pk_bf16_f32 v184, v184, v185\n\t"
            "v_mul_f32 v190, v231, v190\n\t"
            "v_mul_f32 v191, v231, v191\n\t"
            "v_cvt_pk_bf16_f32 v188, v188, v189\n\t"
            "v_cvt_pk_bf16_f32 v161, v162, v163\n\t"
            "v_cvt_pk_bf16_f32 v165, v166, v167\n\t"
            "v_cvt_pk_bf16_f32 v177, v178, v179\n\t"
            "v_cvt_pk_bf16_f32 v181, v182, v183\n\t"
            "v_cvt_pk_bf16_f32 v185, v186, v187\n\t"
            "v_cvt_pk_bf16_f32 v189, v190, v191\n\t"
            "s_waitcnt lgkmcnt(9)\n\t"
            "s_mov_b64 exec, s[80:81]\n\t"
            "ds_write_b64 v234, v[160:161] offset:6144\n\t"
            "ds_write_b64 v234, v[164:165] offset:6272\n\t"
            "ds_write_b64 v234, v[176:177] offset:6400\n\t"
            "ds_write_b64 v228, v[180:181] offset:6144\n\t"
            "ds_write_b64 v228, v[184:185] offset:6272\n\t"
            "ds_write_b64 v228, v[188:189] offset:6400\n\t"
            "s_mov_b64 exec, -1\n\t"
            "s_waitcnt lgkmcnt(14)\n\t"
            "ds_read_b128 v[228:231], v237 offset:12288\n\t"
            "s_waitcnt lgkmcnt(14)\n\t"
            "ds_read_b128 v[232:235], v248 offset:12288\n\t"
            "s_add_u32 s49, s33, 1024\n\t"
            "buffer_load_dwordx4 v[4:7], v247, s[28:31], s49 offen sc1\n\t"
            "s_add_u32 s49, s33, 7168\n\t"
            "buffer_load_dwordx4 v[28:31], v247, s[28:31], s49 offen sc1\n\t"
            "s_waitcnt lgkmcnt(0)\n\t"
            "s_barrier\n\t"
            "s_setprio 3\n\t"
            "s_add_u32 s49, s33, 13312\n\t"
            "buffer_load_dwordx4 v[52:55], v247, s[28:31], s49 offen sc1\n\t"
            "s_add_u32 s49, s33, 19456\n\t"
            "buffer_load_dwordx4 v[76:79], v247, s[28:31], s49 offen sc1\n\t"
            "s_add_u32 s49, s33, 25600\n\t"
            "buffer_load_dwordx4 v[100:103], v247, s[28:31], s49 offen sc1\n\t"
            "s_add_u32 s49, s33, 31744\n\t"
            "buffer_load_dwordx4 v[124:127], v247, s[28:31], s49 offen sc1\n\t"
            "s_add_u32 s49, s33, 2048\n\t"
            "buffer_load_dwordx4 v[8:11], v247, s[28:31], s49 offen sc1\n\t"
            "s_add_u32 s49, s33, 8192\n\t"
            "buffer_load_dwordx4 v[32:35], v247, s[28:31], s49 offen sc1\n\t"
            "s_add_u32 s49, s33, 14336\n\t"
            "buffer_load_dwordx4 v[56:59], v247, s[28:31], s49 offen sc1\n\t"
            "s_add_u32 s49, s33, 20480\n\t"
            "buffer_load_dwordx4 v[80:83], v247, s[28:31], s49 offen sc1\n\t"
            "s_add_u32 s49, s33, 26624\n\t"
            "buffer_load_dwordx4 v[104:107], v247, s[28:31], s49 offen sc1\n\t"
            "s_add_u32 s49, s33, 32768\n\t"
            "buffer_load_dwordx4 v[128:131], v247, s[28:31], s49 offen sc1\n\t"
            "s_add_u32 s49, s33, 3072\n\t"
            "buffer_load_dwordx4 v[12:15], v247, s[28:31], s49 offen sc1\n\t"
            "s_add_u32 s49, s33, 9216\n\t"
            "buffer_load_dwordx4 v[36:39], v247, s[28:31], s49 offen sc1\n\t"
            "s_add_u32 s49, s33, 15360\n\t"
            "buffer_load_dwordx4 v[60:63], v247, s[28:31], s49 offen sc1\n\t"
            "s_add_u32 s49, s33, 21504\n\t"
            "buffer_load_dwordx4 v[84:87], v247, s[28:31], s49 offen sc1\n\t"
            "s_add_u32 s49, s33, 27648\n\t"
            "buffer_load_dwordx4 v[108:111], v247, s[28:31], s49 offen sc1\n\t"
            "s_add_u32 s49, s33, 33792\n\t"
            "buffer_load_dwordx4 v[132:135], v247, s[28:31], s49 offen sc1\n\t"
            "s_add_u32 s49, s33, 4096\n\t"
            "buffer_load_dwordx4 v[16:19], v247, s[28:31], s49 offen sc1\n\t"
            "s_add_u32 s49, s33, 10240\n\t"
            "buffer_load_dwordx4 v[40:43], v247, s[28:31], s49 offen sc1\n\t"
            "s_add_u32 s49, s33, 16384\n\t"
            "buffer_load_dwordx4 v[64:67], v247, s[28:31], s49 offen sc1\n\t"
            "s_add_u32 s49, s33, 22528\n\t"
            "buffer_load_dwordx4 v[88:91], v247, s[28:31], s49 offen sc1\n\t"
            "s_add_u32 s49, s33, 28672\n\t"
            "buffer_load_dwordx4 v[112:115], v247, s[28:31], s49 offen sc1\n\t"
            "s_add_u32 s49, s33, 34816\n\t"
            "buffer_load_dwordx4 v[136:139], v247, s[28:31], s49 offen sc1\n\t"
            "s_add_u32 s49, s33, 5120\n\t"
            "buffer_load_dwordx4 v[20:23], v247, s[28:31], s49 offen sc1\n\t"
            "s_add_u32 s49, s33, 11264\n\t"
            "buffer_load_dwordx4 v[44:47], v247, s[28:31], s49 offen sc1\n\t"
            "s_add_u32 s49, s33, 17408\n\t"
            "buffer_load_dwordx4 v[68:71], v247, s[28:31], s49 offen sc1\n\t"
            "s_add_u32 s49, s33, 23552\n\t"
            "buffer_load_dwordx4 v[92:95], v247, s[28:31], s49 offen sc1\n\t"
            "s_add_u32 s49, s33, 29696\n\t"
            "buffer_load_dwordx4 v[116:119], v247, s[28:31], s49 offen sc1\n\t"
            "s_add_u32 s49, s33, 35840\n\t"
            "buffer_load_dwordx4 v[140:143], v247, s[28:31], s49 offen sc1\n\t"
            "s_add_u32 s59, s59, s84\n\t"
            "s_movk_i32 s84, 800\n\t"
            "s_add_u32 s45, s46, s27\n\t"
            "s_mov_b32 s46, s47\n\t"
            "s_mov_b32 s47, s68\n\t"
            "s_xor_b32 s48, s48, s71\n\t"
            "s_xor_b32 s67, s67, s71\n\t"
            "s_add_u32 s76, s76, 4\n\t"
            "s_add_u32 s70, s70, 4\n\t"
            "s_lshl_b32 s56, s56, 2\n\t"
            "s_lshr_b32 s49, s26, 30\n\t"
            "s_or_b32 s56, s56, s49\n\t"
            "s_cmp_eq_u32 s82, 0\n\t"
            "s_cbranch_scc1 .Lwk_nr%=\n\t"
            "s_waitcnt lgkmcnt(0)\n\t"
            "s_nop 7\n\t"
            "v_add_u32 v236, 127360, v247\n\t"
            "v_add_u32 v237, 136704, v247\n\t"
            "ds_read_b128 v[144:147], v236\n\t"
            "ds_read_b128 v[148:151], v237\n\t"
            "s_waitcnt lgkmcnt(0)\n\t"
            "s_mov_b32 exec_hi, 0x3ffff\n\t"
            "buffer_store_dwordx4 v[144:147], v247, s[28:31], s82 offen sc1\n\t"
            "s_mov_b64 exec, 0xffffff\n\t"
            "buffer_store_dwordx4 v[148:151], v247, s[28:31], s82 offen offset:800 sc1\n\t"
            "s_mov_b64 exec, -1\n\t"
            "ds_read_b128 v[152:155], v252 offset:2560\n\t"
            "s_waitcnt vmcnt(0) lgkmcnt(0)\n\t"
            "s_mov_b64 exec, 1\n\t"
            "buffer_store_dwordx4 v[152:155], off, s[28:31], s82 offset:1216 sc1\n\t"
            "s_mov_b64 exec, -1\n\t"
            "s_branch .Lwk_n2%=\n\t"
            ".Lwk_nr%=:\n\t"
            "s_waitcnt vmcnt(0)\n\t"
            ".Lwk_n2%=:\n\t"
            "s_nop 1\n\t"
            "s_nop 7\n\t"
            ".p2align 6\n\t"
            "s_nop 0\n\t"
            "s_nop 0\n\t"
            "s_nop 0\n\t"
            "s_nop 0\n\t"
            "s_nop 0\n\t"
            "s_nop 0\n\t"
            "s_nop 0\n\t"
            ".Lwb_loop%=:\n\t"
            "s_setprio 1\n\t"
            "ds_read_b128 v[168:171], v254\n\t"
            "ds_read_b128 v[172:175], v254 offset:64\n\t"
            "s_waitcnt vmcnt(9)\n\t"
            "ds_read_b64 v[144:145], v253\n\t"
            "ds_read_b64 v[146:147], v253 offset:96\n\t"
            "ds_read_b64 v[148:149], v253 offset:1744\n\t"
            "ds_read_b64 v[150:151], v253 offset:1840\n\t"
            "ds_read_b64 v[152:153], v253 offset:8\n\t"
            "ds_read_b64 v[154:155], v253 offset:104\n\t"
            "ds_read_b64 v[156:157], v253 offset:1752\n\t"
            "ds_read_b64 v[158:159], v253 offset:1848\n\t"
            "ds_read_b64 v[160:161], v253 offset:16\n\t"
            "ds_read_b64 v[162:163], v253 offset:112\n\t"
            "ds_read_b64 v[164:165], v253 offset:1760\n\t"
            "ds_read_b64 v[166:167], v253 offset:1856\n\t"
            "v_mov_b64 v[176:177], 0\n\t"
            "v_mov_b64 v[178:179], 0\n\t"
            "v_mov_b64 v[180:181], 0\n\t"
            "v_mov_b64 v[182:183], 0\n\t"
            "v_mov_b32 v249, s76\n\t"
            "v_mov_b64 v[184:185], 0\n\t"
            "v_mov_b64 v[186:187], 0\n\t"
            "v_mov_b64 v[188:189], 0\n\t"
            "v_mov_b64 v[190:191], 0\n\t"
            "v_add_u32 v236, s67, v250\n\t"
            "v_add_u32 v248, s67, v251\n\t"
            "v_mov_b32 v247, 0\n\t"
            "ds_read_b32 v249, v249\n\t"
            "s_setprio 3\n\t"
            "s_waitcnt lgkmcnt(9)\n\t"
            "v_smfmac_f32_16x16x64_bf16 v[176:179], v[168:171], v[144:151], v255\n\t"
            "ds_read_b128 v[168:171], v254 offset:128\n\t"
            "s_waitcnt lgkmcnt(6)\n\t"
            "v_smfmac_f32_16x16x64_bf16 v[180:183], v[172:175], v[152:159], v255\n\t"
            "s_setprio 1\n\t"
            "ds_read_b64 v[144:145], v253 offset:24\n\t"
            "ds_read_b64 v[146:147], v253 offset:120\n\t"
            "ds_read_b64 v[148:149], v253 offset:1768\n\t"
            "ds_read_b64 v[150:151], v253 offset:1864\n\t"
            "ds_read_b128 v[172:175], v254 offset:192\n\t"
            "ds_read_b64 v[152:153], v253 offset:32\n\t"
            "ds_read_b64 v[154:155], v253 offset:128\n\t"
            "ds_read_b64 v[156:157], v253 offset:1776\n\t"
            "ds_read_b64 v[158:159], v253 offset:1872\n\t"
            "v_cvt_pk_bf16_f32 v176, v176, v177\n\t"
            "v_cvt_pk_bf16_f32 v177, v178, v179\n\t"
            "v_cvt_pk_bf16_f32 v178, v180, v181\n\t"
            "v_cvt_pk_bf16_f32 v179, v182, v183\n\t"
            "s_setprio 3\n\t"
            "v_mfma_f32_4x4x4_16b_bf16 v[196:199], v[176:177], v[176:177], 0\n\t"
            "v_mov_b64 v[180:181], 0\n\t"
            "v_mfma_f32_16x16x32_bf16 v[192:195], v[238:241], v[176:179], 0\n\t"
            "s_waitcnt lgkmcnt(10)\n\t"
            "v_readfirstlane_b32 s26, v249\n\t"
            "v_mov_b64 v[182:183], 0\n\t"
            "v_mfma_f32_4x4x4_16b_bf16 v[196:199], v[178:179], v[178:179], v[196:199]\n\t"
            "ds_read_b32 v249, v252 offset:12\n\t"
            "s_and_b32 s68, s26, 0x7ffffff\n\t"
            "s_add_u32 s68, s68, s78\n\t"
            "s_and_b32 s69, s26, 0x38000000\n\t"
            "s_or_b32 s69, s69, s39\n\t"
            "s_bitcmp1_b32 s26, 27\n\t"
            "s_cselect_b32 s79, 2.0, s68\n\t"
            "s_and_b32 s57, s56, 3\n\t"
            "s_lshl_b32 s58, s57, 3\n\t"
            "s_lshr_b32 m0, s54, s58\n\t"
            "s_lshl_b32 s58, s57, 4\n\t"
            "s_lshr_b64 s[36:37], s[50:51], s58\n\t"
            "s_and_b32 s57, s36, 0xffff\n\t"
            "s_add_u32 s57, s57, s67\n\t"
            "v_writelane_b32 v236, s57, 63\n\t"
            "v_writelane_b32 v236, s59, m0\n\t"
            "v_xor_b32 v237, 16, v236\n\t"
            "s_lshr_b64 s[36:37], s[52:53], s58\n\t"
            "s_and_b32 s58, s36, 0xffff\n\t"
            "s_add_u32 s58, s58, s67\n\t"
            "ds_write_b128 v236, v[176:179]\n\t"
            "s_waitcnt lgkmcnt(11)\n\t"
            "v_smfmac_f32_16x16x64_bf16 v[184:187], v[168:171], v[160:167], v255\n\t"
            "s_and_b32 s38, s52, 0xffff\n\t"
            "s_add_u32 s38, s38, s67\n\t"
            "ds_read_b128 v[168:171], v254 offset:256\n\t"
            "s_waitcnt lgkmcnt(7)\n\t"
            "v_smfmac_f32_16x16x64_bf16 v[188:191], v[172:175], v[144:151], v255\n\t"
            "s_setprio 1\n\t"
            "ds_read_b64 v[160:161], v253 offset:40\n\t"
            "ds_read_b64 v[162:163], v253 offset:136\n\t"
            "ds_read_b64 v[164:165], v253 offset:1784\n\t"
            "ds_read_b64 v[166:167], v253 offset:1880\n\t"
            "ds_read_b128 v[172:175], v254 offset:320\n\t"
            "ds_read_b64 v[144:145], v253 offset:48\n\t"
            "ds_read_b64 v[146:147], v253 offset:144\n\t"
            "ds_read_b64 v[148:149], v253 offset:1792\n\t"
            "s_waitcnt lgkmcnt(14)\n\t"
            "ds_read_b64 v[150:151], v253 offset:1888\n\t"
            "v_mov_b64 v[176:177], 0\n\t"
            "v_mov_b64 v[178:179], 0\n\t"
            "v_cvt_pk_bf16_f32 v184, v184, v185\n\t"
            "v_cvt_pk_bf16_f32 v185, v186, v187\n\t"
            "v_cvt_pk_bf16_f32 v186, v188, v189\n\t"
            "v_cvt_pk_bf16_f32 v187, v190, v191\n\t"
            "s_setprio 3\n\t"
            "v_mfma_f32_4x4x4_16b_bf16 v[196:199], v[184:185], v[184:185], v[196:199]\n\t"
            "s_waitcnt lgkmcnt(14)\n\t"
            "ds_write_b128 v237, v[184:187]\n\t"
            "v_mfma_f32_16x16x32_bf16 v[192:195], v[238:241], v[184:187], v[192:195]\n\t"
            "v_mov_b64 v[184:185], 0\n\t"
            "v_mov_b64 v[188:189], 0\n\t"
            "v_mfma_f32_4x4x4_16b_bf16 v[196:199], v[186:187], v[186:187], v[196:199]\n\t"
            "v_mov_b64 v[186:187], 0\n\t"
            "v_mov_b64 v[190:191], 0\n\t"
            "v_writelane_b32 v248, s58, 63\n\t"
            "v_writelane_b32 v248, s38, m0\n\t"
            "s_waitcnt lgkmcnt(12)\n\t"
            "v_and_b32 v249, s69, v249\n\t"
            "s_waitcnt lgkmcnt(10)\n\t"
            "v_smfmac_f32_16x16x64_bf16 v[176:179], v[168:171], v[152:159], v255\n\t"
            "ds_read_b128 v[168:171], v254 offset:384\n\t"
            "ds_read_b64 v[152:153], v253 offset:56\n\t"
            "ds_read_b64 v[154:155], v253 offset:152\n\t"
            "ds_read_b64 v[156:157], v253 offset:1800\n\t"
            "ds_read_b64 v[158:159], v253 offset:1896\n\t"
            "s_waitcnt lgkmcnt(10)\n\t"
            "v_smfmac_f32_16x16x64_bf16 v[180:183], v[172:175], v[160:167], v255\n\t"
            "s_setprio 1\n\t"
            "ds_read_b128 v[172:175], v254 offset:448\n\t"
            "ds_read_b64 v[160:161], v253 offset:64\n\t"
            "ds_read_b64 v[162:163], v253 offset:160\n\t"
            "ds_read_b64 v[164:165], v253 offset:1808\n\t"
            "ds_read_b64 v[166:167], v253 offset:1904\n\t"
            "v_cvt_pk_bf16_f32 v176, v176, v177\n\t"
            "v_cvt_pk_bf16_f32 v177, v178, v179\n\t"
            "v_cvt_pk_bf16_f32 v178, v180, v181\n\t"
            "v_cvt_pk_bf16_f32 v179, v182, v183\n\t"
            "s_setprio 3\n\t"
            "v_mfma_f32_4x4x4_16b_bf16 v[196:199], v[176:177], v[176:177], v[196:199]\n\t"
            "s_waitcnt lgkmcnt(14)\n\t"
            "ds_write_b128 v236, v[176:179] offset:128\n\t"
            "v_mfma_f32_16x16x32_bf16 v[192:195], v[238:241], v[176:179], v[192:195]\n\t"
            "v_mov_b64 v[176:177], 0\n\t"
            "v_mov_b64 v[180:181], 0\n\t"
            "v_mfma_f32_4x4x4_16b_bf16 v[196:199], v[178:179], v[178:179], v[196:199]\n\t"
            "v_mov_b64 v[178:179], 0\n\t"
            "v_mov_b64 v[182:183], 0\n\t"
            "s_waitcnt lgkmcnt(10)\n\t"
            "v_smfmac_f32_16x16x64_bf16 v[184:187], v[168:171], v[144:151], v255\n\t"
            "ds_read_b128 v[168:171], v254 offset:512\n\t"
            "ds_read_b64 v[144:145], v253 offset:72\n\t"
            "ds_read_b64 v[146:147], v253 offset:168\n\t"
            "ds_read_b64 v[148:149], v253 offset:1816\n\t"
            "ds_read_b64 v[150:151], v253 offset:1912\n\t"
            "s_waitcnt lgkmcnt(10)\n\t"
            "v_smfmac_f32_16x16x64_bf16 v[188:191], v[172:175], v[152:159], v255\n\t"
            "s_setprio 1\n\t"
            "ds_read_b128 v[172:175], v254 offset:576\n\t"
            "ds_read_b64 v[152:153], v253 offset:80\n\t"
            "ds_read_b64 v[154:155], v253 offset:176\n\t"
            "ds_read_b64 v[156:157], v253 offset:1824\n\t"
            "ds_read_b64 v[158:159], v253 offset:1920\n\t"
            "v_cvt_pk_bf16_f32 v184, v184, v185\n\t"
            "v_cvt_pk_bf16_f32 v185, v186, v187\n\t"
            "v_cvt_pk_bf16_f32 v186, v188, v189\n\t"
            "v_cvt_pk_bf16_f32 v187, v190, v191\n\t"
            "s_setprio 3\n\t"
            "v_mfma_f32_4x4x4_16b_bf16 v[196:199], v[184:185], v[184:185], v[196:199]\n\t"
            "s_waitcnt lgkmcnt(14)\n\t"
            "ds_write_b128 v237, v[184:187] offset:128\n\t"
            "v_mfma_f32_16x16x32_bf16 v[192:195], v[238:241], v[184:187], v[192:195]\n\t"
            "v_mov_b64 v[184:185], 0\n\t"
            "v_mov_b64 v[188:189], 0\n\t"
            "v_mfma_f32_4x4x4_16b_bf16 v[196:199], v[186:187], v[186:187], v[196:199]\n\t"
            "v_mov_b64 v[186:187], 0\n\t"
            "v_mov_b64 v[190:191], 0\n\t"
            "s_waitcnt lgkmcnt(10)\n\t"
            "v_smfmac_f32_16x16x64_bf16 v[176:179], v[168:171], v[160:167], v255\n\t"
            "ds_read_b128 v[168:171], v254 offset:640\n\t"
            "ds_read_b64 v[160:161], v253 offset:88\n\t"
            "ds_read_b64 v[162:163], v253 offset:184\n\t"
            "ds_read_b64 v[164:165], v253 offset:1832\n\t"
            "ds_read_b64 v[166:167], v253 offset:1928\n\t"
            "s_waitcnt lgkmcnt(10)\n\t"
            "v_smfmac_f32_16x16x64_bf16 v[180:183], v[172:175], v[144:151], v255\n\t"
            "s_setprio 1\n\t"
            "ds_read_b128 v[172:175], v254 offset:704\n\t"
            "v_lshlrev_b32 v144, 16, v212\n\t"
            "v_cvt_pk_bf16_f32 v176, v176, v177\n\t"
            "v_cvt_pk_bf16_f32 v177, v178, v179\n\t"
            "v_and_b32 v145, 0xffff0000, v212\n\t"
            "v_lshlrev_b32 v146, 16, v213\n\t"
            "s_setprio 3\n\t"
            "v_mfma_f32_4x4x4_16b_bf16 v[196:199], v[176:177], v[176:177], v[196:199]\n\t"
            "v_and_b32 v147, 0xffff0000, v213\n\t"
            "v_cvt_pk_bf16_f32 v178, v180, v181\n\t"
            "v_cvt_pk_bf16_f32 v179, v182, v183\n\t"
            "v_lshlrev_b32 v180, 16, v210\n\t"
            "v_and_b32 v181, 0xffff0000, v210\n\t"
            "v_mfma_f32_4x4x4_16b_bf16 v[196:199], v[178:179], v[178:179], v[196:199]\n\t"
            "ds_write_b128 v236, v[176:179] offset:256\n\t"
            "v_mfma_f32_16x16x32_bf16 v[192:195], v[238:241], v[176:179], v[192:195]\n\t"
            "s_setprio 1\n\t"
            "v_lshlrev_b32 v176, 16, v208\n\t"
            "v_and_b32 v177, 0xffff0000, v208\n\t"
            "v_lshlrev_b32 v178, 16, v209\n\t"
            "v_and_b32 v179, 0xffff0000, v209\n\t"
            "v_lshlrev_b32 v182, 16, v211\n\t"
            "v_and_b32 v183, 0xffff0000, v211\n\t"
            "v_lshlrev_b32 v148, 16, v214\n\t"
            "v_and_b32 v149, 0xffff0000, v214\n\t"
            "v_lshlrev_b32 v150, 16, v215\n\t"
            "v_and_b32 v151, 0xffff0000, v215\n\t"
            "v_add_u32 v236, s48, v244\n\t"
            "ds_read_b128 v[212:215], v236 offset:6144\n\t"
            "s_setprio 3\n\t"
            "s_waitcnt lgkmcnt(7)\n\t"
            "v_smfmac_f32_16x16x64_bf16 v[184:187], v[168:171], v[152:159], v255\n\t"
            "v_lshlrev_b32 v152, 16, v200\n\t"
            "v_and_b32 v153, 0xffff0000, v200\n\t"
            "v_lshlrev_b32 v154, 16, v201\n\t"
            "v_and_b32 v155, 0xffff0000, v201\n\t"
            "v_lshlrev_b32 v156, 16, v202\n\t"
            "s_waitcnt lgkmcnt(2)\n\t"
            "v_smfmac_f32_16x16x64_bf16 v[188:191], v[172:175], v[160:167], v255\n\t"
            "s_setprio 1\n\t"
            "s_mov_b32 exec_hi, 0x3fffff\n\t"
            "s_add_u32 m0, s66, 0\n\t"
            "s_add_u32 s49, s79, 0\n\t"
            "buffer_load_dwordx4 v249, s[72:75], s49 offen lds\n\t"
            "s_mov_b32 exec_hi, -1\n\t"
            "s_mov_b32 exec_hi, 0x3fffff\n\t"
            "s_add_u32 m0, s66, 872\n\t"
            "s_add_u32 s49, s68, 196224\n\t"
            "buffer_load_dwordx4 v249, s[72:75], s49 offen lds\n\t"
            "s_mov_b32 exec_hi, -1\n\t"
            "s_mov_b32 exec_hi, 0x3fffff\n\t"
            "s_add_u32 m0, s66, 1744\n\t"
            "s_add_u32 s49, s68, 392448\n\t"
            "buffer_load_dwordx4 v249, s[72:75], s49 offen lds\n\t"
            "s_mov_b32 exec_hi, -1\n\t"
            "s_mov_b32 exec_hi, 0x3fffff\n\t"
            "s_add_u32 m0, s66, 2616\n\t"
            "s_add_u32 s49, s68, 588672\n\t"
            "buffer_load_dwordx4 v249, s[72:75], s49 offen lds\n\t"
            "s_mov_b32 exec_hi, -1\n\t"
            "ds_read_b32 v247, v247 offset:7856\n\t"
            "s_mov_b32 exec_hi, 0x3fffff\n\t"
            "s_add_u32 m0, s66, 3488\n\t"
            "s_add_u32 s49, s68, 784896\n\t"
            "buffer_load_dwordx4 v249, s[72:75], s49 offen lds\n\t"
            "s_mov_b32 exec_hi, -1\n\t"
            "s_mov_b32 exec_hi, 0x3fffff\n\t"
            "s_add_u32 m0, s66, 4360\n\t"
            "s_add_u32 s49, s68, 981120\n\t"
            "buffer_load_dwordx4 v249, s[72:75], s49 offen lds\n\t"
            "s_mov_b32 exec_hi, -1\n\t"
            "s_mov_b32 exec_hi, 0x3fffff\n\t"
            "s_add_u32 m0, s66, 5232\n\t"
            "s_add_u32 s49, s68, 1177344\n\t"
            "buffer_load_dwordx4 v249, s[72:75], s49 offen lds\n\t"
            "s_mov_b32 exec_hi, -1\n\t"
            "s_mov_b32 exec_hi, 0x3fffff\n\t"
            "s_add_u32 m0, s66, 6104\n\t"
            "s_add_u32 s49, s68, 1373568\n\t"
            "buffer_load_dwordx4 v249, s[72:75], s49 offen lds\n\t"
            "s_mov_b32 exec_hi, -1\n\t"
            "s_mov_b32 exec_hi, 0x3fffff\n\t"
            "s_add_u32 m0, s66, 6976\n\t"
            "s_add_u32 s49, s68, 1569792\n\t"
            "buffer_load_dwordx4 v249, s[72:75], s49 offen lds\n\t"
            "s_mov_b32 exec_hi, -1\n\t"
            "v_cvt_pk_bf16_f32 v184, v184, v185\n\t"
            "v_cvt_pk_bf16_f32 v185, v186, v187\n\t"
            "v_cvt_pk_bf16_f32 v186, v188, v189\n\t"
            "v_cvt_pk_bf16_f32 v187, v190, v191\n\t"
            "s_setprio 3\n\t"
            "v_mfma_f32_4x4x4_16b_bf16 v[196:199], v[184:185], v[184:185], v[196:199]\n\t"
            "ds_write_b128 v237, v[184:187] offset:256\n\t"
            "v_mfma_f32_16x16x32_bf16 v[192:195], v[238:241], v[184:187], v[192:195]\n\t"
            "s_waitcnt lgkmcnt(1)\n\t"
            "v_readfirstlane_b32 s77, v247\n\t"
            "s_cmp_ge_u32 s77, s70\n\t"
            "s_cbranch_scc0 .Lwb_spin%=\n\t"
            ".Lwb_go%=:\n\t"
            "v_mfma_f32_4x4x4_16b_bf16 v[196:199], v[186:187], v[186:187], v[196:199]\n\t"
            "s_setprio 1\n\t"
            "ds_read_b128 v[184:187], v242\n\t"
            "ds_read_b128 v[188:191], v243\n\t"
            "ds_read_b128 v[172:175], v243 offset:128\n\t"
            "v_and_b32 v157, 0xffff0000, v202\n\t"
            "v_lshlrev_b32 v158, 16, v203\n\t"
            "v_and_b32 v159, 0xffff0000, v203\n\t"
            "v_lshlrev_b32 v160, 16, v204\n\t"
            "v_and_b32 v161, 0xffff0000, v204\n\t"
            "v_lshlrev_b32 v162, 16, v205\n\t"
            "v_cndmask_b32_e64 v193, v196, v197, s[60:61]\n\t"
            "v_cndmask_b32_e64 v193, v193, v198, s[62:63]\n\t"
            "v_cndmask_b32_e64 v193, v193, v199, s[64:65]\n\t"
            "v_and_b32 v163, 0xffff0000, v205\n\t"
            "v_lshlrev_b32 v164, 16, v206\n\t"
            "v_and_b32 v165, 0xffff0000, v206\n\t"
            "v_lshlrev_b32 v166, 16, v207\n\t"
            "v_and_b32 v167, 0xffff0000, v207\n\t"
            "v_lshlrev_b32 v168, 16, v216\n\t"
            "v_and_b32 v169, 0xffff0000, v216\n\t"
            "v_lshlrev_b32 v170, 16, v217\n\t"
            "v_and_b32 v171, 0xffff0000, v217\n\t"
            "s_bfe_u32 s57, s56, 0x20004\n\t"
            "s_lshl_b32 s58, s57, 3\n\t"
            "s_lshr_b32 s58, s55, s58\n\t"
            "s_lshl_b64 s[36:37], s[34:35], s58\n\t"
            "ds_read_b128 v[196:199], v252\n\t"
            "ds_read_b128 v[200:203], v236\n\t"
            "ds_write_b64 v248, v[192:193] offset:18816\n\t"
            "ds_read_b128 v[192:195], v242 offset:128\n\t"
            "s_bitcmp1_b32 s57, 1\n\t"
            "s_cselect_b32 s58, 1177344, 0\n\t"
            "s_bitcmp1_b32 s57, 0\n\t"
            "s_cselect_b32 s57, 2304, 0\n\t"
            "s_add_u32 s57, s57, s58\n\t"
            "s_mov_b64 exec, 1\n\t"
            "ds_inc_u32 v255, v238 offset:7860\n\t"
            "s_mov_b64 exec, -1\n\t"
            "v_add_u32 v237, s48, v245\n\t"
            "v_add_u32 v248, s48, v246\n\t"
            "s_add_u32 s59, s59, s84\n\t"
            "s_movk_i32 s84, 800\n\t"
            "ds_read_b128 v[204:207], v237\n\t"
            "s_setprio 3\n\t"
            "s_waitcnt lgkmcnt(7)\n\t"
            "v_mfma_f32_16x16x32_bf16 v[152:155], v[0:3], v[184:187], v[152:155]\n\t"
            "s_setprio 1\n\t"
            "s_setprio 1\n\t"
            "ds_read_b128 v[208:211], v248\n\t"
            "v_mfma_f32_16x16x32_bf16 v[152:155], v[4:7], v[188:191], v[152:155]\n\t"
            "v_mfma_f32_16x16x32_bf16 v[156:159], v[28:31], v[188:191], v[156:159]\n\t"
            "v_mfma_f32_16x16x32_bf16 v[156:159], v[24:27], v[184:187], v[156:159]\n\t"
            "v_mfma_f32_16x16x32_bf16 v[160:163], v[48:51], v[184:187], v[160:163]\n\t"
            "v_mfma_f32_16x16x32_bf16 v[160:163], v[52:55], v[188:191], v[160:163]\n\t"
            "v_mfma_f32_16x16x32_bf16 v[164:167], v[76:79], v[188:191], v[164:167]\n\t"
            "s_waitcnt lgkmcnt(6)\n\t"
            "s_mov_b64 exec, s[36:37]\n\t"
            "v_add_u32 v196, s57, v196\n\t"
            "s_mov_b64 exec, -1\n\t"
            "v_mfma_f32_16x16x32_bf16 v[164:167], v[72:75], v[184:187], v[164:167]\n\t"
            "v_mfma_f32_16x16x32_bf16 v[176:179], v[96:99], v[184:187], v[176:179]\n\t"
            "v_mfma_f32_16x16x32_bf16 v[176:179], v[100:103], v[188:191], v[176:179]\n\t"
            "v_mfma_f32_16x16x32_bf16 v[180:183], v[124:127], v[188:191], v[180:183]\n\t"
            "ds_read_b128 v[188:191], v243 offset:256\n\t"
            "v_mfma_f32_16x16x32_bf16 v[180:183], v[120:123], v[184:187], v[180:183]\n\t"
            "ds_read_b128 v[184:187], v242 offset:256\n\t"
            "s_waitcnt lgkmcnt(5)\n\t"
            "v_mfma_f32_16x16x32_bf16 v[152:155], v[8:11], v[192:195], v[152:155]\n\t"
            "v_mfma_f32_16x16x32_bf16 v[152:155], v[12:15], v[172:175], v[152:155]\n\t"
            "v_mfma_f32_16x16x32_bf16 v[156:159], v[36:39], v[172:175], v[156:159]\n\t"
            "v_mfma_f32_16x16x32_bf16 v[156:159], v[32:35], v[192:195], v[156:159]\n\t"
            "v_mfma_f32_16x16x32_bf16 v[160:163], v[56:59], v[192:195], v[160:163]\n\t"
            "v_mfma_f32_16x16x32_bf16 v[160:163], v[60:63], v[172:175], v[160:163]\n\t"
            "v_mfma_f32_16x16x32_bf16 v[164:167], v[84:87], v[172:175], v[164:167]\n\t"
            "v_mfma_f32_16x16x32_bf16 v[164:167], v[80:83], v[192:195], v[164:167]\n\t"
            "v_mfma_f32_16x16x32_bf16 v[176:179], v[104:107], v[192:195], v[176:179]\n\t"
            "v_mfma_f32_16x16x32_bf16 v[176:179], v[108:111], v[172:175], v[176:179]\n\t"
            "v_mfma_f32_16x16x32_bf16 v[180:183], v[132:135], v[172:175], v[180:183]\n\t"
            "ds_read_b128 v[172:175], v243 offset:6144\n\t"
            "v_mfma_f32_16x16x32_bf16 v[180:183], v[128:131], v[192:195], v[180:183]\n\t"
            "ds_read_b128 v[192:195], v242 offset:6144\n\t"
            "s_waitcnt lgkmcnt(2)\n\t"
            "v_mfma_f32_16x16x32_bf16 v[152:155], v[16:19], v[184:187], v[152:155]\n\t"
            "v_mfma_f32_16x16x32_bf16 v[152:155], v[20:23], v[188:191], v[152:155]\n\t"
            "v_mfma_f32_16x16x32_bf16 v[156:159], v[44:47], v[188:191], v[156:159]\n\t"
            "v_mfma_f32_16x16x32_bf16 v[156:159], v[40:43], v[184:187], v[156:159]\n\t"
            "v_mfma_f32_16x16x32_bf16 v[160:163], v[64:67], v[184:187], v[160:163]\n\t"
            "v_mfma_f32_16x16x32_bf16 v[160:163], v[68:71], v[188:191], v[160:163]\n\t"
            "v_mfma_f32_16x16x32_bf16 v[164:167], v[92:95], v[188:191], v[164:167]\n\t"
            "v_mfma_f32_16x16x32_bf16 v[164:167], v[88:91], v[184:187], v[164:167]\n\t"
            "s_nop 1\n\t"
            "v_cvt_pk_bf16_f32 v152, v152, v153\n\t"
            "v_mfma_f32_16x16x32_bf16 v[176:179], v[112:115], v[184:187], v[176:179]\n\t"
            "v_cvt_pk_bf16_f32 v153, v154, v155\n\t"
            "v_mfma_f32_16x16x32_bf16 v[176:179], v[116:119], v[188:191], v[176:179]\n\t"
            "v_cvt_pk_bf16_f32 v154, v156, v157\n\t"
            "v_cvt_pk_bf16_f32 v155, v158, v159\n\t"
            "v_mfma_f32_16x16x32_bf16 v[180:183], v[140:143], v[188:191], v[180:183]\n\t"
            "ds_read_b128 v[188:191], v243 offset:6272\n\t"
            "v_cvt_pk_bf16_f32 v160, v160, v161\n\t"
            "v_mfma_f32_16x16x32_bf16 v[180:183], v[136:139], v[184:187], v[180:183]\n\t"
            "ds_read_b128 v[184:187], v242 offset:6272\n\t"
            "v_cvt_pk_bf16_f32 v161, v162, v163\n\t"
            "s_waitcnt lgkmcnt(2)\n\t"
            "v_mfma_f32_16x16x32_bf16 v[144:147], v[0:3], v[192:195], v[144:147]\n\t"
            "v_cvt_pk_bf16_f32 v176, v176, v177\n\t"
            "v_cvt_pk_bf16_f32 v177, v178, v179\n\t"
            "v_mfma_f32_16x16x32_bf16 v[144:147], v[4:7], v[172:175], v[144:147]\n\t"
            "v_cvt_pk_bf16_f32 v162, v164, v165\n\t"
            "v_cvt_pk_bf16_f32 v163, v166, v167\n\t"
            "v_mfma_f32_16x16x32_bf16 v[148:151], v[28:31], v[172:175], v[148:151]\n\t"
            "v_cvt_pk_bf16_f32 v178, v180, v181\n\t"
            "v_cvt_pk_bf16_f32 v179, v182, v183\n\t"
            "v_mfma_f32_16x16x32_bf16 v[148:151], v[24:27], v[192:195], v[148:151]\n\t"
            "v_lshlrev_b32 v156, 16, v218\n\t"
            "v_and_b32 v157, 0xffff0000, v218\n\t"
            "v_mfma_f32_16x16x32_bf16 v[168:171], v[48:51], v[192:195], v[168:171]\n\t"
            "v_lshlrev_b32 v158, 16, v219\n\t"
            "v_and_b32 v159, 0xffff0000, v219\n\t"
            "v_mfma_f32_16x16x32_bf16 v[168:171], v[52:55], v[172:175], v[168:171]\n\t"
            "ds_read_b128 v[216:219], v237 offset:6144\n\t"
            "v_lshlrev_b32 v164, 16, v220\n\t"
            "v_mfma_f32_16x16x32_bf16 v[156:159], v[76:79], v[172:175], v[156:159]\n\t"
            "v_and_b32 v165, 0xffff0000, v220\n\t"
            "v_lshlrev_b32 v166, 16, v221\n\t"
            "v_mfma_f32_16x16x32_bf16 v[156:159], v[72:75], v[192:195], v[156:159]\n\t"
            "v_and_b32 v167, 0xffff0000, v221\n\t"
            "v_lshlrev_b32 v180, 16, v222\n\t"
            "v_and_b32 v181, 0xffff0000, v222\n\t"
            "v_mfma_f32_16x16x32_bf16 v[164:167], v[96:99], v[192:195], v[164:167]\n\t"
            "v_lshlrev_b32 v182, 16, v223\n\t"
            "v_and_b32 v183, 0xffff0000, v223\n\t"
            "v_mfma_f32_16x16x32_bf16 v[164:167], v[100:103], v[172:175], v[164:167]\n\t"
            "ds_read_b128 v[220:223], v248 offset:6144\n\t"
            "buffer_store_dwordx4 v[152:155], v196, s[40:43], s45 offen sc1\n\t"
            "v_mfma_f32_16x16x32_bf16 v[180:183], v[124:127], v[172:175], v[180:183]\n\t"
            "ds_read_b128 v[172:175], v243 offset:6400\n\t"
            "v_lshlrev_b32 v152, 16, v224\n\t"
            "v_mfma_f32_16x16x32_bf16 v[180:183], v[120:123], v[192:195], v[180:183]\n\t"
            "ds_read_b128 v[192:195], v242 offset:6400\n\t"
            "buffer_store_dwordx4 v[160:163], v196, s[40:43], s45 offen offset:64 sc1\n\t"
            "s_waitcnt lgkmcnt(4)\n\t"
            "v_mfma_f32_16x16x32_bf16 v[144:147], v[8:11], v[184:187], v[144:147]\n\t"
            "v_and_b32 v153, 0xffff0000, v224\n\t"
            "v_lshlrev_b32 v154, 16, v225\n\t"
            "v_mfma_f32_16x16x32_bf16 v[144:147], v[12:15], v[188:191], v[144:147]\n\t"
            "buffer_store_dwordx4 v[176:179], v196, s[40:43], s45 offen offset:128 sc1\n\t"
            "v_mfma_f32_16x16x32_bf16 v[148:151], v[36:39], v[188:191], v[148:151]\n\t"
            "v_and_b32 v155, 0xffff0000, v225\n\t"
            "v_lshlrev_b32 v160, 16, v226\n\t"
            "v_mfma_f32_16x16x32_bf16 v[148:151], v[32:35], v[184:187], v[148:151]\n\t"
            "v_and_b32 v161, 0xffff0000, v226\n\t"
            "v_lshlrev_b32 v162, 16, v227\n\t"
            "v_mfma_f32_16x16x32_bf16 v[168:171], v[56:59], v[184:187], v[168:171]\n\t"
            "v_and_b32 v163, 0xffff0000, v227\n\t"
            "v_lshlrev_b32 v176, 16, v228\n\t"
            "v_mfma_f32_16x16x32_bf16 v[168:171], v[60:63], v[188:191], v[168:171]\n\t"
            "ds_read_b128 v[224:227], v236 offset:12288\n\t"
            "v_and_b32 v177, 0xffff0000, v228\n\t"
            "v_mfma_f32_16x16x32_bf16 v[156:159], v[84:87], v[188:191], v[156:159]\n\t"
            "v_lshlrev_b32 v178, 16, v229\n\t"
            "v_and_b32 v179, 0xffff0000, v229\n\t"
            "v_mfma_f32_16x16x32_bf16 v[156:159], v[80:83], v[184:187], v[156:159]\n\t"
            "v_mfma_f32_16x16x32_bf16 v[164:167], v[104:107], v[184:187], v[164:167]\n\t"
            "v_mfma_f32_16x16x32_bf16 v[164:167], v[108:111], v[188:191], v[164:167]\n\t"
            "v_mfma_f32_16x16x32_bf16 v[180:183], v[132:135], v[188:191], v[180:183]\n\t"
            "ds_read_b128 v[188:191], v243 offset:12288\n\t"
            "v_mfma_f32_16x16x32_bf16 v[180:183], v[128:131], v[184:187], v[180:183]\n\t"
            "ds_read_b128 v[184:187], v242 offset:12288\n\t"
            "s_waitcnt lgkmcnt(3)\n\t"
            "v_mfma_f32_16x16x32_bf16 v[144:147], v[16:19], v[192:195], v[144:147]\n\t"
            "v_mfma_f32_16x16x32_bf16 v[144:147], v[20:23], v[172:175], v[144:147]\n\t"
            "v_mfma_f32_16x16x32_bf16 v[168:171], v[64:67], v[192:195], v[168:171]\n\t"
            "v_mfma_f32_16x16x32_bf16 v[168:171], v[68:71], v[172:175], v[168:171]\n\t"
            "v_mfma_f32_16x16x32_bf16 v[164:167], v[112:115], v[192:195], v[164:167]\n\t"
            "v_mfma_f32_16x16x32_bf16 v[164:167], v[116:119], v[172:175], v[164:167]\n\t"
            "v_mfma_f32_16x16x32_bf16 v[148:151], v[44:47], v[172:175], v[148:151]\n\t"
            "v_mfma_f32_16x16x32_bf16 v[148:151], v[40:43], v[192:195], v[148:151]\n\t"
            "s_nop 1\n\t"
            "v_cvt_pk_bf16_f32 v144, v144, v145\n\t"
            "v_mfma_f32_16x16x32_bf16 v[156:159], v[92:95], v[172:175], v[156:159]\n\t"
            "v_cvt_pk_bf16_f32 v145, v146, v147\n\t"
            "v_mfma_f32_16x16x32_bf16 v[156:159], v[88:91], v[192:195], v[156:159]\n\t"
            "v_cvt_pk_bf16_f32 v168, v168, v169\n\t"
            "v_cvt_pk_bf16_f32 v169, v170, v171\n\t"
            "v_mfma_f32_16x16x32_bf16 v[180:183], v[140:143], v[172:175], v[180:183]\n\t"
            "ds_read_b128 v[172:175], v243 offset:12416\n\t"
            "v_cvt_pk_bf16_f32 v164, v164, v165\n\t"
            "v_mfma_f32_16x16x32_bf16 v[180:183], v[136:139], v[192:195], v[180:183]\n\t"
            "ds_read_b128 v[192:195], v242 offset:12416\n\t"
            "v_cvt_pk_bf16_f32 v165, v166, v167\n\t"
            "s_waitcnt lgkmcnt(2)\n\t"
            "v_mfma_f32_16x16x32_bf16 v[152:155], v[0:3], v[184:187], v[152:155]\n\t"
            "v_cvt_pk_bf16_f32 v146, v148, v149\n\t"
            "v_cvt_pk_bf16_f32 v147, v150, v151\n\t"
            "v_mfma_f32_16x16x32_bf16 v[152:155], v[4:7], v[188:191], v[152:155]\n\t"
            "v_cvt_pk_bf16_f32 v170, v156, v157\n\t"
            "v_cvt_pk_bf16_f32 v171, v158, v159\n\t"
            "v_mfma_f32_16x16x32_bf16 v[160:163], v[28:31], v[188:191], v[160:163]\n\t"
            "v_cvt_pk_bf16_f32 v166, v180, v181\n\t"
            "v_cvt_pk_bf16_f32 v167, v182, v183\n\t"
            "v_mfma_f32_16x16x32_bf16 v[160:163], v[24:27], v[184:187], v[160:163]\n\t"
            "v_lshlrev_b32 v148, 16, v230\n\t"
            "v_and_b32 v149, 0xffff0000, v230\n\t"
            "v_mfma_f32_16x16x32_bf16 v[176:179], v[48:51], v[184:187], v[176:179]\n\t"
            "v_lshlrev_b32 v150, 16, v231\n\t"
            "v_and_b32 v151, 0xffff0000, v231\n\t"
            "v_mfma_f32_16x16x32_bf16 v[176:179], v[52:55], v[188:191], v[176:179]\n\t"
            "ds_read_b128 v[228:231], v237 offset:12288\n\t"
            "v_lshlrev_b32 v156, 16, v232\n\t"
            "v_mfma_f32_16x16x32_bf16 v[148:151], v[76:79], v[188:191], v[148:151]\n\t"
            "v_and_b32 v157, 0xffff0000, v232\n\t"
            "v_lshlrev_b32 v158, 16, v233\n\t"
            "v_mfma_f32_16x16x32_bf16 v[148:151], v[72:75], v[184:187], v[148:151]\n\t"
            "v_and_b32 v159, 0xffff0000, v233\n\t"
            "v_lshlrev_b32 v180, 16, v234\n\t"
            "v_and_b32 v181, 0xffff0000, v234\n\t"
            "v_mfma_f32_16x16x32_bf16 v[156:159], v[96:99], v[184:187], v[156:159]\n\t"
            "v_lshlrev_b32 v182, 16, v235\n\t"
            "v_and_b32 v183, 0xffff0000, v235\n\t"
            "v_mfma_f32_16x16x32_bf16 v[156:159], v[100:103], v[188:191], v[156:159]\n\t"
            "ds_read_b128 v[232:235], v248 offset:12288\n\t"
            "buffer_store_dwordx4 v[144:147], v197, s[40:43], s45 offen sc1\n\t"
            "v_mfma_f32_16x16x32_bf16 v[180:183], v[124:127], v[188:191], v[180:183]\n\t"
            "ds_read_b128 v[188:191], v243 offset:12544\n\t"
            "v_mfma_f32_16x16x32_bf16 v[180:183], v[120:123], v[184:187], v[180:183]\n\t"
            "ds_read_b128 v[184:187], v242 offset:12544\n\t"
            "buffer_store_dwordx4 v[168:171], v197, s[40:43], s45 offen offset:64 sc1\n\t"
            "s_waitcnt lgkmcnt(4)\n\t"
            "v_mfma_f32_16x16x32_bf16 v[152:155], v[8:11], v[192:195], v[152:155]\n\t"
            "v_mfma_f32_16x16x32_bf16 v[152:155], v[12:15], v[172:175], v[152:155]\n\t"
            "buffer_store_dwordx4 v[164:167], v197, s[40:43], s45 offen offset:128 sc1\n\t"
            "v_mfma_f32_16x16x32_bf16 v[160:163], v[36:39], v[172:175], v[160:163]\n\t"
            "v_mfma_f32_16x16x32_bf16 v[160:163], v[32:35], v[192:195], v[160:163]\n\t"
            "s_waitcnt lgkmcnt(0)\n\t"
            "s_barrier\n\t"
            "s_setprio 3\n\t"
            "v_mfma_f32_16x16x32_bf16 v[176:179], v[56:59], v[192:195], v[176:179]\n\t"
            "v_mfma_f32_16x16x32_bf16 v[176:179], v[60:63], v[172:175], v[176:179]\n\t"
            "v_mfma_f32_16x16x32_bf16 v[148:151], v[84:87], v[172:175], v[148:151]\n\t"
            "v_mfma_f32_16x16x32_bf16 v[148:151], v[80:83], v[192:195], v[148:151]\n\t"
            "v_mfma_f32_16x16x32_bf16 v[156:159], v[104:107], v[192:195], v[156:159]\n\t"
            "v_mfma_f32_16x16x32_bf16 v[156:159], v[108:111], v[172:175], v[156:159]\n\t"
            "v_mfma_f32_16x16x32_bf16 v[152:155], v[16:19], v[184:187], v[152:155]\n\t"
            "v_mfma_f32_16x16x32_bf16 v[152:155], v[20:23], v[188:191], v[152:155]\n\t"
            "v_mfma_f32_16x16x32_bf16 v[160:163], v[44:47], v[188:191], v[160:163]\n\t"
            "v_mfma_f32_16x16x32_bf16 v[160:163], v[40:43], v[184:187], v[160:163]\n\t"
            "v_mfma_f32_16x16x32_bf16 v[180:183], v[132:135], v[172:175], v[180:183]\n\t"
            "v_mfma_f32_16x16x32_bf16 v[180:183], v[128:131], v[192:195], v[180:183]\n\t"
            "v_mfma_f32_16x16x32_bf16 v[176:179], v[64:67], v[184:187], v[176:179]\n\t"
            "v_mfma_f32_16x16x32_bf16 v[176:179], v[68:71], v[188:191], v[176:179]\n\t"
            "s_nop 1\n\t"
            "v_cvt_pk_bf16_f32 v152, v152, v153\n\t"
            "v_mfma_f32_16x16x32_bf16 v[148:151], v[92:95], v[188:191], v[148:151]\n\t"
            "v_cvt_pk_bf16_f32 v153, v154, v155\n\t"
            "v_mfma_f32_16x16x32_bf16 v[148:151], v[88:91], v[184:187], v[148:151]\n\t"
            "v_cvt_pk_bf16_f32 v154, v160, v161\n\t"
            "v_cvt_pk_bf16_f32 v155, v162, v163\n\t"
            "v_mfma_f32_16x16x32_bf16 v[156:159], v[112:115], v[184:187], v[156:159]\n\t"
            "v_cvt_pk_bf16_f32 v176, v176, v177\n\t"
            "v_cvt_pk_bf16_f32 v177, v178, v179\n\t"
            "v_mfma_f32_16x16x32_bf16 v[156:159], v[116:119], v[188:191], v[156:159]\n\t"
            "buffer_store_dwordx4 v[152:155], v198, s[40:43], s45 offen sc1\n\t"
            "v_mfma_f32_16x16x32_bf16 v[180:183], v[140:143], v[188:191], v[180:183]\n\t"
            "v_cvt_pk_bf16_f32 v178, v148, v149\n\t"
            "v_cvt_pk_bf16_f32 v179, v150, v151\n\t"
            "v_mfma_f32_16x16x32_bf16 v[180:183], v[136:139], v[184:187], v[180:183]\n\t"
            "buffer_store_dwordx4 v[176:179], v198, s[40:43], s45 offen offset:64 sc1\n\t"
            "s_nop 1\n\t"
            "v_cvt_pk_bf16_f32 v156, v156, v157\n\t"
            "v_cvt_pk_bf16_f32 v157, v158, v159\n\t"
            "s_nop 2\n\t"
            "v_cvt_pk_bf16_f32 v158, v180, v181\n\t"
            "v_cvt_pk_bf16_f32 v159, v182, v183\n\t"
            "buffer_store_dwordx4 v[156:159], v198, s[40:43], s45 offen offset:128 sc1\n\t"
            "s_add_u32 s45, s46, s27\n\t"
            "s_mov_b32 s46, s47\n\t"
            "s_mov_b32 s47, s68\n\t"
            "s_xor_b32 s48, s48, s71\n\t"
            "s_xor_b32 s67, s67, s71\n\t"
            "s_add_u32 s76, s76, 4\n\t"
            "s_add_u32 s70, s70, 4\n\t"
            "s_lshl_b32 s56, s56, 2\n\t"
            "s_lshr_b32 s49, s26, 30\n\t"
            "s_or_b32 s56, s56, s49\n\t"
            "s_cmp_lt_u32 s46, 0x7000000\n\t"
            "s_cbranch_scc1 .Lwb_loop%=\n\t"
            "s_branch .Lwb_end%=\n\t"
            ".Lwb_spin%=:\n\t"
            "s_cmp_eq_u32 s83, 0\n\t"
            "s_cbranch_scc1 .Lwb_go%=\n\t"
            "s_sub_u32 s83, s83, 1\n\t"
            "s_sleep 2\n\t"
            "v_mov_b32 v247, 0\n\t"
            "ds_read_b32 v247, v247 offset:7856\n\t"
            "s_waitcnt lgkmcnt(0)\n\t"
            "v_readfirstlane_b32 s77, v247\n\t"
            "s_cmp_ge_u32 s77, s70\n\t"
            "s_cbranch_scc0 .Lwb_spin%=\n\t"
            "s_branch .Lwb_go%=\n\t"
            ".Lwb_end%=:\n\t"
            "s_waitcnt lgkmcnt(0)\n\t"
            "s_nop 7\n\t"
            "ds_read_b128 v[196:199], v252\n\t"
            "s_bfe_u32 s57, s56, 0x20004\n\t"
            "s_lshl_b32 s58, s57, 3\n\t"
            "s_lshr_b32 s58, s55, s58\n\t"
            "s_lshl_b64 s[36:37], s[34:35], s58\n\t"
            "s_bitcmp1_b32 s57, 1\n\t"
            "s_cselect_b32 s58, 1177344, 0\n\t"
            "s_bitcmp1_b32 s57, 0\n\t"
            "s_cselect_b32 s57, 2304, 0\n\t"
            "s_add_u32 s57, s57, s58\n\t"
            "v_lshlrev_b32 v152, 16, v200\n\t"
            "v_and_b32 v153, 0xffff0000, v200\n\t"
            "v_lshlrev_b32 v154, 16, v201\n\t"
            "v_and_b32 v155, 0xffff0000, v201\n\t"
            "v_lshlrev_b32 v156, 16, v202\n\t"
            "v_and_b32 v157, 0xffff0000, v202\n\t"
            "v_lshlrev_b32 v158, 16, v203\n\t"
            "v_and_b32 v159, 0xffff0000, v203\n\t"
            "v_lshlrev_b32 v160, 16, v204\n\t"
            "v_and_b32 v161, 0xffff0000, v204\n\t"
            "v_lshlrev_b32 v162, 16, v205\n\t"
            "v_and_b32 v163, 0xffff0000, v205\n\t"
            "v_lshlrev_b32 v164, 16, v206\n\t"
            "v_and_b32 v165, 0xffff0000, v206\n\t"
            "v_lshlrev_b32 v166, 16, v207\n\t"
            "v_and_b32 v167, 0xffff0000, v207\n\t"
            "v_lshlrev_b32 v176, 16, v208\n\t"
            "v_and_b32 v177, 0xffff0000, v208\n\t"
            "v_lshlrev_b32 v178, 16, v209\n\t"
            "v_and_b32 v179, 0xffff0000, v209\n\t"
            "v_lshlrev_b32 v180, 16, v210\n\t"
            "v_and_b32 v181, 0xffff0000, v210\n\t"
            "v_lshlrev_b32 v182, 16, v211\n\t"
            "v_and_b32 v183, 0xffff0000, v211\n\t"
            "v_lshlrev_b32 v144, 16, v212\n\t"
            "v_and_b32 v145, 0xffff0000, v212\n\t"
            "v_lshlrev_b32 v146, 16, v213\n\t"
            "v_and_b32 v147, 0xffff0000, v213\n\t"
            "v_lshlrev_b32 v148, 16, v214\n\t"
            "v_and_b32 v149, 0xffff0000, v214\n\t"
            "v_lshlrev_b32 v150, 16, v215\n\t"
            "v_and_b32 v151, 0xffff0000, v215\n\t"
            "v_lshlrev_b32 v168, 16, v216\n\t"
            "v_and_b32 v169, 0xffff0000, v216\n\t"
            "v_lshlrev_b32 v170, 16, v217\n\t"
            "s_waitcnt lgkmcnt(0)\n\t"
            "s_mov_b64 exec, s[36:37]\n\t"
            "v_add_u32 v196, s57, v196\n\t"
            "s_mov_b64 exec, -1\n\t"
            ".Lwk_t1%=:\n\t"
            "v_mov_b32 v247, 0\n\t"
            "ds_read_b32 v247, v247 offset:7856\n\t"
            "s_waitcnt lgkmcnt(0)\n\t"
            "v_readfirstlane_b32 s77, v247\n\t"
            "s_cmp_ge_u32 s77, s70\n\t"
            "s_cbranch_scc1 .Lwk_t2%=\n\t"
            "s_cmp_eq_u32 s83, 0\n\t"
            "s_cbranch_scc1 .Lwk_t2%=\n\t"
            "s_sub_u32 s83, s83, 1\n\t"
            "s_sleep 1\n\t"
            "s_branch .Lwk_t1%=\n\t"
            ".Lwk_t2%=:\n\t"
            "v_and_b32 v171, 0xffff0000, v217\n\t"
            "ds_read_b128 v[184:187], v242\n\t"
            "ds_read_b128 v[188:191], v243\n\t"
            "ds_read_b128 v[192:195], v242 offset:128\n\t"
            "ds_read_b128 v[172:175], v243 offset:128\n\t"
            "s_waitcnt lgkmcnt(2)\n\t"
            "v_mfma_f32_16x16x32_bf16 v[152:155], v[0:3], v[184:187], v[152:155]\n\t"
            "v_mfma_f32_16x16x32_bf16 v[152:155], v[4:7], v[188:191], v[152:155]\n\t"
            "v_mfma_f32_16x16x32_bf16 v[156:159], v[28:31], v[188:191], v[156:159]\n\t"
            "v_mfma_f32_16x16x32_bf16 v[156:159], v[24:27], v[184:187], v[156:159]\n\t"
            "v_mfma_f32_16x16x32_bf16 v[160:163], v[48:51], v[184:187], v[160:163]\n\t"
            "v_mfma_f32_16x16x32_bf16 v[160:163], v[52:55], v[188:191], v[160:163]\n\t"
            "v_mfma_f32_16x16x32_bf16 v[164:167], v[76:79], v[188:191], v[164:167]\n\t"
            "v_mfma_f32_16x16x32_bf16 v[164:167], v[72:75], v[184:187], v[164:167]\n\t"
            "v_mfma_f32_16x16x32_bf16 v[176:179], v[96:99], v[184:187], v[176:179]\n\t"
            "v_mfma_f32_16x16x32_bf16 v[176:179], v[100:103], v[188:191], v[176:179]\n\t"
            "v_mfma_f32_16x16x32_bf16 v[180:183], v[124:127], v[188:191], v[180:183]\n\t"
            "ds_read_b128 v[188:191], v243 offset:256\n\t"
            "v_mfma_f32_16x16x32_bf16 v[180:183], v[120:123], v[184:187], v[180:183]\n\t"
            "ds_read_b128 v[184:187], v242 offset:256\n\t"
            "s_waitcnt lgkmcnt(2)\n\t"
            "v_mfma_f32_16x16x32_bf16 v[152:155], v[8:11], v[192:195], v[152:155]\n\t"
            "v_mfma_f32_16x16x32_bf16 v[152:155], v[12:15], v[172:175], v[152:155]\n\t"
            "v_mfma_f32_16x16x32_bf16 v[156:159], v[36:39], v[172:175], v[156:159]\n\t"
            "v_mfma_f32_16x16x32_bf16 v[156:159], v[32:35], v[192:195], v[156:159]\n\t"
            "v_mfma_f32_16x16x32_bf16 v[160:163], v[56:59], v[192:195], v[160:163]\n\t"
            "v_mfma_f32_16x16x32_bf16 v[160:163], v[60:63], v[172:175], v[160:163]\n\t"
            "v_mfma_f32_16x16x32_bf16 v[164:167], v[84:87], v[172:175], v[164:167]\n\t"
            "v_mfma_f32_16x16x32_bf16 v[164:167], v[80:83], v[192:195], v[164:167]\n\t"
            "v_mfma_f32_16x16x32_bf16 v[176:179], v[104:107], v[192:195], v[176:179]\n\t"
            "v_mfma_f32_16x16x32_bf16 v[176:179], v[108:111], v[172:175], v[176:179]\n\t"
            "v_mfma_f32_16x16x32_bf16 v[180:183], v[132:135], v[172:175], v[180:183]\n\t"
            "ds_read_b128 v[172:175], v243 offset:6144\n\t"
            "v_mfma_f32_16x16x32_bf16 v[180:183], v[128:131], v[192:195], v[180:183]\n\t"
            "ds_read_b128 v[192:195], v242 offset:6144\n\t"
            "s_waitcnt lgkmcnt(2)\n\t"
            "v_mfma_f32_16x16x32_bf16 v[152:155], v[16:19], v[184:187], v[152:155]\n\t"
            "v_mfma_f32_16x16x32_bf16 v[152:155], v[20:23], v[188:191], v[152:155]\n\t"
            "v_mfma_f32_16x16x32_bf16 v[156:159], v[44:47], v[188:191], v[156:159]\n\t"
            "v_mfma_f32_16x16x32_bf16 v[156:159], v[40:43], v[184:187], v[156:159]\n\t"
            "v_mfma_f32_16x16x32_bf16 v[160:163], v[64:67], v[184:187], v[160:163]\n\t"
            "v_mfma_f32_16x16x32_bf16 v[160:163], v[68:71], v[188:191], v[160:163]\n\t"
            "v_mfma_f32_16x16x32_bf16 v[164:167], v[92:95], v[188:191], v[164:167]\n\t"
            "v_mfma_f32_16x16x32_bf16 v[164:167], v[88:91], v[184:187], v[164:167]\n\t"
            "s_nop 1\n\t"
            "v_cvt_pk_bf16_f32 v152, v152, v153\n\t"
            "v_mfma_f32_16x16x32_bf16 v[176:179], v[112:115], v[184:187], v[176:179]\n\t"
            "v_cvt_pk_bf16_f32 v153, v154, v155\n\t"
            "v_mfma_f32_16x16x32_bf16 v[176:179], v[116:119], v[188:191], v[176:179]\n\t"
            "v_cvt_pk_bf16_f32 v154, v156, v157\n\t"
            "v_cvt_pk_bf16_f32 v155, v158, v159\n\t"
            "v_mfma_f32_16x16x32_bf16 v[180:183], v[140:143], v[188:191], v[180:183]\n\t"
            "ds_read_b128 v[188:191], v243 offset:6272\n\t"
            "v_cvt_pk_bf16_f32 v160, v160, v161\n\t"
            "v_mfma_f32_16x16x32_bf16 v[180:183], v[136:139], v[184:187], v[180:183]\n\t"
            "ds_read_b128 v[184:187], v242 offset:6272\n\t"
            "v_cvt_pk_bf16_f32 v161, v162, v163\n\t"
            "s_waitcnt lgkmcnt(2)\n\t"
            "v_mfma_f32_16x16x32_bf16 v[144:147], v[0:3], v[192:195], v[144:147]\n\t"
            "v_cvt_pk_bf16_f32 v176, v176, v177\n\t"
            "v_cvt_pk_bf16_f32 v177, v178, v179\n\t"
            "v_mfma_f32_16x16x32_bf16 v[144:147], v[4:7], v[172:175], v[144:147]\n\t"
            "v_cvt_pk_bf16_f32 v162, v164, v165\n\t"
            "v_cvt_pk_bf16_f32 v163, v166, v167\n\t"
            "v_mfma_f32_16x16x32_bf16 v[148:151], v[28:31], v[172:175], v[148:151]\n\t"
            "v_cvt_pk_bf16_f32 v178, v180, v181\n\t"
            "v_cvt_pk_bf16_f32 v179, v182, v183\n\t"
            "v_mfma_f32_16x16x32_bf16 v[148:151], v[24:27], v[192:195], v[148:151]\n\t"
            "v_lshlrev_b32 v156, 16, v218\n\t"
            "v_and_b32 v157, 0xffff0000, v218\n\t"
            "v_mfma_f32_16x16x32_bf16 v[168:171], v[48:51], v[192:195], v[168:171]\n\t"
            "v_lshlrev_b32 v158, 16, v219\n\t"
            "v_and_b32 v159, 0xffff0000, v219\n\t"
            "v_mfma_f32_16x16x32_bf16 v[168:171], v[52:55], v[172:175], v[168:171]\n\t"
            "v_lshlrev_b32 v164, 16, v220\n\t"
            "v_and_b32 v165, 0xffff0000, v220\n\t"
            "v_mfma_f32_16x16x32_bf16 v[156:159], v[76:79], v[172:175], v[156:159]\n\t"
            "v_lshlrev_b32 v166, 16, v221\n\t"
            "v_and_b32 v167, 0xffff0000, v221\n\t"
            "v_mfma_f32_16x16x32_bf16 v[156:159], v[72:75], v[192:195], v[156:159]\n\t"
            "v_lshlrev_b32 v180, 16, v222\n\t"
            "v_and_b32 v181, 0xffff0000, v222\n\t"
            "v_mfma_f32_16x16x32_bf16 v[164:167], v[96:99], v[192:195], v[164:167]\n\t"
            "v_lshlrev_b32 v182, 16, v223\n\t"
            "v_and_b32 v183, 0xffff0000, v223\n\t"
            "v_mfma_f32_16x16x32_bf16 v[164:167], v[100:103], v[172:175], v[164:167]\n\t"
            "buffer_store_dwordx4 v[152:155], v196, s[40:43], s45 offen sc1\n\t"
            "v_mfma_f32_16x16x32_bf16 v[180:183], v[124:127], v[172:175], v[180:183]\n\t"
            "ds_read_b128 v[172:175], v243 offset:6400\n\t"
            "v_lshlrev_b32 v152, 16, v224\n\t"
            "v_mfma_f32_16x16x32_bf16 v[180:183], v[120:123], v[192:195], v[180:183]\n\t"
            "ds_read_b128 v[192:195], v242 offset:6400\n\t"
            "buffer_store_dwordx4 v[160:163], v196, s[40:43], s45 offen offset:64 sc1\n\t"
            "s_waitcnt lgkmcnt(2)\n\t"
            "v_mfma_f32_16x16x32_bf16 v[144:147], v[8:11], v[184:187], v[144:147]\n\t"
            "v_and_b32 v153, 0xffff0000, v224\n\t"
            "v_lshlrev_b32 v154, 16, v225\n\t"
            "v_mfma_f32_16x16x32_bf16 v[144:147], v[12:15], v[188:191], v[144:147]\n\t"
            "buffer_store_dwordx4 v[176:179], v196, s[40:43], s45 offen offset:128 sc1\n\t"
            "v_mfma_f32_16x16x32_bf16 v[148:151], v[36:39], v[188:191], v[148:151]\n\t"
            "v_and_b32 v155, 0xffff0000, v225\n\t"
            "v_lshlrev_b32 v160, 16, v226\n\t"
            "v_mfma_f32_16x16x32_bf16 v[148:151], v[32:35], v[184:187], v[148:151]\n\t"
            "v_and_b32 v161, 0xffff0000, v226\n\t"
            "v_lshlrev_b32 v162, 16, v227\n\t"
            "v_mfma_f32_16x16x32_bf16 v[168:171], v[56:59], v[184:187], v[168:171]\n\t"
            "v_and_b32 v163, 0xffff0000, v227\n\t"
            "v_lshlrev_b32 v176, 16, v228\n\t"
            "v_mfma_f32_16x16x32_bf16 v[168:171], v[60:63], v[188:191], v[168:171]\n\t"
            "v_and_b32 v177, 0xffff0000, v228\n\t"
            "v_lshlrev_b32 v178, 16, v229\n\t"
            "v_mfma_f32_16x16x32_bf16 v[156:159], v[84:87], v[188:191], v[156:159]\n\t"
            "v_and_b32 v179, 0xffff0000, v229\n\t"
            "v_mfma_f32_16x16x32_bf16 v[156:159], v[80:83], v[184:187], v[156:159]\n\t"
            "v_mfma_f32_16x16x32_bf16 v[164:167], v[104:107], v[184:187], v[164:167]\n\t"
            "v_mfma_f32_16x16x32_bf16 v[164:167], v[108:111], v[188:191], v[164:167]\n\t"
            "v_mfma_f32_16x16x32_bf16 v[180:183], v[132:135], v[188:191], v[180:183]\n\t"
            "ds_read_b128 v[188:191], v243 offset:12288\n\t"
            "v_mfma_f32_16x16x32_bf16 v[180:183], v[128:131], v[184:187], v[180:183]\n\t"
            "ds_read_b128 v[184:187], v242 offset:12288\n\t"
            "s_waitcnt lgkmcnt(2)\n\t"
            "v_mfma_f32_16x16x32_bf16 v[144:147], v[16:19], v[192:195], v[144:147]\n\t"
            "v_mfma_f32_16x16x32_bf16 v[144:147], v[20:23], v[172:175], v[144:147]\n\t"
            "v_mfma_f32_16x16x32_bf16 v[168:171], v[64:67], v[192:195], v[168:171]\n\t"
            "v_mfma_f32_16x16x32_bf16 v[168:171], v[68:71], v[172:175], v[168:171]\n\t"
            "v_mfma_f32_16x16x32_bf16 v[164:167], v[112:115], v[192:195], v[164:167]\n\t"
            "v_mfma_f32_16x16x32_bf16 v[164:167], v[116:119], v[172:175], v[164:167]\n\t"
            "v_mfma_f32_16x16x32_bf16 v[148:151], v[44:47], v[172:175], v[148:151]\n\t"
            "v_mfma_f32_16x16x32_bf16 v[148:151], v[40:43], v[192:195], v[148:151]\n\t"
            "s_nop 1\n\t"
            "v_cvt_pk_bf16_f32 v144, v144, v145\n\t"
            "v_mfma_f32_16x16x32_bf16 v[156:159], v[92:95], v[172:175], v[156:159]\n\t"
            "v_cvt_pk_bf16_f32 v145, v146, v147\n\t"
            "v_mfma_f32_16x16x32_bf16 v[156:159], v[88:91], v[192:195], v[156:159]\n\t"
            "v_cvt_pk_bf16_f32 v168, v168, v169\n\t"
            "v_cvt_pk_bf16_f32 v169, v170, v171\n\t"
            "v_mfma_f32_16x16x32_bf16 v[180:183], v[140:143], v[172:175], v[180:183]\n\t"
            "ds_read_b128 v[172:175], v243 offset:12416\n\t"
            "v_cvt_pk_bf16_f32 v164, v164, v165\n\t"
            "v_mfma_f32_16x16x32_bf16 v[180:183], v[136:139], v[192:195], v[180:183]\n\t"
            "ds_read_b128 v[192:195], v242 offset:12416\n\t"
            "v_cvt_pk_bf16_f32 v165, v166, v167\n\t"
            "s_waitcnt lgkmcnt(2)\n\t"
            "v_mfma_f32_16x16x32_bf16 v[152:155], v[0:3], v[184:187], v[152:155]\n\t"
            "v_cvt_pk_bf16_f32 v146, v148, v149\n\t"
            "v_cvt_pk_bf16_f32 v147, v150, v151\n\t"
            "v_mfma_f32_16x16x32_bf16 v[152:155], v[4:7], v[188:191], v[152:155]\n\t"
            "v_cvt_pk_bf16_f32 v170, v156, v157\n\t"
            "v_cvt_pk_bf16_f32 v171, v158, v159\n\t"
            "v_mfma_f32_16x16x32_bf16 v[160:163], v[28:31], v[188:191], v[160:163]\n\t"
            "v_cvt_pk_bf16_f32 v166, v180, v181\n\t"
            "v_cvt_pk_bf16_f32 v167, v182, v183\n\t"
            "v_mfma_f32_16x16x32_bf16 v[160:163], v[24:27], v[184:187], v[160:163]\n\t"
            "v_lshlrev_b32 v148, 16, v230\n\t"
            "v_and_b32 v149, 0xffff0000, v230\n\t"
            "v_mfma_f32_16x16x32_bf16 v[176:179], v[48:51], v[184:187], v[176:179]\n\t"
            "v_lshlrev_b32 v150, 16, v231\n\t"
            "v_and_b32 v151, 0xffff0000, v231\n\t"
            "v_mfma_f32_16x16x32_bf16 v[176:179], v[52:55], v[188:191], v[176:179]\n\t"
            "v_lshlrev_b32 v156, 16, v232\n\t"
            "v_and_b32 v157, 0xffff0000, v232\n\t"
            "v_mfma_f32_16x16x32_bf16 v[148:151], v[76:79], v[188:191], v[148:151]\n\t"
            "v_lshlrev_b32 v158, 16, v233\n\t"
            "v_and_b32 v159, 0xffff0000, v233\n\t"
            "v_mfma_f32_16x16x32_bf16 v[148:151], v[72:75], v[184:187], v[148:151]\n\t"
            "v_lshlrev_b32 v180, 16, v234\n\t"
            "v_and_b32 v181, 0xffff0000, v234\n\t"
            "v_mfma_f32_16x16x32_bf16 v[156:159], v[96:99], v[184:187], v[156:159]\n\t"
            "v_lshlrev_b32 v182, 16, v235\n\t"
            "v_and_b32 v183, 0xffff0000, v235\n\t"
            "v_mfma_f32_16x16x32_bf16 v[156:159], v[100:103], v[188:191], v[156:159]\n\t"
            "buffer_store_dwordx4 v[144:147], v197, s[40:43], s45 offen sc1\n\t"
            "v_mfma_f32_16x16x32_bf16 v[180:183], v[124:127], v[188:191], v[180:183]\n\t"
            "ds_read_b128 v[188:191], v243 offset:12544\n\t"
            "v_mfma_f32_16x16x32_bf16 v[180:183], v[120:123], v[184:187], v[180:183]\n\t"
            "ds_read_b128 v[184:187], v242 offset:12544\n\t"
            "buffer_store_dwordx4 v[168:171], v197, s[40:43], s45 offen offset:64 sc1\n\t"
            "s_waitcnt lgkmcnt(2)\n\t"
            "v_mfma_f32_16x16x32_bf16 v[152:155], v[8:11], v[192:195], v[152:155]\n\t"
            "v_mfma_f32_16x16x32_bf16 v[152:155], v[12:15], v[172:175], v[152:155]\n\t"
            "buffer_store_dwordx4 v[164:167], v197, s[40:43], s45 offen offset:128 sc1\n\t"
            "v_mfma_f32_16x16x32_bf16 v[160:163], v[36:39], v[172:175], v[160:163]\n\t"
            "v_mfma_f32_16x16x32_bf16 v[160:163], v[32:35], v[192:195], v[160:163]\n\t"
            "v_mfma_f32_16x16x32_bf16 v[176:179], v[56:59], v[192:195], v[176:179]\n\t"
            "v_mfma_f32_16x16x32_bf16 v[176:179], v[60:63], v[172:175], v[176:179]\n\t"
            "v_mfma_f32_16x16x32_bf16 v[148:151], v[84:87], v[172:175], v[148:151]\n\t"
            "v_mfma_f32_16x16x32_bf16 v[148:151], v[80:83], v[192:195], v[148:151]\n\t"
            "v_mfma_f32_16x16x32_bf16 v[156:159], v[104:107], v[192:195], v[156:159]\n\t"
            "v_mfma_f32_16x16x32_bf16 v[156:159], v[108:111], v[172:175], v[156:159]\n\t"
            "s_waitcnt lgkmcnt(0)\n\t"
            "v_mfma_f32_16x16x32_bf16 v[152:155], v[16:19], v[184:187], v[152:155]\n\t"
            "v_mfma_f32_16x16x32_bf16 v[152:155], v[20:23], v[188:191], v[152:155]\n\t"
            "v_mfma_f32_16x16x32_bf16 v[160:163], v[44:47], v[188:191], v[160:163]\n\t"
            "v_mfma_f32_16x16x32_bf16 v[160:163], v[40:43], v[184:187], v[160:163]\n\t"
            "v_mfma_f32_16x16x32_bf16 v[180:183], v[132:135], v[172:175], v[180:183]\n\t"
            "v_mfma_f32_16x16x32_bf16 v[180:183], v[128:131], v[192:195], v[180:183]\n\t"
            "v_mfma_f32_16x16x32_bf16 v[176:179], v[64:67], v[184:187], v[176:179]\n\t"
            "v_mfma_f32_16x16x32_bf16 v[176:179], v[68:71], v[188:191], v[176:179]\n\t"
            "s_nop 1\n\t"
            "v_cvt_pk_bf16_f32 v152, v152, v153\n\t"
            "v_mfma_f32_16x16x32_bf16 v[148:151], v[92:95], v[188:191], v[148:151]\n\t"
            "v_cvt_pk_bf16_f32 v153, v154, v155\n\t"
            "v_mfma_f32_16x16x32_bf16 v[148:151], v[88:91], v[184:187], v[148:151]\n\t"
            "v_cvt_pk_bf16_f32 v154, v160, v161\n\t"
            "v_cvt_pk_bf16_f32 v155, v162, v163\n\t"
            "v_mfma_f32_16x16x32_bf16 v[156:159], v[112:115], v[184:187], v[156:159]\n\t"
            "v_cvt_pk_bf16_f32 v176, v176, v177\n\t"
            "v_cvt_pk_bf16_f32 v177, v178, v179\n\t"
            "v_mfma_f32_16x16x32_bf16 v[156:159], v[116:119], v[188:191], v[156:159]\n\t"
            "buffer_store_dwordx4 v[152:155], v198, s[40:43], s45 offen sc1\n\t"
            "v_mfma_f32_16x16x32_bf16 v[180:183], v[140:143], v[188:191], v[180:183]\n\t"
            "v_cvt_pk_bf16_f32 v178, v148, v149\n\t"
            "v_cvt_pk_bf16_f32 v179, v150, v151\n\t"
            "v_mfma_f32_16x16x32_bf16 v[180:183], v[136:139], v[184:187], v[180:183]\n\t"
            "buffer_store_dwordx4 v[176:179], v198, s[40:43], s45 offen offset:64 sc1\n\t"
            "s_nop 1\n\t"
            "v_cvt_pk_bf16_f32 v156, v156, v157\n\t"
            "v_cvt_pk_bf16_f32 v157, v158, v159\n\t"
            "s_nop 2\n\t"
            "v_cvt_pk_bf16_f32 v158, v180, v181\n\t"
            "v_cvt_pk_bf16_f32 v159, v182, v183\n\t"
            "buffer_store_dwordx4 v[156:159], v198, s[40:43], s45 offen offset:128 sc1\n\t"
            "s_nop 7\n\t"
            "s_branch .Lwk_pe%=\n\t"
            "wk_conv:\n\t"
            "s_nop 7\n\t"
            "ds_read_b128 v[168:171], v254\n\t"
            "ds_read_b64 v[144:145], v253\n\t"
            "ds_read_b64 v[146:147], v253 offset:96\n\t"
            "ds_read_b64 v[148:149], v253 offset:1744\n\t"
            "ds_read_b64 v[150:151], v253 offset:1840\n\t"
            "ds_read_b128 v[172:175], v254 offset:64\n\t"
            "ds_read_b64 v[152:153], v253 offset:8\n\t"
            "ds_read_b64 v[154:155], v253 offset:104\n\t"
            "ds_read_b64 v[156:157], v253 offset:1752\n\t"
            "ds_read_b64 v[158:159], v253 offset:1848\n\t"
            "ds_read_b64 v[160:161], v253 offset:16\n\t"
            "ds_read_b64 v[162:163], v253 offset:112\n\t"
            "ds_read_b64 v[164:165], v253 offset:1760\n\t"
            "ds_read_b64 v[166:167], v253 offset:1856\n\t"
            "v_mov_b64 v[176:177], 0\n\t"
            "v_mov_b64 v[178:179], 0\n\t"
            "v_mov_b64 v[180:181], 0\n\t"
            "v_mov_b64 v[182:183], 0\n\t"
            "v_mov_b64 v[184:185], 0\n\t"
            "v_mov_b64 v[186:187], 0\n\t"
            "v_mov_b64 v[188:189], 0\n\t"
            "v_mov_b64 v[190:191], 0\n\t"
            "v_add_u32 v236, s67, v250\n\t"
            "v_xor_b32 v237, 16, v236\n\t"
            "v_add_u32 v248, s67, v251\n\t"
            "s_waitcnt lgkmcnt(9)\n\t"
            "v_smfmac_f32_16x16x64_bf16 v[176:179], v[168:171], v[144:151], v255\n\t"
            "ds_read_b128 v[168:171], v254 offset:128\n\t"
            "ds_read_b64 v[144:145], v253 offset:24\n\t"
            "s_waitcnt lgkmcnt(6)\n\t"
            "v_smfmac_f32_16x16x64_bf16 v[180:183], v[172:175], v[152:159], v255\n\t"
            "ds_read_b64 v[146:147], v253 offset:120\n\t"
            "ds_read_b64 v[148:149], v253 offset:1768\n\t"
            "ds_read_b64 v[150:151], v253 offset:1864\n\t"
            "ds_read_b128 v[172:175], v254 offset:192\n\t"
            "ds_read_b64 v[152:153], v253 offset:32\n\t"
            "ds_read_b64 v[154:155], v253 offset:128\n\t"
            "ds_read_b64 v[156:157], v253 offset:1776\n\t"
            "ds_read_b64 v[158:159], v253 offset:1872\n\t"
            "v_cvt_pk_bf16_f32 v176, v176, v177\n\t"
            "v_cvt_pk_bf16_f32 v177, v178, v179\n\t"
            "v_cvt_pk_bf16_f32 v178, v180, v181\n\t"
            "v_cvt_pk_bf16_f32 v179, v182, v183\n\t"
            "v_mfma_f32_4x4x4_16b_bf16 v[196:199], v[176:177], v[176:177], 0\n\t"
            "ds_write_b128 v236, v[176:179]\n\t"
            "v_mfma_f32_16x16x32_bf16 v[192:195], v[238:241], v[176:179], 0\n\t"
            "v_mov_b64 v[176:177], 0\n\t"
            "v_mov_b64 v[180:181], 0\n\t"
            "v_mfma_f32_4x4x4_16b_bf16 v[196:199], v[178:179], v[178:179], v[196:199]\n\t"
            "v_mov_b64 v[178:179], 0\n\t"
            "v_mov_b64 v[182:183], 0\n\t"
            "s_waitcnt lgkmcnt(10)\n\t"
            "v_smfmac_f32_16x16x64_bf16 v[184:187], v[168:171], v[160:167], v255\n\t"
            "ds_read_b128 v[168:171], v254 offset:256\n\t"
            "ds_read_b64 v[160:161], v253 offset:40\n\t"
            "ds_read_b64 v[162:163], v253 offset:136\n\t"
            "ds_read_b64 v[164:165], v253 offset:1784\n\t"
            "ds_read_b64 v[166:167], v253 offset:1880\n\t"
            "s_waitcnt lgkmcnt(10)\n\t"
            "v_smfmac_f32_16x16x64_bf16 v[188:191], v[172:175], v[144:151], v255\n\t"
            "ds_read_b128 v[172:175], v254 offset:320\n\t"
            "ds_read_b64 v[144:145], v253 offset:48\n\t"
            "ds_read_b64 v[146:147], v253 offset:144\n\t"
            "s_cbranch_scc1 .Lwk_np1%=\n\t"
            "s_mov_b64 exec, 0x3ffff\n\t"
            "buffer_load_dwordx4 v252, s[84:87], s89 offen sc1 lds\n\t"
            "s_mov_b64 exec, -1\n\t"
            ".Lwk_np1%=:\n\t"
            "ds_read_b64 v[148:149], v253 offset:1792\n\t"
            "ds_read_b64 v[150:151], v253 offset:1888\n\t"
            "v_cvt_pk_bf16_f32 v184, v184, v185\n\t"
            "v_cvt_pk_bf16_f32 v185, v186, v187\n\t"
            "s_nop 0\n\t"
            "v_cvt_pk_bf16_f32 v186, v188, v189\n\t"
            "v_cvt_pk_bf16_f32 v187, v190, v191\n\t"
            "v_mfma_f32_4x4x4_16b_bf16 v[196:199], v[184:185], v[184:185], v[196:199]\n\t"
            "s_waitcnt lgkmcnt(14)\n\t"
            "ds_write_b128 v237, v[184:187]\n\t"
            "v_mfma_f32_16x16x32_bf16 v[192:195], v[238:241], v[184:187], v[192:195]\n\t"
            "v_mov_b64 v[184:185], 0\n\t"
            "v_mov_b64 v[188:189], 0\n\t"
            "v_mfma_f32_4x4x4_16b_bf16 v[196:199], v[186:187], v[186:187], v[196:199]\n\t"
            "v_mov_b64 v[186:187], 0\n\t"
            "v_mov_b64 v[190:191], 0\n\t"
            "s_waitcnt lgkmcnt(10)\n\t"
            "v_smfmac_f32_16x16x64_bf16 v[176:179], v[168:171], v[152:159], v255\n\t"
            "ds_read_b128 v[168:171], v254 offset:384\n\t"
            "ds_read_b64 v[152:153], v253 offset:56\n\t"
            "ds_read_b64 v[154:155], v253 offset:152\n\t"
            "ds_read_b64 v[156:157], v253 offset:1800\n\t"
            "ds_read_b64 v[158:159], v253 offset:1896\n\t"
            "s_waitcnt lgkmcnt(10)\n\t"
            "v_smfmac_f32_16x16x64_bf16 v[180:183], v[172:175], v[160:167], v255\n\t"
            "ds_read_b128 v[172:175], v254 offset:448\n\t"
            "ds_read_b64 v[160:161], v253 offset:64\n\t"
            "ds_read_b64 v[162:163], v253 offset:160\n\t"
            "ds_read_b64 v[164:165], v253 offset:1808\n\t"
            "ds_read_b64 v[166:167], v253 offset:1904\n\t"
            "v_cvt_pk_bf16_f32 v176, v176, v177\n\t"
            "v_cvt_pk_bf16_f32 v177, v178, v179\n\t"
            "s_nop 0\n\t"
            "v_cvt_pk_bf16_f32 v178, v180, v181\n\t"
            "v_cvt_pk_bf16_f32 v179, v182, v183\n\t"
            "v_mfma_f32_4x4x4_16b_bf16 v[196:199], v[176:177], v[176:177], v[196:199]\n\t"
            "s_waitcnt lgkmcnt(14)\n\t"
            "ds_write_b128 v236, v[176:179] offset:128\n\t"
            "v_mfma_f32_16x16x32_bf16 v[192:195], v[238:241], v[176:179], v[192:195]\n\t"
            "v_mov_b64 v[176:177], 0\n\t"
            "v_mov_b64 v[180:181], 0\n\t"
            "v_mfma_f32_4x4x4_16b_bf16 v[196:199], v[178:179], v[178:179], v[196:199]\n\t"
            "v_mov_b64 v[178:179], 0\n\t"
            "v_mov_b64 v[182:183], 0\n\t"
            "s_waitcnt lgkmcnt(10)\n\t"
            "v_smfmac_f32_16x16x64_bf16 v[184:187], v[168:171], v[144:151], v255\n\t"
            "ds_read_b128 v[168:171], v254 offset:512\n\t"
            "ds_read_b64 v[144:145], v253 offset:72\n\t"
            "ds_read_b64 v[146:147], v253 offset:168\n\t"
            "ds_read_b64 v[148:149], v253 offset:1816\n\t"
            "ds_read_b64 v[150:151], v253 offset:1912\n\t"
            "s_waitcnt lgkmcnt(10)\n\t"
            "v_smfmac_f32_16x16x64_bf16 v[188:191], v[172:175], v[152:159], v255\n\t"
            "ds_read_b128 v[172:175], v254 offset:576\n\t"
            "ds_read_b64 v[152:153], v253 offset:80\n\t"
            "ds_read_b64 v[154:155], v253 offset:176\n\t"
            "ds_read_b64 v[156:157], v253 offset:1824\n\t"
            "ds_read_b64 v[158:159], v253 offset:1920\n\t"
            "v_cvt_pk_bf16_f32 v184, v184, v185\n\t"
            "v_cvt_pk_bf16_f32 v185, v186, v187\n\t"
            "s_cbranch_scc1 .Lwk_np0%=\n\t"
            "s_mov_b64 exec, 0x3ffff\n\t"
            "buffer_load_dwordx4 v252, s[84:87], s89 offen sc1 lds\n\t"
            "s_mov_b64 exec, -1\n\t"
            ".Lwk_np0%=:\n\t"
            "s_nop 0\n\t"
            "v_cvt_pk_bf16_f32 v186, v188, v189\n\t"
            "v_cvt_pk_bf16_f32 v187, v190, v191\n\t"
            "v_mfma_f32_4x4x4_16b_bf16 v[196:199], v[184:185], v[184:185], v[196:199]\n\t"
            "s_waitcnt lgkmcnt(14)\n\t"
            "ds_write_b128 v237, v[184:187] offset:128\n\t"
            "v_mfma_f32_16x16x32_bf16 v[192:195], v[238:241], v[184:187], v[192:195]\n\t"
            "v_mov_b64 v[184:185], 0\n\t"
            "v_mov_b64 v[188:189], 0\n\t"
            "v_mfma_f32_4x4x4_16b_bf16 v[196:199], v[186:187], v[186:187], v[196:199]\n\t"
            "v_mov_b64 v[186:187], 0\n\t"
            "v_mov_b64 v[190:191], 0\n\t"
            "s_waitcnt lgkmcnt(10)\n\t"
            "v_smfmac_f32_16x16x64_bf16 v[176:179], v[168:171], v[160:167], v255\n\t"
            "ds_read_b128 v[168:171], v254 offset:640\n\t"
            "ds_read_b64 v[160:161], v253 offset:88\n\t"
            "ds_read_b64 v[162:163], v253 offset:184\n\t"
            "ds_read_b64 v[164:165], v253 offset:1832\n\t"
            "ds_read_b64 v[166:167], v253 offset:1928\n\t"
            "s_waitcnt lgkmcnt(10)\n\t"
            "v_smfmac_f32_16x16x64_bf16 v[180:183], v[172:175], v[144:151], v255\n\t"
            "ds_read_b128 v[172:175], v254 offset:704\n\t"
            "v_cvt_pk_bf16_f32 v176, v176, v177\n\t"
            "v_cvt_pk_bf16_f32 v177, v178, v179\n\t"
            "s_nop 1\n\t"
            "v_mfma_f32_4x4x4_16b_bf16 v[196:199], v[176:177], v[176:177], v[196:199]\n\t"
            "s_nop 1\n\t"
            "v_cvt_pk_bf16_f32 v178, v180, v181\n\t"
            "v_cvt_pk_bf16_f32 v179, v182, v183\n\t"
            "s_nop 1\n\t"
            "v_mfma_f32_4x4x4_16b_bf16 v[196:199], v[178:179], v[178:179], v[196:199]\n\t"
            "ds_write_b128 v236, v[176:179] offset:256\n\t"
            "v_mfma_f32_16x16x32_bf16 v[192:195], v[238:241], v[176:179], v[192:195]\n\t"
            "s_waitcnt lgkmcnt(6)\n\t"
            "v_smfmac_f32_16x16x64_bf16 v[184:187], v[168:171], v[152:159], v255\n\t"
            "s_waitcnt lgkmcnt(1)\n\t"
            "v_smfmac_f32_16x16x64_bf16 v[188:191], v[172:175], v[160:167], v255\n\t"
            "s_nop 5\n\t"
            "v_cvt_pk_bf16_f32 v184, v184, v185\n\t"
            "v_cvt_pk_bf16_f32 v185, v186, v187\n\t"
            "s_nop 1\n\t"
            "v_mfma_f32_4x4x4_16b_bf16 v[196:199], v[184:185], v[184:185], v[196:199]\n\t"
            "v_cvt_pk_bf16_f32 v186, v188, v189\n\t"
            "v_cvt_pk_bf16_f32 v187, v190, v191\n\t"
            "s_nop 1\n\t"
            "v_mfma_f32_4x4x4_16b_bf16 v[196:199], v[186:187], v[186:187], v[196:199]\n\t"
            "v_mfma_f32_16x16x32_bf16 v[192:195], v[238:241], v[184:187], v[192:195]\n\t"
            "ds_write_b128 v237, v[184:187] offset:256\n\t"
            "s_nop 6\n\t"
            "v_cndmask_b32_e64 v193, v196, v197, s[60:61]\n\t"
            "v_cndmask_b32_e64 v193, v193, v198, s[62:63]\n\t"
            "v_cndmask_b32_e64 v193, v193, v199, s[64:65]\n\t"
            "ds_write_b64 v248, v[192:193] offset:18816\n\t"
            "s_waitcnt lgkmcnt(0)\n\t"
            "s_nop 7\n\t"
            "s_setpc_b64 vcc\n\t"
            ".Lwk_pe%=:\n\t"
            : B_SIO, "+{v247}"(l16_), "+{v228}"(q0), "+{v229}"(q1), "+{v232}"(q2), "+{v233}"(q3), "+{v234}"(q4)
            : "{v242}"(oE_), "{v243}"(oO_), "{v244}"(pR0_), "{v245}"(pR1_), "{v246}"(pR2_), "{v250}"(pXw_), "{v251}"(pSTw_), "{v252}"(aCA_), "{v253}"(aB_), "{v254}"(aA_), "{v255}"(idx_), B_SIN, "{s44}"(ARR_OFF + blockIdx.x * 8), "{s[80:81]}"(mrow), "{s82}"(wave == 6 && cnt == 21 ? REC_OFF + (start - 20 * ((blockIdx.x & 7) * 32 + xi)) * 1280 : 0)
            : "v144", "v145", "v146", "v147", "v148", "v149", "v150", "v151", "v152", "v153", "v154", "v155", "v156", "v157", "v158", "v159", "v160", "v161", "v162", "v163", "v164", "v165", "v166", "v167", "v168", "v169", "v170", "v171", "v172", "v173", "v174", "v175", "v176", "v177", "v178", "v179", "v180", "v181", "v182", "v183", "v184", "v185", "v186", "v187", "v188", "v189", "v190", "v191", "v192", "v193", "v194", "v195", "v196", "v197", "v198", "v199", "v236", "v237", "v248", "v249", "v0", "v1", "v2", "v3", "v4", "v5", "v6", "v7", "v8", "v9", "v10", "v11", "v12", "v13", "v14", "v15", "v16", "v17", "v18", "v19", "v20", "v21", "v22", "v23", "v24", "v25", "v26", "v27", "v28", "v29", "v30", "v31", "v32", "v33", "v34", "v35", "v36", "v37", "v38", "v39", "v40", "v41", "v42", "v43", "v44", "v45", "v46", "v47", "v48", "v49", "v50", "v51", "v52", "v53", "v54", "v55", "v56", "v57", "v58", "v59", "v60", "v61", "v62", "v63", "v64", "v65", "v66", "v67", "v68", "v69", "v70", "v71", "v72", "v73", "v74", "v75", "v76", "v77", "v78", "v79", "v80", "v81", "v82", "v83", "v84", "v85", "v86", "v87", "v88", "v89", "v90", "v91", "v92", "v93", "v94", "v95", "v96", "v97", "v98", "v99", "v100", "v101", "v102", "v103", "v104", "v105", "v106", "v107", "v108", "v109", "v110", "v111", "v112", "v113", "v114", "v115", "v116", "v117", "v118", "v119", "v120", "v121", "v122", "v123", "v124", "v125", "v126", "v127", "v128", "v129", "v130", "v131", "v132", "v133", "v134", "v135", "v136", "v137", "v138", "v139", "v140", "v141", "v142", "v143", "v200", "v201", "v202", "v204", "v205", "v206", "v207", "v208", "v209", "v210", "v211", "v212", "v213", "v214", "v215", "v216", "v217", "v218", "v219", "v220", "v221", "v222", "v223", "v224", "v225", "v226", "v227", "v230", "v231", "v235", B_SCLOB);
        }
        __builtin_amdgcn_endpgm();
    }
}

extern "C" void kernel_launch(void* const* d_in, const int* in_sizes, int n_in,
                              void* d_out, int out_size, void* d_ws, size_t ws_size,
                              hipStream_t stream) {
    const unsigned short* x = (const unsigned short*)d_in[0];
    const unsigned short* dw = (const unsigned short*)d_in[1];
    const unsigned short* wqkv = (const unsigned short*)d_in[2];
    const unsigned short* wo = (const unsigned short*)d_in[3];
    unsigned short* out = (unsigned short*)d_out;
    u32x4* wf = (u32x4*)d_ws;
    win_attn_kernel<<<GRID, 512, 0, stream>>>(x, dw, wqkv, wo, wf, out);
}
```

```cpp
#include <hip/hip_runtime.h>
#include <hip/hip_bf16.h>
#include <stdint.h>

typedef short bf16x8 __attribute__((ext_vector_type(8)));
typedef float f32x4 __attribute__((ext_vector_type(4)));
typedef float f32x2 __attribute__((ext_vector_type(2)));
typedef unsigned u32x2 __attribute__((ext_vector_type(2)));
typedef unsigned u32x4 __attribute__((ext_vector_type(4)));

constexpr int IMG = 511;
constexpr int CH = 192;
constexpr int NWX = 73;
constexpr int NWIN = NWX * NWX;
constexpr int GRID = 256;
constexpr int ROWBYTES = IMG * CH * 2;
constexpr int XBIAS = (IMG + 1) * CH * 2;
constexpr unsigned OOB = 0x40000000u;

constexpr int FLAG_OFF = 7856;
constexpr int X_OFF = 32768;
constexpr int X1_OFF = 53632;
constexpr int O_OFF = 74496;
constexpr int V_OFF = 92928;
constexpr int KQL_OFF = 111360;
constexpr int OL_OFF = 129024;
constexpr int AT_OFF = 137472;
constexpr int CT_OFF = AT_OFF + 19584;
constexpr int RS_OFF = CT_OFF + 4096;
constexpr int RSX_OFF = 162816;
constexpr int WL_OFF = RS_OFF + 2304 + 64;
constexpr int LDS_BYTES = WL_OFF + 128;
#define RS_BASE (RS_OFF + wave * 384)
constexpr int STL_OFF = O_OFF + 12288;
constexpr int DW_OFF = O_OFF + 8448;
constexpr int CP_OFF = X1_OFF + 16384;
#define LX_OFF(i) ((i) < 2 ? CT_OFF + 1024 + (i) * 384 : KQL_OFF + ((i) - 2) * 800)
constexpr int PF_OFF = CT_OFF + 2048;
#define LB_OFF (w < 3 ? V_OFF + w * 13952 : X1_OFF)
#define LPOS(wc) (((wc) & 1) ? 0 : 6)
constexpr unsigned CROW = 0x060a0230u;
constexpr unsigned CLANE = 0x00132C3Fu;

constexpr unsigned long long TOKMAP[6] = {0x20001c190f2a2202ull, 0x052517121f061a13ull, 0x181e08151d0b260cull, 0x1b2b2903102d0901ull, 0x04162e0e2824142cull, 0x110a072127232f0dull};
constexpr unsigned long long ROWMAP[8] = {0x08321b240f1c0006ull, 0x23310e2a09170d14ull, 0x210c2f030a273418ull, 0x1d2b11221f043739ull, 0x3234363a103e192dull, 0x291a20022c3f1605ull, 0x303a3c2628122e15ull, 0x39253d1e01070b13ull};

constexpr int WF_BYTES = 8 * 36 * 1024;
constexpr int FLG_OFF = WF_BYTES;
constexpr int ARR_OFF = FLG_OFF + 4096;
constexpr int EPO_OFF = ARR_OFF + 2048;
constexpr int CTR_OFF = EPO_OFF + 128;
constexpr int REC_OFF = WF_BYTES + 8192;
constexpr int WS_BYTES = REC_OFF + 209 * 1280;
constexpr int SC1 = 16;

#define MFMA32(a, b, c) __builtin_amdgcn_mfma_f32_16x16x32_bf16((a), (b), (c), 0, 0, 0)
#define BC8(v) __builtin_bit_cast(bf16x8, (v))
#define BAR() asm volatile("s_waitcnt lgkmcnt(0)\n\ts_barrier" ::: "memory")
#define SB() __builtin_amdgcn_sched_barrier(0)
#define OPQ(x) asm volatile("" : "+v"(x))
#define ZERO4 ((f32x4){0.f, 0.f, 0.f, 0.f})

typedef __bf16 nbf16x2 __attribute__((ext_vector_type(2)));
__device__ __forceinline__ unsigned pk2(float a, float b) {
    f32x2 v = {a, b};
    nbf16x2 r = __builtin_convertvector(v, nbf16x2);
    return __builtin_bit_cast(unsigned, r);
}
__device__ __forceinline__ float bflo(unsigned u) { return __uint_as_float(u << 16); }
__device__ __forceinline__ float bfhi(unsigned u) { return __uint_as_float(u & 0xffff0000u); }

__device__ __forceinline__ float bf2f(unsigned short v) { return __uint_as_float((unsigned)v << 16); }
__device__ __forceinline__ unsigned byte_of(unsigned long long a, unsigned long long b, int i) {
    return (unsigned)(((i & 8) ? b : a) >> (8 * (i & 7))) & 0xFFu;
}
#define M24(a, b) ((unsigned)__mul24((int)(a), (int)(b)))
__device__ __forceinline__ void seq_to_win(int seq, int& wy, int& wx) {
    const bool last = seq >= 5110;
    const int s = last ? 35 : M24(seq, 28729) >> 22, r = seq - M24(146, s);
    const int kx = last ? M24(r, 171) >> 10 : r >> 2, pos = r - M24(last ? 6 : 4, kx);
    const bool low = last && pos >= (kx == 36 ? 2 : 4);
    wy = low ? 72 : 2 * s + (pos & 1);
    wx = 2 * kx + (low ? (kx == 36 ? 0 : pos - 4) : pos >> 1);
}
__device__ __forceinline__ int win_to_seq(int wy, int wx) {
    const int kx = wx >> 1;
    return M24(146, wy == 72 ? 35 : wy >> 1) + M24(wy >= 70 ? 6 : 4, kx) + (wy == 72 ? (kx == 36 ? 2 : 4 + (wx & 1)) : 2 * (wx & 1) + (wy & 1));
}
__device__ __forceinline__ int block_of(int wy, int wx) { return wy >= 70 ? 1295 + (wx & ~1) + (wy == 72) : M24(37, wy >> 1) + (wx >> 1); }
__device__ __forceinline__ void block_pos(int b, int& ky, int& kx) {
    const int s = M24(b, 1772) >> 16, l = b - 1295;
    kx = l >= 0 ? l >> 1 : b - M24(37, s);
    ky = l >= 0 ? 70 + 2 * (l & 1) : 2 * s;
}
__device__ __forceinline__ unsigned win_org(int wy, int wx) { return M24(wy, 7 * ROWBYTES) + M24(wx, 7 * CH * 2); }
__device__ __forceinline__ unsigned wl_entry(int wy, int wx) {
    return win_org(wy, wx) | (wy == 0 ? 0x08000000u : 0u) | (wx == 0 ? 0x10000000u : 0u) |
           (wx == NWX - 1 ? 0x20000000u : 0u) | ((unsigned)(wx & 1) << 30) | ((unsigned)(wy & 1) << 31);
}
__device__ __forceinline__ unsigned dma_off(int lane) {
    const int col = (lane * 43) >> 8;
    return (unsigned)(col * (CH * 2 - 96) + 16 * lane) | (col == 0 ? 0x10000000u : 0u) | (col == 8 ? 0x20000000u : 0u);
}

#if defined(__HIP_DEVICE_COMPILE__)
#define NOPK __attribute__((target("no-packed-fp32-ops")))
#else
#define NOPK
#endif
__global__ __launch_bounds__(512, 2) NOPK void win_attn_kernel(const unsigned short* __restrict__ x,
                                                          const unsigned short* __restrict__ dw, const unsigned short* __restrict__ wqkv,
                                                          const unsigned short* __restrict__ wo, u32x4* wf,
                                                          unsigned short* __restrict__ out) {
    __shared__ __attribute__((aligned(16))) char smem[LDS_BYTES];

    asm volatile("wk_code_begin:");
    const int tid = threadIdx.x;
    const int wave = __builtin_amdgcn_readfirstlane(tid >> 6);
    const int w = wave & 3;
    const unsigned ldsb = (unsigned)(size_t)smem;
    {
        unsigned long long pc;
        unsigned len, rel;
        asm volatile("s_getpc_b64 %0\n.Lwk_here%=:\n\ts_mov_b32 %1, wk_code_end-wk_code_begin\n\ts_mov_b32 %2, .Lwk_here%=-wk_code_begin" : "=s"(pc), "=s"(len), "=s"(rel));
        pc -= rel;
        const u32x4 cr = {(unsigned)pc, (unsigned)(pc >> 32) & 0xffffu, len, 0x00020000u};
        asm volatile("s_mov_b32 m0, %0\n\ts_nop 0\n\tbuffer_load_dwordx4 %1, %2, %3 offen lds"
                     :: "s"(ldsb + X1_OFF + 14336 + (w & 1) * 1024), "v"((tid & 63) * 16), "s"(cr), "s"((wave * 32 + (blockIdx.x >> 3)) * 1024) : "m0", "memory");
    }
    *(unsigned*)(smem + PF_OFF + tid * 4) = 0x9E3779B9u * (tid + 1);
    *(unsigned*)(smem + (tid < 192 ? AT_OFF + 18816 + tid * 4 : FLAG_OFF + (tid & 3) * 4)) = 0u;
    BAR();
    asm volatile("" :: "s"(x), "s"(dw), "s"(wqkv), "s"(wo), "s"(wf));
    const auto wrsrc = __builtin_amdgcn_make_buffer_rsrc((void*)wf, 0, WS_BYTES, 0x00020000);
    unsigned tag_lo = 0, tag_hi = 0;
#define TAG_READY                                                                                  \
    {                                                                                              \
        unsigned long long tag_;                                                                   \
        asm volatile("s_load_dwordx2 %0, %1, %2\n\ts_waitcnt lgkmcnt(0)" : "=s"(tag_) : "s"(wf), "n"(EPO_OFF) : "memory"); \
        tag_lo = (unsigned)tag_ + 1u; tag_hi = (unsigned)(tag_ >> 32);                             \
    }
    const int pb = blockIdx.x;
    if (pb < 36) {
        const int lane = tid & 63, n = lane & 15, g = lane >> 4;
        const bool isO = wave >= 6;
        const int fr = isO ? 216 + 2 * pb + (wave - 6) : 6 * pb + wave;
        const int tile = fr / 6, ks = fr - 6 * tile, tw = tile / 6, mdt = tile - 6 * tw;
        const int col = isO ? 96 * (tw - 6) + 32 * (mdt >> 1) + 8 * (n >> 2) + 4 * (mdt & 1) + (n & 3)
                            : (tw < 4 ? (mdt < 3 ? 192 : -48) + 48 * tw : 96 * tw) + 16 * mdt + n;
        const int rb = isO ? 384 : 1152;
        const auto rr = __builtin_amdgcn_make_buffer_rsrc((void*)(isO ? wo : wqkv), 0, 192 * rb, 0x00020000);
        const int vo = (48 * (g >> 1) + 8 * (g & 1)) * rb + 2 * col;
        float v[8];
#pragma unroll
        for (int j = 0; j < 8; ++j) v[j] = bf2f(__builtin_amdgcn_raw_buffer_load_b16(rr, vo, (96 * (ks & 1) + 16 * (ks >> 1) + j) * rb, 0));
        TAG_READY
        float* red = (float*)smem;
        red[n * 36 + wave * 4 + g] = ((v[0] + v[1]) + (v[2] + v[3])) + ((v[4] + v[5]) + (v[6] + v[7]));
        BAR();
        float mean = 0.f;
#pragma unroll
        for (int i = 0; i < 6; ++i) {
            const f32x4 p = *(const f32x4*)(red + n * 36 + 4 * i);
            mean += (p[0] + p[1]) + (p[2] + p[3]);
        }
        mean = isO ? 0.f : mean * (1.f / 192.f);
        u32x4 o;
#pragma unroll
        for (int j = 0; j < 4; ++j) o[j] = pk2(v[2 * j] - mean, v[2 * j + 1] - mean);
        __builtin_amdgcn_raw_buffer_store_b128(o, wrsrc, lane * 16, fr * 1024, SC1);
        if (pb == 0 && tid == 448) __builtin_amdgcn_raw_buffer_store_b32(0u, wrsrc, CTR_OFF, 0, SC1);
        asm volatile("s_waitcnt vmcnt(0)" ::: "memory");
        BAR();
        if (tid < 8) __builtin_amdgcn_raw_buffer_store_b64((u32x2){tag_lo, tag_hi}, wrsrc, FLG_OFF + tid * 512 + pb * 8, 0, SC1);
    }
    const int xi = blockIdx.x >> 3, nshort = (int)(0x0506060606060606ull >> (8 * (blockIdx.x & 7))) & 63;
    const int start = 672 * (blockIdx.x & 7) - ((int)(0x2a241e18120c0600ull >> (8 * (blockIdx.x & 7))) & 63) + 21 * xi - (xi < nshort ? xi : nshort);
    const int cnt = xi < nshort ? 20 : 21;

    const auto xrsrc = __builtin_amdgcn_make_buffer_rsrc((void*)((const char*)x - XBIAS), 0, IMG * IMG * CH * 2 + XBIAS, 0x00020000);
#define LANE_SETUP                                                                                 \
    int lane;                                                                                      \
    asm volatile("v_mbcnt_lo_u32_b32 %0, -1, 0\n\tv_mbcnt_hi_u32_b32 %0, -1, %0" : "=v"(lane));    \
    __builtin_assume(lane >= 0 && lane < 64);                                                      \
    const int g = lane >> 4, l15 = lane & 15, sw = l15 >> 1;                                       \
    int fE = X_OFF + l15 * 384 + ((g ^ sw) << 4);                                                  \
    OPQ(fE);

#define DMA_SLICE(en)                                                                              \
        {                                                                                          \
            const unsigned vo_ = dma_off(lane) & (0x401fffffu | ((en) & 0x38000000u));             \
            const unsigned so_ = ((en) & 0x7ffffffu) + 96 * w;                                     \
            _Pragma("unroll") for (int r = 0; r < 9; ++r) {                                        \
                unsigned m0_ = ldsb + w * 8192 + r * 872;                                          \
                unsigned sr_ = (r == 0 && ((en) & 0x08000000u)) ? OOB : so_ + r * ROWBYTES;        \
                asm volatile("s_mov_b32 m0, %0\n\ts_mov_b32 exec_hi, 0x3fffff\n\tbuffer_load_dwordx4 %1, %2, %3 offen lds\n\ts_mov_b32 exec_hi, -1" \
                             :: "s"(m0_), "v"(vo_), "s"(xrsrc), "s"(sr_) : "m0", "memory");        \
            }                                                                                      \
        }
#define XPAT(r) (((r) < 48 ? (r) : 48) * 384 + (((2 * w) ^ (((r) >> 1) & 7)) << 4))
#define SPAT(r) ((w >> 1) * 1024 + (w & 1) * 8 + (r) * 16)
#define CONV_CONSTS(var) CONV_ROWS(var) CONV_OPS
#define CONV_ROWS(var) \
            const unsigned long long rlo = g == 0 ? ROWMAP[0] : (g == 1 ? ROWMAP[2] : (g == 2 ? ROWMAP[4] : ROWMAP[6])); \
            const unsigned long long rhi = g == 0 ? ROWMAP[1] : (g == 1 ? ROWMAP[3] : (g == 2 ? ROWMAP[5] : ROWMAP[7])); \
            int xrow = byte_of(rlo, rhi, l15); \
            if (lane == (int)((CLANE >> (8 * (var))) & 0xFF)) xrow = 48; \
            if (lane == 63) xrow = (CROW >> (8 * (var))) & 0xFF; \
            const unsigned pXw_ = XPAT(xrow); \
            const unsigned pSTw_ = (w >> 1) * 1024 + (w & 1) * 8 + xrow * 16; \
            const int oy = (0x5320 >> (4 * (l15 >> 2))) & 15, ox = (0x5320 >> (4 * (l15 & 3))) & 15; \
            const unsigned aB_ = ldsb + w * 8192 + (oy + (g >> 1)) * 872 + (ox + 2 * (g & 1)) * 96;
#define CONV_OPS \
            const int dyi = g - (l15 >> 3); \
            const unsigned aA_ = ldsb + AT_OFF + (l15 & 3) * 16 + \
                                 ((dyi < 0 || dyi > 2) ? 18816 : (dyi == 0 ? 0 : (dyi == 1 ? 6208 : 12544)) + ((l15 >> 2) & 1) * 3072 + w * 768); \
            const unsigned idx_ = (l15 & 3) * 0x5555u; \
            const unsigned se = (l15 == 4 * g) ? 0x3F803F80u : 0u; \
            const u32x4 sel4 = {se, se, se, se};

    unsigned en0, en1;
    {
        LANE_SETUP
        const int px = (lane * 43) >> 8;

        int wy, wx;
        seq_to_win(start + (lane < cnt ? lane : cnt - 1), wy, wx);
        const unsigned en = wl_entry(wy, wx);
        en0 = __builtin_amdgcn_readlane(en, 0);
        en1 = __builtin_amdgcn_readlane(en, 1);
        if (wave >= 4) {
            const int bk = block_of(wy, wx);
            const int key0 = __builtin_amdgcn_readlane(bk, 0), nblk = __builtin_amdgcn_readlane(bk, 63) - key0 + 1;
            const int b = l15 < nblk ? l15 : 0;
            int ky, kx;
            block_pos(key0 + b, ky, kx);
            const unsigned org = l15 < nblk ? (win_org(ky, 2 * kx) + (5 * ROWBYTES + 5 * CH * 2 + XBIAS)) | (kx == 36 ? OOB : 0u) : OOB + 0x8000000u;
            const unsigned lo = (px & 3) * (CH * 2) + (lane - 6 * px) * 16, keep = (px & 2) ? ~0u : ~OOB;
#pragma unroll
            for (int p = 0; p < 4; ++p) {
                if (p < 3 || nblk > 6) {
                    const unsigned oa = __builtin_amdgcn_readlane(org, 2 * p), ob = __builtin_amdgcn_readlane(org, 2 * p + 1);
                    const unsigned vo_ = ((lane < 24 ? oa : ob) & keep) + lo;
#pragma unroll
                    for (int r = 0; r < 4; ++r) {
                        unsigned m0_ = ldsb + LB_OFF + (4 * p + r) * 872;
                        asm volatile("s_mov_b32 m0, %0\n\ts_mov_b32 exec_hi, 0xffff\n\tbuffer_load_dwordx4 %1, %2, %3 offen lds\n\ts_mov_b32 exec_hi, -1"
                                     :: "s"(m0_), "v"(vo_), "s"(xrsrc), "s"(96 * w + r * ROWBYTES) : "m0", "memory");
                    }
                }
            }
            SB();
            const int ly = ky + (g >> 1), lx = 2 * kx + (g & 1), li = win_to_seq(ly, lx) - start;
            const bool ok = ly < NWX && lx < NWX && li >= 0 && li < cnt && l15 < nblk;
            const int row = ok ? li : 47;
            unsigned* cp = (unsigned*)(smem + CP_OFF + (w * 64 + lane) * 16);
            *(u32x2*)cp = (u32x2){(unsigned)XPAT(row), SPAT(row) + (unsigned)(STL_OFF - O_OFF - 18816)};
            cp[2] = ldsb + LB_OFF + (4 * (b >> 1) + (g >> 1)) * 872 + (b & 1) * 384 + (g & 1) * 192;
        } else {
            {
                const auto dr = __builtin_amdgcn_make_buffer_rsrc((void*)dw, 0, 9 * 384, 0x00020000);
                unsigned m0_ = ldsb + DW_OFF + w * 864, vo_ = px * 288 + lane * 16;
                asm volatile("s_mov_b32 m0, %0\n\ts_mov_b32 exec_hi, 0x3fffff\n\tbuffer_load_dwordx4 %1, %2, %3 offen lds\n\ts_mov_b32 exec_hi, -1"
                             :: "s"(m0_), "v"(vo_), "s"(dr), "s"(96 * w) : "m0", "memory");
            }
            DMA_SLICE(en0)
            SB();
            if (wave == 2) {
                u32x4 so;
#pragma unroll
                for (int e = 0; e < 3; ++e) {
                    const int tk = byte_of(TOKMAP[2 * e], TOKMAP[2 * e + 1], l15);
                    const int ty = (tk * 37) >> 8, tx = tk - 7 * ty;
                    so[e] = (unsigned)(ty * ROWBYTES + tx * (CH * 2)) + 16 * g;
                }
                so[3] = dma_off(lane);
                *(u32x4*)(smem + CT_OFF + lane * 16) = so;
            }
            if (wave == 1 && lane < 32) *(unsigned*)(smem + WL_OFF + lane * 4) = lane < 20 ? en : 0x7ffffffu;
            if (wave == 1 && lane == 20) *(unsigned*)(smem + PF_OFF + 520) = en;
            asm volatile("s_waitcnt vmcnt(9)" ::: "memory");
            if (lane < 48) {
                unsigned short dv[9];
#pragma unroll
                for (int i = 0; i < 9; ++i) dv[i] = *(const unsigned short*)(smem + DW_OFF + w * 864 + i * 96 + lane * 2);
#pragma unroll
                for (int dy = 0; dy < 3; ++dy) {
                    const unsigned a = dv[3 * dy], b2 = dv[3 * dy + 1] | (dy == 1 ? 0x3F800000u : 0u), c = dv[3 * dy + 2];
                    char* p = smem + AT_OFF + (dy == 0 ? 0 : (dy == 1 ? 6208 : 12544)) + w * 768 + lane * 16;
                    *(u32x4*)p = (u32x4){a, b2, c, 0u};
                    *(u32x4*)(p + 3072) = (u32x4){0u, a, b2, c};
                }
            }
            asm volatile("s_mov_b64 exec, 1\n\tds_add_u32 %0, %1\n\ts_mov_b64 exec, -1" :: "v"(ldsb + FLAG_OFF + 8), "v"(1) : "memory");
        }
    }
    const unsigned long long mrow = 0x0001000100010001ull * ((1u << (cnt - 16)) - 1u);
#define KARGS                                                                                      \
    unsigned long long xb, wb, ob;                                                                 \
    asm volatile("s_load_dwordx2 %0, %3, 0x0\n\ts_load_dwordx2 %1, %3, 0x20\n\ts_load_dwordx2 %2, %3, 0x28\n\ts_waitcnt lgkmcnt(0)" \
                 : "=&s"(xb), "=&s"(wb), "=&s"(ob) : "s"(__builtin_amdgcn_kernarg_segment_ptr()) : "memory"); \
    xb -= XBIAS;                                                                                   \
    const u32x4 wr = {(unsigned)wb, (unsigned)(wb >> 32) & 0xffffu, (unsigned)WS_BYTES, 0x00020000u}; \
    const u32x4 orr = {(unsigned)ob, (unsigned)(ob >> 32) & 0xffffu, (unsigned)(IMG * IMG * CH * 2), 0x00020000u}; \
    const u32x4 xr = {(unsigned)xb, (unsigned)(xb >> 32) & 0xffffu, (unsigned)(IMG * IMG * CH * 2 + XBIAS), 0x00020000u};
    const int tk_ = (xi + 4) / 6;
    const unsigned touch = wave * 36864 + ((xi + 4 - 6 * tk_) * 6 + tk_) * 1024;
#define B_SCALARS \
            CONV_CONSTS(0) \
            const unsigned aCA_ = ldsb + CT_OFF + lane * 16; \
            KARGS \
            unsigned s45 = OOB, s46 = (en0 & 0x7ffffffu) + 96 * w, s47 = (en1 & 0x7ffffffu) + 96 * w; \
            unsigned s48 = ldsb + X_OFF, s67 = ldsb + X1_OFF, s76 = ldsb + WL_OFF + 8, s70 = 0; \
            unsigned s56 = __builtin_amdgcn_readfirstlane((en0 >> 30) * 4 + (en1 >> 30)); \
            unsigned s69 = 0x401fffffu | (en1 & 0x38000000u), s79 = (en1 & 0x08000000u) ? OOB : s47, s59 = ldsb + LX_OFF(1) + 32 * w; \
            unsigned cp_ = ldsb + CP_OFF + (w * 64 + lane) * 16; \
            unsigned long long xp = 0, sp = 0; \
            _Pragma("unroll") for (int v = 0; v < 4; ++v) { \
                const unsigned r = (CROW >> (8 * v)) & 0xFF; \
                xp |= (unsigned long long)XPAT(r) << (16 * v); \
                sp |= (unsigned long long)SPAT(r) << (16 * v); \
            }
#define B_SIO "+{s45}"(s45), "+{s46}"(s46), "+{s47}"(s47), "+{s48}"(s48), "+{s67}"(s67), "+{s76}"(s76), "+{s56}"(s56), "+{s70}"(s70), "+{s69}"(s69), "+{s79}"(s79), "+{s59}"(s59), "+{v203}"(cp_)
#define B_SIN "{v[238:241]}"(sel4), "{s[40:43]}"(orr), "{s[72:75]}"(xr), "{s39}"(0x401fffffu), "{s[28:31]}"(wr), "{s33}"(wave * 36864), "{s78}"(96 * w), \
              "{s27}"(96 * w - 384), "{s[50:51]}"(xp), "{s[52:53]}"(sp), "{s54}"(CLANE), "{s55}"(CROW & 0x0F0F0F0Fu), "{s[34:35]}"(0x0001000100010001ull), \
              "{s[60:61]}"(0x2222222222222222ull), "{s[62:63]}"(0x4444444444444444ull), "{s[64:65]}"(0x8888888888888888ull), \
              "{s66}"(ldsb + w * 8192), "{s71}"((ldsb + X_OFF) ^ (ldsb + X1_OFF))
#define B_SCLOB "s83", "s84", "s26", "s77", "s68", "s36", "s37", "s38", "s57", "s58", "s49", "vcc", "scc", "m0", "memory"

    if (wave < 4) {
        LANE_SETUP
        const int dW = X_OFF + l15 * 384 + (((2 * w + (g >> 1)) ^ sw) << 4) + 8 * (g & 1);
        const int vB = V_OFF + w * 4608 + 1024 + lane * 8, rsR = RS_BASE + l15 * 4;
        {
            const unsigned rsb = ldsb + RS_BASE;
            const unsigned pE_ = fE - X_OFF, aO_ = ldsb + dW + (O_OFF - X_OFF), sel_ = (l15 & 3) ? 0u : 1u, aVB_ = ldsb + vB;
            unsigned pST_ = lane * 16, aPLw_ = rsb + lane * 2, aRSr_ = ldsb + rsR, aRSw_ = rsb + lane * 4;
            unsigned g16_ = (l15 & 3) ? ldsb + AT_OFF + 18816 : g * 16, g8_ = (l15 & 3) ? ldsb + AT_OFF + 18816 : g * 8;
            unsigned loVL_ = ((2 * w + (l15 >> 3)) << 4) + (l15 & 7) * 2, loOL_ = g * 128;
            unsigned on = (l15 & 3) ? 0u : 0x3F803F80u;
            CONV_CONSTS(en0 >> 30)
            unsigned c0 = lane == (int)((CLANE >> (8 * (en0 >> 30))) & 0xFF) ? LX_OFF(0) - X_OFF + 32 * w : pXw_, c1 = pSTw_, c2 = lane * 16, c3 = aB_, c4 = aA_, c5 = idx_;
            unsigned c6 = se, c7 = se, c8 = se, c9 = se, s89 = FLG_OFF + (blockIdx.x & 7) * 512, s46 = wave * 36864;
            unsigned s48 = ldsb + X_OFF, s70 = 0;
            unsigned s72 = ldsb + KQL_OFF + w * 192, s73 = ldsb + OL_OFF, s74 = ldsb + KQL_OFF + 784, s77 = ldsb + OL_OFF, s76 = ldsb + KQL_OFF + 768 + w * 4;
            KARGS
            asm volatile(
            "s_load_dwordx2 s[82:83], s[84:85], 301056\n\t"
            "s_mov_b32 m0, 159104\n\t"
            "s_mov_b32 s67, 32768\n\t"
            "s_mov_b32 s60, 0x22222222\n\t"
            "s_mov_b32 s61, 0x22222222\n\t"
            "s_mov_b32 s62, 0x44444444\n\t"
            "s_mov_b32 s63, 0x44444444\n\t"
            "s_mov_b32 s64, 0x88888888\n\t"
            "s_mov_b32 s65, 0x88888888\n\t"
            "s_waitcnt vmcnt(0) lgkmcnt(0)\n\t"
            "s_add_u32 s82, s82, 1\n\t"
            "s_cmp_lg_u32 s80, 16\n\t"
            "s_cbranch_scc1 .Lwa_p0%=\n\t"
            "s_mov_b64 exec, 0x3ffff\n\t"
            "buffer_load_dwordx4 v252, s[84:87], s89 offen sc1 lds\n\t"
            "s_mov_b64 exec, -1\n\t"
            ".Lwa_p0%=:\n\t"
            "s_call_b64 vcc, wk_conv\n\t"
            "v_min_u32 v146, 0x230, v252\n\t"
            "v_lshrrev_b32 v146, 1, v146\n\t"
            "v_add_u32 v146, 159104, v146\n\t"
            "s_mov_b32 s81, 0x10000\n\t"
            "v_mov_b32 v240, v200\n\t"
            "v_mov_b32 v241, v200\n\t"
            "v_mov_b32 v242, v200\n\t"
            "v_mov_b32 v243, v200\n\t"
            "v_mov_b32 v248, v204\n\t"
            "v_mov_b32 v249, v205\n\t"
            "v_mov_b32 v250, v206\n\t"
            "v_mov_b32 v251, v207\n\t"
            "v_mov_b32 v253, v209\n\t"
            "v_mov_b32 v254, v210\n\t"
            "v_mov_b32 v255, v211\n\t"
            ".Lwa_p1%=:\n\t"
            "ds_read_b64 v[144:145], v146\n\t"
            "s_waitcnt lgkmcnt(0)\n\t"
            "v_cmp_ne_u32 vcc, s82, v144\n\t"
            "v_cmp_ne_u32 s[60:61], s83, v145\n\t"
            "s_or_b64 vcc, vcc, s[60:61]\n\t"
            "s_cbranch_scc0 .Lwa_p3%=\n\t"
            "s_sub_u32 s81, s81, 1\n\t"
            "s_cbranch_scc1 .Lwa_p3%=\n\t"
            "s_cmp_lg_u32 s80, 16\n\t"
            "s_cbranch_scc1 .Lwa_p2%=\n\t"
            "s_mov_b64 exec, 0x3ffff\n\t"
            "buffer_load_dwordx4 v252, s[84:87], s89 offen sc1 lds\n\t"
            "s_mov_b64 exec, -1\n\t"
            "s_waitcnt vmcnt(24)\n\t"
            ".Lwa_p2%=:\n\t"
            "s_sleep 3\n\t"
            "s_branch .Lwa_p1%=\n\t"
            ".Lwa_p3%=:\n\t"
            "v_mov_b32 v252, v208\n\t"
            "v_mov_b32 v144, s82\n\t"
            "v_mov_b32 v145, s83\n\t"
            "v_mov_b32 v146, 159616\n\t"
            "ds_write_b64 v146, v[144:145]\n\t"
            "s_mov_b32 s60, 0xffff0000\n\t"
            "s_mov_b32 s61, 0x0\n\t"
            "s_mov_b32 s62, 0x0\n\t"
            "s_mov_b32 s63, 0xffff\n\t"
            "s_mov_b32 s64, 0x0\n\t"
            "s_mov_b32 s65, 0xffff0000\n\t"
            "s_mov_b32 s66, 0xffffffff\n\t"
            "s_mov_b32 s67, 0x1ffff\n\t"
            "buffer_load_dwordx4 v[140:143], v248, s[84:87], s88 offen sc1\n\t"
            "s_add_u32 s49, s46, 0\n\t"
            "buffer_load_dwordx4 v[0:3], v248, s[84:87], s49 offen sc1\n\t"
            "s_add_u32 s49, s46, 6144\n\t"
            "buffer_load_dwordx4 v[24:27], v248, s[84:87], s49 offen sc1\n\t"
            "s_add_u32 s49, s46, 12288\n\t"
            "buffer_load_dwordx4 v[48:51], v248, s[84:87], s49 offen sc1\n\t"
            "s_add_u32 s49, s46, 18432\n\t"
            "buffer_load_dwordx4 v[72:75], v248, s[84:87], s49 offen sc1\n\t"
            "s_add_u32 s49, s46, 24576\n\t"
            "buffer_load_dwordx4 v[96:99], v248, s[84:87], s49 offen sc1\n\t"
            "s_add_u32 s49, s46, 30720\n\t"
            "buffer_load_dwordx4 v[120:123], v248, s[84:87], s49 offen sc1\n\t"
            "s_add_u32 s49, s46, 1024\n\t"
            "buffer_load_dwordx4 v[4:7], v248, s[84:87], s49 offen sc1\n\t"
            "s_add_u32 s49, s46, 7168\n\t"
            "buffer_load_dwordx4 v[28:31], v248, s[84:87], s49 offen sc1\n\t"
            "s_add_u32 s49, s46, 13312\n\t"
            "buffer_load_dwordx4 v[52:55], v248, s[84:87], s49 offen sc1\n\t"
            "s_add_u32 s49, s46, 19456\n\t"
            "buffer_load_dwordx4 v[76:79], v248, s[84:87], s49 offen sc1\n\t"
            "s_add_u32 s49, s46, 25600\n\t"
            "buffer_load_dwordx4 v[100:103], v248, s[84:87], s49 offen sc1\n\t"
            "s_add_u32 s49, s46, 31744\n\t"
            "buffer_load_dwordx4 v[124:127], v248, s[84:87], s49 offen sc1\n\t"
            "s_add_u32 s49, s46, 2048\n\t"
            "buffer_load_dwordx4 v[8:11], v248, s[84:87], s49 offen sc1\n\t"
            "s_add_u32 s49, s46, 8192\n\t"
            "buffer_load_dwordx4 v[32:35], v248, s[84:87], s49 offen sc1\n\t"
            "s_add_u32 s49, s46, 14336\n\t"
            "buffer_load_dwordx4 v[56:59], v248, s[84:87], s49 offen sc1\n\t"
            "s_add_u32 s49, s46, 20480\n\t"
            "buffer_load_dwordx4 v[80:83], v248, s[84:87], s49 offen sc1\n\t"
            "s_add_u32 s49, s46, 26624\n\t"
            "buffer_load_dwordx4 v[104:107], v248, s[84:87], s49 offen sc1\n\t"
            "s_add_u32 s49, s46, 32768\n\t"
            "buffer_load_dwordx4 v[128:131], v248, s[84:87], s49 offen sc1\n\t"
            "s_add_u32 s49, s46, 3072\n\t"
            "buffer_load_dwordx4 v[12:15], v248, s[84:87], s49 offen sc1\n\t"
            "s_add_u32 s49, s46, 9216\n\t"
            "buffer_load_dwordx4 v[36:39], v248, s[84:87], s49 offen sc1\n\t"
            "s_add_u32 s49, s46, 15360\n\t"
            "buffer_load_dwordx4 v[60:63], v248, s[84:87], s49 offen sc1\n\t"
            "s_add_u32 s49, s46, 21504\n\t"
            "buffer_load_dwordx4 v[84:87], v248, s[84:87], s49 offen sc1\n\t"
            "s_add_u32 s49, s46, 27648\n\t"
            "buffer_load_dwordx4 v[108:111], v248, s[84:87], s49 offen sc1\n\t"
            "s_add_u32 s49, s46, 33792\n\t"
            "buffer_load_dwordx4 v[132:135], v248, s[84:87], s49 offen sc1\n\t"
            "s_add_u32 s49, s46, 4096\n\t"
            "buffer_load_dwordx4 v[16:19], v248, s[84:87], s49 offen sc1\n\t"
            "s_add_u32 s49, s46, 10240\n\t"
            "buffer_load_dwordx4 v[40:43], v248, s[84:87], s49 offen sc1\n\t"
            "s_add_u32 s49, s46, 16384\n\t"
            "buffer_load_dwordx4 v[64:67], v248, s[84:87], s49 offen sc1\n\t"
            "s_add_u32 s49, s46, 22528\n\t"
            "buffer_load_dwordx4 v[88:91], v248, s[84:87], s49 offen sc1\n\t"
            "s_add_u32 s49, s46, 28672\n\t"
            "buffer_load_dwordx4 v[112:115], v248, s[84:87], s49 offen sc1\n\t"
            "s_add_u32 s49, s46, 34816\n\t"
            "buffer_load_dwordx4 v[136:139], v248, s[84:87], s49 offen sc1\n\t"
            "s_add_u32 s49, s46, 5120\n\t"
            "buffer_load_dwordx4 v[20:23], v248, s[84:87], s49 offen sc1\n\t"
            "s_add_u32 s49, s46, 11264\n\t"
            "buffer_load_dwordx4 v[44:47], v248, s[84:87], s49 offen sc1\n\t"
            "s_add_u32 s49, s46, 17408\n\t"
            "buffer_load_dwordx4 v[68:71], v248, s[84:87], s49 offen sc1\n\t"
            "s_add_u32 s49, s46, 23552\n\t"
            "buffer_load_dwordx4 v[92:95], v248, s[84:87], s49 offen sc1\n\t"
            "s_add_u32 s49, s46, 29696\n\t"
            "buffer_load_dwordx4 v[116:119], v248, s[84:87], s49 offen sc1\n\t"
            "s_add_u32 s49, s46, 35840\n\t"
            "buffer_load_dwordx4 v[140:143], v248, s[84:87], s49 offen sc1\n\t"
            "s_waitcnt lgkmcnt(0)\n\t"
            "s_barrier\n\t"
            "s_setprio 2\n\t"
            "v_add_u32 v235, s71, v248\n\t"
            "v_add_u32 v232, s71, v244\n\t"
            "v_xor_b32 v233, 64, v232\n\t"
            "v_subrev_u32 v239, s75, v251\n\t"
            "v_mul_u32_u24 v239, 200, v239\n\t"
            "v_lshrrev_b32 v234, 3, v255\n\t"
            "v_add3_u32 v234, v234, v239, s72\n\t"
            "ds_read_b128 v[228:231], v235 offset:12288\n\t"
            "ds_read_b128 v[144:147], v232\n\t"
            "ds_read_b128 v[148:151], v233\n\t"
            "ds_read_b128 v[204:207], v232 offset:6144\n\t"
            "ds_read_b128 v[208:211], v233 offset:6144\n\t"
            "ds_read_b128 v[152:155], v232 offset:128\n\t"
            "ds_read_b128 v[156:159], v233 offset:128\n\t"
            "ds_read_b128 v[212:215], v232 offset:6272\n\t"
            "ds_read_b128 v[224:227], v233 offset:6272\n\t"
            "ds_read_b128 v[160:163], v232 offset:256\n\t"
            "ds_read_b128 v[164:167], v233 offset:256\n\t"
            "s_waitcnt vmcnt(24)\n\t"
            "s_waitcnt lgkmcnt(10)\n\t"
            "v_add_f32 v236, v228, v230\n\t"
            "v_add_f32 v237, v229, v231\n\t"
            "s_waitcnt lgkmcnt(8)\n\t"
            "v_mfma_f32_16x16x32_bf16 v[168:171], v[0:3], v[144:147], 0\n\t"
            "ds_read_b128 v[228:231], v235 offset:13312\n\t"
            "v_lshrrev_b32 v235, 4, v255\n\t"
            "v_mfma_f32_16x16x32_bf16 v[168:171], v[4:7], v[148:151], v[168:171]\n\t"
            "v_add3_u32 v235, v235, v239, s72\n\t"
            "v_mfma_f32_16x16x32_bf16 v[176:179], v[48:51], v[144:147], 0\n\t"
            "v_mfma_f32_16x16x32_bf16 v[176:179], v[52:55], v[148:151], v[176:179]\n\t"
            "v_mfma_f32_16x16x32_bf16 v[172:175], v[24:27], v[144:147], 0\n\t"
            "v_mfma_f32_16x16x32_bf16 v[172:175], v[28:31], v[148:151], v[172:175]\n\t"
            "v_mfma_f32_16x16x32_bf16 v[180:183], v[72:75], v[144:147], 0\n\t"
            "v_mfma_f32_16x16x32_bf16 v[180:183], v[76:79], v[148:151], v[180:183]\n\t"
            "s_waitcnt lgkmcnt(0)\n\t"
            "v_add_f32 v228, v228, v230\n\t"
            "v_add_f32 v236, v236, v228\n\t"
            "v_mfma_f32_16x16x32_bf16 v[184:187], v[96:99], v[144:147], 0\n\t"
            "v_add_f32 v229, v229, v231\n\t"
            "v_mul_f32 v236, 0x3baaaaab, v236\n\t"
            "v_mfma_f32_16x16x32_bf16 v[184:187], v[100:103], v[148:151], v[184:187]\n\t"
            "v_add_f32 v237, v237, v229\n\t"
            "v_mul_f32_e64 v236, v236, -v236\n\t"
            "v_mfma_f32_16x16x32_bf16 v[188:191], v[120:123], v[144:147], 0\n\t"
            "v_fmac_f32 v236, 0x3baaaaab, v237\n\t"
            "v_max_f32 v236, 0, v236\n\t"
            "v_mfma_f32_16x16x32_bf16 v[188:191], v[124:127], v[148:151], v[188:191]\n\t"
            "v_add_f32 v236, 0x3727c5ac, v236\n\t"
            "v_rsq_f32 v236, v236\n\t"
            "v_mfma_f32_16x16x32_bf16 v[192:195], v[0:3], v[204:207], 0\n\t"
            "v_mfma_f32_16x16x32_bf16 v[192:195], v[4:7], v[208:211], v[192:195]\n\t"
            "ds_write_b32 v249, v236\n\t"
            "v_mfma_f32_16x16x32_bf16 v[196:199], v[24:27], v[204:207], 0\n\t"
            "ds_read_b32 v236, v251\n\t"
            "ds_read_b32 v237, v251 offset:64\n\t"
            "v_mfma_f32_16x16x32_bf16 v[196:199], v[28:31], v[208:211], v[196:199]\n\t"
            "v_mfma_f32_16x16x32_bf16 v[200:203], v[48:51], v[204:207], 0\n\t"
            "v_mfma_f32_16x16x32_bf16 v[200:203], v[52:55], v[208:211], v[200:203]\n\t"
            "v_mfma_f32_16x16x32_bf16 v[216:219], v[72:75], v[204:207], 0\n\t"
            "v_mfma_f32_16x16x32_bf16 v[216:219], v[76:79], v[208:211], v[216:219]\n\t"
            "v_mfma_f32_16x16x32_bf16 v[220:223], v[96:99], v[204:207], 0\n\t"
            "v_mfma_f32_16x16x32_bf16 v[220:223], v[100:103], v[208:211], v[220:223]\n\t"
            "v_mfma_f32_16x16x32_bf16 v[228:231], v[120:123], v[204:207], 0\n\t"
            "ds_read_b128 v[204:207], v232 offset:6400\n\t"
            "v_add_u32 v232, s48, v244\n\t"
            "v_mfma_f32_16x16x32_bf16 v[228:231], v[124:127], v[208:211], v[228:231]\n\t"
            "ds_read_b128 v[208:211], v233 offset:6400\n\t"
            "s_waitcnt vmcnt(12)\n\t"
            "v_mfma_f32_16x16x32_bf16 v[168:171], v[8:11], v[152:155], v[168:171]\n\t"
            "v_xor_b32 v233, 64, v232\n\t"
            "v_mfma_f32_16x16x32_bf16 v[168:171], v[12:15], v[156:159], v[168:171]\n\t"
            "v_mfma_f32_16x16x32_bf16 v[176:179], v[56:59], v[152:155], v[176:179]\n\t"
            "v_mfma_f32_16x16x32_bf16 v[176:179], v[60:63], v[156:159], v[176:179]\n\t"
            "v_mfma_f32_16x16x32_bf16 v[172:175], v[32:35], v[152:155], v[172:175]\n\t"
            "v_mfma_f32_16x16x32_bf16 v[172:175], v[36:39], v[156:159], v[172:175]\n\t"
            "v_mfma_f32_16x16x32_bf16 v[180:183], v[80:83], v[152:155], v[180:183]\n\t"
            "v_mfma_f32_16x16x32_bf16 v[180:183], v[84:87], v[156:159], v[180:183]\n\t"
            "v_mfma_f32_16x16x32_bf16 v[184:187], v[104:107], v[152:155], v[184:187]\n\t"
            "v_mfma_f32_16x16x32_bf16 v[184:187], v[108:111], v[156:159], v[184:187]\n\t"
            "v_mfma_f32_16x16x32_bf16 v[188:191], v[128:131], v[152:155], v[188:191]\n\t"
            "v_mfma_f32_16x16x32_bf16 v[188:191], v[132:135], v[156:159], v[188:191]\n\t"
            "v_mfma_f32_16x16x32_bf16 v[192:195], v[8:11], v[212:215], v[192:195]\n\t"
            "v_mfma_f32_16x16x32_bf16 v[192:195], v[12:15], v[224:227], v[192:195]\n\t"
            "v_mfma_f32_16x16x32_bf16 v[196:199], v[32:35], v[212:215], v[196:199]\n\t"
            "v_mfma_f32_16x16x32_bf16 v[196:199], v[36:39], v[224:227], v[196:199]\n\t"
            "v_mfma_f32_16x16x32_bf16 v[200:203], v[56:59], v[212:215], v[200:203]\n\t"
            "v_mfma_f32_16x16x32_bf16 v[200:203], v[60:63], v[224:227], v[200:203]\n\t"
            "v_mfma_f32_16x16x32_bf16 v[216:219], v[80:83], v[212:215], v[216:219]\n\t"
            "v_mfma_f32_16x16x32_bf16 v[216:219], v[84:87], v[224:227], v[216:219]\n\t"
            "v_mfma_f32_16x16x32_bf16 v[220:223], v[104:107], v[212:215], v[220:223]\n\t"
            "v_mfma_f32_16x16x32_bf16 v[220:223], v[108:111], v[224:227], v[220:223]\n\t"
            "v_mfma_f32_16x16x32_bf16 v[228:231], v[128:131], v[212:215], v[228:231]\n\t"
            "ds_read_b128 v[212:215], v232 offset:256\n\t"
            "v_mfma_f32_16x16x32_bf16 v[228:231], v[132:135], v[224:227], v[228:231]\n\t"
            "ds_read_b128 v[224:227], v233 offset:256\n\t"
            "s_waitcnt vmcnt(0)\n\t"
            "v_mfma_f32_16x16x32_bf16 v[168:171], v[16:19], v[160:163], v[168:171]\n\t"
            "v_mfma_f32_16x16x32_bf16 v[168:171], v[20:23], v[164:167], v[168:171]\n\t"
            "v_mfma_f32_16x16x32_bf16 v[176:179], v[64:67], v[160:163], v[176:179]\n\t"
            "v_mfma_f32_16x16x32_bf16 v[176:179], v[68:71], v[164:167], v[176:179]\n\t"
            "v_mfma_f32_16x16x32_bf16 v[180:183], v[88:91], v[160:163], v[180:183]\n\t"
            "v_mfma_f32_16x16x32_bf16 v[180:183], v[92:95], v[164:167], v[180:183]\n\t"
            "v_mfma_f32_16x16x32_bf16 v[172:175], v[40:43], v[160:163], v[172:175]\n\t"
            "v_mfma_f32_16x16x32_bf16 v[172:175], v[44:47], v[164:167], v[172:175]\n\t"
            "s_waitcnt lgkmcnt(5)\n\t"
            "s_nop 0\n\t"
            "v_mul_f32 v168, v236, v168\n\t"
            "v_mfma_f32_16x16x32_bf16 v[184:187], v[112:115], v[160:163], v[184:187]\n\t"
            "v_mul_f32 v169, v236, v169\n\t"
            "v_mul_f32 v170, v236, v170\n\t"
            "v_mfma_f32_16x16x32_bf16 v[184:187], v[116:119], v[164:167], v[184:187]\n\t"
            "v_mul_f32 v176, v236, v176\n\t"
            "v_mul_f32 v177, v236, v177\n\t"
            "v_mfma_f32_16x16x32_bf16 v[188:191], v[136:139], v[160:163], v[188:191]\n\t"
            "v_mul_f32 v171, v236, v171\n\t"
            "v_mul_f32 v178, v236, v178\n\t"
            "v_mfma_f32_16x16x32_bf16 v[188:191], v[140:143], v[164:167], v[188:191]\n\t"
            "v_mul_f32 v179, v236, v179\n\t"
            "v_cvt_pk_bf16_f32 v168, v168, v169\n\t"
            "s_waitcnt lgkmcnt(2)\n\t"
            "v_mfma_f32_16x16x32_bf16 v[192:195], v[16:19], v[204:207], v[192:195]\n\t"
            "v_cvt_pk_bf16_f32 v176, v176, v177\n\t"
            "v_cvt_pk_bf16_f32 v180, v180, v181\n\t"
            "v_mfma_f32_16x16x32_bf16 v[192:195], v[20:23], v[208:211], v[192:195]\n\t"
            "v_mul_f32 v172, v236, v172\n\t"
            "v_mul_f32 v173, v236, v173\n\t"
            "v_mfma_f32_16x16x32_bf16 v[196:199], v[40:43], v[204:207], v[196:199]\n\t"
            "v_mul_f32 v174, v236, v174\n\t"
            "v_mul_f32 v175, v236, v175\n\t"
            "v_mfma_f32_16x16x32_bf16 v[196:199], v[44:47], v[208:211], v[196:199]\n\t"
            "v_cvt_pk_bf16_f32 v169, v170, v171\n\t"
            "v_cvt_pk_bf16_f32 v177, v178, v179\n\t"
            "v_mfma_f32_16x16x32_bf16 v[200:203], v[64:67], v[204:207], v[200:203]\n\t"
            "v_cvt_pk_bf16_f32 v181, v182, v183\n\t"
            "v_cvt_pk_bf16_f32 v170, v172, v173\n\t"
            "v_mfma_f32_16x16x32_bf16 v[200:203], v[68:71], v[208:211], v[200:203]\n\t"
            "v_cvt_pk_bf16_f32 v171, v174, v175\n\t"
            "v_cvt_pk_bf16_f32 v182, v184, v185\n\t"
            "v_mfma_f32_16x16x32_bf16 v[216:219], v[88:91], v[204:207], v[216:219]\n\t"
            "v_cvt_pk_bf16_f32 v183, v186, v187\n\t"
            "v_cvt_pk_bf16_f32 v178, v188, v189\n\t"
            "v_mfma_f32_16x16x32_bf16 v[216:219], v[92:95], v[208:211], v[216:219]\n\t"
            "v_cvt_pk_bf16_f32 v179, v190, v191\n\t"
            "v_mul_f32 v236, 0x3e553b94, v236\n\t"
            "v_mfma_f32_16x16x32_bf16 v[220:223], v[112:115], v[204:207], v[220:223]\n\t"
            "v_add_u32 v184, s76, v239\n\t"
            "v_add_u32 v185, s74, v239\n\t"
            "v_mfma_f32_16x16x32_bf16 v[220:223], v[116:119], v[208:211], v[220:223]\n\t"
            "s_mov_b64 exec, -1\n\t"
            "ds_write_b128 v234, v[168:171]\n\t"
            "ds_write_b128 v234, v[180:183] offset:64\n\t"
            "ds_write_b64 v235, v[176:177] offset:128\n\t"
            "ds_write_b64 v235, v[178:179] offset:160\n\t"
            "s_mov_b64 exec, -1\n\t"
            "v_mfma_f32_16x16x16_bf16 v[172:175], v[176:177], v[178:179], 0\n\t"
            "v_mul_f32 v192, v237, v192\n\t"
            "v_mul_f32 v193, v237, v193\n\t"
            "v_mul_f32 v200, v237, v200\n\t"
            "v_mfma_f32_16x16x32_bf16 v[228:231], v[136:139], v[204:207], v[228:231]\n\t"
            "ds_read_b128 v[204:207], v232\n\t"
            "v_mul_f32 v201, v237, v201\n\t"
            "v_mfma_f32_16x16x32_bf16 v[228:231], v[140:143], v[208:211], v[228:231]\n\t"
            "ds_read_b128 v[208:211], v233\n\t"
            "v_mul_f32 v194, v237, v194\n\t"
            "v_mfma_f32_16x16x32_bf16 v[172:175], v[168:171], v[180:183], v[172:175]\n\t"
            "v_mul_f32 v195, v237, v195\n\t"
            "v_mul_f32 v202, v237, v202\n\t"
            "v_mul_f32 v203, v237, v203\n\t"
            "v_cvt_pk_bf16_f32 v192, v192, v193\n\t"
            "v_cvt_pk_bf16_f32 v200, v200, v201\n\t"
            "v_cvt_pk_bf16_f32 v216, v216, v217\n\t"
            "v_mul_f32 v196, v237, v196\n\t"
            "v_mul_f32 v197, v237, v197\n\t"
            "v_mul_f32 v198, v237, v198\n\t"
            "v_mul_f32 v199, v237, v199\n\t"
            "v_cvt_pk_bf16_f32 v193, v194, v195\n\t"
            "v_cvt_pk_bf16_f32 v201, v202, v203\n\t"
            "v_cvt_pk_bf16_f32 v217, v218, v219\n\t"
            "v_cvt_pk_bf16_f32 v194, v196, v197\n\t"
            "v_cvt_pk_bf16_f32 v195, v198, v199\n\t"
            "v_cvt_pk_bf16_f32 v218, v220, v221\n\t"
            "v_cvt_pk_bf16_f32 v219, v222, v223\n\t"
            "v_cvt_pk_bf16_f32 v202, v228, v229\n\t"
            "v_cvt_pk_bf16_f32 v203, v230, v231\n\t"
            "v_mul_f32 v237, 0x3e553b94, v237\n\t"
            "v_add_u32 v220, s76, v239\n\t"
            "v_mfma_f32_16x16x16_bf16 v[196:199], v[200:201], v[202:203], 0\n\t"
            "s_and_b32 exec_lo, s56, 0x11111111\n\t"
            "s_and_b32 exec_hi, s57, 0x11111111\n\t"
            "ds_write_b32 v184, v172 offset:0\n\t"
            "s_and_b32 exec_lo, s56, 0x22222222\n\t"
            "s_and_b32 exec_hi, s57, 0x22222222\n\t"
            "ds_write_b32 v184, v173 offset:0\n\t"
            "s_and_b32 exec_lo, s56, 0x44444444\n\t"
            "s_and_b32 exec_hi, s57, 0x44444444\n\t"
            "ds_write_b32 v184, v174 offset:0\n\t"
            "s_and_b32 exec_lo, s56, 0x88888888\n\t"
            "s_and_b32 exec_hi, s57, 0x88888888\n\t"
            "ds_write_b32 v184, v175 offset:0\n\t"
            "s_mov_b64 exec, s[56:57]\n\t"
            "ds_write_b32 v185, v236 offset:0\n\t"
            "s_mov_b64 exec, -1\n\t"
            "s_waitcnt lgkmcnt(5)\n\t"
            "v_mfma_f32_16x16x32_bf16 v[144:147], v[0:3], v[204:207], 0\n\t"
            "v_add_u32 v221, s74, v239\n\t"
            "v_mfma_f32_16x16x32_bf16 v[144:147], v[4:7], v[208:211], v[144:147]\n\t"
            "s_mov_b64 exec, s[54:55]\n\t"
            "ds_write_b128 v234, v[192:195] offset:12800\n\t"
            "ds_write_b128 v234, v[216:219] offset:12864\n\t"
            "ds_write_b64 v235, v[200:201] offset:12928\n\t"
            "ds_write_b64 v235, v[202:203] offset:12960\n\t"
            "s_mov_b64 exec, -1\n\t"
            "v_mfma_f32_16x16x32_bf16 v[148:151], v[28:31], v[208:211], 0\n\t"
            "v_add_u32 v235, s48, v248\n\t"
            "v_mfma_f32_16x16x32_bf16 v[148:151], v[24:27], v[204:207], v[148:151]\n\t"
            "ds_read_b128 v[228:231], v235 offset:18816\n\t"
            "v_mfma_f32_16x16x32_bf16 v[152:155], v[48:51], v[204:207], 0\n\t"
            "v_mfma_f32_16x16x32_bf16 v[152:155], v[52:55], v[208:211], v[152:155]\n\t"
            "v_mfma_f32_16x16x32_bf16 v[156:159], v[76:79], v[208:211], 0\n\t"
            "v_mfma_f32_16x16x32_bf16 v[156:159], v[72:75], v[204:207], v[156:159]\n\t"
            "v_mfma_f32_16x16x32_bf16 v[160:163], v[96:99], v[204:207], 0\n\t"
            "v_mfma_f32_16x16x32_bf16 v[160:163], v[100:103], v[208:211], v[160:163]\n\t"
            "v_mfma_f32_16x16x32_bf16 v[164:167], v[124:127], v[208:211], 0\n\t"
            "ds_read_b128 v[208:211], v233 offset:128\n\t"
            "s_waitcnt lgkmcnt(1)\n\t"
            "v_add_f32 v236, v228, v230\n\t"
            "v_mfma_f32_16x16x32_bf16 v[164:167], v[120:123], v[204:207], v[164:167]\n\t"
            "ds_read_b128 v[204:207], v232 offset:128\n\t"
            "v_mfma_f32_16x16x32_bf16 v[196:199], v[192:195], v[216:219], v[196:199]\n\t"
            "s_nop 7\n\t"
            "s_and_b32 exec_lo, s78, 0x11111111\n\t"
            "s_and_b32 exec_hi, s79, 0x11111111\n\t"
            "ds_write_b32 v220, v196 offset:12800\n\t"
            "s_and_b32 exec_lo, s78, 0x22222222\n\t"
            "s_and_b32 exec_hi, s79, 0x22222222\n\t"
            "ds_write_b32 v220, v197 offset:12800\n\t"
            "s_and_b32 exec_lo, s78, 0x44444444\n\t"
            "s_and_b32 exec_hi, s79, 0x44444444\n\t"
            "ds_write_b32 v220, v198 offset:12800\n\t"
            "s_and_b32 exec_lo, s78, 0x88888888\n\t"
            "s_and_b32 exec_hi, s79, 0x88888888\n\t"
            "ds_write_b32 v220, v199 offset:12800\n\t"
            "s_mov_b64 exec, s[78:79]\n\t"
            "ds_write_b32 v221, v237 offset:12800\n\t"
            "s_mov_b64 exec, -1\n\t"
            "s_waitcnt lgkmcnt(5)\n\t"
            "v_mfma_f32_16x16x32_bf16 v[144:147], v[8:11], v[204:207], v[144:147]\n\t"
            "v_add_f32 v237, v229, v231\n\t"
            "v_mfma_f32_16x16x32_bf16 v[144:147], v[12:15], v[208:211], v[144:147]\n\t"
            "ds_read_b128 v[228:231], v235 offset:19840\n\t"
            "v_mfma_f32_16x16x32_bf16 v[148:151], v[36:39], v[208:211], v[148:151]\n\t"
            "v_mfma_f32_16x16x32_bf16 v[148:151], v[32:35], v[204:207], v[148:151]\n\t"
            "v_mfma_f32_16x16x32_bf16 v[152:155], v[56:59], v[204:207], v[152:155]\n\t"
            "v_mfma_f32_16x16x32_bf16 v[152:155], v[60:63], v[208:211], v[152:155]\n\t"
            "v_mfma_f32_16x16x32_bf16 v[156:159], v[84:87], v[208:211], v[156:159]\n\t"
            "v_mfma_f32_16x16x32_bf16 v[156:159], v[80:83], v[204:207], v[156:159]\n\t"
            "v_mfma_f32_16x16x32_bf16 v[160:163], v[104:107], v[204:207], v[160:163]\n\t"
            "s_waitcnt lgkmcnt(0)\n\t"
            "v_add_f32 v228, v228, v230\n\t"
            "v_add_f32 v236, v236, v228\n\t"
            "v_mfma_f32_16x16x32_bf16 v[160:163], v[108:111], v[208:211], v[160:163]\n\t"
            "v_add_f32 v229, v229, v231\n\t"
            "v_mul_f32 v236, 0x3baaaaab, v236\n\t"
            "v_mfma_f32_16x16x32_bf16 v[164:167], v[132:135], v[208:211], v[164:167]\n\t"
            "ds_read_b128 v[208:211], v233 offset:6144\n\t"
            "v_add_f32 v237, v237, v229\n\t"
            "v_mfma_f32_16x16x32_bf16 v[164:167], v[128:131], v[204:207], v[164:167]\n\t"
            "ds_read_b128 v[204:207], v232 offset:6144\n\t"
            "v_mul_f32_e64 v236, v236, -v236\n\t"
            "v_mfma_f32_16x16x32_bf16 v[144:147], v[16:19], v[212:215], v[144:147]\n\t"
            "v_fmac_f32 v236, 0x3baaaaab, v237\n\t"
            "v_max_f32 v236, 0, v236\n\t"
            "v_mfma_f32_16x16x32_bf16 v[144:147], v[20:23], v[224:227], v[144:147]\n\t"
            "v_add_f32 v236, 0x3727c5ac, v236\n\t"
            "v_rsq_f32 v236, v236\n\t"
            "v_mfma_f32_16x16x32_bf16 v[148:151], v[44:47], v[224:227], v[148:151]\n\t"
            "v_mfma_f32_16x16x32_bf16 v[148:151], v[40:43], v[212:215], v[148:151]\n\t"
            "ds_write_b32 v249, v236\n\t"
            "v_mfma_f32_16x16x32_bf16 v[152:155], v[64:67], v[212:215], v[152:155]\n\t"
            "ds_read_b32 v236, v251\n\t"
            "ds_read_b32 v237, v251 offset:64\n\t"
            "v_mfma_f32_16x16x32_bf16 v[152:155], v[68:71], v[224:227], v[152:155]\n\t"
            "ds_read_b32 v238, v251 offset:128\n\t"
            "v_mfma_f32_16x16x32_bf16 v[156:159], v[92:95], v[224:227], v[156:159]\n\t"
            "v_mfma_f32_16x16x32_bf16 v[156:159], v[88:91], v[212:215], v[156:159]\n\t"
            "v_mfma_f32_16x16x32_bf16 v[160:163], v[112:115], v[212:215], v[160:163]\n\t"
            "v_mfma_f32_16x16x32_bf16 v[160:163], v[116:119], v[224:227], v[160:163]\n\t"
            "v_mfma_f32_16x16x32_bf16 v[164:167], v[140:143], v[224:227], v[164:167]\n\t"
            "ds_read_b128 v[224:227], v233 offset:6272\n\t"
            "v_mfma_f32_16x16x32_bf16 v[164:167], v[136:139], v[212:215], v[164:167]\n\t"
            "ds_read_b128 v[212:215], v232 offset:6272\n\t"
            "s_waitcnt lgkmcnt(4)\n\t"
            "v_mul_f32 v144, v236, v144\n\t"
            "v_mfma_f32_16x16x32_bf16 v[168:171], v[0:3], v[204:207], 0\n\t"
            "v_mul_f32 v145, v236, v145\n\t"
            "v_mul_f32 v152, v236, v152\n\t"
            "v_mfma_f32_16x16x32_bf16 v[168:171], v[4:7], v[208:211], v[168:171]\n\t"
            "v_mul_f32 v153, v236, v153\n\t"
            "v_mul_f32 v146, v236, v146\n\t"
            "v_mfma_f32_16x16x32_bf16 v[172:175], v[28:31], v[208:211], 0\n\t"
            "v_mul_f32 v147, v236, v147\n\t"
            "v_mul_f32 v154, v236, v154\n\t"
            "v_mfma_f32_16x16x32_bf16 v[172:175], v[24:27], v[204:207], v[172:175]\n\t"
            "v_mul_f32 v155, v236, v155\n\t"
            "v_cvt_pk_bf16_f32 v144, v144, v145\n\t"
            "v_mfma_f32_16x16x32_bf16 v[176:179], v[48:51], v[204:207], 0\n\t"
            "v_cvt_pk_bf16_f32 v152, v152, v153\n\t"
            "v_cvt_pk_bf16_f32 v156, v156, v157\n\t"
            "v_mfma_f32_16x16x32_bf16 v[176:179], v[52:55], v[208:211], v[176:179]\n\t"
            "v_mul_f32 v148, v236, v148\n\t"
            "v_mul_f32 v149, v236, v149\n\t"
            "v_mfma_f32_16x16x32_bf16 v[180:183], v[76:79], v[208:211], 0\n\t"
            "v_mul_f32 v150, v236, v150\n\t"
            "v_mul_f32 v151, v236, v151\n\t"
            "v_mfma_f32_16x16x32_bf16 v[180:183], v[72:75], v[204:207], v[180:183]\n\t"
            "v_cvt_pk_bf16_f32 v145, v146, v147\n\t"
            "v_cvt_pk_bf16_f32 v153, v154, v155\n\t"
            "v_mfma_f32_16x16x32_bf16 v[184:187], v[96:99], v[204:207], 0\n\t"
            "v_cvt_pk_bf16_f32 v157, v158, v159\n\t"
            "v_cvt_pk_bf16_f32 v146, v148, v149\n\t"
            "v_mfma_f32_16x16x32_bf16 v[184:187], v[100:103], v[208:211], v[184:187]\n\t"
            "v_cvt_pk_bf16_f32 v147, v150, v151\n\t"
            "v_cvt_pk_bf16_f32 v158, v160, v161\n\t"
            "v_mfma_f32_16x16x32_bf16 v[188:191], v[124:127], v[208:211], 0\n\t"
            "ds_read_b128 v[208:211], v233 offset:6400\n\t"
            "v_cvt_pk_bf16_f32 v159, v162, v163\n\t"
            "v_mfma_f32_16x16x32_bf16 v[188:191], v[120:123], v[204:207], v[188:191]\n\t"
            "ds_read_b128 v[204:207], v232 offset:6400\n\t"
            "v_cvt_pk_bf16_f32 v154, v164, v165\n\t"
            "s_waitcnt lgkmcnt(2)\n\t"
            "v_mfma_f32_16x16x32_bf16 v[168:171], v[8:11], v[212:215], v[168:171]\n\t"
            "v_cvt_pk_bf16_f32 v155, v166, v167\n\t"
            "v_mul_f32 v236, 0x3e553b94, v236\n\t"
            "v_mfma_f32_16x16x32_bf16 v[168:171], v[12:15], v[224:227], v[168:171]\n\t"
            "v_mfma_f32_16x16x32_bf16 v[172:175], v[36:39], v[224:227], v[172:175]\n\t"
            "v_mfma_f32_16x16x32_bf16 v[172:175], v[32:35], v[212:215], v[172:175]\n\t"
            "v_mfma_f32_16x16x32_bf16 v[176:179], v[56:59], v[212:215], v[176:179]\n\t"
            "v_mfma_f32_16x16x32_bf16 v[176:179], v[60:63], v[224:227], v[176:179]\n\t"
            "v_mfma_f32_16x16x32_bf16 v[180:183], v[84:87], v[224:227], v[180:183]\n\t"
            "v_mfma_f32_16x16x32_bf16 v[180:183], v[80:83], v[212:215], v[180:183]\n\t"
            "v_mfma_f32_16x16x32_bf16 v[184:187], v[104:107], v[212:215], v[184:187]\n\t"
            "v_mfma_f32_16x16x32_bf16 v[184:187], v[108:111], v[224:227], v[184:187]\n\t"
            "v_mfma_f32_16x16x32_bf16 v[188:191], v[132:135], v[224:227], v[188:191]\n\t"
            "ds_read_b128 v[224:227], v233 offset:12288\n\t"
            "v_mfma_f32_16x16x32_bf16 v[188:191], v[128:131], v[212:215], v[188:191]\n\t"
            "ds_read_b128 v[212:215], v232 offset:12288\n\t"
            "s_waitcnt lgkmcnt(2)\n\t"
            "v_mfma_f32_16x16x32_bf16 v[168:171], v[16:19], v[204:207], v[168:171]\n\t"
            "v_mfma_f32_16x16x32_bf16 v[168:171], v[20:23], v[208:211], v[168:171]\n\t"
            "v_mfma_f32_16x16x32_bf16 v[172:175], v[44:47], v[208:211], v[172:175]\n\t"
            "v_mfma_f32_16x16x32_bf16 v[172:175], v[40:43], v[204:207], v[172:175]\n\t"
            "v_mfma_f32_16x16x32_bf16 v[176:179], v[64:67], v[204:207], v[176:179]\n\t"
            "v_mfma_f32_16x16x32_bf16 v[176:179], v[68:71], v[208:211], v[176:179]\n\t"
            "v_mfma_f32_16x16x32_bf16 v[180:183], v[92:95], v[208:211], v[180:183]\n\t"
            "v_mfma_f32_16x16x32_bf16 v[180:183], v[88:91], v[204:207], v[180:183]\n\t"
            "v_mfma_f32_16x16x32_bf16 v[184:187], v[112:115], v[204:207], v[184:187]\n\t"
            "v_mfma_f32_16x16x32_bf16 v[184:187], v[116:119], v[208:211], v[184:187]\n\t"
            "v_mfma_f32_16x16x32_bf16 v[188:191], v[140:143], v[208:211], v[188:191]\n\t"
            "ds_read_b128 v[208:211], v233 offset:12416\n\t"
            "v_mfma_f32_16x16x32_bf16 v[188:191], v[136:139], v[204:207], v[188:191]\n\t"
            "ds_read_b128 v[204:207], v232 offset:12416\n\t"
            "s_waitcnt lgkmcnt(0)\n\t"
            "v_subrev_u32 v249, s75, v251\n\t"
            "v_add_u32 v249, 162816, v249\n\t"
            "s_mov_b32 s90, 2\n\t"
            "s_mov_b32 s81, 0\n\t"
            "s_mov_b32 s91, 0\n\t"
            "s_nop 7\n\t"
            ".p2align 6\n\t"
            "s_nop 0\n\t"
            "s_nop 0\n\t"
            "s_nop 0\n\t"
            ".Lwa_loop%=:\n\t"
            "s_waitcnt lgkmcnt(2)\n\t"
            "v_mfma_f32_16x16x32_bf16 v[192:195], v[0:3], v[212:215], 0\n\t"
            "s_bfe_u32 s49, s70, 0x30001\n\t"
            "s_lshl_b32 s49, s49, 4\n\t"
            "s_or_b32 s73, s77, s49\n\t"
            "v_mfma_f32_16x16x32_bf16 v[192:195], v[4:7], v[224:227], v[192:195]\n\t"
            "v_mad_u32_u24 v220, v246, s72, v253\n\t"
            "v_mad_u32_u24 v216, v246, s72, v252\n\t"
            "v_mfma_f32_16x16x32_bf16 v[148:151], v[28:31], v[224:227], 0\n\t"
            "ds_read_b64 v[222:223], v220 offset:128\n\t"
            "v_mov_b32_dpp v221, v220 quad_perm:[1,0,2,3] row_mask:0xf bank_mask:0xf\n\t"
            "v_mfma_f32_16x16x32_bf16 v[148:151], v[24:27], v[212:215], v[148:151]\n\t"
            "ds_read_b64 v[220:221], v221 offset:160\n\t"
            "v_mov_b32_dpp v217, v216 quad_perm:[1,0,2,3] row_mask:0xf bank_mask:0xf\n\t"
            "v_mfma_f32_16x16x32_bf16 v[196:199], v[48:51], v[212:215], 0\n\t"
            "v_mul_f32 v176, v237, v176\n\t"
            "v_mul_f32 v177, v237, v177\n\t"
            "v_mfma_f32_16x16x32_bf16 v[196:199], v[52:55], v[224:227], v[196:199]\n\t"
            "v_mul_f32 v178, v237, v178\n\t"
            "v_mul_f32 v179, v237, v179\n\t"
            "v_mfma_f32_16x16x32_bf16 v[200:203], v[76:79], v[224:227], 0\n\t"
            "v_cvt_pk_bf16_f32 v176, v176, v177\n\t"
            "v_mul_f32 v168, v237, v168\n\t"
            "v_mfma_f32_16x16x32_bf16 v[200:203], v[72:75], v[212:215], v[200:203]\n\t"
            "v_mul_f32 v169, v237, v169\n\t"
            "v_cvt_pk_bf16_f32 v177, v178, v179\n\t"
            "v_mfma_f32_16x16x32_bf16 v[160:163], v[96:99], v[212:215], 0\n\t"
            "v_mul_f32 v170, v237, v170\n\t"
            "v_mul_f32 v171, v237, v171\n\t"
            "v_mfma_f32_16x16x32_bf16 v[160:163], v[100:103], v[224:227], v[160:163]\n\t"
            "v_cvt_pk_bf16_f32 v168, v168, v169\n\t"
            "v_cvt_pk_bf16_f32 v180, v180, v181\n\t"
            "v_mfma_f32_16x16x32_bf16 v[164:167], v[124:127], v[224:227], 0\n\t"
            "ds_read_b128 v[224:227], v233 offset:12544\n\t"
            "v_cvt_pk_bf16_f32 v178, v188, v189\n\t"
            "v_mfma_f32_16x16x32_bf16 v[164:167], v[120:123], v[212:215], v[164:167]\n\t"
            "ds_read_b128 v[212:215], v232 offset:12544\n\t"
            "v_cvt_pk_bf16_f32 v179, v190, v191\n\t"
            "s_waitcnt lgkmcnt(4)\n\t"
            "v_mfma_f32_16x16x32_bf16 v[192:195], v[8:11], v[204:207], v[192:195]\n\t"
            "v_mul_f32 v172, v237, v172\n\t"
            "v_mul_f32 v173, v237, v173\n\t"
            "v_mfma_f32_16x16x32_bf16 v[192:195], v[12:15], v[208:211], v[192:195]\n\t"
            "v_mul_f32 v174, v237, v174\n\t"
            "v_mul_f32 v175, v237, v175\n\t"
            "v_mfma_f32_16x16x32_bf16 v[196:199], v[56:59], v[204:207], v[196:199]\n\t"
            "v_cvt_pk_bf16_f32 v169, v170, v171\n\t"
            "v_cvt_pk_bf16_f32 v181, v182, v183\n\t"
            "v_mfma_f32_16x16x32_bf16 v[196:199], v[60:63], v[208:211], v[196:199]\n\t"
            "v_cvt_pk_bf16_f32 v170, v172, v173\n\t"
            "v_cvt_pk_bf16_f32 v171, v174, v175\n\t"
            "v_mfma_f32_16x16x32_bf16 v[160:163], v[104:107], v[204:207], v[160:163]\n\t"
            "v_cvt_pk_bf16_f32 v182, v184, v185\n\t"
            "v_cvt_pk_bf16_f32 v183, v186, v187\n\t"
            "v_mfma_f32_16x16x32_bf16 v[160:163], v[108:111], v[208:211], v[160:163]\n\t"
            "v_mov_b32 v230, s76\n\t"
            "v_mov_b32 v239, s74\n\t"
            "v_mfma_f32_16x16x32_bf16 v[148:151], v[36:39], v[208:211], v[148:151]\n\t"
            "s_add_u32 s70, s70, 1\n\t"
            "s_add_u32 s72, s72, 800\n\t"
            "s_add_u32 s74, s74, 800\n\t"
            "s_add_u32 s76, s76, 800\n\t"
            "v_mfma_f32_16x16x32_bf16 v[148:151], v[32:35], v[204:207], v[148:151]\n\t"
            "s_add_u32 s77, s77, 384\n\t"
            "s_xor_b32 s48, s48, s47\n\t"
            "ds_read_b32 v230, v230\n\t"
            "v_mfma_f32_16x16x32_bf16 v[200:203], v[84:87], v[208:211], v[200:203]\n\t"
            "ds_read_b32 v239, v239\n\t"
            "v_mul_f32 v237, 0x3e553b94, v237\n\t"
            "v_mfma_f32_16x16x32_bf16 v[200:203], v[80:83], v[204:207], v[200:203]\n\t"
            "v_mfma_f32_16x16x32_bf16 v[164:167], v[132:135], v[208:211], v[164:167]\n\t"
            "v_mfma_f32_16x16x32_bf16 v[164:167], v[128:131], v[204:207], v[164:167]\n\t"
            "s_waitcnt lgkmcnt(2)\n\t"
            "v_mfma_f32_16x16x32_bf16 v[192:195], v[16:19], v[212:215], v[192:195]\n\t"
            "v_mfma_f32_16x16x32_bf16 v[192:195], v[20:23], v[224:227], v[192:195]\n\t"
            "v_mfma_f32_16x16x32_bf16 v[196:199], v[64:67], v[212:215], v[196:199]\n\t"
            "v_mfma_f32_16x16x32_bf16 v[196:199], v[68:71], v[224:227], v[196:199]\n\t"
            "v_mfma_f32_16x16x32_bf16 v[160:163], v[112:115], v[212:215], v[160:163]\n\t"
            "v_mfma_f32_16x16x32_bf16 v[160:163], v[116:119], v[224:227], v[160:163]\n\t"
            "v_mfma_f32_16x16x32_bf16 v[148:151], v[44:47], v[224:227], v[148:151]\n\t"
            "v_mfma_f32_16x16x32_bf16 v[148:151], v[40:43], v[212:215], v[148:151]\n\t"
            "s_nop 1\n\t"
            "v_mul_f32 v192, v238, v192\n\t"
            "v_mfma_f32_16x16x32_bf16 v[200:203], v[92:95], v[224:227], v[200:203]\n\t"
            "v_mul_f32 v193, v238, v193\n\t"
            "v_mul_f32 v194, v238, v194\n\t"
            "v_mfma_f32_16x16x32_bf16 v[200:203], v[88:91], v[212:215], v[200:203]\n\t"
            "v_mul_f32 v196, v238, v196\n\t"
            "v_mul_f32 v197, v238, v197\n\t"
            "v_mfma_f32_16x16x32_bf16 v[164:167], v[140:143], v[224:227], v[164:167]\n\t"
            "ds_read_b128 v[224:227], v217 offset:64\n\t"
            "ds_read_b128 v[216:219], v216\n\t"
            "v_mfma_f32_16x16x32_bf16 v[164:167], v[136:139], v[212:215], v[164:167]\n\t"
            "v_mul_f32 v198, v238, v198\n\t"
            "v_mul_f32 v199, v238, v199\n\t"
            "v_mfma_f32_16x16x32_bf16 v[172:175], v[220:223], v[152:155], 0\n\t"
            "s_setprio 0\n\t"
            "v_cvt_pk_bf16_f32 v196, v196, v197\n\t"
            "v_mul_f32 v195, v238, v195\n\t"
            "s_setprio 3\n\t"
            "v_mfma_f32_16x16x32_bf16 v[212:215], v[220:223], v[176:179], 0\n\t"
            "v_cvt_pk_bf16_f32 v192, v192, v193\n\t"
            "v_cvt_pk_bf16_f32 v197, v198, v199\n\t"
            "v_cvt_pk_bf16_f32 v200, v200, v201\n\t"
            "v_mul_f32 v148, v238, v148\n\t"
            "v_mfma_f32_16x16x16_bf16 v[184:187], v[152:153], v[178:179], 0\n\t"
            "v_mul_f32 v149, v238, v149\n\t"
            "v_mul_f32 v150, v238, v150\n\t"
            "v_mul_f32 v151, v238, v151\n\t"
            "v_mfma_f32_16x16x16_bf16 v[204:207], v[176:177], v[178:179], 0\n\t"
            "v_cvt_pk_bf16_f32 v193, v194, v195\n\t"
            "v_cvt_pk_bf16_f32 v201, v202, v203\n\t"
            "v_cvt_pk_bf16_f32 v194, v148, v149\n\t"
            "v_mfma_f32_16x16x16_bf16 v[188:191], v[196:197], v[178:179], 0\n\t"
            "v_cvt_pk_bf16_f32 v195, v150, v151\n\t"
            "v_cvt_pk_bf16_f32 v202, v160, v161\n\t"
            "v_cvt_pk_bf16_f32 v203, v162, v163\n\t"
            "s_waitcnt lgkmcnt(0)\n\t"
            "v_mfma_f32_16x16x32_bf16 v[172:175], v[216:219], v[156:159], v[172:175]\n\t"
            "v_cvt_pk_bf16_f32 v198, v164, v165\n\t"
            "v_cvt_pk_bf16_f32 v199, v166, v167\n\t"
            "v_mfma_f32_16x16x32_bf16 v[172:175], v[224:227], v[144:147], v[172:175]\n\t"
            "v_mul_f32 v238, 0x3e553b94, v238\n\t"
            "v_mfma_f32_16x16x16_bf16 v[148:151], v[152:153], v[154:155], 0\n\t"
            "v_mfma_f32_16x16x16_bf16 v[160:163], v[176:177], v[154:155], 0\n\t"
            "v_mfma_f32_16x16x16_bf16 v[164:167], v[196:197], v[154:155], 0\n\t"
            "v_mfma_f32_16x16x32_bf16 v[212:215], v[216:219], v[180:183], v[212:215]\n\t"
            "v_mfma_f32_16x16x32_bf16 v[212:215], v[224:227], v[168:171], v[212:215]\n\t"
            "s_nop 1\n\t"
            "v_mfma_f32_16x16x32_bf16 v[148:151], v[144:147], v[156:159], v[148:151]\n\t"
            "v_mfma_f32_16x16x32_bf16 v[184:187], v[144:147], v[180:183], v[184:187]\n\t"
            "v_mfma_f32_16x16x32_bf16 v[160:163], v[168:171], v[156:159], v[160:163]\n\t"
            "v_mfma_f32_16x16x32_bf16 v[204:207], v[168:171], v[180:183], v[204:207]\n\t"
            "v_mfma_f32_16x16x32_bf16 v[164:167], v[192:195], v[156:159], v[164:167]\n\t"
            "v_mfma_f32_16x16x32_bf16 v[188:191], v[192:195], v[180:183], v[188:191]\n\t"
            "v_cndmask_b32_e64 v235, v173, v213, s[60:61]\n\t"
            "v_mfma_f32_16x16x32_bf16 v[208:211], v[220:223], v[196:199], 0\n\t"
            "s_nop 0\n\t"
            "v_max3_f32 v228, v172, v148, v149\n\t"
            "v_max3_f32 v228, v228, v150, v151\n\t"
            "v_mfma_f32_16x16x32_bf16 v[208:211], v[216:219], v[200:203], v[208:211]\n\t"
            "v_max3_f32 v229, v212, v184, v185\n\t"
            "v_max3_f32 v229, v229, v186, v187\n\t"
            "v_mfma_f32_16x16x32_bf16 v[208:211], v[224:227], v[192:195], v[208:211]\n\t"
            "s_setprio 0\n\t"
            "v_max3_f32 v228, v228, v160, v161\n\t"
            "v_max3_f32 v229, v229, v204, v205\n\t"
            "v_max3_f32 v228, v228, v162, v163\n\t"
            "v_max3_f32 v229, v229, v206, v207\n\t"
            "v_max3_f32 v228, v228, v164, v165\n\t"
            "v_max3_f32 v229, v229, v188, v189\n\t"
            "v_max3_f32 v228, v228, v166, v167\n\t"
            "v_max3_f32 v229, v229, v190, v191\n\t"
            "v_max3_f32 v234, v173, v213, v209\n\t"
            "v_max_f32 v234, v234, v230\n\t"
            "v_permlane32_swap_b32 v228, v229\n\t"
            "v_max_f32 v228, v228, v229\n\t"
            "v_mov_b32 v231, v228\n\t"
            "v_max_f32_dpp v234, v234, v234 row_ror:8 row_mask:0xf bank_mask:0xf\n\t"
            "v_cndmask_b32_e64 v235, v235, v209, s[62:63]\n\t"
            "v_permlane16_swap_b32 v228, v231\n\t"
            "v_max_f32 v228, v228, v231\n\t"
            "v_mov_b32 v229, v228\n\t"
            "v_max_f32_dpp v234, v234, v234 row_ror:4 row_mask:0xf bank_mask:0xf\n\t"
            "v_cndmask_b32_e64 v235, v235, v230, s[64:65]\n\t"
            "v_permlane32_swap_b32 v228, v229\n\t"
            "v_mul_f32_e64 v229, -v229, v237\n\t"
            "v_fma_f32 v184, v184, v237, v229\n\t"
            "v_fma_f32 v185, v185, v237, v229\n\t"
            "v_exp_f32 v184, v184\n\t"
            "v_exp_f32 v185, v185\n\t"
            "v_fma_f32 v186, v186, v237, v229\n\t"
            "v_fma_f32 v187, v187, v237, v229\n\t"
            "v_exp_f32 v186, v186\n\t"
            "v_exp_f32 v187, v187\n\t"
            "v_fma_f32 v204, v204, v237, v229\n\t"
            "v_fma_f32 v205, v205, v237, v229\n\t"
            "v_fma_f32 v206, v206, v237, v229\n\t"
            "v_fma_f32 v207, v207, v237, v229\n\t"
            "v_exp_f32 v204, v204\n\t"
            "v_exp_f32 v205, v205\n\t"
            "v_exp_f32 v206, v206\n\t"
            "v_exp_f32 v207, v207\n\t"
            "v_cvt_pk_bf16_f32 v184, v184, v185\n\t"
            "v_mul_f32_e64 v228, -v228, v236\n\t"
            "v_cvt_pk_bf16_f32 v185, v186, v187\n\t"
            "v_fma_f32 v210, v172, v236, v228\n\t"
            "v_cvt_pk_bf16_f32 v186, v204, v205\n\t"
            "v_cvt_pk_bf16_f32 v187, v206, v207\n\t"
            "s_setprio 3\n\t"
            "v_mfma_f32_16x16x16_bf16 v[180:183], v[152:153], v[198:199], 0\n\t"
            "v_max_f32_dpp v234, v234, v234 row_ror:2 row_mask:0xf bank_mask:0xf\n\t"
            "s_mov_b64 exec, s[60:61]\n\t"
            "v_fma_f32 v210, v212, v237, v229\n\t"
            "s_mov_b64 exec, -1\n\t"
            "v_mfma_f32_16x16x16_bf16 v[204:207], v[176:177], v[198:199], 0\n\t"
            "v_max_f32_dpp v234, v234, v234 row_ror:1 row_mask:0xf bank_mask:0xf\n\t"
            "v_sub_f32 v235, v235, v234\n\t"
            "v_mul_f32 v235, v239, v235\n\t"
            "v_mfma_f32_16x16x16_bf16 v[172:175], v[196:197], v[198:199], 0\n\t"
            "v_exp_f32 v235, v235\n\t"
            "v_fma_f32 v148, v148, v236, v228\n\t"
            "v_mfma_f32_16x16x32_bf16 v[180:183], v[144:147], v[200:203], v[180:183]\n\t"
            "v_cvt_pk_bf16_f32 v235, v235, 0\n\t"
            "v_cndmask_b32_e64 v235, 0, v235, s[66:67]\n\t"
            "v_mfma_f32_16x16x32_bf16 v[204:207], v[168:171], v[200:203], v[204:207]\n\t"
            "ds_write_b16 v250, v235 offset:256\n\t"
            "v_mfma_f32_16x16x32_bf16 v[172:175], v[192:195], v[200:203], v[172:175]\n\t"
            "s_setprio 0\n\t"
            "v_fma_f32 v149, v149, v236, v228\n\t"
            "v_fma_f32 v164, v164, v236, v228\n\t"
            "v_fma_f32 v165, v165, v236, v228\n\t"
            "v_max3_f32 v230, v208, v180, v181\n\t"
            "v_max3_f32 v230, v230, v182, v183\n\t"
            "v_exp_f32 v148, v148\n\t"
            "v_max3_f32 v230, v230, v204, v205\n\t"
            "v_max3_f32 v230, v230, v206, v207\n\t"
            "v_max3_f32 v230, v230, v172, v173\n\t"
            "v_max3_f32 v230, v230, v174, v175\n\t"
            "v_mov_b32 v231, v230\n\t"
            "v_exp_f32 v149, v149\n\t"
            "v_fma_f32 v150, v150, v236, v228\n\t"
            "v_permlane32_swap_b32 v230, v231\n\t"
            "v_max_f32 v230, v230, v231\n\t"
            "v_mov_b32 v231, v230\n\t"
            "v_fma_f32 v151, v151, v236, v228\n\t"
            "v_exp_f32 v164, v164\n\t"
            "v_permlane16_swap_b32 v230, v231\n\t"
            "v_max_f32 v230, v230, v231\n\t"
            "v_mul_f32_e64 v230, -v230, v238\n\t"
            "s_mov_b64 exec, s[62:63]\n\t"
            "v_fma_f32 v210, v208, v238, v230\n\t"
            "s_mov_b64 exec, -1\n\t"
            "v_exp_f32 v165, v165\n\t"
            "v_fma_f32 v166, v166, v236, v228\n\t"
            "v_fma_f32 v167, v167, v236, v228\n\t"
            "v_exp_f32 v150, v150\n\t"
            "v_exp_f32 v151, v151\n\t"
            "v_fma_f32 v160, v160, v236, v228\n\t"
            "v_fma_f32 v161, v161, v236, v228\n\t"
            "v_fma_f32 v162, v162, v236, v228\n\t"
            "v_fma_f32 v163, v163, v236, v228\n\t"
            "v_exp_f32 v166, v166\n\t"
            "v_exp_f32 v167, v167\n\t"
            "v_exp_f32 v210, v210\n\t"
            "v_exp_f32 v160, v160\n\t"
            "v_exp_f32 v161, v161\n\t"
            "v_exp_f32 v162, v162\n\t"
            "v_exp_f32 v163, v163\n\t"
            "v_cvt_pk_bf16_f32 v148, v148, v149\n\t"
            "v_cvt_pk_bf16_f32 v164, v164, v165\n\t"
            "v_cvt_pk_bf16_f32 v149, v150, v151\n\t"
            "v_cvt_pk_bf16_f32 v165, v166, v167\n\t"
            "v_cvt_pk_bf16_f32 v210, v210, 0\n\t"
            "v_cvt_pk_bf16_f32 v150, v160, v161\n\t"
            "v_cvt_pk_bf16_f32 v151, v162, v163\n\t"
            "v_mov_b32 v167, 0\n\t"
            "v_cndmask_b32_e64 v166, 0, v210, s[58:59]\n\t"
            "v_fma_f32 v188, v188, v237, v229\n\t"
            "v_fma_f32 v189, v189, v237, v229\n\t"
            "v_fma_f32 v180, v180, v238, v230\n\t"
            "v_fma_f32 v181, v181, v238, v230\n\t"
            "v_fma_f32 v172, v172, v238, v230\n\t"
            "v_fma_f32 v173, v173, v238, v230\n\t"
            "v_exp_f32 v188, v188\n\t"
            "v_exp_f32 v189, v189\n\t"
            "v_fma_f32 v190, v190, v237, v229\n\t"
            "v_fma_f32 v191, v191, v237, v229\n\t"
            "v_exp_f32 v180, v180\n\t"
            "v_exp_f32 v181, v181\n\t"
            "v_fma_f32 v182, v182, v238, v230\n\t"
            "v_fma_f32 v183, v183, v238, v230\n\t"
            "v_exp_f32 v172, v172\n\t"
            "v_exp_f32 v173, v173\n\t"
            "v_fma_f32 v174, v174, v238, v230\n\t"
            "v_fma_f32 v175, v175, v238, v230\n\t"
            "v_exp_f32 v190, v190\n\t"
            "v_exp_f32 v191, v191\n\t"
            "v_exp_f32 v182, v182\n\t"
            "v_exp_f32 v183, v183\n\t"
            "v_fma_f32 v204, v204, v238, v230\n\t"
            "v_fma_f32 v205, v205, v238, v230\n\t"
            "v_fma_f32 v206, v206, v238, v230\n\t"
            "v_fma_f32 v207, v207, v238, v230\n\t"
            "v_exp_f32 v174, v174\n\t"
            "v_exp_f32 v175, v175\n\t"
            "v_exp_f32 v204, v204\n\t"
            "v_exp_f32 v205, v205\n\t"
            "v_exp_f32 v206, v206\n\t"
            "v_exp_f32 v207, v207\n\t"
            "v_cvt_pk_bf16_f32 v188, v188, v189\n\t"
            "v_cvt_pk_bf16_f32 v180, v180, v181\n\t"
            "v_cvt_pk_bf16_f32 v172, v172, v173\n\t"
            "v_cvt_pk_bf16_f32 v189, v190, v191\n\t"
            "v_cvt_pk_bf16_f32 v181, v182, v183\n\t"
            "v_cvt_pk_bf16_f32 v173, v174, v175\n\t"
            "v_cvt_pk_bf16_f32 v182, v204, v205\n\t"
            "v_cvt_pk_bf16_f32 v183, v206, v207\n\t"
            "v_cndmask_b32_e64 v190, 0, v210, s[60:61]\n\t"
            "v_cndmask_b32_e64 v174, 0, v210, s[62:63]\n\t"
            "v_mov_b32 v191, 0\n\t"
            "v_mov_b32 v175, 0\n\t"
            "s_cmp_ge_u32 s70, s80\n\t"
            "s_barrier\n\t"
            "s_setprio 2\n\t"
            "s_cbranch_scc1 .Lwa_h1%=\n\t"
            ".Lwa_r1%=:\n\t"
            "v_mfma_f32_16x16x32_bf16 v[200:203], v[240:243], v[148:151], 0\n\t"
            "ds_read_b64 v[176:177], v247\n\t"
            "ds_read_b64 v[196:197], v247 offset:1536\n\t"
            "v_mfma_f32_16x16x32_bf16 v[200:203], v[240:243], v[164:167], v[200:203]\n\t"
            "ds_read_b64 v[220:221], v247 offset:3072\n\t"
            "v_xor_b32 v234, s73, v254\n\t"
            "v_add_u32 v235, s45, v248\n\t"
            "v_mov_b32 v179, 0\n\t"
            "v_mov_b32 v199, 0\n\t"
            "v_mov_b32 v223, 0\n\t"
            "v_add_u32 v232, s48, v244\n\t"
            "v_xor_b32 v233, 64, v232\n\t"
            "v_rcp_f32 v201, v200\n\t"
            "v_xor_b32 v249, 0x100, v249\n\t"
            "ds_read_b128 v[168:171], v235\n\t"
            "ds_read_u16 v178, v234\n\t"
            "ds_read_b128 v[192:195], v235 offset:1536\n\t"
            "ds_read_u16 v198, v234 offset:128\n\t"
            "ds_read_b128 v[216:219], v235 offset:3072\n\t"
            "ds_read_u16 v222, v234 offset:256\n\t"
            "ds_read_b128 v[204:207], v232\n\t"
            "ds_read_b128 v[208:211], v233\n\t"
            "ds_read_b32 v236, v249\n\t"
            "ds_read_b32 v237, v249 offset:64\n\t"
            "ds_read_b32 v238, v249 offset:128\n\t"
            "v_mad_u32_u24 v234, v246, s75, v253\n\t"
            "s_waitcnt lgkmcnt(9)\n\t"
            "v_mfma_f32_16x16x32_bf16 v[224:227], v[168:171], v[148:151], 0\n\t"
            "v_mfma_f32_16x16x32_bf16 v[224:227], v[176:179], v[164:167], v[224:227]\n\t"
            "s_waitcnt lgkmcnt(7)\n\t"
            "v_mfma_f32_16x16x32_bf16 v[212:215], v[192:195], v[148:151], 0\n\t"
            "v_mfma_f32_16x16x32_bf16 v[212:215], v[196:199], v[164:167], v[212:215]\n\t"
            "s_waitcnt lgkmcnt(5)\n\t"
            "v_mfma_f32_16x16x32_bf16 v[148:151], v[216:219], v[148:151], 0\n\t"
            "v_mfma_f32_16x16x32_bf16 v[148:151], v[220:223], v[164:167], v[148:151]\n\t"
            "s_waitcnt lgkmcnt(3)\n\t"
            "v_mfma_f32_16x16x32_bf16 v[144:147], v[0:3], v[204:207], 0\n\t"
            "v_mfma_f32_16x16x32_bf16 v[144:147], v[4:7], v[208:211], v[144:147]\n\t"
            "v_mul_f32 v224, v201, v224\n\t"
            "v_mfma_f32_16x16x32_bf16 v[152:155], v[48:51], v[204:207], 0\n\t"
            "v_mul_f32 v225, v201, v225\n\t"
            "v_mul_f32 v226, v201, v226\n\t"
            "v_mfma_f32_16x16x32_bf16 v[152:155], v[52:55], v[208:211], v[152:155]\n\t"
            "v_mul_f32 v212, v201, v212\n\t"
            "v_mul_f32 v213, v201, v213\n\t"
            "v_mfma_f32_16x16x32_bf16 v[156:159], v[76:79], v[208:211], 0\n\t"
            "v_mul_f32 v227, v201, v227\n\t"
            "v_cvt_pk_bf16_f32 v224, v224, v225\n\t"
            "v_mfma_f32_16x16x32_bf16 v[156:159], v[72:75], v[204:207], v[156:159]\n\t"
            "v_mul_f32 v214, v201, v214\n\t"
            "v_mul_f32 v215, v201, v215\n\t"
            "v_mfma_f32_16x16x32_bf16 v[160:163], v[96:99], v[204:207], 0\n\t"
            "v_cvt_pk_bf16_f32 v212, v212, v213\n\t"
            "v_cvt_pk_bf16_f32 v225, v226, v227\n\t"
            "v_mfma_f32_16x16x32_bf16 v[160:163], v[100:103], v[208:211], v[160:163]\n\t"
            "v_cvt_pk_bf16_f32 v213, v214, v215\n\t"
            "v_mul_f32 v148, v201, v148\n\t"
            "v_mfma_f32_16x16x32_bf16 v[164:167], v[124:127], v[208:211], 0\n\t"
            "v_mul_f32 v149, v201, v149\n\t"
            "v_mul_f32 v150, v201, v150\n\t"
            "v_mfma_f32_16x16x32_bf16 v[164:167], v[120:123], v[204:207], v[164:167]\n\t"
            "ds_write_b64 v245, v[224:225]\n\t"
            "v_mfma_f32_16x16x32_bf16 v[224:227], v[168:171], v[184:187], 0\n\t"
            "v_mul_f32 v151, v201, v151\n\t"
            "v_cvt_pk_bf16_f32 v148, v148, v149\n\t"
            "v_mfma_f32_16x16x32_bf16 v[224:227], v[176:179], v[188:191], v[224:227]\n\t"
            "ds_write_b64 v245, v[212:213] offset:128\n\t"
            "v_mfma_f32_16x16x32_bf16 v[200:203], v[240:243], v[184:187], 0\n\t"
            "v_cvt_pk_bf16_f32 v149, v150, v151\n\t"
            "v_mfma_f32_16x16x32_bf16 v[200:203], v[240:243], v[188:191], v[200:203]\n\t"
            "ds_write_b64 v245, v[148:149] offset:256\n\t"
            "v_mfma_f32_16x16x32_bf16 v[212:215], v[192:195], v[184:187], 0\n\t"
            "v_mfma_f32_16x16x32_bf16 v[212:215], v[196:199], v[188:191], v[212:215]\n\t"
            "v_mfma_f32_16x16x32_bf16 v[184:187], v[216:219], v[184:187], 0\n\t"
            "v_mfma_f32_16x16x32_bf16 v[184:187], v[220:223], v[188:191], v[184:187]\n\t"
            "v_mfma_f32_16x16x32_bf16 v[148:151], v[28:31], v[208:211], 0\n\t"
            "ds_read_b128 v[208:211], v233 offset:128\n\t"
            "v_mfma_f32_16x16x32_bf16 v[148:151], v[24:27], v[204:207], v[148:151]\n\t"
            "ds_read_b128 v[204:207], v232 offset:128\n\t"
            "v_rcp_f32 v201, v200\n\t"
            "v_mfma_f32_16x16x32_bf16 v[188:191], v[192:195], v[180:183], 0\n\t"
            "v_mul_f32 v224, v201, v224\n\t"
            "v_mul_f32 v225, v201, v225\n\t"
            "v_mfma_f32_16x16x32_bf16 v[188:191], v[196:199], v[172:175], v[188:191]\n\t"
            "v_mul_f32 v226, v201, v226\n\t"
            "v_mul_f32 v227, v201, v227\n\t"
            "v_mul_f32 v212, v201, v212\n\t"
            "v_mul_f32 v213, v201, v213\n\t"
            "v_mul_f32 v214, v201, v214\n\t"
            "v_mul_f32 v215, v201, v215\n\t"
            "v_mul_f32 v184, v201, v184\n\t"
            "v_mul_f32 v185, v201, v185\n\t"
            "v_mul_f32 v186, v201, v186\n\t"
            "v_mul_f32 v187, v201, v187\n\t"
            "v_mfma_f32_16x16x32_bf16 v[200:203], v[240:243], v[180:183], 0\n\t"
            "v_cvt_pk_bf16_f32 v224, v224, v225\n\t"
            "v_cvt_pk_bf16_f32 v225, v226, v227\n\t"
            "v_mfma_f32_16x16x32_bf16 v[200:203], v[240:243], v[172:175], v[200:203]\n\t"
            "ds_write_b64 v245, v[224:225] offset:6144\n\t"
            "v_mfma_f32_16x16x32_bf16 v[224:227], v[168:171], v[180:183], 0\n\t"
            "v_cvt_pk_bf16_f32 v184, v184, v185\n\t"
            "v_cvt_pk_bf16_f32 v185, v186, v187\n\t"
            "v_mfma_f32_16x16x32_bf16 v[224:227], v[176:179], v[172:175], v[224:227]\n\t"
            "ds_read_b64 v[186:187], v234 offset:288\n\t"
            "ds_write_b64 v245, v[184:185] offset:6400\n\t"
            "s_waitcnt lgkmcnt(3)\n\t"
            "v_mfma_f32_16x16x32_bf16 v[144:147], v[8:11], v[204:207], v[144:147]\n\t"
            "ds_read_b64 v[184:185], v234 offset:256\n\t"
            "v_rcp_f32 v201, v200\n\t"
            "v_mfma_f32_16x16x32_bf16 v[144:147], v[12:15], v[208:211], v[144:147]\n\t"
            "v_mul_f32 v188, v201, v188\n\t"
            "v_mul_f32 v189, v201, v189\n\t"
            "v_mfma_f32_16x16x32_bf16 v[148:151], v[36:39], v[208:211], v[148:151]\n\t"
            "v_mul_f32 v190, v201, v190\n\t"
            "v_mul_f32 v191, v201, v191\n\t"
            "v_mfma_f32_16x16x32_bf16 v[148:151], v[32:35], v[204:207], v[148:151]\n\t"
            "v_cvt_pk_bf16_f32 v188, v188, v189\n\t"
            "v_cvt_pk_bf16_f32 v189, v190, v191\n\t"
            "v_mfma_f32_16x16x32_bf16 v[152:155], v[56:59], v[204:207], v[152:155]\n\t"
            "ds_read_b64 v[190:191], v234 offset:352\n\t"
            "v_mul_f32 v224, v201, v224\n\t"
            "v_mfma_f32_16x16x32_bf16 v[152:155], v[60:63], v[208:211], v[152:155]\n\t"
            "ds_write_b64 v245, v[188:189] offset:12416\n\t"
            "v_mfma_f32_16x16x32_bf16 v[156:159], v[84:87], v[208:211], v[156:159]\n\t"
            "ds_read_b64 v[188:189], v234 offset:320\n\t"
            "v_mul_f32 v225, v201, v225\n\t"
            "v_mfma_f32_16x16x32_bf16 v[156:159], v[80:83], v[204:207], v[156:159]\n\t"
            "v_cvt_pk_bf16_f32 v212, v212, v213\n\t"
            "v_mul_f32 v226, v201, v226\n\t"
            "v_mfma_f32_16x16x32_bf16 v[160:163], v[104:107], v[204:207], v[160:163]\n\t"
            "v_mul_f32 v227, v201, v227\n\t"
            "v_cvt_pk_bf16_f32 v224, v224, v225\n\t"
            "v_mfma_f32_16x16x32_bf16 v[160:163], v[108:111], v[208:211], v[160:163]\n\t"
            "v_cvt_pk_bf16_f32 v213, v214, v215\n\t"
            "v_cvt_pk_bf16_f32 v225, v226, v227\n\t"
            "v_mfma_f32_16x16x32_bf16 v[164:167], v[132:135], v[208:211], v[164:167]\n\t"
            "ds_read_b128 v[208:211], v233 offset:6144\n\t"
            "v_mfma_f32_16x16x32_bf16 v[164:167], v[128:131], v[204:207], v[164:167]\n\t"
            "ds_read_b128 v[204:207], v232 offset:6144\n\t"
            "ds_write_b64 v245, v[212:213] offset:6272\n\t"
            "v_mfma_f32_16x16x32_bf16 v[180:183], v[216:219], v[180:183], 0\n\t"
            "ds_read_b128 v[212:215], v232 offset:256\n\t"
            "v_mfma_f32_16x16x32_bf16 v[180:183], v[220:223], v[172:175], v[180:183]\n\t"
            "ds_write_b64 v245, v[224:225] offset:12288\n\t"
            "s_waitcnt lgkmcnt(5)\n\t"
            "v_mfma_f32_16x16x32_bf16 v[168:171], v[184:187], v[168:171], 0\n\t"
            "ds_read_b128 v[224:227], v233 offset:256\n\t"
            "v_mfma_f32_16x16x32_bf16 v[168:171], v[188:191], v[176:179], v[168:171]\n\t"
            "v_mfma_f32_16x16x32_bf16 v[192:195], v[184:187], v[192:195], 0\n\t"
            "v_mfma_f32_16x16x32_bf16 v[192:195], v[188:191], v[196:199], v[192:195]\n\t"
            "s_nop 0\n\t"
            "v_mul_f32 v180, v201, v180\n\t"
            "v_mfma_f32_16x16x32_bf16 v[216:219], v[184:187], v[216:219], 0\n\t"
            "v_mul_f32 v181, v201, v181\n\t"
            "v_mul_f32 v182, v201, v182\n\t"
            "v_mfma_f32_16x16x32_bf16 v[216:219], v[188:191], v[220:223], v[216:219]\n\t"
            "v_mul_f32 v183, v201, v183\n\t"
            "v_cvt_pk_bf16_f32 v180, v180, v181\n\t"
            "v_mfma_f32_16x16x32_bf16 v[200:203], v[240:243], v[184:187], 0\n\t"
            "v_cvt_pk_bf16_f32 v181, v182, v183\n\t"
            "v_mfma_f32_16x16x32_bf16 v[200:203], v[240:243], v[188:191], v[200:203]\n\t"
            "ds_write_b64 v245, v[180:181] offset:12544\n\t"
            "s_waitcnt lgkmcnt(1)\n\t"
            "v_mfma_f32_16x16x32_bf16 v[144:147], v[16:19], v[212:215], v[144:147]\n\t"
            "v_mfma_f32_16x16x32_bf16 v[144:147], v[20:23], v[224:227], v[144:147]\n\t"
            "v_mfma_f32_16x16x32_bf16 v[148:151], v[44:47], v[224:227], v[148:151]\n\t"
            "v_mfma_f32_16x16x32_bf16 v[148:151], v[40:43], v[212:215], v[148:151]\n\t"
            "v_mfma_f32_16x16x32_bf16 v[152:155], v[64:67], v[212:215], v[152:155]\n\t"
            "s_nop 0\n\t"
            "v_cndmask_b32_e64 v203, v168, v192, s[60:61]\n\t"
            "v_mfma_f32_16x16x32_bf16 v[152:155], v[68:71], v[224:227], v[152:155]\n\t"
            "v_rcp_f32 v201, v200\n\t"
            "v_mfma_f32_16x16x32_bf16 v[156:159], v[92:95], v[224:227], v[156:159]\n\t"
            "v_cndmask_b32_e64 v203, v203, v216, s[62:63]\n\t"
            "v_xad_u32 v202, s73, v254, v255\n\t"
            "v_mfma_f32_16x16x32_bf16 v[156:159], v[88:91], v[212:215], v[156:159]\n\t"
            "v_mul_f32_dpp v203, v201, v203 quad_perm:[0,0,0,0] row_mask:0xf bank_mask:0xf\n\t"
            "v_cvt_pk_bf16_f32 v203, v203, 0\n\t"
            "v_mfma_f32_16x16x32_bf16 v[160:163], v[112:115], v[212:215], v[160:163]\n\t"
            "v_mul_f32 v144, v236, v144\n\t"
            "v_mul_f32 v145, v236, v145\n\t"
            "v_mfma_f32_16x16x32_bf16 v[160:163], v[116:119], v[224:227], v[160:163]\n\t"
            "s_mov_b64 exec, s[68:69]\n\t"
            "ds_write_b16 v202, v203\n\t"
            "s_mov_b64 exec, 1\n\t"
            "ds_add_u32 v253, v246 offset:7856\n\t"
            "s_mov_b64 exec, -1\n\t"
            "v_mfma_f32_16x16x32_bf16 v[164:167], v[140:143], v[224:227], v[164:167]\n\t"
            "ds_read_b128 v[224:227], v233 offset:6272\n\t"
            "v_mul_f32 v152, v236, v152\n\t"
            "v_mfma_f32_16x16x32_bf16 v[164:167], v[136:139], v[212:215], v[164:167]\n\t"
            "ds_read_b128 v[212:215], v232 offset:6272\n\t"
            "v_mul_f32 v153, v236, v153\n\t"
            "v_mfma_f32_16x16x32_bf16 v[168:171], v[0:3], v[204:207], 0\n\t"
            "v_mul_f32 v146, v236, v146\n\t"
            "v_mul_f32 v147, v236, v147\n\t"
            "v_mfma_f32_16x16x32_bf16 v[168:171], v[4:7], v[208:211], v[168:171]\n\t"
            "v_mul_f32 v154, v236, v154\n\t"
            "v_mul_f32 v155, v236, v155\n\t"
            "v_mfma_f32_16x16x32_bf16 v[172:175], v[28:31], v[208:211], 0\n\t"
            "v_cvt_pk_bf16_f32 v144, v144, v145\n\t"
            "v_cvt_pk_bf16_f32 v152, v152, v153\n\t"
            "v_mfma_f32_16x16x32_bf16 v[172:175], v[24:27], v[204:207], v[172:175]\n\t"
            "v_cvt_pk_bf16_f32 v156, v156, v157\n\t"
            "v_mul_f32 v148, v236, v148\n\t"
            "v_mfma_f32_16x16x32_bf16 v[176:179], v[48:51], v[204:207], 0\n\t"
            "v_mul_f32 v149, v236, v149\n\t"
            "v_mul_f32 v150, v236, v150\n\t"
            "v_mfma_f32_16x16x32_bf16 v[176:179], v[52:55], v[208:211], v[176:179]\n\t"
            "v_mul_f32 v151, v236, v151\n\t"
            "v_cvt_pk_bf16_f32 v145, v146, v147\n\t"
            "v_mfma_f32_16x16x32_bf16 v[180:183], v[76:79], v[208:211], 0\n\t"
            "v_cvt_pk_bf16_f32 v153, v154, v155\n\t"
            "v_cvt_pk_bf16_f32 v157, v158, v159\n\t"
            "v_mfma_f32_16x16x32_bf16 v[180:183], v[72:75], v[204:207], v[180:183]\n\t"
            "v_cvt_pk_bf16_f32 v146, v148, v149\n\t"
            "v_cvt_pk_bf16_f32 v147, v150, v151\n\t"
            "v_mfma_f32_16x16x32_bf16 v[184:187], v[96:99], v[204:207], 0\n\t"
            "v_cvt_pk_bf16_f32 v158, v160, v161\n\t"
            "v_cvt_pk_bf16_f32 v159, v162, v163\n\t"
            "v_mfma_f32_16x16x32_bf16 v[184:187], v[100:103], v[208:211], v[184:187]\n\t"
            "v_cvt_pk_bf16_f32 v154, v164, v165\n\t"
            "v_cvt_pk_bf16_f32 v155, v166, v167\n\t"
            "v_mfma_f32_16x16x32_bf16 v[188:191], v[124:127], v[208:211], 0\n\t"
            "ds_read_b128 v[208:211], v233 offset:6400\n\t"
            "v_mul_f32 v236, 0x3e553b94, v236\n\t"
            "v_mfma_f32_16x16x32_bf16 v[188:191], v[120:123], v[204:207], v[188:191]\n\t"
            "ds_read_b128 v[204:207], v232 offset:6400\n\t"
            "s_waitcnt lgkmcnt(2)\n\t"
            "v_mfma_f32_16x16x32_bf16 v[168:171], v[8:11], v[212:215], v[168:171]\n\t"
            "v_mfma_f32_16x16x32_bf16 v[168:171], v[12:15], v[224:227], v[168:171]\n\t"
            "v_mfma_f32_16x16x32_bf16 v[172:175], v[36:39], v[224:227], v[172:175]\n\t"
            "v_mfma_f32_16x16x32_bf16 v[172:175], v[32:35], v[212:215], v[172:175]\n\t"
            "v_mfma_f32_16x16x32_bf16 v[176:179], v[56:59], v[212:215], v[176:179]\n\t"
            "v_mfma_f32_16x16x32_bf16 v[176:179], v[60:63], v[224:227], v[176:179]\n\t"
            "v_mfma_f32_16x16x32_bf16 v[180:183], v[84:87], v[224:227], v[180:183]\n\t"
            "v_mfma_f32_16x16x32_bf16 v[180:183], v[80:83], v[212:215], v[180:183]\n\t"
            "v_mfma_f32_16x16x32_bf16 v[184:187], v[104:107], v[212:215], v[184:187]\n\t"
            "v_mfma_f32_16x16x32_bf16 v[184:187], v[108:111], v[224:227], v[184:187]\n\t"
            "v_mfma_f32_16x16x32_bf16 v[188:191], v[132:135], v[224:227], v[188:191]\n\t"
            "ds_read_b128 v[224:227], v233 offset:12288\n\t"
            "v_mfma_f32_16x16x32_bf16 v[188:191], v[128:131], v[212:215], v[188:191]\n\t"
            "ds_read_b128 v[212:215], v232 offset:12288\n\t"
            "s_waitcnt lgkmcnt(2)\n\t"
            "v_mfma_f32_16x16x32_bf16 v[168:171], v[16:19], v[204:207], v[168:171]\n\t"
            "v_mfma_f32_16x16x32_bf16 v[168:171], v[20:23], v[208:211], v[168:171]\n\t"
            "v_mfma_f32_16x16x32_bf16 v[172:175], v[44:47], v[208:211], v[172:175]\n\t"
            "v_mfma_f32_16x16x32_bf16 v[172:175], v[40:43], v[204:207], v[172:175]\n\t"
            "v_mfma_f32_16x16x32_bf16 v[176:179], v[64:67], v[204:207], v[176:179]\n\t"
            "v_mfma_f32_16x16x32_bf16 v[176:179], v[68:71], v[208:211], v[176:179]\n\t"
            "v_mfma_f32_16x16x32_bf16 v[180:183], v[92:95], v[208:211], v[180:183]\n\t"
            "v_mfma_f32_16x16x32_bf16 v[180:183], v[88:91], v[204:207], v[180:183]\n\t"
            "v_mfma_f32_16x16x32_bf16 v[184:187], v[112:115], v[204:207], v[184:187]\n\t"
            "v_mfma_f32_16x16x32_bf16 v[184:187], v[116:119], v[208:211], v[184:187]\n\t"
            "v_mfma_f32_16x16x32_bf16 v[188:191], v[140:143], v[208:211], v[188:191]\n\t"
            "ds_read_b128 v[208:211], v233 offset:12416\n\t"
            "v_mfma_f32_16x16x32_bf16 v[188:191], v[136:139], v[204:207], v[188:191]\n\t"
            "ds_read_b128 v[204:207], v232 offset:12416\n\t"
            "s_cmp_eq_u32 s91, 0\n\t"
            "s_cbranch_scc1 .Lwa_loop%=\n\t"
            "s_mov_b32 s91, 0\n\t"
            "s_waitcnt vmcnt(0)\n\t"
            "s_cmp_eq_u32 s81, 0\n\t"
            "s_cbranch_scc1 .Lwa_h6%=\n\t"
            "s_mov_b32 s89, 0x10000\n\t"
            ".Lwa_h4%=:\n\t"
            "v_readfirstlane_b32 s49, v228\n\t"
            "v_readfirstlane_b32 vcc_lo, v229\n\t"
            "s_xor_b32 s49, s49, s82\n\t"
            "s_xor_b32 vcc_lo, vcc_lo, s83\n\t"
            "s_or_b32 s49, s49, vcc_lo\n\t"
            "s_cbranch_scc0 .Lwa_h5%=\n\t"
            "s_sub_u32 s89, s89, 1\n\t"
            "s_cbranch_scc1 .Lwa_h5%=\n\t"
            "s_sleep 2\n\t"
            "buffer_load_dwordx3 v[228:230], off, s[84:87], s81 offset:1216 sc1\n\t"
            "s_waitcnt vmcnt(0)\n\t"
            "s_branch .Lwa_h4%=\n\t"
            ".Lwa_h5%=:\n\t"
            "s_lshl_b32 s49, s70, 2\n\t"
            "s_add_u32 s49, s49, 163520\n\t"
            "v_mov_b32 v228, s49\n\t"
            "s_mul_i32 s49, s70, 800\n\t"
            "s_add_u32 m0, s49, 113760\n\t"
            "ds_write_b32 v228, v230 offset:12\n\t"
            "s_mov_b32 exec_hi, 0x3ffff\n\t"
            "buffer_load_dwordx4 v248, s[84:87], s81 offen sc1 lds\n\t"
            "s_mul_i32 s49, s70, 384\n\t"
            "s_add_u32 m0, s49, 130176\n\t"
            "s_add_u32 s81, s81, 800\n\t"
            "s_mov_b64 exec, 0xffffff\n\t"
            "buffer_load_dwordx4 v248, s[84:87], s81 offen sc1 lds\n\t"
            "s_mov_b64 exec, -1\n\t"
            "s_mov_b32 s81, 0\n\t"
            "s_waitcnt lgkmcnt(0)\n\t"
            ".Lwa_h6%=:\n\t"
            "s_cmp_eq_u32 s90, 0\n\t"
            "s_cbranch_scc1 .Lwa_loop%=\n\t"
            "v_readfirstlane_b32 s49, v231\n\t"
            "s_sub_u32 s90, s90, 1\n\t"
            "s_cmp_lt_u32 s49, 209\n\t"
            "s_cselect_b32 s90, s90, 0\n\t"
            "s_cbranch_scc0 .Lwa_loop%=\n\t"
            "s_mul_i32 s81, s49, 1280\n\t"
            "s_add_u32 s81, s81, 303104\n\t"
            "s_branch .Lwa_loop%=\n\t"
            ".Lwa_h1%=:\n\t"
            "s_cmp_lt_u32 s70, 20\n\t"
            "s_cbranch_scc1 .Lwa_h2%=\n\t"
            "s_lshl_b32 s49, s70, 2\n\t"
            "s_add_u32 s49, s49, 163520\n\t"
            "v_mov_b32 v228, s49\n\t"
            "ds_read_b32 v228, v228\n\t"
            "s_waitcnt lgkmcnt(0)\n\t"
            "v_readfirstlane_b32 s49, v228\n\t"
            "s_cmp_eq_u32 s49, 0x7ffffff\n\t"
            "s_cbranch_scc1 .Lwa_last%=\n\t"
            "s_branch .Lwa_r1%=\n\t"
            ".Lwa_h2%=:\n\t"
            "s_waitcnt vmcnt(0)\n\t"
            "s_or_b32 s91, s81, s90\n\t"
            "s_cmp_eq_u32 s81, 0\n\t"
            "s_cbranch_scc1 .Lwa_h3%=\n\t"
            "buffer_load_dwordx3 v[228:230], off, s[84:87], s81 offset:1216 sc1\n\t"
            ".Lwa_h3%=:\n\t"
            "s_cmp_eq_u32 s90, 0\n\t"
            "s_cbranch_scc1 .Lwa_r1%=\n\t"
            "v_mov_b32 v231, 1\n\t"
            "s_mov_b32 s49, 301184\n\t"
            "s_mov_b64 exec, 1\n\t"
            "buffer_atomic_add v231, off, s[84:87], s49 sc0\n\t"
            "s_mov_b64 exec, -1\n\t"
            "s_branch .Lwa_r1%=\n\t"
            ".Lwa_last%=:\n\t"
            "s_setprio 2\n\t"
            "s_waitcnt lgkmcnt(0)\n\t"
            "s_nop 7\n\t"
            "v_mfma_f32_16x16x32_bf16 v[156:159], v[240:243], v[184:187], 0\n\t"
            "ds_read_b64 v[176:177], v247\n\t"
            "ds_read_b64 v[196:197], v247 offset:1536\n\t"
            "v_mfma_f32_16x16x32_bf16 v[156:159], v[240:243], v[188:191], v[156:159]\n\t"
            "ds_read_b64 v[220:221], v247 offset:3072\n\t"
            "v_xor_b32 v234, s73, v254\n\t"
            "v_mfma_f32_16x16x32_bf16 v[228:231], v[240:243], v[148:151], 0\n\t"
            "ds_read_u16 v178, v234\n\t"
            "ds_read_u16 v198, v234 offset:128\n\t"
            "v_mfma_f32_16x16x32_bf16 v[228:231], v[240:243], v[164:167], v[228:231]\n\t"
            "ds_read_u16 v222, v234 offset:256\n\t"
            "v_add_u32 v235, s45, v248\n\t"
            "v_mfma_f32_16x16x32_bf16 v[208:211], v[240:243], v[180:183], 0\n\t"
            "ds_read_b128 v[168:171], v235\n\t"
            "ds_read_b128 v[192:195], v235 offset:1536\n\t"
            "v_mfma_f32_16x16x32_bf16 v[208:211], v[240:243], v[172:175], v[208:211]\n\t"
            "ds_read_b128 v[216:219], v235 offset:3072\n\t"
            "v_mov_b32 v179, 0\n\t"
            "v_mov_b32 v199, 0\n\t"
            "v_mov_b32 v223, 0\n\t"
            "v_rcp_f32 v157, v156\n\t"
            "v_mad_u32_u24 v234, v246, s75, v253\n\t"
            "v_rcp_f32 v229, v228\n\t"
            "s_nop 0\n\t"
            "v_rcp_f32 v209, v208\n\t"
            "s_waitcnt lgkmcnt(2)\n\t"
            "v_mfma_f32_16x16x32_bf16 v[144:147], v[168:171], v[184:187], 0\n\t"
            "v_mfma_f32_16x16x32_bf16 v[144:147], v[176:179], v[188:191], v[144:147]\n\t"
            "s_waitcnt lgkmcnt(1)\n\t"
            "v_mfma_f32_16x16x32_bf16 v[152:155], v[192:195], v[184:187], 0\n\t"
            "v_mfma_f32_16x16x32_bf16 v[152:155], v[196:199], v[188:191], v[152:155]\n\t"
            "s_waitcnt lgkmcnt(0)\n\t"
            "v_mfma_f32_16x16x32_bf16 v[184:187], v[216:219], v[184:187], 0\n\t"
            "v_mfma_f32_16x16x32_bf16 v[184:187], v[220:223], v[188:191], v[184:187]\n\t"
            "ds_read_b64 v[188:189], v234 offset:320\n\t"
            "ds_read_b64 v[190:191], v234 offset:352\n\t"
            "v_mfma_f32_16x16x32_bf16 v[224:227], v[168:171], v[148:151], 0\n\t"
            "v_mul_f32 v144, v157, v144\n\t"
            "v_mul_f32 v145, v157, v145\n\t"
            "v_mfma_f32_16x16x32_bf16 v[224:227], v[176:179], v[164:167], v[224:227]\n\t"
            "v_mul_f32 v152, v157, v152\n\t"
            "v_mul_f32 v153, v157, v153\n\t"
            "v_mfma_f32_16x16x32_bf16 v[212:215], v[192:195], v[148:151], 0\n\t"
            "v_mul_f32 v184, v157, v184\n\t"
            "v_mul_f32 v185, v157, v185\n\t"
            "v_mfma_f32_16x16x32_bf16 v[212:215], v[196:199], v[164:167], v[212:215]\n\t"
            "v_mul_f32 v186, v157, v186\n\t"
            "v_mul_f32 v187, v157, v187\n\t"
            "v_mfma_f32_16x16x32_bf16 v[160:163], v[168:171], v[180:183], 0\n\t"
            "v_cvt_pk_bf16_f32 v184, v184, v185\n\t"
            "v_cvt_pk_bf16_f32 v185, v186, v187\n\t"
            "v_mfma_f32_16x16x32_bf16 v[160:163], v[176:179], v[172:175], v[160:163]\n\t"
            "ds_read_b64 v[186:187], v234 offset:288\n\t"
            "ds_write_b64 v245, v[184:185] offset:6400\n\t"
            "v_mfma_f32_16x16x32_bf16 v[204:207], v[192:195], v[180:183], 0\n\t"
            "ds_read_b64 v[184:185], v234 offset:256\n\t"
            "v_mul_f32 v224, v229, v224\n\t"
            "v_mfma_f32_16x16x32_bf16 v[204:207], v[196:199], v[172:175], v[204:207]\n\t"
            "v_mul_f32 v225, v229, v225\n\t"
            "v_mul_f32 v212, v229, v212\n\t"
            "v_mfma_f32_16x16x32_bf16 v[148:151], v[216:219], v[148:151], 0\n\t"
            "v_mul_f32 v213, v229, v213\n\t"
            "v_mul_f32 v160, v209, v160\n\t"
            "v_mfma_f32_16x16x32_bf16 v[148:151], v[220:223], v[164:167], v[148:151]\n\t"
            "v_mul_f32 v161, v209, v161\n\t"
            "v_mul_f32 v226, v229, v226\n\t"
            "v_mfma_f32_16x16x32_bf16 v[180:183], v[216:219], v[180:183], 0\n\t"
            "v_mul_f32 v204, v209, v204\n\t"
            "v_mul_f32 v205, v209, v205\n\t"
            "v_mfma_f32_16x16x32_bf16 v[180:183], v[220:223], v[172:175], v[180:183]\n\t"
            "v_mul_f32 v227, v229, v227\n\t"
            "v_cvt_pk_bf16_f32 v224, v224, v225\n\t"
            "v_mul_f32 v148, v229, v148\n\t"
            "v_mul_f32 v149, v229, v149\n\t"
            "v_mul_f32 v214, v229, v214\n\t"
            "v_mul_f32 v215, v229, v215\n\t"
            "v_cvt_pk_bf16_f32 v212, v212, v213\n\t"
            "s_waitcnt lgkmcnt(0)\n\t"
            "v_mfma_f32_16x16x32_bf16 v[200:203], v[240:243], v[184:187], 0\n\t"
            "v_mul_f32 v180, v209, v180\n\t"
            "v_mul_f32 v181, v209, v181\n\t"
            "v_mfma_f32_16x16x32_bf16 v[200:203], v[240:243], v[188:191], v[200:203]\n\t"
            "v_mul_f32 v150, v229, v150\n\t"
            "v_mul_f32 v151, v229, v151\n\t"
            "v_mfma_f32_16x16x32_bf16 v[168:171], v[184:187], v[168:171], 0\n\t"
            "v_cvt_pk_bf16_f32 v148, v148, v149\n\t"
            "v_mul_f32 v146, v157, v146\n\t"
            "v_mfma_f32_16x16x32_bf16 v[168:171], v[188:191], v[176:179], v[168:171]\n\t"
            "v_mul_f32 v147, v157, v147\n\t"
            "v_cvt_pk_bf16_f32 v144, v144, v145\n\t"
            "v_mfma_f32_16x16x32_bf16 v[192:195], v[184:187], v[192:195], 0\n\t"
            "v_rcp_f32 v201, v200\n\t"
            "v_mfma_f32_16x16x32_bf16 v[192:195], v[188:191], v[196:199], v[192:195]\n\t"
            "v_mul_f32 v154, v157, v154\n\t"
            "v_mul_f32 v155, v157, v155\n\t"
            "v_mfma_f32_16x16x32_bf16 v[216:219], v[184:187], v[216:219], 0\n\t"
            "v_cvt_pk_bf16_f32 v152, v152, v153\n\t"
            "v_mul_f32 v162, v209, v162\n\t"
            "v_mfma_f32_16x16x32_bf16 v[216:219], v[188:191], v[220:223], v[216:219]\n\t"
            "v_mul_f32 v163, v209, v163\n\t"
            "v_cvt_pk_bf16_f32 v160, v160, v161\n\t"
            "v_cndmask_b32_e64 v203, v168, v192, s[60:61]\n\t"
            "v_mul_f32 v206, v209, v206\n\t"
            "v_mul_f32 v207, v209, v207\n\t"
            "v_cvt_pk_bf16_f32 v204, v204, v205\n\t"
            "v_mul_f32 v182, v209, v182\n\t"
            "v_mul_f32 v183, v209, v183\n\t"
            "v_cvt_pk_bf16_f32 v180, v180, v181\n\t"
            "v_cndmask_b32_e64 v203, v203, v216, s[62:63]\n\t"
            "v_cvt_pk_bf16_f32 v225, v226, v227\n\t"
            "v_cvt_pk_bf16_f32 v213, v214, v215\n\t"
            "v_cvt_pk_bf16_f32 v149, v150, v151\n\t"
            "v_cvt_pk_bf16_f32 v145, v146, v147\n\t"
            "v_cvt_pk_bf16_f32 v153, v154, v155\n\t"
            "v_cvt_pk_bf16_f32 v161, v162, v163\n\t"
            "v_cvt_pk_bf16_f32 v205, v206, v207\n\t"
            "v_cvt_pk_bf16_f32 v181, v182, v183\n\t"
            "v_mul_f32_dpp v203, v201, v203 quad_perm:[0,0,0,0] row_mask:0xf bank_mask:0xf\n\t"
            "v_xad_u32 v202, s73, v254, v255\n\t"
            "v_cvt_pk_bf16_f32 v203, v203, 0\n\t"
            "ds_write_b64 v245, v[224:225]\n\t"
            "ds_write_b64 v245, v[212:213] offset:128\n\t"
            "ds_write_b64 v245, v[148:149] offset:256\n\t"
            "ds_write_b64 v245, v[144:145] offset:6144\n\t"
            "ds_write_b64 v245, v[152:153] offset:6272\n\t"
            "ds_write_b64 v245, v[160:161] offset:12288\n\t"
            "ds_write_b64 v245, v[204:205] offset:12416\n\t"
            "ds_write_b64 v245, v[180:181] offset:12544\n\t"
            "s_mov_b64 exec, s[68:69]\n\t"
            "ds_write_b16 v202, v203\n\t"
            "s_mov_b64 exec, 1\n\t"
            "ds_add_u32 v253, v246 offset:7856\n\t"
            "s_mov_b64 exec, -1\n\t"
            ".Lwa_done%=:\n\t"
            "s_waitcnt lgkmcnt(0)\n\t"
            "s_nop 7\n\t"
            : "+{v204}"(pST_), "+{v205}"(aRSw_), "+{v206}"(aPLw_), "+{v207}"(aRSr_), "+{v208}"(g16_), "+{v209}"(g8_), "+{v210}"(loVL_), "+{v211}"(loOL_), "+{v200}"(on), "+{v250}"(c0), "+{v251}"(c1), "+{v252}"(c2), "+{v253}"(c3), "+{v254}"(c4), "+{v255}"(c5),
              "+{v238}"(c6), "+{v239}"(c7), "+{v240}"(c8), "+{v241}"(c9), "+{s89}"(s89), "+{s46}"(s46), "+{s48}"(s48), "+{s70}"(s70), "+{s72}"(s72), "+{s73}"(s73), "+{s74}"(s74), "+{s76}"(s76), "+{s77}"(s77)
            : "{v244}"(pE_), "{v245}"(aO_), "{v246}"(sel_), "{v247}"(aVB_), "{s47}"((ldsb + X_OFF) ^ (ldsb + X1_OFF)),
              "{s[58:59]}"(0xFFFFull), "{s75}"(rsb), "{s45}"(ldsb + V_OFF + w * 4608), "{s[68:69]}"(0xFFFFFFFFFFFFull),
              "{s[84:87]}"(wr), "{s88}"(touch), "{s80}"(wave == 0 ? 16 : 20), "{s71}"(ldsb + O_OFF), "{s[54:55]}"(mrow), "{s[56:57]}"(0xF0000F0000F0000Full),
              "{s[78:79]}"(mrow & 0xF0000F0000F0000Full)
            : "v144", "v145", "v146", "v147", "v148", "v149", "v150", "v151", "v152", "v153", "v154", "v155", "v156", "v157", "v158", "v159", "v160", "v161", "v162", "v163", "v164", "v165", "v166", "v167", "v168", "v169", "v170", "v171", "v172", "v173", "v174", "v175", "v176", "v177", "v178", "v179", "v180", "v181", "v182", "v183", "v184", "v185", "v186", "v187", "v188", "v189", "v190", "v191", "v192", "v193", "v194", "v195", "v196", "v197", "v198", "v199", "v201", "v202", "v203", "v212", "v213", "v214", "v215", "v216", "v217", "v218", "v219", "v220", "v221", "v222", "v223", "v224", "v225", "v226", "v227", "v228", "v229", "v230", "v231", "v232", "v233", "v234", "v235", "v236", "v237", "v242", "v243", "v248", "v249", "v0", "v1", "v2", "v3", "v4", "v5", "v6", "v7", "v8", "v9", "v10", "v11", "v12", "v13", "v14", "v15", "v16", "v17", "v18", "v19", "v20", "v21", "v22", "v23", "v24", "v25", "v26", "v27", "v28", "v29", "v30", "v31", "v32", "v33", "v34", "v35", "v36", "v37", "v38", "v39", "v40", "v41", "v42", "v43", "v44", "v45", "v46", "v47", "v48", "v49", "v50", "v51", "v52", "v53", "v54", "v55", "v56", "v57", "v58", "v59", "v60", "v61", "v62", "v63", "v64", "v65", "v66", "v67", "v68", "v69", "v70", "v71", "v72", "v73", "v74", "v75", "v76", "v77", "v78", "v79", "v80", "v81", "v82", "v83", "v84", "v85", "v86", "v87", "v88", "v89", "v90", "v91", "v92", "v93", "v94", "v95", "v96", "v97", "v98", "v99", "v100", "v101", "v102", "v103", "v104", "v105", "v106", "v107", "v108", "v109", "v110", "v111", "v112", "v113", "v114", "v115", "v116", "v117", "v118", "v119", "v120", "v121", "v122", "v123", "v124", "v125", "v126", "v127", "v128", "v129", "v130", "v131", "v132", "v133", "v134", "v135", "v136", "v137", "v138", "v139", "v140", "v141", "v142", "v143",  "m0", "s60", "s61", "s62", "s63", "s64", "s65", "s66", "s67", "s81", "s82", "s83", "s90", "s91", "s49", "vcc", "scc", "memory");

        }
        if (wave == 0 && blockIdx.x == 0) {
            int ln_;
            asm volatile("v_mbcnt_lo_u32_b32 %0, -1, 0\n\tv_mbcnt_hi_u32_b32 %0, -1, %0" : "=v"(ln_));
            KARGS
            unsigned long long tg;
            asm volatile("s_load_dwordx2 %0, %1, %2\n\ts_waitcnt lgkmcnt(0)" : "=s"(tg) : "s"(wb), "n"(EPO_OFF) : "memory");
            const unsigned lo = (unsigned)tg + 1u, hi = (unsigned)(tg >> 32);
            const auto wq = __builtin_amdgcn_make_buffer_rsrc((void*)wb, 0, WS_BYTES, 0x00020000);
            for (int it_ = 0; it_ < 100000; ++it_) {
                bool bad = false;
#pragma unroll
                for (int k = 0; k < 2; ++k) {
                    const u32x4 a_ = __builtin_amdgcn_raw_buffer_load_b128(wq, ARR_OFF + ln_ * 16, k * 1024, SC1);
                    bad |= a_[0] != lo || a_[1] != hi || a_[2] != lo || a_[3] != hi;
                }
                if (__builtin_amdgcn_ballot_w64(bad) == 0ull) break;
                __builtin_amdgcn_s_sleep(2);
            }
            if (ln_ == 0) __builtin_amdgcn_raw_buffer_store_b64((u32x2){lo, hi}, wq, EPO_OFF, 0, SC1);
        }
        asm volatile("wk_code_end:");
        __builtin_amdgcn_endpgm();
    } else if (wave < 6) {
        LANE_SETUP
        {
            const unsigned rsb = ldsb + RSX_OFF;
            const unsigned pE_ = fE - X_OFF;
            const unsigned aVAw_ = ldsb + V_OFF + w * 9216 + lane * 16, aVBw_ = ldsb + V_OFF + w * 9216 + 1024 + lane * 8;
            const unsigned pST_ = lane * 16;
            unsigned aRSw_ = rsb + 256 + lane * 4, aRSr_ = rsb + (lane & 48);
            B_SCALARS
            unsigned s44 = 4;
            asm volatile(
            "s_waitcnt lgkmcnt(0)\n\t"
            "s_mov_b32 s83, 0x10000\n\t"
            "s_mov_b32 s84, -47104\n\t"
            "s_nop 7\n\t"
            "v_mov_b32 v200, v250\n\t"
            "v_mov_b32 v201, v251\n\t"
            "v_mov_b32 v202, v253\n\t"
            "s_mov_b32 s38, s67\n\t"
            "s_mov_b32 s67, 74496\n\t"
            "ds_read_b64 v[250:251], v203\n\t"
            "ds_read_b32 v253, v203 offset:8\n\t"
            ".Lwk_b1%=:\n\t"
            "v_mov_b32 v236, 0\n\t"
            "ds_read_b32 v236, v236 offset:7864\n\t"
            "s_waitcnt lgkmcnt(0)\n\t"
            "v_readfirstlane_b32 s77, v236\n\t"
            "s_cmp_ge_u32 s77, 4\n\t"
            "s_cbranch_scc1 .Lwk_b2%=\n\t"
            "s_cmp_eq_u32 s83, 0\n\t"
            "s_cbranch_scc1 .Lwk_b2%=\n\t"
            "s_sub_u32 s83, s83, 1\n\t"
            "s_sleep 1\n\t"
            "s_branch .Lwk_b1%=\n\t"
            ".Lwk_b2%=:\n\t"
            "s_waitcnt vmcnt(0)\n\t"
            "s_call_b64 vcc, wk_conv\n\t"
            "v_mov_b32 v250, v200\n\t"
            "v_mov_b32 v251, v201\n\t"
            "v_mov_b32 v253, v202\n\t"
            "s_mov_b32 s67, s38\n\t"
            "s_setprio 3\n\t"
            "s_barrier\n\t"
            "buffer_load_dwordx4 v[140:143], v245, s[28:31], s82 offen sc1\n\t"
            "s_add_u32 s49, s33, 0\n\t"
            "buffer_load_dwordx4 v[0:3], v245, s[28:31], s49 offen sc1\n\t"
            "s_add_u32 s49, s33, 6144\n\t"
            "buffer_load_dwordx4 v[24:27], v245, s[28:31], s49 offen sc1\n\t"
            "s_add_u32 s49, s33, 12288\n\t"
            "buffer_load_dwordx4 v[48:51], v245, s[28:31], s49 offen sc1\n\t"
            "s_add_u32 s49, s33, 18432\n\t"
            "buffer_load_dwordx4 v[72:75], v245, s[28:31], s49 offen sc1\n\t"
            "s_add_u32 s49, s33, 24576\n\t"
            "buffer_load_dwordx4 v[96:99], v245, s[28:31], s49 offen sc1\n\t"
            "s_add_u32 s49, s33, 30720\n\t"
            "buffer_load_dwordx4 v[120:123], v245, s[28:31], s49 offen sc1\n\t"
            "s_add_u32 s49, s33, 1024\n\t"
            "buffer_load_dwordx4 v[4:7], v245, s[28:31], s49 offen sc1\n\t"
            "s_add_u32 s49, s33, 7168\n\t"
            "buffer_load_dwordx4 v[28:31], v245, s[28:31], s49 offen sc1\n\t"
            "s_add_u32 s49, s33, 13312\n\t"
            "buffer_load_dwordx4 v[52:55], v245, s[28:31], s49 offen sc1\n\t"
            "s_add_u32 s49, s33, 19456\n\t"
            "buffer_load_dwordx4 v[76:79], v245, s[28:31], s49 offen sc1\n\t"
            "s_add_u32 s49, s33, 25600\n\t"
            "buffer_load_dwordx4 v[100:103], v245, s[28:31], s49 offen sc1\n\t"
            "s_add_u32 s49, s33, 31744\n\t"
            "buffer_load_dwordx4 v[124:127], v245, s[28:31], s49 offen sc1\n\t"
            "s_add_u32 s49, s33, 2048\n\t"
            "buffer_load_dwordx4 v[8:11], v245, s[28:31], s49 offen sc1\n\t"
            "s_add_u32 s49, s33, 8192\n\t"
            "buffer_load_dwordx4 v[32:35], v245, s[28:31], s49 offen sc1\n\t"
            "s_add_u32 s49, s33, 14336\n\t"
            "buffer_load_dwordx4 v[56:59], v245, s[28:31], s49 offen sc1\n\t"
            "s_add_u32 s49, s33, 20480\n\t"
            "buffer_load_dwordx4 v[80:83], v245, s[28:31], s49 offen sc1\n\t"
            "s_add_u32 s49, s33, 26624\n\t"
            "buffer_load_dwordx4 v[104:107], v245, s[28:31], s49 offen sc1\n\t"
            "s_add_u32 s49, s33, 32768\n\t"
            "buffer_load_dwordx4 v[128:131], v245, s[28:31], s49 offen sc1\n\t"
            "s_add_u32 s49, s33, 3072\n\t"
            "buffer_load_dwordx4 v[12:15], v245, s[28:31], s49 offen sc1\n\t"
            "s_add_u32 s49, s33, 9216\n\t"
            "buffer_load_dwordx4 v[36:39], v245, s[28:31], s49 offen sc1\n\t"
            "s_add_u32 s49, s33, 15360\n\t"
            "buffer_load_dwordx4 v[60:63], v245, s[28:31], s49 offen sc1\n\t"
            "s_add_u32 s49, s33, 21504\n\t"
            "buffer_load_dwordx4 v[84:87], v245, s[28:31], s49 offen sc1\n\t"
            "s_add_u32 s49, s33, 27648\n\t"
            "buffer_load_dwordx4 v[108:111], v245, s[28:31], s49 offen sc1\n\t"
            "s_add_u32 s49, s33, 33792\n\t"
            "buffer_load_dwordx4 v[132:135], v245, s[28:31], s49 offen sc1\n\t"
            "s_add_u32 s49, s33, 4096\n\t"
            "buffer_load_dwordx4 v[16:19], v245, s[28:31], s49 offen sc1\n\t"
            "s_add_u32 s49, s33, 10240\n\t"
            "buffer_load_dwordx4 v[40:43], v245, s[28:31], s49 offen sc1\n\t"
            "s_add_u32 s49, s33, 16384\n\t"
            "buffer_load_dwordx4 v[64:67], v245, s[28:31], s49 offen sc1\n\t"
            "s_add_u32 s49, s33, 22528\n\t"
            "buffer_load_dwordx4 v[88:91], v245, s[28:31], s49 offen sc1\n\t"
            "s_add_u32 s49, s33, 28672\n\t"
            "buffer_load_dwordx4 v[112:115], v245, s[28:31], s49 offen sc1\n\t"
            "s_add_u32 s49, s33, 34816\n\t"
            "buffer_load_dwordx4 v[136:139], v245, s[28:31], s49 offen sc1\n\t"
            "s_add_u32 s49, s33, 5120\n\t"
            "buffer_load_dwordx4 v[20:23], v245, s[28:31], s49 offen sc1\n\t"
            "s_add_u32 s49, s33, 11264\n\t"
            "buffer_load_dwordx4 v[44:47], v245, s[28:31], s49 offen sc1\n\t"
            "s_add_u32 s49, s33, 17408\n\t"
            "buffer_load_dwordx4 v[68:71], v245, s[28:31], s49 offen sc1\n\t"
            "s_add_u32 s49, s33, 23552\n\t"
            "buffer_load_dwordx4 v[92:95], v245, s[28:31], s49 offen sc1\n\t"
            "s_add_u32 s49, s33, 29696\n\t"
            "buffer_load_dwordx4 v[116:119], v245, s[28:31], s49 offen sc1\n\t"
            "s_add_u32 s49, s33, 35840\n\t"
            "buffer_load_dwordx4 v[140:143], v245, s[28:31], s49 offen sc1\n\t"
            "ds_read_b32 v249, v252 offset:12\n\t"
            "s_waitcnt lgkmcnt(0)\n\t"
            "v_and_b32 v249, s69, v249\n\t"
            "s_mov_b32 exec_hi, 0x3fffff\n\t"
            "s_add_u32 m0, s66, 0\n\t"
            "s_add_u32 s49, s79, 0\n\t"
            "buffer_load_dwordx4 v249, s[72:75], s49 offen lds\n\t"
            "s_mov_b32 exec_hi, -1\n\t"
            "s_mov_b32 exec_hi, 0x3fffff\n\t"
            "s_add_u32 m0, s66, 872\n\t"
            "s_add_u32 s49, s47, 196224\n\t"
            "buffer_load_dwordx4 v249, s[72:75], s49 offen lds\n\t"
            "s_mov_b32 exec_hi, -1\n\t"
            "s_mov_b32 exec_hi, 0x3fffff\n\t"
            "s_add_u32 m0, s66, 1744\n\t"
            "s_add_u32 s49, s47, 392448\n\t"
            "buffer_load_dwordx4 v249, s[72:75], s49 offen lds\n\t"
            "s_mov_b32 exec_hi, -1\n\t"
            "s_mov_b32 exec_hi, 0x3fffff\n\t"
            "s_add_u32 m0, s66, 2616\n\t"
            "s_add_u32 s49, s47, 588672\n\t"
            "buffer_load_dwordx4 v249, s[72:75], s49 offen lds\n\t"
            "s_mov_b32 exec_hi, -1\n\t"
            "s_mov_b32 exec_hi, 0x3fffff\n\t"
            "s_add_u32 m0, s66, 3488\n\t"
            "s_add_u32 s49, s47, 784896\n\t"
            "buffer_load_dwordx4 v249, s[72:75], s49 offen lds\n\t"
            "s_mov_b32 exec_hi, -1\n\t"
            "s_mov_b32 exec_hi, 0x3fffff\n\t"
            "s_add_u32 m0, s66, 4360\n\t"
            "s_add_u32 s49, s47, 981120\n\t"
            "buffer_load_dwordx4 v249, s[72:75], s49 offen lds\n\t"
            "s_mov_b32 exec_hi, -1\n\t"
            "s_mov_b32 exec_hi, 0x3fffff\n\t"
            "s_add_u32 m0, s66, 5232\n\t"
            "s_add_u32 s49, s47, 1177344\n\t"
            "buffer_load_dwordx4 v249, s[72:75], s49 offen lds\n\t"
            "s_mov_b32 exec_hi, -1\n\t"
            "s_mov_b32 exec_hi, 0x3fffff\n\t"
            "s_add_u32 m0, s66, 6104\n\t"
            "s_add_u32 s49, s47, 1373568\n\t"
            "buffer_load_dwordx4 v249, s[72:75], s49 offen lds\n\t"
            "s_mov_b32 exec_hi, -1\n\t"
            "s_mov_b32 exec_hi, 0x3fffff\n\t"
            "s_add_u32 m0, s66, 6976\n\t"
            "s_add_u32 s49, s47, 1569792\n\t"
            "buffer_load_dwordx4 v249, s[72:75], s49 offen lds\n\t"
            "s_mov_b32 exec_hi, -1\n\t"
            "ds_read_b128 v[168:171], v254\n\t"
            "ds_read_b128 v[172:175], v254 offset:64\n\t"
            "v_add_u32 v180, s48, v245\n\t"
            "v_add_u32 v232, s48, v242\n\t"
            "v_xor_b32 v233, 64, v232\n\t"
            "v_xor_b32 v246, 0x100, v246\n\t"
            "ds_read_b128 v[176:179], v180 offset:18816\n\t"
            "ds_read_b128 v[180:183], v180 offset:19840\n\t"
            "ds_read_b128 v[192:195], v232\n\t"
            "ds_read_b128 v[196:199], v233\n\t"
            "ds_read_b128 v[224:227], v232 offset:128\n\t"
            "ds_read_b128 v[228:231], v233 offset:128\n\t"
            "s_waitcnt lgkmcnt(5)\n\t"
            "v_add_f32 v176, v176, v178\n\t"
            "s_waitcnt lgkmcnt(4)\n\t"
            "v_add_f32 v180, v180, v182\n\t"
            "v_add_f32 v176, v176, v180\n\t"
            "v_add_f32 v177, v177, v179\n\t"
            "v_add_f32 v181, v181, v183\n\t"
            "v_mul_f32 v176, 0x3baaaaab, v176\n\t"
            "v_add_f32 v177, v177, v181\n\t"
            "v_mul_f32_e64 v176, v176, -v176\n\t"
            "v_fmac_f32 v176, 0x3baaaaab, v177\n\t"
            "v_max_f32 v176, 0, v176\n\t"
            "v_add_f32 v176, 0x3727c5ac, v176\n\t"
            "v_rsq_f32 v176, v176\n\t"
            "s_nop 0\n\t"
            "ds_write_b32 v246, v176\n\t"
            "s_setprio 3\n\t"
            "s_waitcnt vmcnt(9)\n\t"
            "s_waitcnt lgkmcnt(3)\n\t"
            "v_mfma_f32_16x16x32_bf16 v[200:203], v[192:195], v[0:3], 0\n\t"
            "v_mfma_f32_16x16x32_bf16 v[200:203], v[196:199], v[4:7], v[200:203]\n\t"
            "v_mfma_f32_16x16x32_bf16 v[204:207], v[196:199], v[28:31], 0\n\t"
            "v_mfma_f32_16x16x32_bf16 v[204:207], v[192:195], v[24:27], v[204:207]\n\t"
            "v_mfma_f32_16x16x32_bf16 v[208:211], v[192:195], v[48:51], 0\n\t"
            "v_mfma_f32_16x16x32_bf16 v[208:211], v[196:199], v[52:55], v[208:211]\n\t"
            "v_mfma_f32_16x16x32_bf16 v[212:215], v[196:199], v[76:79], 0\n\t"
            "v_mfma_f32_16x16x32_bf16 v[212:215], v[192:195], v[72:75], v[212:215]\n\t"
            "v_mfma_f32_16x16x32_bf16 v[216:219], v[192:195], v[96:99], 0\n\t"
            "v_mfma_f32_16x16x32_bf16 v[216:219], v[196:199], v[100:103], v[216:219]\n\t"
            "v_mfma_f32_16x16x32_bf16 v[220:223], v[196:199], v[124:127], 0\n\t"
            "v_mfma_f32_16x16x32_bf16 v[220:223], v[192:195], v[120:123], v[220:223]\n\t"
            "s_waitcnt lgkmcnt(1)\n\t"
            "v_mfma_f32_16x16x32_bf16 v[200:203], v[224:227], v[8:11], v[200:203]\n\t"
            "v_mfma_f32_16x16x32_bf16 v[200:203], v[228:231], v[12:15], v[200:203]\n\t"
            "v_mfma_f32_16x16x32_bf16 v[204:207], v[228:231], v[36:39], v[204:207]\n\t"
            "v_mfma_f32_16x16x32_bf16 v[204:207], v[224:227], v[32:35], v[204:207]\n\t"
            "v_mfma_f32_16x16x32_bf16 v[208:211], v[224:227], v[56:59], v[208:211]\n\t"
            "v_mfma_f32_16x16x32_bf16 v[208:211], v[228:231], v[60:63], v[208:211]\n\t"
            "v_mfma_f32_16x16x32_bf16 v[212:215], v[228:231], v[84:87], v[212:215]\n\t"
            "v_mfma_f32_16x16x32_bf16 v[212:215], v[224:227], v[80:83], v[212:215]\n\t"
            "v_mfma_f32_16x16x32_bf16 v[216:219], v[224:227], v[104:107], v[216:219]\n\t"
            "s_waitcnt vmcnt(0)\n\t"
            "ds_read_b64 v[144:145], v253\n\t"
            "v_mfma_f32_16x16x32_bf16 v[216:219], v[228:231], v[108:111], v[216:219]\n\t"
            "ds_read_b64 v[146:147], v253 offset:96\n\t"
            "ds_read_b64 v[148:149], v253 offset:1744\n\t"
            "v_mfma_f32_16x16x32_bf16 v[220:223], v[228:231], v[132:135], v[220:223]\n\t"
            "ds_read_b128 v[228:231], v233 offset:256\n\t"
            "ds_read_b64 v[150:151], v253 offset:1840\n\t"
            "v_mfma_f32_16x16x32_bf16 v[220:223], v[224:227], v[128:131], v[220:223]\n\t"
            "ds_read_b128 v[224:227], v232 offset:256\n\t"
            "ds_read_b64 v[152:153], v253 offset:8\n\t"
            "ds_read_b64 v[154:155], v253 offset:104\n\t"
            "ds_read_b64 v[156:157], v253 offset:1752\n\t"
            "ds_read_b64 v[158:159], v253 offset:1848\n\t"
            "ds_read_b64 v[160:161], v253 offset:16\n\t"
            "ds_read_b64 v[162:163], v253 offset:112\n\t"
            "ds_read_b64 v[164:165], v253 offset:1760\n\t"
            "ds_read_b64 v[166:167], v253 offset:1856\n\t"
            "s_waitcnt lgkmcnt(0)\n\t"
            "s_nop 7\n\t"
            ".p2align 6\n\t"
            "s_nop 0\n\t"
            "s_nop 0\n\t"
            "s_nop 0\n\t"
            "s_nop 0\n\t"
            ".Lwc_loop%=:\n\t"
            "s_setprio 1\n\t"
            "s_setprio 3\n\t"
            "v_mfma_f32_16x16x32_bf16 v[200:203], v[224:227], v[16:19], v[200:203]\n\t"
            "v_mov_b64 v[176:177], 0\n\t"
            "v_mov_b64 v[178:179], 0\n\t"
            "v_mfma_f32_16x16x32_bf16 v[200:203], v[228:231], v[20:23], v[200:203]\n\t"
            "v_mov_b64 v[180:181], 0\n\t"
            "v_mov_b64 v[182:183], 0\n\t"
            "s_waitcnt lgkmcnt(0)\n\t"
            "v_smfmac_f32_16x16x64_bf16 v[176:179], v[168:171], v[144:151], v255\n\t"
            "ds_read_b128 v[168:171], v254 offset:128\n\t"
            "v_smfmac_f32_16x16x64_bf16 v[180:183], v[172:175], v[152:159], v255\n\t"
            "ds_read_b64 v[144:145], v253 offset:24\n\t"
            "v_mfma_f32_16x16x32_bf16 v[204:207], v[228:231], v[44:47], v[204:207]\n\t"
            "ds_read_b64 v[146:147], v253 offset:120\n\t"
            "ds_read_b64 v[148:149], v253 offset:1768\n\t"
            "v_mfma_f32_16x16x32_bf16 v[204:207], v[224:227], v[40:43], v[204:207]\n\t"
            "ds_read_b64 v[150:151], v253 offset:1864\n\t"
            "ds_read_b128 v[172:175], v254 offset:192\n\t"
            "v_mfma_f32_16x16x32_bf16 v[208:211], v[224:227], v[64:67], v[208:211]\n\t"
            "ds_read_b64 v[152:153], v253 offset:32\n\t"
            "ds_read_b64 v[154:155], v253 offset:128\n\t"
            "v_mfma_f32_16x16x32_bf16 v[208:211], v[228:231], v[68:71], v[208:211]\n\t"
            "ds_read_b64 v[156:157], v253 offset:1776\n\t"
            "ds_read_b64 v[158:159], v253 offset:1872\n\t"
            "v_mfma_f32_16x16x32_bf16 v[212:215], v[228:231], v[92:95], v[212:215]\n\t"
            "v_mov_b32 v249, s76\n\t"
            "v_mov_b64 v[184:185], 0\n\t"
            "v_mfma_f32_16x16x32_bf16 v[212:215], v[224:227], v[88:91], v[212:215]\n\t"
            "ds_read_b32 v249, v249\n\t"
            "v_mov_b64 v[186:187], 0\n\t"
            "v_mfma_f32_16x16x32_bf16 v[216:219], v[224:227], v[112:115], v[216:219]\n\t"
            "v_mov_b64 v[188:189], 0\n\t"
            "v_mov_b64 v[190:191], 0\n\t"
            "v_mfma_f32_16x16x32_bf16 v[216:219], v[228:231], v[116:119], v[216:219]\n\t"
            "v_cvt_pk_bf16_f32 v176, v176, v177\n\t"
            "v_add_u32 v236, s67, v250\n\t"
            "s_waitcnt lgkmcnt(10)\n\t"
            "v_smfmac_f32_16x16x64_bf16 v[184:187], v[168:171], v[160:167], v255\n\t"
            "ds_read_b128 v[168:171], v254 offset:256\n\t"
            "s_waitcnt lgkmcnt(6)\n\t"
            "v_smfmac_f32_16x16x64_bf16 v[188:191], v[172:175], v[144:151], v255\n\t"
            "ds_read_b64 v[160:161], v253 offset:40\n\t"
            "v_mfma_f32_16x16x32_bf16 v[220:223], v[228:231], v[140:143], v[220:223]\n\t"
            "ds_read_b64 v[162:163], v253 offset:136\n\t"
            "ds_read_b64 v[164:165], v253 offset:1784\n\t"
            "v_mfma_f32_16x16x32_bf16 v[220:223], v[224:227], v[136:139], v[220:223]\n\t"
            "s_setprio 1\n\t"
            "ds_read_b64 v[166:167], v253 offset:1880\n\t"
            "ds_read_b128 v[172:175], v254 offset:320\n\t"
            "ds_read_b64 v[144:145], v253 offset:48\n\t"
            "ds_read_b64 v[146:147], v253 offset:144\n\t"
            "ds_read_b64 v[148:149], v253 offset:1792\n\t"
            "ds_read_b64 v[150:151], v253 offset:1888\n\t"
            "s_waitcnt lgkmcnt(14)\n\t"
            "ds_read_b128 v[224:227], v232 offset:6272\n\t"
            "s_waitcnt lgkmcnt(14)\n\t"
            "ds_read_b128 v[228:231], v233 offset:6272\n\t"
            "s_waitcnt lgkmcnt(12)\n\t"
            "v_readfirstlane_b32 s26, v249\n\t"
            "v_cvt_pk_bf16_f32 v177, v178, v179\n\t"
            "v_cvt_pk_bf16_f32 v178, v180, v181\n\t"
            "s_and_b32 s68, s26, 0x7ffffff\n\t"
            "s_add_u32 s68, s68, s78\n\t"
            "s_and_b32 s69, s26, 0x38000000\n\t"
            "s_or_b32 s69, s69, s39\n\t"
            "s_bitcmp1_b32 s26, 27\n\t"
            "s_cselect_b32 s79, 2.0, s68\n\t"
            "s_setprio 3\n\t"
            "v_mfma_f32_4x4x4_16b_bf16 v[196:199], v[176:177], v[176:177], 0\n\t"
            "s_setprio 1\n\t"
            "ds_read_b32 v249, v252 offset:12\n\t"
            "s_and_b32 s57, s56, 3\n\t"
            "s_lshl_b32 s58, s57, 3\n\t"
            "s_lshr_b32 m0, s54, s58\n\t"
            "s_lshl_b32 s58, s57, 4\n\t"
            "s_lshr_b64 s[36:37], s[50:51], s58\n\t"
            "s_and_b32 s57, s36, 0xffff\n\t"
            "s_add_u32 s57, s57, s67\n\t"
            "v_writelane_b32 v236, s57, 63\n\t"
            "v_writelane_b32 v236, s59, m0\n\t"
            "v_cvt_pk_bf16_f32 v179, v182, v183\n\t"
            "v_mov_b64 v[180:181], 0\n\t"
            "v_mov_b64 v[182:183], 0\n\t"
            "s_setprio 3\n\t"
            "v_mfma_f32_16x16x32_bf16 v[192:195], v[238:241], v[176:179], 0\n\t"
            "s_lshr_b64 s[36:37], s[52:53], s58\n\t"
            "s_and_b32 s58, s36, 0xffff\n\t"
            "s_add_u32 s58, s58, s67\n\t"
            "v_mfma_f32_4x4x4_16b_bf16 v[196:199], v[178:179], v[178:179], v[196:199]\n\t"
            "ds_write_b128 v236, v[176:179]\n\t"
            "s_waitcnt lgkmcnt(8)\n\t"
            "v_smfmac_f32_16x16x64_bf16 v[180:183], v[172:175], v[160:167], v255\n\t"
            "s_setprio 1\n\t"
            "s_and_b32 s38, s52, 0xffff\n\t"
            "s_add_u32 s38, s38, s67\n\t"
            "ds_read_b128 v[172:175], v254 offset:448\n\t"
            "ds_read_b64 v[160:161], v253 offset:64\n\t"
            "ds_read_b64 v[162:163], v253 offset:160\n\t"
            "ds_read_b64 v[164:165], v253 offset:1808\n\t"
            "ds_read_b64 v[166:167], v253 offset:1904\n\t"
            "v_mov_b64 v[176:177], 0\n\t"
            "v_mov_b64 v[178:179], 0\n\t"
            "v_cvt_pk_bf16_f32 v184, v184, v185\n\t"
            "v_cvt_pk_bf16_f32 v185, v186, v187\n\t"
            "s_setprio 3\n\t"
            "v_smfmac_f32_16x16x64_bf16 v[176:179], v[168:171], v[152:159], v255\n\t"
            "ds_read_b128 v[168:171], v254 offset:384\n\t"
            "v_mfma_f32_4x4x4_16b_bf16 v[196:199], v[184:185], v[184:185], v[196:199]\n\t"
            "s_setprio 1\n\t"
            "ds_read_b64 v[152:153], v253 offset:56\n\t"
            "s_waitcnt lgkmcnt(14)\n\t"
            "ds_read_b64 v[154:155], v253 offset:152\n\t"
            "s_waitcnt lgkmcnt(14)\n\t"
            "ds_read_b64 v[156:157], v253 offset:1800\n\t"
            "s_waitcnt lgkmcnt(14)\n\t"
            "ds_read_b64 v[158:159], v253 offset:1896\n\t"
            "v_xor_b32 v237, 16, v236\n\t"
            "v_cvt_pk_bf16_f32 v186, v188, v189\n\t"
            "v_cvt_pk_bf16_f32 v187, v190, v191\n\t"
            "v_mov_b64 v[188:189], 0\n\t"
            "v_mov_b64 v[190:191], 0\n\t"
            "s_setprio 3\n\t"
            "v_mfma_f32_16x16x32_bf16 v[192:195], v[238:241], v[184:187], v[192:195]\n\t"
            "s_waitcnt lgkmcnt(14)\n\t"
            "ds_write_b128 v237, v[184:187]\n\t"
            "v_mfma_f32_4x4x4_16b_bf16 v[196:199], v[186:187], v[186:187], v[196:199]\n\t"
            "s_setprio 1\n\t"
            "v_mov_b64 v[184:185], 0\n\t"
            "v_mov_b64 v[186:187], 0\n\t"
            "v_cvt_pk_bf16_f32 v176, v176, v177\n\t"
            "v_cvt_pk_bf16_f32 v177, v178, v179\n\t"
            "v_cvt_pk_bf16_f32 v178, v180, v181\n\t"
            "v_cvt_pk_bf16_f32 v179, v182, v183\n\t"
            "s_setprio 3\n\t"
            "v_mfma_f32_4x4x4_16b_bf16 v[196:199], v[176:177], v[176:177], v[196:199]\n\t"
            "s_waitcnt lgkmcnt(14)\n\t"
            "ds_write_b128 v236, v[176:179] offset:128\n\t"
            "s_waitcnt lgkmcnt(6)\n\t"
            "v_smfmac_f32_16x16x64_bf16 v[184:187], v[168:171], v[144:151], v255\n\t"
            "ds_read_b128 v[168:171], v254 offset:512\n\t"
            "s_waitcnt lgkmcnt(3)\n\t"
            "v_smfmac_f32_16x16x64_bf16 v[188:191], v[172:175], v[152:159], v255\n\t"
            "ds_read_b64 v[144:145], v253 offset:72\n\t"
            "v_mfma_f32_16x16x32_bf16 v[192:195], v[238:241], v[176:179], v[192:195]\n\t"
            "ds_read_b64 v[146:147], v253 offset:168\n\t"
            "ds_read_b64 v[148:149], v253 offset:1816\n\t"
            "v_mfma_f32_4x4x4_16b_bf16 v[196:199], v[178:179], v[178:179], v[196:199]\n\t"
            "s_setprio 1\n\t"
            "ds_read_b64 v[150:151], v253 offset:1912\n\t"
            "ds_read_b128 v[172:175], v254 offset:576\n\t"
            "ds_read_b64 v[152:153], v253 offset:80\n\t"
            "ds_read_b64 v[154:155], v253 offset:176\n\t"
            "ds_read_b64 v[156:157], v253 offset:1824\n\t"
            "ds_read_b64 v[158:159], v253 offset:1920\n\t"
            "v_mov_b64 v[176:177], 0\n\t"
            "v_mov_b64 v[178:179], 0\n\t"
            "v_mov_b64 v[180:181], 0\n\t"
            "v_mov_b64 v[182:183], 0\n\t"
            "v_cvt_pk_bf16_f32 v184, v184, v185\n\t"
            "v_cvt_pk_bf16_f32 v185, v186, v187\n\t"
            "v_cvt_pk_bf16_f32 v186, v188, v189\n\t"
            "v_cvt_pk_bf16_f32 v187, v190, v191\n\t"
            "s_setprio 3\n\t"
            "v_mfma_f32_4x4x4_16b_bf16 v[196:199], v[184:185], v[184:185], v[196:199]\n\t"
            "ds_write_b128 v237, v[184:187] offset:128\n\t"
            "s_waitcnt lgkmcnt(10)\n\t"
            "v_smfmac_f32_16x16x64_bf16 v[176:179], v[168:171], v[160:167], v255\n\t"
            "ds_read_b128 v[168:171], v254 offset:640\n\t"
            "v_mfma_f32_16x16x32_bf16 v[192:195], v[238:241], v[184:187], v[192:195]\n\t"
            "ds_read_b64 v[160:161], v253 offset:88\n\t"
            "ds_read_b64 v[162:163], v253 offset:184\n\t"
            "s_waitcnt lgkmcnt(8)\n\t"
            "v_smfmac_f32_16x16x64_bf16 v[180:183], v[172:175], v[144:151], v255\n\t"
            "ds_read_b64 v[164:165], v253 offset:1832\n\t"
            "v_mfma_f32_4x4x4_16b_bf16 v[196:199], v[186:187], v[186:187], v[196:199]\n\t"
            "s_setprio 1\n\t"
            "ds_read_b64 v[166:167], v253 offset:1928\n\t"
            "ds_read_b128 v[172:175], v254 offset:704\n\t"
            "v_mov_b64 v[184:185], 0\n\t"
            "v_mov_b64 v[186:187], 0\n\t"
            "v_mov_b64 v[188:189], 0\n\t"
            "v_mov_b64 v[190:191], 0\n\t"
            "v_add_u32 v248, s67, v251\n\t"
            "v_writelane_b32 v248, s58, 63\n\t"
            "v_writelane_b32 v248, s38, m0\n\t"
            "v_and_b32 v249, s69, v249\n\t"
            "v_cvt_pk_bf16_f32 v176, v176, v177\n\t"
            "v_cvt_pk_bf16_f32 v177, v178, v179\n\t"
            "v_cvt_pk_bf16_f32 v178, v180, v181\n\t"
            "v_cvt_pk_bf16_f32 v179, v182, v183\n\t"
            "s_setprio 3\n\t"
            "v_mfma_f32_4x4x4_16b_bf16 v[196:199], v[176:177], v[176:177], v[196:199]\n\t"
            "ds_write_b128 v236, v[176:179] offset:256\n\t"
            "s_waitcnt lgkmcnt(6)\n\t"
            "v_smfmac_f32_16x16x64_bf16 v[184:187], v[168:171], v[152:159], v255\n\t"
            "v_mov_b32 v234, 0\n\t"
            "v_mfma_f32_4x4x4_16b_bf16 v[196:199], v[178:179], v[178:179], v[196:199]\n\t"
            "v_xor_b32 v246, 0x100, v246\n\t"
            "v_mfma_f32_16x16x32_bf16 v[192:195], v[238:241], v[176:179], v[192:195]\n\t"
            "s_waitcnt lgkmcnt(1)\n\t"
            "v_smfmac_f32_16x16x64_bf16 v[188:191], v[172:175], v[160:167], v255\n\t"
            "s_setprio 1\n\t"
            "s_mov_b32 exec_hi, 0x3fffff\n\t"
            "s_add_u32 m0, s66, 0\n\t"
            "s_add_u32 s49, s79, 0\n\t"
            "buffer_load_dwordx4 v249, s[72:75], s49 offen lds\n\t"
            "s_mov_b32 exec_hi, -1\n\t"
            "s_mov_b32 exec_hi, 0x3fffff\n\t"
            "s_add_u32 m0, s66, 872\n\t"
            "s_add_u32 s49, s68, 196224\n\t"
            "buffer_load_dwordx4 v249, s[72:75], s49 offen lds\n\t"
            "s_mov_b32 exec_hi, -1\n\t"
            "s_mov_b32 exec_hi, 0x3fffff\n\t"
            "s_add_u32 m0, s66, 1744\n\t"
            "s_add_u32 s49, s68, 392448\n\t"
            "buffer_load_dwordx4 v249, s[72:75], s49 offen lds\n\t"
            "s_mov_b32 exec_hi, -1\n\t"
            "s_mov_b32 exec_hi, 0x3fffff\n\t"
            "s_add_u32 m0, s66, 2616\n\t"
            "s_add_u32 s49, s68, 588672\n\t"
            "buffer_load_dwordx4 v249, s[72:75], s49 offen lds\n\t"
            "s_mov_b32 exec_hi, -1\n\t"
            "s_mov_b32 exec_hi, 0x3fffff\n\t"
            "s_add_u32 m0, s66, 3488\n\t"
            "s_add_u32 s49, s68, 784896\n\t"
            "buffer_load_dwordx4 v249, s[72:75], s49 offen lds\n\t"
            "s_mov_b32 exec_hi, -1\n\t"
            "v_cvt_pk_bf16_f32 v184, v184, v185\n\t"
            "v_cvt_pk_bf16_f32 v185, v186, v187\n\t"
            "v_cvt_pk_bf16_f32 v186, v188, v189\n\t"
            "v_cvt_pk_bf16_f32 v187, v190, v191\n\t"
            "s_setprio 3\n\t"
            "v_mfma_f32_4x4x4_16b_bf16 v[196:199], v[184:185], v[184:185], v[196:199]\n\t"
            "s_mov_b32 exec_hi, 0x3fffff\n\t"
            "s_add_u32 m0, s66, 4360\n\t"
            "s_add_u32 s49, s68, 981120\n\t"
            "buffer_load_dwordx4 v249, s[72:75], s49 offen lds\n\t"
            "s_mov_b32 exec_hi, -1\n\t"
            "v_mfma_f32_4x4x4_16b_bf16 v[196:199], v[186:187], v[186:187], v[196:199]\n\t"
            "s_mov_b32 exec_hi, 0x3fffff\n\t"
            "s_add_u32 m0, s66, 5232\n\t"
            "s_add_u32 s49, s68, 1177344\n\t"
            "buffer_load_dwordx4 v249, s[72:75], s49 offen lds\n\t"
            "s_mov_b32 exec_hi, -1\n\t"
            "v_mfma_f32_16x16x32_bf16 v[192:195], v[238:241], v[184:187], v[192:195]\n\t"
            "s_setprio 1\n\t"
            "s_mov_b32 exec_hi, 0x3fffff\n\t"
            "s_add_u32 m0, s66, 6104\n\t"
            "s_add_u32 s49, s68, 1373568\n\t"
            "buffer_load_dwordx4 v249, s[72:75], s49 offen lds\n\t"
            "s_mov_b32 exec_hi, -1\n\t"
            "s_mov_b32 exec_hi, 0x3fffff\n\t"
            "s_add_u32 m0, s66, 6976\n\t"
            "s_add_u32 s49, s68, 1569792\n\t"
            "buffer_load_dwordx4 v249, s[72:75], s49 offen lds\n\t"
            "s_mov_b32 exec_hi, -1\n\t"
            "v_cndmask_b32_e64 v193, v196, v197, s[60:61]\n\t"
            "v_cndmask_b32_e64 v193, v193, v198, s[62:63]\n\t"
            "v_cndmask_b32_e64 v193, v193, v199, s[64:65]\n\t"
            "s_setprio 1\n\t"
            "ds_read_b128 v[196:199], v233 offset:6144\n\t"
            "ds_write_b64 v248, v[192:193] offset:18816\n\t"
            "ds_read_b128 v[192:195], v232 offset:6144\n\t"
            "ds_read_b64 v[248:249], v247 offset:8\n\t"
            "ds_write_b128 v237, v[184:187] offset:256\n\t"
            "ds_read_b64 v[236:237], v247\n\t"
            "s_mov_b64 exec, 1\n\t"
            "ds_inc_u32 v255, v238 offset:7860\n\t"
            "s_mov_b64 exec, -1\n\t"
            "s_waitcnt lgkmcnt(4)\n\t"
            "v_mfma_f32_16x16x32_bf16 v[144:147], v[192:195], v[0:3], 0\n\t"
            "s_waitcnt lgkmcnt(3)\n\t"
            "v_mul_f32 v202, v248, v202\n\t"
            "v_mul_f32 v203, v249, v203\n\t"
            "v_mfma_f32_16x16x32_bf16 v[144:147], v[196:199], v[4:7], v[144:147]\n\t"
            "v_mul_f32 v206, v248, v206\n\t"
            "s_waitcnt lgkmcnt(1)\n\t"
            "v_mul_f32 v200, v236, v200\n\t"
            "v_mfma_f32_16x16x32_bf16 v[148:151], v[196:199], v[28:31], 0\n\t"
            "v_mul_f32 v201, v237, v201\n\t"
            "v_mul_f32 v204, v236, v204\n\t"
            "v_mfma_f32_16x16x32_bf16 v[148:151], v[192:195], v[24:27], v[148:151]\n\t"
            "v_mul_f32 v205, v237, v205\n\t"
            "v_mul_f32 v208, v236, v208\n\t"
            "v_mfma_f32_16x16x32_bf16 v[168:171], v[192:195], v[48:51], 0\n\t"
            "v_mul_f32 v209, v237, v209\n\t"
            "v_mul_f32 v212, v236, v212\n\t"
            "v_mfma_f32_16x16x32_bf16 v[168:171], v[196:199], v[52:55], v[168:171]\n\t"
            "v_mul_f32 v213, v237, v213\n\t"
            "v_mul_f32 v216, v236, v216\n\t"
            "v_mfma_f32_16x16x32_bf16 v[172:175], v[196:199], v[76:79], 0\n\t"
            "v_mul_f32 v217, v237, v217\n\t"
            "v_mul_f32 v220, v236, v220\n\t"
            "v_mfma_f32_16x16x32_bf16 v[172:175], v[192:195], v[72:75], v[172:175]\n\t"
            "v_mul_f32 v221, v237, v221\n\t"
            "v_mul_f32 v207, v249, v207\n\t"
            "v_mfma_f32_16x16x32_bf16 v[152:155], v[192:195], v[96:99], 0\n\t"
            "ds_read_b64 v[236:237], v247 offset:64\n\t"
            "v_mul_f32 v210, v248, v210\n\t"
            "v_mfma_f32_16x16x32_bf16 v[152:155], v[196:199], v[100:103], v[152:155]\n\t"
            "v_mul_f32 v211, v249, v211\n\t"
            "v_mul_f32 v214, v248, v214\n\t"
            "v_mfma_f32_16x16x32_bf16 v[156:159], v[196:199], v[124:127], 0\n\t"
            "ds_read_b128 v[196:199], v233 offset:6400\n\t"
            "v_mul_f32 v215, v249, v215\n\t"
            "v_mfma_f32_16x16x32_bf16 v[156:159], v[192:195], v[120:123], v[156:159]\n\t"
            "ds_read_b128 v[192:195], v232 offset:6400\n\t"
            "v_mul_f32 v218, v248, v218\n\t"
            "v_mfma_f32_16x16x32_bf16 v[144:147], v[224:227], v[8:11], v[144:147]\n\t"
            "v_mul_f32 v219, v249, v219\n\t"
            "v_mul_f32 v222, v248, v222\n\t"
            "v_mfma_f32_16x16x32_bf16 v[144:147], v[228:231], v[12:15], v[144:147]\n\t"
            "v_mul_f32 v223, v249, v223\n\t"
            "v_cvt_pk_bf16_f32 v212, v212, v213\n\t"
            "v_mfma_f32_16x16x32_bf16 v[148:151], v[228:231], v[36:39], v[148:151]\n\t"
            "ds_read_b64 v[248:249], v247 offset:72\n\t"
            "v_cvt_pk_bf16_f32 v208, v208, v209\n\t"
            "v_mfma_f32_16x16x32_bf16 v[148:151], v[224:227], v[32:35], v[148:151]\n\t"
            "v_cvt_pk_bf16_f32 v213, v214, v215\n\t"
            "v_cvt_pk_bf16_f32 v209, v210, v211\n\t"
            "v_mfma_f32_16x16x32_bf16 v[168:171], v[224:227], v[56:59], v[168:171]\n\t"
            "v_cvt_pk_bf16_f32 v200, v200, v201\n\t"
            "v_cvt_pk_bf16_f32 v204, v204, v205\n\t"
            "v_mfma_f32_16x16x32_bf16 v[168:171], v[228:231], v[60:63], v[168:171]\n\t"
            "v_cvt_pk_bf16_f32 v216, v216, v217\n\t"
            "v_cvt_pk_bf16_f32 v220, v220, v221\n\t"
            "v_mfma_f32_16x16x32_bf16 v[172:175], v[228:231], v[84:87], v[172:175]\n\t"
            "v_cvt_pk_bf16_f32 v201, v202, v203\n\t"
            "v_cvt_pk_bf16_f32 v205, v206, v207\n\t"
            "v_mfma_f32_16x16x32_bf16 v[172:175], v[224:227], v[80:83], v[172:175]\n\t"
            "v_cvt_pk_bf16_f32 v217, v218, v219\n\t"
            "v_cvt_pk_bf16_f32 v221, v222, v223\n\t"
            "v_mfma_f32_16x16x32_bf16 v[152:155], v[224:227], v[104:107], v[152:155]\n\t"
            "v_mfma_f32_16x16x32_bf16 v[152:155], v[228:231], v[108:111], v[152:155]\n\t"
            "v_mfma_f32_16x16x32_bf16 v[156:159], v[228:231], v[132:135], v[156:159]\n\t"
            "ds_read_b128 v[228:231], v233 offset:12288\n\t"
            "v_mfma_f32_16x16x32_bf16 v[156:159], v[224:227], v[128:131], v[156:159]\n\t"
            "ds_read_b128 v[224:227], v232 offset:12288\n\t"
            "s_waitcnt lgkmcnt(3)\n\t"
            "v_mfma_f32_16x16x32_bf16 v[144:147], v[192:195], v[16:19], v[144:147]\n\t"
            "v_mfma_f32_16x16x32_bf16 v[144:147], v[196:199], v[20:23], v[144:147]\n\t"
            "v_mfma_f32_16x16x32_bf16 v[148:151], v[196:199], v[44:47], v[148:151]\n\t"
            "v_mfma_f32_16x16x32_bf16 v[148:151], v[192:195], v[40:43], v[148:151]\n\t"
            "v_mfma_f32_16x16x32_bf16 v[168:171], v[192:195], v[64:67], v[168:171]\n\t"
            "v_mfma_f32_16x16x32_bf16 v[168:171], v[196:199], v[68:71], v[168:171]\n\t"
            "ds_read_b64 v[234:235], v234 offset:7856\n\t"
            "v_mfma_f32_16x16x32_bf16 v[172:175], v[196:199], v[92:95], v[172:175]\n\t"
            "s_nop 1\n\t"
            "v_mul_f32 v144, v236, v144\n\t"
            "v_mfma_f32_16x16x32_bf16 v[172:175], v[192:195], v[88:91], v[172:175]\n\t"
            "v_mul_f32 v145, v237, v145\n\t"
            "s_waitcnt lgkmcnt(3)\n\t"
            "v_mul_f32 v146, v248, v146\n\t"
            "v_mfma_f32_16x16x32_bf16 v[152:155], v[192:195], v[112:115], v[152:155]\n\t"
            "v_mul_f32 v168, v236, v168\n\t"
            "v_mul_f32 v169, v237, v169\n\t"
            "v_mfma_f32_16x16x32_bf16 v[152:155], v[196:199], v[116:119], v[152:155]\n\t"
            "v_mul_f32 v170, v248, v170\n\t"
            "v_mul_f32 v171, v249, v171\n\t"
            "v_mfma_f32_16x16x32_bf16 v[156:159], v[196:199], v[140:143], v[156:159]\n\t"
            "ds_read_b128 v[196:199], v233 offset:12416\n\t"
            "v_mul_f32 v172, v236, v172\n\t"
            "v_mfma_f32_16x16x32_bf16 v[156:159], v[192:195], v[136:139], v[156:159]\n\t"
            "ds_read_b128 v[192:195], v232 offset:12416\n\t"
            "v_mul_f32 v173, v237, v173\n\t"
            "s_waitcnt lgkmcnt(3)\n\t"
            "v_mfma_f32_16x16x32_bf16 v[160:163], v[224:227], v[0:3], 0\n\t"
            "v_cvt_pk_bf16_f32 v214, v172, v173\n\t"
            "v_cvt_pk_bf16_f32 v210, v168, v169\n\t"
            "v_mfma_f32_16x16x32_bf16 v[160:163], v[228:231], v[4:7], v[160:163]\n\t"
            "s_waitcnt lgkmcnt(2)\n\t"
            "v_readfirstlane_b32 s77, v234\n\t"
            "v_readfirstlane_b32 s38, v235\n\t"
            "s_add_u32 s77, s77, s38\n\t"
            "s_cmp_ge_u32 s77, s44\n\t"
            "s_cbranch_scc0 .Lwc_spin%=\n\t"
            ".Lwc_go%=:\n\t"
            "v_mfma_f32_16x16x32_bf16 v[164:167], v[228:231], v[28:31], 0\n\t"
            "s_add_u32 s59, s59, s84\n\t"
            "s_movk_i32 s84, 800\n\t"
            "v_cvt_pk_bf16_f32 v211, v170, v171\n\t"
            "v_mfma_f32_16x16x32_bf16 v[164:167], v[224:227], v[24:27], v[164:167]\n\t"
            "s_add_u32 s45, s46, s27\n\t"
            "s_mov_b32 s46, s47\n\t"
            "s_mov_b32 s47, s68\n\t"
            "v_mfma_f32_16x16x32_bf16 v[176:179], v[224:227], v[48:51], 0\n\t"
            "v_mul_f32 v174, v248, v174\n\t"
            "v_mul_f32 v175, v249, v175\n\t"
            "v_mfma_f32_16x16x32_bf16 v[176:179], v[228:231], v[52:55], v[176:179]\n\t"
            "v_add_u32 v172, s67, v245\n\t"
            "v_cvt_pk_bf16_f32 v215, v174, v175\n\t"
            "v_mfma_f32_16x16x32_bf16 v[180:183], v[228:231], v[76:79], 0\n\t"
            "ds_read_b128 v[168:171], v172 offset:18816\n\t"
            "ds_read_b128 v[172:175], v172 offset:19840\n\t"
            "v_mfma_f32_16x16x32_bf16 v[180:183], v[224:227], v[72:75], v[180:183]\n\t"
            "v_mul_f32 v148, v236, v148\n\t"
            "v_mul_f32 v149, v237, v149\n\t"
            "v_mfma_f32_16x16x32_bf16 v[184:187], v[224:227], v[96:99], 0\n\t"
            "v_mul_f32 v152, v236, v152\n\t"
            "v_mul_f32 v153, v237, v153\n\t"
            "v_mfma_f32_16x16x32_bf16 v[184:187], v[228:231], v[100:103], v[184:187]\n\t"
            "v_mul_f32 v156, v236, v156\n\t"
            "v_mul_f32 v157, v237, v157\n\t"
            "v_mfma_f32_16x16x32_bf16 v[188:191], v[228:231], v[124:127], 0\n\t"
            "ds_read_b128 v[228:231], v233 offset:12544\n\t"
            "ds_read_b64 v[236:237], v247 offset:128\n\t"
            "v_mfma_f32_16x16x32_bf16 v[188:191], v[224:227], v[120:123], v[188:191]\n\t"
            "ds_read_b128 v[224:227], v232 offset:12544\n\t"
            "v_mul_f32 v147, v249, v147\n\t"
            "s_waitcnt lgkmcnt(5)\n\t"
            "v_mfma_f32_16x16x32_bf16 v[160:163], v[192:195], v[8:11], v[160:163]\n\t"
            "v_mul_f32 v150, v248, v150\n\t"
            "v_mul_f32 v151, v249, v151\n\t"
            "v_mfma_f32_16x16x32_bf16 v[160:163], v[196:199], v[12:15], v[160:163]\n\t"
            "v_mul_f32 v154, v248, v154\n\t"
            "v_mul_f32 v155, v249, v155\n\t"
            "v_mfma_f32_16x16x32_bf16 v[164:167], v[196:199], v[36:39], v[164:167]\n\t"
            "v_mul_f32 v158, v248, v158\n\t"
            "v_mul_f32 v159, v249, v159\n\t"
            "v_mfma_f32_16x16x32_bf16 v[164:167], v[192:195], v[32:35], v[164:167]\n\t"
            "ds_read_b64 v[248:249], v247 offset:136\n\t"
            "s_waitcnt lgkmcnt(5)\n\t"
            "v_add_f32 v168, v168, v170\n\t"
            "v_mfma_f32_16x16x32_bf16 v[176:179], v[192:195], v[56:59], v[176:179]\n\t"
            "s_waitcnt lgkmcnt(4)\n\t"
            "v_add_f32 v172, v172, v174\n\t"
            "v_add_f32 v168, v168, v172\n\t"
            "v_mfma_f32_16x16x32_bf16 v[176:179], v[196:199], v[60:63], v[176:179]\n\t"
            "v_add_f32 v169, v169, v171\n\t"
            "v_add_f32 v173, v173, v175\n\t"
            "v_mfma_f32_16x16x32_bf16 v[180:183], v[196:199], v[84:87], v[180:183]\n\t"
            "v_mul_f32 v168, 0x3baaaaab, v168\n\t"
            "v_add_f32 v169, v169, v173\n\t"
            "v_mfma_f32_16x16x32_bf16 v[180:183], v[192:195], v[80:83], v[180:183]\n\t"
            "ds_read_b128 v[172:175], v254 offset:64\n\t"
            "v_mul_f32_e64 v168, v168, -v168\n\t"
            "v_mfma_f32_16x16x32_bf16 v[184:187], v[192:195], v[104:107], v[184:187]\n\t"
            "v_fmac_f32 v168, 0x3baaaaab, v169\n\t"
            "v_max_f32 v168, 0, v168\n\t"
            "v_mfma_f32_16x16x32_bf16 v[184:187], v[196:199], v[108:111], v[184:187]\n\t"
            "v_cvt_pk_bf16_f32 v202, v144, v145\n\t"
            "v_cvt_pk_bf16_f32 v203, v146, v147\n\t"
            "v_mfma_f32_16x16x32_bf16 v[188:191], v[196:199], v[132:135], v[188:191]\n\t"
            "v_cvt_pk_bf16_f32 v206, v148, v149\n\t"
            "v_cvt_pk_bf16_f32 v207, v150, v151\n\t"
            "v_mfma_f32_16x16x32_bf16 v[188:191], v[192:195], v[128:131], v[188:191]\n\t"
            "v_cvt_pk_bf16_f32 v218, v152, v153\n\t"
            "v_cvt_pk_bf16_f32 v219, v154, v155\n\t"
            "s_waitcnt lgkmcnt(2)\n\t"
            "v_mfma_f32_16x16x32_bf16 v[160:163], v[224:227], v[16:19], v[160:163]\n\t"
            "v_cvt_pk_bf16_f32 v222, v156, v157\n\t"
            "v_cvt_pk_bf16_f32 v223, v158, v159\n\t"
            "v_mfma_f32_16x16x32_bf16 v[160:163], v[228:231], v[20:23], v[160:163]\n\t"
            "v_add_f32 v168, 0x3727c5ac, v168\n\t"
            "v_rsq_f32 v168, v168\n\t"
            "v_mfma_f32_16x16x32_bf16 v[164:167], v[228:231], v[44:47], v[164:167]\n\t"
            "v_add_u32 v232, s67, v242\n\t"
            "v_xor_b32 v233, 64, v232\n\t"
            "v_mfma_f32_16x16x32_bf16 v[164:167], v[224:227], v[40:43], v[164:167]\n\t"
            "s_xor_b32 s48, s48, s71\n\t"
            "s_xor_b32 s67, s67, s71\n\t"
            "s_add_u32 s76, s76, 4\n\t"
            "s_add_u32 s44, s44, 8\n\t"
            "v_mfma_f32_16x16x32_bf16 v[176:179], v[224:227], v[64:67], v[176:179]\n\t"
            "s_lshl_b32 s56, s56, 2\n\t"
            "s_lshr_b32 s49, s26, 30\n\t"
            "s_or_b32 s56, s56, s49\n\t"
            "v_mfma_f32_16x16x32_bf16 v[176:179], v[228:231], v[68:71], v[176:179]\n\t"
            "ds_read_b128 v[192:195], v232\n\t"
            "ds_read_b128 v[196:199], v233\n\t"
            "v_mfma_f32_16x16x32_bf16 v[180:183], v[228:231], v[92:95], v[180:183]\n\t"
            "v_mul_f32 v160, v236, v160\n\t"
            "v_mul_f32 v161, v237, v161\n\t"
            "v_mfma_f32_16x16x32_bf16 v[180:183], v[224:227], v[88:91], v[180:183]\n\t"
            "v_mul_f32 v164, v236, v164\n\t"
            "v_mul_f32 v165, v237, v165\n\t"
            "v_mfma_f32_16x16x32_bf16 v[184:187], v[224:227], v[112:115], v[184:187]\n\t"
            "v_mul_f32 v176, v236, v176\n\t"
            "v_mul_f32 v177, v237, v177\n\t"
            "v_mfma_f32_16x16x32_bf16 v[184:187], v[228:231], v[116:119], v[184:187]\n\t"
            "ds_write_b128 v243, v[200:203]\n\t"
            "v_mfma_f32_16x16x32_bf16 v[188:191], v[228:231], v[140:143], v[188:191]\n\t"
            "ds_read_b128 v[228:231], v233 offset:128\n\t"
            "v_mul_f32 v180, v236, v180\n\t"
            "v_mfma_f32_16x16x32_bf16 v[188:191], v[224:227], v[136:139], v[188:191]\n\t"
            "ds_read_b128 v[224:227], v232 offset:128\n\t"
            "v_mul_f32 v181, v237, v181\n\t"
            "s_waitcnt lgkmcnt(3)\n\t"
            "v_mfma_f32_16x16x32_bf16 v[200:203], v[192:195], v[0:3], 0\n\t"
            "v_mul_f32 v184, v236, v184\n\t"
            "v_mul_f32 v185, v237, v185\n\t"
            "v_mfma_f32_16x16x32_bf16 v[200:203], v[196:199], v[4:7], v[200:203]\n\t"
            "ds_write_b128 v243, v[204:207] offset:1536\n\t"
            "v_mfma_f32_16x16x32_bf16 v[204:207], v[196:199], v[28:31], 0\n\t"
            "v_mul_f32 v188, v236, v188\n\t"
            "v_mul_f32 v189, v237, v189\n\t"
            "v_mfma_f32_16x16x32_bf16 v[204:207], v[192:195], v[24:27], v[204:207]\n\t"
            "ds_write_b128 v243, v[208:211] offset:3072\n\t"
            "v_mfma_f32_16x16x32_bf16 v[208:211], v[192:195], v[48:51], 0\n\t"
            "v_mul_f32 v162, v248, v162\n\t"
            "v_mul_f32 v163, v249, v163\n\t"
            "v_mfma_f32_16x16x32_bf16 v[208:211], v[196:199], v[52:55], v[208:211]\n\t"
            "ds_write_b128 v243, v[212:215] offset:4608\n\t"
            "v_mfma_f32_16x16x32_bf16 v[212:215], v[196:199], v[76:79], 0\n\t"
            "v_cvt_pk_bf16_f32 v160, v160, v161\n\t"
            "v_mul_f32 v166, v248, v166\n\t"
            "v_mfma_f32_16x16x32_bf16 v[212:215], v[192:195], v[72:75], v[212:215]\n\t"
            "ds_write_b128 v243, v[216:219] offset:6144\n\t"
            "v_mfma_f32_16x16x32_bf16 v[216:219], v[192:195], v[96:99], 0\n\t"
            "v_mul_f32 v167, v249, v167\n\t"
            "v_cvt_pk_bf16_f32 v164, v164, v165\n\t"
            "v_mfma_f32_16x16x32_bf16 v[216:219], v[196:199], v[100:103], v[216:219]\n\t"
            "ds_write_b128 v243, v[220:223] offset:7680\n\t"
            "v_mfma_f32_16x16x32_bf16 v[220:223], v[196:199], v[124:127], 0\n\t"
            "v_mul_f32 v178, v248, v178\n\t"
            "v_mul_f32 v179, v249, v179\n\t"
            "v_mfma_f32_16x16x32_bf16 v[220:223], v[192:195], v[120:123], v[220:223]\n\t"
            "v_cvt_pk_bf16_f32 v176, v176, v177\n\t"
            "v_mul_f32 v182, v248, v182\n\t"
            "s_waitcnt lgkmcnt(5)\n\t"
            "v_mfma_f32_16x16x32_bf16 v[200:203], v[224:227], v[8:11], v[200:203]\n\t"
            "s_waitcnt vmcnt(0)\n\t"
            "ds_read_b64 v[144:145], v253\n\t"
            "ds_read_b64 v[146:147], v253 offset:96\n\t"
            "v_mfma_f32_16x16x32_bf16 v[200:203], v[228:231], v[12:15], v[200:203]\n\t"
            "ds_read_b64 v[148:149], v253 offset:1744\n\t"
            "ds_read_b64 v[150:151], v253 offset:1840\n\t"
            "v_mfma_f32_16x16x32_bf16 v[204:207], v[228:231], v[36:39], v[204:207]\n\t"
            "ds_read_b64 v[152:153], v253 offset:8\n\t"
            "ds_read_b64 v[154:155], v253 offset:104\n\t"
            "v_mfma_f32_16x16x32_bf16 v[204:207], v[224:227], v[32:35], v[204:207]\n\t"
            "ds_read_b64 v[156:157], v253 offset:1752\n\t"
            "ds_read_b64 v[158:159], v253 offset:1848\n\t"
            "v_mfma_f32_16x16x32_bf16 v[208:211], v[224:227], v[56:59], v[208:211]\n\t"
            "v_mul_f32 v183, v249, v183\n\t"
            "v_cvt_pk_bf16_f32 v180, v180, v181\n\t"
            "v_mfma_f32_16x16x32_bf16 v[208:211], v[228:231], v[60:63], v[208:211]\n\t"
            "v_mul_f32 v186, v248, v186\n\t"
            "v_mul_f32 v187, v249, v187\n\t"
            "v_mfma_f32_16x16x32_bf16 v[212:215], v[228:231], v[84:87], v[212:215]\n\t"
            "v_cvt_pk_bf16_f32 v184, v184, v185\n\t"
            "v_mul_f32 v190, v248, v190\n\t"
            "v_mfma_f32_16x16x32_bf16 v[212:215], v[224:227], v[80:83], v[212:215]\n\t"
            "v_mul_f32 v191, v249, v191\n\t"
            "v_cvt_pk_bf16_f32 v188, v188, v189\n\t"
            "v_mfma_f32_16x16x32_bf16 v[216:219], v[224:227], v[104:107], v[216:219]\n\t"
            "v_cvt_pk_bf16_f32 v161, v162, v163\n\t"
            "v_cvt_pk_bf16_f32 v165, v166, v167\n\t"
            "v_mfma_f32_16x16x32_bf16 v[216:219], v[228:231], v[108:111], v[216:219]\n\t"
            "ds_read_b64 v[162:163], v253 offset:112\n\t"
            "ds_read_b64 v[166:167], v253 offset:1856\n\t"
            "v_mfma_f32_16x16x32_bf16 v[220:223], v[228:231], v[132:135], v[220:223]\n\t"
            "s_waitcnt lgkmcnt(14)\n\t"
            "ds_read_b128 v[228:231], v233 offset:256\n\t"
            "v_cvt_pk_bf16_f32 v177, v178, v179\n\t"
            "v_mfma_f32_16x16x32_bf16 v[220:223], v[224:227], v[128:131], v[220:223]\n\t"
            "s_waitcnt lgkmcnt(14)\n\t"
            "ds_read_b128 v[224:227], v232 offset:256\n\t"
            "v_cvt_pk_bf16_f32 v181, v182, v183\n\t"
            "v_cvt_pk_bf16_f32 v185, v186, v187\n\t"
            "v_cvt_pk_bf16_f32 v189, v190, v191\n\t"
            "v_xor_b32 v247, 0x100, v247\n\t"
            "s_waitcnt lgkmcnt(14)\n\t"
            "ds_write_b64 v244, v[160:161]\n\t"
            "s_waitcnt lgkmcnt(14)\n\t"
            "ds_read_b64 v[160:161], v253 offset:16\n\t"
            "s_waitcnt lgkmcnt(14)\n\t"
            "ds_write_b64 v244, v[164:165] offset:1536\n\t"
            "s_waitcnt lgkmcnt(14)\n\t"
            "ds_read_b64 v[164:165], v253 offset:1760\n\t"
            "s_waitcnt lgkmcnt(14)\n\t"
            "ds_write_b64 v244, v[176:177] offset:3072\n\t"
            "s_waitcnt lgkmcnt(14)\n\t"
            "ds_write_b64 v244, v[180:181] offset:4608\n\t"
            "s_waitcnt lgkmcnt(14)\n\t"
            "ds_write_b64 v244, v[184:185] offset:6144\n\t"
            "s_waitcnt lgkmcnt(14)\n\t"
            "ds_write_b64 v244, v[188:189] offset:7680\n\t"
            "s_waitcnt lgkmcnt(14)\n\t"
            "ds_write_b32 v246, v168\n\t"
            "s_waitcnt lgkmcnt(14)\n\t"
            "ds_read_b128 v[168:171], v254\n\t"
            "s_waitcnt lgkmcnt(1)\n\t"
            "s_barrier\n\t"
            "s_cmp_lt_u32 s46, 0x7000000\n\t"
            "s_cbranch_scc1 .Lwc_loop%=\n\t"
            "s_branch .Lwc_end%=\n\t"
            ".Lwc_spin%=:\n\t"
            "s_cmp_eq_u32 s83, 0\n\t"
            "s_cbranch_scc1 .Lwc_go%=\n\t"
            "s_sub_u32 s83, s83, 1\n\t"
            "s_sleep 2\n\t"
            "v_mov_b32 v234, 0\n\t"
            "ds_read_b64 v[234:235], v234 offset:7856\n\t"
            "s_waitcnt lgkmcnt(0)\n\t"
            "v_readfirstlane_b32 s77, v234\n\t"
            "v_readfirstlane_b32 s38, v235\n\t"
            "s_add_u32 s77, s77, s38\n\t"
            "s_cmp_ge_u32 s77, s44\n\t"
            "s_cbranch_scc0 .Lwc_spin%=\n\t"
            "s_branch .Lwc_go%=\n\t"
            ".Lwc_end%=:\n\t"
            "s_waitcnt lgkmcnt(0)\n\t"
            "s_nop 7\n\t"
            : "+{v246}"(aRSw_), "+{v247}"(aRSr_), "+{s44}"(s44), B_SIO
            : "{v242}"(pE_), "{v243}"(aVAw_), "{v244}"(aVBw_), "{v245}"(pST_), "{v250}"(pXw_), "{v251}"(pSTw_), "{v252}"(aCA_), "{v253}"(aB_), "{v254}"(aA_), "{v255}"(idx_), B_SIN, "{s82}"(touch)
            : "v144", "v145", "v146", "v147", "v148", "v149", "v150", "v151", "v152", "v153", "v154", "v155", "v156", "v157", "v158", "v159", "v160", "v161", "v162", "v163", "v164", "v165", "v166", "v167", "v168", "v169", "v170", "v171", "v172", "v173", "v174", "v175", "v176", "v177", "v178", "v179", "v180", "v181", "v182", "v183", "v184", "v185", "v186", "v187", "v188", "v189", "v190", "v191", "v192", "v193", "v194", "v195", "v196", "v197", "v198", "v199", "v236", "v237", "v248", "v249", "v0", "v1", "v2", "v3", "v4", "v5", "v6", "v7", "v8", "v9", "v10", "v11", "v12", "v13", "v14", "v15", "v16", "v17", "v18", "v19", "v20", "v21", "v22", "v23", "v24", "v25", "v26", "v27", "v28", "v29", "v30", "v31", "v32", "v33", "v34", "v35", "v36", "v37", "v38", "v39", "v40", "v41", "v42", "v43", "v44", "v45", "v46", "v47", "v48", "v49", "v50", "v51", "v52", "v53", "v54", "v55", "v56", "v57", "v58", "v59", "v60", "v61", "v62", "v63", "v64", "v65", "v66", "v67", "v68", "v69", "v70", "v71", "v72", "v73", "v74", "v75", "v76", "v77", "v78", "v79", "v80", "v81", "v82", "v83", "v84", "v85", "v86", "v87", "v88", "v89", "v90", "v91", "v92", "v93", "v94", "v95", "v96", "v97", "v98", "v99", "v100", "v101", "v102", "v103", "v104", "v105", "v106", "v107", "v108", "v109", "v110", "v111", "v112", "v113", "v114", "v115", "v116", "v117", "v118", "v119", "v120", "v121", "v122", "v123", "v124", "v125", "v126", "v127", "v128", "v129", "v130", "v131", "v132", "v133", "v134", "v135", "v136", "v137", "v138", "v139", "v140", "v141", "v142", "v143", "v200", "v201", "v202", "v204", "v205", "v206", "v207", "v208", "v209", "v210", "v211", "v212", "v213", "v214", "v215", "v216", "v217", "v218", "v219", "v220", "v221", "v222", "v223", "v224", "v225", "v226", "v227", "v228", "v229", "v230", "v231", "v232", "v233", "v234", "v235", B_SCLOB);
        }
        {
            __builtin_amdgcn_s_setprio(3);
            int ln_;
            asm volatile("v_mbcnt_lo_u32_b32 %0, -1, 0\n\tv_mbcnt_hi_u32_b32 %0, -1, %0" : "=v"(ln_));
            const int l15_ = ln_ & 15, g_ = ln_ >> 4;
            KARGS
            const auto orsrc = __builtin_amdgcn_make_buffer_rsrc((void*)ob, 0, IMG * IMG * CH * 2, 0x00020000);
            const auto wq = __builtin_amdgcn_make_buffer_rsrc((void*)wb, 0, WS_BYTES, 0x00020000);
            bf16x8 W[36];
#pragma unroll
            for (int ks = 0; ks < 6; ++ks)
#pragma unroll
                for (int j = 0; j < 6; ++j) W[j * 6 + ks] = BC8(__builtin_amdgcn_raw_buffer_load_b128(wq, ln_ * 16, (wave + 2) * 36864 + (j * 6 + ks) * 1024, SC1));
            SB();
            int lE[2], nw;
            unsigned o[2];
            f32x4 acc[2][6];
#pragma unroll
            for (int lt = 0; lt < 2; ++lt) {
                int i = 16 * lt + l15_;
                const unsigned en = *(const unsigned*)(smem + WL_OFF + i * 4);
                const bool ok = en != 0x7ffffffu;
                if (lt == 1) nw = 16 + __builtin_popcount((unsigned)__builtin_amdgcn_ballot_w64(ok) & 0xffffu);
                i = ok ? i : 0;
                lE[lt] = OL_OFF + i * 384 + ((g_ ^ ((i >> 1) & 7)) << 4);
                o[lt] = ok ? (en & 0x7ffffffu) + LPOS(en >> 31) * ROWBYTES + LPOS(en >> 30) * (CH * 2) + 192 * w + 16 * g_ : OOB;
#pragma unroll
                for (int k = 0; k < 3; ++k) {
                    const int c8 = 12 * w + 4 * k + g_, ws = (c8 * 43) >> 8, r6 = c8 - 6 * ws;
                    const u32x4 r = *(const u32x4*)(smem + LX_OFF(i) + (r6 >> 1) * 128 + ((2 * ws + (r6 & 1)) << 4));
                    acc[lt][2 * k] = (f32x4){bflo(r[0]), bfhi(r[0]), bflo(r[1]), bfhi(r[1])};
                    acc[lt][2 * k + 1] = (f32x4){bflo(r[2]), bfhi(r[2]), bflo(r[3]), bfhi(r[3])};
                }
            }
            SB();
            for (int it_ = 0; it_ < 100000; ++it_) {
                unsigned n_;
                asm volatile("ds_read_b32 %0, %1\n\ts_waitcnt lgkmcnt(0)" : "=v"(n_) : "v"(ldsb + FLAG_OFF) : "memory");
                if (__builtin_amdgcn_readfirstlane(n_) >= 4 * nw) break;
                __builtin_amdgcn_s_sleep(1);
            }
            bf16x8 of[2][6];
#pragma unroll
            for (int ks = 0; ks < 6; ++ks)
#pragma unroll
                for (int lt = 0; lt < 2; ++lt) of[lt][ks] = *(const bf16x8*)(smem + ((ks & 1) ? (lE[lt] ^ 64) : lE[lt]) + (ks >> 1) * 128);
            SB();
#pragma unroll
            for (int lt = 0; lt < 2; ++lt) {
#pragma unroll
                for (int ks = 0; ks < 6; ++ks)
#pragma unroll
                    for (int j = 0; j < 6; ++j) acc[lt][j] = MFMA32(W[j * 6 + ks], of[lt][ks], acc[lt][j]);
#pragma unroll
                for (int k = 0; k < 3; ++k) {
                    const f32x4 a = acc[lt][2 * k], b = acc[lt][2 * k + 1];
                    const u32x4 ov = {pk2(a[0], a[1]), pk2(a[2], a[3]), pk2(b[0], b[1]), pk2(b[2], b[3])};
                    __builtin_amdgcn_raw_buffer_store_b128(ov, orsrc, o[lt] + 64 * k, 0, SC1);
                }
            }
        }
        __builtin_amdgcn_endpgm();
    } else {
        LANE_SETUP
        {
            const unsigned oE_ = ldsb + fE + (O_OFF - X_OFF), oO_ = oE_ ^ 64;
            unsigned l16_ = lane * 16;
            unsigned pr[3];
#pragma unroll
            for (int k = 0; k < 3; ++k) {
                const int c8 = 12 * (w - 2) + 4 * k + g;
                const int ws = c8 / 6, r6 = c8 - 6 * ws;
                pr[k] = l15 * 384 + (r6 >> 1) * 128 + (((2 * ws + (r6 & 1)) ^ sw) << 4);
            }
            const unsigned pR0_ = pr[0], pR1_ = pr[1], pR2_ = pr[2];
            unsigned q0 = ldsb + PF_OFF + 1024 + lane * 4, q1 = ldsb + O_OFF + lane * 16, q2 = oE_, q3 = ldsb + PF_OFF + 1024 + l15 * 4;
            unsigned q4 = ldsb + OL_OFF + l15 * 384 + (((4 * (w - 2) + (g >> 1)) ^ sw) << 4) + 8 * (g & 1);
            B_SCALARS
            asm volatile(
            "s_waitcnt lgkmcnt(0)\n\t"
            "s_mov_b32 s83, 0x10000\n\t"
            "s_mov_b32 s84, -47104\n\t"
            "s_nop 7\n\t"
            "v_mov_b32 v200, v250\n\t"
            "v_mov_b32 v201, v251\n\t"
            "v_mov_b32 v202, v253\n\t"
            "s_mov_b32 s38, s67\n\t"
            "s_mov_b32 s67, 74496\n\t"
            "ds_read_b64 v[250:251], v203\n\t"
            "ds_read_b32 v253, v203 offset:8\n\t"
            ".Lwk_b1%=:\n\t"
            "v_mov_b32 v236, 0\n\t"
            "ds_read_b32 v236, v236 offset:7864\n\t"
            "s_waitcnt lgkmcnt(0)\n\t"
            "v_readfirstlane_b32 s77, v236\n\t"
            "s_cmp_ge_u32 s77, 4\n\t"
            "s_cbranch_scc1 .Lwk_b2%=\n\t"
            "s_cmp_eq_u32 s83, 0\n\t"
            "s_cbranch_scc1 .Lwk_b2%=\n\t"
            "s_sub_u32 s83, s83, 1\n\t"
            "s_sleep 1\n\t"
            "s_branch .Lwk_b1%=\n\t"
            ".Lwk_b2%=:\n\t"
            "s_waitcnt vmcnt(0)\n\t"
            "s_call_b64 vcc, wk_conv\n\t"
            "v_mov_b32 v250, v200\n\t"
            "v_mov_b32 v251, v201\n\t"
            "v_mov_b32 v253, v202\n\t"
            "s_mov_b32 s67, s38\n\t"
            "s_barrier\n\t"
            "ds_read_b64 v[236:237], v252 offset:2560\n\t"
            "s_waitcnt lgkmcnt(0)\n\t"
            "s_mov_b64 exec, 1\n\t"
            "buffer_store_dwordx2 v[236:237], off, s[28:31], s44 sc1\n\t"
            "s_mov_b64 exec, -1\n\t"
            "s_sleep 30\n\t"
            "ds_read_b32 v249, v252 offset:12\n\t"
            "s_waitcnt lgkmcnt(0)\n\t"
            "v_and_b32 v249, s69, v249\n\t"
            "s_mov_b32 exec_hi, 0x3fffff\n\t"
            "s_add_u32 m0, s66, 0\n\t"
            "s_add_u32 s49, s79, 0\n\t"
            "buffer_load_dwordx4 v249, s[72:75], s49 offen lds\n\t"
            "s_mov_b32 exec_hi, -1\n\t"
            "s_mov_b32 exec_hi, 0x3fffff\n\t"
            "s_add_u32 m0, s66, 872\n\t"
            "s_add_u32 s49, s47, 196224\n\t"
            "buffer_load_dwordx4 v249, s[72:75], s49 offen lds\n\t"
            "s_mov_b32 exec_hi, -1\n\t"
            "s_mov_b32 exec_hi, 0x3fffff\n\t"
            "s_add_u32 m0, s66, 1744\n\t"
            "s_add_u32 s49, s47, 392448\n\t"
            "buffer_load_dwordx4 v249, s[72:75], s49 offen lds\n\t"
            "s_mov_b32 exec_hi, -1\n\t"
            "s_mov_b32 exec_hi, 0x3fffff\n\t"
            "s_add_u32 m0, s66, 2616\n\t"
            "s_add_u32 s49, s47, 588672\n\t"
            "buffer_load_dwordx4 v249, s[72:75], s49 offen lds\n\t"
            "s_mov_b32 exec_hi, -1\n\t"
            "s_mov_b32 exec_hi, 0x3fffff\n\t"
            "s_add_u32 m0, s66, 3488\n\t"
            "s_add_u32 s49, s47, 784896\n\t"
            "buffer_load_dwordx4 v249, s[72:75], s49 offen lds\n\t"
            "s_mov_b32 exec_hi, -1\n\t"
            "s_mov_b32 exec_hi, 0x3fffff\n\t"
            "s_add_u32 m0, s66, 4360\n\t"
            "s_add_u32 s49, s47, 981120\n\t"
            "buffer_load_dwordx4 v249, s[72:75], s49 offen lds\n\t"
            "s_mov_b32 exec_hi, -1\n\t"
            "s_mov_b32 exec_hi, 0x3fffff\n\t"
            "s_add_u32 m0, s66, 5232\n\t"
            "s_add_u32 s49, s47, 1177344\n\t"
            "buffer_load_dwordx4 v249, s[72:75], s49 offen lds\n\t"
            "s_mov_b32 exec_hi, -1\n\t"
            "s_mov_b32 exec_hi, 0x3fffff\n\t"
            "s_add_u32 m0, s66, 6104\n\t"
            "s_add_u32 s49, s47, 1373568\n\t"
            "buffer_load_dwordx4 v249, s[72:75], s49 offen lds\n\t"
            "s_mov_b32 exec_hi, -1\n\t"
            "s_mov_b32 exec_hi, 0x3fffff\n\t"
            "s_add_u32 m0, s66, 6976\n\t"
            "s_add_u32 s49, s47, 1569792\n\t"
            "buffer_load_dwordx4 v249, s[72:75], s49 offen lds\n\t"
            "s_mov_b32 exec_hi, -1\n\t"
            "s_add_u32 s49, s33, -73728\n\t"
            "buffer_load_dwordx4 v[0:3], v247, s[28:31], s49 offen sc1\n\t"
            "s_add_u32 s49, s33, -67584\n\t"
            "buffer_load_dwordx4 v[24:27], v247, s[28:31], s49 offen sc1\n\t"
            "s_add_u32 s49, s33, -61440\n\t"
            "buffer_load_dwordx4 v[48:51], v247, s[28:31], s49 offen sc1\n\t"
            "s_add_u32 s49, s33, -55296\n\t"
            "buffer_load_dwordx4 v[72:75], v247, s[28:31], s49 offen sc1\n\t"
            "s_add_u32 s49, s33, -49152\n\t"
            "buffer_load_dwordx4 v[96:99], v247, s[28:31], s49 offen sc1\n\t"
            "s_add_u32 s49, s33, -43008\n\t"
            "buffer_load_dwordx4 v[120:123], v247, s[28:31], s49 offen sc1\n\t"
            "s_add_u32 s49, s33, -72704\n\t"
            "buffer_load_dwordx4 v[4:7], v247, s[28:31], s49 offen sc1\n\t"
            "s_add_u32 s49, s33, -66560\n\t"
            "buffer_load_dwordx4 v[28:31], v247, s[28:31], s49 offen sc1\n\t"
            "s_add_u32 s49, s33, -60416\n\t"
            "buffer_load_dwordx4 v[52:55], v247, s[28:31], s49 offen sc1\n\t"
            "s_add_u32 s49, s33, -54272\n\t"
            "buffer_load_dwordx4 v[76:79], v247, s[28:31], s49 offen sc1\n\t"
            "s_add_u32 s49, s33, -48128\n\t"
            "buffer_load_dwordx4 v[100:103], v247, s[28:31], s49 offen sc1\n\t"
            "s_add_u32 s49, s33, -41984\n\t"
            "buffer_load_dwordx4 v[124:127], v247, s[28:31], s49 offen sc1\n\t"
            "s_add_u32 s49, s33, -71680\n\t"
            "buffer_load_dwordx4 v[8:11], v247, s[28:31], s49 offen sc1\n\t"
            "s_add_u32 s49, s33, -65536\n\t"
            "buffer_load_dwordx4 v[32:35], v247, s[28:31], s49 offen sc1\n\t"
            "s_add_u32 s49, s33, -59392\n\t"
            "buffer_load_dwordx4 v[56:59], v247, s[28:31], s49 offen sc1\n\t"
            "s_add_u32 s49, s33, -53248\n\t"
            "buffer_load_dwordx4 v[80:83], v247, s[28:31], s49 offen sc1\n\t"
            "s_add_u32 s49, s33, -47104\n\t"
            "buffer_load_dwordx4 v[104:107], v247, s[28:31], s49 offen sc1\n\t"
            "s_add_u32 s49, s33, -40960\n\t"
            "buffer_load_dwordx4 v[128:131], v247, s[28:31], s49 offen sc1\n\t"
            "s_add_u32 s49, s33, -70656\n\t"
            "buffer_load_dwordx4 v[12:15], v247, s[28:31], s49 offen sc1\n\t"
            "s_add_u32 s49, s33, -64512\n\t"
            "buffer_load_dwordx4 v[36:39], v247, s[28:31], s49 offen sc1\n\t"
            "s_add_u32 s49, s33, -58368\n\t"
            "buffer_load_dwordx4 v[60:63], v247, s[28:31], s49 offen sc1\n\t"
            "s_add_u32 s49, s33, -52224\n\t"
            "buffer_load_dwordx4 v[84:87], v247, s[28:31], s49 offen sc1\n\t"
            "s_add_u32 s49, s33, -46080\n\t"
            "buffer_load_dwordx4 v[108:111], v247, s[28:31], s49 offen sc1\n\t"
            "s_add_u32 s49, s33, -39936\n\t"
            "buffer_load_dwordx4 v[132:135], v247, s[28:31], s49 offen sc1\n\t"
            "s_add_u32 s49, s33, -69632\n\t"
            "buffer_load_dwordx4 v[16:19], v247, s[28:31], s49 offen sc1\n\t"
            "s_add_u32 s49, s33, -63488\n\t"
            "buffer_load_dwordx4 v[40:43], v247, s[28:31], s49 offen sc1\n\t"
            "s_add_u32 s49, s33, -57344\n\t"
            "buffer_load_dwordx4 v[64:67], v247, s[28:31], s49 offen sc1\n\t"
            "s_add_u32 s49, s33, -51200\n\t"
            "buffer_load_dwordx4 v[88:91], v247, s[28:31], s49 offen sc1\n\t"
            "s_add_u32 s49, s33, -45056\n\t"
            "buffer_load_dwordx4 v[112:115], v247, s[28:31], s49 offen sc1\n\t"
            "s_add_u32 s49, s33, -38912\n\t"
            "buffer_load_dwordx4 v[136:139], v247, s[28:31], s49 offen sc1\n\t"
            "s_add_u32 s49, s33, -68608\n\t"
            "buffer_load_dwordx4 v[20:23], v247, s[28:31], s49 offen sc1\n\t"
            "s_add_u32 s49, s33, -62464\n\t"
            "buffer_load_dwordx4 v[44:47], v247, s[28:31], s49 offen sc1\n\t"
            "s_add_u32 s49, s33, -56320\n\t"
            "buffer_load_dwordx4 v[68:71], v247, s[28:31], s49 offen sc1\n\t"
            "s_add_u32 s49, s33, -50176\n\t"
            "buffer_load_dwordx4 v[92:95], v247, s[28:31], s49 offen sc1\n\t"
            "s_add_u32 s49, s33, -44032\n\t"
            "buffer_load_dwordx4 v[116:119], v247, s[28:31], s49 offen sc1\n\t"
            "s_add_u32 s49, s33, -37888\n\t"
            "buffer_load_dwordx4 v[140:143], v247, s[28:31], s49 offen sc1\n\t"
            "s_setprio 3\n\t"
            "ds_read_b128 v[168:171], v254\n\t"
            "ds_read_b128 v[172:175], v254 offset:64\n\t"
            "ds_read_b128 v[220:223], v229 offset:12288\n\t"
            "ds_read_b128 v[224:227], v229 offset:13312\n\t"
            "ds_read_b128 v[200:203], v232\n\t"
            "ds_read_b128 v[208:211], v232 offset:128\n\t"
            "ds_read_b128 v[216:219], v232 offset:256\n\t"
            "s_waitcnt vmcnt(36)\n\t"
            "ds_read_b64 v[144:145], v253\n\t"
            "ds_read_b64 v[146:147], v253 offset:96\n\t"
            "ds_read_b64 v[148:149], v253 offset:1744\n\t"
            "ds_read_b64 v[150:151], v253 offset:1840\n\t"
            "ds_read_b64 v[152:153], v253 offset:8\n\t"
            "ds_read_b64 v[154:155], v253 offset:104\n\t"
            "ds_read_b64 v[156:157], v253 offset:1752\n\t"
            "ds_read_b64 v[158:159], v253 offset:1848\n\t"
            "s_waitcnt lgkmcnt(14)\n\t"
            "ds_read_b64 v[160:161], v253 offset:16\n\t"
            "s_waitcnt lgkmcnt(14)\n\t"
            "ds_read_b64 v[162:163], v253 offset:112\n\t"
            "s_waitcnt lgkmcnt(14)\n\t"
            "ds_read_b64 v[164:165], v253 offset:1760\n\t"
            "s_waitcnt lgkmcnt(14)\n\t"
            "ds_read_b64 v[166:167], v253 offset:1856\n\t"
            "v_mov_b64 v[176:177], 0\n\t"
            "v_mov_b64 v[178:179], 0\n\t"
            "v_mov_b64 v[180:181], 0\n\t"
            "v_mov_b64 v[182:183], 0\n\t"
            "v_mov_b32 v249, s76\n\t"
            "v_mov_b64 v[184:185], 0\n\t"
            "v_mov_b64 v[186:187], 0\n\t"
            "v_mov_b64 v[188:189], 0\n\t"
            "v_mov_b64 v[190:191], 0\n\t"
            "v_add_u32 v236, s67, v250\n\t"
            "v_add_u32 v248, s67, v251\n\t"
            "v_xor_b32 v235, 64, v232\n\t"
            "s_nop 0\n\t"
            "s_waitcnt lgkmcnt(14)\n\t"
            "ds_read_b32 v249, v249\n\t"
            "s_waitcnt lgkmcnt(14)\n\t"
            "ds_read_b128 v[204:207], v235\n\t"
            "s_waitcnt lgkmcnt(14)\n\t"
            "ds_read_b128 v[212:215], v235 offset:128\n\t"
            "v_add_f32 v220, v220, v222\n\t"
            "v_add_f32 v224, v224, v226\n\t"
            "v_add_f32 v220, v220, v224\n\t"
            "v_add_f32 v221, v221, v223\n\t"
            "v_add_f32 v225, v225, v227\n\t"
            "v_mul_f32 v220, 0x3baaaaab, v220\n\t"
            "v_add_f32 v221, v221, v225\n\t"
            "v_mul_f32_e64 v220, v220, -v220\n\t"
            "v_fmac_f32 v220, 0x3baaaaab, v221\n\t"
            "s_waitcnt lgkmcnt(11)\n\t"
            "v_smfmac_f32_16x16x64_bf16 v[176:179], v[168:171], v[144:151], v255\n\t"
            "ds_read_b128 v[168:171], v254 offset:128\n\t"
            "s_waitcnt lgkmcnt(8)\n\t"
            "v_smfmac_f32_16x16x64_bf16 v[180:183], v[172:175], v[152:159], v255\n\t"
            "ds_read_b64 v[144:145], v253 offset:24\n\t"
            "ds_read_b64 v[146:147], v253 offset:120\n\t"
            "ds_read_b64 v[148:149], v253 offset:1768\n\t"
            "ds_read_b64 v[150:151], v253 offset:1864\n\t"
            "ds_read_b128 v[172:175], v254 offset:192\n\t"
            "ds_read_b64 v[152:153], v253 offset:32\n\t"
            "ds_read_b64 v[154:155], v253 offset:128\n\t"
            "s_waitcnt lgkmcnt(14)\n\t"
            "ds_read_b64 v[156:157], v253 offset:1776\n\t"
            "s_waitcnt lgkmcnt(14)\n\t"
            "ds_read_b64 v[158:159], v253 offset:1872\n\t"
            "s_waitcnt lgkmcnt(14)\n\t"
            "ds_read_b128 v[224:227], v232 offset:6144\n\t"
            "v_cvt_pk_bf16_f32 v176, v176, v177\n\t"
            "v_cvt_pk_bf16_f32 v177, v178, v179\n\t"
            "v_cvt_pk_bf16_f32 v178, v180, v181\n\t"
            "v_cvt_pk_bf16_f32 v179, v182, v183\n\t"
            "v_mfma_f32_4x4x4_16b_bf16 v[196:199], v[176:177], v[176:177], 0\n\t"
            "v_mov_b64 v[180:181], 0\n\t"
            "v_mfma_f32_16x16x32_bf16 v[192:195], v[238:241], v[176:179], 0\n\t"
            "s_waitcnt lgkmcnt(13)\n\t"
            "v_readfirstlane_b32 s26, v249\n\t"
            "v_mov_b64 v[182:183], 0\n\t"
            "v_mfma_f32_4x4x4_16b_bf16 v[196:199], v[178:179], v[178:179], v[196:199]\n\t"
            "ds_read_b32 v249, v252 offset:12\n\t"
            "s_and_b32 s68, s26, 0x7ffffff\n\t"
            "s_add_u32 s68, s68, s78\n\t"
            "s_and_b32 s69, s26, 0x38000000\n\t"
            "s_or_b32 s69, s69, s39\n\t"
            "s_bitcmp1_b32 s26, 27\n\t"
            "s_cselect_b32 s79, 2.0, s68\n\t"
            "s_and_b32 s57, s56, 3\n\t"
            "s_lshl_b32 s58, s57, 3\n\t"
            "s_lshr_b32 m0, s54, s58\n\t"
            "s_lshl_b32 s58, s57, 4\n\t"
            "s_lshr_b64 s[36:37], s[50:51], s58\n\t"
            "s_and_b32 s57, s36, 0xffff\n\t"
            "s_add_u32 s57, s57, s67\n\t"
            "v_writelane_b32 v236, s57, 63\n\t"
            "v_writelane_b32 v236, s59, m0\n\t"
            "v_xor_b32 v237, 16, v236\n\t"
            "s_lshr_b64 s[36:37], s[52:53], s58\n\t"
            "s_and_b32 s58, s36, 0xffff\n\t"
            "s_add_u32 s58, s58, s67\n\t"
            "s_waitcnt lgkmcnt(11)\n\t"
            "v_smfmac_f32_16x16x64_bf16 v[184:187], v[168:171], v[160:167], v255\n\t"
            "s_and_b32 s38, s52, 0xffff\n\t"
            "s_add_u32 s38, s38, s67\n\t"
            "ds_read_b128 v[168:171], v254 offset:256\n\t"
            "ds_read_b64 v[160:161], v253 offset:40\n\t"
            "ds_read_b64 v[162:163], v253 offset:136\n\t"
            "ds_read_b64 v[164:165], v253 offset:1784\n\t"
            "s_waitcnt lgkmcnt(14)\n\t"
            "ds_read_b64 v[166:167], v253 offset:1880\n\t"
            "s_waitcnt lgkmcnt(11)\n\t"
            "v_smfmac_f32_16x16x64_bf16 v[188:191], v[172:175], v[144:151], v255\n\t"
            "ds_read_b128 v[172:175], v254 offset:320\n\t"
            "ds_read_b64 v[144:145], v253 offset:48\n\t"
            "ds_read_b64 v[146:147], v253 offset:144\n\t"
            "ds_read_b64 v[148:149], v253 offset:1792\n\t"
            "s_waitcnt lgkmcnt(14)\n\t"
            "ds_read_b64 v[150:151], v253 offset:1888\n\t"
            "s_waitcnt lgkmcnt(14)\n\t"
            "ds_write_b128 v236, v[176:179]\n\t"
            "v_mov_b64 v[176:177], 0\n\t"
            "v_mov_b64 v[178:179], 0\n\t"
            "v_cvt_pk_bf16_f32 v184, v184, v185\n\t"
            "v_cvt_pk_bf16_f32 v185, v186, v187\n\t"
            "v_cvt_pk_bf16_f32 v186, v188, v189\n\t"
            "v_cvt_pk_bf16_f32 v187, v190, v191\n\t"
            "v_mfma_f32_4x4x4_16b_bf16 v[196:199], v[184:185], v[184:185], v[196:199]\n\t"
            "s_waitcnt lgkmcnt(14)\n\t"
            "ds_write_b128 v237, v[184:187]\n\t"
            "v_mfma_f32_16x16x32_bf16 v[192:195], v[238:241], v[184:187], v[192:195]\n\t"
            "v_mov_b64 v[184:185], 0\n\t"
            "v_mov_b64 v[188:189], 0\n\t"
            "v_mfma_f32_4x4x4_16b_bf16 v[196:199], v[186:187], v[186:187], v[196:199]\n\t"
            "v_mov_b64 v[186:187], 0\n\t"
            "v_mov_b64 v[190:191], 0\n\t"
            "v_writelane_b32 v248, s58, 63\n\t"
            "v_writelane_b32 v248, s38, m0\n\t"
            "s_waitcnt lgkmcnt(12)\n\t"
            "v_and_b32 v249, s69, v249\n\t"
            "v_max_f32 v220, 0, v220\n\t"
            "v_add_f32 v220, 0x3727c5ac, v220\n\t"
            "v_rsq_f32 v220, v220\n\t"
            "s_waitcnt lgkmcnt(11)\n\t"
            "v_smfmac_f32_16x16x64_bf16 v[176:179], v[168:171], v[152:159], v255\n\t"
            "ds_read_b128 v[168:171], v254 offset:384\n\t"
            "ds_read_b64 v[152:153], v253 offset:56\n\t"
            "ds_read_b64 v[154:155], v253 offset:152\n\t"
            "ds_read_b64 v[156:157], v253 offset:1800\n\t"
            "s_waitcnt lgkmcnt(14)\n\t"
            "ds_read_b64 v[158:159], v253 offset:1896\n\t"
            "s_waitcnt lgkmcnt(11)\n\t"
            "v_smfmac_f32_16x16x64_bf16 v[180:183], v[172:175], v[160:167], v255\n\t"
            "ds_read_b128 v[172:175], v254 offset:448\n\t"
            "ds_read_b64 v[160:161], v253 offset:64\n\t"
            "ds_read_b64 v[162:163], v253 offset:160\n\t"
            "ds_read_b64 v[164:165], v253 offset:1808\n\t"
            "s_waitcnt lgkmcnt(14)\n\t"
            "ds_read_b64 v[166:167], v253 offset:1904\n\t"
            "v_cvt_pk_bf16_f32 v176, v176, v177\n\t"
            "v_cvt_pk_bf16_f32 v177, v178, v179\n\t"
            "v_cvt_pk_bf16_f32 v178, v180, v181\n\t"
            "v_cvt_pk_bf16_f32 v179, v182, v183\n\t"
            "v_mfma_f32_4x4x4_16b_bf16 v[196:199], v[176:177], v[176:177], v[196:199]\n\t"
            "s_waitcnt lgkmcnt(14)\n\t"
            "ds_write_b128 v236, v[176:179] offset:128\n\t"
            "v_mfma_f32_16x16x32_bf16 v[192:195], v[238:241], v[176:179], v[192:195]\n\t"
            "v_mov_b64 v[176:177], 0\n\t"
            "v_mov_b64 v[180:181], 0\n\t"
            "v_mfma_f32_4x4x4_16b_bf16 v[196:199], v[178:179], v[178:179], v[196:199]\n\t"
            "v_mov_b64 v[178:179], 0\n\t"
            "v_mov_b64 v[182:183], 0\n\t"
            "s_waitcnt lgkmcnt(14)\n\t"
            "ds_write_b32 v228, v220\n\t"
            "s_waitcnt lgkmcnt(14)\n\t"
            "ds_read_b128 v[220:223], v235 offset:256\n\t"
            "s_waitcnt lgkmcnt(14)\n\t"
            "ds_read_b32 v230, v233\n\t"
            "s_waitcnt lgkmcnt(14)\n\t"
            "ds_read_b32 v231, v233 offset:64\n\t"
            "v_xor_b32 v228, 32, v234\n\t"
            "s_waitcnt lgkmcnt(14)\n\t"
            "v_smfmac_f32_16x16x64_bf16 v[184:187], v[168:171], v[144:151], v255\n\t"
            "ds_read_b128 v[168:171], v254 offset:512\n\t"
            "s_waitcnt lgkmcnt(14)\n\t"
            "ds_read_b64 v[144:145], v253 offset:72\n\t"
            "s_waitcnt lgkmcnt(14)\n\t"
            "ds_read_b64 v[146:147], v253 offset:168\n\t"
            "s_waitcnt lgkmcnt(14)\n\t"
            "ds_read_b64 v[148:149], v253 offset:1816\n\t"
            "s_waitcnt lgkmcnt(14)\n\t"
            "ds_read_b64 v[150:151], v253 offset:1912\n\t"
            "s_waitcnt lgkmcnt(14)\n\t"
            "v_smfmac_f32_16x16x64_bf16 v[188:191], v[172:175], v[152:159], v255\n\t"
            "ds_read_b128 v[172:175], v254 offset:576\n\t"
            "s_waitcnt lgkmcnt(14)\n\t"
            "ds_read_b64 v[152:153], v253 offset:80\n\t"
            "s_waitcnt lgkmcnt(14)\n\t"
            "ds_read_b64 v[154:155], v253 offset:176\n\t"
            "s_waitcnt lgkmcnt(14)\n\t"
            "ds_read_b64 v[156:157], v253 offset:1824\n\t"
            "s_waitcnt lgkmcnt(14)\n\t"
            "ds_read_b64 v[158:159], v253 offset:1920\n\t"
            "v_cvt_pk_bf16_f32 v184, v184, v185\n\t"
            "v_cvt_pk_bf16_f32 v185, v186, v187\n\t"
            "v_cvt_pk_bf16_f32 v186, v188, v189\n\t"
            "v_cvt_pk_bf16_f32 v187, v190, v191\n\t"
            "v_mfma_f32_4x4x4_16b_bf16 v[196:199], v[184:185], v[184:185], v[196:199]\n\t"
            "s_waitcnt lgkmcnt(14)\n\t"
            "ds_write_b128 v237, v[184:187] offset:128\n\t"
            "v_mfma_f32_16x16x32_bf16 v[192:195], v[238:241], v[184:187], v[192:195]\n\t"
            "v_mov_b64 v[184:185], 0\n\t"
            "v_mov_b64 v[188:189], 0\n\t"
            "v_mfma_f32_4x4x4_16b_bf16 v[196:199], v[186:187], v[186:187], v[196:199]\n\t"
            "v_mov_b64 v[186:187], 0\n\t"
            "v_mov_b64 v[190:191], 0\n\t"
            "s_waitcnt lgkmcnt(10)\n\t"
            "v_smfmac_f32_16x16x64_bf16 v[176:179], v[168:171], v[160:167], v255\n\t"
            "ds_read_b128 v[168:171], v254 offset:640\n\t"
            "ds_read_b64 v[160:161], v253 offset:88\n\t"
            "ds_read_b64 v[162:163], v253 offset:184\n\t"
            "ds_read_b64 v[164:165], v253 offset:1832\n\t"
            "ds_read_b64 v[166:167], v253 offset:1928\n\t"
            "s_waitcnt lgkmcnt(10)\n\t"
            "v_smfmac_f32_16x16x64_bf16 v[180:183], v[172:175], v[144:151], v255\n\t"
            "ds_read_b128 v[172:175], v254 offset:704\n\t"
            "v_cvt_pk_bf16_f32 v176, v176, v177\n\t"
            "v_cvt_pk_bf16_f32 v177, v178, v179\n\t"
            "s_nop 1\n\t"
            "v_mfma_f32_4x4x4_16b_bf16 v[196:199], v[176:177], v[176:177], v[196:199]\n\t"
            "s_nop 1\n\t"
            "v_cvt_pk_bf16_f32 v178, v180, v181\n\t"
            "v_cvt_pk_bf16_f32 v179, v182, v183\n\t"
            "s_nop 1\n\t"
            "v_mfma_f32_4x4x4_16b_bf16 v[196:199], v[178:179], v[178:179], v[196:199]\n\t"
            "ds_write_b128 v236, v[176:179] offset:256\n\t"
            "v_mfma_f32_16x16x32_bf16 v[192:195], v[238:241], v[176:179], v[192:195]\n\t"
            "v_add_u32 v236, s48, v244\n\t"
            "s_waitcnt lgkmcnt(6)\n\t"
            "v_smfmac_f32_16x16x64_bf16 v[184:187], v[168:171], v[152:159], v255\n\t"
            "s_waitcnt lgkmcnt(1)\n\t"
            "v_smfmac_f32_16x16x64_bf16 v[188:191], v[172:175], v[160:167], v255\n\t"
            "s_mov_b32 exec_hi, 0x3fffff\n\t"
            "s_add_u32 m0, s66, 0\n\t"
            "s_add_u32 s49, s79, 0\n\t"
            "buffer_load_dwordx4 v249, s[72:75], s49 offen lds\n\t"
            "s_mov_b32 exec_hi, -1\n\t"
            "s_mov_b32 exec_hi, 0x3fffff\n\t"
            "s_add_u32 m0, s66, 872\n\t"
            "s_add_u32 s49, s68, 196224\n\t"
            "buffer_load_dwordx4 v249, s[72:75], s49 offen lds\n\t"
            "s_mov_b32 exec_hi, -1\n\t"
            "s_mov_b32 exec_hi, 0x3fffff\n\t"
            "s_add_u32 m0, s66, 1744\n\t"
            "s_add_u32 s49, s68, 392448\n\t"
            "buffer_load_dwordx4 v249, s[72:75], s49 offen lds\n\t"
            "s_mov_b32 exec_hi, -1\n\t"
            "s_mov_b32 exec_hi, 0x3fffff\n\t"
            "s_add_u32 m0, s66, 2616\n\t"
            "s_add_u32 s49, s68, 588672\n\t"
            "buffer_load_dwordx4 v249, s[72:75], s49 offen lds\n\t"
            "s_mov_b32 exec_hi, -1\n\t"
            "s_mov_b32 exec_hi, 0x3fffff\n\t"
            "s_add_u32 m0, s66, 3488\n\t"
            "s_add_u32 s49, s68, 784896\n\t"
            "buffer_load_dwordx4 v249, s[72:75], s49 offen lds\n\t"
            "s_mov_b32 exec_hi, -1\n\t"
            "s_mov_b32 exec_hi, 0x3fffff\n\t"
            "s_add_u32 m0, s66, 4360\n\t"
            "s_add_u32 s49, s68, 981120\n\t"
            "buffer_load_dwordx4 v249, s[72:75], s49 offen lds\n\t"
            "s_mov_b32 exec_hi, -1\n\t"
            "s_mov_b32 exec_hi, 0x3fffff\n\t"
            "s_add_u32 m0, s66, 5232\n\t"
            "s_add_u32 s49, s68, 1177344\n\t"
            "buffer_load_dwordx4 v249, s[72:75], s49 offen lds\n\t"
            "s_mov_b32 exec_hi, -1\n\t"
            "v_cvt_pk_bf16_f32 v184, v184, v185\n\t"
            "v_cvt_pk_bf16_f32 v185, v186, v187\n\t"
            "v_cvt_pk_bf16_f32 v186, v188, v189\n\t"
            "v_cvt_pk_bf16_f32 v187, v190, v191\n\t"
            "v_mfma_f32_4x4x4_16b_bf16 v[196:199], v[184:185], v[184:185], v[196:199]\n\t"
            "s_mov_b32 exec_hi, 0x3fffff\n\t"
            "s_add_u32 m0, s66, 6104\n\t"
            "s_add_u32 s49, s68, 1373568\n\t"
            "buffer_load_dwordx4 v249, s[72:75], s49 offen lds\n\t"
            "s_mov_b32 exec_hi, -1\n\t"
            "v_mfma_f32_4x4x4_16b_bf16 v[196:199], v[186:187], v[186:187], v[196:199]\n\t"
            "s_mov_b32 exec_hi, 0x3fffff\n\t"
            "s_add_u32 m0, s66, 6976\n\t"
            "s_add_u32 s49, s68, 1569792\n\t"
            "buffer_load_dwordx4 v249, s[72:75], s49 offen lds\n\t"
            "s_mov_b32 exec_hi, -1\n\t"
            "v_mfma_f32_16x16x32_bf16 v[192:195], v[238:241], v[184:187], v[192:195]\n\t"
            "s_waitcnt vmcnt(9)\n\t"
            "ds_write_b128 v237, v[184:187] offset:256\n\t"
            "v_mfma_f32_16x16x32_bf16 v[144:147], v[0:3], v[200:203], 0\n\t"
            "v_add_u32 v237, s48, v245\n\t"
            "v_mfma_f32_16x16x32_bf16 v[144:147], v[4:7], v[204:207], v[144:147]\n\t"
            "v_mfma_f32_16x16x32_bf16 v[148:151], v[24:27], v[200:203], 0\n\t"
            "s_nop 1\n\t"
            "v_cndmask_b32_e64 v193, v196, v197, s[60:61]\n\t"
            "v_mfma_f32_16x16x32_bf16 v[148:151], v[28:31], v[204:207], v[148:151]\n\t"
            "v_cndmask_b32_e64 v193, v193, v198, s[62:63]\n\t"
            "v_cndmask_b32_e64 v193, v193, v199, s[64:65]\n\t"
            "v_mfma_f32_16x16x32_bf16 v[168:171], v[48:51], v[200:203], 0\n\t"
            "ds_read_b128 v[196:199], v232 offset:6272\n\t"
            "v_mfma_f32_16x16x32_bf16 v[168:171], v[52:55], v[204:207], v[168:171]\n\t"
            "ds_write_b64 v248, v[192:193] offset:18816\n\t"
            "v_mfma_f32_16x16x32_bf16 v[172:175], v[72:75], v[200:203], 0\n\t"
            "ds_read_b128 v[192:195], v235 offset:6144\n\t"
            "v_add_u32 v248, s48, v246\n\t"
            "v_mfma_f32_16x16x32_bf16 v[172:175], v[76:79], v[204:207], v[172:175]\n\t"
            "s_mov_b64 exec, 1\n\t"
            "ds_inc_u32 v255, v238 offset:7860\n\t"
            "s_mov_b64 exec, -1\n\t"
            "v_mfma_f32_16x16x32_bf16 v[152:155], v[96:99], v[200:203], 0\n\t"
            "v_mfma_f32_16x16x32_bf16 v[152:155], v[100:103], v[204:207], v[152:155]\n\t"
            "v_mfma_f32_16x16x32_bf16 v[156:159], v[120:123], v[200:203], 0\n\t"
            "ds_read_b128 v[200:203], v235 offset:6272\n\t"
            "v_mfma_f32_16x16x32_bf16 v[156:159], v[124:127], v[204:207], v[156:159]\n\t"
            "ds_read_b128 v[204:207], v232 offset:6400\n\t"
            "v_mfma_f32_16x16x32_bf16 v[144:147], v[8:11], v[208:211], v[144:147]\n\t"
            "v_mfma_f32_16x16x32_bf16 v[144:147], v[12:15], v[212:215], v[144:147]\n\t"
            "v_mfma_f32_16x16x32_bf16 v[148:151], v[32:35], v[208:211], v[148:151]\n\t"
            "v_mfma_f32_16x16x32_bf16 v[148:151], v[36:39], v[212:215], v[148:151]\n\t"
            "v_mfma_f32_16x16x32_bf16 v[168:171], v[56:59], v[208:211], v[168:171]\n\t"
            "v_mfma_f32_16x16x32_bf16 v[168:171], v[60:63], v[212:215], v[168:171]\n\t"
            "v_mfma_f32_16x16x32_bf16 v[172:175], v[80:83], v[208:211], v[172:175]\n\t"
            "v_mfma_f32_16x16x32_bf16 v[172:175], v[84:87], v[212:215], v[172:175]\n\t"
            "v_mfma_f32_16x16x32_bf16 v[152:155], v[104:107], v[208:211], v[152:155]\n\t"
            "v_mfma_f32_16x16x32_bf16 v[152:155], v[108:111], v[212:215], v[152:155]\n\t"
            "v_mfma_f32_16x16x32_bf16 v[156:159], v[128:131], v[208:211], v[156:159]\n\t"
            "ds_read_b128 v[208:211], v235 offset:6400\n\t"
            "v_mfma_f32_16x16x32_bf16 v[156:159], v[132:135], v[212:215], v[156:159]\n\t"
            "ds_read_b128 v[212:215], v236 offset:6144\n\t"
            "v_mfma_f32_16x16x32_bf16 v[144:147], v[16:19], v[216:219], v[144:147]\n\t"
            "v_mfma_f32_16x16x32_bf16 v[144:147], v[20:23], v[220:223], v[144:147]\n\t"
            "v_mfma_f32_16x16x32_bf16 v[148:151], v[40:43], v[216:219], v[148:151]\n\t"
            "v_mfma_f32_16x16x32_bf16 v[148:151], v[44:47], v[220:223], v[148:151]\n\t"
            "v_mfma_f32_16x16x32_bf16 v[168:171], v[64:67], v[216:219], v[168:171]\n\t"
            "v_mfma_f32_16x16x32_bf16 v[168:171], v[68:71], v[220:223], v[168:171]\n\t"
            "v_mfma_f32_16x16x32_bf16 v[172:175], v[88:91], v[216:219], v[172:175]\n\t"
            "v_mfma_f32_16x16x32_bf16 v[172:175], v[92:95], v[220:223], v[172:175]\n\t"
            "s_nop 1\n\t"
            "v_mul_f32 v144, v230, v144\n\t"
            "v_mfma_f32_16x16x32_bf16 v[152:155], v[112:115], v[216:219], v[152:155]\n\t"
            "v_mul_f32 v145, v230, v145\n\t"
            "v_mul_f32 v146, v230, v146\n\t"
            "v_mfma_f32_16x16x32_bf16 v[152:155], v[116:119], v[220:223], v[152:155]\n\t"
            "v_mul_f32 v148, v230, v148\n\t"
            "v_mul_f32 v149, v230, v149\n\t"
            "v_mfma_f32_16x16x32_bf16 v[156:159], v[136:139], v[216:219], v[156:159]\n\t"
            "ds_read_b128 v[216:219], v237 offset:6144\n\t"
            "v_mul_f32 v168, v230, v168\n\t"
            "v_mfma_f32_16x16x32_bf16 v[156:159], v[140:143], v[220:223], v[156:159]\n\t"
            "ds_read_b128 v[220:223], v248 offset:6144\n\t"
            "v_mul_f32 v169, v230, v169\n\t"
            "s_waitcnt lgkmcnt(7)\n\t"
            "v_mfma_f32_16x16x32_bf16 v[160:163], v[0:3], v[224:227], 0\n\t"
            "v_mul_f32 v172, v230, v172\n\t"
            "v_mul_f32 v173, v230, v173\n\t"
            "v_mfma_f32_16x16x32_bf16 v[160:163], v[4:7], v[192:195], v[160:163]\n\t"
            "v_mul_f32 v152, v230, v152\n\t"
            "v_mul_f32 v153, v230, v153\n\t"
            "v_mfma_f32_16x16x32_bf16 v[164:167], v[24:27], v[224:227], 0\n\t"
            "v_mul_f32 v156, v230, v156\n\t"
            "v_mul_f32 v157, v230, v157\n\t"
            "v_mfma_f32_16x16x32_bf16 v[164:167], v[28:31], v[192:195], v[164:167]\n\t"
            "v_mul_f32 v147, v230, v147\n\t"
            "v_cvt_pk_bf16_f32 v144, v144, v145\n\t"
            "v_mfma_f32_16x16x32_bf16 v[176:179], v[48:51], v[224:227], 0\n\t"
            "v_mul_f32 v150, v230, v150\n\t"
            "v_mul_f32 v151, v230, v151\n\t"
            "v_mfma_f32_16x16x32_bf16 v[176:179], v[52:55], v[192:195], v[176:179]\n\t"
            "v_cvt_pk_bf16_f32 v148, v148, v149\n\t"
            "v_mul_f32 v170, v230, v170\n\t"
            "v_mfma_f32_16x16x32_bf16 v[180:183], v[72:75], v[224:227], 0\n\t"
            "v_mul_f32 v171, v230, v171\n\t"
            "v_cvt_pk_bf16_f32 v168, v168, v169\n\t"
            "v_mfma_f32_16x16x32_bf16 v[180:183], v[76:79], v[192:195], v[180:183]\n\t"
            "v_mul_f32 v174, v230, v174\n\t"
            "v_mul_f32 v175, v230, v175\n\t"
            "v_mfma_f32_16x16x32_bf16 v[184:187], v[96:99], v[224:227], 0\n\t"
            "v_cvt_pk_bf16_f32 v172, v172, v173\n\t"
            "v_mul_f32 v154, v230, v154\n\t"
            "v_mfma_f32_16x16x32_bf16 v[184:187], v[100:103], v[192:195], v[184:187]\n\t"
            "v_mul_f32 v155, v230, v155\n\t"
            "v_cvt_pk_bf16_f32 v152, v152, v153\n\t"
            "v_mfma_f32_16x16x32_bf16 v[188:191], v[120:123], v[224:227], 0\n\t"
            "ds_read_b128 v[224:227], v236 offset:12288\n\t"
            "v_mul_f32 v158, v230, v158\n\t"
            "v_mfma_f32_16x16x32_bf16 v[188:191], v[124:127], v[192:195], v[188:191]\n\t"
            "v_mul_f32 v159, v230, v159\n\t"
            "v_cvt_pk_bf16_f32 v156, v156, v157\n\t"
            "s_waitcnt lgkmcnt(6)\n\t"
            "v_mfma_f32_16x16x32_bf16 v[160:163], v[8:11], v[196:199], v[160:163]\n\t"
            "v_cvt_pk_bf16_f32 v145, v146, v147\n\t"
            "v_cvt_pk_bf16_f32 v149, v150, v151\n\t"
            "v_mfma_f32_16x16x32_bf16 v[160:163], v[12:15], v[200:203], v[160:163]\n\t"
            "v_cvt_pk_bf16_f32 v169, v170, v171\n\t"
            "v_cvt_pk_bf16_f32 v173, v174, v175\n\t"
            "v_mfma_f32_16x16x32_bf16 v[164:167], v[32:35], v[196:199], v[164:167]\n\t"
            "v_cvt_pk_bf16_f32 v153, v154, v155\n\t"
            "v_cvt_pk_bf16_f32 v157, v158, v159\n\t"
            "v_mfma_f32_16x16x32_bf16 v[164:167], v[36:39], v[200:203], v[164:167]\n\t"
            "s_mov_b64 exec, -1\n\t"
            "ds_write_b64 v234, v[144:145]\n\t"
            "ds_write_b64 v234, v[148:149] offset:128\n\t"
            "ds_write_b64 v234, v[168:169] offset:256\n\t"
            "ds_write_b64 v228, v[172:173]\n\t"
            "ds_write_b64 v228, v[152:153] offset:128\n\t"
            "ds_write_b64 v228, v[156:157] offset:256\n\t"
            "s_mov_b64 exec, -1\n\t"
            "v_mfma_f32_16x16x32_bf16 v[176:179], v[56:59], v[196:199], v[176:179]\n\t"
            "v_mfma_f32_16x16x32_bf16 v[176:179], v[60:63], v[200:203], v[176:179]\n\t"
            "s_add_u32 s49, s33, 0\n\t"
            "buffer_load_dwordx4 v[0:3], v247, s[28:31], s49 offen sc1\n\t"
            "v_mfma_f32_16x16x32_bf16 v[180:183], v[80:83], v[196:199], v[180:183]\n\t"
            "v_mfma_f32_16x16x32_bf16 v[180:183], v[84:87], v[200:203], v[180:183]\n\t"
            "s_add_u32 s49, s33, 6144\n\t"
            "buffer_load_dwordx4 v[24:27], v247, s[28:31], s49 offen sc1\n\t"
            "v_mfma_f32_16x16x32_bf16 v[184:187], v[104:107], v[196:199], v[184:187]\n\t"
            "v_mfma_f32_16x16x32_bf16 v[184:187], v[108:111], v[200:203], v[184:187]\n\t"
            "s_add_u32 s49, s33, 12288\n\t"
            "buffer_load_dwordx4 v[48:51], v247, s[28:31], s49 offen sc1\n\t"
            "v_mfma_f32_16x16x32_bf16 v[188:191], v[128:131], v[196:199], v[188:191]\n\t"
            "v_mfma_f32_16x16x32_bf16 v[188:191], v[132:135], v[200:203], v[188:191]\n\t"
            "ds_read_b128 v[200:203], v236\n\t"
            "s_add_u32 s49, s33, 18432\n\t"
            "buffer_load_dwordx4 v[72:75], v247, s[28:31], s49 offen sc1\n\t"
            "s_waitcnt lgkmcnt(11)\n\t"
            "v_mfma_f32_16x16x32_bf16 v[160:163], v[16:19], v[204:207], v[160:163]\n\t"
            "v_mfma_f32_16x16x32_bf16 v[160:163], v[20:23], v[208:211], v[160:163]\n\t"
            "s_add_u32 s49, s33, 24576\n\t"
            "buffer_load_dwordx4 v[96:99], v247, s[28:31], s49 offen sc1\n\t"
            "v_mfma_f32_16x16x32_bf16 v[164:167], v[40:43], v[204:207], v[164:167]\n\t"
            "v_mfma_f32_16x16x32_bf16 v[164:167], v[44:47], v[208:211], v[164:167]\n\t"
            "s_add_u32 s49, s33, 30720\n\t"
            "buffer_load_dwordx4 v[120:123], v247, s[28:31], s49 offen sc1\n\t"
            "v_mfma_f32_16x16x32_bf16 v[176:179], v[64:67], v[204:207], v[176:179]\n\t"
            "s_nop 0\n\t"
            "v_mul_f32 v160, v231, v160\n\t"
            "v_mul_f32 v161, v231, v161\n\t"
            "v_mfma_f32_16x16x32_bf16 v[176:179], v[68:71], v[208:211], v[176:179]\n\t"
            "v_mul_f32 v162, v231, v162\n\t"
            "v_mul_f32 v163, v231, v163\n\t"
            "v_mfma_f32_16x16x32_bf16 v[180:183], v[88:91], v[204:207], v[180:183]\n\t"
            "v_mul_f32 v164, v231, v164\n\t"
            "v_mul_f32 v165, v231, v165\n\t"
            "v_mfma_f32_16x16x32_bf16 v[180:183], v[92:95], v[208:211], v[180:183]\n\t"
            "v_cvt_pk_bf16_f32 v160, v160, v161\n\t"
            "v_mul_f32 v166, v231, v166\n\t"
            "v_mfma_f32_16x16x32_bf16 v[184:187], v[112:115], v[204:207], v[184:187]\n\t"
            "v_mul_f32 v176, v231, v176\n\t"
            "v_mul_f32 v177, v231, v177\n\t"
            "v_mfma_f32_16x16x32_bf16 v[184:187], v[116:119], v[208:211], v[184:187]\n\t"
            "v_mul_f32 v167, v231, v167\n\t"
            "v_cvt_pk_bf16_f32 v164, v164, v165\n\t"
            "v_mfma_f32_16x16x32_bf16 v[188:191], v[136:139], v[204:207], v[188:191]\n\t"
            "ds_read_b128 v[204:207], v237\n\t"
            "v_mul_f32 v180, v231, v180\n\t"
            "v_mfma_f32_16x16x32_bf16 v[188:191], v[140:143], v[208:211], v[188:191]\n\t"
            "ds_read_b128 v[208:211], v248\n\t"
            "v_mul_f32 v181, v231, v181\n\t"
            "v_mul_f32 v184, v231, v184\n\t"
            "v_mul_f32 v185, v231, v185\n\t"
            "v_mul_f32 v178, v231, v178\n\t"
            "v_mul_f32 v179, v231, v179\n\t"
            "v_cvt_pk_bf16_f32 v176, v176, v177\n\t"
            "v_mul_f32 v182, v231, v182\n\t"
            "v_mul_f32 v188, v231, v188\n\t"
            "v_mul_f32 v189, v231, v189\n\t"
            "v_mul_f32 v183, v231, v183\n\t"
            "v_cvt_pk_bf16_f32 v180, v180, v181\n\t"
            "v_mul_f32 v186, v231, v186\n\t"
            "v_mul_f32 v187, v231, v187\n\t"
            "v_cvt_pk_bf16_f32 v184, v184, v185\n\t"
            "v_mul_f32 v190, v231, v190\n\t"
            "v_mul_f32 v191, v231, v191\n\t"
            "v_cvt_pk_bf16_f32 v188, v188, v189\n\t"
            "v_cvt_pk_bf16_f32 v161, v162, v163\n\t"
            "v_cvt_pk_bf16_f32 v165, v166, v167\n\t"
            "v_cvt_pk_bf16_f32 v177, v178, v179\n\t"
            "v_cvt_pk_bf16_f32 v181, v182, v183\n\t"
            "v_cvt_pk_bf16_f32 v185, v186, v187\n\t"
            "v_cvt_pk_bf16_f32 v189, v190, v191\n\t"
            "s_waitcnt lgkmcnt(9)\n\t"
            "s_mov_b64 exec, s[80:81]\n\t"
            "ds_write_b64 v234, v[160:161] offset:6144\n\t"
            "ds_write_b64 v234, v[164:165] offset:6272\n\t"
            "ds_write_b64 v234, v[176:177] offset:6400\n\t"
            "ds_write_b64 v228, v[180:181] offset:6144\n\t"
            "ds_write_b64 v228, v[184:185] offset:6272\n\t"
            "ds_write_b64 v228, v[188:189] offset:6400\n\t"
            "s_mov_b64 exec, -1\n\t"
            "s_waitcnt lgkmcnt(14)\n\t"
            "ds_read_b128 v[228:231], v237 offset:12288\n\t"
            "s_waitcnt lgkmcnt(14)\n\t"
            "ds_read_b128 v[232:235], v248 offset:12288\n\t"
            "s_add_u32 s49, s33, 1024\n\t"
            "buffer_load_dwordx4 v[4:7], v247, s[28:31], s49 offen sc1\n\t"
            "s_add_u32 s49, s33, 7168\n\t"
            "buffer_load_dwordx4 v[28:31], v247, s[28:31], s49 offen sc1\n\t"
            "s_waitcnt lgkmcnt(0)\n\t"
            "s_barrier\n\t"
            "s_setprio 3\n\t"
            "s_add_u32 s49, s33, 13312\n\t"
            "buffer_load_dwordx4 v[52:55], v247, s[28:31], s49 offen sc1\n\t"
            "s_add_u32 s49, s33, 19456\n\t"
            "buffer_load_dwordx4 v[76:79], v247, s[28:31], s49 offen sc1\n\t"
            "s_add_u32 s49, s33, 25600\n\t"
            "buffer_load_dwordx4 v[100:103], v247, s[28:31], s49 offen sc1\n\t"
            "s_add_u32 s49, s33, 31744\n\t"
            "buffer_load_dwordx4 v[124:127], v247, s[28:31], s49 offen sc1\n\t"
            "s_add_u32 s49, s33, 2048\n\t"
            "buffer_load_dwordx4 v[8:11], v247, s[28:31], s49 offen sc1\n\t"
            "s_add_u32 s49, s33, 8192\n\t"
            "buffer_load_dwordx4 v[32:35], v247, s[28:31], s49 offen sc1\n\t"
            "s_add_u32 s49, s33, 14336\n\t"
            "buffer_load_dwordx4 v[56:59], v247, s[28:31], s49 offen sc1\n\t"
            "s_add_u32 s49, s33, 20480\n\t"
            "buffer_load_dwordx4 v[80:83], v247, s[28:31], s49 offen sc1\n\t"
            "s_add_u32 s49, s33, 26624\n\t"
            "buffer_load_dwordx4 v[104:107], v247, s[28:31], s49 offen sc1\n\t"
            "s_add_u32 s49, s33, 32768\n\t"
            "buffer_load_dwordx4 v[128:131], v247, s[28:31], s49 offen sc1\n\t"
            "s_add_u32 s49, s33, 3072\n\t"
            "buffer_load_dwordx4 v[12:15], v247, s[28:31], s49 offen sc1\n\t"
            "s_add_u32 s49, s33, 9216\n\t"
            "buffer_load_dwordx4 v[36:39], v247, s[28:31], s49 offen sc1\n\t"
            "s_add_u32 s49, s33, 15360\n\t"
            "buffer_load_dwordx4 v[60:63], v247, s[28:31], s49 offen sc1\n\t"
            "s_add_u32 s49, s33, 21504\n\t"
            "buffer_load_dwordx4 v[84:87], v247, s[28:31], s49 offen sc1\n\t"
            "s_add_u32 s49, s33, 27648\n\t"
            "buffer_load_dwordx4 v[108:111], v247, s[28:31], s49 offen sc1\n\t"
            "s_add_u32 s49, s33, 33792\n\t"
            "buffer_load_dwordx4 v[132:135], v247, s[28:31], s49 offen sc1\n\t"
            "s_add_u32 s49, s33, 4096\n\t"
            "buffer_load_dwordx4 v[16:19], v247, s[28:31], s49 offen sc1\n\t"
            "s_add_u32 s49, s33, 10240\n\t"
            "buffer_load_dwordx4 v[40:43], v247, s[28:31], s49 offen sc1\n\t"
            "s_add_u32 s49, s33, 16384\n\t"
            "buffer_load_dwordx4 v[64:67], v247, s[28:31], s49 offen sc1\n\t"
            "s_add_u32 s49, s33, 22528\n\t"
            "buffer_load_dwordx4 v[88:91], v247, s[28:31], s49 offen sc1\n\t"
            "s_add_u32 s49, s33, 28672\n\t"
            "buffer_load_dwordx4 v[112:115], v247, s[28:31], s49 offen sc1\n\t"
            "s_add_u32 s49, s33, 34816\n\t"
            "buffer_load_dwordx4 v[136:139], v247, s[28:31], s49 offen sc1\n\t"
            "s_add_u32 s49, s33, 5120\n\t"
            "buffer_load_dwordx4 v[20:23], v247, s[28:31], s49 offen sc1\n\t"
            "s_add_u32 s49, s33, 11264\n\t"
            "buffer_load_dwordx4 v[44:47], v247, s[28:31], s49 offen sc1\n\t"
            "s_add_u32 s49, s33, 17408\n\t"
            "buffer_load_dwordx4 v[68:71], v247, s[28:31], s49 offen sc1\n\t"
            "s_add_u32 s49, s33, 23552\n\t"
            "buffer_load_dwordx4 v[92:95], v247, s[28:31], s49 offen sc1\n\t"
            "s_add_u32 s49, s33, 29696\n\t"
            "buffer_load_dwordx4 v[116:119], v247, s[28:31], s49 offen sc1\n\t"
            "s_add_u32 s49, s33, 35840\n\t"
            "buffer_load_dwordx4 v[140:143], v247, s[28:31], s49 offen sc1\n\t"
            "s_add_u32 s59, s59, s84\n\t"
            "s_movk_i32 s84, 800\n\t"
            "s_add_u32 s45, s46, s27\n\t"
            "s_mov_b32 s46, s47\n\t"
            "s_mov_b32 s47, s68\n\t"
            "s_xor_b32 s48, s48, s71\n\t"
            "s_xor_b32 s67, s67, s71\n\t"
            "s_add_u32 s76, s76, 4\n\t"
            "s_add_u32 s70, s70, 4\n\t"
            "s_lshl_b32 s56, s56, 2\n\t"
            "s_lshr_b32 s49, s26, 30\n\t"
            "s_or_b32 s56, s56, s49\n\t"
            "s_cmp_eq_u32 s82, 0\n\t"
            "s_cbranch_scc1 .Lwk_nr%=\n\t"
            "s_waitcnt lgkmcnt(0)\n\t"
            "s_nop 7\n\t"
            "v_add_u32 v236, 127360, v247\n\t"
            "v_add_u32 v237, 136704, v247\n\t"
            "ds_read_b128 v[144:147], v236\n\t"
            "ds_read_b128 v[148:151], v237\n\t"
            "s_waitcnt lgkmcnt(0)\n\t"
            "s_mov_b32 exec_hi, 0x3ffff\n\t"
            "buffer_store_dwordx4 v[144:147], v247, s[28:31], s82 offen sc1\n\t"
            "s_mov_b64 exec, 0xffffff\n\t"
            "buffer_store_dwordx4 v[148:151], v247, s[28:31], s82 offen offset:800 sc1\n\t"
            "s_mov_b64 exec, -1\n\t"
            "ds_read_b128 v[152:155], v252 offset:2560\n\t"
            "s_waitcnt vmcnt(0) lgkmcnt(0)\n\t"
            "s_mov_b64 exec, 1\n\t"
            "buffer_store_dwordx4 v[152:155], off, s[28:31], s82 offset:1216 sc1\n\t"
            "s_mov_b64 exec, -1\n\t"
            "s_branch .Lwk_n2%=\n\t"
            ".Lwk_nr%=:\n\t"
            "s_waitcnt vmcnt(0)\n\t"
            ".Lwk_n2%=:\n\t"
            "s_nop 1\n\t"
            "s_nop 7\n\t"
            ".p2align 6\n\t"
            "s_nop 0\n\t"
            "s_nop 0\n\t"
            "s_nop 0\n\t"
            "s_nop 0\n\t"
            "s_nop 0\n\t"
            "s_nop 0\n\t"
            "s_nop 0\n\t"
            ".Lwb_loop%=:\n\t"
            "s_setprio 1\n\t"
            "ds_read_b128 v[168:171], v254\n\t"
            "ds_read_b128 v[172:175], v254 offset:64\n\t"
            "s_waitcnt vmcnt(9)\n\t"
            "ds_read_b64 v[144:145], v253\n\t"
            "ds_read_b64 v[146:147], v253 offset:96\n\t"
            "ds_read_b64 v[148:149], v253 offset:1744\n\t"
            "ds_read_b64 v[150:151], v253 offset:1840\n\t"
            "ds_read_b64 v[152:153], v253 offset:8\n\t"
            "ds_read_b64 v[154:155], v253 offset:104\n\t"
            "ds_read_b64 v[156:157], v253 offset:1752\n\t"
            "ds_read_b64 v[158:159], v253 offset:1848\n\t"
            "ds_read_b64 v[160:161], v253 offset:16\n\t"
            "ds_read_b64 v[162:163], v253 offset:112\n\t"
            "ds_read_b64 v[164:165], v253 offset:1760\n\t"
            "ds_read_b64 v[166:167], v253 offset:1856\n\t"
            "v_mov_b64 v[176:177], 0\n\t"
            "v_mov_b64 v[178:179], 0\n\t"
            "v_mov_b64 v[180:181], 0\n\t"
            "v_mov_b64 v[182:183], 0\n\t"
            "v_mov_b32 v249, s76\n\t"
            "v_mov_b64 v[184:185], 0\n\t"
            "v_mov_b64 v[186:187], 0\n\t"
            "v_mov_b64 v[188:189], 0\n\t"
            "v_mov_b64 v[190:191], 0\n\t"
            "v_add_u32 v236, s67, v250\n\t"
            "v_add_u32 v248, s67, v251\n\t"
            "v_mov_b32 v247, 0\n\t"
            "ds_read_b32 v249, v249\n\t"
            "s_setprio 3\n\t"
            "s_waitcnt lgkmcnt(9)\n\t"
            "v_smfmac_f32_16x16x64_bf16 v[176:179], v[168:171], v[144:151], v255\n\t"
            "ds_read_b128 v[168:171], v254 offset:128\n\t"
            "s_waitcnt lgkmcnt(6)\n\t"
            "v_smfmac_f32_16x16x64_bf16 v[180:183], v[172:175], v[152:159], v255\n\t"
            "s_setprio 1\n\t"
            "ds_read_b64 v[144:145], v253 offset:24\n\t"
            "ds_read_b64 v[146:147], v253 offset:120\n\t"
            "ds_read_b64 v[148:149], v253 offset:1768\n\t"
            "ds_read_b64 v[150:151], v253 offset:1864\n\t"
            "ds_read_b128 v[172:175], v254 offset:192\n\t"
            "ds_read_b64 v[152:153], v253 offset:32\n\t"
            "ds_read_b64 v[154:155], v253 offset:128\n\t"
            "ds_read_b64 v[156:157], v253 offset:1776\n\t"
            "ds_read_b64 v[158:159], v253 offset:1872\n\t"
            "v_cvt_pk_bf16_f32 v176, v176, v177\n\t"
            "v_cvt_pk_bf16_f32 v177, v178, v179\n\t"
            "v_cvt_pk_bf16_f32 v178, v180, v181\n\t"
            "v_cvt_pk_bf16_f32 v179, v182, v183\n\t"
            "s_setprio 3\n\t"
            "v_mfma_f32_4x4x4_16b_bf16 v[196:199], v[176:177], v[176:177], 0\n\t"
            "v_mov_b64 v[180:181], 0\n\t"
            "v_mfma_f32_16x16x32_bf16 v[192:195], v[238:241], v[176:179], 0\n\t"
            "s_waitcnt lgkmcnt(10)\n\t"
            "v_readfirstlane_b32 s26, v249\n\t"
            "v_mov_b64 v[182:183], 0\n\t"
            "v_mfma_f32_4x4x4_16b_bf16 v[196:199], v[178:179], v[178:179], v[196:199]\n\t"
            "ds_read_b32 v249, v252 offset:12\n\t"
            "s_and_b32 s68, s26, 0x7ffffff\n\t"
            "s_add_u32 s68, s68, s78\n\t"
            "s_and_b32 s69, s26, 0x38000000\n\t"
            "s_or_b32 s69, s69, s39\n\t"
            "s_bitcmp1_b32 s26, 27\n\t"
            "s_cselect_b32 s79, 2.0, s68\n\t"
            "s_and_b32 s57, s56, 3\n\t"
            "s_lshl_b32 s58, s57, 3\n\t"
            "s_lshr_b32 m0, s54, s58\n\t"
            "s_lshl_b32 s58, s57, 4\n\t"
            "s_lshr_b64 s[36:37], s[50:51], s58\n\t"
            "s_and_b32 s57, s36, 0xffff\n\t"
            "s_add_u32 s57, s57, s67\n\t"
            "v_writelane_b32 v236, s57, 63\n\t"
            "v_writelane_b32 v236, s59, m0\n\t"
            "v_xor_b32 v237, 16, v236\n\t"
            "s_lshr_b64 s[36:37], s[52:53], s58\n\t"
            "s_and_b32 s58, s36, 0xffff\n\t"
            "s_add_u32 s58, s58, s67\n\t"
            "ds_write_b128 v236, v[176:179]\n\t"
            "s_waitcnt lgkmcnt(11)\n\t"
            "v_smfmac_f32_16x16x64_bf16 v[184:187], v[168:171], v[160:167], v255\n\t"
            "s_and_b32 s38, s52, 0xffff\n\t"
            "s_add_u32 s38, s38, s67\n\t"
            "ds_read_b128 v[168:171], v254 offset:256\n\t"
            "s_waitcnt lgkmcnt(7)\n\t"
            "v_smfmac_f32_16x16x64_bf16 v[188:191], v[172:175], v[144:151], v255\n\t"
            "s_setprio 1\n\t"
            "ds_read_b64 v[160:161], v253 offset:40\n\t"
            "ds_read_b64 v[162:163], v253 offset:136\n\t"
            "ds_read_b64 v[164:165], v253 offset:1784\n\t"
            "ds_read_b64 v[166:167], v253 offset:1880\n\t"
            "ds_read_b128 v[172:175], v254 offset:320\n\t"
            "ds_read_b64 v[144:145], v253 offset:48\n\t"
            "ds_read_b64 v[146:147], v253 offset:144\n\t"
            "ds_read_b64 v[148:149], v253 offset:1792\n\t"
            "s_waitcnt lgkmcnt(14)\n\t"
            "ds_read_b64 v[150:151], v253 offset:1888\n\t"
            "v_mov_b64 v[176:177], 0\n\t"
            "v_mov_b64 v[178:179], 0\n\t"
            "v_cvt_pk_bf16_f32 v184, v184, v185\n\t"
            "v_cvt_pk_bf16_f32 v185, v186, v187\n\t"
            "v_cvt_pk_bf16_f32 v186, v188, v189\n\t"
            "v_cvt_pk_bf16_f32 v187, v190, v191\n\t"
            "s_setprio 3\n\t"
            "v_mfma_f32_4x4x4_16b_bf16 v[196:199], v[184:185], v[184:185], v[196:199]\n\t"
            "s_waitcnt lgkmcnt(14)\n\t"
            "ds_write_b128 v237, v[184:187]\n\t"
            "v_mfma_f32_16x16x32_bf16 v[192:195], v[238:241], v[184:187], v[192:195]\n\t"
            "v_mov_b64 v[184:185], 0\n\t"
            "v_mov_b64 v[188:189], 0\n\t"
            "v_mfma_f32_4x4x4_16b_bf16 v[196:199], v[186:187], v[186:187], v[196:199]\n\t"
            "v_mov_b64 v[186:187], 0\n\t"
            "v_mov_b64 v[190:191], 0\n\t"
            "v_writelane_b32 v248, s58, 63\n\t"
            "v_writelane_b32 v248, s38, m0\n\t"
            "s_waitcnt lgkmcnt(12)\n\t"
            "v_and_b32 v249, s69, v249\n\t"
            "s_waitcnt lgkmcnt(10)\n\t"
            "v_smfmac_f32_16x16x64_bf16 v[176:179], v[168:171], v[152:159], v255\n\t"
            "ds_read_b128 v[168:171], v254 offset:384\n\t"
            "ds_read_b64 v[152:153], v253 offset:56\n\t"
            "ds_read_b64 v[154:155], v253 offset:152\n\t"
            "ds_read_b64 v[156:157], v253 offset:1800\n\t"
            "ds_read_b64 v[158:159], v253 offset:1896\n\t"
            "s_waitcnt lgkmcnt(10)\n\t"
            "v_smfmac_f32_16x16x64_bf16 v[180:183], v[172:175], v[160:167], v255\n\t"
            "s_setprio 1\n\t"
            "ds_read_b128 v[172:175], v254 offset:448\n\t"
            "ds_read_b64 v[160:161], v253 offset:64\n\t"
            "ds_read_b64 v[162:163], v253 offset:160\n\t"
            "ds_read_b64 v[164:165], v253 offset:1808\n\t"
            "ds_read_b64 v[166:167], v253 offset:1904\n\t"
            "v_cvt_pk_bf16_f32 v176, v176, v177\n\t"
            "v_cvt_pk_bf16_f32 v177, v178, v179\n\t"
            "v_cvt_pk_bf16_f32 v178, v180, v181\n\t"
            "v_cvt_pk_bf16_f32 v179, v182, v183\n\t"
            "s_setprio 3\n\t"
            "v_mfma_f32_4x4x4_16b_bf16 v[196:199], v[176:177], v[176:177], v[196:199]\n\t"
            "s_waitcnt lgkmcnt(14)\n\t"
            "ds_write_b128 v236, v[176:179] offset:128\n\t"
            "v_mfma_f32_16x16x32_bf16 v[192:195], v[238:241], v[176:179], v[192:195]\n\t"
            "v_mov_b64 v[176:177], 0\n\t"
            "v_mov_b64 v[180:181], 0\n\t"
            "v_mfma_f32_4x4x4_16b_bf16 v[196:199], v[178:179], v[178:179], v[196:199]\n\t"
            "v_mov_b64 v[178:179], 0\n\t"
            "v_mov_b64 v[182:183], 0\n\t"
            "s_waitcnt lgkmcnt(10)\n\t"
            "v_smfmac_f32_16x16x64_bf16 v[184:187], v[168:171], v[144:151], v255\n\t"
            "ds_read_b128 v[168:171], v254 offset:512\n\t"
            "ds_read_b64 v[144:145], v253 offset:72\n\t"
            "ds_read_b64 v[146:147], v253 offset:168\n\t"
            "ds_read_b64 v[148:149], v253 offset:1816\n\t"
            "ds_read_b64 v[150:151], v253 offset:1912\n\t"
            "s_waitcnt lgkmcnt(10)\n\t"
            "v_smfmac_f32_16x16x64_bf16 v[188:191], v[172:175], v[152:159], v255\n\t"
            "s_setprio 1\n\t"
            "ds_read_b128 v[172:175], v254 offset:576\n\t"
            "ds_read_b64 v[152:153], v253 offset:80\n\t"
            "ds_read_b64 v[154:155], v253 offset:176\n\t"
            "ds_read_b64 v[156:157], v253 offset:1824\n\t"
            "ds_read_b64 v[158:159], v253 offset:1920\n\t"
            "v_cvt_pk_bf16_f32 v184, v184, v185\n\t"
            "v_cvt_pk_bf16_f32 v185, v186, v187\n\t"
            "v_cvt_pk_bf16_f32 v186, v188, v189\n\t"
            "v_cvt_pk_bf16_f32 v187, v190, v191\n\t"
            "s_setprio 3\n\t"
            "v_mfma_f32_4x4x4_16b_bf16 v[196:199], v[184:185], v[184:185], v[196:199]\n\t"
            "s_waitcnt lgkmcnt(14)\n\t"
            "ds_write_b128 v237, v[184:187] offset:128\n\t"
            "v_mfma_f32_16x16x32_bf16 v[192:195], v[238:241], v[184:187], v[192:195]\n\t"
            "v_mov_b64 v[184:185], 0\n\t"
            "v_mov_b64 v[188:189], 0\n\t"
            "v_mfma_f32_4x4x4_16b_bf16 v[196:199], v[186:187], v[186:187], v[196:199]\n\t"
            "v_mov_b64 v[186:187], 0\n\t"
            "v_mov_b64 v[190:191], 0\n\t"
            "s_waitcnt lgkmcnt(10)\n\t"
            "v_smfmac_f32_16x16x64_bf16 v[176:179], v[168:171], v[160:167], v255\n\t"
            "ds_read_b128 v[168:171], v254 offset:640\n\t"
            "ds_read_b64 v[160:161], v253 offset:88\n\t"
            "ds_read_b64 v[162:163], v253 offset:184\n\t"
            "ds_read_b64 v[164:165], v253 offset:1832\n\t"
            "ds_read_b64 v[166:167], v253 offset:1928\n\t"
            "s_waitcnt lgkmcnt(10)\n\t"
            "v_smfmac_f32_16x16x64_bf16 v[180:183], v[172:175], v[144:151], v255\n\t"
            "s_setprio 1\n\t"
            "ds_read_b128 v[172:175], v254 offset:704\n\t"
            "v_lshlrev_b32 v144, 16, v212\n\t"
            "v_cvt_pk_bf16_f32 v176, v176, v177\n\t"
            "v_cvt_pk_bf16_f32 v177, v178, v179\n\t"
            "v_and_b32 v145, 0xffff0000, v212\n\t"
            "v_lshlrev_b32 v146, 16, v213\n\t"
            "s_setprio 3\n\t"
            "v_mfma_f32_4x4x4_16b_bf16 v[196:199], v[176:177], v[176:177], v[196:199]\n\t"
            "v_and_b32 v147, 0xffff0000, v213\n\t"
            "v_cvt_pk_bf16_f32 v178, v180, v181\n\t"
            "v_cvt_pk_bf16_f32 v179, v182, v183\n\t"
            "v_lshlrev_b32 v180, 16, v210\n\t"
            "v_and_b32 v181, 0xffff0000, v210\n\t"
            "v_mfma_f32_4x4x4_16b_bf16 v[196:199], v[178:179], v[178:179], v[196:199]\n\t"
            "ds_write_b128 v236, v[176:179] offset:256\n\t"
            "v_mfma_f32_16x16x32_bf16 v[192:195], v[238:241], v[176:179], v[192:195]\n\t"
            "s_setprio 1\n\t"
            "v_lshlrev_b32 v176, 16, v208\n\t"
            "v_and_b32 v177, 0xffff0000, v208\n\t"
            "v_lshlrev_b32 v178, 16, v209\n\t"
            "v_and_b32 v179, 0xffff0000, v209\n\t"
            "v_lshlrev_b32 v182, 16, v211\n\t"
            "v_and_b32 v183, 0xffff0000, v211\n\t"
            "v_lshlrev_b32 v148, 16, v214\n\t"
            "v_and_b32 v149, 0xffff0000, v214\n\t"
            "v_lshlrev_b32 v150, 16, v215\n\t"
            "v_and_b32 v151, 0xffff0000, v215\n\t"
            "v_add_u32 v236, s48, v244\n\t"
            "ds_read_b128 v[212:215], v236 offset:6144\n\t"
            "s_setprio 3\n\t"
            "s_waitcnt lgkmcnt(7)\n\t"
            "v_smfmac_f32_16x16x64_bf16 v[184:187], v[168:171], v[152:159], v255\n\t"
            "v_lshlrev_b32 v152, 16, v200\n\t"
            "v_and_b32 v153, 0xffff0000, v200\n\t"
            "v_lshlrev_b32 v154, 16, v201\n\t"
            "v_and_b32 v155, 0xffff0000, v201\n\t"
            "v_lshlrev_b32 v156, 16, v202\n\t"
            "s_waitcnt lgkmcnt(2)\n\t"
            "v_smfmac_f32_16x16x64_bf16 v[188:191], v[172:175], v[160:167], v255\n\t"
            "s_setprio 1\n\t"
            "s_mov_b32 exec_hi, 0x3fffff\n\t"
            "s_add_u32 m0, s66, 0\n\t"
            "s_add_u32 s49, s79, 0\n\t"
            "buffer_load_dwordx4 v249, s[72:75], s49 offen lds\n\t"
            "s_mov_b32 exec_hi, -1\n\t"
            "s_mov_b32 exec_hi, 0x3fffff\n\t"
            "s_add_u32 m0, s66, 872\n\t"
            "s_add_u32 s49, s68, 196224\n\t"
            "buffer_load_dwordx4 v249, s[72:75], s49 offen lds\n\t"
            "s_mov_b32 exec_hi, -1\n\t"
            "s_mov_b32 exec_hi, 0x3fffff\n\t"
            "s_add_u32 m0, s66, 1744\n\t"
            "s_add_u32 s49, s68, 392448\n\t"
            "buffer_load_dwordx4 v249, s[72:75], s49 offen lds\n\t"
            "s_mov_b32 exec_hi, -1\n\t"
            "s_mov_b32 exec_hi, 0x3fffff\n\t"
            "s_add_u32 m0, s66, 2616\n\t"
            "s_add_u32 s49, s68, 588672\n\t"
            "buffer_load_dwordx4 v249, s[72:75], s49 offen lds\n\t"
            "s_mov_b32 exec_hi, -1\n\t"
            "ds_read_b32 v247, v247 offset:7856\n\t"
            "s_mov_b32 exec_hi, 0x3fffff\n\t"
            "s_add_u32 m0, s66, 3488\n\t"
            "s_add_u32 s49, s68, 784896\n\t"
            "buffer_load_dwordx4 v249, s[72:75], s49 offen lds\n\t"
            "s_mov_b32 exec_hi, -1\n\t"
            "s_mov_b32 exec_hi, 0x3fffff\n\t"
            "s_add_u32 m0, s66, 4360\n\t"
            "s_add_u32 s49, s68, 981120\n\t"
            "buffer_load_dwordx4 v249, s[72:75], s49 offen lds\n\t"
            "s_mov_b32 exec_hi, -1\n\t"
            "s_mov_b32 exec_hi, 0x3fffff\n\t"
            "s_add_u32 m0, s66, 5232\n\t"
            "s_add_u32 s49, s68, 1177344\n\t"
            "buffer_load_dwordx4 v249, s[72:75], s49 offen lds\n\t"
            "s_mov_b32 exec_hi, -1\n\t"
            "s_mov_b32 exec_hi, 0x3fffff\n\t"
            "s_add_u32 m0, s66, 6104\n\t"
            "s_add_u32 s49, s68, 1373568\n\t"
            "buffer_load_dwordx4 v249, s[72:75], s49 offen lds\n\t"
            "s_mov_b32 exec_hi, -1\n\t"
            "s_mov_b32 exec_hi, 0x3fffff\n\t"
            "s_add_u32 m0, s66, 6976\n\t"
            "s_add_u32 s49, s68, 1569792\n\t"
            "buffer_load_dwordx4 v249, s[72:75], s49 offen lds\n\t"
            "s_mov_b32 exec_hi, -1\n\t"
            "v_cvt_pk_bf16_f32 v184, v184, v185\n\t"
            "v_cvt_pk_bf16_f32 v185, v186, v187\n\t"
            "v_cvt_pk_bf16_f32 v186, v188, v189\n\t"
            "v_cvt_pk_bf16_f32 v187, v190, v191\n\t"
            "s_setprio 3\n\t"
            "v_mfma_f32_4x4x4_16b_bf16 v[196:199], v[184:185], v[184:185], v[196:199]\n\t"
            "ds_write_b128 v237, v[184:187] offset:256\n\t"
            "v_mfma_f32_16x16x32_bf16 v[192:195], v[238:241], v[184:187], v[192:195]\n\t"
            "s_waitcnt lgkmcnt(1)\n\t"
            "v_readfirstlane_b32 s77, v247\n\t"
            "s_cmp_ge_u32 s77, s70\n\t"
            "s_cbranch_scc0 .Lwb_spin%=\n\t"
            ".Lwb_go%=:\n\t"
            "v_mfma_f32_4x4x4_16b_bf16 v[196:199], v[186:187], v[186:187], v[196:199]\n\t"
            "s_setprio 1\n\t"
            "ds_read_b128 v[184:187], v242\n\t"
            "ds_read_b128 v[188:191], v243\n\t"
            "ds_read_b128 v[172:175], v243 offset:128\n\t"
            "v_and_b32 v157, 0xffff0000, v202\n\t"
            "v_lshlrev_b32 v158, 16, v203\n\t"
            "v_and_b32 v159, 0xffff0000, v203\n\t"
            "v_lshlrev_b32 v160, 16, v204\n\t"
            "v_and_b32 v161, 0xffff0000, v204\n\t"
            "v_lshlrev_b32 v162, 16, v205\n\t"
            "v_cndmask_b32_e64 v193, v196, v197, s[60:61]\n\t"
            "v_cndmask_b32_e64 v193, v193, v198, s[62:63]\n\t"
            "v_cndmask_b32_e64 v193, v193, v199, s[64:65]\n\t"
            "v_and_b32 v163, 0xffff0000, v205\n\t"
            "v_lshlrev_b32 v164, 16, v206\n\t"
            "v_and_b32 v165, 0xffff0000, v206\n\t"
            "v_lshlrev_b32 v166, 16, v207\n\t"
            "v_and_b32 v167, 0xffff0000, v207\n\t"
            "v_lshlrev_b32 v168, 16, v216\n\t"
            "v_and_b32 v169, 0xffff0000, v216\n\t"
            "v_lshlrev_b32 v170, 16, v217\n\t"
            "v_and_b32 v171, 0xffff0000, v217\n\t"
            "s_bfe_u32 s57, s56, 0x20004\n\t"
            "s_lshl_b32 s58, s57, 3\n\t"
            "s_lshr_b32 s58, s55, s58\n\t"
            "s_lshl_b64 s[36:37], s[34:35], s58\n\t"
            "ds_read_b128 v[196:199], v252\n\t"
            "ds_read_b128 v[200:203], v236\n\t"
            "ds_write_b64 v248, v[192:193] offset:18816\n\t"
            "ds_read_b128 v[192:195], v242 offset:128\n\t"
            "s_bitcmp1_b32 s57, 1\n\t"
            "s_cselect_b32 s58, 1177344, 0\n\t"
            "s_bitcmp1_b32 s57, 0\n\t"
            "s_cselect_b32 s57, 2304, 0\n\t"
            "s_add_u32 s57, s57, s58\n\t"
            "s_mov_b64 exec, 1\n\t"
            "ds_inc_u32 v255, v238 offset:7860\n\t"
            "s_mov_b64 exec, -1\n\t"
            "v_add_u32 v237, s48, v245\n\t"
            "v_add_u32 v248, s48, v246\n\t"
            "s_add_u32 s59, s59, s84\n\t"
            "s_movk_i32 s84, 800\n\t"
            "ds_read_b128 v[204:207], v237\n\t"
            "s_setprio 3\n\t"
            "s_waitcnt lgkmcnt(7)\n\t"
            "v_mfma_f32_16x16x32_bf16 v[152:155], v[0:3], v[184:187], v[152:155]\n\t"
            "s_setprio 1\n\t"
            "s_setprio 1\n\t"
            "ds_read_b128 v[208:211], v248\n\t"
            "v_mfma_f32_16x16x32_bf16 v[152:155], v[4:7], v[188:191], v[152:155]\n\t"
            "v_mfma_f32_16x16x32_bf16 v[156:159], v[28:31], v[188:191], v[156:159]\n\t"
            "v_mfma_f32_16x16x32_bf16 v[156:159], v[24:27], v[184:187], v[156:159]\n\t"
            "v_mfma_f32_16x16x32_bf16 v[160:163], v[48:51], v[184:187], v[160:163]\n\t"
            "v_mfma_f32_16x16x32_bf16 v[160:163], v[52:55], v[188:191], v[160:163]\n\t"
            "v_mfma_f32_16x16x32_bf16 v[164:167], v[76:79], v[188:191], v[164:167]\n\t"
            "s_waitcnt lgkmcnt(6)\n\t"
            "s_mov_b64 exec, s[36:37]\n\t"
            "v_add_u32 v196, s57, v196\n\t"
            "s_mov_b64 exec, -1\n\t"
            "v_mfma_f32_16x16x32_bf16 v[164:167], v[72:75], v[184:187], v[164:167]\n\t"
            "v_mfma_f32_16x16x32_bf16 v[176:179], v[96:99], v[184:187], v[176:179]\n\t"
            "v_mfma_f32_16x16x32_bf16 v[176:179], v[100:103], v[188:191], v[176:179]\n\t"
            "v_mfma_f32_16x16x32_bf16 v[180:183], v[124:127], v[188:191], v[180:183]\n\t"
            "ds_read_b128 v[188:191], v243 offset:256\n\t"
            "v_mfma_f32_16x16x32_bf16 v[180:183], v[120:123], v[184:187], v[180:183]\n\t"
            "ds_read_b128 v[184:187], v242 offset:256\n\t"
            "s_waitcnt lgkmcnt(5)\n\t"
            "v_mfma_f32_16x16x32_bf16 v[152:155], v[8:11], v[192:195], v[152:155]\n\t"
            "v_mfma_f32_16x16x32_bf16 v[152:155], v[12:15], v[172:175], v[152:155]\n\t"
            "v_mfma_f32_16x16x32_bf16 v[156:159], v[36:39], v[172:175], v[156:159]\n\t"
            "v_mfma_f32_16x16x32_bf16 v[156:159], v[32:35], v[192:195], v[156:159]\n\t"
            "v_mfma_f32_16x16x32_bf16 v[160:163], v[56:59], v[192:195], v[160:163]\n\t"
            "v_mfma_f32_16x16x32_bf16 v[160:163], v[60:63], v[172:175], v[160:163]\n\t"
            "v_mfma_f32_16x16x32_bf16 v[164:167], v[84:87], v[172:175], v[164:167]\n\t"
            "v_mfma_f32_16x16x32_bf16 v[164:167], v[80:83], v[192:195], v[164:167]\n\t"
            "v_mfma_f32_16x16x32_bf16 v[176:179], v[104:107], v[192:195], v[176:179]\n\t"
            "v_mfma_f32_16x16x32_bf16 v[176:179], v[108:111], v[172:175], v[176:179]\n\t"
            "v_mfma_f32_16x16x32_bf16 v[180:183], v[132:135], v[172:175], v[180:183]\n\t"
            "ds_read_b128 v[172:175], v243 offset:6144\n\t"
            "v_mfma_f32_16x16x32_bf16 v[180:183], v[128:131], v[192:195], v[180:183]\n\t"
            "ds_read_b128 v[192:195], v242 offset:6144\n\t"
            "s_waitcnt lgkmcnt(2)\n\t"
            "v_mfma_f32_16x16x32_bf16 v[152:155], v[16:19], v[184:187], v[152:155]\n\t"
            "v_mfma_f32_16x16x32_bf16 v[152:155], v[20:23], v[188:191], v[152:155]\n\t"
            "v_mfma_f32_16x16x32_bf16 v[156:159], v[44:47], v[188:191], v[156:159]\n\t"
            "v_mfma_f32_16x16x32_bf16 v[156:159], v[40:43], v[184:187], v[156:159]\n\t"
            "v_mfma_f32_16x16x32_bf16 v[160:163], v[64:67], v[184:187], v[160:163]\n\t"
            "v_mfma_f32_16x16x32_bf16 v[160:163], v[68:71], v[188:191], v[160:163]\n\t"
            "v_mfma_f32_16x16x32_bf16 v[164:167], v[92:95], v[188:191], v[164:167]\n\t"
            "v_mfma_f32_16x16x32_bf16 v[164:167], v[88:91], v[184:187], v[164:167]\n\t"
            "s_nop 1\n\t"
            "v_cvt_pk_bf16_f32 v152, v152, v153\n\t"
            "v_mfma_f32_16x16x32_bf16 v[176:179], v[112:115], v[184:187], v[176:179]\n\t"
            "v_cvt_pk_bf16_f32 v153, v154, v155\n\t"
            "v_mfma_f32_16x16x32_bf16 v[176:179], v[116:119], v[188:191], v[176:179]\n\t"
            "v_cvt_pk_bf16_f32 v154, v156, v157\n\t"
            "v_cvt_pk_bf16_f32 v155, v158, v159\n\t"
            "v_mfma_f32_16x16x32_bf16 v[180:183], v[140:143], v[188:191], v[180:183]\n\t"
            "ds_read_b128 v[188:191], v243 offset:6272\n\t"
            "v_cvt_pk_bf16_f32 v160, v160, v161\n\t"
            "v_mfma_f32_16x16x32_bf16 v[180:183], v[136:139], v[184:187], v[180:183]\n\t"
            "ds_read_b128 v[184:187], v242 offset:6272\n\t"
            "v_cvt_pk_bf16_f32 v161, v162, v163\n\t"
            "s_waitcnt lgkmcnt(2)\n\t"
            "v_mfma_f32_16x16x32_bf16 v[144:147], v[0:3], v[192:195], v[144:147]\n\t"
            "v_cvt_pk_bf16_f32 v176, v176, v177\n\t"
            "v_cvt_pk_bf16_f32 v177, v178, v179\n\t"
            "v_mfma_f32_16x16x32_bf16 v[144:147], v[4:7], v[172:175], v[144:147]\n\t"
            "v_cvt_pk_bf16_f32 v162, v164, v165\n\t"
            "v_cvt_pk_bf16_f32 v163, v166, v167\n\t"
            "v_mfma_f32_16x16x32_bf16 v[148:151], v[28:31], v[172:175], v[148:151]\n\t"
            "v_cvt_pk_bf16_f32 v178, v180, v181\n\t"
            "v_cvt_pk_bf16_f32 v179, v182, v183\n\t"
            "v_mfma_f32_16x16x32_bf16 v[148:151], v[24:27], v[192:195], v[148:151]\n\t"
            "v_lshlrev_b32 v156, 16, v218\n\t"
            "v_and_b32 v157, 0xffff0000, v218\n\t"
            "v_mfma_f32_16x16x32_bf16 v[168:171], v[48:51], v[192:195], v[168:171]\n\t"
            "v_lshlrev_b32 v158, 16, v219\n\t"
            "v_and_b32 v159, 0xffff0000, v219\n\t"
            "v_mfma_f32_16x16x32_bf16 v[168:171], v[52:55], v[172:175], v[168:171]\n\t"
            "ds_read_b128 v[216:219], v237 offset:6144\n\t"
            "v_lshlrev_b32 v164, 16, v220\n\t"
            "v_mfma_f32_16x16x32_bf16 v[156:159], v[76:79], v[172:175], v[156:159]\n\t"
            "v_and_b32 v165, 0xffff0000, v220\n\t"
            "v_lshlrev_b32 v166, 16, v221\n\t"
            "v_mfma_f32_16x16x32_bf16 v[156:159], v[72:75], v[192:195], v[156:159]\n\t"
            "v_and_b32 v167, 0xffff0000, v221\n\t"
            "v_lshlrev_b32 v180, 16, v222\n\t"
            "v_and_b32 v181, 0xffff0000, v222\n\t"
            "v_mfma_f32_16x16x32_bf16 v[164:167], v[96:99], v[192:195], v[164:167]\n\t"
            "v_lshlrev_b32 v182, 16, v223\n\t"
            "v_and_b32 v183, 0xffff0000, v223\n\t"
            "v_mfma_f32_16x16x32_bf16 v[164:167], v[100:103], v[172:175], v[164:167]\n\t"
            "ds_read_b128 v[220:223], v248 offset:6144\n\t"
            "buffer_store_dwordx4 v[152:155], v196, s[40:43], s45 offen sc1\n\t"
            "v_mfma_f32_16x16x32_bf16 v[180:183], v[124:127], v[172:175], v[180:183]\n\t"
            "ds_read_b128 v[172:175], v243 offset:6400\n\t"
            "v_lshlrev_b32 v152, 16, v224\n\t"
            "v_mfma_f32_16x16x32_bf16 v[180:183], v[120:123], v[192:195], v[180:183]\n\t"
            "ds_read_b128 v[192:195], v242 offset:6400\n\t"
            "buffer_store_dwordx4 v[160:163], v196, s[40:43], s45 offen offset:64 sc1\n\t"
            "s_waitcnt lgkmcnt(4)\n\t"
            "v_mfma_f32_16x16x32_bf16 v[144:147], v[8:11], v[184:187], v[144:147]\n\t"
            "v_and_b32 v153, 0xffff0000, v224\n\t"
            "v_lshlrev_b32 v154, 16, v225\n\t"
            "v_mfma_f32_16x16x32_bf16 v[144:147], v[12:15], v[188:191], v[144:147]\n\t"
            "buffer_store_dwordx4 v[176:179], v196, s[40:43], s45 offen offset:128 sc1\n\t"
            "v_mfma_f32_16x16x32_bf16 v[148:151], v[36:39], v[188:191], v[148:151]\n\t"
            "v_and_b32 v155, 0xffff0000, v225\n\t"
            "v_lshlrev_b32 v160, 16, v226\n\t"
            "v_mfma_f32_16x16x32_bf16 v[148:151], v[32:35], v[184:187], v[148:151]\n\t"
            "v_and_b32 v161, 0xffff0000, v226\n\t"
            "v_lshlrev_b32 v162, 16, v227\n\t"
            "v_mfma_f32_16x16x32_bf16 v[168:171], v[56:59], v[184:187], v[168:171]\n\t"
            "v_and_b32 v163, 0xffff0000, v227\n\t"
            "v_lshlrev_b32 v176, 16, v228\n\t"
            "v_mfma_f32_16x16x32_bf16 v[168:171], v[60:63], v[188:191], v[168:171]\n\t"
            "ds_read_b128 v[224:227], v236 offset:12288\n\t"
            "v_and_b32 v177, 0xffff0000, v228\n\t"
            "v_mfma_f32_16x16x32_bf16 v[156:159], v[84:87], v[188:191], v[156:159]\n\t"
            "v_lshlrev_b32 v178, 16, v229\n\t"
            "v_and_b32 v179, 0xffff0000, v229\n\t"
            "v_mfma_f32_16x16x32_bf16 v[156:159], v[80:83], v[184:187], v[156:159]\n\t"
            "v_mfma_f32_16x16x32_bf16 v[164:167], v[104:107], v[184:187], v[164:167]\n\t"
            "v_mfma_f32_16x16x32_bf16 v[164:167], v[108:111], v[188:191], v[164:167]\n\t"
            "v_mfma_f32_16x16x32_bf16 v[180:183], v[132:135], v[188:191], v[180:183]\n\t"
            "ds_read_b128 v[188:191], v243 offset:12288\n\t"
            "v_mfma_f32_16x16x32_bf16 v[180:183], v[128:131], v[184:187], v[180:183]\n\t"
            "ds_read_b128 v[184:187], v242 offset:12288\n\t"
            "s_waitcnt lgkmcnt(3)\n\t"
            "v_mfma_f32_16x16x32_bf16 v[144:147], v[16:19], v[192:195], v[144:147]\n\t"
            "v_mfma_f32_16x16x32_bf16 v[144:147], v[20:23], v[172:175], v[144:147]\n\t"
            "v_mfma_f32_16x16x32_bf16 v[168:171], v[64:67], v[192:195], v[168:171]\n\t"
            "v_mfma_f32_16x16x32_bf16 v[168:171], v[68:71], v[172:175], v[168:171]\n\t"
            "v_mfma_f32_16x16x32_bf16 v[164:167], v[112:115], v[192:195], v[164:167]\n\t"
            "v_mfma_f32_16x16x32_bf16 v[164:167], v[116:119], v[172:175], v[164:167]\n\t"
            "v_mfma_f32_16x16x32_bf16 v[148:151], v[44:47], v[172:175], v[148:151]\n\t"
            "v_mfma_f32_16x16x32_bf16 v[148:151], v[40:43], v[192:195], v[148:151]\n\t"
            "s_nop 1\n\t"
            "v_cvt_pk_bf16_f32 v144, v144, v145\n\t"
            "v_mfma_f32_16x16x32_bf16 v[156:159], v[92:95], v[172:175], v[156:159]\n\t"
            "v_cvt_pk_bf16_f32 v145, v146, v147\n\t"
            "v_mfma_f32_16x16x32_bf16 v[156:159], v[88:91], v[192:195], v[156:159]\n\t"
            "v_cvt_pk_bf16_f32 v168, v168, v169\n\t"
            "v_cvt_pk_bf16_f32 v169, v170, v171\n\t"
            "v_mfma_f32_16x16x32_bf16 v[180:183], v[140:143], v[172:175], v[180:183]\n\t"
            "ds_read_b128 v[172:175], v243 offset:12416\n\t"
            "v_cvt_pk_bf16_f32 v164, v164, v165\n\t"
            "v_mfma_f32_16x16x32_bf16 v[180:183], v[136:139], v[192:195], v[180:183]\n\t"
            "ds_read_b128 v[192:195], v242 offset:12416\n\t"
            "v_cvt_pk_bf16_f32 v165, v166, v167\n\t"
            "s_waitcnt lgkmcnt(2)\n\t"
            "v_mfma_f32_16x16x32_bf16 v[152:155], v[0:3], v[184:187], v[152:155]\n\t"
            "v_cvt_pk_bf16_f32 v146, v148, v149\n\t"
            "v_cvt_pk_bf16_f32 v147, v150, v151\n\t"
            "v_mfma_f32_16x16x32_bf16 v[152:155], v[4:7], v[188:191], v[152:155]\n\t"
            "v_cvt_pk_bf16_f32 v170, v156, v157\n\t"
            "v_cvt_pk_bf16_f32 v171, v158, v159\n\t"
            "v_mfma_f32_16x16x32_bf16 v[160:163], v[28:31], v[188:191], v[160:163]\n\t"
            "v_cvt_pk_bf16_f32 v166, v180, v181\n\t"
            "v_cvt_pk_bf16_f32 v167, v182, v183\n\t"
            "v_mfma_f32_16x16x32_bf16 v[160:163], v[24:27], v[184:187], v[160:163]\n\t"
            "v_lshlrev_b32 v148, 16, v230\n\t"
            "v_and_b32 v149, 0xffff0000, v230\n\t"
            "v_mfma_f32_16x16x32_bf16 v[176:179], v[48:51], v[184:187], v[176:179]\n\t"
            "v_lshlrev_b32 v150, 16, v231\n\t"
            "v_and_b32 v151, 0xffff0000, v231\n\t"
            "v_mfma_f32_16x16x32_bf16 v[176:179], v[52:55], v[188:191], v[176:179]\n\t"
            "ds_read_b128 v[228:231], v237 offset:12288\n\t"
            "v_lshlrev_b32 v156, 16, v232\n\t"
            "v_mfma_f32_16x16x32_bf16 v[148:151], v[76:79], v[188:191], v[148:151]\n\t"
            "v_and_b32 v157, 0xffff0000, v232\n\t"
            "v_lshlrev_b32 v158, 16, v233\n\t"
            "v_mfma_f32_16x16x32_bf16 v[148:151], v[72:75], v[184:187], v[148:151]\n\t"
            "v_and_b32 v159, 0xffff0000, v233\n\t"
            "v_lshlrev_b32 v180, 16, v234\n\t"
            "v_and_b32 v181, 0xffff0000, v234\n\t"
            "v_mfma_f32_16x16x32_bf16 v[156:159], v[96:99], v[184:187], v[156:159]\n\t"
            "v_lshlrev_b32 v182, 16, v235\n\t"
            "v_and_b32 v183, 0xffff0000, v235\n\t"
            "v_mfma_f32_16x16x32_bf16 v[156:159], v[100:103], v[188:191], v[156:159]\n\t"
            "ds_read_b128 v[232:235], v248 offset:12288\n\t"
            "buffer_store_dwordx4 v[144:147], v197, s[40:43], s45 offen sc1\n\t"
            "v_mfma_f32_16x16x32_bf16 v[180:183], v[124:127], v[188:191], v[180:183]\n\t"
            "ds_read_b128 v[188:191], v243 offset:12544\n\t"
            "v_mfma_f32_16x16x32_bf16 v[180:183], v[120:123], v[184:187], v[180:183]\n\t"
            "ds_read_b128 v[184:187], v242 offset:12544\n\t"
            "buffer_store_dwordx4 v[168:171], v197, s[40:43], s45 offen offset:64 sc1\n\t"
            "s_waitcnt lgkmcnt(4)\n\t"
            "v_mfma_f32_16x16x32_bf16 v[152:155], v[8:11], v[192:195], v[152:155]\n\t"
            "v_mfma_f32_16x16x32_bf16 v[152:155], v[12:15], v[172:175], v[152:155]\n\t"
            "buffer_store_dwordx4 v[164:167], v197, s[40:43], s45 offen offset:128 sc1\n\t"
            "v_mfma_f32_16x16x32_bf16 v[160:163], v[36:39], v[172:175], v[160:163]\n\t"
            "v_mfma_f32_16x16x32_bf16 v[160:163], v[32:35], v[192:195], v[160:163]\n\t"
            "s_waitcnt lgkmcnt(0)\n\t"
            "s_barrier\n\t"
            "s_setprio 3\n\t"
            "v_mfma_f32_16x16x32_bf16 v[176:179], v[56:59], v[192:195], v[176:179]\n\t"
            "v_mfma_f32_16x16x32_bf16 v[176:179], v[60:63], v[172:175], v[176:179]\n\t"
            "v_mfma_f32_16x16x32_bf16 v[148:151], v[84:87], v[172:175], v[148:151]\n\t"
            "v_mfma_f32_16x16x32_bf16 v[148:151], v[80:83], v[192:195], v[148:151]\n\t"
            "v_mfma_f32_16x16x32_bf16 v[156:159], v[104:107], v[192:195], v[156:159]\n\t"
            "v_mfma_f32_16x16x32_bf16 v[156:159], v[108:111], v[172:175], v[156:159]\n\t"
            "v_mfma_f32_16x16x32_bf16 v[152:155], v[16:19], v[184:187], v[152:155]\n\t"
            "v_mfma_f32_16x16x32_bf16 v[152:155], v[20:23], v[188:191], v[152:155]\n\t"
            "v_mfma_f32_16x16x32_bf16 v[160:163], v[44:47], v[188:191], v[160:163]\n\t"
            "v_mfma_f32_16x16x32_bf16 v[160:163], v[40:43], v[184:187], v[160:163]\n\t"
            "v_mfma_f32_16x16x32_bf16 v[180:183], v[132:135], v[172:175], v[180:183]\n\t"
            "v_mfma_f32_16x16x32_bf16 v[180:183], v[128:131], v[192:195], v[180:183]\n\t"
            "v_mfma_f32_16x16x32_bf16 v[176:179], v[64:67], v[184:187], v[176:179]\n\t"
            "v_mfma_f32_16x16x32_bf16 v[176:179], v[68:71], v[188:191], v[176:179]\n\t"
            "s_nop 1\n\t"
            "v_cvt_pk_bf16_f32 v152, v152, v153\n\t"
            "v_mfma_f32_16x16x32_bf16 v[148:151], v[92:95], v[188:191], v[148:151]\n\t"
            "v_cvt_pk_bf16_f32 v153, v154, v155\n\t"
            "v_mfma_f32_16x16x32_bf16 v[148:151], v[88:91], v[184:187], v[148:151]\n\t"
            "v_cvt_pk_bf16_f32 v154, v160, v161\n\t"
            "v_cvt_pk_bf16_f32 v155, v162, v163\n\t"
            "v_mfma_f32_16x16x32_bf16 v[156:159], v[112:115], v[184:187], v[156:159]\n\t"
            "v_cvt_pk_bf16_f32 v176, v176, v177\n\t"
            "v_cvt_pk_bf16_f32 v177, v178, v179\n\t"
            "v_mfma_f32_16x16x32_bf16 v[156:159], v[116:119], v[188:191], v[156:159]\n\t"
            "buffer_store_dwordx4 v[152:155], v198, s[40:43], s45 offen sc1\n\t"
            "v_mfma_f32_16x16x32_bf16 v[180:183], v[140:143], v[188:191], v[180:183]\n\t"
            "v_cvt_pk_bf16_f32 v178, v148, v149\n\t"
            "v_cvt_pk_bf16_f32 v179, v150, v151\n\t"
            "v_mfma_f32_16x16x32_bf16 v[180:183], v[136:139], v[184:187], v[180:183]\n\t"
            "buffer_store_dwordx4 v[176:179], v198, s[40:43], s45 offen offset:64 sc1\n\t"
            "s_nop 1\n\t"
            "v_cvt_pk_bf16_f32 v156, v156, v157\n\t"
            "v_cvt_pk_bf16_f32 v157, v158, v159\n\t"
            "s_nop 2\n\t"
            "v_cvt_pk_bf16_f32 v158, v180, v181\n\t"
            "v_cvt_pk_bf16_f32 v159, v182, v183\n\t"
            "buffer_store_dwordx4 v[156:159], v198, s[40:43], s45 offen offset:128 sc1\n\t"
            "s_add_u32 s45, s46, s27\n\t"
            "s_mov_b32 s46, s47\n\t"
            "s_mov_b32 s47, s68\n\t"
            "s_xor_b32 s48, s48, s71\n\t"
            "s_xor_b32 s67, s67, s71\n\t"
            "s_add_u32 s76, s76, 4\n\t"
            "s_add_u32 s70, s70, 4\n\t"
            "s_lshl_b32 s56, s56, 2\n\t"
            "s_lshr_b32 s49, s26, 30\n\t"
            "s_or_b32 s56, s56, s49\n\t"
            "s_cmp_lt_u32 s46, 0x7000000\n\t"
            "s_cbranch_scc1 .Lwb_loop%=\n\t"
            "s_branch .Lwb_end%=\n\t"
            ".Lwb_spin%=:\n\t"
            "s_cmp_eq_u32 s83, 0\n\t"
            "s_cbranch_scc1 .Lwb_go%=\n\t"
            "s_sub_u32 s83, s83, 1\n\t"
            "s_sleep 2\n\t"
            "v_mov_b32 v247, 0\n\t"
            "ds_read_b32 v247, v247 offset:7856\n\t"
            "s_waitcnt lgkmcnt(0)\n\t"
            "v_readfirstlane_b32 s77, v247\n\t"
            "s_cmp_ge_u32 s77, s70\n\t"
            "s_cbranch_scc0 .Lwb_spin%=\n\t"
            "s_branch .Lwb_go%=\n\t"
            ".Lwb_end%=:\n\t"
            "s_waitcnt lgkmcnt(0)\n\t"
            "s_nop 7\n\t"
            "ds_read_b128 v[196:199], v252\n\t"
            "s_bfe_u32 s57, s56, 0x20004\n\t"
            "s_lshl_b32 s58, s57, 3\n\t"
            "s_lshr_b32 s58, s55, s58\n\t"
            "s_lshl_b64 s[36:37], s[34:35], s58\n\t"
            "s_bitcmp1_b32 s57, 1\n\t"
            "s_cselect_b32 s58, 1177344, 0\n\t"
            "s_bitcmp1_b32 s57, 0\n\t"
            "s_cselect_b32 s57, 2304, 0\n\t"
            "s_add_u32 s57, s57, s58\n\t"
            "v_lshlrev_b32 v152, 16, v200\n\t"
            "v_and_b32 v153, 0xffff0000, v200\n\t"
            "v_lshlrev_b32 v154, 16, v201\n\t"
            "v_and_b32 v155, 0xffff0000, v201\n\t"
            "v_lshlrev_b32 v156, 16, v202\n\t"
            "v_and_b32 v157, 0xffff0000, v202\n\t"
            "v_lshlrev_b32 v158, 16, v203\n\t"
            "v_and_b32 v159, 0xffff0000, v203\n\t"
            "v_lshlrev_b32 v160, 16, v204\n\t"
            "v_and_b32 v161, 0xffff0000, v204\n\t"
            "v_lshlrev_b32 v162, 16, v205\n\t"
            "v_and_b32 v163, 0xffff0000, v205\n\t"
            "v_lshlrev_b32 v164, 16, v206\n\t"
            "v_and_b32 v165, 0xffff0000, v206\n\t"
            "v_lshlrev_b32 v166, 16, v207\n\t"
            "v_and_b32 v167, 0xffff0000, v207\n\t"
            "v_lshlrev_b32 v176, 16, v208\n\t"
            "v_and_b32 v177, 0xffff0000, v208\n\t"
            "v_lshlrev_b32 v178, 16, v209\n\t"
            "v_and_b32 v179, 0xffff0000, v209\n\t"
            "v_lshlrev_b32 v180, 16, v210\n\t"
            "v_and_b32 v181, 0xffff0000, v210\n\t"
            "v_lshlrev_b32 v182, 16, v211\n\t"
            "v_and_b32 v183, 0xffff0000, v211\n\t"
            "v_lshlrev_b32 v144, 16, v212\n\t"
            "v_and_b32 v145, 0xffff0000, v212\n\t"
            "v_lshlrev_b32 v146, 16, v213\n\t"
            "v_and_b32 v147, 0xffff0000, v213\n\t"
            "v_lshlrev_b32 v148, 16, v214\n\t"
            "v_and_b32 v149, 0xffff0000, v214\n\t"
            "v_lshlrev_b32 v150, 16, v215\n\t"
            "v_and_b32 v151, 0xffff0000, v215\n\t"
            "v_lshlrev_b32 v168, 16, v216\n\t"
            "v_and_b32 v169, 0xffff0000, v216\n\t"
            "v_lshlrev_b32 v170, 16, v217\n\t"
            "s_waitcnt lgkmcnt(0)\n\t"
            "s_mov_b64 exec, s[36:37]\n\t"
            "v_add_u32 v196, s57, v196\n\t"
            "s_mov_b64 exec, -1\n\t"
            ".Lwk_t1%=:\n\t"
            "v_mov_b32 v247, 0\n\t"
            "ds_read_b32 v247, v247 offset:7856\n\t"
            "s_waitcnt lgkmcnt(0)\n\t"
            "v_readfirstlane_b32 s77, v247\n\t"
            "s_cmp_ge_u32 s77, s70\n\t"
            "s_cbranch_scc1 .Lwk_t2%=\n\t"
            "s_cmp_eq_u32 s83, 0\n\t"
            "s_cbranch_scc1 .Lwk_t2%=\n\t"
            "s_sub_u32 s83, s83, 1\n\t"
            "s_sleep 1\n\t"
            "s_branch .Lwk_t1%=\n\t"
            ".Lwk_t2%=:\n\t"
            "v_and_b32 v171, 0xffff0000, v217\n\t"
            "ds_read_b128 v[184:187], v242\n\t"
            "ds_read_b128 v[188:191], v243\n\t"
            "ds_read_b128 v[192:195], v242 offset:128\n\t"
            "ds_read_b128 v[172:175], v243 offset:128\n\t"
            "s_waitcnt lgkmcnt(2)\n\t"
            "v_mfma_f32_16x16x32_bf16 v[152:155], v[0:3], v[184:187], v[152:155]\n\t"
            "v_mfma_f32_16x16x32_bf16 v[152:155], v[4:7], v[188:191], v[152:155]\n\t"
            "v_mfma_f32_16x16x32_bf16 v[156:159], v[28:31], v[188:191], v[156:159]\n\t"
            "v_mfma_f32_16x16x32_bf16 v[156:159], v[24:27], v[184:187], v[156:159]\n\t"
            "v_mfma_f32_16x16x32_bf16 v[160:163], v[48:51], v[184:187], v[160:163]\n\t"
            "v_mfma_f32_16x16x32_bf16 v[160:163], v[52:55], v[188:191], v[160:163]\n\t"
            "v_mfma_f32_16x16x32_bf16 v[164:167], v[76:79], v[188:191], v[164:167]\n\t"
            "v_mfma_f32_16x16x32_bf16 v[164:167], v[72:75], v[184:187], v[164:167]\n\t"
            "v_mfma_f32_16x16x32_bf16 v[176:179], v[96:99], v[184:187], v[176:179]\n\t"
            "v_mfma_f32_16x16x32_bf16 v[176:179], v[100:103], v[188:191], v[176:179]\n\t"
            "v_mfma_f32_16x16x32_bf16 v[180:183], v[124:127], v[188:191], v[180:183]\n\t"
            "ds_read_b128 v[188:191], v243 offset:256\n\t"
            "v_mfma_f32_16x16x32_bf16 v[180:183], v[120:123], v[184:187], v[180:183]\n\t"
            "ds_read_b128 v[184:187], v242 offset:256\n\t"
            "s_waitcnt lgkmcnt(2)\n\t"
            "v_mfma_f32_16x16x32_bf16 v[152:155], v[8:11], v[192:195], v[152:155]\n\t"
            "v_mfma_f32_16x16x32_bf16 v[152:155], v[12:15], v[172:175], v[152:155]\n\t"
            "v_mfma_f32_16x16x32_bf16 v[156:159], v[36:39], v[172:175], v[156:159]\n\t"
            "v_mfma_f32_16x16x32_bf16 v[156:159], v[32:35], v[192:195], v[156:159]\n\t"
            "v_mfma_f32_16x16x32_bf16 v[160:163], v[56:59], v[192:195], v[160:163]\n\t"
            "v_mfma_f32_16x16x32_bf16 v[160:163], v[60:63], v[172:175], v[160:163]\n\t"
            "v_mfma_f32_16x16x32_bf16 v[164:167], v[84:87], v[172:175], v[164:167]\n\t"
            "v_mfma_f32_16x16x32_bf16 v[164:167], v[80:83], v[192:195], v[164:167]\n\t"
            "v_mfma_f32_16x16x32_bf16 v[176:179], v[104:107], v[192:195], v[176:179]\n\t"
            "v_mfma_f32_16x16x32_bf16 v[176:179], v[108:111], v[172:175], v[176:179]\n\t"
            "v_mfma_f32_16x16x32_bf16 v[180:183], v[132:135], v[172:175], v[180:183]\n\t"
            "ds_read_b128 v[172:175], v243 offset:6144\n\t"
            "v_mfma_f32_16x16x32_bf16 v[180:183], v[128:131], v[192:195], v[180:183]\n\t"
            "ds_read_b128 v[192:195], v242 offset:6144\n\t"
            "s_waitcnt lgkmcnt(2)\n\t"
            "v_mfma_f32_16x16x32_bf16 v[152:155], v[16:19], v[184:187], v[152:155]\n\t"
            "v_mfma_f32_16x16x32_bf16 v[152:155], v[20:23], v[188:191], v[152:155]\n\t"
            "v_mfma_f32_16x16x32_bf16 v[156:159], v[44:47], v[188:191], v[156:159]\n\t"
            "v_mfma_f32_16x16x32_bf16 v[156:159], v[40:43], v[184:187], v[156:159]\n\t"
            "v_mfma_f32_16x16x32_bf16 v[160:163], v[64:67], v[184:187], v[160:163]\n\t"
            "v_mfma_f32_16x16x32_bf16 v[160:163], v[68:71], v[188:191], v[160:163]\n\t"
            "v_mfma_f32_16x16x32_bf16 v[164:167], v[92:95], v[188:191], v[164:167]\n\t"
            "v_mfma_f32_16x16x32_bf16 v[164:167], v[88:91], v[184:187], v[164:167]\n\t"
            "s_nop 1\n\t"
            "v_cvt_pk_bf16_f32 v152, v152, v153\n\t"
            "v_mfma_f32_16x16x32_bf16 v[176:179], v[112:115], v[184:187], v[176:179]\n\t"
            "v_cvt_pk_bf16_f32 v153, v154, v155\n\t"
            "v_mfma_f32_16x16x32_bf16 v[176:179], v[116:119], v[188:191], v[176:179]\n\t"
            "v_cvt_pk_bf16_f32 v154, v156, v157\n\t"
            "v_cvt_pk_bf16_f32 v155, v158, v159\n\t"
            "v_mfma_f32_16x16x32_bf16 v[180:183], v[140:143], v[188:191], v[180:183]\n\t"
            "ds_read_b128 v[188:191], v243 offset:6272\n\t"
            "v_cvt_pk_bf16_f32 v160, v160, v161\n\t"
            "v_mfma_f32_16x16x32_bf16 v[180:183], v[136:139], v[184:187], v[180:183]\n\t"
            "ds_read_b128 v[184:187], v242 offset:6272\n\t"
            "v_cvt_pk_bf16_f32 v161, v162, v163\n\t"
            "s_waitcnt lgkmcnt(2)\n\t"
            "v_mfma_f32_16x16x32_bf16 v[144:147], v[0:3], v[192:195], v[144:147]\n\t"
            "v_cvt_pk_bf16_f32 v176, v176, v177\n\t"
            "v_cvt_pk_bf16_f32 v177, v178, v179\n\t"
            "v_mfma_f32_16x16x32_bf16 v[144:147], v[4:7], v[172:175], v[144:147]\n\t"
            "v_cvt_pk_bf16_f32 v162, v164, v165\n\t"
            "v_cvt_pk_bf16_f32 v163, v166, v167\n\t"
            "v_mfma_f32_16x16x32_bf16 v[148:151], v[28:31], v[172:175], v[148:151]\n\t"
            "v_cvt_pk_bf16_f32 v178, v180, v181\n\t"
            "v_cvt_pk_bf16_f32 v179, v182, v183\n\t"
            "v_mfma_f32_16x16x32_bf16 v[148:151], v[24:27], v[192:195], v[148:151]\n\t"
            "v_lshlrev_b32 v156, 16, v218\n\t"
            "v_and_b32 v157, 0xffff0000, v218\n\t"
            "v_mfma_f32_16x16x32_bf16 v[168:171], v[48:51], v[192:195], v[168:171]\n\t"
            "v_lshlrev_b32 v158, 16, v219\n\t"
            "v_and_b32 v159, 0xffff0000, v219\n\t"
            "v_mfma_f32_16x16x32_bf16 v[168:171], v[52:55], v[172:175], v[168:171]\n\t"
            "v_lshlrev_b32 v164, 16, v220\n\t"
            "v_and_b32 v165, 0xffff0000, v220\n\t"
            "v_mfma_f32_16x16x32_bf16 v[156:159], v[76:79], v[172:175], v[156:159]\n\t"
            "v_lshlrev_b32 v166, 16, v221\n\t"
            "v_and_b32 v167, 0xffff0000, v221\n\t"
            "v_mfma_f32_16x16x32_bf16 v[156:159], v[72:75], v[192:195], v[156:159]\n\t"
            "v_lshlrev_b32 v180, 16, v222\n\t"
            "v_and_b32 v181, 0xffff0000, v222\n\t"
            "v_mfma_f32_16x16x32_bf16 v[164:167], v[96:99], v[192:195], v[164:167]\n\t"
            "v_lshlrev_b32 v182, 16, v223\n\t"
            "v_and_b32 v183, 0xffff0000, v223\n\t"
            "v_mfma_f32_16x16x32_bf16 v[164:167], v[100:103], v[172:175], v[164:167]\n\t"
            "buffer_store_dwordx4 v[152:155], v196, s[40:43], s45 offen sc1\n\t"
            "v_mfma_f32_16x16x32_bf16 v[180:183], v[124:127], v[172:175], v[180:183]\n\t"
            "ds_read_b128 v[172:175], v243 offset:6400\n\t"
            "v_lshlrev_b32 v152, 16, v224\n\t"
            "v_mfma_f32_16x16x32_bf16 v[180:183], v[120:123], v[192:195], v[180:183]\n\t"
            "ds_read_b128 v[192:195], v242 offset:6400\n\t"
            "buffer_store_dwordx4 v[160:163], v196, s[40:43], s45 offen offset:64 sc1\n\t"
            "s_waitcnt lgkmcnt(2)\n\t"
            "v_mfma_f32_16x16x32_bf16 v[144:147], v[8:11], v[184:187], v[144:147]\n\t"
            "v_and_b32 v153, 0xffff0000, v224\n\t"
            "v_lshlrev_b32 v154, 16, v225\n\t"
            "v_mfma_f32_16x16x32_bf16 v[144:147], v[12:15], v[188:191], v[144:147]\n\t"
            "buffer_store_dwordx4 v[176:179], v196, s[40:43], s45 offen offset:128 sc1\n\t"
            "v_mfma_f32_16x16x32_bf16 v[148:151], v[36:39], v[188:191], v[148:151]\n\t"
            "v_and_b32 v155, 0xffff0000, v225\n\t"
            "v_lshlrev_b32 v160, 16, v226\n\t"
            "v_mfma_f32_16x16x32_bf16 v[148:151], v[32:35], v[184:187], v[148:151]\n\t"
            "v_and_b32 v161, 0xffff0000, v226\n\t"
            "v_lshlrev_b32 v162, 16, v227\n\t"
            "v_mfma_f32_16x16x32_bf16 v[168:171], v[56:59], v[184:187], v[168:171]\n\t"
            "v_and_b32 v163, 0xffff0000, v227\n\t"
            "v_lshlrev_b32 v176, 16, v228\n\t"
            "v_mfma_f32_16x16x32_bf16 v[168:171], v[60:63], v[188:191], v[168:171]\n\t"
            "v_and_b32 v177, 0xffff0000, v228\n\t"
            "v_lshlrev_b32 v178, 16, v229\n\t"
            "v_mfma_f32_16x16x32_bf16 v[156:159], v[84:87], v[188:191], v[156:159]\n\t"
            "v_and_b32 v179, 0xffff0000, v229\n\t"
            "v_mfma_f32_16x16x32_bf16 v[156:159], v[80:83], v[184:187], v[156:159]\n\t"
            "v_mfma_f32_16x16x32_bf16 v[164:167], v[104:107], v[184:187], v[164:167]\n\t"
            "v_mfma_f32_16x16x32_bf16 v[164:167], v[108:111], v[188:191], v[164:167]\n\t"
            "v_mfma_f32_16x16x32_bf16 v[180:183], v[132:135], v[188:191], v[180:183]\n\t"
            "ds_read_b128 v[188:191], v243 offset:12288\n\t"
            "v_mfma_f32_16x16x32_bf16 v[180:183], v[128:131], v[184:187], v[180:183]\n\t"
            "ds_read_b128 v[184:187], v242 offset:12288\n\t"
            "s_waitcnt lgkmcnt(2)\n\t"
            "v_mfma_f32_16x16x32_bf16 v[144:147], v[16:19], v[192:195], v[144:147]\n\t"
            "v_mfma_f32_16x16x32_bf16 v[144:147], v[20:23], v[172:175], v[144:147]\n\t"
            "v_mfma_f32_16x16x32_bf16 v[168:171], v[64:67], v[192:195], v[168:171]\n\t"
            "v_mfma_f32_16x16x32_bf16 v[168:171], v[68:71], v[172:175], v[168:171]\n\t"
            "v_mfma_f32_16x16x32_bf16 v[164:167], v[112:115], v[192:195], v[164:167]\n\t"
            "v_mfma_f32_16x16x32_bf16 v[164:167], v[116:119], v[172:175], v[164:167]\n\t"
            "v_mfma_f32_16x16x32_bf16 v[148:151], v[44:47], v[172:175], v[148:151]\n\t"
            "v_mfma_f32_16x16x32_bf16 v[148:151], v[40:43], v[192:195], v[148:151]\n\t"
            "s_nop 1\n\t"
            "v_cvt_pk_bf16_f32 v144, v144, v145\n\t"
            "v_mfma_f32_16x16x32_bf16 v[156:159], v[92:95], v[172:175], v[156:159]\n\t"
            "v_cvt_pk_bf16_f32 v145, v146, v147\n\t"
            "v_mfma_f32_16x16x32_bf16 v[156:159], v[88:91], v[192:195], v[156:159]\n\t"
            "v_cvt_pk_bf16_f32 v168, v168, v169\n\t"
            "v_cvt_pk_bf16_f32 v169, v170, v171\n\t"
            "v_mfma_f32_16x16x32_bf16 v[180:183], v[140:143], v[172:175], v[180:183]\n\t"
            "ds_read_b128 v[172:175], v243 offset:12416\n\t"
            "v_cvt_pk_bf16_f32 v164, v164, v165\n\t"
            "v_mfma_f32_16x16x32_bf16 v[180:183], v[136:139], v[192:195], v[180:183]\n\t"
            "ds_read_b128 v[192:195], v242 offset:12416\n\t"
            "v_cvt_pk_bf16_f32 v165, v166, v167\n\t"
            "s_waitcnt lgkmcnt(2)\n\t"
            "v_mfma_f32_16x16x32_bf16 v[152:155], v[0:3], v[184:187], v[152:155]\n\t"
            "v_cvt_pk_bf16_f32 v146, v148, v149\n\t"
            "v_cvt_pk_bf16_f32 v147, v150, v151\n\t"
            "v_mfma_f32_16x16x32_bf16 v[152:155], v[4:7], v[188:191], v[152:155]\n\t"
            "v_cvt_pk_bf16_f32 v170, v156, v157\n\t"
            "v_cvt_pk_bf16_f32 v171, v158, v159\n\t"
            "v_mfma_f32_16x16x32_bf16 v[160:163], v[28:31], v[188:191], v[160:163]\n\t"
            "v_cvt_pk_bf16_f32 v166, v180, v181\n\t"
            "v_cvt_pk_bf16_f32 v167, v182, v183\n\t"
            "v_mfma_f32_16x16x32_bf16 v[160:163], v[24:27], v[184:187], v[160:163]\n\t"
            "v_lshlrev_b32 v148, 16, v230\n\t"
            "v_and_b32 v149, 0xffff0000, v230\n\t"
            "v_mfma_f32_16x16x32_bf16 v[176:179], v[48:51], v[184:187], v[176:179]\n\t"
            "v_lshlrev_b32 v150, 16, v231\n\t"
            "v_and_b32 v151, 0xffff0000, v231\n\t"
            "v_mfma_f32_16x16x32_bf16 v[176:179], v[52:55], v[188:191], v[176:179]\n\t"
            "v_lshlrev_b32 v156, 16, v232\n\t"
            "v_and_b32 v157, 0xffff0000, v232\n\t"
            "v_mfma_f32_16x16x32_bf16 v[148:151], v[76:79], v[188:191], v[148:151]\n\t"
            "v_lshlrev_b32 v158, 16, v233\n\t"
            "v_and_b32 v159, 0xffff0000, v233\n\t"
            "v_mfma_f32_16x16x32_bf16 v[148:151], v[72:75], v[184:187], v[148:151]\n\t"
            "v_lshlrev_b32 v180, 16, v234\n\t"
            "v_and_b32 v181, 0xffff0000, v234\n\t"
            "v_mfma_f32_16x16x32_bf16 v[156:159], v[96:99], v[184:187], v[156:159]\n\t"
            "v_lshlrev_b32 v182, 16, v235\n\t"
            "v_and_b32 v183, 0xffff0000, v235\n\t"
            "v_mfma_f32_16x16x32_bf16 v[156:159], v[100:103], v[188:191], v[156:159]\n\t"
            "buffer_store_dwordx4 v[144:147], v197, s[40:43], s45 offen sc1\n\t"
            "v_mfma_f32_16x16x32_bf16 v[180:183], v[124:127], v[188:191], v[180:183]\n\t"
            "ds_read_b128 v[188:191], v243 offset:12544\n\t"
            "v_mfma_f32_16x16x32_bf16 v[180:183], v[120:123], v[184:187], v[180:183]\n\t"
            "ds_read_b128 v[184:187], v242 offset:12544\n\t"
            "buffer_store_dwordx4 v[168:171], v197, s[40:43], s45 offen offset:64 sc1\n\t"
            "s_waitcnt lgkmcnt(2)\n\t"
            "v_mfma_f32_16x16x32_bf16 v[152:155], v[8:11], v[192:195], v[152:155]\n\t"
            "v_mfma_f32_16x16x32_bf16 v[152:155], v[12:15], v[172:175], v[152:155]\n\t"
            "buffer_store_dwordx4 v[164:167], v197, s[40:43], s45 offen offset:128 sc1\n\t"
            "v_mfma_f32_16x16x32_bf16 v[160:163], v[36:39], v[172:175], v[160:163]\n\t"
            "v_mfma_f32_16x16x32_bf16 v[160:163], v[32:35], v[192:195], v[160:163]\n\t"
            "v_mfma_f32_16x16x32_bf16 v[176:179], v[56:59], v[192:195], v[176:179]\n\t"
            "v_mfma_f32_16x16x32_bf16 v[176:179], v[60:63], v[172:175], v[176:179]\n\t"
            "v_mfma_f32_16x16x32_bf16 v[148:151], v[84:87], v[172:175], v[148:151]\n\t"
            "v_mfma_f32_16x16x32_bf16 v[148:151], v[80:83], v[192:195], v[148:151]\n\t"
            "v_mfma_f32_16x16x32_bf16 v[156:159], v[104:107], v[192:195], v[156:159]\n\t"
            "v_mfma_f32_16x16x32_bf16 v[156:159], v[108:111], v[172:175], v[156:159]\n\t"
            "s_waitcnt lgkmcnt(0)\n\t"
            "v_mfma_f32_16x16x32_bf16 v[152:155], v[16:19], v[184:187], v[152:155]\n\t"
            "v_mfma_f32_16x16x32_bf16 v[152:155], v[20:23], v[188:191], v[152:155]\n\t"
            "v_mfma_f32_16x16x32_bf16 v[160:163], v[44:47], v[188:191], v[160:163]\n\t"
            "v_mfma_f32_16x16x32_bf16 v[160:163], v[40:43], v[184:187], v[160:163]\n\t"
            "v_mfma_f32_16x16x32_bf16 v[180:183], v[132:135], v[172:175], v[180:183]\n\t"
            "v_mfma_f32_16x16x32_bf16 v[180:183], v[128:131], v[192:195], v[180:183]\n\t"
            "v_mfma_f32_16x16x32_bf16 v[176:179], v[64:67], v[184:187], v[176:179]\n\t"
            "v_mfma_f32_16x16x32_bf16 v[176:179], v[68:71], v[188:191], v[176:179]\n\t"
            "s_nop 1\n\t"
            "v_cvt_pk_bf16_f32 v152, v152, v153\n\t"
            "v_mfma_f32_16x16x32_bf16 v[148:151], v[92:95], v[188:191], v[148:151]\n\t"
            "v_cvt_pk_bf16_f32 v153, v154, v155\n\t"
            "v_mfma_f32_16x16x32_bf16 v[148:151], v[88:91], v[184:187], v[148:151]\n\t"
            "v_cvt_pk_bf16_f32 v154, v160, v161\n\t"
            "v_cvt_pk_bf16_f32 v155, v162, v163\n\t"
            "v_mfma_f32_16x16x32_bf16 v[156:159], v[112:115], v[184:187], v[156:159]\n\t"
            "v_cvt_pk_bf16_f32 v176, v176, v177\n\t"
            "v_cvt_pk_bf16_f32 v177, v178, v179\n\t"
            "v_mfma_f32_16x16x32_bf16 v[156:159], v[116:119], v[188:191], v[156:159]\n\t"
            "buffer_store_dwordx4 v[152:155], v198, s[40:43], s45 offen sc1\n\t"
            "v_mfma_f32_16x16x32_bf16 v[180:183], v[140:143], v[188:191], v[180:183]\n\t"
            "v_cvt_pk_bf16_f32 v178, v148, v149\n\t"
            "v_cvt_pk_bf16_f32 v179, v150, v151\n\t"
            "v_mfma_f32_16x16x32_bf16 v[180:183], v[136:139], v[184:187], v[180:183]\n\t"
            "buffer_store_dwordx4 v[176:179], v198, s[40:43], s45 offen offset:64 sc1\n\t"
            "s_nop 1\n\t"
            "v_cvt_pk_bf16_f32 v156, v156, v157\n\t"
            "v_cvt_pk_bf16_f32 v157, v158, v159\n\t"
            "s_nop 2\n\t"
            "v_cvt_pk_bf16_f32 v158, v180, v181\n\t"
            "v_cvt_pk_bf16_f32 v159, v182, v183\n\t"
            "buffer_store_dwordx4 v[156:159], v198, s[40:43], s45 offen offset:128 sc1\n\t"
            "s_nop 7\n\t"
            "s_branch .Lwk_pe%=\n\t"
            "wk_conv:\n\t"
            "s_nop 7\n\t"
            "ds_read_b128 v[168:171], v254\n\t"
            "ds_read_b64 v[144:145], v253\n\t"
            "ds_read_b64 v[146:147], v253 offset:96\n\t"
            "ds_read_b64 v[148:149], v253 offset:1744\n\t"
            "ds_read_b64 v[150:151], v253 offset:1840\n\t"
            "ds_read_b128 v[172:175], v254 offset:64\n\t"
            "ds_read_b64 v[152:153], v253 offset:8\n\t"
            "ds_read_b64 v[154:155], v253 offset:104\n\t"
            "ds_read_b64 v[156:157], v253 offset:1752\n\t"
            "ds_read_b64 v[158:159], v253 offset:1848\n\t"
            "ds_read_b64 v[160:161], v253 offset:16\n\t"
            "ds_read_b64 v[162:163], v253 offset:112\n\t"
            "ds_read_b64 v[164:165], v253 offset:1760\n\t"
            "ds_read_b64 v[166:167], v253 offset:1856\n\t"
            "v_mov_b64 v[176:177], 0\n\t"
            "v_mov_b64 v[178:179], 0\n\t"
            "v_mov_b64 v[180:181], 0\n\t"
            "v_mov_b64 v[182:183], 0\n\t"
            "v_mov_b64 v[184:185], 0\n\t"
            "v_mov_b64 v[186:187], 0\n\t"
            "v_mov_b64 v[188:189], 0\n\t"
            "v_mov_b64 v[190:191], 0\n\t"
            "v_add_u32 v236, s67, v250\n\t"
            "v_xor_b32 v237, 16, v236\n\t"
            "v_add_u32 v248, s67, v251\n\t"
            "s_waitcnt lgkmcnt(9)\n\t"
            "v_smfmac_f32_16x16x64_bf16 v[176:179], v[168:171], v[144:151], v255\n\t"
            "ds_read_b128 v[168:171], v254 offset:128\n\t"
            "ds_read_b64 v[144:145], v253 offset:24\n\t"
            "s_waitcnt lgkmcnt(6)\n\t"
            "v_smfmac_f32_16x16x64_bf16 v[180:183], v[172:175], v[152:159], v255\n\t"
            "ds_read_b64 v[146:147], v253 offset:120\n\t"
            "ds_read_b64 v[148:149], v253 offset:1768\n\t"
            "ds_read_b64 v[150:151], v253 offset:1864\n\t"
            "ds_read_b128 v[172:175], v254 offset:192\n\t"
            "ds_read_b64 v[152:153], v253 offset:32\n\t"
            "ds_read_b64 v[154:155], v253 offset:128\n\t"
            "ds_read_b64 v[156:157], v253 offset:1776\n\t"
            "ds_read_b64 v[158:159], v253 offset:1872\n\t"
            "v_cvt_pk_bf16_f32 v176, v176, v177\n\t"
            "v_cvt_pk_bf16_f32 v177, v178, v179\n\t"
            "v_cvt_pk_bf16_f32 v178, v180, v181\n\t"
            "v_cvt_pk_bf16_f32 v179, v182, v183\n\t"
            "v_mfma_f32_4x4x4_16b_bf16 v[196:199], v[176:177], v[176:177], 0\n\t"
            "ds_write_b128 v236, v[176:179]\n\t"
            "v_mfma_f32_16x16x32_bf16 v[192:195], v[238:241], v[176:179], 0\n\t"
            "v_mov_b64 v[176:177], 0\n\t"
            "v_mov_b64 v[180:181], 0\n\t"
            "v_mfma_f32_4x4x4_16b_bf16 v[196:199], v[178:179], v[178:179], v[196:199]\n\t"
            "v_mov_b64 v[178:179], 0\n\t"
            "v_mov_b64 v[182:183], 0\n\t"
            "s_waitcnt lgkmcnt(10)\n\t"
            "v_smfmac_f32_16x16x64_bf16 v[184:187], v[168:171], v[160:167], v255\n\t"
            "ds_read_b128 v[168:171], v254 offset:256\n\t"
            "ds_read_b64 v[160:161], v253 offset:40\n\t"
            "ds_read_b64 v[162:163], v253 offset:136\n\t"
            "ds_read_b64 v[164:165], v253 offset:1784\n\t"
            "ds_read_b64 v[166:167], v253 offset:1880\n\t"
            "s_waitcnt lgkmcnt(10)\n\t"
            "v_smfmac_f32_16x16x64_bf16 v[188:191], v[172:175], v[144:151], v255\n\t"
            "ds_read_b128 v[172:175], v254 offset:320\n\t"
            "ds_read_b64 v[144:145], v253 offset:48\n\t"
            "ds_read_b64 v[146:147], v253 offset:144\n\t"
            "s_cbranch_scc1 .Lwk_np1%=\n\t"
            "s_mov_b64 exec, 0x3ffff\n\t"
            "buffer_load_dwordx4 v252, s[84:87], s89 offen sc1 lds\n\t"
            "s_mov_b64 exec, -1\n\t"
            ".Lwk_np1%=:\n\t"
            "ds_read_b64 v[148:149], v253 offset:1792\n\t"
            "ds_read_b64 v[150:151], v253 offset:1888\n\t"
            "v_cvt_pk_bf16_f32 v184, v184, v185\n\t"
            "v_cvt_pk_bf16_f32 v185, v186, v187\n\t"
            "s_nop 0\n\t"
            "v_cvt_pk_bf16_f32 v186, v188, v189\n\t"
            "v_cvt_pk_bf16_f32 v187, v190, v191\n\t"
            "v_mfma_f32_4x4x4_16b_bf16 v[196:199], v[184:185], v[184:185], v[196:199]\n\t"
            "s_waitcnt lgkmcnt(14)\n\t"
            "ds_write_b128 v237, v[184:187]\n\t"
            "v_mfma_f32_16x16x32_bf16 v[192:195], v[238:241], v[184:187], v[192:195]\n\t"
            "v_mov_b64 v[184:185], 0\n\t"
            "v_mov_b64 v[188:189], 0\n\t"
            "v_mfma_f32_4x4x4_16b_bf16 v[196:199], v[186:187], v[186:187], v[196:199]\n\t"
            "v_mov_b64 v[186:187], 0\n\t"
            "v_mov_b64 v[190:191], 0\n\t"
            "s_waitcnt lgkmcnt(10)\n\t"
            "v_smfmac_f32_16x16x64_bf16 v[176:179], v[168:171], v[152:159], v255\n\t"
            "ds_read_b128 v[168:171], v254 offset:384\n\t"
            "ds_read_b64 v[152:153], v253 offset:56\n\t"
            "ds_read_b64 v[154:155], v253 offset:152\n\t"
            "ds_read_b64 v[156:157], v253 offset:1800\n\t"
            "ds_read_b64 v[158:159], v253 offset:1896\n\t"
            "s_waitcnt lgkmcnt(10)\n\t"
            "v_smfmac_f32_16x16x64_bf16 v[180:183], v[172:175], v[160:167], v255\n\t"
            "ds_read_b128 v[172:175], v254 offset:448\n\t"
            "ds_read_b64 v[160:161], v253 offset:64\n\t"
            "ds_read_b64 v[162:163], v253 offset:160\n\t"
            "ds_read_b64 v[164:165], v253 offset:1808\n\t"
            "ds_read_b64 v[166:167], v253 offset:1904\n\t"
            "v_cvt_pk_bf16_f32 v176, v176, v177\n\t"
            "v_cvt_pk_bf16_f32 v177, v178, v179\n\t"
            "s_nop 0\n\t"
            "v_cvt_pk_bf16_f32 v178, v180, v181\n\t"
            "v_cvt_pk_bf16_f32 v179, v182, v183\n\t"
            "v_mfma_f32_4x4x4_16b_bf16 v[196:199], v[176:177], v[176:177], v[196:199]\n\t"
            "s_waitcnt lgkmcnt(14)\n\t"
            "ds_write_b128 v236, v[176:179] offset:128\n\t"
            "v_mfma_f32_16x16x32_bf16 v[192:195], v[238:241], v[176:179], v[192:195]\n\t"
            "v_mov_b64 v[176:177], 0\n\t"
            "v_mov_b64 v[180:181], 0\n\t"
            "v_mfma_f32_4x4x4_16b_bf16 v[196:199], v[178:179], v[178:179], v[196:199]\n\t"
            "v_mov_b64 v[178:179], 0\n\t"
            "v_mov_b64 v[182:183], 0\n\t"
            "s_waitcnt lgkmcnt(10)\n\t"
            "v_smfmac_f32_16x16x64_bf16 v[184:187], v[168:171], v[144:151], v255\n\t"
            "ds_read_b128 v[168:171], v254 offset:512\n\t"
            "ds_read_b64 v[144:145], v253 offset:72\n\t"
            "ds_read_b64 v[146:147], v253 offset:168\n\t"
            "ds_read_b64 v[148:149], v253 offset:1816\n\t"
            "ds_read_b64 v[150:151], v253 offset:1912\n\t"
            "s_waitcnt lgkmcnt(10)\n\t"
            "v_smfmac_f32_16x16x64_bf16 v[188:191], v[172:175], v[152:159], v255\n\t"
            "ds_read_b128 v[172:175], v254 offset:576\n\t"
            "ds_read_b64 v[152:153], v253 offset:80\n\t"
            "ds_read_b64 v[154:155], v253 offset:176\n\t"
            "ds_read_b64 v[156:157], v253 offset:1824\n\t"
            "ds_read_b64 v[158:159], v253 offset:1920\n\t"
            "v_cvt_pk_bf16_f32 v184, v184, v185\n\t"
            "v_cvt_pk_bf16_f32 v185, v186, v187\n\t"
            "s_cbranch_scc1 .Lwk_np0%=\n\t"
            "s_mov_b64 exec, 0x3ffff\n\t"
            "buffer_load_dwordx4 v252, s[84:87], s89 offen sc1 lds\n\t"
            "s_mov_b64 exec, -1\n\t"
            ".Lwk_np0%=:\n\t"
            "s_nop 0\n\t"
            "v_cvt_pk_bf16_f32 v186, v188, v189\n\t"
            "v_cvt_pk_bf16_f32 v187, v190, v191\n\t"
            "v_mfma_f32_4x4x4_16b_bf16 v[196:199], v[184:185], v[184:185], v[196:199]\n\t"
            "s_waitcnt lgkmcnt(14)\n\t"
            "ds_write_b128 v237, v[184:187] offset:128\n\t"
            "v_mfma_f32_16x16x32_bf16 v[192:195], v[238:241], v[184:187], v[192:195]\n\t"
            "v_mov_b64 v[184:185], 0\n\t"
            "v_mov_b64 v[188:189], 0\n\t"
            "v_mfma_f32_4x4x4_16b_bf16 v[196:199], v[186:187], v[186:187], v[196:199]\n\t"
            "v_mov_b64 v[186:187], 0\n\t"
            "v_mov_b64 v[190:191], 0\n\t"
            "s_waitcnt lgkmcnt(10)\n\t"
            "v_smfmac_f32_16x16x64_bf16 v[176:179], v[168:171], v[160:167], v255\n\t"
            "ds_read_b128 v[168:171], v254 offset:640\n\t"
            "ds_read_b64 v[160:161], v253 offset:88\n\t"
            "ds_read_b64 v[162:163], v253 offset:184\n\t"
            "ds_read_b64 v[164:165], v253 offset:1832\n\t"
            "ds_read_b64 v[166:167], v253 offset:1928\n\t"
            "s_waitcnt lgkmcnt(10)\n\t"
            "v_smfmac_f32_16x16x64_bf16 v[180:183], v[172:175], v[144:151], v255\n\t"
            "ds_read_b128 v[172:175], v254 offset:704\n\t"
            "v_cvt_pk_bf16_f32 v176, v176, v177\n\t"
            "v_cvt_pk_bf16_f32 v177, v178, v179\n\t"
            "s_nop 1\n\t"
            "v_mfma_f32_4x4x4_16b_bf16 v[196:199], v[176:177], v[176:177], v[196:199]\n\t"
            "s_nop 1\n\t"
            "v_cvt_pk_bf16_f32 v178, v180, v181\n\t"
            "v_cvt_pk_bf16_f32 v179, v182, v183\n\t"
            "s_nop 1\n\t"
            "v_mfma_f32_4x4x4_16b_bf16 v[196:199], v[178:179], v[178:179], v[196:199]\n\t"
            "ds_write_b128 v236, v[176:179] offset:256\n\t"
            "v_mfma_f32_16x16x32_bf16 v[192:195], v[238:241], v[176:179], v[192:195]\n\t"
            "s_waitcnt lgkmcnt(6)\n\t"
            "v_smfmac_f32_16x16x64_bf16 v[184:187], v[168:171], v[152:159], v255\n\t"
            "s_waitcnt lgkmcnt(1)\n\t"
            "v_smfmac_f32_16x16x64_bf16 v[188:191], v[172:175], v[160:167], v255\n\t"
            "s_nop 5\n\t"
            "v_cvt_pk_bf16_f32 v184, v184, v185\n\t"
            "v_cvt_pk_bf16_f32 v185, v186, v187\n\t"
            "s_nop 1\n\t"
            "v_mfma_f32_4x4x4_16b_bf16 v[196:199], v[184:185], v[184:185], v[196:199]\n\t"
            "v_cvt_pk_bf16_f32 v186, v188, v189\n\t"
            "v_cvt_pk_bf16_f32 v187, v190, v191\n\t"
            "s_nop 1\n\t"
            "v_mfma_f32_4x4x4_16b_bf16 v[196:199], v[186:187], v[186:187], v[196:199]\n\t"
            "v_mfma_f32_16x16x32_bf16 v[192:195], v[238:241], v[184:187], v[192:195]\n\t"
            "ds_write_b128 v237, v[184:187] offset:256\n\t"
            "s_nop 6\n\t"
            "v_cndmask_b32_e64 v193, v196, v197, s[60:61]\n\t"
            "v_cndmask_b32_e64 v193, v193, v198, s[62:63]\n\t"
            "v_cndmask_b32_e64 v193, v193, v199, s[64:65]\n\t"
            "ds_write_b64 v248, v[192:193] offset:18816\n\t"
            "s_waitcnt lgkmcnt(0)\n\t"
            "s_nop 7\n\t"
            "s_setpc_b64 vcc\n\t"
            ".Lwk_pe%=:\n\t"
            : B_SIO, "+{v247}"(l16_), "+{v228}"(q0), "+{v229}"(q1), "+{v232}"(q2), "+{v233}"(q3), "+{v234}"(q4)
            : "{v242}"(oE_), "{v243}"(oO_), "{v244}"(pR0_), "{v245}"(pR1_), "{v246}"(pR2_), "{v250}"(pXw_), "{v251}"(pSTw_), "{v252}"(aCA_), "{v253}"(aB_), "{v254}"(aA_), "{v255}"(idx_), B_SIN, "{s44}"(ARR_OFF + blockIdx.x * 8), "{s[80:81]}"(mrow), "{s82}"(wave == 6 && cnt == 21 ? REC_OFF + (start - 20 * ((blockIdx.x & 7) * 32 + xi)) * 1280 : 0)
            : "v144", "v145", "v146", "v147", "v148", "v149", "v150", "v151", "v152", "v153", "v154", "v155", "v156", "v157", "v158", "v159", "v160", "v161", "v162", "v163", "v164", "v165", "v166", "v167", "v168", "v169", "v170", "v171", "v172", "v173", "v174", "v175", "v176", "v177", "v178", "v179", "v180", "v181", "v182", "v183", "v184", "v185", "v186", "v187", "v188", "v189", "v190", "v191", "v192", "v193", "v194", "v195", "v196", "v197", "v198", "v199", "v236", "v237", "v248", "v249", "v0", "v1", "v2", "v3", "v4", "v5", "v6", "v7", "v8", "v9", "v10", "v11", "v12", "v13", "v14", "v15", "v16", "v17", "v18", "v19", "v20", "v21", "v22", "v23", "v24", "v25", "v26", "v27", "v28", "v29", "v30", "v31", "v32", "v33", "v34", "v35", "v36", "v37", "v38", "v39", "v40", "v41", "v42", "v43", "v44", "v45", "v46", "v47", "v48", "v49", "v50", "v51", "v52", "v53", "v54", "v55", "v56", "v57", "v58", "v59", "v60", "v61", "v62", "v63", "v64", "v65", "v66", "v67", "v68", "v69", "v70", "v71", "v72", "v73", "v74", "v75", "v76", "v77", "v78", "v79", "v80", "v81", "v82", "v83", "v84", "v85", "v86", "v87", "v88", "v89", "v90", "v91", "v92", "v93", "v94", "v95", "v96", "v97", "v98", "v99", "v100", "v101", "v102", "v103", "v104", "v105", "v106", "v107", "v108", "v109", "v110", "v111", "v112", "v113", "v114", "v115", "v116", "v117", "v118", "v119", "v120", "v121", "v122", "v123", "v124", "v125", "v126", "v127", "v128", "v129", "v130", "v131", "v132", "v133", "v134", "v135", "v136", "v137", "v138", "v139", "v140", "v141", "v142", "v143", "v200", "v201", "v202", "v204", "v205", "v206", "v207", "v208", "v209", "v210", "v211", "v212", "v213", "v214", "v215", "v216", "v217", "v218", "v219", "v220", "v221", "v222", "v223", "v224", "v225", "v226", "v227", "v230", "v231", "v235", B_SCLOB);
        }
        __builtin_amdgcn_endpgm();
    }
}

extern "C" void kernel_launch(void* const* d_in, const int* in_sizes, int n_in,
                              void* d_out, int out_size, void* d_ws, size_t ws_size,
                              hipStream_t stream) {
    const unsigned short* x = (const unsigned short*)d_in[0];
    const unsigned short* dw = (const unsigned short*)d_in[1];
    const unsigned short* wqkv = (const unsigned short*)d_in[2];
    const unsigned short* wo = (const unsigned short*)d_in[3];
    unsigned short* out = (unsigned short*)d_out;
    u32x4* wf = (u32x4*)d_ws;
    win_attn_kernel<<<GRID, 512, 0, stream>>>(x, dw, wqkv, wo, wf, out);
}
```

```cpp
#include <hip/hip_runtime.h>
#include <hip/hip_bf16.h>
#include <stdint.h>

typedef short bf16x8 __attribute__((ext_vector_type(8)));
typedef float f32x4 __attribute__((ext_vector_type(4)));
typedef float f32x2 __attribute__((ext_vector_type(2)));
typedef unsigned u32x2 __attribute__((ext_vector_type(2)));
typedef unsigned u32x4 __attribute__((ext_vector_type(4)));

constexpr int IMG = 511;
constexpr int CH = 192;
constexpr int NWX = 73;
constexpr int NWIN = NWX * NWX;
constexpr int GRID = 256;
constexpr int ROWBYTES = IMG * CH * 2;
constexpr int XBIAS = (IMG + 1) * CH * 2;
constexpr unsigned OOB = 0x40000000u;

constexpr int FLAG_OFF = 7856;
constexpr int X_OFF = 32768;
constexpr int X1_OFF = 53632;
constexpr int O_OFF = 74496;
constexpr int V_OFF = 92928;
constexpr int KQL_OFF = 111360;
constexpr int OL_OFF = 129024;
constexpr int AT_OFF = 137472;
constexpr int CT_OFF = AT_OFF + 19584;
constexpr int RS_OFF = CT_OFF + 4096;
constexpr int RSX_OFF = 162816;
constexpr int WL_OFF = RS_OFF + 2304 + 64;
constexpr int LDS_BYTES = WL_OFF + 128;
#define RS_BASE (RS_OFF + wave * 384)
constexpr int STL_OFF = O_OFF + 12288;
constexpr int DW_OFF = O_OFF + 8448;
constexpr int CP_OFF = X1_OFF + 16384;
#define LX_OFF(i) ((i) < 2 ? CT_OFF + 1024 + (i) * 384 : KQL_OFF + ((i) - 2) * 800)
constexpr int PF_OFF = CT_OFF + 2048;
#define LB_OFF (w < 3 ? V_OFF + w * 13952 : X1_OFF)
#define LPOS(wc) (((wc) & 1) ? 0 : 6)
constexpr unsigned CROW = 0x060a0230u;
constexpr unsigned CLANE = 0x00132C3Fu;

constexpr unsigned long long TOKMAP[6] = {0x20001c190f2a2202ull, 0x052517121f061a13ull, 0x181e08151d0b260cull, 0x1b2b2903102d0901ull, 0x04162e0e2824142cull, 0x110a072127232f0dull};
constexpr unsigned long long ROWMAP[8] = {0x08321b240f1c0006ull, 0x23310e2a09170d14ull, 0x210c2f030a273418ull, 0x1d2b11221f043739ull, 0x3234363a103e192dull, 0x291a20022c3f1605ull, 0x303a3c2628122e15ull, 0x39253d1e01070b13ull};

constexpr int WF_BYTES = 8 * 36 * 1024;
constexpr int FLG_OFF = WF_BYTES;
constexpr int ARR_OFF = FLG_OFF + 4096;
constexpr int EPO_OFF = ARR_OFF + 2048;
constexpr int CTR_OFF = EPO_OFF + 128;
constexpr int REC_OFF = WF_BYTES + 8192;
constexpr int WS_BYTES = REC_OFF + 209 * 1280;
constexpr int SC1 = 16;

#define MFMA32(a, b, c) __builtin_amdgcn_mfma_f32_16x16x32_bf16((a), (b), (c), 0, 0, 0)
#define BC8(v) __builtin_bit_cast(bf16x8, (v))
#define BAR() asm volatile("s_waitcnt lgkmcnt(0)\n\ts_barrier" ::: "memory")
#define SB() __builtin_amdgcn_sched_barrier(0)
#define OPQ(x) asm volatile("" : "+v"(x))
#define ZERO4 ((f32x4){0.f, 0.f, 0.f, 0.f})

typedef __bf16 nbf16x2 __attribute__((ext_vector_type(2)));
__device__ __forceinline__ unsigned pk2(float a, float b) {
    f32x2 v = {a, b};
    nbf16x2 r = __builtin_convertvector(v, nbf16x2);
    return __builtin_bit_cast(unsigned, r);
}
__device__ __forceinline__ float bflo(unsigned u) { return __uint_as_float(u << 16); }
__device__ __forceinline__ float bfhi(unsigned u) { return __uint_as_float(u & 0xffff0000u); }

__device__ __forceinline__ float bf2f(unsigned short v) { return __uint_as_float((unsigned)v << 16); }
__device__ __forceinline__ unsigned byte_of(unsigned long long a, unsigned long long b, int i) {
    return (unsigned)(((i & 8) ? b : a) >> (8 * (i & 7))) & 0xFFu;
}
#define M24(a, b) ((unsigned)__mul24((int)(a), (int)(b)))
__device__ __forceinline__ void seq_to_win(int seq, int& wy, int& wx) {
    const bool last = seq >= 5110;
    const int s = last ? 35 : M24(seq, 28729) >> 22, r = seq - M24(146, s);
    const int kx = last ? M24(r, 171) >> 10 : r >> 2, pos = r - M24(last ? 6 : 4, kx);
    const bool low = last && pos >= (kx == 36 ? 2 : 4);
    wy = low ? 72 : 2 * s + (pos & 1);
    wx = 2 * kx + (low ? (kx == 36 ? 0 : pos - 4) : pos >> 1);
}
__device__ __forceinline__ int win_to_seq(int wy, int wx) {
    const int kx = wx >> 1;
    return M24(146, wy == 72 ? 35 : wy >> 1) + M24(wy >= 70 ? 6 : 4, kx) + (wy == 72 ? (kx == 36 ? 2 : 4 + (wx & 1)) : 2 * (wx & 1) + (wy & 1));
}
__device__ __forceinline__ int block_of(int wy, int wx) { return wy >= 70 ? 1295 + (wx & ~1) + (wy == 72) : M24(37, wy >> 1) + (wx >> 1); }
__device__ __forceinline__ void block_pos(int b, int& ky, int& kx) {
    const int s = M24(b, 1772) >> 16, l = b - 1295;
    kx = l >= 0 ? l >> 1 : b - M24(37, s);
    ky = l >= 0 ? 70 + 2 * (l & 1) : 2 * s;
}
__device__ __forceinline__ unsigned win_org(int wy, int wx) { return M24(wy, 7 * ROWBYTES) + M24(wx, 7 * CH * 2); }
__device__ __forceinline__ unsigned wl_entry(int wy, int wx) {
    return win_org(wy, wx) | (wy == 0 ? 0x08000000u : 0u) | (wx == 0 ? 0x10000000u : 0u) |
           (wx == NWX - 1 ? 0x20000000u : 0u) | ((unsigned)(wx & 1) << 30) | ((unsigned)(wy & 1) << 31);
}
__device__ __forceinline__ unsigned dma_off(int lane) {
    const int col = (lane * 43) >> 8;
    return (unsigned)(col * (CH * 2 - 96) + 16 * lane) | (col == 0 ? 0x10000000u : 0u) | (col == 8 ? 0x20000000u : 0u);
}

#if defined(__HIP_DEVICE_COMPILE__)
#define NOPK __attribute__((target("no-packed-fp32-ops")))
#else
#define NOPK
#endif
__global__ __launch_bounds__(512, 2) NOPK void win_attn_kernel(const unsigned short* __restrict__ x,
                                                          const unsigned short* __restrict__ dw, const unsigned short* __restrict__ wqkv,
                                                          const unsigned short* __restrict__ wo, u32x4* wf,
                                                          unsigned short* __restrict__ out) {
    __shared__ __attribute__((aligned(16))) char smem[LDS_BYTES];

    asm volatile("wk_code_begin:");
    const int tid = threadIdx.x;
    const int wave = __builtin_amdgcn_readfirstlane(tid >> 6);
    const int w = wave & 3;
    const unsigned ldsb = (unsigned)(size_t)smem;
    {
        unsigned long long pc;
        unsigned len, rel;
        asm volatile("s_getpc_b64 %0\n.Lwk_here%=:\n\ts_mov_b32 %1, wk_code_end-wk_code_begin\n\ts_mov_b32 %2, .Lwk_here%=-wk_code_begin" : "=s"(pc), "=s"(len), "=s"(rel));
        pc -= rel;
        const u32x4 cr = {(unsigned)pc, (unsigned)(pc >> 32) & 0xffffu, len, 0x00020000u};
        asm volatile("s_mov_b32 m0, %0\n\ts_nop 0\n\tbuffer_load_dwordx4 %1, %2, %3 offen lds"
                     :: "s"(ldsb + X1_OFF + 14336 + (w & 1) * 1024), "v"((tid & 63) * 16), "s"(cr), "s"((wave * 32 + (blockIdx.x >> 3)) * 1024) : "m0", "memory");
    }
    *(unsigned*)(smem + PF_OFF + tid * 4) = 0x9E3779B9u * (tid + 1);
    *(unsigned*)(smem + (tid < 192 ? AT_OFF + 18816 + tid * 4 : FLAG_OFF + (tid & 3) * 4)) = 0u;
    BAR();
    asm volatile("" :: "s"(x), "s"(dw), "s"(wqkv), "s"(wo), "s"(wf));
    const auto wrsrc = __builtin_amdgcn_make_buffer_rsrc((void*)wf, 0, WS_BYTES, 0x00020000);
    unsigned tag_lo = 0, tag_hi = 0;
#define TAG_READY                                                                                  \
    {                                                                                              \
        unsigned long long tag_;                                                                   \
        asm volatile("s_load_dwordx2 %0, %1, %2\n\ts_waitcnt lgkmcnt(0)" : "=s"(tag_) : "s"(wf), "n"(EPO_OFF) : "memory"); \
        tag_lo = (unsigned)tag_ + 1u; tag_hi = (unsigned)(tag_ >> 32);                             \
    }
    const int pb = blockIdx.x;
    if (pb < 36) {
        const int lane = tid & 63, n = lane & 15, g = lane >> 4;
        const bool isO = wave >= 6;
        const int fr = isO ? 216 + 2 * pb + (wave - 6) : 6 * pb + wave;
        const int tile = fr / 6, ks = fr - 6 * tile, tw = tile / 6, mdt = tile - 6 * tw;
        const int col = isO ? 96 * (tw - 6) + 32 * (mdt >> 1) + 8 * (n >> 2) + 4 * (mdt & 1) + (n & 3)
                            : (tw < 4 ? (mdt < 3 ? 192 : -48) + 48 * tw : 96 * tw) + 16 * mdt + n;
        const int rb = isO ? 384 : 1152;
        const auto rr = __builtin_amdgcn_make_buffer_rsrc((void*)(isO ? wo : wqkv), 0, 192 * rb, 0x00020000);
        const int vo = (48 * (g >> 1) + 8 * (g & 1)) * rb + 2 * col;
        float v[8];
#pragma unroll
        for (int j = 0; j < 8; ++j) v[j] = bf2f(__builtin_amdgcn_raw_buffer_load_b16(rr, vo, (96 * (ks & 1) + 16 * (ks >> 1) + j) * rb, 0));
        TAG_READY
        float* red = (float*)smem;
        red[n * 36 + wave * 4 + g] = ((v[0] + v[1]) + (v[2] + v[3])) + ((v[4] + v[5]) + (v[6] + v[7]));
        BAR();
        float mean = 0.f;
#pragma unroll
        for (int i = 0; i < 6; ++i) {
            const f32x4 p = *(const f32x4*)(red + n * 36 + 4 * i);
            mean += (p[0] + p[1]) + (p[2] + p[3]);
        }
        mean = isO ? 0.f : mean * (1.f / 192.f);
        u32x4 o;
#pragma unroll
        for (int j = 0; j < 4; ++j) o[j] = pk2(v[2 * j] - mean, v[2 * j + 1] - mean);
        __builtin_amdgcn_raw_buffer_store_b128(o, wrsrc, lane * 16, fr * 1024, SC1);
        if (pb == 0 && tid == 448) __builtin_amdgcn_raw_buffer_store_b32(0u, wrsrc, CTR_OFF, 0, SC1);
        asm volatile("s_waitcnt vmcnt(0)" ::: "memory");
        BAR();
        if (tid < 8) __builtin_amdgcn_raw_buffer_store_b64((u32x2){tag_lo, tag_hi}, wrsrc, FLG_OFF + tid * 512 + pb * 8, 0, SC1);
    }
    const int xi = blockIdx.x >> 3, nshort = (int)(0x0506060606060606ull >> (8 * (blockIdx.x & 7))) & 63;
    const int start = 672 * (blockIdx.x & 7) - ((int)(0x2a241e18120c0600ull >> (8 * (blockIdx.x & 7))) & 63) + 21 * xi - (xi < nshort ? xi : nshort);
    const int cnt = xi < nshort ? 20 : 21;

    const auto xrsrc = __builtin_amdgcn_make_buffer_rsrc((void*)((const char*)x - XBIAS), 0, IMG * IMG * CH * 2 + XBIAS, 0x00020000);
#define LANE_SETUP                                                                                 \
    int lane;                                                                                      \
    asm volatile("v_mbcnt_lo_u32_b32 %0, -1, 0\n\tv_mbcnt_hi_u32_b32 %0, -1, %0" : "=v"(lane));    \
    __builtin_assume(lane >= 0 && lane < 64);                                                      \
    const int g = lane >> 4, l15 = lane & 15, sw = l15 >> 1;                                       \
    int fE = X_OFF + l15 * 384 + ((g ^ sw) << 4);                                                  \
    OPQ(fE);

#define DMA_SLICE(en)                                                                              \
        {                                                                                          \
            const unsigned vo_ = dma_off(lane) & (0x401fffffu | ((en) & 0x38000000u));             \
            const unsigned so_ = ((en) & 0x7ffffffu) + 96 * w;                                     \
            _Pragma("unroll") for (int r = 0; r < 9; ++r) {                                        \
                unsigned m0_ = ldsb + w * 8192 + r * 872;                                          \
                unsigned sr_ = (r == 0 && ((en) & 0x08000000u)) ? OOB : so_ + r * ROWBYTES;        \
                asm volatile("s_mov_b32 m0, %0\n\ts_mov_b32 exec_hi, 0x3fffff\n\tbuffer_load_dwordx4 %1, %2, %3 offen lds\n\ts_mov_b32 exec_hi, -1" \
                             :: "s"(m0_), "v"(vo_), "s"(xrsrc), "s"(sr_) : "m0", "memory");        \
            }                                                                                      \
        }
#define XPAT(r) (((r) < 48 ? (r) : 48) * 384 + (((2 * w) ^ (((r) >> 1) & 7)) << 4))
#define SPAT(r) ((w >> 1) * 1024 + (w & 1) * 8 + (r) * 16)
#define CONV_CONSTS(var) CONV_ROWS(var) CONV_OPS
#define CONV_ROWS(var) \
            const unsigned long long rlo = g == 0 ? ROWMAP[0] : (g == 1 ? ROWMAP[2] : (g == 2 ? ROWMAP[4] : ROWMAP[6])); \
            const unsigned long long rhi = g == 0 ? ROWMAP[1] : (g == 1 ? ROWMAP[3] : (g == 2 ? ROWMAP[5] : ROWMAP[7])); \
            int xrow = byte_of(rlo, rhi, l15); \
            if (lane == (int)((CLANE >> (8 * (var))) & 0xFF)) xrow = 48; \
            if (lane == 63) xrow = (CROW >> (8 * (var))) & 0xFF; \
            const unsigned pXw_ = XPAT(xrow); \
            const unsigned pSTw_ = (w >> 1) * 1024 + (w & 1) * 8 + xrow * 16; \
            const int oy = (0x5320 >> (4 * (l15 >> 2))) & 15, ox = (0x5320 >> (4 * (l15 & 3))) & 15; \
            const unsigned aB_ = ldsb + w * 8192 + (oy + (g >> 1)) * 872 + (ox + 2 * (g & 1)) * 96;
#define CONV_OPS \
            const int dyi = g - (l15 >> 3); \
            const unsigned aA_ = ldsb + AT_OFF + (l15 & 3) * 16 + \
                                 ((dyi < 0 || dyi > 2) ? 18816 : (dyi == 0 ? 0 : (dyi == 1 ? 6208 : 12544)) + ((l15 >> 2) & 1) * 3072 + w * 768); \
            const unsigned idx_ = (l15 & 3) * 0x5555u; \
            const unsigned se = (l15 == 4 * g) ? 0x3F803F80u : 0u; \
            const u32x4 sel4 = {se, se, se, se};

    unsigned en0, en1;
    {
        LANE_SETUP
        const int px = (lane * 43) >> 8;

        int wy, wx;
        seq_to_win(start + (lane < cnt ? lane : cnt - 1), wy, wx);
        const unsigned en = wl_entry(wy, wx);
        en0 = __builtin_amdgcn_readlane(en, 0);
        en1 = __builtin_amdgcn_readlane(en, 1);
        if (wave >= 4) {
            const int bk = block_of(wy, wx);
            const int key0 = __builtin_amdgcn_readlane(bk, 0), nblk = __builtin_amdgcn_readlane(bk, 63) - key0 + 1;
            const int b = l15 < nblk ? l15 : 0;
            int ky, kx;
            block_pos(key0 + b, ky, kx);
            const unsigned org = l15 < nblk ? (win_org(ky, 2 * kx) + (5 * ROWBYTES + 5 * CH * 2 + XBIAS)) | (kx == 36 ? OOB : 0u) : OOB + 0x8000000u;
            const unsigned lo = (px & 3) * (CH * 2) + (lane - 6 * px) * 16, keep = (px & 2) ? ~0u : ~OOB;
#pragma unroll
            for (int p = 0; p < 4; ++p) {
                if (p < 3 || nblk > 6) {
                    const unsigned oa = __builtin_amdgcn_readlane(org, 2 * p), ob = __builtin_amdgcn_readlane(org, 2 * p + 1);
                    const unsigned vo_ = ((lane < 24 ? oa : ob) & keep) + lo;
#pragma unroll
                    for (int r = 0; r < 4; ++r) {
                        unsigned m0_ = ldsb + LB_OFF + (4 * p + r) * 872;
                        asm volatile("s_mov_b32 m0, %0\n\ts_mov_b32 exec_hi, 0xffff\n\tbuffer_load_dwordx4 %1, %2, %3 offen lds\n\ts_mov_b32 exec_hi, -1"
                                     :: "s"(m0_), "v"(vo_), "s"(xrsrc), "s"(96 * w + r * ROWBYTES) : "m0", "memory");
                    }
                }
            }
            SB();
            const int ly = ky + (g >> 1), lx = 2 * kx + (g & 1), li = win_to_seq(ly, lx) - start;
            const bool ok = ly < NWX && lx < NWX && li >= 0 && li < cnt && l15 < nblk;
            const int row = ok ? li : 47;
            unsigned* cp = (unsigned*)(smem + CP_OFF + (w * 64 + lane) * 16);
            *(u32x2*)cp = (u32x2){(unsigned)XPAT(row), SPAT(row) + (unsigned)(STL_OFF - O_OFF - 18816)};
            cp[2] = ldsb + LB_OFF + (4 * (b >> 1) + (g >> 1)) * 872 + (b & 1) * 384 + (g & 1) * 192;
        } else {
            {
                const auto dr = __builtin_amdgcn_make_buffer_rsrc((void*)dw, 0, 9 * 384, 0x00020000);
                unsigned m0_ = ldsb + DW_OFF + w * 864, vo_ = px * 288 + lane * 16;
                asm volatile("s_mov_b32 m0, %0\n\ts_mov_b32 exec_hi, 0x3fffff\n\tbuffer_load_dwordx4 %1, %2, %3 offen lds\n\ts_mov_b32 exec_hi, -1"
                             :: "s"(m0_), "v"(vo_), "s"(dr), "s"(96 * w) : "m0", "memory");
            }
            DMA_SLICE(en0)
            SB();
            if (wave == 2) {
                u32x4 so;
#pragma unroll
                for (int e = 0; e < 3; ++e) {
                    const int tk = byte_of(TOKMAP[2 * e], TOKMAP[2 * e + 1], l15);
                    const int ty = (tk * 37) >> 8, tx = tk - 7 * ty;
                    so[e] = (unsigned)(ty * ROWBYTES + tx * (CH * 2)) + 16 * g;
                }
                so[3] = dma_off(lane);
                *(u32x4*)(smem + CT_OFF + lane * 16) = so;
            }
            if (wave == 1 && lane < 32) *(unsigned*)(smem + WL_OFF + lane * 4) = lane < 20 ? en : 0x7ffffffu;
            if (wave == 1 && lane == 20) *(unsigned*)(smem + PF_OFF + 520) = en;
            asm volatile("s_waitcnt vmcnt(9)" ::: "memory");
            if (lane < 48) {
                unsigned short dv[9];
#pragma unroll
                for (int i = 0; i < 9; ++i) dv[i] = *(const unsigned short*)(smem + DW_OFF + w * 864 + i * 96 + lane * 2);
#pragma unroll
                for (int dy = 0; dy < 3; ++dy) {
                    const unsigned a = dv[3 * dy], b2 = dv[3 * dy + 1] | (dy == 1 ? 0x3F800000u : 0u), c = dv[3 * dy + 2];
                    char* p = smem + AT_OFF + (dy == 0 ? 0 : (dy == 1 ? 6208 : 12544)) + w * 768 + lane * 16;
                    *(u32x4*)p = (u32x4){a, b2, c, 0u};
                    *(u32x4*)(p + 3072) = (u32x4){0u, a, b2, c};
                }
            }
            asm volatile("s_mov_b64 exec, 1\n\tds_add_u32 %0, %1\n\ts_mov_b64 exec, -1" :: "v"(ldsb + FLAG_OFF + 8), "v"(1) : "memory");
        }
    }
    const unsigned long long mrow = 0x0001000100010001ull * ((1u << (cnt - 16)) - 1u);
#define KARGS                                                                                      \
    unsigned long long xb, wb, ob;                                                                 \
    asm volatile("s_load_dwordx2 %0, %3, 0x0\n\ts_load_dwordx2 %1, %3, 0x20\n\ts_load_dwordx2 %2, %3, 0x28\n\ts_waitcnt lgkmcnt(0)" \
                 : "=&s"(xb), "=&s"(wb), "=&s"(ob) : "s"(__builtin_amdgcn_kernarg_segment_ptr()) : "memory"); \
    xb -= XBIAS;                                                                                   \
    const u32x4 wr = {(unsigned)wb, (unsigned)(wb >> 32) & 0xffffu, (unsigned)WS_BYTES, 0x00020000u}; \
    const u32x4 orr = {(unsigned)ob, (unsigned)(ob >> 32) & 0xffffu, (unsigned)(IMG * IMG * CH * 2), 0x00020000u}; \
    const u32x4 xr = {(unsigned)xb, (unsigned)(xb >> 32) & 0xffffu, (unsigned)(IMG * IMG * CH * 2 + XBIAS), 0x00020000u};
    const int tk_ = (xi + 4) / 6;
    const unsigned touch = wave * 36864 + ((xi + 4 - 6 * tk_) * 6 + tk_) * 1024;
#define B_SCALARS \
            CONV_CONSTS(0) \
            const unsigned aCA_ = ldsb + CT_OFF + lane * 16; \
            KARGS \
            unsigned s45 = OOB, s46 = (en0 & 0x7ffffffu) + 96 * w, s47 = (en1 & 0x7ffffffu) + 96 * w; \
            unsigned s48 = ldsb + X_OFF, s67 = ldsb + X1_OFF, s76 = ldsb + WL_OFF + 8, s70 = 0; \
            unsigned s56 = __builtin_amdgcn_readfirstlane((en0 >> 30) * 4 + (en1 >> 30)); \
            unsigned s69 = 0x401fffffu | (en1 & 0x38000000u), s79 = (en1 & 0x08000000u) ? OOB : s47, s59 = ldsb + LX_OFF(1) + 32 * w; \
            unsigned cp_ = ldsb + CP_OFF + (w * 64 + lane) * 16; \
            unsigned long long xp = 0, sp = 0; \
            _Pragma("unroll") for (int v = 0; v < 4; ++v) { \
                const unsigned r = (CROW >> (8 * v)) & 0xFF; \
                xp |= (unsigned long long)XPAT(r) << (16 * v); \
                sp |= (unsigned long long)SPAT(r) << (16 * v); \
            }
#define B_SIO "+{s45}"(s45), "+{s46}"(s46), "+{s47}"(s47), "+{s48}"(s48), "+{s67}"(s67), "+{s76}"(s76), "+{s56}"(s56), "+{s70}"(s70), "+{s69}"(s69), "+{s79}"(s79), "+{s59}"(s59), "+{v203}"(cp_)
#define B_SIN "{v[238:241]}"(sel4), "{s[40:43]}"(orr), "{s[72:75]}"(xr), "{s39}"(0x401fffffu), "{s[28:31]}"(wr), "{s33}"(wave * 36864), "{s78}"(96 * w), \
              "{s27}"(96 * w - 384), "{s[50:51]}"(xp), "{s[52:53]}"(sp), "{s54}"(CLANE), "{s55}"(CROW & 0x0F0F0F0Fu), "{s[34:35]}"(0x0001000100010001ull), \
              "{s[60:61]}"(0x2222222222222222ull), "{s[62:63]}"(0x4444444444444444ull), "{s[64:65]}"(0x8888888888888888ull), \
              "{s66}"(ldsb + w * 8192), "{s71}"((ldsb + X_OFF) ^ (ldsb + X1_OFF))
#define B_SCLOB "s83", "s84", "s26", "s77", "s68", "s36", "s37", "s38", "s57", "s58", "s49", "vcc", "scc", "m0", "memory"

    if (wave < 4) {
        LANE_SETUP
        const int dW = X_OFF + l15 * 384 + (((2 * w + (g >> 1)) ^ sw) << 4) + 8 * (g & 1);
        const int vB = V_OFF + w * 4608 + 1024 + lane * 8, rsR = RS_BASE + l15 * 4;
        {
            const unsigned rsb = ldsb + RS_BASE;
            const unsigned pE_ = fE - X_OFF, aO_ = ldsb + dW + (O_OFF - X_OFF), sel_ = (l15 & 3) ? 0u : 1u, aVB_ = ldsb + vB;
            unsigned pST_ = lane * 16, aPLw_ = rsb + lane * 2, aRSr_ = ldsb + rsR, aRSw_ = rsb + lane * 4;
            unsigned g16_ = (l15 & 3) ? ldsb + AT_OFF + 18816 : g * 16, g8_ = (l15 & 3) ? ldsb + AT_OFF + 18816 : g * 8;
            unsigned loVL_ = ((2 * w + (l15 >> 3)) << 4) + (l15 & 7) * 2, loOL_ = g * 128;
            unsigned on = (l15 & 3) ? 0u : 0x3F803F80u;
            CONV_CONSTS(en0 >> 30)
            unsigned c0 = lane == (int)((CLANE >> (8 * (en0 >> 30))) & 0xFF) ? LX_OFF(0) - X_OFF + 32 * w : pXw_, c1 = pSTw_, c2 = lane * 16, c3 = aB_, c4 = aA_, c5 = idx_;
            unsigned c6 = se, c7 = se, c8 = se, c9 = se, s89 = FLG_OFF + (blockIdx.x & 7) * 512, s46 = wave * 36864;
            unsigned s48 = ldsb + X_OFF, s70 = 0;
            unsigned s72 = ldsb + KQL_OFF + w * 192, s73 = ldsb + OL_OFF, s74 = ldsb + KQL_OFF + 784, s77 = ldsb + OL_OFF, s76 = ldsb + KQL_OFF + 768 + w * 4;
            KARGS
            asm volatile(
            "s_load_dwordx2 s[82:83], s[84:85], 301056\n\t"
            "s_mov_b32 m0, 159104\n\t"
            "s_mov_b32 s67, 32768\n\t"
            "s_mov_b32 s60, 0x22222222\n\t"
            "s_mov_b32 s61, 0x22222222\n\t"
            "s_mov_b32 s62, 0x44444444\n\t"
            "s_mov_b32 s63, 0x44444444\n\t"
            "s_mov_b32 s64, 0x88888888\n\t"
            "s_mov_b32 s65, 0x88888888\n\t"
            "s_waitcnt vmcnt(0) lgkmcnt(0)\n\t"
            "s_add_u32 s82, s82, 1\n\t"
            "s_cmp_lg_u32 s80, 16\n\t"
            "s_cbranch_scc1 .Lwa_p0%=\n\t"
            "s_mov_b64 exec, 0x3ffff\n\t"
            "buffer_load_dwordx4 v252, s[84:87], s89 offen sc1 lds\n\t"
            "s_mov_b64 exec, -1\n\t"
            ".Lwa_p0%=:\n\t"
            "s_call_b64 vcc, wk_conv\n\t"
            "v_min_u32 v146, 0x230, v252\n\t"
            "v_lshrrev_b32 v146, 1, v146\n\t"
            "v_add_u32 v146, 159104, v146\n\t"
            "s_mov_b32 s81, 0x10000\n\t"
            "v_mov_b32 v240, v200\n\t"
            "v_mov_b32 v241, v200\n\t"
            "v_mov_b32 v242, v200\n\t"
            "v_mov_b32 v243, v200\n\t"
            "v_mov_b32 v248, v204\n\t"
            "v_mov_b32 v249, v205\n\t"
            "v_mov_b32 v250, v206\n\t"
            "v_mov_b32 v251, v207\n\t"
            "v_mov_b32 v253, v209\n\t"
            "v_mov_b32 v254, v210\n\t"
            "v_mov_b32 v255, v211\n\t"
            ".Lwa_p1%=:\n\t"
            "ds_read_b64 v[144:145], v146\n\t"
            "s_waitcnt lgkmcnt(0)\n\t"
            "v_cmp_ne_u32 vcc, s82, v144\n\t"
            "v_cmp_ne_u32 s[60:61], s83, v145\n\t"
            "s_or_b64 vcc, vcc, s[60:61]\n\t"
            "s_cbranch_scc0 .Lwa_p3%=\n\t"
            "s_sub_u32 s81, s81, 1\n\t"
            "s_cbranch_scc1 .Lwa_p3%=\n\t"
            "s_cmp_lg_u32 s80, 16\n\t"
            "s_cbranch_scc1 .Lwa_p2%=\n\t"
            "s_mov_b64 exec, 0x3ffff\n\t"
            "buffer_load_dwordx4 v252, s[84:87], s89 offen sc1 lds\n\t"
            "s_mov_b64 exec, -1\n\t"
            "s_waitcnt vmcnt(24)\n\t"
            ".Lwa_p2%=:\n\t"
            "s_sleep 3\n\t"
            "s_branch .Lwa_p1%=\n\t"
            ".Lwa_p3%=:\n\t"
            "v_mov_b32 v252, v208\n\t"
            "v_mov_b32 v144, s82\n\t"
            "v_mov_b32 v145, s83\n\t"
            "v_mov_b32 v146, 159616\n\t"
            "ds_write_b64 v146, v[144:145]\n\t"
            "s_mov_b32 s60, 0xffff0000\n\t"
            "s_mov_b32 s61, 0x0\n\t"
            "s_mov_b32 s62, 0x0\n\t"
            "s_mov_b32 s63, 0xffff\n\t"
            "s_mov_b32 s64, 0x0\n\t"
            "s_mov_b32 s65, 0xffff0000\n\t"
            "s_mov_b32 s66, 0xffffffff\n\t"
            "s_mov_b32 s67, 0x1ffff\n\t"
            "buffer_load_dwordx4 v[140:143], v248, s[84:87], s88 offen sc1\n\t"
            "s_add_u32 s49, s46, 0\n\t"
            "buffer_load_dwordx4 v[0:3], v248, s[84:87], s49 offen sc1\n\t"
            "s_add_u32 s49, s46, 6144\n\t"
            "buffer_load_dwordx4 v[24:27], v248, s[84:87], s49 offen sc1\n\t"
            "s_add_u32 s49, s46, 12288\n\t"
            "buffer_load_dwordx4 v[48:51], v248, s[84:87], s49 offen sc1\n\t"
            "s_add_u32 s49, s46, 18432\n\t"
            "buffer_load_dwordx4 v[72:75], v248, s[84:87], s49 offen sc1\n\t"
            "s_add_u32 s49, s46, 24576\n\t"
            "buffer_load_dwordx4 v[96:99], v248, s[84:87], s49 offen sc1\n\t"
            "s_add_u32 s49, s46, 30720\n\t"
            "buffer_load_dwordx4 v[120:123], v248, s[84:87], s49 offen sc1\n\t"
            "s_add_u32 s49, s46, 1024\n\t"
            "buffer_load_dwordx4 v[4:7], v248, s[84:87], s49 offen sc1\n\t"
            "s_add_u32 s49, s46, 7168\n\t"
            "buffer_load_dwordx4 v[28:31], v248, s[84:87], s49 offen sc1\n\t"
            "s_add_u32 s49, s46, 13312\n\t"
            "buffer_load_dwordx4 v[52:55], v248, s[84:87], s49 offen sc1\n\t"
            "s_add_u32 s49, s46, 19456\n\t"
            "buffer_load_dwordx4 v[76:79], v248, s[84:87], s49 offen sc1\n\t"
            "s_add_u32 s49, s46, 25600\n\t"
            "buffer_load_dwordx4 v[100:103], v248, s[84:87], s49 offen sc1\n\t"
            "s_add_u32 s49, s46, 31744\n\t"
            "buffer_load_dwordx4 v[124:127], v248, s[84:87], s49 offen sc1\n\t"
            "s_add_u32 s49, s46, 2048\n\t"
            "buffer_load_dwordx4 v[8:11], v248, s[84:87], s49 offen sc1\n\t"
            "s_add_u32 s49, s46, 8192\n\t"
            "buffer_load_dwordx4 v[32:35], v248, s[84:87], s49 offen sc1\n\t"
            "s_add_u32 s49, s46, 14336\n\t"
            "buffer_load_dwordx4 v[56:59], v248, s[84:87], s49 offen sc1\n\t"
            "s_add_u32 s49, s46, 20480\n\t"
            "buffer_load_dwordx4 v[80:83], v248, s[84:87], s49 offen sc1\n\t"
            "s_add_u32 s49, s46, 26624\n\t"
            "buffer_load_dwordx4 v[104:107], v248, s[84:87], s49 offen sc1\n\t"
            "s_add_u32 s49, s46, 32768\n\t"
            "buffer_load_dwordx4 v[128:131], v248, s[84:87], s49 offen sc1\n\t"
            "s_add_u32 s49, s46, 3072\n\t"
            "buffer_load_dwordx4 v[12:15], v248, s[84:87], s49 offen sc1\n\t"
            "s_add_u32 s49, s46, 9216\n\t"
            "buffer_load_dwordx4 v[36:39], v248, s[84:87], s49 offen sc1\n\t"
            "s_add_u32 s49, s46, 15360\n\t"
            "buffer_load_dwordx4 v[60:63], v248, s[84:87], s49 offen sc1\n\t"
            "s_add_u32 s49, s46, 21504\n\t"
            "buffer_load_dwordx4 v[84:87], v248, s[84:87], s49 offen sc1\n\t"
            "s_add_u32 s49, s46, 27648\n\t"
            "buffer_load_dwordx4 v[108:111], v248, s[84:87], s49 offen sc1\n\t"
            "s_add_u32 s49, s46, 33792\n\t"
            "buffer_load_dwordx4 v[132:135], v248, s[84:87], s49 offen sc1\n\t"
            "s_add_u32 s49, s46, 4096\n\t"
            "buffer_load_dwordx4 v[16:19], v248, s[84:87], s49 offen sc1\n\t"
            "s_add_u32 s49, s46, 10240\n\t"
            "buffer_load_dwordx4 v[40:43], v248, s[84:87], s49 offen sc1\n\t"
            "s_add_u32 s49, s46, 16384\n\t"
            "buffer_load_dwordx4 v[64:67], v248, s[84:87], s49 offen sc1\n\t"
            "s_add_u32 s49, s46, 22528\n\t"
            "buffer_load_dwordx4 v[88:91], v248, s[84:87], s49 offen sc1\n\t"
            "s_add_u32 s49, s46, 28672\n\t"
            "buffer_load_dwordx4 v[112:115], v248, s[84:87], s49 offen sc1\n\t"
            "s_add_u32 s49, s46, 34816\n\t"
            "buffer_load_dwordx4 v[136:139], v248, s[84:87], s49 offen sc1\n\t"
            "s_add_u32 s49, s46, 5120\n\t"
            "buffer_load_dwordx4 v[20:23], v248, s[84:87], s49 offen sc1\n\t"
            "s_add_u32 s49, s46, 11264\n\t"
            "buffer_load_dwordx4 v[44:47], v248, s[84:87], s49 offen sc1\n\t"
            "s_add_u32 s49, s46, 17408\n\t"
            "buffer_load_dwordx4 v[68:71], v248, s[84:87], s49 offen sc1\n\t"
            "s_add_u32 s49, s46, 23552\n\t"
            "buffer_load_dwordx4 v[92:95], v248, s[84:87], s49 offen sc1\n\t"
            "s_add_u32 s49, s46, 29696\n\t"
            "buffer_load_dwordx4 v[116:119], v248, s[84:87], s49 offen sc1\n\t"
            "s_add_u32 s49, s46, 35840\n\t"
            "buffer_load_dwordx4 v[140:143], v248, s[84:87], s49 offen sc1\n\t"
            "s_waitcnt lgkmcnt(0)\n\t"
            "s_barrier\n\t"
            "s_setprio 2\n\t"
            "v_add_u32 v235, s71, v248\n\t"
            "v_add_u32 v232, s71, v244\n\t"
            "v_xor_b32 v233, 64, v232\n\t"
            "v_subrev_u32 v239, s75, v251\n\t"
            "v_mul_u32_u24 v239, 200, v239\n\t"
            "v_lshrrev_b32 v234, 3, v255\n\t"
            "v_add3_u32 v234, v234, v239, s72\n\t"
            "ds_read_b128 v[228:231], v235 offset:12288\n\t"
            "ds_read_b128 v[144:147], v232\n\t"
            "ds_read_b128 v[148:151], v233\n\t"
            "ds_read_b128 v[204:207], v232 offset:6144\n\t"
            "ds_read_b128 v[208:211], v233 offset:6144\n\t"
            "ds_read_b128 v[152:155], v232 offset:128\n\t"
            "ds_read_b128 v[156:159], v233 offset:128\n\t"
            "ds_read_b128 v[212:215], v232 offset:6272\n\t"
            "ds_read_b128 v[224:227], v233 offset:6272\n\t"
            "ds_read_b128 v[160:163], v232 offset:256\n\t"
            "ds_read_b128 v[164:167], v233 offset:256\n\t"
            "s_waitcnt vmcnt(24)\n\t"
            "s_waitcnt lgkmcnt(10)\n\t"
            "v_add_f32 v236, v228, v230\n\t"
            "v_add_f32 v237, v229, v231\n\t"
            "s_waitcnt lgkmcnt(8)\n\t"
            "v_mfma_f32_16x16x32_bf16 v[168:171], v[0:3], v[144:147], 0\n\t"
            "ds_read_b128 v[228:231], v235 offset:13312\n\t"
            "v_lshrrev_b32 v235, 4, v255\n\t"
            "v_mfma_f32_16x16x32_bf16 v[168:171], v[4:7], v[148:151], v[168:171]\n\t"
            "v_add3_u32 v235, v235, v239, s72\n\t"
            "v_mfma_f32_16x16x32_bf16 v[176:179], v[48:51], v[144:147], 0\n\t"
            "v_mfma_f32_16x16x32_bf16 v[176:179], v[52:55], v[148:151], v[176:179]\n\t"
            "v_mfma_f32_16x16x32_bf16 v[172:175], v[24:27], v[144:147], 0\n\t"
            "v_mfma_f32_16x16x32_bf16 v[172:175], v[28:31], v[148:151], v[172:175]\n\t"
            "v_mfma_f32_16x16x32_bf16 v[180:183], v[72:75], v[144:147], 0\n\t"
            "v_mfma_f32_16x16x32_bf16 v[180:183], v[76:79], v[148:151], v[180:183]\n\t"
            "s_waitcnt lgkmcnt(0)\n\t"
            "v_add_f32 v228, v228, v230\n\t"
            "v_add_f32 v236, v236, v228\n\t"
            "v_mfma_f32_16x16x32_bf16 v[184:187], v[96:99], v[144:147], 0\n\t"
            "v_add_f32 v229, v229, v231\n\t"
            "v_mul_f32 v236, 0x3baaaaab, v236\n\t"
            "v_mfma_f32_16x16x32_bf16 v[184:187], v[100:103], v[148:151], v[184:187]\n\t"
            "v_add_f32 v237, v237, v229\n\t"
            "v_mul_f32_e64 v236, v236, -v236\n\t"
            "v_mfma_f32_16x16x32_bf16 v[188:191], v[120:123], v[144:147], 0\n\t"
            "v_fmac_f32 v236, 0x3baaaaab, v237\n\t"
            "v_max_f32 v236, 0, v236\n\t"
            "v_mfma_f32_16x16x32_bf16 v[188:191], v[124:127], v[148:151], v[188:191]\n\t"
            "v_add_f32 v236, 0x3727c5ac, v236\n\t"
            "v_rsq_f32 v236, v236\n\t"
            "v_mfma_f32_16x16x32_bf16 v[192:195], v[0:3], v[204:207], 0\n\t"
            "v_mfma_f32_16x16x32_bf16 v[192:195], v[4:7], v[208:211], v[192:195]\n\t"
            "ds_write_b32 v249, v236\n\t"
            "v_mfma_f32_16x16x32_bf16 v[196:199], v[24:27], v[204:207], 0\n\t"
            "ds_read_b32 v236, v251\n\t"
            "ds_read_b32 v237, v251 offset:64\n\t"
            "v_mfma_f32_16x16x32_bf16 v[196:199], v[28:31], v[208:211], v[196:199]\n\t"
            "v_mfma_f32_16x16x32_bf16 v[200:203], v[48:51], v[204:207], 0\n\t"
            "v_mfma_f32_16x16x32_bf16 v[200:203], v[52:55], v[208:211], v[200:203]\n\t"
            "v_mfma_f32_16x16x32_bf16 v[216:219], v[72:75], v[204:207], 0\n\t"
            "v_mfma_f32_16x16x32_bf16 v[216:219], v[76:79], v[208:211], v[216:219]\n\t"
            "v_mfma_f32_16x16x32_bf16 v[220:223], v[96:99], v[204:207], 0\n\t"
            "v_mfma_f32_16x16x32_bf16 v[220:223], v[100:103], v[208:211], v[220:223]\n\t"
            "v_mfma_f32_16x16x32_bf16 v[228:231], v[120:123], v[204:207], 0\n\t"
            "ds_read_b128 v[204:207], v232 offset:6400\n\t"
            "v_add_u32 v232, s48, v244\n\t"
            "v_mfma_f32_16x16x32_bf16 v[228:231], v[124:127], v[208:211], v[228:231]\n\t"
            "ds_read_b128 v[208:211], v233 offset:6400\n\t"
            "s_waitcnt vmcnt(12)\n\t"
            "v_mfma_f32_16x16x32_bf16 v[168:171], v[8:11], v[152:155], v[168:171]\n\t"
            "v_xor_b32 v233, 64, v232\n\t"
            "v_mfma_f32_16x16x32_bf16 v[168:171], v[12:15], v[156:159], v[168:171]\n\t"
            "v_mfma_f32_16x16x32_bf16 v[176:179], v[56:59], v[152:155], v[176:179]\n\t"
            "v_mfma_f32_16x16x32_bf16 v[176:179], v[60:63], v[156:159], v[176:179]\n\t"
            "v_mfma_f32_16x16x32_bf16 v[172:175], v[32:35], v[152:155], v[172:175]\n\t"
            "v_mfma_f32_16x16x32_bf16 v[172:175], v[36:39], v[156:159], v[172:175]\n\t"
            "v_mfma_f32_16x16x32_bf16 v[180:183], v[80:83], v[152:155], v[180:183]\n\t"
            "v_mfma_f32_16x16x32_bf16 v[180:183], v[84:87], v[156:159], v[180:183]\n\t"
            "v_mfma_f32_16x16x32_bf16 v[184:187], v[104:107], v[152:155], v[184:187]\n\t"
            "v_mfma_f32_16x16x32_bf16 v[184:187], v[108:111], v[156:159], v[184:187]\n\t"
            "v_mfma_f32_16x16x32_bf16 v[188:191], v[128:131], v[152:155], v[188:191]\n\t"
            "v_mfma_f32_16x16x32_bf16 v[188:191], v[132:135], v[156:159], v[188:191]\n\t"
            "v_mfma_f32_16x16x32_bf16 v[192:195], v[8:11], v[212:215], v[192:195]\n\t"
            "v_mfma_f32_16x16x32_bf16 v[192:195], v[12:15], v[224:227], v[192:195]\n\t"
            "v_mfma_f32_16x16x32_bf16 v[196:199], v[32:35], v[212:215], v[196:199]\n\t"
            "v_mfma_f32_16x16x32_bf16 v[196:199], v[36:39], v[224:227], v[196:199]\n\t"
            "v_mfma_f32_16x16x32_bf16 v[200:203], v[56:59], v[212:215], v[200:203]\n\t"
            "v_mfma_f32_16x16x32_bf16 v[200:203], v[60:63], v[224:227], v[200:203]\n\t"
            "v_mfma_f32_16x16x32_bf16 v[216:219], v[80:83], v[212:215], v[216:219]\n\t"
            "v_mfma_f32_16x16x32_bf16 v[216:219], v[84:87], v[224:227], v[216:219]\n\t"
            "v_mfma_f32_16x16x32_bf16 v[220:223], v[104:107], v[212:215], v[220:223]\n\t"
            "v_mfma_f32_16x16x32_bf16 v[220:223], v[108:111], v[224:227], v[220:223]\n\t"
            "v_mfma_f32_16x16x32_bf16 v[228:231], v[128:131], v[212:215], v[228:231]\n\t"
            "ds_read_b128 v[212:215], v232 offset:256\n\t"
            "v_mfma_f32_16x16x32_bf16 v[228:231], v[132:135], v[224:227], v[228:231]\n\t"
            "ds_read_b128 v[224:227], v233 offset:256\n\t"
            "s_waitcnt vmcnt(0)\n\t"
            "v_mfma_f32_16x16x32_bf16 v[168:171], v[16:19], v[160:163], v[168:171]\n\t"
            "v_mfma_f32_16x16x32_bf16 v[168:171], v[20:23], v[164:167], v[168:171]\n\t"
            "v_mfma_f32_16x16x32_bf16 v[176:179], v[64:67], v[160:163], v[176:179]\n\t"
            "v_mfma_f32_16x16x32_bf16 v[176:179], v[68:71], v[164:167], v[176:179]\n\t"
            "v_mfma_f32_16x16x32_bf16 v[180:183], v[88:91], v[160:163], v[180:183]\n\t"
            "v_mfma_f32_16x16x32_bf16 v[180:183], v[92:95], v[164:167], v[180:183]\n\t"
            "v_mfma_f32_16x16x32_bf16 v[172:175], v[40:43], v[160:163], v[172:175]\n\t"
            "v_mfma_f32_16x16x32_bf16 v[172:175], v[44:47], v[164:167], v[172:175]\n\t"
            "s_waitcnt lgkmcnt(5)\n\t"
            "s_nop 0\n\t"
            "v_mul_f32 v168, v236, v168\n\t"
            "v_mfma_f32_16x16x32_bf16 v[184:187], v[112:115], v[160:163], v[184:187]\n\t"
            "v_mul_f32 v169, v236, v169\n\t"
            "v_mul_f32 v170, v236, v170\n\t"
            "v_mfma_f32_16x16x32_bf16 v[184:187], v[116:119], v[164:167], v[184:187]\n\t"
            "v_mul_f32 v176, v236, v176\n\t"
            "v_mul_f32 v177, v236, v177\n\t"
            "v_mfma_f32_16x16x32_bf16 v[188:191], v[136:139], v[160:163], v[188:191]\n\t"
            "v_mul_f32 v171, v236, v171\n\t"
            "v_mul_f32 v178, v236, v178\n\t"
            "v_mfma_f32_16x16x32_bf16 v[188:191], v[140:143], v[164:167], v[188:191]\n\t"
            "v_mul_f32 v179, v236, v179\n\t"
            "v_cvt_pk_bf16_f32 v168, v168, v169\n\t"
            "s_waitcnt lgkmcnt(2)\n\t"
            "v_mfma_f32_16x16x32_bf16 v[192:195], v[16:19], v[204:207], v[192:195]\n\t"
            "v_cvt_pk_bf16_f32 v176, v176, v177\n\t"
            "v_cvt_pk_bf16_f32 v180, v180, v181\n\t"
            "v_mfma_f32_16x16x32_bf16 v[192:195], v[20:23], v[208:211], v[192:195]\n\t"
            "v_mul_f32 v172, v236, v172\n\t"
            "v_mul_f32 v173, v236, v173\n\t"
            "v_mfma_f32_16x16x32_bf16 v[196:199], v[40:43], v[204:207], v[196:199]\n\t"
            "v_mul_f32 v174, v236, v174\n\t"
            "v_mul_f32 v175, v236, v175\n\t"
            "v_mfma_f32_16x16x32_bf16 v[196:199], v[44:47], v[208:211], v[196:199]\n\t"
            "v_cvt_pk_bf16_f32 v169, v170, v171\n\t"
            "v_cvt_pk_bf16_f32 v177, v178, v179\n\t"
            "v_mfma_f32_16x16x32_bf16 v[200:203], v[64:67], v[204:207], v[200:203]\n\t"
            "v_cvt_pk_bf16_f32 v181, v182, v183\n\t"
            "v_cvt_pk_bf16_f32 v170, v172, v173\n\t"
            "v_mfma_f32_16x16x32_bf16 v[200:203], v[68:71], v[208:211], v[200:203]\n\t"
            "v_cvt_pk_bf16_f32 v171, v174, v175\n\t"
            "v_cvt_pk_bf16_f32 v182, v184, v185\n\t"
            "v_mfma_f32_16x16x32_bf16 v[216:219], v[88:91], v[204:207], v[216:219]\n\t"
            "v_cvt_pk_bf16_f32 v183, v186, v187\n\t"
            "v_cvt_pk_bf16_f32 v178, v188, v189\n\t"
            "v_mfma_f32_16x16x32_bf16 v[216:219], v[92:95], v[208:211], v[216:219]\n\t"
            "v_cvt_pk_bf16_f32 v179, v190, v191\n\t"
            "v_mul_f32 v236, 0x3e553b94, v236\n\t"
            "v_mfma_f32_16x16x32_bf16 v[220:223], v[112:115], v[204:207], v[220:223]\n\t"
            "v_add_u32 v184, s76, v239\n\t"
            "v_add_u32 v185, s74, v239\n\t"
            "v_mfma_f32_16x16x32_bf16 v[220:223], v[116:119], v[208:211], v[220:223]\n\t"
            "s_mov_b64 exec, -1\n\t"
            "ds_write_b128 v234, v[168:171]\n\t"
            "ds_write_b128 v234, v[180:183] offset:64\n\t"
            "ds_write_b64 v235, v[176:177] offset:128\n\t"
            "ds_write_b64 v235, v[178:179] offset:160\n\t"
            "s_mov_b64 exec, -1\n\t"
            "v_mfma_f32_16x16x16_bf16 v[172:175], v[176:177], v[178:179], 0\n\t"
            "v_mul_f32 v192, v237, v192\n\t"
            "v_mul_f32 v193, v237, v193\n\t"
            "v_mul_f32 v200, v237, v200\n\t"
            "v_mfma_f32_16x16x32_bf16 v[228:231], v[136:139], v[204:207], v[228:231]\n\t"
            "ds_read_b128 v[204:207], v232\n\t"
            "v_mul_f32 v201, v237, v201\n\t"
            "v_mfma_f32_16x16x32_bf16 v[228:231], v[140:143], v[208:211], v[228:231]\n\t"
            "ds_read_b128 v[208:211], v233\n\t"
            "v_mul_f32 v194, v237, v194\n\t"
            "v_mfma_f32_16x16x32_bf16 v[172:175], v[168:171], v[180:183], v[172:175]\n\t"
            "v_mul_f32 v195, v237, v195\n\t"
            "v_mul_f32 v202, v237, v202\n\t"
            "v_mul_f32 v203, v237, v203\n\t"
            "v_cvt_pk_bf16_f32 v192, v192, v193\n\t"
            "v_cvt_pk_bf16_f32 v200, v200, v201\n\t"
            "v_cvt_pk_bf16_f32 v216, v216, v217\n\t"
            "v_mul_f32 v196, v237, v196\n\t"
            "v_mul_f32 v197, v237, v197\n\t"
            "v_mul_f32 v198, v237, v198\n\t"
            "v_mul_f32 v199, v237, v199\n\t"
            "v_cvt_pk_bf16_f32 v193, v194, v195\n\t"
            "v_cvt_pk_bf16_f32 v201, v202, v203\n\t"
            "v_cvt_pk_bf16_f32 v217, v218, v219\n\t"
            "v_cvt_pk_bf16_f32 v194, v196, v197\n\t"
            "v_cvt_pk_bf16_f32 v195, v198, v199\n\t"
            "v_cvt_pk_bf16_f32 v218, v220, v221\n\t"
            "v_cvt_pk_bf16_f32 v219, v222, v223\n\t"
            "v_cvt_pk_bf16_f32 v202, v228, v229\n\t"
            "v_cvt_pk_bf16_f32 v203, v230, v231\n\t"
            "v_mul_f32 v237, 0x3e553b94, v237\n\t"
            "v_add_u32 v220, s76, v239\n\t"
            "v_mfma_f32_16x16x16_bf16 v[196:199], v[200:201], v[202:203], 0\n\t"
            "s_and_b32 exec_lo, s56, 0x11111111\n\t"
            "s_and_b32 exec_hi, s57, 0x11111111\n\t"
            "ds_write_b32 v184, v172 offset:0\n\t"
            "s_and_b32 exec_lo, s56, 0x22222222\n\t"
            "s_and_b32 exec_hi, s57, 0x22222222\n\t"
            "ds_write_b32 v184, v173 offset:0\n\t"
            "s_and_b32 exec_lo, s56, 0x44444444\n\t"
            "s_and_b32 exec_hi, s57, 0x44444444\n\t"
            "ds_write_b32 v184, v174 offset:0\n\t"
            "s_and_b32 exec_lo, s56, 0x88888888\n\t"
            "s_and_b32 exec_hi, s57, 0x88888888\n\t"
            "ds_write_b32 v184, v175 offset:0\n\t"
            "s_mov_b64 exec, s[56:57]\n\t"
            "ds_write_b32 v185, v236 offset:0\n\t"
            "s_mov_b64 exec, -1\n\t"
            "s_waitcnt lgkmcnt(5)\n\t"
            "v_mfma_f32_16x16x32_bf16 v[144:147], v[0:3], v[204:207], 0\n\t"
            "v_add_u32 v221, s74, v239\n\t"
            "v_mfma_f32_16x16x32_bf16 v[144:147], v[4:7], v[208:211], v[144:147]\n\t"
            "s_mov_b64 exec, s[54:55]\n\t"
            "ds_write_b128 v234, v[192:195] offset:12800\n\t"
            "ds_write_b128 v234, v[216:219] offset:12864\n\t"
            "ds_write_b64 v235, v[200:201] offset:12928\n\t"
            "ds_write_b64 v235, v[202:203] offset:12960\n\t"
            "s_mov_b64 exec, -1\n\t"
            "v_mfma_f32_16x16x32_bf16 v[148:151], v[28:31], v[208:211], 0\n\t"
            "v_add_u32 v235, s48, v248\n\t"
            "v_mfma_f32_16x16x32_bf16 v[148:151], v[24:27], v[204:207], v[148:151]\n\t"
            "ds_read_b128 v[228:231], v235 offset:18816\n\t"
            "v_mfma_f32_16x16x32_bf16 v[152:155], v[48:51], v[204:207], 0\n\t"
            "v_mfma_f32_16x16x32_bf16 v[152:155], v[52:55], v[208:211], v[152:155]\n\t"
            "v_mfma_f32_16x16x32_bf16 v[156:159], v[76:79], v[208:211], 0\n\t"
            "v_mfma_f32_16x16x32_bf16 v[156:159], v[72:75], v[204:207], v[156:159]\n\t"
            "v_mfma_f32_16x16x32_bf16 v[160:163], v[96:99], v[204:207], 0\n\t"
            "v_mfma_f32_16x16x32_bf16 v[160:163], v[100:103], v[208:211], v[160:163]\n\t"
            "v_mfma_f32_16x16x32_bf16 v[164:167], v[124:127], v[208:211], 0\n\t"
            "ds_read_b128 v[208:211], v233 offset:128\n\t"
            "s_waitcnt lgkmcnt(1)\n\t"
            "v_add_f32 v236, v228, v230\n\t"
            "v_mfma_f32_16x16x32_bf16 v[164:167], v[120:123], v[204:207], v[164:167]\n\t"
            "ds_read_b128 v[204:207], v232 offset:128\n\t"
            "v_mfma_f32_16x16x32_bf16 v[196:199], v[192:195], v[216:219], v[196:199]\n\t"
            "s_nop 7\n\t"
            "s_and_b32 exec_lo, s78, 0x11111111\n\t"
            "s_and_b32 exec_hi, s79, 0x11111111\n\t"
            "ds_write_b32 v220, v196 offset:12800\n\t"
            "s_and_b32 exec_lo, s78, 0x22222222\n\t"
            "s_and_b32 exec_hi, s79, 0x22222222\n\t"
            "ds_write_b32 v220, v197 offset:12800\n\t"
            "s_and_b32 exec_lo, s78, 0x44444444\n\t"
            "s_and_b32 exec_hi, s79, 0x44444444\n\t"
            "ds_write_b32 v220, v198 offset:12800\n\t"
            "s_and_b32 exec_lo, s78, 0x88888888\n\t"
            "s_and_b32 exec_hi, s79, 0x88888888\n\t"
            "ds_write_b32 v220, v199 offset:12800\n\t"
            "s_mov_b64 exec, s[78:79]\n\t"
            "ds_write_b32 v221, v237 offset:12800\n\t"
            "s_mov_b64 exec, -1\n\t"
            "s_waitcnt lgkmcnt(5)\n\t"
            "v_mfma_f32_16x16x32_bf16 v[144:147], v[8:11], v[204:207], v[144:147]\n\t"
            "v_add_f32 v237, v229, v231\n\t"
            "v_mfma_f32_16x16x32_bf16 v[144:147], v[12:15], v[208:211], v[144:147]\n\t"
            "ds_read_b128 v[228:231], v235 offset:19840\n\t"
            "v_mfma_f32_16x16x32_bf16 v[148:151], v[36:39], v[208:211], v[148:151]\n\t"
            "v_mfma_f32_16x16x32_bf16 v[148:151], v[32:35], v[204:207], v[148:151]\n\t"
            "v_mfma_f32_16x16x32_bf16 v[152:155], v[56:59], v[204:207], v[152:155]\n\t"
            "v_mfma_f32_16x16x32_bf16 v[152:155], v[60:63], v[208:211], v[152:155]\n\t"
            "v_mfma_f32_16x16x32_bf16 v[156:159], v[84:87], v[208:211], v[156:159]\n\t"
            "v_mfma_f32_16x16x32_bf16 v[156:159], v[80:83], v[204:207], v[156:159]\n\t"
            "v_mfma_f32_16x16x32_bf16 v[160:163], v[104:107], v[204:207], v[160:163]\n\t"
            "s_waitcnt lgkmcnt(0)\n\t"
            "v_add_f32 v228, v228, v230\n\t"
            "v_add_f32 v236, v236, v228\n\t"
            "v_mfma_f32_16x16x32_bf16 v[160:163], v[108:111], v[208:211], v[160:163]\n\t"
            "v_add_f32 v229, v229, v231\n\t"
            "v_mul_f32 v236, 0x3baaaaab, v236\n\t"
            "v_mfma_f32_16x16x32_bf16 v[164:167], v[132:135], v[208:211], v[164:167]\n\t"
            "ds_read_b128 v[208:211], v233 offset:6144\n\t"
            "v_add_f32 v237, v237, v229\n\t"
            "v_mfma_f32_16x16x32_bf16 v[164:167], v[128:131], v[204:207], v[164:167]\n\t"
            "ds_read_b128 v[204:207], v232 offset:6144\n\t"
            "v_mul_f32_e64 v236, v236, -v236\n\t"
            "v_mfma_f32_16x16x32_bf16 v[144:147], v[16:19], v[212:215], v[144:147]\n\t"
            "v_fmac_f32 v236, 0x3baaaaab, v237\n\t"
            "v_max_f32 v236, 0, v236\n\t"
            "v_mfma_f32_16x16x32_bf16 v[144:147], v[20:23], v[224:227], v[144:147]\n\t"
            "v_add_f32 v236, 0x3727c5ac, v236\n\t"
            "v_rsq_f32 v236, v236\n\t"
            "v_mfma_f32_16x16x32_bf16 v[148:151], v[44:47], v[224:227], v[148:151]\n\t"
            "v_mfma_f32_16x16x32_bf16 v[148:151], v[40:43], v[212:215], v[148:151]\n\t"
            "ds_write_b32 v249, v236\n\t"
            "v_mfma_f32_16x16x32_bf16 v[152:155], v[64:67], v[212:215], v[152:155]\n\t"
            "ds_read_b32 v236, v251\n\t"
            "ds_read_b32 v237, v251 offset:64\n\t"
            "v_mfma_f32_16x16x32_bf16 v[152:155], v[68:71], v[224:227], v[152:155]\n\t"
            "ds_read_b32 v238, v251 offset:128\n\t"
            "v_mfma_f32_16x16x32_bf16 v[156:159], v[92:95], v[224:227], v[156:159]\n\t"
            "v_mfma_f32_16x16x32_bf16 v[156:159], v[88:91], v[212:215], v[156:159]\n\t"
            "v_mfma_f32_16x16x32_bf16 v[160:163], v[112:115], v[212:215], v[160:163]\n\t"
            "v_mfma_f32_16x16x32_bf16 v[160:163], v[116:119], v[224:227], v[160:163]\n\t"
            "v_mfma_f32_16x16x32_bf16 v[164:167], v[140:143], v[224:227], v[164:167]\n\t"
            "ds_read_b128 v[224:227], v233 offset:6272\n\t"
            "v_mfma_f32_16x16x32_bf16 v[164:167], v[136:139], v[212:215], v[164:167]\n\t"
            "ds_read_b128 v[212:215], v232 offset:6272\n\t"
            "s_waitcnt lgkmcnt(4)\n\t"
            "v_mul_f32 v144, v236, v144\n\t"
            "v_mfma_f32_16x16x32_bf16 v[168:171], v[0:3], v[204:207], 0\n\t"
            "v_mul_f32 v145, v236, v145\n\t"
            "v_mul_f32 v152, v236, v152\n\t"
            "v_mfma_f32_16x16x32_bf16 v[168:171], v[4:7], v[208:211], v[168:171]\n\t"
            "v_mul_f32 v153, v236, v153\n\t"
            "v_mul_f32 v146, v236, v146\n\t"
            "v_mfma_f32_16x16x32_bf16 v[172:175], v[28:31], v[208:211], 0\n\t"
            "v_mul_f32 v147, v236, v147\n\t"
            "v_mul_f32 v154, v236, v154\n\t"
            "v_mfma_f32_16x16x32_bf16 v[172:175], v[24:27], v[204:207], v[172:175]\n\t"
            "v_mul_f32 v155, v236, v155\n\t"
            "v_cvt_pk_bf16_f32 v144, v144, v145\n\t"
            "v_mfma_f32_16x16x32_bf16 v[176:179], v[48:51], v[204:207], 0\n\t"
            "v_cvt_pk_bf16_f32 v152, v152, v153\n\t"
            "v_cvt_pk_bf16_f32 v156, v156, v157\n\t"
            "v_mfma_f32_16x16x32_bf16 v[176:179], v[52:55], v[208:211], v[176:179]\n\t"
            "v_mul_f32 v148, v236, v148\n\t"
            "v_mul_f32 v149, v236, v149\n\t"
            "v_mfma_f32_16x16x32_bf16 v[180:183], v[76:79], v[208:211], 0\n\t"
            "v_mul_f32 v150, v236, v150\n\t"
            "v_mul_f32 v151, v236, v151\n\t"
            "v_mfma_f32_16x16x32_bf16 v[180:183], v[72:75], v[204:207], v[180:183]\n\t"
            "v_cvt_pk_bf16_f32 v145, v146, v147\n\t"
            "v_cvt_pk_bf16_f32 v153, v154, v155\n\t"
            "v_mfma_f32_16x16x32_bf16 v[184:187], v[96:99], v[204:207], 0\n\t"
            "v_cvt_pk_bf16_f32 v157, v158, v159\n\t"
            "v_cvt_pk_bf16_f32 v146, v148, v149\n\t"
            "v_mfma_f32_16x16x32_bf16 v[184:187], v[100:103], v[208:211], v[184:187]\n\t"
            "v_cvt_pk_bf16_f32 v147, v150, v151\n\t"
            "v_cvt_pk_bf16_f32 v158, v160, v161\n\t"
            "v_mfma_f32_16x16x32_bf16 v[188:191], v[124:127], v[208:211], 0\n\t"
            "ds_read_b128 v[208:211], v233 offset:6400\n\t"
            "v_cvt_pk_bf16_f32 v159, v162, v163\n\t"
            "v_mfma_f32_16x16x32_bf16 v[188:191], v[120:123], v[204:207], v[188:191]\n\t"
            "ds_read_b128 v[204:207], v232 offset:6400\n\t"
            "v_cvt_pk_bf16_f32 v154, v164, v165\n\t"
            "s_waitcnt lgkmcnt(2)\n\t"
            "v_mfma_f32_16x16x32_bf16 v[168:171], v[8:11], v[212:215], v[168:171]\n\t"
            "v_cvt_pk_bf16_f32 v155, v166, v167\n\t"
            "v_mul_f32 v236, 0x3e553b94, v236\n\t"
            "v_mfma_f32_16x16x32_bf16 v[168:171], v[12:15], v[224:227], v[168:171]\n\t"
            "v_mfma_f32_16x16x32_bf16 v[172:175], v[36:39], v[224:227], v[172:175]\n\t"
            "v_mfma_f32_16x16x32_bf16 v[172:175], v[32:35], v[212:215], v[172:175]\n\t"
            "v_mfma_f32_16x16x32_bf16 v[176:179], v[56:59], v[212:215], v[176:179]\n\t"
            "v_mfma_f32_16x16x32_bf16 v[176:179], v[60:63], v[224:227], v[176:179]\n\t"
            "v_mfma_f32_16x16x32_bf16 v[180:183], v[84:87], v[224:227], v[180:183]\n\t"
            "v_mfma_f32_16x16x32_bf16 v[180:183], v[80:83], v[212:215], v[180:183]\n\t"
            "v_mfma_f32_16x16x32_bf16 v[184:187], v[104:107], v[212:215], v[184:187]\n\t"
            "v_mfma_f32_16x16x32_bf16 v[184:187], v[108:111], v[224:227], v[184:187]\n\t"
            "v_mfma_f32_16x16x32_bf16 v[188:191], v[132:135], v[224:227], v[188:191]\n\t"
            "ds_read_b128 v[224:227], v233 offset:12288\n\t"
            "v_mfma_f32_16x16x32_bf16 v[188:191], v[128:131], v[212:215], v[188:191]\n\t"
            "ds_read_b128 v[212:215], v232 offset:12288\n\t"
            "s_waitcnt lgkmcnt(2)\n\t"
            "v_mfma_f32_16x16x32_bf16 v[168:171], v[16:19], v[204:207], v[168:171]\n\t"
            "v_mfma_f32_16x16x32_bf16 v[168:171], v[20:23], v[208:211], v[168:171]\n\t"
            "v_mfma_f32_16x16x32_bf16 v[172:175], v[44:47], v[208:211], v[172:175]\n\t"
            "v_mfma_f32_16x16x32_bf16 v[172:175], v[40:43], v[204:207], v[172:175]\n\t"
            "v_mfma_f32_16x16x32_bf16 v[176:179], v[64:67], v[204:207], v[176:179]\n\t"
            "v_mfma_f32_16x16x32_bf16 v[176:179], v[68:71], v[208:211], v[176:179]\n\t"
            "v_mfma_f32_16x16x32_bf16 v[180:183], v[92:95], v[208:211], v[180:183]\n\t"
            "v_mfma_f32_16x16x32_bf16 v[180:183], v[88:91], v[204:207], v[180:183]\n\t"
            "v_mfma_f32_16x16x32_bf16 v[184:187], v[112:115], v[204:207], v[184:187]\n\t"
            "v_mfma_f32_16x16x32_bf16 v[184:187], v[116:119], v[208:211], v[184:187]\n\t"
            "v_mfma_f32_16x16x32_bf16 v[188:191], v[140:143], v[208:211], v[188:191]\n\t"
            "ds_read_b128 v[208:211], v233 offset:12416\n\t"
            "v_mfma_f32_16x16x32_bf16 v[188:191], v[136:139], v[204:207], v[188:191]\n\t"
            "ds_read_b128 v[204:207], v232 offset:12416\n\t"
            "s_waitcnt lgkmcnt(0)\n\t"
            "v_subrev_u32 v249, s75, v251\n\t"
            "v_add_u32 v249, 162816, v249\n\t"
            "s_mov_b32 s90, 2\n\t"
            "s_mov_b32 s81, 0\n\t"
            "s_mov_b32 s91, 0\n\t"
            "s_nop 7\n\t"
            ".p2align 6\n\t"
            "s_nop 0\n\t"
            "s_nop 0\n\t"
            "s_nop 0\n\t"
            ".Lwa_loop%=:\n\t"
            "s_waitcnt lgkmcnt(2)\n\t"
            "v_mfma_f32_16x16x32_bf16 v[192:195], v[0:3], v[212:215], 0\n\t"
            "s_bfe_u32 s49, s70, 0x30001\n\t"
            "s_lshl_b32 s49, s49, 4\n\t"
            "s_or_b32 s73, s77, s49\n\t"
            "v_mfma_f32_16x16x32_bf16 v[192:195], v[4:7], v[224:227], v[192:195]\n\t"
            "v_mad_u32_u24 v220, v246, s72, v253\n\t"
            "v_mad_u32_u24 v216, v246, s72, v252\n\t"
            "v_mfma_f32_16x16x32_bf16 v[148:151], v[28:31], v[224:227], 0\n\t"
            "ds_read_b64 v[222:223], v220 offset:128\n\t"
            "v_mov_b32_dpp v221, v220 quad_perm:[1,0,2,3] row_mask:0xf bank_mask:0xf\n\t"
            "v_mfma_f32_16x16x32_bf16 v[148:151], v[24:27], v[212:215], v[148:151]\n\t"
            "ds_read_b64 v[220:221], v221 offset:160\n\t"
            "v_mov_b32_dpp v217, v216 quad_perm:[1,0,2,3] row_mask:0xf bank_mask:0xf\n\t"
            "v_mfma_f32_16x16x32_bf16 v[196:199], v[48:51], v[212:215], 0\n\t"
            "v_mul_f32 v176, v237, v176\n\t"
            "v_mul_f32 v177, v237, v177\n\t"
            "v_mfma_f32_16x16x32_bf16 v[196:199], v[52:55], v[224:227], v[196:199]\n\t"
            "v_mul_f32 v178, v237, v178\n\t"
            "v_mul_f32 v179, v237, v179\n\t"
            "v_mfma_f32_16x16x32_bf16 v[200:203], v[76:79], v[224:227], 0\n\t"
            "v_cvt_pk_bf16_f32 v176, v176, v177\n\t"
            "v_mul_f32 v168, v237, v168\n\t"
            "v_mfma_f32_16x16x32_bf16 v[200:203], v[72:75], v[212:215], v[200:203]\n\t"
            "v_mul_f32 v169, v237, v169\n\t"
            "v_cvt_pk_bf16_f32 v177, v178, v179\n\t"
            "v_mfma_f32_16x16x32_bf16 v[160:163], v[96:99], v[212:215], 0\n\t"
            "v_mul_f32 v170, v237, v170\n\t"
            "v_mul_f32 v171, v237, v171\n\t"
            "v_mfma_f32_16x16x32_bf16 v[160:163], v[100:103], v[224:227], v[160:163]\n\t"
            "v_cvt_pk_bf16_f32 v168, v168, v169\n\t"
            "v_cvt_pk_bf16_f32 v180, v180, v181\n\t"
            "v_mfma_f32_16x16x32_bf16 v[164:167], v[124:127], v[224:227], 0\n\t"
            "ds_read_b128 v[224:227], v233 offset:12544\n\t"
            "v_cvt_pk_bf16_f32 v178, v188, v189\n\t"
            "v_mfma_f32_16x16x32_bf16 v[164:167], v[120:123], v[212:215], v[164:167]\n\t"
            "ds_read_b128 v[212:215], v232 offset:12544\n\t"
            "v_cvt_pk_bf16_f32 v179, v190, v191\n\t"
            "s_waitcnt lgkmcnt(4)\n\t"
            "v_mfma_f32_16x16x32_bf16 v[192:195], v[8:11], v[204:207], v[192:195]\n\t"
            "v_mul_f32 v172, v237, v172\n\t"
            "v_mul_f32 v173, v237, v173\n\t"
            "v_mfma_f32_16x16x32_bf16 v[192:195], v[12:15], v[208:211], v[192:195]\n\t"
            "v_mul_f32 v174, v237, v174\n\t"
            "v_mul_f32 v175, v237, v175\n\t"
            "v_mfma_f32_16x16x32_bf16 v[196:199], v[56:59], v[204:207], v[196:199]\n\t"
            "v_cvt_pk_bf16_f32 v169, v170, v171\n\t"
            "v_cvt_pk_bf16_f32 v181, v182, v183\n\t"
            "v_mfma_f32_16x16x32_bf16 v[196:199], v[60:63], v[208:211], v[196:199]\n\t"
            "v_cvt_pk_bf16_f32 v170, v172, v173\n\t"
            "v_cvt_pk_bf16_f32 v171, v174, v175\n\t"
            "v_mfma_f32_16x16x32_bf16 v[160:163], v[104:107], v[204:207], v[160:163]\n\t"
            "v_cvt_pk_bf16_f32 v182, v184, v185\n\t"
            "v_cvt_pk_bf16_f32 v183, v186, v187\n\t"
            "v_mfma_f32_16x16x32_bf16 v[160:163], v[108:111], v[208:211], v[160:163]\n\t"
            "v_mov_b32 v230, s76\n\t"
            "v_mov_b32 v239, s74\n\t"
            "v_mfma_f32_16x16x32_bf16 v[148:151], v[36:39], v[208:211], v[148:151]\n\t"
            "s_add_u32 s70, s70, 1\n\t"
            "s_add_u32 s72, s72, 800\n\t"
            "s_add_u32 s74, s74, 800\n\t"
            "s_add_u32 s76, s76, 800\n\t"
            "v_mfma_f32_16x16x32_bf16 v[148:151], v[32:35], v[204:207], v[148:151]\n\t"
            "s_add_u32 s77, s77, 384\n\t"
            "s_xor_b32 s48, s48, s47\n\t"
            "ds_read_b32 v230, v230\n\t"
            "v_mfma_f32_16x16x32_bf16 v[200:203], v[84:87], v[208:211], v[200:203]\n\t"
            "ds_read_b32 v239, v239\n\t"
            "v_mul_f32 v237, 0x3e553b94, v237\n\t"
            "v_mfma_f32_16x16x32_bf16 v[200:203], v[80:83], v[204:207], v[200:203]\n\t"
            "v_mfma_f32_16x16x32_bf16 v[164:167], v[132:135], v[208:211], v[164:167]\n\t"
            "v_mfma_f32_16x16x32_bf16 v[164:167], v[128:131], v[204:207], v[164:167]\n\t"
            "s_waitcnt lgkmcnt(2)\n\t"
            "v_mfma_f32_16x16x32_bf16 v[192:195], v[16:19], v[212:215], v[192:195]\n\t"
            "v_mfma_f32_16x16x32_bf16 v[192:195], v[20:23], v[224:227], v[192:195]\n\t"
            "v_mfma_f32_16x16x32_bf16 v[196:199], v[64:67], v[212:215], v[196:199]\n\t"
            "v_mfma_f32_16x16x32_bf16 v[196:199], v[68:71], v[224:227], v[196:199]\n\t"
            "v_mfma_f32_16x16x32_bf16 v[160:163], v[112:115], v[212:215], v[160:163]\n\t"
            "v_mfma_f32_16x16x32_bf16 v[160:163], v[116:119], v[224:227], v[160:163]\n\t"
            "v_mfma_f32_16x16x32_bf16 v[148:151], v[44:47], v[224:227], v[148:151]\n\t"
            "v_mfma_f32_16x16x32_bf16 v[148:151], v[40:43], v[212:215], v[148:151]\n\t"
            "s_nop 1\n\t"
            "v_mul_f32 v192, v238, v192\n\t"
            "v_mfma_f32_16x16x32_bf16 v[200:203], v[92:95], v[224:227], v[200:203]\n\t"
            "v_mul_f32 v193, v238, v193\n\t"
            "v_mul_f32 v194, v238, v194\n\t"
            "v_mfma_f32_16x16x32_bf16 v[200:203], v[88:91], v[212:215], v[200:203]\n\t"
            "v_mul_f32 v196, v238, v196\n\t"
            "v_mul_f32 v197, v238, v197\n\t"
            "v_mfma_f32_16x16x32_bf16 v[164:167], v[140:143], v[224:227], v[164:167]\n\t"
            "ds_read_b128 v[224:227], v217 offset:64\n\t"
            "ds_read_b128 v[216:219], v216\n\t"
            "v_mfma_f32_16x16x32_bf16 v[164:167], v[136:139], v[212:215], v[164:167]\n\t"
            "v_mul_f32 v198, v238, v198\n\t"
            "v_mul_f32 v199, v238, v199\n\t"
            "v_mfma_f32_16x16x32_bf16 v[172:175], v[220:223], v[152:155], 0\n\t"
            "s_setprio 0\n\t"
            "v_cvt_pk_bf16_f32 v196, v196, v197\n\t"
            "v_mul_f32 v195, v238, v195\n\t"
            "s_setprio 3\n\t"
            "v_mfma_f32_16x16x32_bf16 v[212:215], v[220:223], v[176:179], 0\n\t"
            "v_cvt_pk_bf16_f32 v192, v192, v193\n\t"
            "v_cvt_pk_bf16_f32 v197, v198, v199\n\t"
            "v_cvt_pk_bf16_f32 v200, v200, v201\n\t"
            "v_mul_f32 v148, v238, v148\n\t"
            "v_mfma_f32_16x16x16_bf16 v[184:187], v[152:153], v[178:179], 0\n\t"
            "v_mul_f32 v149, v238, v149\n\t"
            "v_mul_f32 v150, v238, v150\n\t"
            "v_mul_f32 v151, v238, v151\n\t"
            "v_mfma_f32_16x16x16_bf16 v[204:207], v[176:177], v[178:179], 0\n\t"
            "v_cvt_pk_bf16_f32 v193, v194, v195\n\t"
            "v_cvt_pk_bf16_f32 v201, v202, v203\n\t"
            "v_cvt_pk_bf16_f32 v194, v148, v149\n\t"
            "v_mfma_f32_16x16x16_bf16 v[188:191], v[196:197], v[178:179], 0\n\t"
            "v_cvt_pk_bf16_f32 v195, v150, v151\n\t"
            "v_cvt_pk_bf16_f32 v202, v160, v161\n\t"
            "v_cvt_pk_bf16_f32 v203, v162, v163\n\t"
            "s_waitcnt lgkmcnt(0)\n\t"
            "v_mfma_f32_16x16x32_bf16 v[172:175], v[216:219], v[156:159], v[172:175]\n\t"
            "v_cvt_pk_bf16_f32 v198, v164, v165\n\t"
            "v_cvt_pk_bf16_f32 v199, v166, v167\n\t"
            "v_mfma_f32_16x16x32_bf16 v[172:175], v[224:227], v[144:147], v[172:175]\n\t"
            "v_mul_f32 v238, 0x3e553b94, v238\n\t"
            "v_mfma_f32_16x16x16_bf16 v[148:151], v[152:153], v[154:155], 0\n\t"
            "v_mfma_f32_16x16x16_bf16 v[160:163], v[176:177], v[154:155], 0\n\t"
            "v_mfma_f32_16x16x16_bf16 v[164:167], v[196:197], v[154:155], 0\n\t"
            "v_mfma_f32_16x16x32_bf16 v[212:215], v[216:219], v[180:183], v[212:215]\n\t"
            "v_mfma_f32_16x16x32_bf16 v[212:215], v[224:227], v[168:171], v[212:215]\n\t"
            "s_nop 1\n\t"
            "v_mfma_f32_16x16x32_bf16 v[148:151], v[144:147], v[156:159], v[148:151]\n\t"
            "v_mfma_f32_16x16x32_bf16 v[184:187], v[144:147], v[180:183], v[184:187]\n\t"
            "v_mfma_f32_16x16x32_bf16 v[160:163], v[168:171], v[156:159], v[160:163]\n\t"
            "v_mfma_f32_16x16x32_bf16 v[204:207], v[168:171], v[180:183], v[204:207]\n\t"
            "v_mfma_f32_16x16x32_bf16 v[164:167], v[192:195], v[156:159], v[164:167]\n\t"
            "v_mfma_f32_16x16x32_bf16 v[188:191], v[192:195], v[180:183], v[188:191]\n\t"
            "v_cndmask_b32_e64 v235, v173, v213, s[60:61]\n\t"
            "v_mfma_f32_16x16x32_bf16 v[208:211], v[220:223], v[196:199], 0\n\t"
            "s_nop 0\n\t"
            "v_max3_f32 v228, v172, v148, v149\n\t"
            "v_max3_f32 v228, v228, v150, v151\n\t"
            "v_mfma_f32_16x16x32_bf16 v[208:211], v[216:219], v[200:203], v[208:211]\n\t"
            "v_max3_f32 v229, v212, v184, v185\n\t"
            "v_max3_f32 v229, v229, v186, v187\n\t"
            "v_mfma_f32_16x16x32_bf16 v[208:211], v[224:227], v[192:195], v[208:211]\n\t"
            "s_setprio 0\n\t"
            "v_max3_f32 v228, v228, v160, v161\n\t"
            "v_max3_f32 v229, v229, v204, v205\n\t"
            "v_max3_f32 v228, v228, v162, v163\n\t"
            "v_max3_f32 v229, v229, v206, v207\n\t"
            "v_max3_f32 v228, v228, v164, v165\n\t"
            "v_max3_f32 v229, v229, v188, v189\n\t"
            "v_max3_f32 v228, v228, v166, v167\n\t"
            "v_max3_f32 v229, v229, v190, v191\n\t"
            "v_max3_f32 v234, v173, v213, v209\n\t"
            "v_max_f32 v234, v234, v230\n\t"
            "v_permlane32_swap_b32 v228, v229\n\t"
            "v_max_f32 v228, v228, v229\n\t"
            "v_mov_b32 v231, v228\n\t"
            "v_max_f32_dpp v234, v234, v234 row_ror:8 row_mask:0xf bank_mask:0xf\n\t"
            "v_cndmask_b32_e64 v235, v235, v209, s[62:63]\n\t"
            "v_permlane16_swap_b32 v228, v231\n\t"
            "v_max_f32 v228, v228, v231\n\t"
            "v_mov_b32 v229, v228\n\t"
            "v_max_f32_dpp v234, v234, v234 row_ror:4 row_mask:0xf bank_mask:0xf\n\t"
            "v_cndmask_b32_e64 v235, v235, v230, s[64:65]\n\t"
            "v_permlane32_swap_b32 v228, v229\n\t"
            "v_mul_f32_e64 v229, -v229, v237\n\t"
            "v_fma_f32 v184, v184, v237, v229\n\t"
            "v_fma_f32 v185, v185, v237, v229\n\t"
            "v_exp_f32 v184, v184\n\t"
            "v_exp_f32 v185, v185\n\t"
            "v_fma_f32 v186, v186, v237, v229\n\t"
            "v_fma_f32 v187, v187, v237, v229\n\t"
            "v_exp_f32 v186, v186\n\t"
            "v_exp_f32 v187, v187\n\t"
            "v_fma_f32 v204, v204, v237, v229\n\t"
            "v_fma_f32 v205, v205, v237, v229\n\t"
            "v_fma_f32 v206, v206, v237, v229\n\t"
            "v_fma_f32 v207, v207, v237, v229\n\t"
            "v_exp_f32 v204, v204\n\t"
            "v_exp_f32 v205, v205\n\t"
            "v_exp_f32 v206, v206\n\t"
            "v_exp_f32 v207, v207\n\t"
            "v_cvt_pk_bf16_f32 v184, v184, v185\n\t"
            "v_mul_f32_e64 v228, -v228, v236\n\t"
            "v_cvt_pk_bf16_f32 v185, v186, v187\n\t"
            "v_fma_f32 v210, v172, v236, v228\n\t"
            "v_cvt_pk_bf16_f32 v186, v204, v205\n\t"
            "v_cvt_pk_bf16_f32 v187, v206, v207\n\t"
            "s_setprio 3\n\t"
            "v_mfma_f32_16x16x16_bf16 v[180:183], v[152:153], v[198:199], 0\n\t"
            "v_max_f32_dpp v234, v234, v234 row_ror:2 row_mask:0xf bank_mask:0xf\n\t"
            "s_mov_b64 exec, s[60:61]\n\t"
            "v_fma_f32 v210, v212, v237, v229\n\t"
            "s_mov_b64 exec, -1\n\t"
            "v_mfma_f32_16x16x16_bf16 v[204:207], v[176:177], v[198:199], 0\n\t"
            "v_max_f32_dpp v234, v234, v234 row_ror:1 row_mask:0xf bank_mask:0xf\n\t"
            "v_sub_f32 v235, v235, v234\n\t"
            "v_mul_f32 v235, v239, v235\n\t"
            "v_mfma_f32_16x16x16_bf16 v[172:175], v[196:197], v[198:199], 0\n\t"
            "v_exp_f32 v235, v235\n\t"
            "v_fma_f32 v148, v148, v236, v228\n\t"
            "v_mfma_f32_16x16x32_bf16 v[180:183], v[144:147], v[200:203], v[180:183]\n\t"
            "v_cvt_pk_bf16_f32 v235, v235, 0\n\t"
            "v_cndmask_b32_e64 v235, 0, v235, s[66:67]\n\t"
            "v_mfma_f32_16x16x32_bf16 v[204:207], v[168:171], v[200:203], v[204:207]\n\t"
            "ds_write_b16 v250, v235 offset:256\n\t"
            "v_mfma_f32_16x16x32_bf16 v[172:175], v[192:195], v[200:203], v[172:175]\n\t"
            "s_setprio 0\n\t"
            "v_fma_f32 v149, v149, v236, v228\n\t"
            "v_fma_f32 v164, v164, v236, v228\n\t"
            "v_fma_f32 v165, v165, v236, v228\n\t"
            "v_max3_f32 v230, v208, v180, v181\n\t"
            "v_max3_f32 v230, v230, v182, v183\n\t"
            "v_exp_f32 v148, v148\n\t"
            "v_max3_f32 v230, v230, v204, v205\n\t"
            "v_max3_f32 v230, v230, v206, v207\n\t"
            "v_max3_f32 v230, v230, v172, v173\n\t"
            "v_max3_f32 v230, v230, v174, v175\n\t"
            "v_mov_b32 v231, v230\n\t"
            "v_exp_f32 v149, v149\n\t"
            "v_fma_f32 v150, v150, v236, v228\n\t"
            "v_permlane32_swap_b32 v230, v231\n\t"
            "v_max_f32 v230, v230, v231\n\t"
            "v_mov_b32 v231, v230\n\t"
            "v_fma_f32 v151, v151, v236, v228\n\t"
            "v_exp_f32 v164, v164\n\t"
            "v_permlane16_swap_b32 v230, v231\n\t"
            "v_max_f32 v230, v230, v231\n\t"
            "v_mul_f32_e64 v230, -v230, v238\n\t"
            "s_mov_b64 exec, s[62:63]\n\t"
            "v_fma_f32 v210, v208, v238, v230\n\t"
            "s_mov_b64 exec, -1\n\t"
            "v_exp_f32 v165, v165\n\t"
            "v_fma_f32 v166, v166, v236, v228\n\t"
            "v_fma_f32 v167, v167, v236, v228\n\t"
            "v_exp_f32 v150, v150\n\t"
            "v_exp_f32 v151, v151\n\t"
            "v_fma_f32 v160, v160, v236, v228\n\t"
            "v_fma_f32 v161, v161, v236, v228\n\t"
            "v_fma_f32 v162, v162, v236, v228\n\t"
            "v_fma_f32 v163, v163, v236, v228\n\t"
            "v_exp_f32 v166, v166\n\t"
            "v_exp_f32 v167, v167\n\t"
            "v_exp_f32 v210, v210\n\t"
            "v_exp_f32 v160, v160\n\t"
            "v_exp_f32 v161, v161\n\t"
            "v_exp_f32 v162, v162\n\t"
            "v_exp_f32 v163, v163\n\t"
            "v_cvt_pk_bf16_f32 v148, v148, v149\n\t"
            "v_cvt_pk_bf16_f32 v164, v164, v165\n\t"
            "v_cvt_pk_bf16_f32 v149, v150, v151\n\t"
            "v_cvt_pk_bf16_f32 v165, v166, v167\n\t"
            "v_cvt_pk_bf16_f32 v210, v210, 0\n\t"
            "v_cvt_pk_bf16_f32 v150, v160, v161\n\t"
            "v_cvt_pk_bf16_f32 v151, v162, v163\n\t"
            "v_mov_b32 v167, 0\n\t"
            "v_cndmask_b32_e64 v166, 0, v210, s[58:59]\n\t"
            "v_fma_f32 v188, v188, v237, v229\n\t"
            "v_fma_f32 v189, v189, v237, v229\n\t"
            "v_fma_f32 v180, v180, v238, v230\n\t"
            "v_fma_f32 v181, v181, v238, v230\n\t"
            "v_fma_f32 v172, v172, v238, v230\n\t"
            "v_fma_f32 v173, v173, v238, v230\n\t"
            "v_exp_f32 v188, v188\n\t"
            "v_exp_f32 v189, v189\n\t"
            "v_fma_f32 v190, v190, v237, v229\n\t"
            "v_fma_f32 v191, v191, v237, v229\n\t"
            "v_exp_f32 v180, v180\n\t"
            "v_exp_f32 v181, v181\n\t"
            "v_fma_f32 v182, v182, v238, v230\n\t"
            "v_fma_f32 v183, v183, v238, v230\n\t"
            "v_exp_f32 v172, v172\n\t"
            "v_exp_f32 v173, v173\n\t"
            "v_fma_f32 v174, v174, v238, v230\n\t"
            "v_fma_f32 v175, v175, v238, v230\n\t"
            "v_exp_f32 v190, v190\n\t"
            "v_exp_f32 v191, v191\n\t"
            "v_exp_f32 v182, v182\n\t"
            "v_exp_f32 v183, v183\n\t"
            "v_fma_f32 v204, v204, v238, v230\n\t"
            "v_fma_f32 v205, v205, v238, v230\n\t"
            "v_fma_f32 v206, v206, v238, v230\n\t"
            "v_fma_f32 v207, v207, v238, v230\n\t"
            "v_exp_f32 v174, v174\n\t"
            "v_exp_f32 v175, v175\n\t"
            "v_exp_f32 v204, v204\n\t"
            "v_exp_f32 v205, v205\n\t"
            "v_exp_f32 v206, v206\n\t"
            "v_exp_f32 v207, v207\n\t"
            "v_cvt_pk_bf16_f32 v188, v188, v189\n\t"
            "v_cvt_pk_bf16_f32 v180, v180, v181\n\t"
            "v_cvt_pk_bf16_f32 v172, v172, v173\n\t"
            "v_cvt_pk_bf16_f32 v189, v190, v191\n\t"
            "v_cvt_pk_bf16_f32 v181, v182, v183\n\t"
            "v_cvt_pk_bf16_f32 v173, v174, v175\n\t"
            "v_cvt_pk_bf16_f32 v182, v204, v205\n\t"
            "v_cvt_pk_bf16_f32 v183, v206, v207\n\t"
            "v_cndmask_b32_e64 v190, 0, v210, s[60:61]\n\t"
            "v_cndmask_b32_e64 v174, 0, v210, s[62:63]\n\t"
            "v_mov_b32 v191, 0\n\t"
            "v_mov_b32 v175, 0\n\t"
            "s_cmp_ge_u32 s70, s80\n\t"
            "s_barrier\n\t"
            "s_setprio 2\n\t"
            "s_cbranch_scc1 .Lwa_h1%=\n\t"
            ".Lwa_r1%=:\n\t"
            "v_mfma_f32_16x16x32_bf16 v[200:203], v[240:243], v[148:151], 0\n\t"
            "ds_read_b64 v[176:177], v247\n\t"
            "ds_read_b64 v[196:197], v247 offset:1536\n\t"
            "v_mfma_f32_16x16x32_bf16 v[200:203], v[240:243], v[164:167], v[200:203]\n\t"
            "ds_read_b64 v[220:221], v247 offset:3072\n\t"
            "v_xor_b32 v234, s73, v254\n\t"
            "v_add_u32 v235, s45, v248\n\t"
            "v_mov_b32 v179, 0\n\t"
            "v_mov_b32 v199, 0\n\t"
            "v_mov_b32 v223, 0\n\t"
            "v_add_u32 v232, s48, v244\n\t"
            "v_xor_b32 v233, 64, v232\n\t"
            "v_rcp_f32 v201, v200\n\t"
            "v_xor_b32 v249, 0x100, v249\n\t"
            "ds_read_b128 v[168:171], v235\n\t"
            "ds_read_u16 v178, v234\n\t"
            "ds_read_b128 v[192:195], v235 offset:1536\n\t"
            "ds_read_u16 v198, v234 offset:128\n\t"
            "ds_read_b128 v[216:219], v235 offset:3072\n\t"
            "ds_read_u16 v222, v234 offset:256\n\t"
            "ds_read_b128 v[204:207], v232\n\t"
            "ds_read_b128 v[208:211], v233\n\t"
            "ds_read_b32 v236, v249\n\t"
            "ds_read_b32 v237, v249 offset:64\n\t"
            "ds_read_b32 v238, v249 offset:128\n\t"
            "v_mad_u32_u24 v234, v246, s75, v253\n\t"
            "s_waitcnt lgkmcnt(9)\n\t"
            "v_mfma_f32_16x16x32_bf16 v[224:227], v[168:171], v[148:151], 0\n\t"
            "v_mfma_f32_16x16x32_bf16 v[224:227], v[176:179], v[164:167], v[224:227]\n\t"
            "s_waitcnt lgkmcnt(7)\n\t"
            "v_mfma_f32_16x16x32_bf16 v[212:215], v[192:195], v[148:151], 0\n\t"
            "v_mfma_f32_16x16x32_bf16 v[212:215], v[196:199], v[164:167], v[212:215]\n\t"
            "s_waitcnt lgkmcnt(5)\n\t"
            "v_mfma_f32_16x16x32_bf16 v[148:151], v[216:219], v[148:151], 0\n\t"
            "v_mfma_f32_16x16x32_bf16 v[148:151], v[220:223], v[164:167], v[148:151]\n\t"
            "s_waitcnt lgkmcnt(3)\n\t"
            "v_mfma_f32_16x16x32_bf16 v[144:147], v[0:3], v[204:207], 0\n\t"
            "v_mfma_f32_16x16x32_bf16 v[144:147], v[4:7], v[208:211], v[144:147]\n\t"
            "v_mul_f32 v224, v201, v224\n\t"
            "v_mfma_f32_16x16x32_bf16 v[152:155], v[48:51], v[204:207], 0\n\t"
            "v_mul_f32 v225, v201, v225\n\t"
            "v_mul_f32 v226, v201, v226\n\t"
            "v_mfma_f32_16x16x32_bf16 v[152:155], v[52:55], v[208:211], v[152:155]\n\t"
            "v_mul_f32 v212, v201, v212\n\t"
            "v_mul_f32 v213, v201, v213\n\t"
            "v_mfma_f32_16x16x32_bf16 v[156:159], v[76:79], v[208:211], 0\n\t"
            "v_mul_f32 v227, v201, v227\n\t"
            "v_cvt_pk_bf16_f32 v224, v224, v225\n\t"
            "v_mfma_f32_16x16x32_bf16 v[156:159], v[72:75], v[204:207], v[156:159]\n\t"
            "v_mul_f32 v214, v201, v214\n\t"
            "v_mul_f32 v215, v201, v215\n\t"
            "v_mfma_f32_16x16x32_bf16 v[160:163], v[96:99], v[204:207], 0\n\t"
            "v_cvt_pk_bf16_f32 v212, v212, v213\n\t"
            "v_cvt_pk_bf16_f32 v225, v226, v227\n\t"
            "v_mfma_f32_16x16x32_bf16 v[160:163], v[100:103], v[208:211], v[160:163]\n\t"
            "v_cvt_pk_bf16_f32 v213, v214, v215\n\t"
            "v_mul_f32 v148, v201, v148\n\t"
            "v_mfma_f32_16x16x32_bf16 v[164:167], v[124:127], v[208:211], 0\n\t"
            "v_mul_f32 v149, v201, v149\n\t"
            "v_mul_f32 v150, v201, v150\n\t"
            "v_mfma_f32_16x16x32_bf16 v[164:167], v[120:123], v[204:207], v[164:167]\n\t"
            "ds_write_b64 v245, v[224:225]\n\t"
            "v_mfma_f32_16x16x32_bf16 v[224:227], v[168:171], v[184:187], 0\n\t"
            "v_mul_f32 v151, v201, v151\n\t"
            "v_cvt_pk_bf16_f32 v148, v148, v149\n\t"
            "v_mfma_f32_16x16x32_bf16 v[224:227], v[176:179], v[188:191], v[224:227]\n\t"
            "ds_write_b64 v245, v[212:213] offset:128\n\t"
            "v_mfma_f32_16x16x32_bf16 v[200:203], v[240:243], v[184:187], 0\n\t"
            "v_cvt_pk_bf16_f32 v149, v150, v151\n\t"
            "v_mfma_f32_16x16x32_bf16 v[200:203], v[240:243], v[188:191], v[200:203]\n\t"
            "ds_write_b64 v245, v[148:149] offset:256\n\t"
            "v_mfma_f32_16x16x32_bf16 v[212:215], v[192:195], v[184:187], 0\n\t"
            "v_mfma_f32_16x16x32_bf16 v[212:215], v[196:199], v[188:191], v[212:215]\n\t"
            "v_mfma_f32_16x16x32_bf16 v[184:187], v[216:219], v[184:187], 0\n\t"
            "v_mfma_f32_16x16x32_bf16 v[184:187], v[220:223], v[188:191], v[184:187]\n\t"
            "v_mfma_f32_16x16x32_bf16 v[148:151], v[28:31], v[208:211], 0\n\t"
            "ds_read_b128 v[208:211], v233 offset:128\n\t"
            "v_mfma_f32_16x16x32_bf16 v[148:151], v[24:27], v[204:207], v[148:151]\n\t"
            "ds_read_b128 v[204:207], v232 offset:128\n\t"
            "v_rcp_f32 v201, v200\n\t"
            "v_mfma_f32_16x16x32_bf16 v[188:191], v[192:195], v[180:183], 0\n\t"
            "v_mul_f32 v224, v201, v224\n\t"
            "v_mul_f32 v225, v201, v225\n\t"
            "v_mfma_f32_16x16x32_bf16 v[188:191], v[196:199], v[172:175], v[188:191]\n\t"
            "v_mul_f32 v226, v201, v226\n\t"
            "v_mul_f32 v227, v201, v227\n\t"
            "v_mul_f32 v212, v201, v212\n\t"
            "v_mul_f32 v213, v201, v213\n\t"
            "v_mul_f32 v214, v201, v214\n\t"
            "v_mul_f32 v215, v201, v215\n\t"
            "v_mul_f32 v184, v201, v184\n\t"
            "v_mul_f32 v185, v201, v185\n\t"
            "v_mul_f32 v186, v201, v186\n\t"
            "v_mul_f32 v187, v201, v187\n\t"
            "v_mfma_f32_16x16x32_bf16 v[200:203], v[240:243], v[180:183], 0\n\t"
            "v_cvt_pk_bf16_f32 v224, v224, v225\n\t"
            "v_cvt_pk_bf16_f32 v225, v226, v227\n\t"
            "v_mfma_f32_16x16x32_bf16 v[200:203], v[240:243], v[172:175], v[200:203]\n\t"
            "ds_write_b64 v245, v[224:225] offset:6144\n\t"
            "v_mfma_f32_16x16x32_bf16 v[224:227], v[168:171], v[180:183], 0\n\t"
            "v_cvt_pk_bf16_f32 v184, v184, v185\n\t"
            "v_cvt_pk_bf16_f32 v185, v186, v187\n\t"
            "v_mfma_f32_16x16x32_bf16 v[224:227], v[176:179], v[172:175], v[224:227]\n\t"
            "ds_read_b64 v[186:187], v234 offset:288\n\t"
            "ds_write_b64 v245, v[184:185] offset:6400\n\t"
            "s_waitcnt lgkmcnt(3)\n\t"
            "v_mfma_f32_16x16x32_bf16 v[144:147], v[8:11], v[204:207], v[144:147]\n\t"
            "ds_read_b64 v[184:185], v234 offset:256\n\t"
            "v_rcp_f32 v201, v200\n\t"
            "v_mfma_f32_16x16x32_bf16 v[144:147], v[12:15], v[208:211], v[144:147]\n\t"
            "v_mul_f32 v188, v201, v188\n\t"
            "v_mul_f32 v189, v201, v189\n\t"
            "v_mfma_f32_16x16x32_bf16 v[148:151], v[36:39], v[208:211], v[148:151]\n\t"
            "v_mul_f32 v190, v201, v190\n\t"
            "v_mul_f32 v191, v201, v191\n\t"
            "v_mfma_f32_16x16x32_bf16 v[148:151], v[32:35], v[204:207], v[148:151]\n\t"
            "v_cvt_pk_bf16_f32 v188, v188, v189\n\t"
            "v_cvt_pk_bf16_f32 v189, v190, v191\n\t"
            "v_mfma_f32_16x16x32_bf16 v[152:155], v[56:59], v[204:207], v[152:155]\n\t"
            "ds_read_b64 v[190:191], v234 offset:352\n\t"
            "v_mul_f32 v224, v201, v224\n\t"
            "v_mfma_f32_16x16x32_bf16 v[152:155], v[60:63], v[208:211], v[152:155]\n\t"
            "ds_write_b64 v245, v[188:189] offset:12416\n\t"
            "v_mfma_f32_16x16x32_bf16 v[156:159], v[84:87], v[208:211], v[156:159]\n\t"
            "ds_read_b64 v[188:189], v234 offset:320\n\t"
            "v_mul_f32 v225, v201, v225\n\t"
            "v_mfma_f32_16x16x32_bf16 v[156:159], v[80:83], v[204:207], v[156:159]\n\t"
            "v_cvt_pk_bf16_f32 v212, v212, v213\n\t"
            "v_mul_f32 v226, v201, v226\n\t"
            "v_mfma_f32_16x16x32_bf16 v[160:163], v[104:107], v[204:207], v[160:163]\n\t"
            "v_mul_f32 v227, v201, v227\n\t"
            "v_cvt_pk_bf16_f32 v224, v224, v225\n\t"
            "v_mfma_f32_16x16x32_bf16 v[160:163], v[108:111], v[208:211], v[160:163]\n\t"
            "v_cvt_pk_bf16_f32 v213, v214, v215\n\t"
            "v_cvt_pk_bf16_f32 v225, v226, v227\n\t"
            "v_mfma_f32_16x16x32_bf16 v[164:167], v[132:135], v[208:211], v[164:167]\n\t"
            "ds_read_b128 v[208:211], v233 offset:6144\n\t"
            "v_mfma_f32_16x16x32_bf16 v[164:167], v[128:131], v[204:207], v[164:167]\n\t"
            "ds_read_b128 v[204:207], v232 offset:6144\n\t"
            "ds_write_b64 v245, v[212:213] offset:6272\n\t"
            "v_mfma_f32_16x16x32_bf16 v[180:183], v[216:219], v[180:183], 0\n\t"
            "ds_read_b128 v[212:215], v232 offset:256\n\t"
            "v_mfma_f32_16x16x32_bf16 v[180:183], v[220:223], v[172:175], v[180:183]\n\t"
            "ds_write_b64 v245, v[224:225] offset:12288\n\t"
            "s_waitcnt lgkmcnt(5)\n\t"
            "v_mfma_f32_16x16x32_bf16 v[168:171], v[184:187], v[168:171], 0\n\t"
            "ds_read_b128 v[224:227], v233 offset:256\n\t"
            "v_mfma_f32_16x16x32_bf16 v[168:171], v[188:191], v[176:179], v[168:171]\n\t"
            "v_mfma_f32_16x16x32_bf16 v[192:195], v[184:187], v[192:195], 0\n\t"
            "v_mfma_f32_16x16x32_bf16 v[192:195], v[188:191], v[196:199], v[192:195]\n\t"
            "s_nop 0\n\t"
            "v_mul_f32 v180, v201, v180\n\t"
            "v_mfma_f32_16x16x32_bf16 v[216:219], v[184:187], v[216:219], 0\n\t"
            "v_mul_f32 v181, v201, v181\n\t"
            "v_mul_f32 v182, v201, v182\n\t"
            "v_mfma_f32_16x16x32_bf16 v[216:219], v[188:191], v[220:223], v[216:219]\n\t"
            "v_mul_f32 v183, v201, v183\n\t"
            "v_cvt_pk_bf16_f32 v180, v180, v181\n\t"
            "v_mfma_f32_16x16x32_bf16 v[200:203], v[240:243], v[184:187], 0\n\t"
            "v_cvt_pk_bf16_f32 v181, v182, v183\n\t"
            "v_mfma_f32_16x16x32_bf16 v[200:203], v[240:243], v[188:191], v[200:203]\n\t"
            "ds_write_b64 v245, v[180:181] offset:12544\n\t"
            "s_waitcnt lgkmcnt(1)\n\t"
            "v_mfma_f32_16x16x32_bf16 v[144:147], v[16:19], v[212:215], v[144:147]\n\t"
            "v_mfma_f32_16x16x32_bf16 v[144:147], v[20:23], v[224:227], v[144:147]\n\t"
            "v_mfma_f32_16x16x32_bf16 v[148:151], v[44:47], v[224:227], v[148:151]\n\t"
            "v_mfma_f32_16x16x32_bf16 v[148:151], v[40:43], v[212:215], v[148:151]\n\t"
            "v_mfma_f32_16x16x32_bf16 v[152:155], v[64:67], v[212:215], v[152:155]\n\t"
            "s_nop 0\n\t"
            "v_cndmask_b32_e64 v203, v168, v192, s[60:61]\n\t"
            "v_mfma_f32_16x16x32_bf16 v[152:155], v[68:71], v[224:227], v[152:155]\n\t"
            "v_rcp_f32 v201, v200\n\t"
            "v_mfma_f32_16x16x32_bf16 v[156:159], v[92:95], v[224:227], v[156:159]\n\t"
            "v_cndmask_b32_e64 v203, v203, v216, s[62:63]\n\t"
            "v_xad_u32 v202, s73, v254, v255\n\t"
            "v_mfma_f32_16x16x32_bf16 v[156:159], v[88:91], v[212:215], v[156:159]\n\t"
            "v_mul_f32_dpp v203, v201, v203 quad_perm:[0,0,0,0] row_mask:0xf bank_mask:0xf\n\t"
            "v_cvt_pk_bf16_f32 v203, v203, 0\n\t"
            "v_mfma_f32_16x16x32_bf16 v[160:163], v[112:115], v[212:215], v[160:163]\n\t"
            "v_mul_f32 v144, v236, v144\n\t"
            "v_mul_f32 v145, v236, v145\n\t"
            "v_mfma_f32_16x16x32_bf16 v[160:163], v[116:119], v[224:227], v[160:163]\n\t"
            "s_mov_b64 exec, s[68:69]\n\t"
            "ds_write_b16 v202, v203\n\t"
            "s_mov_b64 exec, 1\n\t"
            "ds_add_u32 v253, v246 offset:7856\n\t"
            "s_mov_b64 exec, -1\n\t"
            "v_mfma_f32_16x16x32_bf16 v[164:167], v[140:143], v[224:227], v[164:167]\n\t"
            "ds_read_b128 v[224:227], v233 offset:6272\n\t"
            "v_mul_f32 v152, v236, v152\n\t"
            "v_mfma_f32_16x16x32_bf16 v[164:167], v[136:139], v[212:215], v[164:167]\n\t"
            "ds_read_b128 v[212:215], v232 offset:6272\n\t"
            "v_mul_f32 v153, v236, v153\n\t"
            "v_mfma_f32_16x16x32_bf16 v[168:171], v[0:3], v[204:207], 0\n\t"
            "v_mul_f32 v146, v236, v146\n\t"
            "v_mul_f32 v147, v236, v147\n\t"
            "v_mfma_f32_16x16x32_bf16 v[168:171], v[4:7], v[208:211], v[168:171]\n\t"
            "v_mul_f32 v154, v236, v154\n\t"
            "v_mul_f32 v155, v236, v155\n\t"
            "v_mfma_f32_16x16x32_bf16 v[172:175], v[28:31], v[208:211], 0\n\t"
            "v_cvt_pk_bf16_f32 v144, v144, v145\n\t"
            "v_cvt_pk_bf16_f32 v152, v152, v153\n\t"
            "v_mfma_f32_16x16x32_bf16 v[172:175], v[24:27], v[204:207], v[172:175]\n\t"
            "v_cvt_pk_bf16_f32 v156, v156, v157\n\t"
            "v_mul_f32 v148, v236, v148\n\t"
            "v_mfma_f32_16x16x32_bf16 v[176:179], v[48:51], v[204:207], 0\n\t"
            "v_mul_f32 v149, v236, v149\n\t"
            "v_mul_f32 v150, v236, v150\n\t"
            "v_mfma_f32_16x16x32_bf16 v[176:179], v[52:55], v[208:211], v[176:179]\n\t"
            "v_mul_f32 v151, v236, v151\n\t"
            "v_cvt_pk_bf16_f32 v145, v146, v147\n\t"
            "v_mfma_f32_16x16x32_bf16 v[180:183], v[76:79], v[208:211], 0\n\t"
            "v_cvt_pk_bf16_f32 v153, v154, v155\n\t"
            "v_cvt_pk_bf16_f32 v157, v158, v159\n\t"
            "v_mfma_f32_16x16x32_bf16 v[180:183], v[72:75], v[204:207], v[180:183]\n\t"
            "v_cvt_pk_bf16_f32 v146, v148, v149\n\t"
            "v_cvt_pk_bf16_f32 v147, v150, v151\n\t"
            "v_mfma_f32_16x16x32_bf16 v[184:187], v[96:99], v[204:207], 0\n\t"
            "v_cvt_pk_bf16_f32 v158, v160, v161\n\t"
            "v_cvt_pk_bf16_f32 v159, v162, v163\n\t"
            "v_mfma_f32_16x16x32_bf16 v[184:187], v[100:103], v[208:211], v[184:187]\n\t"
            "v_cvt_pk_bf16_f32 v154, v164, v165\n\t"
            "v_cvt_pk_bf16_f32 v155, v166, v167\n\t"
            "v_mfma_f32_16x16x32_bf16 v[188:191], v[124:127], v[208:211], 0\n\t"
            "ds_read_b128 v[208:211], v233 offset:6400\n\t"
            "v_mul_f32 v236, 0x3e553b94, v236\n\t"
            "v_mfma_f32_16x16x32_bf16 v[188:191], v[120:123], v[204:207], v[188:191]\n\t"
            "ds_read_b128 v[204:207], v232 offset:6400\n\t"
            "s_waitcnt lgkmcnt(2)\n\t"
            "v_mfma_f32_16x16x32_bf16 v[168:171], v[8:11], v[212:215], v[168:171]\n\t"
            "v_mfma_f32_16x16x32_bf16 v[168:171], v[12:15], v[224:227], v[168:171]\n\t"
            "v_mfma_f32_16x16x32_bf16 v[172:175], v[36:39], v[224:227], v[172:175]\n\t"
            "v_mfma_f32_16x16x32_bf16 v[172:175], v[32:35], v[212:215], v[172:175]\n\t"
            "v_mfma_f32_16x16x32_bf16 v[176:179], v[56:59], v[212:215], v[176:179]\n\t"
            "v_mfma_f32_16x16x32_bf16 v[176:179], v[60:63], v[224:227], v[176:179]\n\t"
            "v_mfma_f32_16x16x32_bf16 v[180:183], v[84:87], v[224:227], v[180:183]\n\t"
            "v_mfma_f32_16x16x32_bf16 v[180:183], v[80:83], v[212:215], v[180:183]\n\t"
            "v_mfma_f32_16x16x32_bf16 v[184:187], v[104:107], v[212:215], v[184:187]\n\t"
            "v_mfma_f32_16x16x32_bf16 v[184:187], v[108:111], v[224:227], v[184:187]\n\t"
            "v_mfma_f32_16x16x32_bf16 v[188:191], v[132:135], v[224:227], v[188:191]\n\t"
            "ds_read_b128 v[224:227], v233 offset:12288\n\t"
            "v_mfma_f32_16x16x32_bf16 v[188:191], v[128:131], v[212:215], v[188:191]\n\t"
            "ds_read_b128 v[212:215], v232 offset:12288\n\t"
            "s_waitcnt lgkmcnt(2)\n\t"
            "v_mfma_f32_16x16x32_bf16 v[168:171], v[16:19], v[204:207], v[168:171]\n\t"
            "v_mfma_f32_16x16x32_bf16 v[168:171], v[20:23], v[208:211], v[168:171]\n\t"
            "v_mfma_f32_16x16x32_bf16 v[172:175], v[44:47], v[208:211], v[172:175]\n\t"
            "v_mfma_f32_16x16x32_bf16 v[172:175], v[40:43], v[204:207], v[172:175]\n\t"
            "v_mfma_f32_16x16x32_bf16 v[176:179], v[64:67], v[204:207], v[176:179]\n\t"
            "v_mfma_f32_16x16x32_bf16 v[176:179], v[68:71], v[208:211], v[176:179]\n\t"
            "v_mfma_f32_16x16x32_bf16 v[180:183], v[92:95], v[208:211], v[180:183]\n\t"
            "v_mfma_f32_16x16x32_bf16 v[180:183], v[88:91], v[204:207], v[180:183]\n\t"
            "v_mfma_f32_16x16x32_bf16 v[184:187], v[112:115], v[204:207], v[184:187]\n\t"
            "v_mfma_f32_16x16x32_bf16 v[184:187], v[116:119], v[208:211], v[184:187]\n\t"
            "v_mfma_f32_16x16x32_bf16 v[188:191], v[140:143], v[208:211], v[188:191]\n\t"
            "ds_read_b128 v[208:211], v233 offset:12416\n\t"
            "v_mfma_f32_16x16x32_bf16 v[188:191], v[136:139], v[204:207], v[188:191]\n\t"
            "ds_read_b128 v[204:207], v232 offset:12416\n\t"
            "s_cmp_eq_u32 s91, 0\n\t"
            "s_cbranch_scc1 .Lwa_loop%=\n\t"
            "s_mov_b32 s91, 0\n\t"
            "s_waitcnt vmcnt(0)\n\t"
            "s_cmp_eq_u32 s81, 0\n\t"
            "s_cbranch_scc1 .Lwa_h6%=\n\t"
            "s_mov_b32 s89, 0x10000\n\t"
            ".Lwa_h4%=:\n\t"
            "v_readfirstlane_b32 s49, v228\n\t"
            "v_readfirstlane_b32 vcc_lo, v229\n\t"
            "s_xor_b32 s49, s49, s82\n\t"
            "s_xor_b32 vcc_lo, vcc_lo, s83\n\t"
            "s_or_b32 s49, s49, vcc_lo\n\t"
            "s_cbranch_scc0 .Lwa_h5%=\n\t"
            "s_sub_u32 s89, s89, 1\n\t"
            "s_cbranch_scc1 .Lwa_h5%=\n\t"
            "s_sleep 2\n\t"
            "buffer_load_dwordx3 v[228:230], off, s[84:87], s81 offset:1216 sc1\n\t"
            "s_waitcnt vmcnt(0)\n\t"
            "s_branch .Lwa_h4%=\n\t"
            ".Lwa_h5%=:\n\t"
            "s_lshl_b32 s49, s70, 2\n\t"
            "s_add_u32 s49, s49, 163520\n\t"
            "v_mov_b32 v228, s49\n\t"
            "s_mul_i32 s49, s70, 800\n\t"
            "s_add_u32 m0, s49, 113760\n\t"
            "ds_write_b32 v228, v230 offset:12\n\t"
            "s_mov_b32 exec_hi, 0x3ffff\n\t"
            "buffer_load_dwordx4 v248, s[84:87], s81 offen sc1 lds\n\t"
            "s_mul_i32 s49, s70, 384\n\t"
            "s_add_u32 m0, s49, 130176\n\t"
            "s_add_u32 s81, s81, 800\n\t"
            "s_mov_b64 exec, 0xffffff\n\t"
            "buffer_load_dwordx4 v248, s[84:87], s81 offen sc1 lds\n\t"
            "s_mov_b64 exec, -1\n\t"
            "s_mov_b32 s81, 0\n\t"
            "s_waitcnt lgkmcnt(0)\n\t"
            ".Lwa_h6%=:\n\t"
            "s_cmp_eq_u32 s90, 0\n\t"
            "s_cbranch_scc1 .Lwa_loop%=\n\t"
            "v_readfirstlane_b32 s49, v231\n\t"
            "s_sub_u32 s90, s90, 1\n\t"
            "s_cmp_lt_u32 s49, 209\n\t"
            "s_cselect_b32 s90, s90, 0\n\t"
            "s_cbranch_scc0 .Lwa_loop%=\n\t"
            "s_mul_i32 s81, s49, 1280\n\t"
            "s_add_u32 s81, s81, 303104\n\t"
            "s_branch .Lwa_loop%=\n\t"
            ".Lwa_h1%=:\n\t"
            "s_cmp_lt_u32 s70, 20\n\t"
            "s_cbranch_scc1 .Lwa_h2%=\n\t"
            "s_lshl_b32 s49, s70, 2\n\t"
            "s_add_u32 s49, s49, 163520\n\t"
            "v_mov_b32 v228, s49\n\t"
            "ds_read_b32 v228, v228\n\t"
            "s_waitcnt lgkmcnt(0)\n\t"
            "v_readfirstlane_b32 s49, v228\n\t"
            "s_cmp_eq_u32 s49, 0x7ffffff\n\t"
            "s_cbranch_scc1 .Lwa_last%=\n\t"
            "s_branch .Lwa_r1%=\n\t"
            ".Lwa_h2%=:\n\t"
            "s_waitcnt vmcnt(0)\n\t"
            "s_or_b32 s91, s81, s90\n\t"
            "s_cmp_eq_u32 s81, 0\n\t"
            "s_cbranch_scc1 .Lwa_h3%=\n\t"
            "buffer_load_dwordx3 v[228:230], off, s[84:87], s81 offset:1216 sc1\n\t"
            ".Lwa_h3%=:\n\t"
            "s_cmp_eq_u32 s90, 0\n\t"
            "s_cbranch_scc1 .Lwa_r1%=\n\t"
            "v_mov_b32 v231, 1\n\t"
            "s_mov_b32 s49, 301184\n\t"
            "s_mov_b64 exec, 1\n\t"
            "buffer_atomic_add v231, off, s[84:87], s49 sc0\n\t"
            "s_mov_b64 exec, -1\n\t"
            "s_branch .Lwa_r1%=\n\t"
            ".Lwa_last%=:\n\t"
            "s_setprio 2\n\t"
            "s_waitcnt lgkmcnt(0)\n\t"
            "s_nop 7\n\t"
            "v_mfma_f32_16x16x32_bf16 v[156:159], v[240:243], v[184:187], 0\n\t"
            "ds_read_b64 v[176:177], v247\n\t"
            "ds_read_b64 v[196:197], v247 offset:1536\n\t"
            "v_mfma_f32_16x16x32_bf16 v[156:159], v[240:243], v[188:191], v[156:159]\n\t"
            "ds_read_b64 v[220:221], v247 offset:3072\n\t"
            "v_xor_b32 v234, s73, v254\n\t"
            "v_mfma_f32_16x16x32_bf16 v[228:231], v[240:243], v[148:151], 0\n\t"
            "ds_read_u16 v178, v234\n\t"
            "ds_read_u16 v198, v234 offset:128\n\t"
            "v_mfma_f32_16x16x32_bf16 v[228:231], v[240:243], v[164:167], v[228:231]\n\t"
            "ds_read_u16 v222, v234 offset:256\n\t"
            "v_add_u32 v235, s45, v248\n\t"
            "v_mfma_f32_16x16x32_bf16 v[208:211], v[240:243], v[180:183], 0\n\t"
            "ds_read_b128 v[168:171], v235\n\t"
            "ds_read_b128 v[192:195], v235 offset:1536\n\t"
            "v_mfma_f32_16x16x32_bf16 v[208:211], v[240:243], v[172:175], v[208:211]\n\t"
            "ds_read_b128 v[216:219], v235 offset:3072\n\t"
            "v_mov_b32 v179, 0\n\t"
            "v_mov_b32 v199, 0\n\t"
            "v_mov_b32 v223, 0\n\t"
            "v_rcp_f32 v157, v156\n\t"
            "v_mad_u32_u24 v234, v246, s75, v253\n\t"
            "v_rcp_f32 v229, v228\n\t"
            "s_nop 0\n\t"
            "v_rcp_f32 v209, v208\n\t"
            "s_waitcnt lgkmcnt(2)\n\t"
            "v_mfma_f32_16x16x32_bf16 v[144:147], v[168:171], v[184:187], 0\n\t"
            "v_mfma_f32_16x16x32_bf16 v[144:147], v[176:179], v[188:191], v[144:147]\n\t"
            "s_waitcnt lgkmcnt(1)\n\t"
            "v_mfma_f32_16x16x32_bf16 v[152:155], v[192:195], v[184:187], 0\n\t"
            "v_mfma_f32_16x16x32_bf16 v[152:155], v[196:199], v[188:191], v[152:155]\n\t"
            "s_waitcnt lgkmcnt(0)\n\t"
            "v_mfma_f32_16x16x32_bf16 v[184:187], v[216:219], v[184:187], 0\n\t"
            "v_mfma_f32_16x16x32_bf16 v[184:187], v[220:223], v[188:191], v[184:187]\n\t"
            "ds_read_b64 v[188:189], v234 offset:320\n\t"
            "ds_read_b64 v[190:191], v234 offset:352\n\t"
            "v_mfma_f32_16x16x32_bf16 v[224:227], v[168:171], v[148:151], 0\n\t"
            "v_mul_f32 v144, v157, v144\n\t"
            "v_mul_f32 v145, v157, v145\n\t"
            "v_mfma_f32_16x16x32_bf16 v[224:227], v[176:179], v[164:167], v[224:227]\n\t"
            "v_mul_f32 v152, v157, v152\n\t"
            "v_mul_f32 v153, v157, v153\n\t"
            "v_mfma_f32_16x16x32_bf16 v[212:215], v[192:195], v[148:151], 0\n\t"
            "v_mul_f32 v184, v157, v184\n\t"
            "v_mul_f32 v185, v157, v185\n\t"
            "v_mfma_f32_16x16x32_bf16 v[212:215], v[196:199], v[164:167], v[212:215]\n\t"
            "v_mul_f32 v186, v157, v186\n\t"
            "v_mul_f32 v187, v157, v187\n\t"
            "v_mfma_f32_16x16x32_bf16 v[160:163], v[168:171], v[180:183], 0\n\t"
            "v_cvt_pk_bf16_f32 v184, v184, v185\n\t"
            "v_cvt_pk_bf16_f32 v185, v186, v187\n\t"
            "v_mfma_f32_16x16x32_bf16 v[160:163], v[176:179], v[172:175], v[160:163]\n\t"
            "ds_read_b64 v[186:187], v234 offset:288\n\t"
            "ds_write_b64 v245, v[184:185] offset:6400\n\t"
            "v_mfma_f32_16x16x32_bf16 v[204:207], v[192:195], v[180:183], 0\n\t"
            "ds_read_b64 v[184:185], v234 offset:256\n\t"
            "v_mul_f32 v224, v229, v224\n\t"
            "v_mfma_f32_16x16x32_bf16 v[204:207], v[196:199], v[172:175], v[204:207]\n\t"
            "v_mul_f32 v225, v229, v225\n\t"
            "v_mul_f32 v212, v229, v212\n\t"
            "v_mfma_f32_16x16x32_bf16 v[148:151], v[216:219], v[148:151], 0\n\t"
            "v_mul_f32 v213, v229, v213\n\t"
            "v_mul_f32 v160, v209, v160\n\t"
            "v_mfma_f32_16x16x32_bf16 v[148:151], v[220:223], v[164:167], v[148:151]\n\t"
            "v_mul_f32 v161, v209, v161\n\t"
            "v_mul_f32 v226, v229, v226\n\t"
            "v_mfma_f32_16x16x32_bf16 v[180:183], v[216:219], v[180:183], 0\n\t"
            "v_mul_f32 v204, v209, v204\n\t"
            "v_mul_f32 v205, v209, v205\n\t"
            "v_mfma_f32_16x16x32_bf16 v[180:183], v[220:223], v[172:175], v[180:183]\n\t"
            "v_mul_f32 v227, v229, v227\n\t"
            "v_cvt_pk_bf16_f32 v224, v224, v225\n\t"
            "v_mul_f32 v148, v229, v148\n\t"
            "v_mul_f32 v149, v229, v149\n\t"
            "v_mul_f32 v214, v229, v214\n\t"
            "v_mul_f32 v215, v229, v215\n\t"
            "v_cvt_pk_bf16_f32 v212, v212, v213\n\t"
            "s_waitcnt lgkmcnt(0)\n\t"
            "v_mfma_f32_16x16x32_bf16 v[200:203], v[240:243], v[184:187], 0\n\t"
            "v_mul_f32 v180, v209, v180\n\t"
            "v_mul_f32 v181, v209, v181\n\t"
            "v_mfma_f32_16x16x32_bf16 v[200:203], v[240:243], v[188:191], v[200:203]\n\t"
            "v_mul_f32 v150, v229, v150\n\t"
            "v_mul_f32 v151, v229, v151\n\t"
            "v_mfma_f32_16x16x32_bf16 v[168:171], v[184:187], v[168:171], 0\n\t"
            "v_cvt_pk_bf16_f32 v148, v148, v149\n\t"
            "v_mul_f32 v146, v157, v146\n\t"
            "v_mfma_f32_16x16x32_bf16 v[168:171], v[188:191], v[176:179], v[168:171]\n\t"
            "v_mul_f32 v147, v157, v147\n\t"
            "v_cvt_pk_bf16_f32 v144, v144, v145\n\t"
            "v_mfma_f32_16x16x32_bf16 v[192:195], v[184:187], v[192:195], 0\n\t"
            "v_rcp_f32 v201, v200\n\t"
            "v_mfma_f32_16x16x32_bf16 v[192:195], v[188:191], v[196:199], v[192:195]\n\t"
            "v_mul_f32 v154, v157, v154\n\t"
            "v_mul_f32 v155, v157, v155\n\t"
            "v_mfma_f32_16x16x32_bf16 v[216:219], v[184:187], v[216:219], 0\n\t"
            "v_cvt_pk_bf16_f32 v152, v152, v153\n\t"
            "v_mul_f32 v162, v209, v162\n\t"
            "v_mfma_f32_16x16x32_bf16 v[216:219], v[188:191], v[220:223], v[216:219]\n\t"
            "v_mul_f32 v163, v209, v163\n\t"
            "v_cvt_pk_bf16_f32 v160, v160, v161\n\t"
            "v_cndmask_b32_e64 v203, v168, v192, s[60:61]\n\t"
            "v_mul_f32 v206, v209, v206\n\t"
            "v_mul_f32 v207, v209, v207\n\t"
            "v_cvt_pk_bf16_f32 v204, v204, v205\n\t"
            "v_mul_f32 v182, v209, v182\n\t"
            "v_mul_f32 v183, v209, v183\n\t"
            "v_cvt_pk_bf16_f32 v180, v180, v181\n\t"
            "v_cndmask_b32_e64 v203, v203, v216, s[62:63]\n\t"
            "v_cvt_pk_bf16_f32 v225, v226, v227\n\t"
            "v_cvt_pk_bf16_f32 v213, v214, v215\n\t"
            "v_cvt_pk_bf16_f32 v149, v150, v151\n\t"
            "v_cvt_pk_bf16_f32 v145, v146, v147\n\t"
            "v_cvt_pk_bf16_f32 v153, v154, v155\n\t"
            "v_cvt_pk_bf16_f32 v161, v162, v163\n\t"
            "v_cvt_pk_bf16_f32 v205, v206, v207\n\t"
            "v_cvt_pk_bf16_f32 v181, v182, v183\n\t"
            "v_mul_f32_dpp v203, v201, v203 quad_perm:[0,0,0,0] row_mask:0xf bank_mask:0xf\n\t"
            "v_xad_u32 v202, s73, v254, v255\n\t"
            "v_cvt_pk_bf16_f32 v203, v203, 0\n\t"
            "ds_write_b64 v245, v[224:225]\n\t"
            "ds_write_b64 v245, v[212:213] offset:128\n\t"
            "ds_write_b64 v245, v[148:149] offset:256\n\t"
            "ds_write_b64 v245, v[144:145] offset:6144\n\t"
            "ds_write_b64 v245, v[152:153] offset:6272\n\t"
            "ds_write_b64 v245, v[160:161] offset:12288\n\t"
            "ds_write_b64 v245, v[204:205] offset:12416\n\t"
            "ds_write_b64 v245, v[180:181] offset:12544\n\t"
            "s_mov_b64 exec, s[68:69]\n\t"
            "ds_write_b16 v202, v203\n\t"
            "s_mov_b64 exec, 1\n\t"
            "ds_add_u32 v253, v246 offset:7856\n\t"
            "s_mov_b64 exec, -1\n\t"
            ".Lwa_done%=:\n\t"
            "s_waitcnt lgkmcnt(0)\n\t"
            "s_nop 7\n\t"
            : "+{v204}"(pST_), "+{v205}"(aRSw_), "+{v206}"(aPLw_), "+{v207}"(aRSr_), "+{v208}"(g16_), "+{v209}"(g8_), "+{v210}"(loVL_), "+{v211}"(loOL_), "+{v200}"(on), "+{v250}"(c0), "+{v251}"(c1), "+{v252}"(c2), "+{v253}"(c3), "+{v254}"(c4), "+{v255}"(c5),
              "+{v238}"(c6), "+{v239}"(c7), "+{v240}"(c8), "+{v241}"(c9), "+{s89}"(s89), "+{s46}"(s46), "+{s48}"(s48), "+{s70}"(s70), "+{s72}"(s72), "+{s73}"(s73), "+{s74}"(s74), "+{s76}"(s76), "+{s77}"(s77)
            : "{v244}"(pE_), "{v245}"(aO_), "{v246}"(sel_), "{v247}"(aVB_), "{s47}"((ldsb + X_OFF) ^ (ldsb + X1_OFF)),
              "{s[58:59]}"(0xFFFFull), "{s75}"(rsb), "{s45}"(ldsb + V_OFF + w * 4608), "{s[68:69]}"(0xFFFFFFFFFFFFull),
              "{s[84:87]}"(wr), "{s88}"(touch), "{s80}"(wave == 0 ? 16 : 20), "{s71}"(ldsb + O_OFF), "{s[54:55]}"(mrow), "{s[56:57]}"(0xF0000F0000F0000Full),
              "{s[78:79]}"(mrow & 0xF0000F0000F0000Full)
            : "v144", "v145", "v146", "v147", "v148", "v149", "v150", "v151", "v152", "v153", "v154", "v155", "v156", "v157", "v158", "v159", "v160", "v161", "v162", "v163", "v164", "v165", "v166", "v167", "v168", "v169", "v170", "v171", "v172", "v173", "v174", "v175", "v176", "v177", "v178", "v179", "v180", "v181", "v182", "v183", "v184", "v185", "v186", "v187", "v188", "v189", "v190", "v191", "v192", "v193", "v194", "v195", "v196", "v197", "v198", "v199", "v201", "v202", "v203", "v212", "v213", "v214", "v215", "v216", "v217", "v218", "v219", "v220", "v221", "v222", "v223", "v224", "v225", "v226", "v227", "v228", "v229", "v230", "v231", "v232", "v233", "v234", "v235", "v236", "v237", "v242", "v243", "v248", "v249", "v0", "v1", "v2", "v3", "v4", "v5", "v6", "v7", "v8", "v9", "v10", "v11", "v12", "v13", "v14", "v15", "v16", "v17", "v18", "v19", "v20", "v21", "v22", "v23", "v24", "v25", "v26", "v27", "v28", "v29", "v30", "v31", "v32", "v33", "v34", "v35", "v36", "v37", "v38", "v39", "v40", "v41", "v42", "v43", "v44", "v45", "v46", "v47", "v48", "v49", "v50", "v51", "v52", "v53", "v54", "v55", "v56", "v57", "v58", "v59", "v60", "v61", "v62", "v63", "v64", "v65", "v66", "v67", "v68", "v69", "v70", "v71", "v72", "v73", "v74", "v75", "v76", "v77", "v78", "v79", "v80", "v81", "v82", "v83", "v84", "v85", "v86", "v87", "v88", "v89", "v90", "v91", "v92", "v93", "v94", "v95", "v96", "v97", "v98", "v99", "v100", "v101", "v102", "v103", "v104", "v105", "v106", "v107", "v108", "v109", "v110", "v111", "v112", "v113", "v114", "v115", "v116", "v117", "v118", "v119", "v120", "v121", "v122", "v123", "v124", "v125", "v126", "v127", "v128", "v129", "v130", "v131", "v132", "v133", "v134", "v135", "v136", "v137", "v138", "v139", "v140", "v141", "v142", "v143",  "m0", "s60", "s61", "s62", "s63", "s64", "s65", "s66", "s67", "s81", "s82", "s83", "s90", "s91", "s49", "vcc", "scc", "memory");

        }
        if (wave == 0 && blockIdx.x == 0) {
            int ln_;
            asm volatile("v_mbcnt_lo_u32_b32 %0, -1, 0\n\tv_mbcnt_hi_u32_b32 %0, -1, %0" : "=v"(ln_));
            KARGS
            unsigned long long tg;
            asm volatile("s_load_dwordx2 %0, %1, %2\n\ts_waitcnt lgkmcnt(0)" : "=s"(tg) : "s"(wb), "n"(EPO_OFF) : "memory");
            const unsigned lo = (unsigned)tg + 1u, hi = (unsigned)(tg >> 32);
            const auto wq = __builtin_amdgcn_make_buffer_rsrc((void*)wb, 0, WS_BYTES, 0x00020000);
#pragma unroll 1
            for (int it_ = 0; it_ < 100000; ++it_) {
                bool bad = false;
#pragma unroll
                for (int k = 0; k < 2; ++k) {
                    const u32x4 a_ = __builtin_amdgcn_raw_buffer_load_b128(wq, ARR_OFF + ln_ * 16, k * 1024, SC1);
                    bad |= a_[0] != lo || a_[1] != hi || a_[2] != lo || a_[3] != hi;
                }
                if (__builtin_amdgcn_ballot_w64(bad) == 0ull) break;
                __builtin_amdgcn_s_sleep(2);
            }
            if (ln_ == 0) __builtin_amdgcn_raw_buffer_store_b64((u32x2){lo, hi}, wq, EPO_OFF, 0, SC1);
        }
        asm volatile("wk_code_end:");
        __builtin_amdgcn_endpgm();
    } else if (wave < 6) {
        LANE_SETUP
        {
            const unsigned rsb = ldsb + RSX_OFF;
            const unsigned pE_ = fE - X_OFF;
            const unsigned aVAw_ = ldsb + V_OFF + w * 9216 + lane * 16, aVBw_ = ldsb + V_OFF + w * 9216 + 1024 + lane * 8;
            const unsigned pST_ = lane * 16;
            unsigned aRSw_ = rsb + 256 + lane * 4, aRSr_ = rsb + (lane & 48);
            B_SCALARS
            unsigned s44 = 4;
            asm volatile(
            "s_waitcnt lgkmcnt(0)\n\t"
            "s_mov_b32 s83, 0x10000\n\t"
            "s_mov_b32 s84, -47104\n\t"
            "s_nop 7\n\t"
            "v_mov_b32 v200, v250\n\t"
            "v_mov_b32 v201, v251\n\t"
            "v_mov_b32 v202, v253\n\t"
            "s_mov_b32 s38, s67\n\t"
            "s_mov_b32 s67, 74496\n\t"
            "ds_read_b64 v[250:251], v203\n\t"
            "ds_read_b32 v253, v203 offset:8\n\t"
            ".Lwk_b1%=:\n\t"
            "v_mov_b32 v236, 0\n\t"
            "ds_read_b32 v236, v236 offset:7864\n\t"
            "s_waitcnt lgkmcnt(0)\n\t"
            "v_readfirstlane_b32 s77, v236\n\t"
            "s_cmp_ge_u32 s77, 4\n\t"
            "s_cbranch_scc1 .Lwk_b2%=\n\t"
            "s_cmp_eq_u32 s83, 0\n\t"
            "s_cbranch_scc1 .Lwk_b2%=\n\t"
            "s_sub_u32 s83, s83, 1\n\t"
            "s_sleep 1\n\t"
            "s_branch .Lwk_b1%=\n\t"
            ".Lwk_b2%=:\n\t"
            "s_waitcnt vmcnt(0)\n\t"
            "s_call_b64 vcc, wk_conv\n\t"
            "v_mov_b32 v250, v200\n\t"
            "v_mov_b32 v251, v201\n\t"
            "v_mov_b32 v253, v202\n\t"
            "s_mov_b32 s67, s38\n\t"
            "s_setprio 3\n\t"
            "s_barrier\n\t"
            "buffer_load_dwordx4 v[140:143], v245, s[28:31], s82 offen sc1\n\t"
            "s_add_u32 s49, s33, 0\n\t"
            "buffer_load_dwordx4 v[0:3], v245, s[28:31], s49 offen sc1\n\t"
            "s_add_u32 s49, s33, 6144\n\t"
            "buffer_load_dwordx4 v[24:27], v245, s[28:31], s49 offen sc1\n\t"
            "s_add_u32 s49, s33, 12288\n\t"
            "buffer_load_dwordx4 v[48:51], v245, s[28:31], s49 offen sc1\n\t"
            "s_add_u32 s49, s33, 18432\n\t"
            "buffer_load_dwordx4 v[72:75], v245, s[28:31], s49 offen sc1\n\t"
            "s_add_u32 s49, s33, 24576\n\t"
            "buffer_load_dwordx4 v[96:99], v245, s[28:31], s49 offen sc1\n\t"
            "s_add_u32 s49, s33, 30720\n\t"
            "buffer_load_dwordx4 v[120:123], v245, s[28:31], s49 offen sc1\n\t"
            "s_add_u32 s49, s33, 1024\n\t"
            "buffer_load_dwordx4 v[4:7], v245, s[28:31], s49 offen sc1\n\t"
            "s_add_u32 s49, s33, 7168\n\t"
            "buffer_load_dwordx4 v[28:31], v245, s[28:31], s49 offen sc1\n\t"
            "s_add_u32 s49, s33, 13312\n\t"
            "buffer_load_dwordx4 v[52:55], v245, s[28:31], s49 offen sc1\n\t"
            "s_add_u32 s49, s33, 19456\n\t"
            "buffer_load_dwordx4 v[76:79], v245, s[28:31], s49 offen sc1\n\t"
            "s_add_u32 s49, s33, 25600\n\t"
            "buffer_load_dwordx4 v[100:103], v245, s[28:31], s49 offen sc1\n\t"
            "s_add_u32 s49, s33, 31744\n\t"
            "buffer_load_dwordx4 v[124:127], v245, s[28:31], s49 offen sc1\n\t"
            "s_add_u32 s49, s33, 2048\n\t"
            "buffer_load_dwordx4 v[8:11], v245, s[28:31], s49 offen sc1\n\t"
            "s_add_u32 s49, s33, 8192\n\t"
            "buffer_load_dwordx4 v[32:35], v245, s[28:31], s49 offen sc1\n\t"
            "s_add_u32 s49, s33, 14336\n\t"
            "buffer_load_dwordx4 v[56:59], v245, s[28:31], s49 offen sc1\n\t"
            "s_add_u32 s49, s33, 20480\n\t"
            "buffer_load_dwordx4 v[80:83], v245, s[28:31], s49 offen sc1\n\t"
            "s_add_u32 s49, s33, 26624\n\t"
            "buffer_load_dwordx4 v[104:107], v245, s[28:31], s49 offen sc1\n\t"
            "s_add_u32 s49, s33, 32768\n\t"
            "buffer_load_dwordx4 v[128:131], v245, s[28:31], s49 offen sc1\n\t"
            "s_add_u32 s49, s33, 3072\n\t"
            "buffer_load_dwordx4 v[12:15], v245, s[28:31], s49 offen sc1\n\t"
            "s_add_u32 s49, s33, 9216\n\t"
            "buffer_load_dwordx4 v[36:39], v245, s[28:31], s49 offen sc1\n\t"
            "s_add_u32 s49, s33, 15360\n\t"
            "buffer_load_dwordx4 v[60:63], v245, s[28:31], s49 offen sc1\n\t"
            "s_add_u32 s49, s33, 21504\n\t"
            "buffer_load_dwordx4 v[84:87], v245, s[28:31], s49 offen sc1\n\t"
            "s_add_u32 s49, s33, 27648\n\t"
            "buffer_load_dwordx4 v[108:111], v245, s[28:31], s49 offen sc1\n\t"
            "s_add_u32 s49, s33, 33792\n\t"
            "buffer_load_dwordx4 v[132:135], v245, s[28:31], s49 offen sc1\n\t"
            "s_add_u32 s49, s33, 4096\n\t"
            "buffer_load_dwordx4 v[16:19], v245, s[28:31], s49 offen sc1\n\t"
            "s_add_u32 s49, s33, 10240\n\t"
            "buffer_load_dwordx4 v[40:43], v245, s[28:31], s49 offen sc1\n\t"
            "s_add_u32 s49, s33, 16384\n\t"
            "buffer_load_dwordx4 v[64:67], v245, s[28:31], s49 offen sc1\n\t"
            "s_add_u32 s49, s33, 22528\n\t"
            "buffer_load_dwordx4 v[88:91], v245, s[28:31], s49 offen sc1\n\t"
            "s_add_u32 s49, s33, 28672\n\t"
            "buffer_load_dwordx4 v[112:115], v245, s[28:31], s49 offen sc1\n\t"
            "s_add_u32 s49, s33, 34816\n\t"
            "buffer_load_dwordx4 v[136:139], v245, s[28:31], s49 offen sc1\n\t"
            "s_add_u32 s49, s33, 5120\n\t"
            "buffer_load_dwordx4 v[20:23], v245, s[28:31], s49 offen sc1\n\t"
            "s_add_u32 s49, s33, 11264\n\t"
            "buffer_load_dwordx4 v[44:47], v245, s[28:31], s49 offen sc1\n\t"
            "s_add_u32 s49, s33, 17408\n\t"
            "buffer_load_dwordx4 v[68:71], v245, s[28:31], s49 offen sc1\n\t"
            "s_add_u32 s49, s33, 23552\n\t"
            "buffer_load_dwordx4 v[92:95], v245, s[28:31], s49 offen sc1\n\t"
            "s_add_u32 s49, s33, 29696\n\t"
            "buffer_load_dwordx4 v[116:119], v245, s[28:31], s49 offen sc1\n\t"
            "s_add_u32 s49, s33, 35840\n\t"
            "buffer_load_dwordx4 v[140:143], v245, s[28:31], s49 offen sc1\n\t"
            "ds_read_b32 v249, v252 offset:12\n\t"
            "s_waitcnt lgkmcnt(0)\n\t"
            "v_and_b32 v249, s69, v249\n\t"
            "s_mov_b32 exec_hi, 0x3fffff\n\t"
            "s_add_u32 m0, s66, 0\n\t"
            "s_add_u32 s49, s79, 0\n\t"
            "buffer_load_dwordx4 v249, s[72:75], s49 offen lds\n\t"
            "s_mov_b32 exec_hi, -1\n\t"
            "s_mov_b32 exec_hi, 0x3fffff\n\t"
            "s_add_u32 m0, s66, 872\n\t"
            "s_add_u32 s49, s47, 196224\n\t"
            "buffer_load_dwordx4 v249, s[72:75], s49 offen lds\n\t"
            "s_mov_b32 exec_hi, -1\n\t"
            "s_mov_b32 exec_hi, 0x3fffff\n\t"
            "s_add_u32 m0, s66, 1744\n\t"
            "s_add_u32 s49, s47, 392448\n\t"
            "buffer_load_dwordx4 v249, s[72:75], s49 offen lds\n\t"
            "s_mov_b32 exec_hi, -1\n\t"
            "s_mov_b32 exec_hi, 0x3fffff\n\t"
            "s_add_u32 m0, s66, 2616\n\t"
            "s_add_u32 s49, s47, 588672\n\t"
            "buffer_load_dwordx4 v249, s[72:75], s49 offen lds\n\t"
            "s_mov_b32 exec_hi, -1\n\t"
            "s_mov_b32 exec_hi, 0x3fffff\n\t"
            "s_add_u32 m0, s66, 3488\n\t"
            "s_add_u32 s49, s47, 784896\n\t"
            "buffer_load_dwordx4 v249, s[72:75], s49 offen lds\n\t"
            "s_mov_b32 exec_hi, -1\n\t"
            "s_mov_b32 exec_hi, 0x3fffff\n\t"
            "s_add_u32 m0, s66, 4360\n\t"
            "s_add_u32 s49, s47, 981120\n\t"
            "buffer_load_dwordx4 v249, s[72:75], s49 offen lds\n\t"
            "s_mov_b32 exec_hi, -1\n\t"
            "s_mov_b32 exec_hi, 0x3fffff\n\t"
            "s_add_u32 m0, s66, 5232\n\t"
            "s_add_u32 s49, s47, 1177344\n\t"
            "buffer_load_dwordx4 v249, s[72:75], s49 offen lds\n\t"
            "s_mov_b32 exec_hi, -1\n\t"
            "s_mov_b32 exec_hi, 0x3fffff\n\t"
            "s_add_u32 m0, s66, 6104\n\t"
            "s_add_u32 s49, s47, 1373568\n\t"
            "buffer_load_dwordx4 v249, s[72:75], s49 offen lds\n\t"
            "s_mov_b32 exec_hi, -1\n\t"
            "s_mov_b32 exec_hi, 0x3fffff\n\t"
            "s_add_u32 m0, s66, 6976\n\t"
            "s_add_u32 s49, s47, 1569792\n\t"
            "buffer_load_dwordx4 v249, s[72:75], s49 offen lds\n\t"
            "s_mov_b32 exec_hi, -1\n\t"
            "ds_read_b128 v[168:171], v254\n\t"
            "ds_read_b128 v[172:175], v254 offset:64\n\t"
            "v_add_u32 v180, s48, v245\n\t"
            "v_add_u32 v232, s48, v242\n\t"
            "v_xor_b32 v233, 64, v232\n\t"
            "v_xor_b32 v246, 0x100, v246\n\t"
            "ds_read_b128 v[176:179], v180 offset:18816\n\t"
            "ds_read_b128 v[180:183], v180 offset:19840\n\t"
            "ds_read_b128 v[192:195], v232\n\t"
            "ds_read_b128 v[196:199], v233\n\t"
            "ds_read_b128 v[224:227], v232 offset:128\n\t"
            "ds_read_b128 v[228:231], v233 offset:128\n\t"
            "s_waitcnt lgkmcnt(5)\n\t"
            "v_add_f32 v176, v176, v178\n\t"
            "s_waitcnt lgkmcnt(4)\n\t"
            "v_add_f32 v180, v180, v182\n\t"
            "v_add_f32 v176, v176, v180\n\t"
            "v_add_f32 v177, v177, v179\n\t"
            "v_add_f32 v181, v181, v183\n\t"
            "v_mul_f32 v176, 0x3baaaaab, v176\n\t"
            "v_add_f32 v177, v177, v181\n\t"
            "v_mul_f32_e64 v176, v176, -v176\n\t"
            "v_fmac_f32 v176, 0x3baaaaab, v177\n\t"
            "v_max_f32 v176, 0, v176\n\t"
            "v_add_f32 v176, 0x3727c5ac, v176\n\t"
            "v_rsq_f32 v176, v176\n\t"
            "s_nop 0\n\t"
            "ds_write_b32 v246, v176\n\t"
            "s_setprio 3\n\t"
            "s_waitcnt vmcnt(9)\n\t"
            "s_waitcnt lgkmcnt(3)\n\t"
            "v_mfma_f32_16x16x32_bf16 v[200:203], v[192:195], v[0:3], 0\n\t"
            "v_mfma_f32_16x16x32_bf16 v[200:203], v[196:199], v[4:7], v[200:203]\n\t"
            "v_mfma_f32_16x16x32_bf16 v[204:207], v[196:199], v[28:31], 0\n\t"
            "v_mfma_f32_16x16x32_bf16 v[204:207], v[192:195], v[24:27], v[204:207]\n\t"
            "v_mfma_f32_16x16x32_bf16 v[208:211], v[192:195], v[48:51], 0\n\t"
            "v_mfma_f32_16x16x32_bf16 v[208:211], v[196:199], v[52:55], v[208:211]\n\t"
            "v_mfma_f32_16x16x32_bf16 v[212:215], v[196:199], v[76:79], 0\n\t"
            "v_mfma_f32_16x16x32_bf16 v[212:215], v[192:195], v[72:75], v[212:215]\n\t"
            "v_mfma_f32_16x16x32_bf16 v[216:219], v[192:195], v[96:99], 0\n\t"
            "v_mfma_f32_16x16x32_bf16 v[216:219], v[196:199], v[100:103], v[216:219]\n\t"
            "v_mfma_f32_16x16x32_bf16 v[220:223], v[196:199], v[124:127], 0\n\t"
            "v_mfma_f32_16x16x32_bf16 v[220:223], v[192:195], v[120:123], v[220:223]\n\t"
            "s_waitcnt lgkmcnt(1)\n\t"
            "v_mfma_f32_16x16x32_bf16 v[200:203], v[224:227], v[8:11], v[200:203]\n\t"
            "v_mfma_f32_16x16x32_bf16 v[200:203], v[228:231], v[12:15], v[200:203]\n\t"
            "v_mfma_f32_16x16x32_bf16 v[204:207], v[228:231], v[36:39], v[204:207]\n\t"
            "v_mfma_f32_16x16x32_bf16 v[204:207], v[224:227], v[32:35], v[204:207]\n\t"
            "v_mfma_f32_16x16x32_bf16 v[208:211], v[224:227], v[56:59], v[208:211]\n\t"
            "v_mfma_f32_16x16x32_bf16 v[208:211], v[228:231], v[60:63], v[208:211]\n\t"
            "v_mfma_f32_16x16x32_bf16 v[212:215], v[228:231], v[84:87], v[212:215]\n\t"
            "v_mfma_f32_16x16x32_bf16 v[212:215], v[224:227], v[80:83], v[212:215]\n\t"
            "v_mfma_f32_16x16x32_bf16 v[216:219], v[224:227], v[104:107], v[216:219]\n\t"
            "s_waitcnt vmcnt(0)\n\t"
            "ds_read_b64 v[144:145], v253\n\t"
            "v_mfma_f32_16x16x32_bf16 v[216:219], v[228:231], v[108:111], v[216:219]\n\t"
            "ds_read_b64 v[146:147], v253 offset:96\n\t"
            "ds_read_b64 v[148:149], v253 offset:1744\n\t"
            "v_mfma_f32_16x16x32_bf16 v[220:223], v[228:231], v[132:135], v[220:223]\n\t"
            "ds_read_b128 v[228:231], v233 offset:256\n\t"
            "ds_read_b64 v[150:151], v253 offset:1840\n\t"
            "v_mfma_f32_16x16x32_bf16 v[220:223], v[224:227], v[128:131], v[220:223]\n\t"
            "ds_read_b128 v[224:227], v232 offset:256\n\t"
            "ds_read_b64 v[152:153], v253 offset:8\n\t"
            "ds_read_b64 v[154:155], v253 offset:104\n\t"
            "ds_read_b64 v[156:157], v253 offset:1752\n\t"
            "ds_read_b64 v[158:159], v253 offset:1848\n\t"
            "ds_read_b64 v[160:161], v253 offset:16\n\t"
            "ds_read_b64 v[162:163], v253 offset:112\n\t"
            "ds_read_b64 v[164:165], v253 offset:1760\n\t"
            "ds_read_b64 v[166:167], v253 offset:1856\n\t"
            "s_waitcnt lgkmcnt(0)\n\t"
            "s_nop 7\n\t"
            ".p2align 6\n\t"
            "s_nop 0\n\t"
            "s_nop 0\n\t"
            "s_nop 0\n\t"
            "s_nop 0\n\t"
            ".Lwc_loop%=:\n\t"
            "s_setprio 1\n\t"
            "s_setprio 3\n\t"
            "v_mfma_f32_16x16x32_bf16 v[200:203], v[224:227], v[16:19], v[200:203]\n\t"
            "v_mov_b64 v[176:177], 0\n\t"
            "v_mov_b64 v[178:179], 0\n\t"
            "v_mfma_f32_16x16x32_bf16 v[200:203], v[228:231], v[20:23], v[200:203]\n\t"
            "v_mov_b64 v[180:181], 0\n\t"
            "v_mov_b64 v[182:183], 0\n\t"
            "s_waitcnt lgkmcnt(0)\n\t"
            "v_smfmac_f32_16x16x64_bf16 v[176:179], v[168:171], v[144:151], v255\n\t"
            "ds_read_b128 v[168:171], v254 offset:128\n\t"
            "v_smfmac_f32_16x16x64_bf16 v[180:183], v[172:175], v[152:159], v255\n\t"
            "ds_read_b64 v[144:145], v253 offset:24\n\t"
            "v_mfma_f32_16x16x32_bf16 v[204:207], v[228:231], v[44:47], v[204:207]\n\t"
            "ds_read_b64 v[146:147], v253 offset:120\n\t"
            "ds_read_b64 v[148:149], v253 offset:1768\n\t"
            "v_mfma_f32_16x16x32_bf16 v[204:207], v[224:227], v[40:43], v[204:207]\n\t"
            "ds_read_b64 v[150:151], v253 offset:1864\n\t"
            "ds_read_b128 v[172:175], v254 offset:192\n\t"
            "v_mfma_f32_16x16x32_bf16 v[208:211], v[224:227], v[64:67], v[208:211]\n\t"
            "ds_read_b64 v[152:153], v253 offset:32\n\t"
            "ds_read_b64 v[154:155], v253 offset:128\n\t"
            "v_mfma_f32_16x16x32_bf16 v[208:211], v[228:231], v[68:71], v[208:211]\n\t"
            "ds_read_b64 v[156:157], v253 offset:1776\n\t"
            "ds_read_b64 v[158:159], v253 offset:1872\n\t"
            "v_mfma_f32_16x16x32_bf16 v[212:215], v[228:231], v[92:95], v[212:215]\n\t"
            "v_mov_b32 v249, s76\n\t"
            "v_mov_b64 v[184:185], 0\n\t"
            "v_mfma_f32_16x16x32_bf16 v[212:215], v[224:227], v[88:91], v[212:215]\n\t"
            "ds_read_b32 v249, v249\n\t"
            "v_mov_b64 v[186:187], 0\n\t"
            "v_mfma_f32_16x16x32_bf16 v[216:219], v[224:227], v[112:115], v[216:219]\n\t"
            "v_mov_b64 v[188:189], 0\n\t"
            "v_mov_b64 v[190:191], 0\n\t"
            "v_mfma_f32_16x16x32_bf16 v[216:219], v[228:231], v[116:119], v[216:219]\n\t"
            "v_cvt_pk_bf16_f32 v176, v176, v177\n\t"
            "v_add_u32 v236, s67, v250\n\t"
            "s_waitcnt lgkmcnt(10)\n\t"
            "v_smfmac_f32_16x16x64_bf16 v[184:187], v[168:171], v[160:167], v255\n\t"
            "ds_read_b128 v[168:171], v254 offset:256\n\t"
            "s_waitcnt lgkmcnt(6)\n\t"
            "v_smfmac_f32_16x16x64_bf16 v[188:191], v[172:175], v[144:151], v255\n\t"
            "ds_read_b64 v[160:161], v253 offset:40\n\t"
            "v_mfma_f32_16x16x32_bf16 v[220:223], v[228:231], v[140:143], v[220:223]\n\t"
            "ds_read_b64 v[162:163], v253 offset:136\n\t"
            "ds_read_b64 v[164:165], v253 offset:1784\n\t"
            "v_mfma_f32_16x16x32_bf16 v[220:223], v[224:227], v[136:139], v[220:223]\n\t"
            "s_setprio 1\n\t"
            "ds_read_b64 v[166:167], v253 offset:1880\n\t"
            "ds_read_b128 v[172:175], v254 offset:320\n\t"
            "ds_read_b64 v[144:145], v253 offset:48\n\t"
            "ds_read_b64 v[146:147], v253 offset:144\n\t"
            "ds_read_b64 v[148:149], v253 offset:1792\n\t"
            "ds_read_b64 v[150:151], v253 offset:1888\n\t"
            "s_waitcnt lgkmcnt(14)\n\t"
            "ds_read_b128 v[224:227], v232 offset:6272\n\t"
            "s_waitcnt lgkmcnt(14)\n\t"
            "ds_read_b128 v[228:231], v233 offset:6272\n\t"
            "s_waitcnt lgkmcnt(12)\n\t"
            "v_readfirstlane_b32 s26, v249\n\t"
            "v_cvt_pk_bf16_f32 v177, v178, v179\n\t"
            "v_cvt_pk_bf16_f32 v178, v180, v181\n\t"
            "s_and_b32 s68, s26, 0x7ffffff\n\t"
            "s_add_u32 s68, s68, s78\n\t"
            "s_and_b32 s69, s26, 0x38000000\n\t"
            "s_or_b32 s69, s69, s39\n\t"
            "s_bitcmp1_b32 s26, 27\n\t"
            "s_cselect_b32 s79, 2.0, s68\n\t"
            "s_setprio 3\n\t"
            "v_mfma_f32_4x4x4_16b_bf16 v[196:199], v[176:177], v[176:177], 0\n\t"
            "s_setprio 1\n\t"
            "ds_read_b32 v249, v252 offset:12\n\t"
            "s_and_b32 s57, s56, 3\n\t"
            "s_lshl_b32 s58, s57, 3\n\t"
            "s_lshr_b32 m0, s54, s58\n\t"
            "s_lshl_b32 s58, s57, 4\n\t"
            "s_lshr_b64 s[36:37], s[50:51], s58\n\t"
            "s_and_b32 s57, s36, 0xffff\n\t"
            "s_add_u32 s57, s57, s67\n\t"
            "v_writelane_b32 v236, s57, 63\n\t"
            "v_writelane_b32 v236, s59, m0\n\t"
            "v_cvt_pk_bf16_f32 v179, v182, v183\n\t"
            "v_mov_b64 v[180:181], 0\n\t"
            "v_mov_b64 v[182:183], 0\n\t"
            "s_setprio 3\n\t"
            "v_mfma_f32_16x16x32_bf16 v[192:195], v[238:241], v[176:179], 0\n\t"
            "s_lshr_b64 s[36:37], s[52:53], s58\n\t"
            "s_and_b32 s58, s36, 0xffff\n\t"
            "s_add_u32 s58, s58, s67\n\t"
            "v_mfma_f32_4x4x4_16b_bf16 v[196:199], v[178:179], v[178:179], v[196:199]\n\t"
            "ds_write_b128 v236, v[176:179]\n\t"
            "s_waitcnt lgkmcnt(8)\n\t"
            "v_smfmac_f32_16x16x64_bf16 v[180:183], v[172:175], v[160:167], v255\n\t"
            "s_setprio 1\n\t"
            "s_and_b32 s38, s52, 0xffff\n\t"
            "s_add_u32 s38, s38, s67\n\t"
            "ds_read_b128 v[172:175], v254 offset:448\n\t"
            "ds_read_b64 v[160:161], v253 offset:64\n\t"
            "ds_read_b64 v[162:163], v253 offset:160\n\t"
            "ds_read_b64 v[164:165], v253 offset:1808\n\t"
            "ds_read_b64 v[166:167], v253 offset:1904\n\t"
            "v_mov_b64 v[176:177], 0\n\t"
            "v_mov_b64 v[178:179], 0\n\t"
            "v_cvt_pk_bf16_f32 v184, v184, v185\n\t"
            "v_cvt_pk_bf16_f32 v185, v186, v187\n\t"
            "s_setprio 3\n\t"
            "v_smfmac_f32_16x16x64_bf16 v[176:179], v[168:171], v[152:159], v255\n\t"
            "ds_read_b128 v[168:171], v254 offset:384\n\t"
            "v_mfma_f32_4x4x4_16b_bf16 v[196:199], v[184:185], v[184:185], v[196:199]\n\t"
            "s_setprio 1\n\t"
            "ds_read_b64 v[152:153], v253 offset:56\n\t"
            "s_waitcnt lgkmcnt(14)\n\t"
            "ds_read_b64 v[154:155], v253 offset:152\n\t"
            "s_waitcnt lgkmcnt(14)\n\t"
            "ds_read_b64 v[156:157], v253 offset:1800\n\t"
            "s_waitcnt lgkmcnt(14)\n\t"
            "ds_read_b64 v[158:159], v253 offset:1896\n\t"
            "v_xor_b32 v237, 16, v236\n\t"
            "v_cvt_pk_bf16_f32 v186, v188, v189\n\t"
            "v_cvt_pk_bf16_f32 v187, v190, v191\n\t"
            "v_mov_b64 v[188:189], 0\n\t"
            "v_mov_b64 v[190:191], 0\n\t"
            "s_setprio 3\n\t"
            "v_mfma_f32_16x16x32_bf16 v[192:195], v[238:241], v[184:187], v[192:195]\n\t"
            "s_waitcnt lgkmcnt(14)\n\t"
            "ds_write_b128 v237, v[184:187]\n\t"
            "v_mfma_f32_4x4x4_16b_bf16 v[196:199], v[186:187], v[186:187], v[196:199]\n\t"
            "s_setprio 1\n\t"
            "v_mov_b64 v[184:185], 0\n\t"
            "v_mov_b64 v[186:187], 0\n\t"
            "v_cvt_pk_bf16_f32 v176, v176, v177\n\t"
            "v_cvt_pk_bf16_f32 v177, v178, v179\n\t"
            "v_cvt_pk_bf16_f32 v178, v180, v181\n\t"
            "v_cvt_pk_bf16_f32 v179, v182, v183\n\t"
            "s_setprio 3\n\t"
            "v_mfma_f32_4x4x4_16b_bf16 v[196:199], v[176:177], v[176:177], v[196:199]\n\t"
            "s_waitcnt lgkmcnt(14)\n\t"
            "ds_write_b128 v236, v[176:179] offset:128\n\t"
            "s_waitcnt lgkmcnt(6)\n\t"
            "v_smfmac_f32_16x16x64_bf16 v[184:187], v[168:171], v[144:151], v255\n\t"
            "ds_read_b128 v[168:171], v254 offset:512\n\t"
            "s_waitcnt lgkmcnt(3)\n\t"
            "v_smfmac_f32_16x16x64_bf16 v[188:191], v[172:175], v[152:159], v255\n\t"
            "ds_read_b64 v[144:145], v253 offset:72\n\t"
            "v_mfma_f32_16x16x32_bf16 v[192:195], v[238:241], v[176:179], v[192:195]\n\t"
            "ds_read_b64 v[146:147], v253 offset:168\n\t"
            "ds_read_b64 v[148:149], v253 offset:1816\n\t"
            "v_mfma_f32_4x4x4_16b_bf16 v[196:199], v[178:179], v[178:179], v[196:199]\n\t"
            "s_setprio 1\n\t"
            "ds_read_b64 v[150:151], v253 offset:1912\n\t"
            "ds_read_b128 v[172:175], v254 offset:576\n\t"
            "ds_read_b64 v[152:153], v253 offset:80\n\t"
            "ds_read_b64 v[154:155], v253 offset:176\n\t"
            "ds_read_b64 v[156:157], v253 offset:1824\n\t"
            "ds_read_b64 v[158:159], v253 offset:1920\n\t"
            "v_mov_b64 v[176:177], 0\n\t"
            "v_mov_b64 v[178:179], 0\n\t"
            "v_mov_b64 v[180:181], 0\n\t"
            "v_mov_b64 v[182:183], 0\n\t"
            "v_cvt_pk_bf16_f32 v184, v184, v185\n\t"
            "v_cvt_pk_bf16_f32 v185, v186, v187\n\t"
            "v_cvt_pk_bf16_f32 v186, v188, v189\n\t"
            "v_cvt_pk_bf16_f32 v187, v190, v191\n\t"
            "s_setprio 3\n\t"
            "v_mfma_f32_4x4x4_16b_bf16 v[196:199], v[184:185], v[184:185], v[196:199]\n\t"
            "ds_write_b128 v237, v[184:187] offset:128\n\t"
            "s_waitcnt lgkmcnt(10)\n\t"
            "v_smfmac_f32_16x16x64_bf16 v[176:179], v[168:171], v[160:167], v255\n\t"
            "ds_read_b128 v[168:171], v254 offset:640\n\t"
            "v_mfma_f32_16x16x32_bf16 v[192:195], v[238:241], v[184:187], v[192:195]\n\t"
            "ds_read_b64 v[160:161], v253 offset:88\n\t"
            "ds_read_b64 v[162:163], v253 offset:184\n\t"
            "s_waitcnt lgkmcnt(8)\n\t"
            "v_smfmac_f32_16x16x64_bf16 v[180:183], v[172:175], v[144:151], v255\n\t"
            "ds_read_b64 v[164:165], v253 offset:1832\n\t"
            "v_mfma_f32_4x4x4_16b_bf16 v[196:199], v[186:187], v[186:187], v[196:199]\n\t"
            "s_setprio 1\n\t"
            "ds_read_b64 v[166:167], v253 offset:1928\n\t"
            "ds_read_b128 v[172:175], v254 offset:704\n\t"
            "v_mov_b64 v[184:185], 0\n\t"
            "v_mov_b64 v[186:187], 0\n\t"
            "v_mov_b64 v[188:189], 0\n\t"
            "v_mov_b64 v[190:191], 0\n\t"
            "v_add_u32 v248, s67, v251\n\t"
            "v_writelane_b32 v248, s58, 63\n\t"
            "v_writelane_b32 v248, s38, m0\n\t"
            "v_and_b32 v249, s69, v249\n\t"
            "v_cvt_pk_bf16_f32 v176, v176, v177\n\t"
            "v_cvt_pk_bf16_f32 v177, v178, v179\n\t"
            "v_cvt_pk_bf16_f32 v178, v180, v181\n\t"
            "v_cvt_pk_bf16_f32 v179, v182, v183\n\t"
            "s_setprio 3\n\t"
            "v_mfma_f32_4x4x4_16b_bf16 v[196:199], v[176:177], v[176:177], v[196:199]\n\t"
            "ds_write_b128 v236, v[176:179] offset:256\n\t"
            "s_waitcnt lgkmcnt(6)\n\t"
            "v_smfmac_f32_16x16x64_bf16 v[184:187], v[168:171], v[152:159], v255\n\t"
            "v_mov_b32 v234, 0\n\t"
            "v_mfma_f32_4x4x4_16b_bf16 v[196:199], v[178:179], v[178:179], v[196:199]\n\t"
            "v_xor_b32 v246, 0x100, v246\n\t"
            "v_mfma_f32_16x16x32_bf16 v[192:195], v[238:241], v[176:179], v[192:195]\n\t"
            "s_waitcnt lgkmcnt(1)\n\t"
            "v_smfmac_f32_16x16x64_bf16 v[188:191], v[172:175], v[160:167], v255\n\t"
            "s_setprio 1\n\t"
            "s_mov_b32 exec_hi, 0x3fffff\n\t"
            "s_add_u32 m0, s66, 0\n\t"
            "s_add_u32 s49, s79, 0\n\t"
            "buffer_load_dwordx4 v249, s[72:75], s49 offen lds\n\t"
            "s_mov_b32 exec_hi, -1\n\t"
            "s_mov_b32 exec_hi, 0x3fffff\n\t"
            "s_add_u32 m0, s66, 872\n\t"
            "s_add_u32 s49, s68, 196224\n\t"
            "buffer_load_dwordx4 v249, s[72:75], s49 offen lds\n\t"
            "s_mov_b32 exec_hi, -1\n\t"
            "s_mov_b32 exec_hi, 0x3fffff\n\t"
            "s_add_u32 m0, s66, 1744\n\t"
            "s_add_u32 s49, s68, 392448\n\t"
            "buffer_load_dwordx4 v249, s[72:75], s49 offen lds\n\t"
            "s_mov_b32 exec_hi, -1\n\t"
            "s_mov_b32 exec_hi, 0x3fffff\n\t"
            "s_add_u32 m0, s66, 2616\n\t"
            "s_add_u32 s49, s68, 588672\n\t"
            "buffer_load_dwordx4 v249, s[72:75], s49 offen lds\n\t"
            "s_mov_b32 exec_hi, -1\n\t"
            "s_mov_b32 exec_hi, 0x3fffff\n\t"
            "s_add_u32 m0, s66, 3488\n\t"
            "s_add_u32 s49, s68, 784896\n\t"
            "buffer_load_dwordx4 v249, s[72:75], s49 offen lds\n\t"
            "s_mov_b32 exec_hi, -1\n\t"
            "v_cvt_pk_bf16_f32 v184, v184, v185\n\t"
            "v_cvt_pk_bf16_f32 v185, v186, v187\n\t"
            "v_cvt_pk_bf16_f32 v186, v188, v189\n\t"
            "v_cvt_pk_bf16_f32 v187, v190, v191\n\t"
            "s_setprio 3\n\t"
            "v_mfma_f32_4x4x4_16b_bf16 v[196:199], v[184:185], v[184:185], v[196:199]\n\t"
            "s_mov_b32 exec_hi, 0x3fffff\n\t"
            "s_add_u32 m0, s66, 4360\n\t"
            "s_add_u32 s49, s68, 981120\n\t"
            "buffer_load_dwordx4 v249, s[72:75], s49 offen lds\n\t"
            "s_mov_b32 exec_hi, -1\n\t"
            "v_mfma_f32_4x4x4_16b_bf16 v[196:199], v[186:187], v[186:187], v[196:199]\n\t"
            "s_mov_b32 exec_hi, 0x3fffff\n\t"
            "s_add_u32 m0, s66, 5232\n\t"
            "s_add_u32 s49, s68, 1177344\n\t"
            "buffer_load_dwordx4 v249, s[72:75], s49 offen lds\n\t"
            "s_mov_b32 exec_hi, -1\n\t"
            "v_mfma_f32_16x16x32_bf16 v[192:195], v[238:241], v[184:187], v[192:195]\n\t"
            "s_setprio 1\n\t"
            "s_mov_b32 exec_hi, 0x3fffff\n\t"
            "s_add_u32 m0, s66, 6104\n\t"
            "s_add_u32 s49, s68, 1373568\n\t"
            "buffer_load_dwordx4 v249, s[72:75], s49 offen lds\n\t"
            "s_mov_b32 exec_hi, -1\n\t"
            "s_mov_b32 exec_hi, 0x3fffff\n\t"
            "s_add_u32 m0, s66, 6976\n\t"
            "s_add_u32 s49, s68, 1569792\n\t"
            "buffer_load_dwordx4 v249, s[72:75], s49 offen lds\n\t"
            "s_mov_b32 exec_hi, -1\n\t"
            "v_cndmask_b32_e64 v193, v196, v197, s[60:61]\n\t"
            "v_cndmask_b32_e64 v193, v193, v198, s[62:63]\n\t"
            "v_cndmask_b32_e64 v193, v193, v199, s[64:65]\n\t"
            "s_setprio 1\n\t"
            "ds_read_b128 v[196:199], v233 offset:6144\n\t"
            "ds_write_b64 v248, v[192:193] offset:18816\n\t"
            "ds_read_b128 v[192:195], v232 offset:6144\n\t"
            "ds_read_b64 v[248:249], v247 offset:8\n\t"
            "ds_write_b128 v237, v[184:187] offset:256\n\t"
            "ds_read_b64 v[236:237], v247\n\t"
            "s_mov_b64 exec, 1\n\t"
            "ds_inc_u32 v255, v238 offset:7860\n\t"
            "s_mov_b64 exec, -1\n\t"
            "s_waitcnt lgkmcnt(4)\n\t"
            "v_mfma_f32_16x16x32_bf16 v[144:147], v[192:195], v[0:3], 0\n\t"
            "s_waitcnt lgkmcnt(3)\n\t"
            "v_mul_f32 v202, v248, v202\n\t"
            "v_mul_f32 v203, v249, v203\n\t"
            "v_mfma_f32_16x16x32_bf16 v[144:147], v[196:199], v[4:7], v[144:147]\n\t"
            "v_mul_f32 v206, v248, v206\n\t"
            "s_waitcnt lgkmcnt(1)\n\t"
            "v_mul_f32 v200, v236, v200\n\t"
            "v_mfma_f32_16x16x32_bf16 v[148:151], v[196:199], v[28:31], 0\n\t"
            "v_mul_f32 v201, v237, v201\n\t"
            "v_mul_f32 v204, v236, v204\n\t"
            "v_mfma_f32_16x16x32_bf16 v[148:151], v[192:195], v[24:27], v[148:151]\n\t"
            "v_mul_f32 v205, v237, v205\n\t"
            "v_mul_f32 v208, v236, v208\n\t"
            "v_mfma_f32_16x16x32_bf16 v[168:171], v[192:195], v[48:51], 0\n\t"
            "v_mul_f32 v209, v237, v209\n\t"
            "v_mul_f32 v212, v236, v212\n\t"
            "v_mfma_f32_16x16x32_bf16 v[168:171], v[196:199], v[52:55], v[168:171]\n\t"
            "v_mul_f32 v213, v237, v213\n\t"
            "v_mul_f32 v216, v236, v216\n\t"
            "v_mfma_f32_16x16x32_bf16 v[172:175], v[196:199], v[76:79], 0\n\t"
            "v_mul_f32 v217, v237, v217\n\t"
            "v_mul_f32 v220, v236, v220\n\t"
            "v_mfma_f32_16x16x32_bf16 v[172:175], v[192:195], v[72:75], v[172:175]\n\t"
            "v_mul_f32 v221, v237, v221\n\t"
            "v_mul_f32 v207, v249, v207\n\t"
            "v_mfma_f32_16x16x32_bf16 v[152:155], v[192:195], v[96:99], 0\n\t"
            "ds_read_b64 v[236:237], v247 offset:64\n\t"
            "v_mul_f32 v210, v248, v210\n\t"
            "v_mfma_f32_16x16x32_bf16 v[152:155], v[196:199], v[100:103], v[152:155]\n\t"
            "v_mul_f32 v211, v249, v211\n\t"
            "v_mul_f32 v214, v248, v214\n\t"
            "v_mfma_f32_16x16x32_bf16 v[156:159], v[196:199], v[124:127], 0\n\t"
            "ds_read_b128 v[196:199], v233 offset:6400\n\t"
            "v_mul_f32 v215, v249, v215\n\t"
            "v_mfma_f32_16x16x32_bf16 v[156:159], v[192:195], v[120:123], v[156:159]\n\t"
            "ds_read_b128 v[192:195], v232 offset:6400\n\t"
            "v_mul_f32 v218, v248, v218\n\t"
            "v_mfma_f32_16x16x32_bf16 v[144:147], v[224:227], v[8:11], v[144:147]\n\t"
            "v_mul_f32 v219, v249, v219\n\t"
            "v_mul_f32 v222, v248, v222\n\t"
            "v_mfma_f32_16x16x32_bf16 v[144:147], v[228:231], v[12:15], v[144:147]\n\t"
            "v_mul_f32 v223, v249, v223\n\t"
            "v_cvt_pk_bf16_f32 v212, v212, v213\n\t"
            "v_mfma_f32_16x16x32_bf16 v[148:151], v[228:231], v[36:39], v[148:151]\n\t"
            "ds_read_b64 v[248:249], v247 offset:72\n\t"
            "v_cvt_pk_bf16_f32 v208, v208, v209\n\t"
            "v_mfma_f32_16x16x32_bf16 v[148:151], v[224:227], v[32:35], v[148:151]\n\t"
            "v_cvt_pk_bf16_f32 v213, v214, v215\n\t"
            "v_cvt_pk_bf16_f32 v209, v210, v211\n\t"
            "v_mfma_f32_16x16x32_bf16 v[168:171], v[224:227], v[56:59], v[168:171]\n\t"
            "v_cvt_pk_bf16_f32 v200, v200, v201\n\t"
            "v_cvt_pk_bf16_f32 v204, v204, v205\n\t"
            "v_mfma_f32_16x16x32_bf16 v[168:171], v[228:231], v[60:63], v[168:171]\n\t"
            "v_cvt_pk_bf16_f32 v216, v216, v217\n\t"
            "v_cvt_pk_bf16_f32 v220, v220, v221\n\t"
            "v_mfma_f32_16x16x32_bf16 v[172:175], v[228:231], v[84:87], v[172:175]\n\t"
            "v_cvt_pk_bf16_f32 v201, v202, v203\n\t"
            "v_cvt_pk_bf16_f32 v205, v206, v207\n\t"
            "v_mfma_f32_16x16x32_bf16 v[172:175], v[224:227], v[80:83], v[172:175]\n\t"
            "v_cvt_pk_bf16_f32 v217, v218, v219\n\t"
            "v_cvt_pk_bf16_f32 v221, v222, v223\n\t"
            "v_mfma_f32_16x16x32_bf16 v[152:155], v[224:227], v[104:107], v[152:155]\n\t"
            "v_mfma_f32_16x16x32_bf16 v[152:155], v[228:231], v[108:111], v[152:155]\n\t"
            "v_mfma_f32_16x16x32_bf16 v[156:159], v[228:231], v[132:135], v[156:159]\n\t"
            "ds_read_b128 v[228:231], v233 offset:12288\n\t"
            "v_mfma_f32_16x16x32_bf16 v[156:159], v[224:227], v[128:131], v[156:159]\n\t"
            "ds_read_b128 v[224:227], v232 offset:12288\n\t"
            "s_waitcnt lgkmcnt(3)\n\t"
            "v_mfma_f32_16x16x32_bf16 v[144:147], v[192:195], v[16:19], v[144:147]\n\t"
            "v_mfma_f32_16x16x32_bf16 v[144:147], v[196:199], v[20:23], v[144:147]\n\t"
            "v_mfma_f32_16x16x32_bf16 v[148:151], v[196:199], v[44:47], v[148:151]\n\t"
            "v_mfma_f32_16x16x32_bf16 v[148:151], v[192:195], v[40:43], v[148:151]\n\t"
            "v_mfma_f32_16x16x32_bf16 v[168:171], v[192:195], v[64:67], v[168:171]\n\t"
            "v_mfma_f32_16x16x32_bf16 v[168:171], v[196:199], v[68:71], v[168:171]\n\t"
            "ds_read_b64 v[234:235], v234 offset:7856\n\t"
            "v_mfma_f32_16x16x32_bf16 v[172:175], v[196:199], v[92:95], v[172:175]\n\t"
            "s_nop 1\n\t"
            "v_mul_f32 v144, v236, v144\n\t"
            "v_mfma_f32_16x16x32_bf16 v[172:175], v[192:195], v[88:91], v[172:175]\n\t"
            "v_mul_f32 v145, v237, v145\n\t"
            "s_waitcnt lgkmcnt(3)\n\t"
            "v_mul_f32 v146, v248, v146\n\t"
            "v_mfma_f32_16x16x32_bf16 v[152:155], v[192:195], v[112:115], v[152:155]\n\t"
            "v_mul_f32 v168, v236, v168\n\t"
            "v_mul_f32 v169, v237, v169\n\t"
            "v_mfma_f32_16x16x32_bf16 v[152:155], v[196:199], v[116:119], v[152:155]\n\t"
            "v_mul_f32 v170, v248, v170\n\t"
            "v_mul_f32 v171, v249, v171\n\t"
            "v_mfma_f32_16x16x32_bf16 v[156:159], v[196:199], v[140:143], v[156:159]\n\t"
            "ds_read_b128 v[196:199], v233 offset:12416\n\t"
            "v_mul_f32 v172, v236, v172\n\t"
            "v_mfma_f32_16x16x32_bf16 v[156:159], v[192:195], v[136:139], v[156:159]\n\t"
            "ds_read_b128 v[192:195], v232 offset:12416\n\t"
            "v_mul_f32 v173, v237, v173\n\t"
            "s_waitcnt lgkmcnt(3)\n\t"
            "v_mfma_f32_16x16x32_bf16 v[160:163], v[224:227], v[0:3], 0\n\t"
            "v_cvt_pk_bf16_f32 v214, v172, v173\n\t"
            "v_cvt_pk_bf16_f32 v210, v168, v169\n\t"
            "v_mfma_f32_16x16x32_bf16 v[160:163], v[228:231], v[4:7], v[160:163]\n\t"
            "s_waitcnt lgkmcnt(2)\n\t"
            "v_readfirstlane_b32 s77, v234\n\t"
            "v_readfirstlane_b32 s38, v235\n\t"
            "s_add_u32 s77, s77, s38\n\t"
            "s_cmp_ge_u32 s77, s44\n\t"
            "s_cbranch_scc0 .Lwc_spin%=\n\t"
            ".Lwc_go%=:\n\t"
            "v_mfma_f32_16x16x32_bf16 v[164:167], v[228:231], v[28:31], 0\n\t"
            "s_add_u32 s59, s59, s84\n\t"
            "s_movk_i32 s84, 800\n\t"
            "v_cvt_pk_bf16_f32 v211, v170, v171\n\t"
            "v_mfma_f32_16x16x32_bf16 v[164:167], v[224:227], v[24:27], v[164:167]\n\t"
            "s_add_u32 s45, s46, s27\n\t"
            "s_mov_b32 s46, s47\n\t"
            "s_mov_b32 s47, s68\n\t"
            "v_mfma_f32_16x16x32_bf16 v[176:179], v[224:227], v[48:51], 0\n\t"
            "v_mul_f32 v174, v248, v174\n\t"
            "v_mul_f32 v175, v249, v175\n\t"
            "v_mfma_f32_16x16x32_bf16 v[176:179], v[228:231], v[52:55], v[176:179]\n\t"
            "v_add_u32 v172, s67, v245\n\t"
            "v_cvt_pk_bf16_f32 v215, v174, v175\n\t"
            "v_mfma_f32_16x16x32_bf16 v[180:183], v[228:231], v[76:79], 0\n\t"
            "ds_read_b128 v[168:171], v172 offset:18816\n\t"
            "ds_read_b128 v[172:175], v172 offset:19840\n\t"
            "v_mfma_f32_16x16x32_bf16 v[180:183], v[224:227], v[72:75], v[180:183]\n\t"
            "v_mul_f32 v148, v236, v148\n\t"
            "v_mul_f32 v149, v237, v149\n\t"
            "v_mfma_f32_16x16x32_bf16 v[184:187], v[224:227], v[96:99], 0\n\t"
            "v_mul_f32 v152, v236, v152\n\t"
            "v_mul_f32 v153, v237, v153\n\t"
            "v_mfma_f32_16x16x32_bf16 v[184:187], v[228:231], v[100:103], v[184:187]\n\t"
            "v_mul_f32 v156, v236, v156\n\t"
            "v_mul_f32 v157, v237, v157\n\t"
            "v_mfma_f32_16x16x32_bf16 v[188:191], v[228:231], v[124:127], 0\n\t"
            "ds_read_b128 v[228:231], v233 offset:12544\n\t"
            "ds_read_b64 v[236:237], v247 offset:128\n\t"
            "v_mfma_f32_16x16x32_bf16 v[188:191], v[224:227], v[120:123], v[188:191]\n\t"
            "ds_read_b128 v[224:227], v232 offset:12544\n\t"
            "v_mul_f32 v147, v249, v147\n\t"
            "s_waitcnt lgkmcnt(5)\n\t"
            "v_mfma_f32_16x16x32_bf16 v[160:163], v[192:195], v[8:11], v[160:163]\n\t"
            "v_mul_f32 v150, v248, v150\n\t"
            "v_mul_f32 v151, v249, v151\n\t"
            "v_mfma_f32_16x16x32_bf16 v[160:163], v[196:199], v[12:15], v[160:163]\n\t"
            "v_mul_f32 v154, v248, v154\n\t"
            "v_mul_f32 v155, v249, v155\n\t"
            "v_mfma_f32_16x16x32_bf16 v[164:167], v[196:199], v[36:39], v[164:167]\n\t"
            "v_mul_f32 v158, v248, v158\n\t"
            "v_mul_f32 v159, v249, v159\n\t"
            "v_mfma_f32_16x16x32_bf16 v[164:167], v[192:195], v[32:35], v[164:167]\n\t"
            "ds_read_b64 v[248:249], v247 offset:136\n\t"
            "s_waitcnt lgkmcnt(5)\n\t"
            "v_add_f32 v168, v168, v170\n\t"
            "v_mfma_f32_16x16x32_bf16 v[176:179], v[192:195], v[56:59], v[176:179]\n\t"
            "s_waitcnt lgkmcnt(4)\n\t"
            "v_add_f32 v172, v172, v174\n\t"
            "v_add_f32 v168, v168, v172\n\t"
            "v_mfma_f32_16x16x32_bf16 v[176:179], v[196:199], v[60:63], v[176:179]\n\t"
            "v_add_f32 v169, v169, v171\n\t"
            "v_add_f32 v173, v173, v175\n\t"
            "v_mfma_f32_16x16x32_bf16 v[180:183], v[196:199], v[84:87], v[180:183]\n\t"
            "v_mul_f32 v168, 0x3baaaaab, v168\n\t"
            "v_add_f32 v169, v169, v173\n\t"
            "v_mfma_f32_16x16x32_bf16 v[180:183], v[192:195], v[80:83], v[180:183]\n\t"
            "ds_read_b128 v[172:175], v254 offset:64\n\t"
            "v_mul_f32_e64 v168, v168, -v168\n\t"
            "v_mfma_f32_16x16x32_bf16 v[184:187], v[192:195], v[104:107], v[184:187]\n\t"
            "v_fmac_f32 v168, 0x3baaaaab, v169\n\t"
            "v_max_f32 v168, 0, v168\n\t"
            "v_mfma_f32_16x16x32_bf16 v[184:187], v[196:199], v[108:111], v[184:187]\n\t"
            "v_cvt_pk_bf16_f32 v202, v144, v145\n\t"
            "v_cvt_pk_bf16_f32 v203, v146, v147\n\t"
            "v_mfma_f32_16x16x32_bf16 v[188:191], v[196:199], v[132:135], v[188:191]\n\t"
            "v_cvt_pk_bf16_f32 v206, v148, v149\n\t"
            "v_cvt_pk_bf16_f32 v207, v150, v151\n\t"
            "v_mfma_f32_16x16x32_bf16 v[188:191], v[192:195], v[128:131], v[188:191]\n\t"
            "v_cvt_pk_bf16_f32 v218, v152, v153\n\t"
            "v_cvt_pk_bf16_f32 v219, v154, v155\n\t"
            "s_waitcnt lgkmcnt(2)\n\t"
            "v_mfma_f32_16x16x32_bf16 v[160:163], v[224:227], v[16:19], v[160:163]\n\t"
            "v_cvt_pk_bf16_f32 v222, v156, v157\n\t"
            "v_cvt_pk_bf16_f32 v223, v158, v159\n\t"
            "v_mfma_f32_16x16x32_bf16 v[160:163], v[228:231], v[20:23], v[160:163]\n\t"
            "v_add_f32 v168, 0x3727c5ac, v168\n\t"
            "v_rsq_f32 v168, v168\n\t"
            "v_mfma_f32_16x16x32_bf16 v[164:167], v[228:231], v[44:47], v[164:167]\n\t"
            "v_add_u32 v232, s67, v242\n\t"
            "v_xor_b32 v233, 64, v232\n\t"
            "v_mfma_f32_16x16x32_bf16 v[164:167], v[224:227], v[40:43], v[164:167]\n\t"
            "s_xor_b32 s48, s48, s71\n\t"
            "s_xor_b32 s67, s67, s71\n\t"
            "s_add_u32 s76, s76, 4\n\t"
            "s_add_u32 s44, s44, 8\n\t"
            "v_mfma_f32_16x16x32_bf16 v[176:179], v[224:227], v[64:67], v[176:179]\n\t"
            "s_lshl_b32 s56, s56, 2\n\t"
            "s_lshr_b32 s49, s26, 30\n\t"
            "s_or_b32 s56, s56, s49\n\t"
            "v_mfma_f32_16x16x32_bf16 v[176:179], v[228:231], v[68:71], v[176:179]\n\t"
            "ds_read_b128 v[192:195], v232\n\t"
            "ds_read_b128 v[196:199], v233\n\t"
            "v_mfma_f32_16x16x32_bf16 v[180:183], v[228:231], v[92:95], v[180:183]\n\t"
            "v_mul_f32 v160, v236, v160\n\t"
            "v_mul_f32 v161, v237, v161\n\t"
            "v_mfma_f32_16x16x32_bf16 v[180:183], v[224:227], v[88:91], v[180:183]\n\t"
            "v_mul_f32 v164, v236, v164\n\t"
            "v_mul_f32 v165, v237, v165\n\t"
            "v_mfma_f32_16x16x32_bf16 v[184:187], v[224:227], v[112:115], v[184:187]\n\t"
            "v_mul_f32 v176, v236, v176\n\t"
            "v_mul_f32 v177, v237, v177\n\t"
            "v_mfma_f32_16x16x32_bf16 v[184:187], v[228:231], v[116:119], v[184:187]\n\t"
            "ds_write_b128 v243, v[200:203]\n\t"
            "v_mfma_f32_16x16x32_bf16 v[188:191], v[228:231], v[140:143], v[188:191]\n\t"
            "ds_read_b128 v[228:231], v233 offset:128\n\t"
            "v_mul_f32 v180, v236, v180\n\t"
            "v_mfma_f32_16x16x32_bf16 v[188:191], v[224:227], v[136:139], v[188:191]\n\t"
            "ds_read_b128 v[224:227], v232 offset:128\n\t"
            "v_mul_f32 v181, v237, v181\n\t"
            "s_waitcnt lgkmcnt(3)\n\t"
            "v_mfma_f32_16x16x32_bf16 v[200:203], v[192:195], v[0:3], 0\n\t"
            "v_mul_f32 v184, v236, v184\n\t"
            "v_mul_f32 v185, v237, v185\n\t"
            "v_mfma_f32_16x16x32_bf16 v[200:203], v[196:199], v[4:7], v[200:203]\n\t"
            "ds_write_b128 v243, v[204:207] offset:1536\n\t"
            "v_mfma_f32_16x16x32_bf16 v[204:207], v[196:199], v[28:31], 0\n\t"
            "v_mul_f32 v188, v236, v188\n\t"
            "v_mul_f32 v189, v237, v189\n\t"
            "v_mfma_f32_16x16x32_bf16 v[204:207], v[192:195], v[24:27], v[204:207]\n\t"
            "ds_write_b128 v243, v[208:211] offset:3072\n\t"
            "v_mfma_f32_16x16x32_bf16 v[208:211], v[192:195], v[48:51], 0\n\t"
            "v_mul_f32 v162, v248, v162\n\t"
            "v_mul_f32 v163, v249, v163\n\t"
            "v_mfma_f32_16x16x32_bf16 v[208:211], v[196:199], v[52:55], v[208:211]\n\t"
            "ds_write_b128 v243, v[212:215] offset:4608\n\t"
            "v_mfma_f32_16x16x32_bf16 v[212:215], v[196:199], v[76:79], 0\n\t"
            "v_cvt_pk_bf16_f32 v160, v160, v161\n\t"
            "v_mul_f32 v166, v248, v166\n\t"
            "v_mfma_f32_16x16x32_bf16 v[212:215], v[192:195], v[72:75], v[212:215]\n\t"
            "ds_write_b128 v243, v[216:219] offset:6144\n\t"
            "v_mfma_f32_16x16x32_bf16 v[216:219], v[192:195], v[96:99], 0\n\t"
            "v_mul_f32 v167, v249, v167\n\t"
            "v_cvt_pk_bf16_f32 v164, v164, v165\n\t"
            "v_mfma_f32_16x16x32_bf16 v[216:219], v[196:199], v[100:103], v[216:219]\n\t"
            "ds_write_b128 v243, v[220:223] offset:7680\n\t"
            "v_mfma_f32_16x16x32_bf16 v[220:223], v[196:199], v[124:127], 0\n\t"
            "v_mul_f32 v178, v248, v178\n\t"
            "v_mul_f32 v179, v249, v179\n\t"
            "v_mfma_f32_16x16x32_bf16 v[220:223], v[192:195], v[120:123], v[220:223]\n\t"
            "v_cvt_pk_bf16_f32 v176, v176, v177\n\t"
            "v_mul_f32 v182, v248, v182\n\t"
            "s_waitcnt lgkmcnt(5)\n\t"
            "v_mfma_f32_16x16x32_bf16 v[200:203], v[224:227], v[8:11], v[200:203]\n\t"
            "s_waitcnt vmcnt(0)\n\t"
            "ds_read_b64 v[144:145], v253\n\t"
            "ds_read_b64 v[146:147], v253 offset:96\n\t"
            "v_mfma_f32_16x16x32_bf16 v[200:203], v[228:231], v[12:15], v[200:203]\n\t"
            "ds_read_b64 v[148:149], v253 offset:1744\n\t"
            "ds_read_b64 v[150:151], v253 offset:1840\n\t"
            "v_mfma_f32_16x16x32_bf16 v[204:207], v[228:231], v[36:39], v[204:207]\n\t"
            "ds_read_b64 v[152:153], v253 offset:8\n\t"
            "ds_read_b64 v[154:155], v253 offset:104\n\t"
            "v_mfma_f32_16x16x32_bf16 v[204:207], v[224:227], v[32:35], v[204:207]\n\t"
            "ds_read_b64 v[156:157], v253 offset:1752\n\t"
            "ds_read_b64 v[158:159], v253 offset:1848\n\t"
            "v_mfma_f32_16x16x32_bf16 v[208:211], v[224:227], v[56:59], v[208:211]\n\t"
            "v_mul_f32 v183, v249, v183\n\t"
            "v_cvt_pk_bf16_f32 v180, v180, v181\n\t"
            "v_mfma_f32_16x16x32_bf16 v[208:211], v[228:231], v[60:63], v[208:211]\n\t"
            "v_mul_f32 v186, v248, v186\n\t"
            "v_mul_f32 v187, v249, v187\n\t"
            "v_mfma_f32_16x16x32_bf16 v[212:215], v[228:231], v[84:87], v[212:215]\n\t"
            "v_cvt_pk_bf16_f32 v184, v184, v185\n\t"
            "v_mul_f32 v190, v248, v190\n\t"
            "v_mfma_f32_16x16x32_bf16 v[212:215], v[224:227], v[80:83], v[212:215]\n\t"
            "v_mul_f32 v191, v249, v191\n\t"
            "v_cvt_pk_bf16_f32 v188, v188, v189\n\t"
            "v_mfma_f32_16x16x32_bf16 v[216:219], v[224:227], v[104:107], v[216:219]\n\t"
            "v_cvt_pk_bf16_f32 v161, v162, v163\n\t"
            "v_cvt_pk_bf16_f32 v165, v166, v167\n\t"
            "v_mfma_f32_16x16x32_bf16 v[216:219], v[228:231], v[108:111], v[216:219]\n\t"
            "ds_read_b64 v[162:163], v253 offset:112\n\t"
            "ds_read_b64 v[166:167], v253 offset:1856\n\t"
            "v_mfma_f32_16x16x32_bf16 v[220:223], v[228:231], v[132:135], v[220:223]\n\t"
            "s_waitcnt lgkmcnt(14)\n\t"
            "ds_read_b128 v[228:231], v233 offset:256\n\t"
            "v_cvt_pk_bf16_f32 v177, v178, v179\n\t"
            "v_mfma_f32_16x16x32_bf16 v[220:223], v[224:227], v[128:131], v[220:223]\n\t"
            "s_waitcnt lgkmcnt(14)\n\t"
            "ds_read_b128 v[224:227], v232 offset:256\n\t"
            "v_cvt_pk_bf16_f32 v181, v182, v183\n\t"
            "v_cvt_pk_bf16_f32 v185, v186, v187\n\t"
            "v_cvt_pk_bf16_f32 v189, v190, v191\n\t"
            "v_xor_b32 v247, 0x100, v247\n\t"
            "s_waitcnt lgkmcnt(14)\n\t"
            "ds_write_b64 v244, v[160:161]\n\t"
            "s_waitcnt lgkmcnt(14)\n\t"
            "ds_read_b64 v[160:161], v253 offset:16\n\t"
            "s_waitcnt lgkmcnt(14)\n\t"
            "ds_write_b64 v244, v[164:165] offset:1536\n\t"
            "s_waitcnt lgkmcnt(14)\n\t"
            "ds_read_b64 v[164:165], v253 offset:1760\n\t"
            "s_waitcnt lgkmcnt(14)\n\t"
            "ds_write_b64 v244, v[176:177] offset:3072\n\t"
            "s_waitcnt lgkmcnt(14)\n\t"
            "ds_write_b64 v244, v[180:181] offset:4608\n\t"
            "s_waitcnt lgkmcnt(14)\n\t"
            "ds_write_b64 v244, v[184:185] offset:6144\n\t"
            "s_waitcnt lgkmcnt(14)\n\t"
            "ds_write_b64 v244, v[188:189] offset:7680\n\t"
            "s_waitcnt lgkmcnt(14)\n\t"
            "ds_write_b32 v246, v168\n\t"
            "s_waitcnt lgkmcnt(14)\n\t"
            "ds_read_b128 v[168:171], v254\n\t"
            "s_waitcnt lgkmcnt(1)\n\t"
            "s_barrier\n\t"
            "s_cmp_lt_u32 s46, 0x7000000\n\t"
            "s_cbranch_scc1 .Lwc_loop%=\n\t"
            "s_branch .Lwc_end%=\n\t"
            ".Lwc_spin%=:\n\t"
            "s_cmp_eq_u32 s83, 0\n\t"
            "s_cbranch_scc1 .Lwc_go%=\n\t"
            "s_sub_u32 s83, s83, 1\n\t"
            "s_sleep 2\n\t"
            "v_mov_b32 v234, 0\n\t"
            "ds_read_b64 v[234:235], v234 offset:7856\n\t"
            "s_waitcnt lgkmcnt(0)\n\t"
            "v_readfirstlane_b32 s77, v234\n\t"
            "v_readfirstlane_b32 s38, v235\n\t"
            "s_add_u32 s77, s77, s38\n\t"
            "s_cmp_ge_u32 s77, s44\n\t"
            "s_cbranch_scc0 .Lwc_spin%=\n\t"
            "s_branch .Lwc_go%=\n\t"
            ".Lwc_end%=:\n\t"
            "s_waitcnt lgkmcnt(0)\n\t"
            "s_nop 7\n\t"
            : "+{v246}"(aRSw_), "+{v247}"(aRSr_), "+{s44}"(s44), B_SIO
            : "{v242}"(pE_), "{v243}"(aVAw_), "{v244}"(aVBw_), "{v245}"(pST_), "{v250}"(pXw_), "{v251}"(pSTw_), "{v252}"(aCA_), "{v253}"(aB_), "{v254}"(aA_), "{v255}"(idx_), B_SIN, "{s82}"(touch)
            : "v144", "v145", "v146", "v147", "v148", "v149", "v150", "v151", "v152", "v153", "v154", "v155", "v156", "v157", "v158", "v159", "v160", "v161", "v162", "v163", "v164", "v165", "v166", "v167", "v168", "v169", "v170", "v171", "v172", "v173", "v174", "v175", "v176", "v177", "v178", "v179", "v180", "v181", "v182", "v183", "v184", "v185", "v186", "v187", "v188", "v189", "v190", "v191", "v192", "v193", "v194", "v195", "v196", "v197", "v198", "v199", "v236", "v237", "v248", "v249", "v0", "v1", "v2", "v3", "v4", "v5", "v6", "v7", "v8", "v9", "v10", "v11", "v12", "v13", "v14", "v15", "v16", "v17", "v18", "v19", "v20", "v21", "v22", "v23", "v24", "v25", "v26", "v27", "v28", "v29", "v30", "v31", "v32", "v33", "v34", "v35", "v36", "v37", "v38", "v39", "v40", "v41", "v42", "v43", "v44", "v45", "v46", "v47", "v48", "v49", "v50", "v51", "v52", "v53", "v54", "v55", "v56", "v57", "v58", "v59", "v60", "v61", "v62", "v63", "v64", "v65", "v66", "v67", "v68", "v69", "v70", "v71", "v72", "v73", "v74", "v75", "v76", "v77", "v78", "v79", "v80", "v81", "v82", "v83", "v84", "v85", "v86", "v87", "v88", "v89", "v90", "v91", "v92", "v93", "v94", "v95", "v96", "v97", "v98", "v99", "v100", "v101", "v102", "v103", "v104", "v105", "v106", "v107", "v108", "v109", "v110", "v111", "v112", "v113", "v114", "v115", "v116", "v117", "v118", "v119", "v120", "v121", "v122", "v123", "v124", "v125", "v126", "v127", "v128", "v129", "v130", "v131", "v132", "v133", "v134", "v135", "v136", "v137", "v138", "v139", "v140", "v141", "v142", "v143", "v200", "v201", "v202", "v204", "v205", "v206", "v207", "v208", "v209", "v210", "v211", "v212", "v213", "v214", "v215", "v216", "v217", "v218", "v219", "v220", "v221", "v222", "v223", "v224", "v225", "v226", "v227", "v228", "v229", "v230", "v231", "v232", "v233", "v234", "v235", B_SCLOB);
        }
        {
            __builtin_amdgcn_s_setprio(3);
            int ln_;
            asm volatile("v_mbcnt_lo_u32_b32 %0, -1, 0\n\tv_mbcnt_hi_u32_b32 %0, -1, %0" : "=v"(ln_));
            const int l15_ = ln_ & 15, g_ = ln_ >> 4;
            KARGS
            const auto orsrc = __builtin_amdgcn_make_buffer_rsrc((void*)ob, 0, IMG * IMG * CH * 2, 0x00020000);
            const auto wq = __builtin_amdgcn_make_buffer_rsrc((void*)wb, 0, WS_BYTES, 0x00020000);
            bf16x8 W[36];
#define WO_LOAD(k0)                                                                                \
    SB();                                                                                          \
    _Pragma("unroll") for (int ks = (k0); ks < (k0) + 2; ++ks)                                     \
        _Pragma("unroll") for (int j = 0; j < 6; ++j)                                              \
            W[j * 6 + ks] = BC8(__builtin_amdgcn_raw_buffer_load_b128(wq, ln_ * 16, (wave + 2) * 36864 + (j * 6 + ks) * 1024, SC1)); \
    SB();
            int lE[2], nw;
            unsigned o[2];
            f32x4 acc[2][6];
#pragma unroll
            for (int lt = 0; lt < 2; ++lt) {
                WO_LOAD(2 * lt)
                int i = 16 * lt + l15_;
                const unsigned en = *(const unsigned*)(smem + WL_OFF + i * 4);
                const bool ok = en != 0x7ffffffu;
                if (lt == 1) nw = 16 + __builtin_popcount((unsigned)__builtin_amdgcn_ballot_w64(ok) & 0xffffu);
                i = ok ? i : 0;
                lE[lt] = OL_OFF + i * 384 + ((g_ ^ ((i >> 1) & 7)) << 4);
                o[lt] = ok ? (en & 0x7ffffffu) + LPOS(en >> 31) * ROWBYTES + LPOS(en >> 30) * (CH * 2) + 192 * w + 16 * g_ : OOB;
#pragma unroll
                for (int k = 0; k < 3; ++k) {
                    const int c8 = 12 * w + 4 * k + g_, ws = (c8 * 43) >> 8, r6 = c8 - 6 * ws;
                    const u32x4 r = *(const u32x4*)(smem + LX_OFF(i) + (r6 >> 1) * 128 + ((2 * ws + (r6 & 1)) << 4));
                    acc[lt][2 * k] = (f32x4){bflo(r[0]), bfhi(r[0]), bflo(r[1]), bfhi(r[1])};
                    acc[lt][2 * k + 1] = (f32x4){bflo(r[2]), bfhi(r[2]), bflo(r[3]), bfhi(r[3])};
                }
            }
            bf16x8 of[2][6];
#pragma unroll
            for (int ks = 0; ks < 6; ++ks) of[0][ks] = *(const bf16x8*)(smem + ((ks & 1) ? (lE[0] ^ 64) : lE[0]) + (ks >> 1) * 128);
            WO_LOAD(4)
#pragma unroll
            for (int i = 0; i < 12; ++i) OPQ(acc[i / 6][i % 6]);
            OPQ(lE[1]);
#pragma unroll 1
            for (int it_ = 0; it_ < 100000; ++it_) {
                unsigned n_;
                asm volatile("ds_read_b32 %0, %1\n\ts_waitcnt lgkmcnt(0)" : "=v"(n_) : "v"(ldsb + FLAG_OFF) : "memory");
                if (__builtin_amdgcn_readfirstlane(n_) >= 4 * nw) break;
                __builtin_amdgcn_s_sleep(1);
            }
#pragma unroll
            for (int ks = 0; ks < 6; ++ks) of[1][ks] = *(const bf16x8*)(smem + ((ks & 1) ? (lE[1] ^ 64) : lE[1]) + (ks >> 1) * 128);
            SB();
#pragma unroll
            for (int lt = 0; lt < 2; ++lt) {
#pragma unroll
                for (int ks = 0; ks < 6; ++ks)
#pragma unroll
                    for (int j = 0; j < 6; ++j) acc[lt][j] = MFMA32(W[j * 6 + ks], of[lt][ks], acc[lt][j]);
#pragma unroll
                for (int k = 0; k < 3; ++k) {
                    const f32x4 a = acc[lt][2 * k], b = acc[lt][2 * k + 1];
                    const u32x4 ov = {pk2(a[0], a[1]), pk2(a[2], a[3]), pk2(b[0], b[1]), pk2(b[2], b[3])};
                    __builtin_amdgcn_raw_buffer_store_b128(ov, orsrc, o[lt] + 64 * k, 0, SC1);
                }
            }
        }
        __builtin_amdgcn_endpgm();
    } else {
        LANE_SETUP
        {
            const unsigned oE_ = ldsb + fE + (O_OFF - X_OFF), oO_ = oE_ ^ 64;
            unsigned l16_ = lane * 16;
            unsigned pr[3];
#pragma unroll
            for (int k = 0; k < 3; ++k) {
                const int c8 = 12 * (w - 2) + 4 * k + g;
                const int ws = c8 / 6, r6 = c8 - 6 * ws;
                pr[k] = l15 * 384 + (r6 >> 1) * 128 + (((2 * ws + (r6 & 1)) ^ sw) << 4);
            }
            const unsigned pR0_ = pr[0], pR1_ = pr[1], pR2_ = pr[2];
            unsigned q0 = ldsb + PF_OFF + 1024 + lane * 4, q1 = ldsb + O_OFF + lane * 16, q2 = oE_, q3 = ldsb + PF_OFF + 1024 + l15 * 4;
            unsigned q4 = ldsb + OL_OFF + l15 * 384 + (((4 * (w - 2) + (g >> 1)) ^ sw) << 4) + 8 * (g & 1);
            B_SCALARS
            asm volatile(
            "s_waitcnt lgkmcnt(0)\n\t"
            "s_mov_b32 s83, 0x10000\n\t"
            "s_mov_b32 s84, -47104\n\t"
            "s_nop 7\n\t"
            "v_mov_b32 v200, v250\n\t"
            "v_mov_b32 v201, v251\n\t"
            "v_mov_b32 v202, v253\n\t"
            "s_mov_b32 s38, s67\n\t"
            "s_mov_b32 s67, 74496\n\t"
            "ds_read_b64 v[250:251], v203\n\t"
            "ds_read_b32 v253, v203 offset:8\n\t"
            ".Lwk_b1%=:\n\t"
            "v_mov_b32 v236, 0\n\t"
            "ds_read_b32 v236, v236 offset:7864\n\t"
            "s_waitcnt lgkmcnt(0)\n\t"
            "v_readfirstlane_b32 s77, v236\n\t"
            "s_cmp_ge_u32 s77, 4\n\t"
            "s_cbranch_scc1 .Lwk_b2%=\n\t"
            "s_cmp_eq_u32 s83, 0\n\t"
            "s_cbranch_scc1 .Lwk_b2%=\n\t"
            "s_sub_u32 s83, s83, 1\n\t"
            "s_sleep 1\n\t"
            "s_branch .Lwk_b1%=\n\t"
            ".Lwk_b2%=:\n\t"
            "s_waitcnt vmcnt(0)\n\t"
            "s_call_b64 vcc, wk_conv\n\t"
            "v_mov_b32 v250, v200\n\t"
            "v_mov_b32 v251, v201\n\t"
            "v_mov_b32 v253, v202\n\t"
            "s_mov_b32 s67, s38\n\t"
            "s_barrier\n\t"
            "ds_read_b64 v[236:237], v252 offset:2560\n\t"
            "s_waitcnt lgkmcnt(0)\n\t"
            "s_mov_b64 exec, 1\n\t"
            "buffer_store_dwordx2 v[236:237], off, s[28:31], s44 sc1\n\t"
            "s_mov_b64 exec, -1\n\t"
            "s_sleep 30\n\t"
            "ds_read_b32 v249, v252 offset:12\n\t"
            "s_waitcnt lgkmcnt(0)\n\t"
            "v_and_b32 v249, s69, v249\n\t"
            "s_mov_b32 exec_hi, 0x3fffff\n\t"
            "s_add_u32 m0, s66, 0\n\t"
            "s_add_u32 s49, s79, 0\n\t"
            "buffer_load_dwordx4 v249, s[72:75], s49 offen lds\n\t"
            "s_mov_b32 exec_hi, -1\n\t"
            "s_mov_b32 exec_hi, 0x3fffff\n\t"
            "s_add_u32 m0, s66, 872\n\t"
            "s_add_u32 s49, s47, 196224\n\t"
            "buffer_load_dwordx4 v249, s[72:75], s49 offen lds\n\t"
            "s_mov_b32 exec_hi, -1\n\t"
            "s_mov_b32 exec_hi, 0x3fffff\n\t"
            "s_add_u32 m0, s66, 1744\n\t"
            "s_add_u32 s49, s47, 392448\n\t"
            "buffer_load_dwordx4 v249, s[72:75], s49 offen lds\n\t"
            "s_mov_b32 exec_hi, -1\n\t"
            "s_mov_b32 exec_hi, 0x3fffff\n\t"
            "s_add_u32 m0, s66, 2616\n\t"
            "s_add_u32 s49, s47, 588672\n\t"
            "buffer_load_dwordx4 v249, s[72:75], s49 offen lds\n\t"
            "s_mov_b32 exec_hi, -1\n\t"
            "s_mov_b32 exec_hi, 0x3fffff\n\t"
            "s_add_u32 m0, s66, 3488\n\t"
            "s_add_u32 s49, s47, 784896\n\t"
            "buffer_load_dwordx4 v249, s[72:75], s49 offen lds\n\t"
            "s_mov_b32 exec_hi, -1\n\t"
            "s_mov_b32 exec_hi, 0x3fffff\n\t"
            "s_add_u32 m0, s66, 4360\n\t"
            "s_add_u32 s49, s47, 981120\n\t"
            "buffer_load_dwordx4 v249, s[72:75], s49 offen lds\n\t"
            "s_mov_b32 exec_hi, -1\n\t"
            "s_mov_b32 exec_hi, 0x3fffff\n\t"
            "s_add_u32 m0, s66, 5232\n\t"
            "s_add_u32 s49, s47, 1177344\n\t"
            "buffer_load_dwordx4 v249, s[72:75], s49 offen lds\n\t"
            "s_mov_b32 exec_hi, -1\n\t"
            "s_mov_b32 exec_hi, 0x3fffff\n\t"
            "s_add_u32 m0, s66, 6104\n\t"
            "s_add_u32 s49, s47, 1373568\n\t"
            "buffer_load_dwordx4 v249, s[72:75], s49 offen lds\n\t"
            "s_mov_b32 exec_hi, -1\n\t"
            "s_mov_b32 exec_hi, 0x3fffff\n\t"
            "s_add_u32 m0, s66, 6976\n\t"
            "s_add_u32 s49, s47, 1569792\n\t"
            "buffer_load_dwordx4 v249, s[72:75], s49 offen lds\n\t"
            "s_mov_b32 exec_hi, -1\n\t"
            "s_add_u32 s49, s33, -73728\n\t"
            "buffer_load_dwordx4 v[0:3], v247, s[28:31], s49 offen sc1\n\t"
            "s_add_u32 s49, s33, -67584\n\t"
            "buffer_load_dwordx4 v[24:27], v247, s[28:31], s49 offen sc1\n\t"
            "s_add_u32 s49, s33, -61440\n\t"
            "buffer_load_dwordx4 v[48:51], v247, s[28:31], s49 offen sc1\n\t"
            "s_add_u32 s49, s33, -55296\n\t"
            "buffer_load_dwordx4 v[72:75], v247, s[28:31], s49 offen sc1\n\t"
            "s_add_u32 s49, s33, -49152\n\t"
            "buffer_load_dwordx4 v[96:99], v247, s[28:31], s49 offen sc1\n\t"
            "s_add_u32 s49, s33, -43008\n\t"
            "buffer_load_dwordx4 v[120:123], v247, s[28:31], s49 offen sc1\n\t"
            "s_add_u32 s49, s33, -72704\n\t"
            "buffer_load_dwordx4 v[4:7], v247, s[28:31], s49 offen sc1\n\t"
            "s_add_u32 s49, s33, -66560\n\t"
            "buffer_load_dwordx4 v[28:31], v247, s[28:31], s49 offen sc1\n\t"
            "s_add_u32 s49, s33, -60416\n\t"
            "buffer_load_dwordx4 v[52:55], v247, s[28:31], s49 offen sc1\n\t"
            "s_add_u32 s49, s33, -54272\n\t"
            "buffer_load_dwordx4 v[76:79], v247, s[28:31], s49 offen sc1\n\t"
            "s_add_u32 s49, s33, -48128\n\t"
            "buffer_load_dwordx4 v[100:103], v247, s[28:31], s49 offen sc1\n\t"
            "s_add_u32 s49, s33, -41984\n\t"
            "buffer_load_dwordx4 v[124:127], v247, s[28:31], s49 offen sc1\n\t"
            "s_add_u32 s49, s33, -71680\n\t"
            "buffer_load_dwordx4 v[8:11], v247, s[28:31], s49 offen sc1\n\t"
            "s_add_u32 s49, s33, -65536\n\t"
            "buffer_load_dwordx4 v[32:35], v247, s[28:31], s49 offen sc1\n\t"
            "s_add_u32 s49, s33, -59392\n\t"
            "buffer_load_dwordx4 v[56:59], v247, s[28:31], s49 offen sc1\n\t"
            "s_add_u32 s49, s33, -53248\n\t"
            "buffer_load_dwordx4 v[80:83], v247, s[28:31], s49 offen sc1\n\t"
            "s_add_u32 s49, s33, -47104\n\t"
            "buffer_load_dwordx4 v[104:107], v247, s[28:31], s49 offen sc1\n\t"
            "s_add_u32 s49, s33, -40960\n\t"
            "buffer_load_dwordx4 v[128:131], v247, s[28:31], s49 offen sc1\n\t"
            "s_add_u32 s49, s33, -70656\n\t"
            "buffer_load_dwordx4 v[12:15], v247, s[28:31], s49 offen sc1\n\t"
            "s_add_u32 s49, s33, -64512\n\t"
            "buffer_load_dwordx4 v[36:39], v247, s[28:31], s49 offen sc1\n\t"
            "s_add_u32 s49, s33, -58368\n\t"
            "buffer_load_dwordx4 v[60:63], v247, s[28:31], s49 offen sc1\n\t"
            "s_add_u32 s49, s33, -52224\n\t"
            "buffer_load_dwordx4 v[84:87], v247, s[28:31], s49 offen sc1\n\t"
            "s_add_u32 s49, s33, -46080\n\t"
            "buffer_load_dwordx4 v[108:111], v247, s[28:31], s49 offen sc1\n\t"
            "s_add_u32 s49, s33, -39936\n\t"
            "buffer_load_dwordx4 v[132:135], v247, s[28:31], s49 offen sc1\n\t"
            "s_add_u32 s49, s33, -69632\n\t"
            "buffer_load_dwordx4 v[16:19], v247, s[28:31], s49 offen sc1\n\t"
            "s_add_u32 s49, s33, -63488\n\t"
            "buffer_load_dwordx4 v[40:43], v247, s[28:31], s49 offen sc1\n\t"
            "s_add_u32 s49, s33, -57344\n\t"
            "buffer_load_dwordx4 v[64:67], v247, s[28:31], s49 offen sc1\n\t"
            "s_add_u32 s49, s33, -51200\n\t"
            "buffer_load_dwordx4 v[88:91], v247, s[28:31], s49 offen sc1\n\t"
            "s_add_u32 s49, s33, -45056\n\t"
            "buffer_load_dwordx4 v[112:115], v247, s[28:31], s49 offen sc1\n\t"
            "s_add_u32 s49, s33, -38912\n\t"
            "buffer_load_dwordx4 v[136:139], v247, s[28:31], s49 offen sc1\n\t"
            "s_add_u32 s49, s33, -68608\n\t"
            "buffer_load_dwordx4 v[20:23], v247, s[28:31], s49 offen sc1\n\t"
            "s_add_u32 s49, s33, -62464\n\t"
            "buffer_load_dwordx4 v[44:47], v247, s[28:31], s49 offen sc1\n\t"
            "s_add_u32 s49, s33, -56320\n\t"
            "buffer_load_dwordx4 v[68:71], v247, s[28:31], s49 offen sc1\n\t"
            "s_add_u32 s49, s33, -50176\n\t"
            "buffer_load_dwordx4 v[92:95], v247, s[28:31], s49 offen sc1\n\t"
            "s_add_u32 s49, s33, -44032\n\t"
            "buffer_load_dwordx4 v[116:119], v247, s[28:31], s49 offen sc1\n\t"
            "s_add_u32 s49, s33, -37888\n\t"
            "buffer_load_dwordx4 v[140:143], v247, s[28:31], s49 offen sc1\n\t"
            "s_setprio 3\n\t"
            "ds_read_b128 v[168:171], v254\n\t"
            "ds_read_b128 v[172:175], v254 offset:64\n\t"
            "ds_read_b128 v[220:223], v229 offset:12288\n\t"
            "ds_read_b128 v[224:227], v229 offset:13312\n\t"
            "ds_read_b128 v[200:203], v232\n\t"
            "ds_read_b128 v[208:211], v232 offset:128\n\t"
            "ds_read_b128 v[216:219], v232 offset:256\n\t"
            "s_waitcnt vmcnt(36)\n\t"
            "ds_read_b64 v[144:145], v253\n\t"
            "ds_read_b64 v[146:147], v253 offset:96\n\t"
            "ds_read_b64 v[148:149], v253 offset:1744\n\t"
            "ds_read_b64 v[150:151], v253 offset:1840\n\t"
            "ds_read_b64 v[152:153], v253 offset:8\n\t"
            "ds_read_b64 v[154:155], v253 offset:104\n\t"
            "ds_read_b64 v[156:157], v253 offset:1752\n\t"
            "ds_read_b64 v[158:159], v253 offset:1848\n\t"
            "s_waitcnt lgkmcnt(14)\n\t"
            "ds_read_b64 v[160:161], v253 offset:16\n\t"
            "s_waitcnt lgkmcnt(14)\n\t"
            "ds_read_b64 v[162:163], v253 offset:112\n\t"
            "s_waitcnt lgkmcnt(14)\n\t"
            "ds_read_b64 v[164:165], v253 offset:1760\n\t"
            "s_waitcnt lgkmcnt(14)\n\t"
            "ds_read_b64 v[166:167], v253 offset:1856\n\t"
            "v_mov_b64 v[176:177], 0\n\t"
            "v_mov_b64 v[178:179], 0\n\t"
            "v_mov_b64 v[180:181], 0\n\t"
            "v_mov_b64 v[182:183], 0\n\t"
            "v_mov_b32 v249, s76\n\t"
            "v_mov_b64 v[184:185], 0\n\t"
            "v_mov_b64 v[186:187], 0\n\t"
            "v_mov_b64 v[188:189], 0\n\t"
            "v_mov_b64 v[190:191], 0\n\t"
            "v_add_u32 v236, s67, v250\n\t"
            "v_add_u32 v248, s67, v251\n\t"
            "v_xor_b32 v235, 64, v232\n\t"
            "s_nop 0\n\t"
            "s_waitcnt lgkmcnt(14)\n\t"
            "ds_read_b32 v249, v249\n\t"
            "s_waitcnt lgkmcnt(14)\n\t"
            "ds_read_b128 v[204:207], v235\n\t"
            "s_waitcnt lgkmcnt(14)\n\t"
            "ds_read_b128 v[212:215], v235 offset:128\n\t"
            "v_add_f32 v220, v220, v222\n\t"
            "v_add_f32 v224, v224, v226\n\t"
            "v_add_f32 v220, v220, v224\n\t"
            "v_add_f32 v221, v221, v223\n\t"
            "v_add_f32 v225, v225, v227\n\t"
            "v_mul_f32 v220, 0x3baaaaab, v220\n\t"
            "v_add_f32 v221, v221, v225\n\t"
            "v_mul_f32_e64 v220, v220, -v220\n\t"
            "v_fmac_f32 v220, 0x3baaaaab, v221\n\t"
            "s_waitcnt lgkmcnt(11)\n\t"
            "v_smfmac_f32_16x16x64_bf16 v[176:179], v[168:171], v[144:151], v255\n\t"
            "ds_read_b128 v[168:171], v254 offset:128\n\t"
            "s_waitcnt lgkmcnt(8)\n\t"
            "v_smfmac_f32_16x16x64_bf16 v[180:183], v[172:175], v[152:159], v255\n\t"
            "ds_read_b64 v[144:145], v253 offset:24\n\t"
            "ds_read_b64 v[146:147], v253 offset:120\n\t"
            "ds_read_b64 v[148:149], v253 offset:1768\n\t"
            "ds_read_b64 v[150:151], v253 offset:1864\n\t"
            "ds_read_b128 v[172:175], v254 offset:192\n\t"
            "ds_read_b64 v[152:153], v253 offset:32\n\t"
            "ds_read_b64 v[154:155], v253 offset:128\n\t"
            "s_waitcnt lgkmcnt(14)\n\t"
            "ds_read_b64 v[156:157], v253 offset:1776\n\t"
            "s_waitcnt lgkmcnt(14)\n\t"
            "ds_read_b64 v[158:159], v253 offset:1872\n\t"
            "s_waitcnt lgkmcnt(14)\n\t"
            "ds_read_b128 v[224:227], v232 offset:6144\n\t"
            "v_cvt_pk_bf16_f32 v176, v176, v177\n\t"
            "v_cvt_pk_bf16_f32 v177, v178, v179\n\t"
            "v_cvt_pk_bf16_f32 v178, v180, v181\n\t"
            "v_cvt_pk_bf16_f32 v179, v182, v183\n\t"
            "v_mfma_f32_4x4x4_16b_bf16 v[196:199], v[176:177], v[176:177], 0\n\t"
            "v_mov_b64 v[180:181], 0\n\t"
            "v_mfma_f32_16x16x32_bf16 v[192:195], v[238:241], v[176:179], 0\n\t"
            "s_waitcnt lgkmcnt(13)\n\t"
            "v_readfirstlane_b32 s26, v249\n\t"
            "v_mov_b64 v[182:183], 0\n\t"
            "v_mfma_f32_4x4x4_16b_bf16 v[196:199], v[178:179], v[178:179], v[196:199]\n\t"
            "ds_read_b32 v249, v252 offset:12\n\t"
            "s_and_b32 s68, s26, 0x7ffffff\n\t"
            "s_add_u32 s68, s68, s78\n\t"
            "s_and_b32 s69, s26, 0x38000000\n\t"
            "s_or_b32 s69, s69, s39\n\t"
            "s_bitcmp1_b32 s26, 27\n\t"
            "s_cselect_b32 s79, 2.0, s68\n\t"
            "s_and_b32 s57, s56, 3\n\t"
            "s_lshl_b32 s58, s57, 3\n\t"
            "s_lshr_b32 m0, s54, s58\n\t"
            "s_lshl_b32 s58, s57, 4\n\t"
            "s_lshr_b64 s[36:37], s[50:51], s58\n\t"
            "s_and_b32 s57, s36, 0xffff\n\t"
            "s_add_u32 s57, s57, s67\n\t"
            "v_writelane_b32 v236, s57, 63\n\t"
            "v_writelane_b32 v236, s59, m0\n\t"
            "v_xor_b32 v237, 16, v236\n\t"
            "s_lshr_b64 s[36:37], s[52:53], s58\n\t"
            "s_and_b32 s58, s36, 0xffff\n\t"
            "s_add_u32 s58, s58, s67\n\t"
            "s_waitcnt lgkmcnt(11)\n\t"
            "v_smfmac_f32_16x16x64_bf16 v[184:187], v[168:171], v[160:167], v255\n\t"
            "s_and_b32 s38, s52, 0xffff\n\t"
            "s_add_u32 s38, s38, s67\n\t"
            "ds_read_b128 v[168:171], v254 offset:256\n\t"
            "ds_read_b64 v[160:161], v253 offset:40\n\t"
            "ds_read_b64 v[162:163], v253 offset:136\n\t"
            "ds_read_b64 v[164:165], v253 offset:1784\n\t"
            "s_waitcnt lgkmcnt(14)\n\t"
            "ds_read_b64 v[166:167], v253 offset:1880\n\t"
            "s_waitcnt lgkmcnt(11)\n\t"
            "v_smfmac_f32_16x16x64_bf16 v[188:191], v[172:175], v[144:151], v255\n\t"
            "ds_read_b128 v[172:175], v254 offset:320\n\t"
            "ds_read_b64 v[144:145], v253 offset:48\n\t"
            "ds_read_b64 v[146:147], v253 offset:144\n\t"
            "ds_read_b64 v[148:149], v253 offset:1792\n\t"
            "s_waitcnt lgkmcnt(14)\n\t"
            "ds_read_b64 v[150:151], v253 offset:1888\n\t"
            "s_waitcnt lgkmcnt(14)\n\t"
            "ds_write_b128 v236, v[176:179]\n\t"
            "v_mov_b64 v[176:177], 0\n\t"
            "v_mov_b64 v[178:179], 0\n\t"
            "v_cvt_pk_bf16_f32 v184, v184, v185\n\t"
            "v_cvt_pk_bf16_f32 v185, v186, v187\n\t"
            "v_cvt_pk_bf16_f32 v186, v188, v189\n\t"
            "v_cvt_pk_bf16_f32 v187, v190, v191\n\t"
            "v_mfma_f32_4x4x4_16b_bf16 v[196:199], v[184:185], v[184:185], v[196:199]\n\t"
            "s_waitcnt lgkmcnt(14)\n\t"
            "ds_write_b128 v237, v[184:187]\n\t"
            "v_mfma_f32_16x16x32_bf16 v[192:195], v[238:241], v[184:187], v[192:195]\n\t"
            "v_mov_b64 v[184:185], 0\n\t"
            "v_mov_b64 v[188:189], 0\n\t"
            "v_mfma_f32_4x4x4_16b_bf16 v[196:199], v[186:187], v[186:187], v[196:199]\n\t"
            "v_mov_b64 v[186:187], 0\n\t"
            "v_mov_b64 v[190:191], 0\n\t"
            "v_writelane_b32 v248, s58, 63\n\t"
            "v_writelane_b32 v248, s38, m0\n\t"
            "s_waitcnt lgkmcnt(12)\n\t"
            "v_and_b32 v249, s69, v249\n\t"
            "v_max_f32 v220, 0, v220\n\t"
            "v_add_f32 v220, 0x3727c5ac, v220\n\t"
            "v_rsq_f32 v220, v220\n\t"
            "s_waitcnt lgkmcnt(11)\n\t"
            "v_smfmac_f32_16x16x64_bf16 v[176:179], v[168:171], v[152:159], v255\n\t"
            "ds_read_b128 v[168:171], v254 offset:384\n\t"
            "ds_read_b64 v[152:153], v253 offset:56\n\t"
            "ds_read_b64 v[154:155], v253 offset:152\n\t"
            "ds_read_b64 v[156:157], v253 offset:1800\n\t"
            "s_waitcnt lgkmcnt(14)\n\t"
            "ds_read_b64 v[158:159], v253 offset:1896\n\t"
            "s_waitcnt lgkmcnt(11)\n\t"
            "v_smfmac_f32_16x16x64_bf16 v[180:183], v[172:175], v[160:167], v255\n\t"
            "ds_read_b128 v[172:175], v254 offset:448\n\t"
            "ds_read_b64 v[160:161], v253 offset:64\n\t"
            "ds_read_b64 v[162:163], v253 offset:160\n\t"
            "ds_read_b64 v[164:165], v253 offset:1808\n\t"
            "s_waitcnt lgkmcnt(14)\n\t"
            "ds_read_b64 v[166:167], v253 offset:1904\n\t"
            "v_cvt_pk_bf16_f32 v176, v176, v177\n\t"
            "v_cvt_pk_bf16_f32 v177, v178, v179\n\t"
            "v_cvt_pk_bf16_f32 v178, v180, v181\n\t"
            "v_cvt_pk_bf16_f32 v179, v182, v183\n\t"
            "v_mfma_f32_4x4x4_16b_bf16 v[196:199], v[176:177], v[176:177], v[196:199]\n\t"
            "s_waitcnt lgkmcnt(14)\n\t"
            "ds_write_b128 v236, v[176:179] offset:128\n\t"
            "v_mfma_f32_16x16x32_bf16 v[192:195], v[238:241], v[176:179], v[192:195]\n\t"
            "v_mov_b64 v[176:177], 0\n\t"
            "v_mov_b64 v[180:181], 0\n\t"
            "v_mfma_f32_4x4x4_16b_bf16 v[196:199], v[178:179], v[178:179], v[196:199]\n\t"
            "v_mov_b64 v[178:179], 0\n\t"
            "v_mov_b64 v[182:183], 0\n\t"
            "s_waitcnt lgkmcnt(14)\n\t"
            "ds_write_b32 v228, v220\n\t"
            "s_waitcnt lgkmcnt(14)\n\t"
            "ds_read_b128 v[220:223], v235 offset:256\n\t"
            "s_waitcnt lgkmcnt(14)\n\t"
            "ds_read_b32 v230, v233\n\t"
            "s_waitcnt lgkmcnt(14)\n\t"
            "ds_read_b32 v231, v233 offset:64\n\t"
            "v_xor_b32 v228, 32, v234\n\t"
            "s_waitcnt lgkmcnt(14)\n\t"
            "v_smfmac_f32_16x16x64_bf16 v[184:187], v[168:171], v[144:151], v255\n\t"
            "ds_read_b128 v[168:171], v254 offset:512\n\t"
            "s_waitcnt lgkmcnt(14)\n\t"
            "ds_read_b64 v[144:145], v253 offset:72\n\t"
            "s_waitcnt lgkmcnt(14)\n\t"
            "ds_read_b64 v[146:147], v253 offset:168\n\t"
            "s_waitcnt lgkmcnt(14)\n\t"
            "ds_read_b64 v[148:149], v253 offset:1816\n\t"
            "s_waitcnt lgkmcnt(14)\n\t"
            "ds_read_b64 v[150:151], v253 offset:1912\n\t"
            "s_waitcnt lgkmcnt(14)\n\t"
            "v_smfmac_f32_16x16x64_bf16 v[188:191], v[172:175], v[152:159], v255\n\t"
            "ds_read_b128 v[172:175], v254 offset:576\n\t"
            "s_waitcnt lgkmcnt(14)\n\t"
            "ds_read_b64 v[152:153], v253 offset:80\n\t"
            "s_waitcnt lgkmcnt(14)\n\t"
            "ds_read_b64 v[154:155], v253 offset:176\n\t"
            "s_waitcnt lgkmcnt(14)\n\t"
            "ds_read_b64 v[156:157], v253 offset:1824\n\t"
            "s_waitcnt lgkmcnt(14)\n\t"
            "ds_read_b64 v[158:159], v253 offset:1920\n\t"
            "v_cvt_pk_bf16_f32 v184, v184, v185\n\t"
            "v_cvt_pk_bf16_f32 v185, v186, v187\n\t"
            "v_cvt_pk_bf16_f32 v186, v188, v189\n\t"
            "v_cvt_pk_bf16_f32 v187, v190, v191\n\t"
            "v_mfma_f32_4x4x4_16b_bf16 v[196:199], v[184:185], v[184:185], v[196:199]\n\t"
            "s_waitcnt lgkmcnt(14)\n\t"
            "ds_write_b128 v237, v[184:187] offset:128\n\t"
            "v_mfma_f32_16x16x32_bf16 v[192:195], v[238:241], v[184:187], v[192:195]\n\t"
            "v_mov_b64 v[184:185], 0\n\t"
            "v_mov_b64 v[188:189], 0\n\t"
            "v_mfma_f32_4x4x4_16b_bf16 v[196:199], v[186:187], v[186:187], v[196:199]\n\t"
            "v_mov_b64 v[186:187], 0\n\t"
            "v_mov_b64 v[190:191], 0\n\t"
            "s_waitcnt lgkmcnt(10)\n\t"
            "v_smfmac_f32_16x16x64_bf16 v[176:179], v[168:171], v[160:167], v255\n\t"
            "ds_read_b128 v[168:171], v254 offset:640\n\t"
            "ds_read_b64 v[160:161], v253 offset:88\n\t"
            "ds_read_b64 v[162:163], v253 offset:184\n\t"
            "ds_read_b64 v[164:165], v253 offset:1832\n\t"
            "ds_read_b64 v[166:167], v253 offset:1928\n\t"
            "s_waitcnt lgkmcnt(10)\n\t"
            "v_smfmac_f32_16x16x64_bf16 v[180:183], v[172:175], v[144:151], v255\n\t"
            "ds_read_b128 v[172:175], v254 offset:704\n\t"
            "v_cvt_pk_bf16_f32 v176, v176, v177\n\t"
            "v_cvt_pk_bf16_f32 v177, v178, v179\n\t"
            "s_nop 1\n\t"
            "v_mfma_f32_4x4x4_16b_bf16 v[196:199], v[176:177], v[176:177], v[196:199]\n\t"
            "s_nop 1\n\t"
            "v_cvt_pk_bf16_f32 v178, v180, v181\n\t"
            "v_cvt_pk_bf16_f32 v179, v182, v183\n\t"
            "s_nop 1\n\t"
            "v_mfma_f32_4x4x4_16b_bf16 v[196:199], v[178:179], v[178:179], v[196:199]\n\t"
            "ds_write_b128 v236, v[176:179] offset:256\n\t"
            "v_mfma_f32_16x16x32_bf16 v[192:195], v[238:241], v[176:179], v[192:195]\n\t"
            "v_add_u32 v236, s48, v244\n\t"
            "s_waitcnt lgkmcnt(6)\n\t"
            "v_smfmac_f32_16x16x64_bf16 v[184:187], v[168:171], v[152:159], v255\n\t"
            "s_waitcnt lgkmcnt(1)\n\t"
            "v_smfmac_f32_16x16x64_bf16 v[188:191], v[172:175], v[160:167], v255\n\t"
            "s_mov_b32 exec_hi, 0x3fffff\n\t"
            "s_add_u32 m0, s66, 0\n\t"
            "s_add_u32 s49, s79, 0\n\t"
            "buffer_load_dwordx4 v249, s[72:75], s49 offen lds\n\t"
            "s_mov_b32 exec_hi, -1\n\t"
            "s_mov_b32 exec_hi, 0x3fffff\n\t"
            "s_add_u32 m0, s66, 872\n\t"
            "s_add_u32 s49, s68, 196224\n\t"
            "buffer_load_dwordx4 v249, s[72:75], s49 offen lds\n\t"
            "s_mov_b32 exec_hi, -1\n\t"
            "s_mov_b32 exec_hi, 0x3fffff\n\t"
            "s_add_u32 m0, s66, 1744\n\t"
            "s_add_u32 s49, s68, 392448\n\t"
            "buffer_load_dwordx4 v249, s[72:75], s49 offen lds\n\t"
            "s_mov_b32 exec_hi, -1\n\t"
            "s_mov_b32 exec_hi, 0x3fffff\n\t"
            "s_add_u32 m0, s66, 2616\n\t"
            "s_add_u32 s49, s68, 588672\n\t"
            "buffer_load_dwordx4 v249, s[72:75], s49 offen lds\n\t"
            "s_mov_b32 exec_hi, -1\n\t"
            "s_mov_b32 exec_hi, 0x3fffff\n\t"
            "s_add_u32 m0, s66, 3488\n\t"
            "s_add_u32 s49, s68, 784896\n\t"
            "buffer_load_dwordx4 v249, s[72:75], s49 offen lds\n\t"
            "s_mov_b32 exec_hi, -1\n\t"
            "s_mov_b32 exec_hi, 0x3fffff\n\t"
            "s_add_u32 m0, s66, 4360\n\t"
            "s_add_u32 s49, s68, 981120\n\t"
            "buffer_load_dwordx4 v249, s[72:75], s49 offen lds\n\t"
            "s_mov_b32 exec_hi, -1\n\t"
            "s_mov_b32 exec_hi, 0x3fffff\n\t"
            "s_add_u32 m0, s66, 5232\n\t"
            "s_add_u32 s49, s68, 1177344\n\t"
            "buffer_load_dwordx4 v249, s[72:75], s49 offen lds\n\t"
            "s_mov_b32 exec_hi, -1\n\t"
            "v_cvt_pk_bf16_f32 v184, v184, v185\n\t"
            "v_cvt_pk_bf16_f32 v185, v186, v187\n\t"
            "v_cvt_pk_bf16_f32 v186, v188, v189\n\t"
            "v_cvt_pk_bf16_f32 v187, v190, v191\n\t"
            "v_mfma_f32_4x4x4_16b_bf16 v[196:199], v[184:185], v[184:185], v[196:199]\n\t"
            "s_mov_b32 exec_hi, 0x3fffff\n\t"
            "s_add_u32 m0, s66, 6104\n\t"
            "s_add_u32 s49, s68, 1373568\n\t"
            "buffer_load_dwordx4 v249, s[72:75], s49 offen lds\n\t"
            "s_mov_b32 exec_hi, -1\n\t"
            "v_mfma_f32_4x4x4_16b_bf16 v[196:199], v[186:187], v[186:187], v[196:199]\n\t"
            "s_mov_b32 exec_hi, 0x3fffff\n\t"
            "s_add_u32 m0, s66, 6976\n\t"
            "s_add_u32 s49, s68, 1569792\n\t"
            "buffer_load_dwordx4 v249, s[72:75], s49 offen lds\n\t"
            "s_mov_b32 exec_hi, -1\n\t"
            "v_mfma_f32_16x16x32_bf16 v[192:195], v[238:241], v[184:187], v[192:195]\n\t"
            "s_waitcnt vmcnt(9)\n\t"
            "ds_write_b128 v237, v[184:187] offset:256\n\t"
            "v_mfma_f32_16x16x32_bf16 v[144:147], v[0:3], v[200:203], 0\n\t"
            "v_add_u32 v237, s48, v245\n\t"
            "v_mfma_f32_16x16x32_bf16 v[144:147], v[4:7], v[204:207], v[144:147]\n\t"
            "v_mfma_f32_16x16x32_bf16 v[148:151], v[24:27], v[200:203], 0\n\t"
            "s_nop 1\n\t"
            "v_cndmask_b32_e64 v193, v196, v197, s[60:61]\n\t"
            "v_mfma_f32_16x16x32_bf16 v[148:151], v[28:31], v[204:207], v[148:151]\n\t"
            "v_cndmask_b32_e64 v193, v193, v198, s[62:63]\n\t"
            "v_cndmask_b32_e64 v193, v193, v199, s[64:65]\n\t"
            "v_mfma_f32_16x16x32_bf16 v[168:171], v[48:51], v[200:203], 0\n\t"
            "ds_read_b128 v[196:199], v232 offset:6272\n\t"
            "v_mfma_f32_16x16x32_bf16 v[168:171], v[52:55], v[204:207], v[168:171]\n\t"
            "ds_write_b64 v248, v[192:193] offset:18816\n\t"
            "v_mfma_f32_16x16x32_bf16 v[172:175], v[72:75], v[200:203], 0\n\t"
            "ds_read_b128 v[192:195], v235 offset:6144\n\t"
            "v_add_u32 v248, s48, v246\n\t"
            "v_mfma_f32_16x16x32_bf16 v[172:175], v[76:79], v[204:207], v[172:175]\n\t"
            "s_mov_b64 exec, 1\n\t"
            "ds_inc_u32 v255, v238 offset:7860\n\t"
            "s_mov_b64 exec, -1\n\t"
            "v_mfma_f32_16x16x32_bf16 v[152:155], v[96:99], v[200:203], 0\n\t"
            "v_mfma_f32_16x16x32_bf16 v[152:155], v[100:103], v[204:207], v[152:155]\n\t"
            "v_mfma_f32_16x16x32_bf16 v[156:159], v[120:123], v[200:203], 0\n\t"
            "ds_read_b128 v[200:203], v235 offset:6272\n\t"
            "v_mfma_f32_16x16x32_bf16 v[156:159], v[124:127], v[204:207], v[156:159]\n\t"
            "ds_read_b128 v[204:207], v232 offset:6400\n\t"
            "v_mfma_f32_16x16x32_bf16 v[144:147], v[8:11], v[208:211], v[144:147]\n\t"
            "v_mfma_f32_16x16x32_bf16 v[144:147], v[12:15], v[212:215], v[144:147]\n\t"
            "v_mfma_f32_16x16x32_bf16 v[148:151], v[32:35], v[208:211], v[148:151]\n\t"
            "v_mfma_f32_16x16x32_bf16 v[148:151], v[36:39], v[212:215], v[148:151]\n\t"
            "v_mfma_f32_16x16x32_bf16 v[168:171], v[56:59], v[208:211], v[168:171]\n\t"
            "v_mfma_f32_16x16x32_bf16 v[168:171], v[60:63], v[212:215], v[168:171]\n\t"
            "v_mfma_f32_16x16x32_bf16 v[172:175], v[80:83], v[208:211], v[172:175]\n\t"
            "v_mfma_f32_16x16x32_bf16 v[172:175], v[84:87], v[212:215], v[172:175]\n\t"
            "v_mfma_f32_16x16x32_bf16 v[152:155], v[104:107], v[208:211], v[152:155]\n\t"
            "v_mfma_f32_16x16x32_bf16 v[152:155], v[108:111], v[212:215], v[152:155]\n\t"
            "v_mfma_f32_16x16x32_bf16 v[156:159], v[128:131], v[208:211], v[156:159]\n\t"
            "ds_read_b128 v[208:211], v235 offset:6400\n\t"
            "v_mfma_f32_16x16x32_bf16 v[156:159], v[132:135], v[212:215], v[156:159]\n\t"
            "ds_read_b128 v[212:215], v236 offset:6144\n\t"
            "v_mfma_f32_16x16x32_bf16 v[144:147], v[16:19], v[216:219], v[144:147]\n\t"
            "v_mfma_f32_16x16x32_bf16 v[144:147], v[20:23], v[220:223], v[144:147]\n\t"
            "v_mfma_f32_16x16x32_bf16 v[148:151], v[40:43], v[216:219], v[148:151]\n\t"
            "v_mfma_f32_16x16x32_bf16 v[148:151], v[44:47], v[220:223], v[148:151]\n\t"
            "v_mfma_f32_16x16x32_bf16 v[168:171], v[64:67], v[216:219], v[168:171]\n\t"
            "v_mfma_f32_16x16x32_bf16 v[168:171], v[68:71], v[220:223], v[168:171]\n\t"
            "v_mfma_f32_16x16x32_bf16 v[172:175], v[88:91], v[216:219], v[172:175]\n\t"
            "v_mfma_f32_16x16x32_bf16 v[172:175], v[92:95], v[220:223], v[172:175]\n\t"
            "s_nop 1\n\t"
            "v_mul_f32 v144, v230, v144\n\t"
            "v_mfma_f32_16x16x32_bf16 v[152:155], v[112:115], v[216:219], v[152:155]\n\t"
            "v_mul_f32 v145, v230, v145\n\t"
            "v_mul_f32 v146, v230, v146\n\t"
            "v_mfma_f32_16x16x32_bf16 v[152:155], v[116:119], v[220:223], v[152:155]\n\t"
            "v_mul_f32 v148, v230, v148\n\t"
            "v_mul_f32 v149, v230, v149\n\t"
            "v_mfma_f32_16x16x32_bf16 v[156:159], v[136:139], v[216:219], v[156:159]\n\t"
            "ds_read_b128 v[216:219], v237 offset:6144\n\t"
            "v_mul_f32 v168, v230, v168\n\t"
            "v_mfma_f32_16x16x32_bf16 v[156:159], v[140:143], v[220:223], v[156:159]\n\t"
            "ds_read_b128 v[220:223], v248 offset:6144\n\t"
            "v_mul_f32 v169, v230, v169\n\t"
            "s_waitcnt lgkmcnt(7)\n\t"
            "v_mfma_f32_16x16x32_bf16 v[160:163], v[0:3], v[224:227], 0\n\t"
            "v_mul_f32 v172, v230, v172\n\t"
            "v_mul_f32 v173, v230, v173\n\t"
            "v_mfma_f32_16x16x32_bf16 v[160:163], v[4:7], v[192:195], v[160:163]\n\t"
            "v_mul_f32 v152, v230, v152\n\t"
            "v_mul_f32 v153, v230, v153\n\t"
            "v_mfma_f32_16x16x32_bf16 v[164:167], v[24:27], v[224:227], 0\n\t"
            "v_mul_f32 v156, v230, v156\n\t"
            "v_mul_f32 v157, v230, v157\n\t"
            "v_mfma_f32_16x16x32_bf16 v[164:167], v[28:31], v[192:195], v[164:167]\n\t"
            "v_mul_f32 v147, v230, v147\n\t"
            "v_cvt_pk_bf16_f32 v144, v144, v145\n\t"
            "v_mfma_f32_16x16x32_bf16 v[176:179], v[48:51], v[224:227], 0\n\t"
            "v_mul_f32 v150, v230, v150\n\t"
            "v_mul_f32 v151, v230, v151\n\t"
            "v_mfma_f32_16x16x32_bf16 v[176:179], v[52:55], v[192:195], v[176:179]\n\t"
            "v_cvt_pk_bf16_f32 v148, v148, v149\n\t"
            "v_mul_f32 v170, v230, v170\n\t"
            "v_mfma_f32_16x16x32_bf16 v[180:183], v[72:75], v[224:227], 0\n\t"
            "v_mul_f32 v171, v230, v171\n\t"
            "v_cvt_pk_bf16_f32 v168, v168, v169\n\t"
            "v_mfma_f32_16x16x32_bf16 v[180:183], v[76:79], v[192:195], v[180:183]\n\t"
            "v_mul_f32 v174, v230, v174\n\t"
            "v_mul_f32 v175, v230, v175\n\t"
            "v_mfma_f32_16x16x32_bf16 v[184:187], v[96:99], v[224:227], 0\n\t"
            "v_cvt_pk_bf16_f32 v172, v172, v173\n\t"
            "v_mul_f32 v154, v230, v154\n\t"
            "v_mfma_f32_16x16x32_bf16 v[184:187], v[100:103], v[192:195], v[184:187]\n\t"
            "v_mul_f32 v155, v230, v155\n\t"
            "v_cvt_pk_bf16_f32 v152, v152, v153\n\t"
            "v_mfma_f32_16x16x32_bf16 v[188:191], v[120:123], v[224:227], 0\n\t"
            "ds_read_b128 v[224:227], v236 offset:12288\n\t"
            "v_mul_f32 v158, v230, v158\n\t"
            "v_mfma_f32_16x16x32_bf16 v[188:191], v[124:127], v[192:195], v[188:191]\n\t"
            "v_mul_f32 v159, v230, v159\n\t"
            "v_cvt_pk_bf16_f32 v156, v156, v157\n\t"
            "s_waitcnt lgkmcnt(6)\n\t"
            "v_mfma_f32_16x16x32_bf16 v[160:163], v[8:11], v[196:199], v[160:163]\n\t"
            "v_cvt_pk_bf16_f32 v145, v146, v147\n\t"
            "v_cvt_pk_bf16_f32 v149, v150, v151\n\t"
            "v_mfma_f32_16x16x32_bf16 v[160:163], v[12:15], v[200:203], v[160:163]\n\t"
            "v_cvt_pk_bf16_f32 v169, v170, v171\n\t"
            "v_cvt_pk_bf16_f32 v173, v174, v175\n\t"
            "v_mfma_f32_16x16x32_bf16 v[164:167], v[32:35], v[196:199], v[164:167]\n\t"
            "v_cvt_pk_bf16_f32 v153, v154, v155\n\t"
            "v_cvt_pk_bf16_f32 v157, v158, v159\n\t"
            "v_mfma_f32_16x16x32_bf16 v[164:167], v[36:39], v[200:203], v[164:167]\n\t"
            "s_mov_b64 exec, -1\n\t"
            "ds_write_b64 v234, v[144:145]\n\t"
            "ds_write_b64 v234, v[148:149] offset:128\n\t"
            "ds_write_b64 v234, v[168:169] offset:256\n\t"
            "ds_write_b64 v228, v[172:173]\n\t"
            "ds_write_b64 v228, v[152:153] offset:128\n\t"
            "ds_write_b64 v228, v[156:157] offset:256\n\t"
            "s_mov_b64 exec, -1\n\t"
            "v_mfma_f32_16x16x32_bf16 v[176:179], v[56:59], v[196:199], v[176:179]\n\t"
            "v_mfma_f32_16x16x32_bf16 v[176:179], v[60:63], v[200:203], v[176:179]\n\t"
            "s_add_u32 s49, s33, 0\n\t"
            "buffer_load_dwordx4 v[0:3], v247, s[28:31], s49 offen sc1\n\t"
            "v_mfma_f32_16x16x32_bf16 v[180:183], v[80:83], v[196:199], v[180:183]\n\t"
            "v_mfma_f32_16x16x32_bf16 v[180:183], v[84:87], v[200:203], v[180:183]\n\t"
            "s_add_u32 s49, s33, 6144\n\t"
            "buffer_load_dwordx4 v[24:27], v247, s[28:31], s49 offen sc1\n\t"
            "v_mfma_f32_16x16x32_bf16 v[184:187], v[104:107], v[196:199], v[184:187]\n\t"
            "v_mfma_f32_16x16x32_bf16 v[184:187], v[108:111], v[200:203], v[184:187]\n\t"
            "s_add_u32 s49, s33, 12288\n\t"
            "buffer_load_dwordx4 v[48:51], v247, s[28:31], s49 offen sc1\n\t"
            "v_mfma_f32_16x16x32_bf16 v[188:191], v[128:131], v[196:199], v[188:191]\n\t"
            "v_mfma_f32_16x16x32_bf16 v[188:191], v[132:135], v[200:203], v[188:191]\n\t"
            "ds_read_b128 v[200:203], v236\n\t"
            "s_add_u32 s49, s33, 18432\n\t"
            "buffer_load_dwordx4 v[72:75], v247, s[28:31], s49 offen sc1\n\t"
            "s_waitcnt lgkmcnt(11)\n\t"
            "v_mfma_f32_16x16x32_bf16 v[160:163], v[16:19], v[204:207], v[160:163]\n\t"
            "v_mfma_f32_16x16x32_bf16 v[160:163], v[20:23], v[208:211], v[160:163]\n\t"
            "s_add_u32 s49, s33, 24576\n\t"
            "buffer_load_dwordx4 v[96:99], v247, s[28:31], s49 offen sc1\n\t"
            "v_mfma_f32_16x16x32_bf16 v[164:167], v[40:43], v[204:207], v[164:167]\n\t"
            "v_mfma_f32_16x16x32_bf16 v[164:167], v[44:47], v[208:211], v[164:167]\n\t"
            "s_add_u32 s49, s33, 30720\n\t"
            "buffer_load_dwordx4 v[120:123], v247, s[28:31], s49 offen sc1\n\t"
            "v_mfma_f32_16x16x32_bf16 v[176:179], v[64:67], v[204:207], v[176:179]\n\t"
            "s_nop 0\n\t"
            "v_mul_f32 v160, v231, v160\n\t"
            "v_mul_f32 v161, v231, v161\n\t"
            "v_mfma_f32_16x16x32_bf16 v[176:179], v[68:71], v[208:211], v[176:179]\n\t"
            "v_mul_f32 v162, v231, v162\n\t"
            "v_mul_f32 v163, v231, v163\n\t"
            "v_mfma_f32_16x16x32_bf16 v[180:183], v[88:91], v[204:207], v[180:183]\n\t"
            "v_mul_f32 v164, v231, v164\n\t"
            "v_mul_f32 v165, v231, v165\n\t"
            "v_mfma_f32_16x16x32_bf16 v[180:183], v[92:95], v[208:211], v[180:183]\n\t"
            "v_cvt_pk_bf16_f32 v160, v160, v161\n\t"
            "v_mul_f32 v166, v231, v166\n\t"
            "v_mfma_f32_16x16x32_bf16 v[184:187], v[112:115], v[204:207], v[184:187]\n\t"
            "v_mul_f32 v176, v231, v176\n\t"
            "v_mul_f32 v177, v231, v177\n\t"
            "v_mfma_f32_16x16x32_bf16 v[184:187], v[116:119], v[208:211], v[184:187]\n\t"
            "v_mul_f32 v167, v231, v167\n\t"
            "v_cvt_pk_bf16_f32 v164, v164, v165\n\t"
            "v_mfma_f32_16x16x32_bf16 v[188:191], v[136:139], v[204:207], v[188:191]\n\t"
            "ds_read_b128 v[204:207], v237\n\t"
            "v_mul_f32 v180, v231, v180\n\t"
            "v_mfma_f32_16x16x32_bf16 v[188:191], v[140:143], v[208:211], v[188:191]\n\t"
            "ds_read_b128 v[208:211], v248\n\t"
            "v_mul_f32 v181, v231, v181\n\t"
            "v_mul_f32 v184, v231, v184\n\t"
            "v_mul_f32 v185, v231, v185\n\t"
            "v_mul_f32 v178, v231, v178\n\t"
            "v_mul_f32 v179, v231, v179\n\t"
            "v_cvt_pk_bf16_f32 v176, v176, v177\n\t"
            "v_mul_f32 v182, v231, v182\n\t"
            "v_mul_f32 v188, v231, v188\n\t"
            "v_mul_f32 v189, v231, v189\n\t"
            "v_mul_f32 v183, v231, v183\n\t"
            "v_cvt_pk_bf16_f32 v180, v180, v181\n\t"
            "v_mul_f32 v186, v231, v186\n\t"
            "v_mul_f32 v187, v231, v187\n\t"
            "v_cvt_pk_bf16_f32 v184, v184, v185\n\t"
            "v_mul_f32 v190, v231, v190\n\t"
            "v_mul_f32 v191, v231, v191\n\t"
            "v_cvt_pk_bf16_f32 v188, v188, v189\n\t"
            "v_cvt_pk_bf16_f32 v161, v162, v163\n\t"
            "v_cvt_pk_bf16_f32 v165, v166, v167\n\t"
            "v_cvt_pk_bf16_f32 v177, v178, v179\n\t"
            "v_cvt_pk_bf16_f32 v181, v182, v183\n\t"
            "v_cvt_pk_bf16_f32 v185, v186, v187\n\t"
            "v_cvt_pk_bf16_f32 v189, v190, v191\n\t"
            "s_waitcnt lgkmcnt(9)\n\t"
            "s_mov_b64 exec, s[80:81]\n\t"
            "ds_write_b64 v234, v[160:161] offset:6144\n\t"
            "ds_write_b64 v234, v[164:165] offset:6272\n\t"
            "ds_write_b64 v234, v[176:177] offset:6400\n\t"
            "ds_write_b64 v228, v[180:181] offset:6144\n\t"
            "ds_write_b64 v228, v[184:185] offset:6272\n\t"
            "ds_write_b64 v228, v[188:189] offset:6400\n\t"
            "s_mov_b64 exec, -1\n\t"
            "s_waitcnt lgkmcnt(14)\n\t"
            "ds_read_b128 v[228:231], v237 offset:12288\n\t"
            "s_waitcnt lgkmcnt(14)\n\t"
            "ds_read_b128 v[232:235], v248 offset:12288\n\t"
            "s_add_u32 s49, s33, 1024\n\t"
            "buffer_load_dwordx4 v[4:7], v247, s[28:31], s49 offen sc1\n\t"
            "s_add_u32 s49, s33, 7168\n\t"
            "buffer_load_dwordx4 v[28:31], v247, s[28:31], s49 offen sc1\n\t"
            "s_waitcnt lgkmcnt(0)\n\t"
            "s_barrier\n\t"
            "s_setprio 3\n\t"
            "s_add_u32 s49, s33, 13312\n\t"
            "buffer_load_dwordx4 v[52:55], v247, s[28:31], s49 offen sc1\n\t"
            "s_add_u32 s49, s33, 19456\n\t"
            "buffer_load_dwordx4 v[76:79], v247, s[28:31], s49 offen sc1\n\t"
            "s_add_u32 s49, s33, 25600\n\t"
            "buffer_load_dwordx4 v[100:103], v247, s[28:31], s49 offen sc1\n\t"
            "s_add_u32 s49, s33, 31744\n\t"
            "buffer_load_dwordx4 v[124:127], v247, s[28:31], s49 offen sc1\n\t"
            "s_add_u32 s49, s33, 2048\n\t"
            "buffer_load_dwordx4 v[8:11], v247, s[28:31], s49 offen sc1\n\t"
            "s_add_u32 s49, s33, 8192\n\t"
            "buffer_load_dwordx4 v[32:35], v247, s[28:31], s49 offen sc1\n\t"
            "s_add_u32 s49, s33, 14336\n\t"
            "buffer_load_dwordx4 v[56:59], v247, s[28:31], s49 offen sc1\n\t"
            "s_add_u32 s49, s33, 20480\n\t"
            "buffer_load_dwordx4 v[80:83], v247, s[28:31], s49 offen sc1\n\t"
            "s_add_u32 s49, s33, 26624\n\t"
            "buffer_load_dwordx4 v[104:107], v247, s[28:31], s49 offen sc1\n\t"
            "s_add_u32 s49, s33, 32768\n\t"
            "buffer_load_dwordx4 v[128:131], v247, s[28:31], s49 offen sc1\n\t"
            "s_add_u32 s49, s33, 3072\n\t"
            "buffer_load_dwordx4 v[12:15], v247, s[28:31], s49 offen sc1\n\t"
            "s_add_u32 s49, s33, 9216\n\t"
            "buffer_load_dwordx4 v[36:39], v247, s[28:31], s49 offen sc1\n\t"
            "s_add_u32 s49, s33, 15360\n\t"
            "buffer_load_dwordx4 v[60:63], v247, s[28:31], s49 offen sc1\n\t"
            "s_add_u32 s49, s33, 21504\n\t"
            "buffer_load_dwordx4 v[84:87], v247, s[28:31], s49 offen sc1\n\t"
            "s_add_u32 s49, s33, 27648\n\t"
            "buffer_load_dwordx4 v[108:111], v247, s[28:31], s49 offen sc1\n\t"
            "s_add_u32 s49, s33, 33792\n\t"
            "buffer_load_dwordx4 v[132:135], v247, s[28:31], s49 offen sc1\n\t"
            "s_add_u32 s49, s33, 4096\n\t"
            "buffer_load_dwordx4 v[16:19], v247, s[28:31], s49 offen sc1\n\t"
            "s_add_u32 s49, s33, 10240\n\t"
            "buffer_load_dwordx4 v[40:43], v247, s[28:31], s49 offen sc1\n\t"
            "s_add_u32 s49, s33, 16384\n\t"
            "buffer_load_dwordx4 v[64:67], v247, s[28:31], s49 offen sc1\n\t"
            "s_add_u32 s49, s33, 22528\n\t"
            "buffer_load_dwordx4 v[88:91], v247, s[28:31], s49 offen sc1\n\t"
            "s_add_u32 s49, s33, 28672\n\t"
            "buffer_load_dwordx4 v[112:115], v247, s[28:31], s49 offen sc1\n\t"
            "s_add_u32 s49, s33, 34816\n\t"
            "buffer_load_dwordx4 v[136:139], v247, s[28:31], s49 offen sc1\n\t"
            "s_add_u32 s49, s33, 5120\n\t"
            "buffer_load_dwordx4 v[20:23], v247, s[28:31], s49 offen sc1\n\t"
            "s_add_u32 s49, s33, 11264\n\t"
            "buffer_load_dwordx4 v[44:47], v247, s[28:31], s49 offen sc1\n\t"
            "s_add_u32 s49, s33, 17408\n\t"
            "buffer_load_dwordx4 v[68:71], v247, s[28:31], s49 offen sc1\n\t"
            "s_add_u32 s49, s33, 23552\n\t"
            "buffer_load_dwordx4 v[92:95], v247, s[28:31], s49 offen sc1\n\t"
            "s_add_u32 s49, s33, 29696\n\t"
            "buffer_load_dwordx4 v[116:119], v247, s[28:31], s49 offen sc1\n\t"
            "s_add_u32 s49, s33, 35840\n\t"
            "buffer_load_dwordx4 v[140:143], v247, s[28:31], s49 offen sc1\n\t"
            "s_add_u32 s59, s59, s84\n\t"
            "s_movk_i32 s84, 800\n\t"
            "s_add_u32 s45, s46, s27\n\t"
            "s_mov_b32 s46, s47\n\t"
            "s_mov_b32 s47, s68\n\t"
            "s_xor_b32 s48, s48, s71\n\t"
            "s_xor_b32 s67, s67, s71\n\t"
            "s_add_u32 s76, s76, 4\n\t"
            "s_add_u32 s70, s70, 4\n\t"
            "s_lshl_b32 s56, s56, 2\n\t"
            "s_lshr_b32 s49, s26, 30\n\t"
            "s_or_b32 s56, s56, s49\n\t"
            "s_cmp_eq_u32 s82, 0\n\t"
            "s_cbranch_scc1 .Lwk_nr%=\n\t"
            "s_waitcnt lgkmcnt(0)\n\t"
            "s_nop 7\n\t"
            "v_add_u32 v236, 127360, v247\n\t"
            "v_add_u32 v237, 136704, v247\n\t"
            "ds_read_b128 v[144:147], v236\n\t"
            "ds_read_b128 v[148:151], v237\n\t"
            "s_waitcnt lgkmcnt(0)\n\t"
            "s_mov_b32 exec_hi, 0x3ffff\n\t"
            "buffer_store_dwordx4 v[144:147], v247, s[28:31], s82 offen sc1\n\t"
            "s_mov_b64 exec, 0xffffff\n\t"
            "buffer_store_dwordx4 v[148:151], v247, s[28:31], s82 offen offset:800 sc1\n\t"
            "s_mov_b64 exec, -1\n\t"
            "ds_read_b128 v[152:155], v252 offset:2560\n\t"
            "s_waitcnt vmcnt(0) lgkmcnt(0)\n\t"
            "s_mov_b64 exec, 1\n\t"
            "buffer_store_dwordx4 v[152:155], off, s[28:31], s82 offset:1216 sc1\n\t"
            "s_mov_b64 exec, -1\n\t"
            "s_branch .Lwk_n2%=\n\t"
            ".Lwk_nr%=:\n\t"
            "s_waitcnt vmcnt(0)\n\t"
            ".Lwk_n2%=:\n\t"
            "s_nop 1\n\t"
            "s_nop 7\n\t"
            ".p2align 6\n\t"
            "s_nop 0\n\t"
            "s_nop 0\n\t"
            "s_nop 0\n\t"
            "s_nop 0\n\t"
            "s_nop 0\n\t"
            "s_nop 0\n\t"
            "s_nop 0\n\t"
            ".Lwb_loop%=:\n\t"
            "s_setprio 1\n\t"
            "ds_read_b128 v[168:171], v254\n\t"
            "ds_read_b128 v[172:175], v254 offset:64\n\t"
            "s_waitcnt vmcnt(9)\n\t"
            "ds_read_b64 v[144:145], v253\n\t"
            "ds_read_b64 v[146:147], v253 offset:96\n\t"
            "ds_read_b64 v[148:149], v253 offset:1744\n\t"
            "ds_read_b64 v[150:151], v253 offset:1840\n\t"
            "ds_read_b64 v[152:153], v253 offset:8\n\t"
            "ds_read_b64 v[154:155], v253 offset:104\n\t"
            "ds_read_b64 v[156:157], v253 offset:1752\n\t"
            "ds_read_b64 v[158:159], v253 offset:1848\n\t"
            "ds_read_b64 v[160:161], v253 offset:16\n\t"
            "ds_read_b64 v[162:163], v253 offset:112\n\t"
            "ds_read_b64 v[164:165], v253 offset:1760\n\t"
            "ds_read_b64 v[166:167], v253 offset:1856\n\t"
            "v_mov_b64 v[176:177], 0\n\t"
            "v_mov_b64 v[178:179], 0\n\t"
            "v_mov_b64 v[180:181], 0\n\t"
            "v_mov_b64 v[182:183], 0\n\t"
            "v_mov_b32 v249, s76\n\t"
            "v_mov_b64 v[184:185], 0\n\t"
            "v_mov_b64 v[186:187], 0\n\t"
            "v_mov_b64 v[188:189], 0\n\t"
            "v_mov_b64 v[190:191], 0\n\t"
            "v_add_u32 v236, s67, v250\n\t"
            "v_add_u32 v248, s67, v251\n\t"
            "v_mov_b32 v247, 0\n\t"
            "ds_read_b32 v249, v249\n\t"
            "s_setprio 3\n\t"
            "s_waitcnt lgkmcnt(9)\n\t"
            "v_smfmac_f32_16x16x64_bf16 v[176:179], v[168:171], v[144:151], v255\n\t"
            "ds_read_b128 v[168:171], v254 offset:128\n\t"
            "s_waitcnt lgkmcnt(6)\n\t"
            "v_smfmac_f32_16x16x64_bf16 v[180:183], v[172:175], v[152:159], v255\n\t"
            "s_setprio 1\n\t"
            "ds_read_b64 v[144:145], v253 offset:24\n\t"
            "ds_read_b64 v[146:147], v253 offset:120\n\t"
            "ds_read_b64 v[148:149], v253 offset:1768\n\t"
            "ds_read_b64 v[150:151], v253 offset:1864\n\t"
            "ds_read_b128 v[172:175], v254 offset:192\n\t"
            "ds_read_b64 v[152:153], v253 offset:32\n\t"
            "ds_read_b64 v[154:155], v253 offset:128\n\t"
            "ds_read_b64 v[156:157], v253 offset:1776\n\t"
            "ds_read_b64 v[158:159], v253 offset:1872\n\t"
            "v_cvt_pk_bf16_f32 v176, v176, v177\n\t"
            "v_cvt_pk_bf16_f32 v177, v178, v179\n\t"
            "v_cvt_pk_bf16_f32 v178, v180, v181\n\t"
            "v_cvt_pk_bf16_f32 v179, v182, v183\n\t"
            "s_setprio 3\n\t"
            "v_mfma_f32_4x4x4_16b_bf16 v[196:199], v[176:177], v[176:177], 0\n\t"
            "v_mov_b64 v[180:181], 0\n\t"
            "v_mfma_f32_16x16x32_bf16 v[192:195], v[238:241], v[176:179], 0\n\t"
            "s_waitcnt lgkmcnt(10)\n\t"
            "v_readfirstlane_b32 s26, v249\n\t"
            "v_mov_b64 v[182:183], 0\n\t"
            "v_mfma_f32_4x4x4_16b_bf16 v[196:199], v[178:179], v[178:179], v[196:199]\n\t"
            "ds_read_b32 v249, v252 offset:12\n\t"
            "s_and_b32 s68, s26, 0x7ffffff\n\t"
            "s_add_u32 s68, s68, s78\n\t"
            "s_and_b32 s69, s26, 0x38000000\n\t"
            "s_or_b32 s69, s69, s39\n\t"
            "s_bitcmp1_b32 s26, 27\n\t"
            "s_cselect_b32 s79, 2.0, s68\n\t"
            "s_and_b32 s57, s56, 3\n\t"
            "s_lshl_b32 s58, s57, 3\n\t"
            "s_lshr_b32 m0, s54, s58\n\t"
            "s_lshl_b32 s58, s57, 4\n\t"
            "s_lshr_b64 s[36:37], s[50:51], s58\n\t"
            "s_and_b32 s57, s36, 0xffff\n\t"
            "s_add_u32 s57, s57, s67\n\t"
            "v_writelane_b32 v236, s57, 63\n\t"
            "v_writelane_b32 v236, s59, m0\n\t"
            "v_xor_b32 v237, 16, v236\n\t"
            "s_lshr_b64 s[36:37], s[52:53], s58\n\t"
            "s_and_b32 s58, s36, 0xffff\n\t"
            "s_add_u32 s58, s58, s67\n\t"
            "ds_write_b128 v236, v[176:179]\n\t"
            "s_waitcnt lgkmcnt(11)\n\t"
            "v_smfmac_f32_16x16x64_bf16 v[184:187], v[168:171], v[160:167], v255\n\t"
            "s_and_b32 s38, s52, 0xffff\n\t"
            "s_add_u32 s38, s38, s67\n\t"
            "ds_read_b128 v[168:171], v254 offset:256\n\t"
            "s_waitcnt lgkmcnt(7)\n\t"
            "v_smfmac_f32_16x16x64_bf16 v[188:191], v[172:175], v[144:151], v255\n\t"
            "s_setprio 1\n\t"
            "ds_read_b64 v[160:161], v253 offset:40\n\t"
            "ds_read_b64 v[162:163], v253 offset:136\n\t"
            "ds_read_b64 v[164:165], v253 offset:1784\n\t"
            "ds_read_b64 v[166:167], v253 offset:1880\n\t"
            "ds_read_b128 v[172:175], v254 offset:320\n\t"
            "ds_read_b64 v[144:145], v253 offset:48\n\t"
            "ds_read_b64 v[146:147], v253 offset:144\n\t"
            "ds_read_b64 v[148:149], v253 offset:1792\n\t"
            "s_waitcnt lgkmcnt(14)\n\t"
            "ds_read_b64 v[150:151], v253 offset:1888\n\t"
            "v_mov_b64 v[176:177], 0\n\t"
            "v_mov_b64 v[178:179], 0\n\t"
            "v_cvt_pk_bf16_f32 v184, v184, v185\n\t"
            "v_cvt_pk_bf16_f32 v185, v186, v187\n\t"
            "v_cvt_pk_bf16_f32 v186, v188, v189\n\t"
            "v_cvt_pk_bf16_f32 v187, v190, v191\n\t"
            "s_setprio 3\n\t"
            "v_mfma_f32_4x4x4_16b_bf16 v[196:199], v[184:185], v[184:185], v[196:199]\n\t"
            "s_waitcnt lgkmcnt(14)\n\t"
            "ds_write_b128 v237, v[184:187]\n\t"
            "v_mfma_f32_16x16x32_bf16 v[192:195], v[238:241], v[184:187], v[192:195]\n\t"
            "v_mov_b64 v[184:185], 0\n\t"
            "v_mov_b64 v[188:189], 0\n\t"
            "v_mfma_f32_4x4x4_16b_bf16 v[196:199], v[186:187], v[186:187], v[196:199]\n\t"
            "v_mov_b64 v[186:187], 0\n\t"
            "v_mov_b64 v[190:191], 0\n\t"
            "v_writelane_b32 v248, s58, 63\n\t"
            "v_writelane_b32 v248, s38, m0\n\t"
            "s_waitcnt lgkmcnt(12)\n\t"
            "v_and_b32 v249, s69, v249\n\t"
            "s_waitcnt lgkmcnt(10)\n\t"
            "v_smfmac_f32_16x16x64_bf16 v[176:179], v[168:171], v[152:159], v255\n\t"
            "ds_read_b128 v[168:171], v254 offset:384\n\t"
            "ds_read_b64 v[152:153], v253 offset:56\n\t"
            "ds_read_b64 v[154:155], v253 offset:152\n\t"
            "ds_read_b64 v[156:157], v253 offset:1800\n\t"
            "ds_read_b64 v[158:159], v253 offset:1896\n\t"
            "s_waitcnt lgkmcnt(10)\n\t"
            "v_smfmac_f32_16x16x64_bf16 v[180:183], v[172:175], v[160:167], v255\n\t"
            "s_setprio 1\n\t"
            "ds_read_b128 v[172:175], v254 offset:448\n\t"
            "ds_read_b64 v[160:161], v253 offset:64\n\t"
            "ds_read_b64 v[162:163], v253 offset:160\n\t"
            "ds_read_b64 v[164:165], v253 offset:1808\n\t"
            "ds_read_b64 v[166:167], v253 offset:1904\n\t"
            "v_cvt_pk_bf16_f32 v176, v176, v177\n\t"
            "v_cvt_pk_bf16_f32 v177, v178, v179\n\t"
            "v_cvt_pk_bf16_f32 v178, v180, v181\n\t"
            "v_cvt_pk_bf16_f32 v179, v182, v183\n\t"
            "s_setprio 3\n\t"
            "v_mfma_f32_4x4x4_16b_bf16 v[196:199], v[176:177], v[176:177], v[196:199]\n\t"
            "s_waitcnt lgkmcnt(14)\n\t"
            "ds_write_b128 v236, v[176:179] offset:128\n\t"
            "v_mfma_f32_16x16x32_bf16 v[192:195], v[238:241], v[176:179], v[192:195]\n\t"
            "v_mov_b64 v[176:177], 0\n\t"
            "v_mov_b64 v[180:181], 0\n\t"
            "v_mfma_f32_4x4x4_16b_bf16 v[196:199], v[178:179], v[178:179], v[196:199]\n\t"
            "v_mov_b64 v[178:179], 0\n\t"
            "v_mov_b64 v[182:183], 0\n\t"
            "s_waitcnt lgkmcnt(10)\n\t"
            "v_smfmac_f32_16x16x64_bf16 v[184:187], v[168:171], v[144:151], v255\n\t"
            "ds_read_b128 v[168:171], v254 offset:512\n\t"
            "ds_read_b64 v[144:145], v253 offset:72\n\t"
            "ds_read_b64 v[146:147], v253 offset:168\n\t"
            "ds_read_b64 v[148:149], v253 offset:1816\n\t"
            "ds_read_b64 v[150:151], v253 offset:1912\n\t"
            "s_waitcnt lgkmcnt(10)\n\t"
            "v_smfmac_f32_16x16x64_bf16 v[188:191], v[172:175], v[152:159], v255\n\t"
            "s_setprio 1\n\t"
            "ds_read_b128 v[172:175], v254 offset:576\n\t"
            "ds_read_b64 v[152:153], v253 offset:80\n\t"
            "ds_read_b64 v[154:155], v253 offset:176\n\t"
            "ds_read_b64 v[156:157], v253 offset:1824\n\t"
            "ds_read_b64 v[158:159], v253 offset:1920\n\t"
            "v_cvt_pk_bf16_f32 v184, v184, v185\n\t"
            "v_cvt_pk_bf16_f32 v185, v186, v187\n\t"
            "v_cvt_pk_bf16_f32 v186, v188, v189\n\t"
            "v_cvt_pk_bf16_f32 v187, v190, v191\n\t"
            "s_setprio 3\n\t"
            "v_mfma_f32_4x4x4_16b_bf16 v[196:199], v[184:185], v[184:185], v[196:199]\n\t"
            "s_waitcnt lgkmcnt(14)\n\t"
            "ds_write_b128 v237, v[184:187] offset:128\n\t"
            "v_mfma_f32_16x16x32_bf16 v[192:195], v[238:241], v[184:187], v[192:195]\n\t"
            "v_mov_b64 v[184:185], 0\n\t"
            "v_mov_b64 v[188:189], 0\n\t"
            "v_mfma_f32_4x4x4_16b_bf16 v[196:199], v[186:187], v[186:187], v[196:199]\n\t"
            "v_mov_b64 v[186:187], 0\n\t"
            "v_mov_b64 v[190:191], 0\n\t"
            "s_waitcnt lgkmcnt(10)\n\t"
            "v_smfmac_f32_16x16x64_bf16 v[176:179], v[168:171], v[160:167], v255\n\t"
            "ds_read_b128 v[168:171], v254 offset:640\n\t"
            "ds_read_b64 v[160:161], v253 offset:88\n\t"
            "ds_read_b64 v[162:163], v253 offset:184\n\t"
            "ds_read_b64 v[164:165], v253 offset:1832\n\t"
            "ds_read_b64 v[166:167], v253 offset:1928\n\t"
            "s_waitcnt lgkmcnt(10)\n\t"
            "v_smfmac_f32_16x16x64_bf16 v[180:183], v[172:175], v[144:151], v255\n\t"
            "s_setprio 1\n\t"
            "ds_read_b128 v[172:175], v254 offset:704\n\t"
            "v_lshlrev_b32 v144, 16, v212\n\t"
            "v_cvt_pk_bf16_f32 v176, v176, v177\n\t"
            "v_cvt_pk_bf16_f32 v177, v178, v179\n\t"
            "v_and_b32 v145, 0xffff0000, v212\n\t"
            "v_lshlrev_b32 v146, 16, v213\n\t"
            "s_setprio 3\n\t"
            "v_mfma_f32_4x4x4_16b_bf16 v[196:199], v[176:177], v[176:177], v[196:199]\n\t"
            "v_and_b32 v147, 0xffff0000, v213\n\t"
            "v_cvt_pk_bf16_f32 v178, v180, v181\n\t"
            "v_cvt_pk_bf16_f32 v179, v182, v183\n\t"
            "v_lshlrev_b32 v180, 16, v210\n\t"
            "v_and_b32 v181, 0xffff0000, v210\n\t"
            "v_mfma_f32_4x4x4_16b_bf16 v[196:199], v[178:179], v[178:179], v[196:199]\n\t"
            "ds_write_b128 v236, v[176:179] offset:256\n\t"
            "v_mfma_f32_16x16x32_bf16 v[192:195], v[238:241], v[176:179], v[192:195]\n\t"
            "s_setprio 1\n\t"
            "v_lshlrev_b32 v176, 16, v208\n\t"
            "v_and_b32 v177, 0xffff0000, v208\n\t"
            "v_lshlrev_b32 v178, 16, v209\n\t"
            "v_and_b32 v179, 0xffff0000, v209\n\t"
            "v_lshlrev_b32 v182, 16, v211\n\t"
            "v_and_b32 v183, 0xffff0000, v211\n\t"
            "v_lshlrev_b32 v148, 16, v214\n\t"
            "v_and_b32 v149, 0xffff0000, v214\n\t"
            "v_lshlrev_b32 v150, 16, v215\n\t"
            "v_and_b32 v151, 0xffff0000, v215\n\t"
            "v_add_u32 v236, s48, v244\n\t"
            "ds_read_b128 v[212:215], v236 offset:6144\n\t"
            "s_setprio 3\n\t"
            "s_waitcnt lgkmcnt(7)\n\t"
            "v_smfmac_f32_16x16x64_bf16 v[184:187], v[168:171], v[152:159], v255\n\t"
            "v_lshlrev_b32 v152, 16, v200\n\t"
            "v_and_b32 v153, 0xffff0000, v200\n\t"
            "v_lshlrev_b32 v154, 16, v201\n\t"
            "v_and_b32 v155, 0xffff0000, v201\n\t"
            "v_lshlrev_b32 v156, 16, v202\n\t"
            "s_waitcnt lgkmcnt(2)\n\t"
            "v_smfmac_f32_16x16x64_bf16 v[188:191], v[172:175], v[160:167], v255\n\t"
            "s_setprio 1\n\t"
            "s_mov_b32 exec_hi, 0x3fffff\n\t"
            "s_add_u32 m0, s66, 0\n\t"
            "s_add_u32 s49, s79, 0\n\t"
            "buffer_load_dwordx4 v249, s[72:75], s49 offen lds\n\t"
            "s_mov_b32 exec_hi, -1\n\t"
            "s_mov_b32 exec_hi, 0x3fffff\n\t"
            "s_add_u32 m0, s66, 872\n\t"
            "s_add_u32 s49, s68, 196224\n\t"
            "buffer_load_dwordx4 v249, s[72:75], s49 offen lds\n\t"
            "s_mov_b32 exec_hi, -1\n\t"
            "s_mov_b32 exec_hi, 0x3fffff\n\t"
            "s_add_u32 m0, s66, 1744\n\t"
            "s_add_u32 s49, s68, 392448\n\t"
            "buffer_load_dwordx4 v249, s[72:75], s49 offen lds\n\t"
            "s_mov_b32 exec_hi, -1\n\t"
            "s_mov_b32 exec_hi, 0x3fffff\n\t"
            "s_add_u32 m0, s66, 2616\n\t"
            "s_add_u32 s49, s68, 588672\n\t"
            "buffer_load_dwordx4 v249, s[72:75], s49 offen lds\n\t"
            "s_mov_b32 exec_hi, -1\n\t"
            "ds_read_b32 v247, v247 offset:7856\n\t"
            "s_mov_b32 exec_hi, 0x3fffff\n\t"
            "s_add_u32 m0, s66, 3488\n\t"
            "s_add_u32 s49, s68, 784896\n\t"
            "buffer_load_dwordx4 v249, s[72:75], s49 offen lds\n\t"
            "s_mov_b32 exec_hi, -1\n\t"
            "s_mov_b32 exec_hi, 0x3fffff\n\t"
            "s_add_u32 m0, s66, 4360\n\t"
            "s_add_u32 s49, s68, 981120\n\t"
            "buffer_load_dwordx4 v249, s[72:75], s49 offen lds\n\t"
            "s_mov_b32 exec_hi, -1\n\t"
            "s_mov_b32 exec_hi, 0x3fffff\n\t"
            "s_add_u32 m0, s66, 5232\n\t"
            "s_add_u32 s49, s68, 1177344\n\t"
            "buffer_load_dwordx4 v249, s[72:75], s49 offen lds\n\t"
            "s_mov_b32 exec_hi, -1\n\t"
            "s_mov_b32 exec_hi, 0x3fffff\n\t"
            "s_add_u32 m0, s66, 6104\n\t"
            "s_add_u32 s49, s68, 1373568\n\t"
            "buffer_load_dwordx4 v249, s[72:75], s49 offen lds\n\t"
            "s_mov_b32 exec_hi, -1\n\t"
            "s_mov_b32 exec_hi, 0x3fffff\n\t"
            "s_add_u32 m0, s66, 6976\n\t"
            "s_add_u32 s49, s68, 1569792\n\t"
            "buffer_load_dwordx4 v249, s[72:75], s49 offen lds\n\t"
            "s_mov_b32 exec_hi, -1\n\t"
            "v_cvt_pk_bf16_f32 v184, v184, v185\n\t"
            "v_cvt_pk_bf16_f32 v185, v186, v187\n\t"
            "v_cvt_pk_bf16_f32 v186, v188, v189\n\t"
            "v_cvt_pk_bf16_f32 v187, v190, v191\n\t"
            "s_setprio 3\n\t"
            "v_mfma_f32_4x4x4_16b_bf16 v[196:199], v[184:185], v[184:185], v[196:199]\n\t"
            "ds_write_b128 v237, v[184:187] offset:256\n\t"
            "v_mfma_f32_16x16x32_bf16 v[192:195], v[238:241], v[184:187], v[192:195]\n\t"
            "s_waitcnt lgkmcnt(1)\n\t"
            "v_readfirstlane_b32 s77, v247\n\t"
            "s_cmp_ge_u32 s77, s70\n\t"
            "s_cbranch_scc0 .Lwb_spin%=\n\t"
            ".Lwb_go%=:\n\t"
            "v_mfma_f32_4x4x4_16b_bf16 v[196:199], v[186:187], v[186:187], v[196:199]\n\t"
            "s_setprio 1\n\t"
            "ds_read_b128 v[184:187], v242\n\t"
            "ds_read_b128 v[188:191], v243\n\t"
            "ds_read_b128 v[172:175], v243 offset:128\n\t"
            "v_and_b32 v157, 0xffff0000, v202\n\t"
            "v_lshlrev_b32 v158, 16, v203\n\t"
            "v_and_b32 v159, 0xffff0000, v203\n\t"
            "v_lshlrev_b32 v160, 16, v204\n\t"
            "v_and_b32 v161, 0xffff0000, v204\n\t"
            "v_lshlrev_b32 v162, 16, v205\n\t"
            "v_cndmask_b32_e64 v193, v196, v197, s[60:61]\n\t"
            "v_cndmask_b32_e64 v193, v193, v198, s[62:63]\n\t"
            "v_cndmask_b32_e64 v193, v193, v199, s[64:65]\n\t"
            "v_and_b32 v163, 0xffff0000, v205\n\t"
            "v_lshlrev_b32 v164, 16, v206\n\t"
            "v_and_b32 v165, 0xffff0000, v206\n\t"
            "v_lshlrev_b32 v166, 16, v207\n\t"
            "v_and_b32 v167, 0xffff0000, v207\n\t"
            "v_lshlrev_b32 v168, 16, v216\n\t"
            "v_and_b32 v169, 0xffff0000, v216\n\t"
            "v_lshlrev_b32 v170, 16, v217\n\t"
            "v_and_b32 v171, 0xffff0000, v217\n\t"
            "s_bfe_u32 s57, s56, 0x20004\n\t"
            "s_lshl_b32 s58, s57, 3\n\t"
            "s_lshr_b32 s58, s55, s58\n\t"
            "s_lshl_b64 s[36:37], s[34:35], s58\n\t"
            "ds_read_b128 v[196:199], v252\n\t"
            "ds_read_b128 v[200:203], v236\n\t"
            "ds_write_b64 v248, v[192:193] offset:18816\n\t"
            "ds_read_b128 v[192:195], v242 offset:128\n\t"
            "s_bitcmp1_b32 s57, 1\n\t"
            "s_cselect_b32 s58, 1177344, 0\n\t"
            "s_bitcmp1_b32 s57, 0\n\t"
            "s_cselect_b32 s57, 2304, 0\n\t"
            "s_add_u32 s57, s57, s58\n\t"
            "s_mov_b64 exec, 1\n\t"
            "ds_inc_u32 v255, v238 offset:7860\n\t"
            "s_mov_b64 exec, -1\n\t"
            "v_add_u32 v237, s48, v245\n\t"
            "v_add_u32 v248, s48, v246\n\t"
            "s_add_u32 s59, s59, s84\n\t"
            "s_movk_i32 s84, 800\n\t"
            "ds_read_b128 v[204:207], v237\n\t"
            "s_setprio 3\n\t"
            "s_waitcnt lgkmcnt(7)\n\t"
            "v_mfma_f32_16x16x32_bf16 v[152:155], v[0:3], v[184:187], v[152:155]\n\t"
            "s_setprio 1\n\t"
            "s_setprio 1\n\t"
            "ds_read_b128 v[208:211], v248\n\t"
            "v_mfma_f32_16x16x32_bf16 v[152:155], v[4:7], v[188:191], v[152:155]\n\t"
            "v_mfma_f32_16x16x32_bf16 v[156:159], v[28:31], v[188:191], v[156:159]\n\t"
            "v_mfma_f32_16x16x32_bf16 v[156:159], v[24:27], v[184:187], v[156:159]\n\t"
            "v_mfma_f32_16x16x32_bf16 v[160:163], v[48:51], v[184:187], v[160:163]\n\t"
            "v_mfma_f32_16x16x32_bf16 v[160:163], v[52:55], v[188:191], v[160:163]\n\t"
            "v_mfma_f32_16x16x32_bf16 v[164:167], v[76:79], v[188:191], v[164:167]\n\t"
            "s_waitcnt lgkmcnt(6)\n\t"
            "s_mov_b64 exec, s[36:37]\n\t"
            "v_add_u32 v196, s57, v196\n\t"
            "s_mov_b64 exec, -1\n\t"
            "v_mfma_f32_16x16x32_bf16 v[164:167], v[72:75], v[184:187], v[164:167]\n\t"
            "v_mfma_f32_16x16x32_bf16 v[176:179], v[96:99], v[184:187], v[176:179]\n\t"
            "v_mfma_f32_16x16x32_bf16 v[176:179], v[100:103], v[188:191], v[176:179]\n\t"
            "v_mfma_f32_16x16x32_bf16 v[180:183], v[124:127], v[188:191], v[180:183]\n\t"
            "ds_read_b128 v[188:191], v243 offset:256\n\t"
            "v_mfma_f32_16x16x32_bf16 v[180:183], v[120:123], v[184:187], v[180:183]\n\t"
            "ds_read_b128 v[184:187], v242 offset:256\n\t"
            "s_waitcnt lgkmcnt(5)\n\t"
            "v_mfma_f32_16x16x32_bf16 v[152:155], v[8:11], v[192:195], v[152:155]\n\t"
            "v_mfma_f32_16x16x32_bf16 v[152:155], v[12:15], v[172:175], v[152:155]\n\t"
            "v_mfma_f32_16x16x32_bf16 v[156:159], v[36:39], v[172:175], v[156:159]\n\t"
            "v_mfma_f32_16x16x32_bf16 v[156:159], v[32:35], v[192:195], v[156:159]\n\t"
            "v_mfma_f32_16x16x32_bf16 v[160:163], v[56:59], v[192:195], v[160:163]\n\t"
            "v_mfma_f32_16x16x32_bf16 v[160:163], v[60:63], v[172:175], v[160:163]\n\t"
            "v_mfma_f32_16x16x32_bf16 v[164:167], v[84:87], v[172:175], v[164:167]\n\t"
            "v_mfma_f32_16x16x32_bf16 v[164:167], v[80:83], v[192:195], v[164:167]\n\t"
            "v_mfma_f32_16x16x32_bf16 v[176:179], v[104:107], v[192:195], v[176:179]\n\t"
            "v_mfma_f32_16x16x32_bf16 v[176:179], v[108:111], v[172:175], v[176:179]\n\t"
            "v_mfma_f32_16x16x32_bf16 v[180:183], v[132:135], v[172:175], v[180:183]\n\t"
            "ds_read_b128 v[172:175], v243 offset:6144\n\t"
            "v_mfma_f32_16x16x32_bf16 v[180:183], v[128:131], v[192:195], v[180:183]\n\t"
            "ds_read_b128 v[192:195], v242 offset:6144\n\t"
            "s_waitcnt lgkmcnt(2)\n\t"
            "v_mfma_f32_16x16x32_bf16 v[152:155], v[16:19], v[184:187], v[152:155]\n\t"
            "v_mfma_f32_16x16x32_bf16 v[152:155], v[20:23], v[188:191], v[152:155]\n\t"
            "v_mfma_f32_16x16x32_bf16 v[156:159], v[44:47], v[188:191], v[156:159]\n\t"
            "v_mfma_f32_16x16x32_bf16 v[156:159], v[40:43], v[184:187], v[156:159]\n\t"
            "v_mfma_f32_16x16x32_bf16 v[160:163], v[64:67], v[184:187], v[160:163]\n\t"
            "v_mfma_f32_16x16x32_bf16 v[160:163], v[68:71], v[188:191], v[160:163]\n\t"
            "v_mfma_f32_16x16x32_bf16 v[164:167], v[92:95], v[188:191], v[164:167]\n\t"
            "v_mfma_f32_16x16x32_bf16 v[164:167], v[88:91], v[184:187], v[164:167]\n\t"
            "s_nop 1\n\t"
            "v_cvt_pk_bf16_f32 v152, v152, v153\n\t"
            "v_mfma_f32_16x16x32_bf16 v[176:179], v[112:115], v[184:187], v[176:179]\n\t"
            "v_cvt_pk_bf16_f32 v153, v154, v155\n\t"
            "v_mfma_f32_16x16x32_bf16 v[176:179], v[116:119], v[188:191], v[176:179]\n\t"
            "v_cvt_pk_bf16_f32 v154, v156, v157\n\t"
            "v_cvt_pk_bf16_f32 v155, v158, v159\n\t"
            "v_mfma_f32_16x16x32_bf16 v[180:183], v[140:143], v[188:191], v[180:183]\n\t"
            "ds_read_b128 v[188:191], v243 offset:6272\n\t"
            "v_cvt_pk_bf16_f32 v160, v160, v161\n\t"
            "v_mfma_f32_16x16x32_bf16 v[180:183], v[136:139], v[184:187], v[180:183]\n\t"
            "ds_read_b128 v[184:187], v242 offset:6272\n\t"
            "v_cvt_pk_bf16_f32 v161, v162, v163\n\t"
            "s_waitcnt lgkmcnt(2)\n\t"
            "v_mfma_f32_16x16x32_bf16 v[144:147], v[0:3], v[192:195], v[144:147]\n\t"
            "v_cvt_pk_bf16_f32 v176, v176, v177\n\t"
            "v_cvt_pk_bf16_f32 v177, v178, v179\n\t"
            "v_mfma_f32_16x16x32_bf16 v[144:147], v[4:7], v[172:175], v[144:147]\n\t"
            "v_cvt_pk_bf16_f32 v162, v164, v165\n\t"
            "v_cvt_pk_bf16_f32 v163, v166, v167\n\t"
            "v_mfma_f32_16x16x32_bf16 v[148:151], v[28:31], v[172:175], v[148:151]\n\t"
            "v_cvt_pk_bf16_f32 v178, v180, v181\n\t"
            "v_cvt_pk_bf16_f32 v179, v182, v183\n\t"
            "v_mfma_f32_16x16x32_bf16 v[148:151], v[24:27], v[192:195], v[148:151]\n\t"
            "v_lshlrev_b32 v156, 16, v218\n\t"
            "v_and_b32 v157, 0xffff0000, v218\n\t"
            "v_mfma_f32_16x16x32_bf16 v[168:171], v[48:51], v[192:195], v[168:171]\n\t"
            "v_lshlrev_b32 v158, 16, v219\n\t"
            "v_and_b32 v159, 0xffff0000, v219\n\t"
            "v_mfma_f32_16x16x32_bf16 v[168:171], v[52:55], v[172:175], v[168:171]\n\t"
            "ds_read_b128 v[216:219], v237 offset:6144\n\t"
            "v_lshlrev_b32 v164, 16, v220\n\t"
            "v_mfma_f32_16x16x32_bf16 v[156:159], v[76:79], v[172:175], v[156:159]\n\t"
            "v_and_b32 v165, 0xffff0000, v220\n\t"
            "v_lshlrev_b32 v166, 16, v221\n\t"
            "v_mfma_f32_16x16x32_bf16 v[156:159], v[72:75], v[192:195], v[156:159]\n\t"
            "v_and_b32 v167, 0xffff0000, v221\n\t"
            "v_lshlrev_b32 v180, 16, v222\n\t"
            "v_and_b32 v181, 0xffff0000, v222\n\t"
            "v_mfma_f32_16x16x32_bf16 v[164:167], v[96:99], v[192:195], v[164:167]\n\t"
            "v_lshlrev_b32 v182, 16, v223\n\t"
            "v_and_b32 v183, 0xffff0000, v223\n\t"
            "v_mfma_f32_16x16x32_bf16 v[164:167], v[100:103], v[172:175], v[164:167]\n\t"
            "ds_read_b128 v[220:223], v248 offset:6144\n\t"
            "buffer_store_dwordx4 v[152:155], v196, s[40:43], s45 offen sc1\n\t"
            "v_mfma_f32_16x16x32_bf16 v[180:183], v[124:127], v[172:175], v[180:183]\n\t"
            "ds_read_b128 v[172:175], v243 offset:6400\n\t"
            "v_lshlrev_b32 v152, 16, v224\n\t"
            "v_mfma_f32_16x16x32_bf16 v[180:183], v[120:123], v[192:195], v[180:183]\n\t"
            "ds_read_b128 v[192:195], v242 offset:6400\n\t"
            "buffer_store_dwordx4 v[160:163], v196, s[40:43], s45 offen offset:64 sc1\n\t"
            "s_waitcnt lgkmcnt(4)\n\t"
            "v_mfma_f32_16x16x32_bf16 v[144:147], v[8:11], v[184:187], v[144:147]\n\t"
            "v_and_b32 v153, 0xffff0000, v224\n\t"
            "v_lshlrev_b32 v154, 16, v225\n\t"
            "v_mfma_f32_16x16x32_bf16 v[144:147], v[12:15], v[188:191], v[144:147]\n\t"
            "buffer_store_dwordx4 v[176:179], v196, s[40:43], s45 offen offset:128 sc1\n\t"
            "v_mfma_f32_16x16x32_bf16 v[148:151], v[36:39], v[188:191], v[148:151]\n\t"
            "v_and_b32 v155, 0xffff0000, v225\n\t"
            "v_lshlrev_b32 v160, 16, v226\n\t"
            "v_mfma_f32_16x16x32_bf16 v[148:151], v[32:35], v[184:187], v[148:151]\n\t"
            "v_and_b32 v161, 0xffff0000, v226\n\t"
            "v_lshlrev_b32 v162, 16, v227\n\t"
            "v_mfma_f32_16x16x32_bf16 v[168:171], v[56:59], v[184:187], v[168:171]\n\t"
            "v_and_b32 v163, 0xffff0000, v227\n\t"
            "v_lshlrev_b32 v176, 16, v228\n\t"
            "v_mfma_f32_16x16x32_bf16 v[168:171], v[60:63], v[188:191], v[168:171]\n\t"
            "ds_read_b128 v[224:227], v236 offset:12288\n\t"
            "v_and_b32 v177, 0xffff0000, v228\n\t"
            "v_mfma_f32_16x16x32_bf16 v[156:159], v[84:87], v[188:191], v[156:159]\n\t"
            "v_lshlrev_b32 v178, 16, v229\n\t"
            "v_and_b32 v179, 0xffff0000, v229\n\t"
            "v_mfma_f32_16x16x32_bf16 v[156:159], v[80:83], v[184:187], v[156:159]\n\t"
            "v_mfma_f32_16x16x32_bf16 v[164:167], v[104:107], v[184:187], v[164:167]\n\t"
            "v_mfma_f32_16x16x32_bf16 v[164:167], v[108:111], v[188:191], v[164:167]\n\t"
            "v_mfma_f32_16x16x32_bf16 v[180:183], v[132:135], v[188:191], v[180:183]\n\t"
            "ds_read_b128 v[188:191], v243 offset:12288\n\t"
            "v_mfma_f32_16x16x32_bf16 v[180:183], v[128:131], v[184:187], v[180:183]\n\t"
            "ds_read_b128 v[184:187], v242 offset:12288\n\t"
            "s_waitcnt lgkmcnt(3)\n\t"
            "v_mfma_f32_16x16x32_bf16 v[144:147], v[16:19], v[192:195], v[144:147]\n\t"
            "v_mfma_f32_16x16x32_bf16 v[144:147], v[20:23], v[172:175], v[144:147]\n\t"
            "v_mfma_f32_16x16x32_bf16 v[168:171], v[64:67], v[192:195], v[168:171]\n\t"
            "v_mfma_f32_16x16x32_bf16 v[168:171], v[68:71], v[172:175], v[168:171]\n\t"
            "v_mfma_f32_16x16x32_bf16 v[164:167], v[112:115], v[192:195], v[164:167]\n\t"
            "v_mfma_f32_16x16x32_bf16 v[164:167], v[116:119], v[172:175], v[164:167]\n\t"
            "v_mfma_f32_16x16x32_bf16 v[148:151], v[44:47], v[172:175], v[148:151]\n\t"
            "v_mfma_f32_16x16x32_bf16 v[148:151], v[40:43], v[192:195], v[148:151]\n\t"
            "s_nop 1\n\t"
            "v_cvt_pk_bf16_f32 v144, v144, v145\n\t"
            "v_mfma_f32_16x16x32_bf16 v[156:159], v[92:95], v[172:175], v[156:159]\n\t"
            "v_cvt_pk_bf16_f32 v145, v146, v147\n\t"
            "v_mfma_f32_16x16x32_bf16 v[156:159], v[88:91], v[192:195], v[156:159]\n\t"
            "v_cvt_pk_bf16_f32 v168, v168, v169\n\t"
            "v_cvt_pk_bf16_f32 v169, v170, v171\n\t"
            "v_mfma_f32_16x16x32_bf16 v[180:183], v[140:143], v[172:175], v[180:183]\n\t"
            "ds_read_b128 v[172:175], v243 offset:12416\n\t"
            "v_cvt_pk_bf16_f32 v164, v164, v165\n\t"
            "v_mfma_f32_16x16x32_bf16 v[180:183], v[136:139], v[192:195], v[180:183]\n\t"
            "ds_read_b128 v[192:195], v242 offset:12416\n\t"
            "v_cvt_pk_bf16_f32 v165, v166, v167\n\t"
            "s_waitcnt lgkmcnt(2)\n\t"
            "v_mfma_f32_16x16x32_bf16 v[152:155], v[0:3], v[184:187], v[152:155]\n\t"
            "v_cvt_pk_bf16_f32 v146, v148, v149\n\t"
            "v_cvt_pk_bf16_f32 v147, v150, v151\n\t"
            "v_mfma_f32_16x16x32_bf16 v[152:155], v[4:7], v[188:191], v[152:155]\n\t"
            "v_cvt_pk_bf16_f32 v170, v156, v157\n\t"
            "v_cvt_pk_bf16_f32 v171, v158, v159\n\t"
            "v_mfma_f32_16x16x32_bf16 v[160:163], v[28:31], v[188:191], v[160:163]\n\t"
            "v_cvt_pk_bf16_f32 v166, v180, v181\n\t"
            "v_cvt_pk_bf16_f32 v167, v182, v183\n\t"
            "v_mfma_f32_16x16x32_bf16 v[160:163], v[24:27], v[184:187], v[160:163]\n\t"
            "v_lshlrev_b32 v148, 16, v230\n\t"
            "v_and_b32 v149, 0xffff0000, v230\n\t"
            "v_mfma_f32_16x16x32_bf16 v[176:179], v[48:51], v[184:187], v[176:179]\n\t"
            "v_lshlrev_b32 v150, 16, v231\n\t"
            "v_and_b32 v151, 0xffff0000, v231\n\t"
            "v_mfma_f32_16x16x32_bf16 v[176:179], v[52:55], v[188:191], v[176:179]\n\t"
            "ds_read_b128 v[228:231], v237 offset:12288\n\t"
            "v_lshlrev_b32 v156, 16, v232\n\t"
            "v_mfma_f32_16x16x32_bf16 v[148:151], v[76:79], v[188:191], v[148:151]\n\t"
            "v_and_b32 v157, 0xffff0000, v232\n\t"
            "v_lshlrev_b32 v158, 16, v233\n\t"
            "v_mfma_f32_16x16x32_bf16 v[148:151], v[72:75], v[184:187], v[148:151]\n\t"
            "v_and_b32 v159, 0xffff0000, v233\n\t"
            "v_lshlrev_b32 v180, 16, v234\n\t"
            "v_and_b32 v181, 0xffff0000, v234\n\t"
            "v_mfma_f32_16x16x32_bf16 v[156:159], v[96:99], v[184:187], v[156:159]\n\t"
            "v_lshlrev_b32 v182, 16, v235\n\t"
            "v_and_b32 v183, 0xffff0000, v235\n\t"
            "v_mfma_f32_16x16x32_bf16 v[156:159], v[100:103], v[188:191], v[156:159]\n\t"
            "ds_read_b128 v[232:235], v248 offset:12288\n\t"
            "buffer_store_dwordx4 v[144:147], v197, s[40:43], s45 offen sc1\n\t"
            "v_mfma_f32_16x16x32_bf16 v[180:183], v[124:127], v[188:191], v[180:183]\n\t"
            "ds_read_b128 v[188:191], v243 offset:12544\n\t"
            "v_mfma_f32_16x16x32_bf16 v[180:183], v[120:123], v[184:187], v[180:183]\n\t"
            "ds_read_b128 v[184:187], v242 offset:12544\n\t"
            "buffer_store_dwordx4 v[168:171], v197, s[40:43], s45 offen offset:64 sc1\n\t"
            "s_waitcnt lgkmcnt(4)\n\t"
            "v_mfma_f32_16x16x32_bf16 v[152:155], v[8:11], v[192:195], v[152:155]\n\t"
            "v_mfma_f32_16x16x32_bf16 v[152:155], v[12:15], v[172:175], v[152:155]\n\t"
            "buffer_store_dwordx4 v[164:167], v197, s[40:43], s45 offen offset:128 sc1\n\t"
            "v_mfma_f32_16x16x32_bf16 v[160:163], v[36:39], v[172:175], v[160:163]\n\t"
            "v_mfma_f32_16x16x32_bf16 v[160:163], v[32:35], v[192:195], v[160:163]\n\t"
            "s_waitcnt lgkmcnt(0)\n\t"
            "s_barrier\n\t"
            "s_setprio 3\n\t"
            "v_mfma_f32_16x16x32_bf16 v[176:179], v[56:59], v[192:195], v[176:179]\n\t"
            "v_mfma_f32_16x16x32_bf16 v[176:179], v[60:63], v[172:175], v[176:179]\n\t"
            "v_mfma_f32_16x16x32_bf16 v[148:151], v[84:87], v[172:175], v[148:151]\n\t"
            "v_mfma_f32_16x16x32_bf16 v[148:151], v[80:83], v[192:195], v[148:151]\n\t"
            "v_mfma_f32_16x16x32_bf16 v[156:159], v[104:107], v[192:195], v[156:159]\n\t"
            "v_mfma_f32_16x16x32_bf16 v[156:159], v[108:111], v[172:175], v[156:159]\n\t"
            "v_mfma_f32_16x16x32_bf16 v[152:155], v[16:19], v[184:187], v[152:155]\n\t"
            "v_mfma_f32_16x16x32_bf16 v[152:155], v[20:23], v[188:191], v[152:155]\n\t"
            "v_mfma_f32_16x16x32_bf16 v[160:163], v[44:47], v[188:191], v[160:163]\n\t"
            "v_mfma_f32_16x16x32_bf16 v[160:163], v[40:43], v[184:187], v[160:163]\n\t"
            "v_mfma_f32_16x16x32_bf16 v[180:183], v[132:135], v[172:175], v[180:183]\n\t"
            "v_mfma_f32_16x16x32_bf16 v[180:183], v[128:131], v[192:195], v[180:183]\n\t"
            "v_mfma_f32_16x16x32_bf16 v[176:179], v[64:67], v[184:187], v[176:179]\n\t"
            "v_mfma_f32_16x16x32_bf16 v[176:179], v[68:71], v[188:191], v[176:179]\n\t"
            "s_nop 1\n\t"
            "v_cvt_pk_bf16_f32 v152, v152, v153\n\t"
            "v_mfma_f32_16x16x32_bf16 v[148:151], v[92:95], v[188:191], v[148:151]\n\t"
            "v_cvt_pk_bf16_f32 v153, v154, v155\n\t"
            "v_mfma_f32_16x16x32_bf16 v[148:151], v[88:91], v[184:187], v[148:151]\n\t"
            "v_cvt_pk_bf16_f32 v154, v160, v161\n\t"
            "v_cvt_pk_bf16_f32 v155, v162, v163\n\t"
            "v_mfma_f32_16x16x32_bf16 v[156:159], v[112:115], v[184:187], v[156:159]\n\t"
            "v_cvt_pk_bf16_f32 v176, v176, v177\n\t"
            "v_cvt_pk_bf16_f32 v177, v178, v179\n\t"
            "v_mfma_f32_16x16x32_bf16 v[156:159], v[116:119], v[188:191], v[156:159]\n\t"
            "buffer_store_dwordx4 v[152:155], v198, s[40:43], s45 offen sc1\n\t"
            "v_mfma_f32_16x16x32_bf16 v[180:183], v[140:143], v[188:191], v[180:183]\n\t"
            "v_cvt_pk_bf16_f32 v178, v148, v149\n\t"
            "v_cvt_pk_bf16_f32 v179, v150, v151\n\t"
            "v_mfma_f32_16x16x32_bf16 v[180:183], v[136:139], v[184:187], v[180:183]\n\t"
            "buffer_store_dwordx4 v[176:179], v198, s[40:43], s45 offen offset:64 sc1\n\t"
            "s_nop 1\n\t"
            "v_cvt_pk_bf16_f32 v156, v156, v157\n\t"
            "v_cvt_pk_bf16_f32 v157, v158, v159\n\t"
            "s_nop 2\n\t"
            "v_cvt_pk_bf16_f32 v158, v180, v181\n\t"
            "v_cvt_pk_bf16_f32 v159, v182, v183\n\t"
            "buffer_store_dwordx4 v[156:159], v198, s[40:43], s45 offen offset:128 sc1\n\t"
            "s_add_u32 s45, s46, s27\n\t"
            "s_mov_b32 s46, s47\n\t"
            "s_mov_b32 s47, s68\n\t"
            "s_xor_b32 s48, s48, s71\n\t"
            "s_xor_b32 s67, s67, s71\n\t"
            "s_add_u32 s76, s76, 4\n\t"
            "s_add_u32 s70, s70, 4\n\t"
            "s_lshl_b32 s56, s56, 2\n\t"
            "s_lshr_b32 s49, s26, 30\n\t"
            "s_or_b32 s56, s56, s49\n\t"
            "s_cmp_lt_u32 s46, 0x7000000\n\t"
            "s_cbranch_scc1 .Lwb_loop%=\n\t"
            "s_branch .Lwb_end%=\n\t"
            ".Lwb_spin%=:\n\t"
            "s_cmp_eq_u32 s83, 0\n\t"
            "s_cbranch_scc1 .Lwb_go%=\n\t"
            "s_sub_u32 s83, s83, 1\n\t"
            "s_sleep 2\n\t"
            "v_mov_b32 v247, 0\n\t"
            "ds_read_b32 v247, v247 offset:7856\n\t"
            "s_waitcnt lgkmcnt(0)\n\t"
            "v_readfirstlane_b32 s77, v247\n\t"
            "s_cmp_ge_u32 s77, s70\n\t"
            "s_cbranch_scc0 .Lwb_spin%=\n\t"
            "s_branch .Lwb_go%=\n\t"
            ".Lwb_end%=:\n\t"
            "s_waitcnt lgkmcnt(0)\n\t"
            "s_nop 7\n\t"
            "ds_read_b128 v[196:199], v252\n\t"
            "s_bfe_u32 s57, s56, 0x20004\n\t"
            "s_lshl_b32 s58, s57, 3\n\t"
            "s_lshr_b32 s58, s55, s58\n\t"
            "s_lshl_b64 s[36:37], s[34:35], s58\n\t"
            "s_bitcmp1_b32 s57, 1\n\t"
            "s_cselect_b32 s58, 1177344, 0\n\t"
            "s_bitcmp1_b32 s57, 0\n\t"
            "s_cselect_b32 s57, 2304, 0\n\t"
            "s_add_u32 s57, s57, s58\n\t"
            "v_lshlrev_b32 v152, 16, v200\n\t"
            "v_and_b32 v153, 0xffff0000, v200\n\t"
            "v_lshlrev_b32 v154, 16, v201\n\t"
            "v_and_b32 v155, 0xffff0000, v201\n\t"
            "v_lshlrev_b32 v156, 16, v202\n\t"
            "v_and_b32 v157, 0xffff0000, v202\n\t"
            "v_lshlrev_b32 v158, 16, v203\n\t"
            "v_and_b32 v159, 0xffff0000, v203\n\t"
            "v_lshlrev_b32 v160, 16, v204\n\t"
            "v_and_b32 v161, 0xffff0000, v204\n\t"
            "v_lshlrev_b32 v162, 16, v205\n\t"
            "v_and_b32 v163, 0xffff0000, v205\n\t"
            "v_lshlrev_b32 v164, 16, v206\n\t"
            "v_and_b32 v165, 0xffff0000, v206\n\t"
            "v_lshlrev_b32 v166, 16, v207\n\t"
            "v_and_b32 v167, 0xffff0000, v207\n\t"
            "v_lshlrev_b32 v176, 16, v208\n\t"
            "v_and_b32 v177, 0xffff0000, v208\n\t"
            "v_lshlrev_b32 v178, 16, v209\n\t"
            "v_and_b32 v179, 0xffff0000, v209\n\t"
            "v_lshlrev_b32 v180, 16, v210\n\t"
            "v_and_b32 v181, 0xffff0000, v210\n\t"
            "v_lshlrev_b32 v182, 16, v211\n\t"
            "v_and_b32 v183, 0xffff0000, v211\n\t"
            "v_lshlrev_b32 v144, 16, v212\n\t"
            "v_and_b32 v145, 0xffff0000, v212\n\t"
            "v_lshlrev_b32 v146, 16, v213\n\t"
            "v_and_b32 v147, 0xffff0000, v213\n\t"
            "v_lshlrev_b32 v148, 16, v214\n\t"
            "v_and_b32 v149, 0xffff0000, v214\n\t"
            "v_lshlrev_b32 v150, 16, v215\n\t"
            "v_and_b32 v151, 0xffff0000, v215\n\t"
            "v_lshlrev_b32 v168, 16, v216\n\t"
            "v_and_b32 v169, 0xffff0000, v216\n\t"
            "v_lshlrev_b32 v170, 16, v217\n\t"
            "s_waitcnt lgkmcnt(0)\n\t"
            "s_mov_b64 exec, s[36:37]\n\t"
            "v_add_u32 v196, s57, v196\n\t"
            "s_mov_b64 exec, -1\n\t"
            ".Lwk_t1%=:\n\t"
            "v_mov_b32 v247, 0\n\t"
            "ds_read_b32 v247, v247 offset:7856\n\t"
            "s_waitcnt lgkmcnt(0)\n\t"
            "v_readfirstlane_b32 s77, v247\n\t"
            "s_cmp_ge_u32 s77, s70\n\t"
            "s_cbranch_scc1 .Lwk_t2%=\n\t"
            "s_cmp_eq_u32 s83, 0\n\t"
            "s_cbranch_scc1 .Lwk_t2%=\n\t"
            "s_sub_u32 s83, s83, 1\n\t"
            "s_sleep 1\n\t"
            "s_branch .Lwk_t1%=\n\t"
            ".Lwk_t2%=:\n\t"
            "v_and_b32 v171, 0xffff0000, v217\n\t"
            "ds_read_b128 v[184:187], v242\n\t"
            "ds_read_b128 v[188:191], v243\n\t"
            "ds_read_b128 v[192:195], v242 offset:128\n\t"
            "ds_read_b128 v[172:175], v243 offset:128\n\t"
            "s_waitcnt lgkmcnt(2)\n\t"
            "v_mfma_f32_16x16x32_bf16 v[152:155], v[0:3], v[184:187], v[152:155]\n\t"
            "v_mfma_f32_16x16x32_bf16 v[152:155], v[4:7], v[188:191], v[152:155]\n\t"
            "v_mfma_f32_16x16x32_bf16 v[156:159], v[28:31], v[188:191], v[156:159]\n\t"
            "v_mfma_f32_16x16x32_bf16 v[156:159], v[24:27], v[184:187], v[156:159]\n\t"
            "v_mfma_f32_16x16x32_bf16 v[160:163], v[48:51], v[184:187], v[160:163]\n\t"
            "v_mfma_f32_16x16x32_bf16 v[160:163], v[52:55], v[188:191], v[160:163]\n\t"
            "v_mfma_f32_16x16x32_bf16 v[164:167], v[76:79], v[188:191], v[164:167]\n\t"
            "v_mfma_f32_16x16x32_bf16 v[164:167], v[72:75], v[184:187], v[164:167]\n\t"
            "v_mfma_f32_16x16x32_bf16 v[176:179], v[96:99], v[184:187], v[176:179]\n\t"
            "v_mfma_f32_16x16x32_bf16 v[176:179], v[100:103], v[188:191], v[176:179]\n\t"
            "v_mfma_f32_16x16x32_bf16 v[180:183], v[124:127], v[188:191], v[180:183]\n\t"
            "ds_read_b128 v[188:191], v243 offset:256\n\t"
            "v_mfma_f32_16x16x32_bf16 v[180:183], v[120:123], v[184:187], v[180:183]\n\t"
            "ds_read_b128 v[184:187], v242 offset:256\n\t"
            "s_waitcnt lgkmcnt(2)\n\t"
            "v_mfma_f32_16x16x32_bf16 v[152:155], v[8:11], v[192:195], v[152:155]\n\t"
            "v_mfma_f32_16x16x32_bf16 v[152:155], v[12:15], v[172:175], v[152:155]\n\t"
            "v_mfma_f32_16x16x32_bf16 v[156:159], v[36:39], v[172:175], v[156:159]\n\t"
            "v_mfma_f32_16x16x32_bf16 v[156:159], v[32:35], v[192:195], v[156:159]\n\t"
            "v_mfma_f32_16x16x32_bf16 v[160:163], v[56:59], v[192:195], v[160:163]\n\t"
            "v_mfma_f32_16x16x32_bf16 v[160:163], v[60:63], v[172:175], v[160:163]\n\t"
            "v_mfma_f32_16x16x32_bf16 v[164:167], v[84:87], v[172:175], v[164:167]\n\t"
            "v_mfma_f32_16x16x32_bf16 v[164:167], v[80:83], v[192:195], v[164:167]\n\t"
            "v_mfma_f32_16x16x32_bf16 v[176:179], v[104:107], v[192:195], v[176:179]\n\t"
            "v_mfma_f32_16x16x32_bf16 v[176:179], v[108:111], v[172:175], v[176:179]\n\t"
            "v_mfma_f32_16x16x32_bf16 v[180:183], v[132:135], v[172:175], v[180:183]\n\t"
            "ds_read_b128 v[172:175], v243 offset:6144\n\t"
            "v_mfma_f32_16x16x32_bf16 v[180:183], v[128:131], v[192:195], v[180:183]\n\t"
            "ds_read_b128 v[192:195], v242 offset:6144\n\t"
            "s_waitcnt lgkmcnt(2)\n\t"
            "v_mfma_f32_16x16x32_bf16 v[152:155], v[16:19], v[184:187], v[152:155]\n\t"
            "v_mfma_f32_16x16x32_bf16 v[152:155], v[20:23], v[188:191], v[152:155]\n\t"
            "v_mfma_f32_16x16x32_bf16 v[156:159], v[44:47], v[188:191], v[156:159]\n\t"
            "v_mfma_f32_16x16x32_bf16 v[156:159], v[40:43], v[184:187], v[156:159]\n\t"
            "v_mfma_f32_16x16x32_bf16 v[160:163], v[64:67], v[184:187], v[160:163]\n\t"
            "v_mfma_f32_16x16x32_bf16 v[160:163], v[68:71], v[188:191], v[160:163]\n\t"
            "v_mfma_f32_16x16x32_bf16 v[164:167], v[92:95], v[188:191], v[164:167]\n\t"
            "v_mfma_f32_16x16x32_bf16 v[164:167], v[88:91], v[184:187], v[164:167]\n\t"
            "s_nop 1\n\t"
            "v_cvt_pk_bf16_f32 v152, v152, v153\n\t"
            "v_mfma_f32_16x16x32_bf16 v[176:179], v[112:115], v[184:187], v[176:179]\n\t"
            "v_cvt_pk_bf16_f32 v153, v154, v155\n\t"
            "v_mfma_f32_16x16x32_bf16 v[176:179], v[116:119], v[188:191], v[176:179]\n\t"
            "v_cvt_pk_bf16_f32 v154, v156, v157\n\t"
            "v_cvt_pk_bf16_f32 v155, v158, v159\n\t"
            "v_mfma_f32_16x16x32_bf16 v[180:183], v[140:143], v[188:191], v[180:183]\n\t"
            "ds_read_b128 v[188:191], v243 offset:6272\n\t"
            "v_cvt_pk_bf16_f32 v160, v160, v161\n\t"
            "v_mfma_f32_16x16x32_bf16 v[180:183], v[136:139], v[184:187], v[180:183]\n\t"
            "ds_read_b128 v[184:187], v242 offset:6272\n\t"
            "v_cvt_pk_bf16_f32 v161, v162, v163\n\t"
            "s_waitcnt lgkmcnt(2)\n\t"
            "v_mfma_f32_16x16x32_bf16 v[144:147], v[0:3], v[192:195], v[144:147]\n\t"
            "v_cvt_pk_bf16_f32 v176, v176, v177\n\t"
            "v_cvt_pk_bf16_f32 v177, v178, v179\n\t"
            "v_mfma_f32_16x16x32_bf16 v[144:147], v[4:7], v[172:175], v[144:147]\n\t"
            "v_cvt_pk_bf16_f32 v162, v164, v165\n\t"
            "v_cvt_pk_bf16_f32 v163, v166, v167\n\t"
            "v_mfma_f32_16x16x32_bf16 v[148:151], v[28:31], v[172:175], v[148:151]\n\t"
            "v_cvt_pk_bf16_f32 v178, v180, v181\n\t"
            "v_cvt_pk_bf16_f32 v179, v182, v183\n\t"
            "v_mfma_f32_16x16x32_bf16 v[148:151], v[24:27], v[192:195], v[148:151]\n\t"
            "v_lshlrev_b32 v156, 16, v218\n\t"
            "v_and_b32 v157, 0xffff0000, v218\n\t"
            "v_mfma_f32_16x16x32_bf16 v[168:171], v[48:51], v[192:195], v[168:171]\n\t"
            "v_lshlrev_b32 v158, 16, v219\n\t"
            "v_and_b32 v159, 0xffff0000, v219\n\t"
            "v_mfma_f32_16x16x32_bf16 v[168:171], v[52:55], v[172:175], v[168:171]\n\t"
            "v_lshlrev_b32 v164, 16, v220\n\t"
            "v_and_b32 v165, 0xffff0000, v220\n\t"
            "v_mfma_f32_16x16x32_bf16 v[156:159], v[76:79], v[172:175], v[156:159]\n\t"
            "v_lshlrev_b32 v166, 16, v221\n\t"
            "v_and_b32 v167, 0xffff0000, v221\n\t"
            "v_mfma_f32_16x16x32_bf16 v[156:159], v[72:75], v[192:195], v[156:159]\n\t"
            "v_lshlrev_b32 v180, 16, v222\n\t"
            "v_and_b32 v181, 0xffff0000, v222\n\t"
            "v_mfma_f32_16x16x32_bf16 v[164:167], v[96:99], v[192:195], v[164:167]\n\t"
            "v_lshlrev_b32 v182, 16, v223\n\t"
            "v_and_b32 v183, 0xffff0000, v223\n\t"
            "v_mfma_f32_16x16x32_bf16 v[164:167], v[100:103], v[172:175], v[164:167]\n\t"
            "buffer_store_dwordx4 v[152:155], v196, s[40:43], s45 offen sc1\n\t"
            "v_mfma_f32_16x16x32_bf16 v[180:183], v[124:127], v[172:175], v[180:183]\n\t"
            "ds_read_b128 v[172:175], v243 offset:6400\n\t"
            "v_lshlrev_b32 v152, 16, v224\n\t"
            "v_mfma_f32_16x16x32_bf16 v[180:183], v[120:123], v[192:195], v[180:183]\n\t"
            "ds_read_b128 v[192:195], v242 offset:6400\n\t"
            "buffer_store_dwordx4 v[160:163], v196, s[40:43], s45 offen offset:64 sc1\n\t"
            "s_waitcnt lgkmcnt(2)\n\t"
            "v_mfma_f32_16x16x32_bf16 v[144:147], v[8:11], v[184:187], v[144:147]\n\t"
            "v_and_b32 v153, 0xffff0000, v224\n\t"
            "v_lshlrev_b32 v154, 16, v225\n\t"
            "v_mfma_f32_16x16x32_bf16 v[144:147], v[12:15], v[188:191], v[144:147]\n\t"
            "buffer_store_dwordx4 v[176:179], v196, s[40:43], s45 offen offset:128 sc1\n\t"
            "v_mfma_f32_16x16x32_bf16 v[148:151], v[36:39], v[188:191], v[148:151]\n\t"
            "v_and_b32 v155, 0xffff0000, v225\n\t"
            "v_lshlrev_b32 v160, 16, v226\n\t"
            "v_mfma_f32_16x16x32_bf16 v[148:151], v[32:35], v[184:187], v[148:151]\n\t"
            "v_and_b32 v161, 0xffff0000, v226\n\t"
            "v_lshlrev_b32 v162, 16, v227\n\t"
            "v_mfma_f32_16x16x32_bf16 v[168:171], v[56:59], v[184:187], v[168:171]\n\t"
            "v_and_b32 v163, 0xffff0000, v227\n\t"
            "v_lshlrev_b32 v176, 16, v228\n\t"
            "v_mfma_f32_16x16x32_bf16 v[168:171], v[60:63], v[188:191], v[168:171]\n\t"
            "v_and_b32 v177, 0xffff0000, v228\n\t"
            "v_lshlrev_b32 v178, 16, v229\n\t"
            "v_mfma_f32_16x16x32_bf16 v[156:159], v[84:87], v[188:191], v[156:159]\n\t"
            "v_and_b32 v179, 0xffff0000, v229\n\t"
            "v_mfma_f32_16x16x32_bf16 v[156:159], v[80:83], v[184:187], v[156:159]\n\t"
            "v_mfma_f32_16x16x32_bf16 v[164:167], v[104:107], v[184:187], v[164:167]\n\t"
            "v_mfma_f32_16x16x32_bf16 v[164:167], v[108:111], v[188:191], v[164:167]\n\t"
            "v_mfma_f32_16x16x32_bf16 v[180:183], v[132:135], v[188:191], v[180:183]\n\t"
            "ds_read_b128 v[188:191], v243 offset:12288\n\t"
            "v_mfma_f32_16x16x32_bf16 v[180:183], v[128:131], v[184:187], v[180:183]\n\t"
            "ds_read_b128 v[184:187], v242 offset:12288\n\t"
            "s_waitcnt lgkmcnt(2)\n\t"
            "v_mfma_f32_16x16x32_bf16 v[144:147], v[16:19], v[192:195], v[144:147]\n\t"
            "v_mfma_f32_16x16x32_bf16 v[144:147], v[20:23], v[172:175], v[144:147]\n\t"
            "v_mfma_f32_16x16x32_bf16 v[168:171], v[64:67], v[192:195], v[168:171]\n\t"
            "v_mfma_f32_16x16x32_bf16 v[168:171], v[68:71], v[172:175], v[168:171]\n\t"
            "v_mfma_f32_16x16x32_bf16 v[164:167], v[112:115], v[192:195], v[164:167]\n\t"
            "v_mfma_f32_16x16x32_bf16 v[164:167], v[116:119], v[172:175], v[164:167]\n\t"
            "v_mfma_f32_16x16x32_bf16 v[148:151], v[44:47], v[172:175], v[148:151]\n\t"
            "v_mfma_f32_16x16x32_bf16 v[148:151], v[40:43], v[192:195], v[148:151]\n\t"
            "s_nop 1\n\t"
            "v_cvt_pk_bf16_f32 v144, v144, v145\n\t"
            "v_mfma_f32_16x16x32_bf16 v[156:159], v[92:95], v[172:175], v[156:159]\n\t"
            "v_cvt_pk_bf16_f32 v145, v146, v147\n\t"
            "v_mfma_f32_16x16x32_bf16 v[156:159], v[88:91], v[192:195], v[156:159]\n\t"
            "v_cvt_pk_bf16_f32 v168, v168, v169\n\t"
            "v_cvt_pk_bf16_f32 v169, v170, v171\n\t"
            "v_mfma_f32_16x16x32_bf16 v[180:183], v[140:143], v[172:175], v[180:183]\n\t"
            "ds_read_b128 v[172:175], v243 offset:12416\n\t"
            "v_cvt_pk_bf16_f32 v164, v164, v165\n\t"
            "v_mfma_f32_16x16x32_bf16 v[180:183], v[136:139], v[192:195], v[180:183]\n\t"
            "ds_read_b128 v[192:195], v242 offset:12416\n\t"
            "v_cvt_pk_bf16_f32 v165, v166, v167\n\t"
            "s_waitcnt lgkmcnt(2)\n\t"
            "v_mfma_f32_16x16x32_bf16 v[152:155], v[0:3], v[184:187], v[152:155]\n\t"
            "v_cvt_pk_bf16_f32 v146, v148, v149\n\t"
            "v_cvt_pk_bf16_f32 v147, v150, v151\n\t"
            "v_mfma_f32_16x16x32_bf16 v[152:155], v[4:7], v[188:191], v[152:155]\n\t"
            "v_cvt_pk_bf16_f32 v170, v156, v157\n\t"
            "v_cvt_pk_bf16_f32 v171, v158, v159\n\t"
            "v_mfma_f32_16x16x32_bf16 v[160:163], v[28:31], v[188:191], v[160:163]\n\t"
            "v_cvt_pk_bf16_f32 v166, v180, v181\n\t"
            "v_cvt_pk_bf16_f32 v167, v182, v183\n\t"
            "v_mfma_f32_16x16x32_bf16 v[160:163], v[24:27], v[184:187], v[160:163]\n\t"
            "v_lshlrev_b32 v148, 16, v230\n\t"
            "v_and_b32 v149, 0xffff0000, v230\n\t"
            "v_mfma_f32_16x16x32_bf16 v[176:179], v[48:51], v[184:187], v[176:179]\n\t"
            "v_lshlrev_b32 v150, 16, v231\n\t"
            "v_and_b32 v151, 0xffff0000, v231\n\t"
            "v_mfma_f32_16x16x32_bf16 v[176:179], v[52:55], v[188:191], v[176:179]\n\t"
            "v_lshlrev_b32 v156, 16, v232\n\t"
            "v_and_b32 v157, 0xffff0000, v232\n\t"
            "v_mfma_f32_16x16x32_bf16 v[148:151], v[76:79], v[188:191], v[148:151]\n\t"
            "v_lshlrev_b32 v158, 16, v233\n\t"
            "v_and_b32 v159, 0xffff0000, v233\n\t"
            "v_mfma_f32_16x16x32_bf16 v[148:151], v[72:75], v[184:187], v[148:151]\n\t"
            "v_lshlrev_b32 v180, 16, v234\n\t"
            "v_and_b32 v181, 0xffff0000, v234\n\t"
            "v_mfma_f32_16x16x32_bf16 v[156:159], v[96:99], v[184:187], v[156:159]\n\t"
            "v_lshlrev_b32 v182, 16, v235\n\t"
            "v_and_b32 v183, 0xffff0000, v235\n\t"
            "v_mfma_f32_16x16x32_bf16 v[156:159], v[100:103], v[188:191], v[156:159]\n\t"
            "buffer_store_dwordx4 v[144:147], v197, s[40:43], s45 offen sc1\n\t"
            "v_mfma_f32_16x16x32_bf16 v[180:183], v[124:127], v[188:191], v[180:183]\n\t"
            "ds_read_b128 v[188:191], v243 offset:12544\n\t"
            "v_mfma_f32_16x16x32_bf16 v[180:183], v[120:123], v[184:187], v[180:183]\n\t"
            "ds_read_b128 v[184:187], v242 offset:12544\n\t"
            "buffer_store_dwordx4 v[168:171], v197, s[40:43], s45 offen offset:64 sc1\n\t"
            "s_waitcnt lgkmcnt(2)\n\t"
            "v_mfma_f32_16x16x32_bf16 v[152:155], v[8:11], v[192:195], v[152:155]\n\t"
            "v_mfma_f32_16x16x32_bf16 v[152:155], v[12:15], v[172:175], v[152:155]\n\t"
            "buffer_store_dwordx4 v[164:167], v197, s[40:43], s45 offen offset:128 sc1\n\t"
            "v_mfma_f32_16x16x32_bf16 v[160:163], v[36:39], v[172:175], v[160:163]\n\t"
            "v_mfma_f32_16x16x32_bf16 v[160:163], v[32:35], v[192:195], v[160:163]\n\t"
            "v_mfma_f32_16x16x32_bf16 v[176:179], v[56:59], v[192:195], v[176:179]\n\t"
            "v_mfma_f32_16x16x32_bf16 v[176:179], v[60:63], v[172:175], v[176:179]\n\t"
            "v_mfma_f32_16x16x32_bf16 v[148:151], v[84:87], v[172:175], v[148:151]\n\t"
            "v_mfma_f32_16x16x32_bf16 v[148:151], v[80:83], v[192:195], v[148:151]\n\t"
            "v_mfma_f32_16x16x32_bf16 v[156:159], v[104:107], v[192:195], v[156:159]\n\t"
            "v_mfma_f32_16x16x32_bf16 v[156:159], v[108:111], v[172:175], v[156:159]\n\t"
            "s_waitcnt lgkmcnt(0)\n\t"
            "v_mfma_f32_16x16x32_bf16 v[152:155], v[16:19], v[184:187], v[152:155]\n\t"
            "v_mfma_f32_16x16x32_bf16 v[152:155], v[20:23], v[188:191], v[152:155]\n\t"
            "v_mfma_f32_16x16x32_bf16 v[160:163], v[44:47], v[188:191], v[160:163]\n\t"
            "v_mfma_f32_16x16x32_bf16 v[160:163], v[40:43], v[184:187], v[160:163]\n\t"
            "v_mfma_f32_16x16x32_bf16 v[180:183], v[132:135], v[172:175], v[180:183]\n\t"
            "v_mfma_f32_16x16x32_bf16 v[180:183], v[128:131], v[192:195], v[180:183]\n\t"
            "v_mfma_f32_16x16x32_bf16 v[176:179], v[64:67], v[184:187], v[176:179]\n\t"
            "v_mfma_f32_16x16x32_bf16 v[176:179], v[68:71], v[188:191], v[176:179]\n\t"
            "s_nop 1\n\t"
            "v_cvt_pk_bf16_f32 v152, v152, v153\n\t"
            "v_mfma_f32_16x16x32_bf16 v[148:151], v[92:95], v[188:191], v[148:151]\n\t"
            "v_cvt_pk_bf16_f32 v153, v154, v155\n\t"
            "v_mfma_f32_16x16x32_bf16 v[148:151], v[88:91], v[184:187], v[148:151]\n\t"
            "v_cvt_pk_bf16_f32 v154, v160, v161\n\t"
            "v_cvt_pk_bf16_f32 v155, v162, v163\n\t"
            "v_mfma_f32_16x16x32_bf16 v[156:159], v[112:115], v[184:187], v[156:159]\n\t"
            "v_cvt_pk_bf16_f32 v176, v176, v177\n\t"
            "v_cvt_pk_bf16_f32 v177, v178, v179\n\t"
            "v_mfma_f32_16x16x32_bf16 v[156:159], v[116:119], v[188:191], v[156:159]\n\t"
            "buffer_store_dwordx4 v[152:155], v198, s[40:43], s45 offen sc1\n\t"
            "v_mfma_f32_16x16x32_bf16 v[180:183], v[140:143], v[188:191], v[180:183]\n\t"
            "v_cvt_pk_bf16_f32 v178, v148, v149\n\t"
            "v_cvt_pk_bf16_f32 v179, v150, v151\n\t"
            "v_mfma_f32_16x16x32_bf16 v[180:183], v[136:139], v[184:187], v[180:183]\n\t"
            "buffer_store_dwordx4 v[176:179], v198, s[40:43], s45 offen offset:64 sc1\n\t"
            "s_nop 1\n\t"
            "v_cvt_pk_bf16_f32 v156, v156, v157\n\t"
            "v_cvt_pk_bf16_f32 v157, v158, v159\n\t"
            "s_nop 2\n\t"
            "v_cvt_pk_bf16_f32 v158, v180, v181\n\t"
            "v_cvt_pk_bf16_f32 v159, v182, v183\n\t"
            "buffer_store_dwordx4 v[156:159], v198, s[40:43], s45 offen offset:128 sc1\n\t"
            "s_nop 7\n\t"
            "s_branch .Lwk_pe%=\n\t"
            "wk_conv:\n\t"
            "s_nop 7\n\t"
            "ds_read_b128 v[168:171], v254\n\t"
            "ds_read_b64 v[144:145], v253\n\t"
            "ds_read_b64 v[146:147], v253 offset:96\n\t"
            "ds_read_b64 v[148:149], v253 offset:1744\n\t"
            "ds_read_b64 v[150:151], v253 offset:1840\n\t"
            "ds_read_b128 v[172:175], v254 offset:64\n\t"
            "ds_read_b64 v[152:153], v253 offset:8\n\t"
            "ds_read_b64 v[154:155], v253 offset:104\n\t"
            "ds_read_b64 v[156:157], v253 offset:1752\n\t"
            "ds_read_b64 v[158:159], v253 offset:1848\n\t"
            "ds_read_b64 v[160:161], v253 offset:16\n\t"
            "ds_read_b64 v[162:163], v253 offset:112\n\t"
            "ds_read_b64 v[164:165], v253 offset:1760\n\t"
            "ds_read_b64 v[166:167], v253 offset:1856\n\t"
            "v_mov_b64 v[176:177], 0\n\t"
            "v_mov_b64 v[178:179], 0\n\t"
            "v_mov_b64 v[180:181], 0\n\t"
            "v_mov_b64 v[182:183], 0\n\t"
            "v_mov_b64 v[184:185], 0\n\t"
            "v_mov_b64 v[186:187], 0\n\t"
            "v_mov_b64 v[188:189], 0\n\t"
            "v_mov_b64 v[190:191], 0\n\t"
            "v_add_u32 v236, s67, v250\n\t"
            "v_xor_b32 v237, 16, v236\n\t"
            "v_add_u32 v248, s67, v251\n\t"
            "s_waitcnt lgkmcnt(9)\n\t"
            "v_smfmac_f32_16x16x64_bf16 v[176:179], v[168:171], v[144:151], v255\n\t"
            "ds_read_b128 v[168:171], v254 offset:128\n\t"
            "ds_read_b64 v[144:145], v253 offset:24\n\t"
            "s_waitcnt lgkmcnt(6)\n\t"
            "v_smfmac_f32_16x16x64_bf16 v[180:183], v[172:175], v[152:159], v255\n\t"
            "ds_read_b64 v[146:147], v253 offset:120\n\t"
            "ds_read_b64 v[148:149], v253 offset:1768\n\t"
            "ds_read_b64 v[150:151], v253 offset:1864\n\t"
            "ds_read_b128 v[172:175], v254 offset:192\n\t"
            "ds_read_b64 v[152:153], v253 offset:32\n\t"
            "ds_read_b64 v[154:155], v253 offset:128\n\t"
            "ds_read_b64 v[156:157], v253 offset:1776\n\t"
            "ds_read_b64 v[158:159], v253 offset:1872\n\t"
            "v_cvt_pk_bf16_f32 v176, v176, v177\n\t"
            "v_cvt_pk_bf16_f32 v177, v178, v179\n\t"
            "v_cvt_pk_bf16_f32 v178, v180, v181\n\t"
            "v_cvt_pk_bf16_f32 v179, v182, v183\n\t"
            "v_mfma_f32_4x4x4_16b_bf16 v[196:199], v[176:177], v[176:177], 0\n\t"
            "ds_write_b128 v236, v[176:179]\n\t"
            "v_mfma_f32_16x16x32_bf16 v[192:195], v[238:241], v[176:179], 0\n\t"
            "v_mov_b64 v[176:177], 0\n\t"
            "v_mov_b64 v[180:181], 0\n\t"
            "v_mfma_f32_4x4x4_16b_bf16 v[196:199], v[178:179], v[178:179], v[196:199]\n\t"
            "v_mov_b64 v[178:179], 0\n\t"
            "v_mov_b64 v[182:183], 0\n\t"
            "s_waitcnt lgkmcnt(10)\n\t"
            "v_smfmac_f32_16x16x64_bf16 v[184:187], v[168:171], v[160:167], v255\n\t"
            "ds_read_b128 v[168:171], v254 offset:256\n\t"
            "ds_read_b64 v[160:161], v253 offset:40\n\t"
            "ds_read_b64 v[162:163], v253 offset:136\n\t"
            "ds_read_b64 v[164:165], v253 offset:1784\n\t"
            "ds_read_b64 v[166:167], v253 offset:1880\n\t"
            "s_waitcnt lgkmcnt(10)\n\t"
            "v_smfmac_f32_16x16x64_bf16 v[188:191], v[172:175], v[144:151], v255\n\t"
            "ds_read_b128 v[172:175], v254 offset:320\n\t"
            "ds_read_b64 v[144:145], v253 offset:48\n\t"
            "ds_read_b64 v[146:147], v253 offset:144\n\t"
            "s_cbranch_scc1 .Lwk_np1%=\n\t"
            "s_mov_b64 exec, 0x3ffff\n\t"
            "buffer_load_dwordx4 v252, s[84:87], s89 offen sc1 lds\n\t"
            "s_mov_b64 exec, -1\n\t"
            ".Lwk_np1%=:\n\t"
            "ds_read_b64 v[148:149], v253 offset:1792\n\t"
            "ds_read_b64 v[150:151], v253 offset:1888\n\t"
            "v_cvt_pk_bf16_f32 v184, v184, v185\n\t"
            "v_cvt_pk_bf16_f32 v185, v186, v187\n\t"
            "s_nop 0\n\t"
            "v_cvt_pk_bf16_f32 v186, v188, v189\n\t"
            "v_cvt_pk_bf16_f32 v187, v190, v191\n\t"
            "v_mfma_f32_4x4x4_16b_bf16 v[196:199], v[184:185], v[184:185], v[196:199]\n\t"
            "s_waitcnt lgkmcnt(14)\n\t"
            "ds_write_b128 v237, v[184:187]\n\t"
            "v_mfma_f32_16x16x32_bf16 v[192:195], v[238:241], v[184:187], v[192:195]\n\t"
            "v_mov_b64 v[184:185], 0\n\t"
            "v_mov_b64 v[188:189], 0\n\t"
            "v_mfma_f32_4x4x4_16b_bf16 v[196:199], v[186:187], v[186:187], v[196:199]\n\t"
            "v_mov_b64 v[186:187], 0\n\t"
            "v_mov_b64 v[190:191], 0\n\t"
            "s_waitcnt lgkmcnt(10)\n\t"
            "v_smfmac_f32_16x16x64_bf16 v[176:179], v[168:171], v[152:159], v255\n\t"
            "ds_read_b128 v[168:171], v254 offset:384\n\t"
            "ds_read_b64 v[152:153], v253 offset:56\n\t"
            "ds_read_b64 v[154:155], v253 offset:152\n\t"
            "ds_read_b64 v[156:157], v253 offset:1800\n\t"
            "ds_read_b64 v[158:159], v253 offset:1896\n\t"
            "s_waitcnt lgkmcnt(10)\n\t"
            "v_smfmac_f32_16x16x64_bf16 v[180:183], v[172:175], v[160:167], v255\n\t"
            "ds_read_b128 v[172:175], v254 offset:448\n\t"
            "ds_read_b64 v[160:161], v253 offset:64\n\t"
            "ds_read_b64 v[162:163], v253 offset:160\n\t"
            "ds_read_b64 v[164:165], v253 offset:1808\n\t"
            "ds_read_b64 v[166:167], v253 offset:1904\n\t"
            "v_cvt_pk_bf16_f32 v176, v176, v177\n\t"
            "v_cvt_pk_bf16_f32 v177, v178, v179\n\t"
            "s_nop 0\n\t"
            "v_cvt_pk_bf16_f32 v178, v180, v181\n\t"
            "v_cvt_pk_bf16_f32 v179, v182, v183\n\t"
            "v_mfma_f32_4x4x4_16b_bf16 v[196:199], v[176:177], v[176:177], v[196:199]\n\t"
            "s_waitcnt lgkmcnt(14)\n\t"
            "ds_write_b128 v236, v[176:179] offset:128\n\t"
            "v_mfma_f32_16x16x32_bf16 v[192:195], v[238:241], v[176:179], v[192:195]\n\t"
            "v_mov_b64 v[176:177], 0\n\t"
            "v_mov_b64 v[180:181], 0\n\t"
            "v_mfma_f32_4x4x4_16b_bf16 v[196:199], v[178:179], v[178:179], v[196:199]\n\t"
            "v_mov_b64 v[178:179], 0\n\t"
            "v_mov_b64 v[182:183], 0\n\t"
            "s_waitcnt lgkmcnt(10)\n\t"
            "v_smfmac_f32_16x16x64_bf16 v[184:187], v[168:171], v[144:151], v255\n\t"
            "ds_read_b128 v[168:171], v254 offset:512\n\t"
            "ds_read_b64 v[144:145], v253 offset:72\n\t"
            "ds_read_b64 v[146:147], v253 offset:168\n\t"
            "ds_read_b64 v[148:149], v253 offset:1816\n\t"
            "ds_read_b64 v[150:151], v253 offset:1912\n\t"
            "s_waitcnt lgkmcnt(10)\n\t"
            "v_smfmac_f32_16x16x64_bf16 v[188:191], v[172:175], v[152:159], v255\n\t"
            "ds_read_b128 v[172:175], v254 offset:576\n\t"
            "ds_read_b64 v[152:153], v253 offset:80\n\t"
            "ds_read_b64 v[154:155], v253 offset:176\n\t"
            "ds_read_b64 v[156:157], v253 offset:1824\n\t"
            "ds_read_b64 v[158:159], v253 offset:1920\n\t"
            "v_cvt_pk_bf16_f32 v184, v184, v185\n\t"
            "v_cvt_pk_bf16_f32 v185, v186, v187\n\t"
            "s_cbranch_scc1 .Lwk_np0%=\n\t"
            "s_mov_b64 exec, 0x3ffff\n\t"
            "buffer_load_dwordx4 v252, s[84:87], s89 offen sc1 lds\n\t"
            "s_mov_b64 exec, -1\n\t"
            ".Lwk_np0%=:\n\t"
            "s_nop 0\n\t"
            "v_cvt_pk_bf16_f32 v186, v188, v189\n\t"
            "v_cvt_pk_bf16_f32 v187, v190, v191\n\t"
            "v_mfma_f32_4x4x4_16b_bf16 v[196:199], v[184:185], v[184:185], v[196:199]\n\t"
            "s_waitcnt lgkmcnt(14)\n\t"
            "ds_write_b128 v237, v[184:187] offset:128\n\t"
            "v_mfma_f32_16x16x32_bf16 v[192:195], v[238:241], v[184:187], v[192:195]\n\t"
            "v_mov_b64 v[184:185], 0\n\t"
            "v_mov_b64 v[188:189], 0\n\t"
            "v_mfma_f32_4x4x4_16b_bf16 v[196:199], v[186:187], v[186:187], v[196:199]\n\t"
            "v_mov_b64 v[186:187], 0\n\t"
            "v_mov_b64 v[190:191], 0\n\t"
            "s_waitcnt lgkmcnt(10)\n\t"
            "v_smfmac_f32_16x16x64_bf16 v[176:179], v[168:171], v[160:167], v255\n\t"
            "ds_read_b128 v[168:171], v254 offset:640\n\t"
            "ds_read_b64 v[160:161], v253 offset:88\n\t"
            "ds_read_b64 v[162:163], v253 offset:184\n\t"
            "ds_read_b64 v[164:165], v253 offset:1832\n\t"
            "ds_read_b64 v[166:167], v253 offset:1928\n\t"
            "s_waitcnt lgkmcnt(10)\n\t"
            "v_smfmac_f32_16x16x64_bf16 v[180:183], v[172:175], v[144:151], v255\n\t"
            "ds_read_b128 v[172:175], v254 offset:704\n\t"
            "v_cvt_pk_bf16_f32 v176, v176, v177\n\t"
            "v_cvt_pk_bf16_f32 v177, v178, v179\n\t"
            "s_nop 1\n\t"
            "v_mfma_f32_4x4x4_16b_bf16 v[196:199], v[176:177], v[176:177], v[196:199]\n\t"
            "s_nop 1\n\t"
            "v_cvt_pk_bf16_f32 v178, v180, v181\n\t"
            "v_cvt_pk_bf16_f32 v179, v182, v183\n\t"
            "s_nop 1\n\t"
            "v_mfma_f32_4x4x4_16b_bf16 v[196:199], v[178:179], v[178:179], v[196:199]\n\t"
            "ds_write_b128 v236, v[176:179] offset:256\n\t"
            "v_mfma_f32_16x16x32_bf16 v[192:195], v[238:241], v[176:179], v[192:195]\n\t"
            "s_waitcnt lgkmcnt(6)\n\t"
            "v_smfmac_f32_16x16x64_bf16 v[184:187], v[168:171], v[152:159], v255\n\t"
            "s_waitcnt lgkmcnt(1)\n\t"
            "v_smfmac_f32_16x16x64_bf16 v[188:191], v[172:175], v[160:167], v255\n\t"
            "s_nop 5\n\t"
            "v_cvt_pk_bf16_f32 v184, v184, v185\n\t"
            "v_cvt_pk_bf16_f32 v185, v186, v187\n\t"
            "s_nop 1\n\t"
            "v_mfma_f32_4x4x4_16b_bf16 v[196:199], v[184:185], v[184:185], v[196:199]\n\t"
            "v_cvt_pk_bf16_f32 v186, v188, v189\n\t"
            "v_cvt_pk_bf16_f32 v187, v190, v191\n\t"
            "s_nop 1\n\t"
            "v_mfma_f32_4x4x4_16b_bf16 v[196:199], v[186:187], v[186:187], v[196:199]\n\t"
            "v_mfma_f32_16x16x32_bf16 v[192:195], v[238:241], v[184:187], v[192:195]\n\t"
            "ds_write_b128 v237, v[184:187] offset:256\n\t"
            "s_nop 6\n\t"
            "v_cndmask_b32_e64 v193, v196, v197, s[60:61]\n\t"
            "v_cndmask_b32_e64 v193, v193, v198, s[62:63]\n\t"
            "v_cndmask_b32_e64 v193, v193, v199, s[64:65]\n\t"
            "ds_write_b64 v248, v[192:193] offset:18816\n\t"
            "s_waitcnt lgkmcnt(0)\n\t"
            "s_nop 7\n\t"
            "s_setpc_b64 vcc\n\t"
            ".Lwk_pe%=:\n\t"
            : B_SIO, "+{v247}"(l16_), "+{v228}"(q0), "+{v229}"(q1), "+{v232}"(q2), "+{v233}"(q3), "+{v234}"(q4)
            : "{v242}"(oE_), "{v243}"(oO_), "{v244}"(pR0_), "{v245}"(pR1_), "{v246}"(pR2_), "{v250}"(pXw_), "{v251}"(pSTw_), "{v252}"(aCA_), "{v253}"(aB_), "{v254}"(aA_), "{v255}"(idx_), B_SIN, "{s44}"(ARR_OFF + blockIdx.x * 8), "{s[80:81]}"(mrow), "{s82}"(wave == 6 && cnt == 21 ? REC_OFF + (start - 20 * ((blockIdx.x & 7) * 32 + xi)) * 1280 : 0)
            : "v144", "v145", "v146", "v147", "v148", "v149", "v150", "v151", "v152", "v153", "v154", "v155", "v156", "v157", "v158", "v159", "v160", "v161", "v162", "v163", "v164", "v165", "v166", "v167", "v168", "v169", "v170", "v171", "v172", "v173", "v174", "v175", "v176", "v177", "v178", "v179", "v180", "v181", "v182", "v183", "v184", "v185", "v186", "v187", "v188", "v189", "v190", "v191", "v192", "v193", "v194", "v195", "v196", "v197", "v198", "v199", "v236", "v237", "v248", "v249", "v0", "v1", "v2", "v3", "v4", "v5", "v6", "v7", "v8", "v9", "v10", "v11", "v12", "v13", "v14", "v15", "v16", "v17", "v18", "v19", "v20", "v21", "v22", "v23", "v24", "v25", "v26", "v27", "v28", "v29", "v30", "v31", "v32", "v33", "v34", "v35", "v36", "v37", "v38", "v39", "v40", "v41", "v42", "v43", "v44", "v45", "v46", "v47", "v48", "v49", "v50", "v51", "v52", "v53", "v54", "v55", "v56", "v57", "v58", "v59", "v60", "v61", "v62", "v63", "v64", "v65", "v66", "v67", "v68", "v69", "v70", "v71", "v72", "v73", "v74", "v75", "v76", "v77", "v78", "v79", "v80", "v81", "v82", "v83", "v84", "v85", "v86", "v87", "v88", "v89", "v90", "v91", "v92", "v93", "v94", "v95", "v96", "v97", "v98", "v99", "v100", "v101", "v102", "v103", "v104", "v105", "v106", "v107", "v108", "v109", "v110", "v111", "v112", "v113", "v114", "v115", "v116", "v117", "v118", "v119", "v120", "v121", "v122", "v123", "v124", "v125", "v126", "v127", "v128", "v129", "v130", "v131", "v132", "v133", "v134", "v135", "v136", "v137", "v138", "v139", "v140", "v141", "v142", "v143", "v200", "v201", "v202", "v204", "v205", "v206", "v207", "v208", "v209", "v210", "v211", "v212", "v213", "v214", "v215", "v216", "v217", "v218", "v219", "v220", "v221", "v222", "v223", "v224", "v225", "v226", "v227", "v230", "v231", "v235", B_SCLOB);
        }
        __builtin_amdgcn_endpgm();
    }
}

extern "C" void kernel_launch(void* const* d_in, const int* in_sizes, int n_in,
                              void* d_out, int out_size, void* d_ws, size_t ws_size,
                              hipStream_t stream) {
    const unsigned short* x = (const unsigned short*)d_in[0];
    const unsigned short* dw = (const unsigned short*)d_in[1];
    const unsigned short* wqkv = (const unsigned short*)d_in[2];
    const unsigned short* wo = (const unsigned short*)d_in[3];
    unsigned short* out = (unsigned short*)d_out;
    u32x4* wf = (u32x4*)d_ws;
    win_attn_kernel<<<GRID, 512, 0, stream>>>(x, dw, wqkv, wo, wf, out);
}
```

```cpp
#include <hip/hip_runtime.h>
#include <hip/hip_bf16.h>
#include <stdint.h>

typedef short bf16x8 __attribute__((ext_vector_type(8)));
typedef float f32x4 __attribute__((ext_vector_type(4)));
typedef float f32x2 __attribute__((ext_vector_type(2)));
typedef unsigned u32x2 __attribute__((ext_vector_type(2)));
typedef unsigned u32x4 __attribute__((ext_vector_type(4)));

constexpr int IMG = 511;
constexpr int CH = 192;
constexpr int NWX = 73;
constexpr int NWIN = NWX * NWX;
constexpr int GRID = 256;
constexpr int ROWBYTES = IMG * CH * 2;
constexpr int XBIAS = (IMG + 1) * CH * 2;
constexpr unsigned OOB = 0x40000000u;

constexpr int FLAG_OFF = 7856;
constexpr int X_OFF = 32768;
constexpr int X1_OFF = 53632;
constexpr int O_OFF = 74496;
constexpr int V_OFF = 92928;
constexpr int KQL_OFF = 111360;
constexpr int OL_OFF = 129024;
constexpr int AT_OFF = 137472;
constexpr int CT_OFF = AT_OFF + 19584;
constexpr int RS_OFF = CT_OFF + 4096;
constexpr int RSX_OFF = 162816;
constexpr int WL_OFF = RS_OFF + 2304 + 64;
constexpr int LDS_BYTES = WL_OFF + 128;
#define RS_BASE (RS_OFF + wave * 384)
constexpr int STL_OFF = O_OFF + 12288;
constexpr int DW_OFF = O_OFF + 8448;
constexpr int CP_OFF = X1_OFF + 16384;
#define LX_OFF(i) ((i) < 2 ? CT_OFF + 1024 + (i) * 384 : KQL_OFF + ((i) - 2) * 800)
constexpr int PF_OFF = CT_OFF + 2048;
#define LB_OFF (w < 3 ? V_OFF + w * 13952 : X1_OFF)
#define LPOS(wc) (((wc) & 1) ? 0 : 6)
constexpr unsigned CROW = 0x060a0230u;
constexpr unsigned CLANE = 0x00132C3Fu;

constexpr unsigned long long TOKMAP[6] = {0x20001c190f2a2202ull, 0x052517121f061a13ull, 0x181e08151d0b260cull, 0x1b2b2903102d0901ull, 0x04162e0e2824142cull, 0x110a072127232f0dull};
constexpr unsigned long long ROWMAP[8] = {0x08321b240f1c0006ull, 0x23310e2a09170d14ull, 0x210c2f030a273418ull, 0x1d2b11221f043739ull, 0x3234363a103e192dull, 0x291a20022c3f1605ull, 0x303a3c2628122e15ull, 0x39253d1e01070b13ull};

constexpr int WF_BYTES = 8 * 36 * 1024;
constexpr int FLG_OFF = WF_BYTES;
constexpr int ARR_OFF = FLG_OFF + 4096;
constexpr int EPO_OFF = ARR_OFF + 2048;
constexpr int CTR_OFF = EPO_OFF + 128;
constexpr int REC_OFF = WF_BYTES + 8192;
constexpr int WS_BYTES = REC_OFF + 209 * 1280;
constexpr int SC1 = 16;

#define MFMA32(a, b, c) __builtin_amdgcn_mfma_f32_16x16x32_bf16((a), (b), (c), 0, 0, 0)
#define BC8(v) __builtin_bit_cast(bf16x8, (v))
#define BAR() asm volatile("s_waitcnt lgkmcnt(0)\n\ts_barrier" ::: "memory")
#define SB() __builtin_amdgcn_sched_barrier(0)
#define OPQ(x) asm volatile("" : "+v"(x))
#define ZERO4 ((f32x4){0.f, 0.f, 0.f, 0.f})

typedef __bf16 nbf16x2 __attribute__((ext_vector_type(2)));
__device__ __forceinline__ unsigned pk2(float a, float b) {
    f32x2 v = {a, b};
    nbf16x2 r = __builtin_convertvector(v, nbf16x2);
    return __builtin_bit_cast(unsigned, r);
}
__device__ __forceinline__ float bflo(unsigned u) { return __uint_as_float(u << 16); }
__device__ __forceinline__ float bfhi(unsigned u) { return __uint_as_float(u & 0xffff0000u); }

__device__ __forceinline__ float bf2f(unsigned short v) { return __uint_as_float((unsigned)v << 16); }
__device__ __forceinline__ unsigned byte_of(unsigned long long a, unsigned long long b, int i) {
    return (unsigned)(((i & 8) ? b : a) >> (8 * (i & 7))) & 0xFFu;
}
#define M24(a, b) ((unsigned)__mul24((int)(a), (int)(b)))
__device__ __forceinline__ void seq_to_win(int seq, int& wy, int& wx) {
    const bool last = seq >= 5110;
    const int s = last ? 35 : M24(seq, 28729) >> 22, r = seq - M24(146, s);
    const int kx = last ? M24(r, 171) >> 10 : r >> 2, pos = r - M24(last ? 6 : 4, kx);
    const bool low = last && pos >= (kx == 36 ? 2 : 4);
    wy = low ? 72 : 2 * s + (pos & 1);
    wx = 2 * kx + (low ? (kx == 36 ? 0 : pos - 4) : pos >> 1);
}
__device__ __forceinline__ int win_to_seq(int wy, int wx) {
    const int kx = wx >> 1;
    return M24(146, wy == 72 ? 35 : wy >> 1) + M24(wy >= 70 ? 6 : 4, kx) + (wy == 72 ? (kx == 36 ? 2 : 4 + (wx & 1)) : 2 * (wx & 1) + (wy & 1));
}
__device__ __forceinline__ int block_of(int wy, int wx) { return wy >= 70 ? 1295 + (wx & ~1) + (wy == 72) : M24(37, wy >> 1) + (wx >> 1); }
__device__ __forceinline__ void block_pos(int b, int& ky, int& kx) {
    const int s = M24(b, 1772) >> 16, l = b - 1295;
    kx = l >= 0 ? l >> 1 : b - M24(37, s);
    ky = l >= 0 ? 70 + 2 * (l & 1) : 2 * s;
}
__device__ __forceinline__ unsigned win_org(int wy, int wx) { return M24(wy, 7 * ROWBYTES) + M24(wx, 7 * CH * 2); }
__device__ __forceinline__ unsigned wl_entry(int wy, int wx) {
    return win_org(wy, wx) | (wy == 0 ? 0x08000000u : 0u) | (wx == 0 ? 0x10000000u : 0u) |
           (wx == NWX - 1 ? 0x20000000u : 0u) | ((unsigned)(wx & 1) << 30) | ((unsigned)(wy & 1) << 31);
}
__device__ __forceinline__ unsigned dma_off(int lane) {
    const int col = (lane * 43) >> 8;
    return (unsigned)(col * (CH * 2 - 96) + 16 * lane) | (col == 0 ? 0x10000000u : 0u) | (col == 8 ? 0x20000000u : 0u);
}

#if defined(__HIP_DEVICE_COMPILE__)
#define NOPK __attribute__((target("no-packed-fp32-ops")))
#else
#define NOPK
#endif
__global__ __launch_bounds__(512, 2) NOPK void win_attn_kernel(const unsigned short* __restrict__ x,
                                                          const unsigned short* __restrict__ dw, const unsigned short* __restrict__ wqkv,
                                                          const unsigned short* __restrict__ wo, u32x4* wf,
                                                          unsigned short* __restrict__ out) {
    __shared__ __attribute__((aligned(16))) char smem[LDS_BYTES];

    asm volatile("wk_code_begin:");
    const int tid = threadIdx.x;
    const int wave = __builtin_amdgcn_readfirstlane(tid >> 6);
    const int w = wave & 3;
    const unsigned ldsb = (unsigned)(size_t)smem;
    {
        unsigned long long pc;
        unsigned len, rel;
        asm volatile("s_getpc_b64 %0\n.Lwk_here%=:\n\ts_mov_b32 %1, wk_code_end-wk_code_begin\n\ts_mov_b32 %2, .Lwk_here%=-wk_code_begin" : "=s"(pc), "=s"(len), "=s"(rel));
        pc -= rel;
        const u32x4 cr = {(unsigned)pc, (unsigned)(pc >> 32) & 0xffffu, len, 0x00020000u};
        asm volatile("s_mov_b32 m0, %0\n\ts_nop 0\n\tbuffer_load_dwordx4 %1, %2, %3 offen lds"
                     :: "s"(ldsb + X1_OFF + 14336 + (w & 1) * 1024), "v"((tid & 63) * 16), "s"(cr), "s"((wave * 32 + (blockIdx.x >> 3)) * 1024) : "m0", "memory");
    }
    *(unsigned*)(smem + PF_OFF + tid * 4) = 0x9E3779B9u * (tid + 1);
    *(unsigned*)(smem + (tid < 192 ? AT_OFF + 18816 + tid * 4 : FLAG_OFF + (tid & 3) * 4)) = 0u;
    BAR();
    asm volatile("" :: "s"(x), "s"(dw), "s"(wqkv), "s"(wo), "s"(wf));
    const auto wrsrc = __builtin_amdgcn_make_buffer_rsrc((void*)wf, 0, WS_BYTES, 0x00020000);
    unsigned tag_lo = 0, tag_hi = 0;
#define TAG_READY                                                                                  \
    {                                                                                              \
        unsigned long long tag_;                                                                   \
        asm volatile("s_load_dwordx2 %0, %1, %2\n\ts_waitcnt lgkmcnt(0)" : "=s"(tag_) : "s"(wf), "n"(EPO_OFF) : "memory"); \
        tag_lo = (unsigned)tag_ + 1u; tag_hi = (unsigned)(tag_ >> 32);                             \
    }
    const int pb = blockIdx.x;
    if (pb < 36) {
        const int lane = tid & 63, n = lane & 15, g = lane >> 4;
        const bool isO = wave >= 6;
        const int fr = isO ? 216 + 2 * pb + (wave - 6) : 6 * pb + wave;
        const int tile = fr / 6, ks = fr - 6 * tile, tw = tile / 6, mdt = tile - 6 * tw;
        const int col = isO ? 96 * (tw - 6) + 32 * (mdt >> 1) + 8 * (n >> 2) + 4 * (mdt & 1) + (n & 3)
                            : (tw < 4 ? (mdt < 3 ? 192 : -48) + 48 * tw : 96 * tw) + 16 * mdt + n;
        const int rb = isO ? 384 : 1152;
        const auto rr = __builtin_amdgcn_make_buffer_rsrc((void*)(isO ? wo : wqkv), 0, 192 * rb, 0x00020000);
        const int vo = (48 * (g >> 1) + 8 * (g & 1)) * rb + 2 * col;
        float v[8];
#pragma unroll
        for (int j = 0; j < 8; ++j) v[j] = bf2f(__builtin_amdgcn_raw_buffer_load_b16(rr, vo, (96 * (ks & 1) + 16 * (ks >> 1) + j) * rb, 0));
        TAG_READY
        float* red = (float*)smem;
        red[n * 36 + wave * 4 + g] = ((v[0] + v[1]) + (v[2] + v[3])) + ((v[4] + v[5]) + (v[6] + v[7]));
        BAR();
        float mean = 0.f;
#pragma unroll
        for (int i = 0; i < 6; ++i) {
            const f32x4 p = *(const f32x4*)(red + n * 36 + 4 * i);
            mean += (p[0] + p[1]) + (p[2] + p[3]);
        }
        mean = isO ? 0.f : mean * (1.f / 192.f);
        u32x4 o;
#pragma unroll
        for (int j = 0; j < 4; ++j) o[j] = pk2(v[2 * j] - mean, v[2 * j + 1] - mean);
        __builtin_amdgcn_raw_buffer_store_b128(o, wrsrc, lane * 16, fr * 1024, SC1);
        if (pb == 0 && tid == 448) __builtin_amdgcn_raw_buffer_store_b32(0u, wrsrc, CTR_OFF, 0, SC1);
        asm volatile("s_waitcnt vmcnt(0)" ::: "memory");
        BAR();
        if (tid < 8) __builtin_amdgcn_raw_buffer_store_b64((u32x2){tag_lo, tag_hi}, wrsrc, FLG_OFF + tid * 512 + pb * 8, 0, SC1);
    }
    const int xi = blockIdx.x >> 3, nshort = (int)(0x0506060606060606ull >> (8 * (blockIdx.x & 7))) & 63;
    const int start = 672 * (blockIdx.x & 7) - ((int)(0x2a241e18120c0600ull >> (8 * (blockIdx.x & 7))) & 63) + 21 * xi - (xi < nshort ? xi : nshort);
    const int cnt = xi < nshort ? 20 : 21;

    const auto xrsrc = __builtin_amdgcn_make_buffer_rsrc((void*)((const char*)x - XBIAS), 0, IMG * IMG * CH * 2 + XBIAS, 0x00020000);
#define LANE_SETUP                                                                                 \
    int lane;                                                                                      \
    asm volatile("v_mbcnt_lo_u32_b32 %0, -1, 0\n\tv_mbcnt_hi_u32_b32 %0, -1, %0" : "=v"(lane));    \
    __builtin_assume(lane >= 0 && lane < 64);                                                      \
    const int g = lane >> 4, l15 = lane & 15, sw = l15 >> 1;                                       \
    int fE = X_OFF + l15 * 384 + ((g ^ sw) << 4);                                                  \
    OPQ(fE);

#define DMA_SLICE(en)                                                                              \
        {                                                                                          \
            const unsigned vo_ = dma_off(lane) & (0x401fffffu | ((en) & 0x38000000u));             \
            const unsigned so_ = ((en) & 0x7ffffffu) + 96 * w;                                     \
            _Pragma("unroll") for (int r = 0; r < 9; ++r) {                                        \
                unsigned m0_ = ldsb + w * 8192 + r * 872;                                          \
                unsigned sr_ = (r == 0 && ((en) & 0x08000000u)) ? OOB : so_ + r * ROWBYTES;        \
                asm volatile("s_mov_b32 m0, %0\n\ts_mov_b32 exec_hi, 0x3fffff\n\tbuffer_load_dwordx4 %1, %2, %3 offen lds\n\ts_mov_b32 exec_hi, -1" \
                             :: "s"(m0_), "v"(vo_), "s"(xrsrc), "s"(sr_) : "m0", "memory");        \
            }                                                                                      \
        }
#define XPAT(r) (((r) < 48 ? (r) : 48) * 384 + (((2 * w) ^ (((r) >> 1) & 7)) << 4))
#define SPAT(r) ((w >> 1) * 1024 + (w & 1) * 8 + (r) * 16)
#define CONV_CONSTS(var) CONV_ROWS(var) CONV_OPS
#define CONV_ROWS(var) \
            const unsigned long long rlo = g == 0 ? ROWMAP[0] : (g == 1 ? ROWMAP[2] : (g == 2 ? ROWMAP[4] : ROWMAP[6])); \
            const unsigned long long rhi = g == 0 ? ROWMAP[1] : (g == 1 ? ROWMAP[3] : (g == 2 ? ROWMAP[5] : ROWMAP[7])); \
            int xrow = byte_of(rlo, rhi, l15); \
            if (lane == (int)((CLANE >> (8 * (var))) & 0xFF)) xrow = 48; \
            if (lane == 63) xrow = (CROW >> (8 * (var))) & 0xFF; \
            const unsigned pXw_ = XPAT(xrow); \
            const unsigned pSTw_ = (w >> 1) * 1024 + (w & 1) * 8 + xrow * 16; \
            const int oy = (0x5320 >> (4 * (l15 >> 2))) & 15, ox = (0x5320 >> (4 * (l15 & 3))) & 15; \
            const unsigned aB_ = ldsb + w * 8192 + (oy + (g >> 1)) * 872 + (ox + 2 * (g & 1)) * 96;
#define CONV_OPS \
            const int dyi = g - (l15 >> 3); \
            const unsigned aA_ = ldsb + AT_OFF + (l15 & 3) * 16 + \
                                 ((dyi < 0 || dyi > 2) ? 18816 : (dyi == 0 ? 0 : (dyi == 1 ? 6208 : 12544)) + ((l15 >> 2) & 1) * 3072 + w * 768); \
            const unsigned idx_ = (l15 & 3) * 0x5555u; \
            const unsigned se = (l15 == 4 * g) ? 0x3F803F80u : 0u; \
            const u32x4 sel4 = {se, se, se, se};

    unsigned en0, en1;
    {
        LANE_SETUP
        const int px = (lane * 43) >> 8;

        int wy, wx;
        seq_to_win(start + (lane < cnt ? lane : cnt - 1), wy, wx);
        const unsigned en = wl_entry(wy, wx);
        en0 = __builtin_amdgcn_readlane(en, 0);
        en1 = __builtin_amdgcn_readlane(en, 1);
        if (wave >= 4) {
            const int bk = block_of(wy, wx);
            const int key0 = __builtin_amdgcn_readlane(bk, 0), nblk = __builtin_amdgcn_readlane(bk, 63) - key0 + 1;
            const int b = l15 < nblk ? l15 : 0;
            int ky, kx;
            block_pos(key0 + b, ky, kx);
            const unsigned org = l15 < nblk ? (win_org(ky, 2 * kx) + (5 * ROWBYTES + 5 * CH * 2 + XBIAS)) | (kx == 36 ? OOB : 0u) : OOB + 0x8000000u;
            const unsigned lo = (px & 3) * (CH * 2) + (lane - 6 * px) * 16, keep = (px & 2) ? ~0u : ~OOB;
#pragma unroll
            for (int p = 0; p < 4; ++p) {
                if (p < 3 || nblk > 6) {
                    const unsigned oa = __builtin_amdgcn_readlane(org, 2 * p), ob = __builtin_amdgcn_readlane(org, 2 * p + 1);
                    const unsigned vo_ = ((lane < 24 ? oa : ob) & keep) + lo;
#pragma unroll
                    for (int r = 0; r < 4; ++r) {
                        unsigned m0_ = ldsb + LB_OFF + (4 * p + r) * 872;
                        asm volatile("s_mov_b32 m0, %0\n\ts_mov_b32 exec_hi, 0xffff\n\tbuffer_load_dwordx4 %1, %2, %3 offen lds\n\ts_mov_b32 exec_hi, -1"
                                     :: "s"(m0_), "v"(vo_), "s"(xrsrc), "s"(96 * w + r * ROWBYTES) : "m0", "memory");
                    }
                }
            }
            SB();
            const int ly = ky + (g >> 1), lx = 2 * kx + (g & 1), li = win_to_seq(ly, lx) - start;
            const bool ok = ly < NWX && lx < NWX && li >= 0 && li < cnt && l15 < nblk;
            const int row = ok ? li : 47;
            unsigned* cp = (unsigned*)(smem + CP_OFF + (w * 64 + lane) * 16);
            *(u32x2*)cp = (u32x2){(unsigned)XPAT(row), SPAT(row) + (unsigned)(STL_OFF - O_OFF - 18816)};
            cp[2] = ldsb + LB_OFF + (4 * (b >> 1) + (g >> 1)) * 872 + (b & 1) * 384 + (g & 1) * 192;
        } else {
            {
                const auto dr = __builtin_amdgcn_make_buffer_rsrc((void*)dw, 0, 9 * 384, 0x00020000);
                unsigned m0_ = ldsb + DW_OFF + w * 864, vo_ = px * 288 + lane * 16;
                asm volatile("s_mov_b32 m0, %0\n\ts_mov_b32 exec_hi, 0x3fffff\n\tbuffer_load_dwordx4 %1, %2, %3 offen lds\n\ts_mov_b32 exec_hi, -1"
                             :: "s"(m0_), "v"(vo_), "s"(dr), "s"(96 * w) : "m0", "memory");
            }
            DMA_SLICE(en0)
            SB();
            if (wave == 2) {
                u32x4 so;
#pragma unroll
                for (int e = 0; e < 3; ++e) {
                    const int tk = byte_of(TOKMAP[2 * e], TOKMAP[2 * e + 1], l15);
                    const int ty = (tk * 37) >> 8, tx = tk - 7 * ty;
                    so[e] = (unsigned)(ty * ROWBYTES + tx * (CH * 2)) + 16 * g;
                }
                so[3] = dma_off(lane);
                *(u32x4*)(smem + CT_OFF + lane * 16) = so;
            }
            if (wave == 1 && lane < 32) *(unsigned*)(smem + WL_OFF + lane * 4) = lane < 20 ? en : 0x7ffffffu;
            if (wave == 1 && lane == 20) *(unsigned*)(smem + PF_OFF + 520) = en;
            asm volatile("s_waitcnt vmcnt(9)" ::: "memory");
            if (lane < 48) {
                unsigned short dv[9];
#pragma unroll
                for (int i = 0; i < 9; ++i) dv[i] = *(const unsigned short*)(smem + DW_OFF + w * 864 + i * 96 + lane * 2);
#pragma unroll
                for (int dy = 0; dy < 3; ++dy) {
                    const unsigned a = dv[3 * dy], b2 = dv[3 * dy + 1] | (dy == 1 ? 0x3F800000u : 0u), c = dv[3 * dy + 2];
                    char* p = smem + AT_OFF + (dy == 0 ? 0 : (dy == 1 ? 6208 : 12544)) + w * 768 + lane * 16;
                    *(u32x4*)p = (u32x4){a, b2, c, 0u};
                    *(u32x4*)(p + 3072) = (u32x4){0u, a, b2, c};
                }
            }
            asm volatile("s_mov_b64 exec, 1\n\tds_add_u32 %0, %1\n\ts_mov_b64 exec, -1" :: "v"(ldsb + FLAG_OFF + 8), "v"(1) : "memory");
        }
    }
    const unsigned long long mrow = 0x0001000100010001ull * ((1u << (cnt - 16)) - 1u);
#define KARGS                                                                                      \
    unsigned long long xb, wb, ob;                                                                 \
    asm volatile("s_load_dwordx2 %0, %3, 0x0\n\ts_load_dwordx2 %1, %3, 0x20\n\ts_load_dwordx2 %2, %3, 0x28\n\ts_waitcnt lgkmcnt(0)" \
                 : "=&s"(xb), "=&s"(wb), "=&s"(ob) : "s"(__builtin_amdgcn_kernarg_segment_ptr()) : "memory"); \
    xb -= XBIAS;                                                                                   \
    const u32x4 wr = {(unsigned)wb, (unsigned)(wb >> 32) & 0xffffu, (unsigned)WS_BYTES, 0x00020000u}; \
    const u32x4 orr = {(unsigned)ob, (unsigned)(ob >> 32) & 0xffffu, (unsigned)(IMG * IMG * CH * 2), 0x00020000u}; \
    const u32x4 xr = {(unsigned)xb, (unsigned)(xb >> 32) & 0xffffu, (unsigned)(IMG * IMG * CH * 2 + XBIAS), 0x00020000u};
    const int tk_ = (xi + 4) / 6;
    const unsigned touch = wave * 36864 + ((xi + 4 - 6 * tk_) * 6 + tk_) * 1024;
#define B_SCALARS \
            CONV_CONSTS(0) \
            const unsigned aCA_ = ldsb + CT_OFF + lane * 16; \
            KARGS \
            unsigned s45 = OOB, s46 = (en0 & 0x7ffffffu) + 96 * w, s47 = (en1 & 0x7ffffffu) + 96 * w; \
            unsigned s48 = ldsb + X_OFF, s67 = ldsb + X1_OFF, s76 = ldsb + WL_OFF + 8, s70 = 0; \
            unsigned s56 = __builtin_amdgcn_readfirstlane((en0 >> 30) * 4 + (en1 >> 30)); \
            unsigned s69 = 0x401fffffu | (en1 & 0x38000000u), s79 = (en1 & 0x08000000u) ? OOB : s47, s59 = ldsb + LX_OFF(1) + 32 * w; \
            unsigned cp_ = ldsb + CP_OFF + (w * 64 + lane) * 16; \
            unsigned long long xp = 0, sp = 0; \
            _Pragma("unroll") for (int v = 0; v < 4; ++v) { \
                const unsigned r = (CROW >> (8 * v)) & 0xFF; \
                xp |= (unsigned long long)XPAT(r) << (16 * v); \
                sp |= (unsigned long long)SPAT(r) << (16 * v); \
            }
#define B_SIO "+{s45}"(s45), "+{s46}"(s46), "+{s47}"(s47), "+{s48}"(s48), "+{s67}"(s67), "+{s76}"(s76), "+{s56}"(s56), "+{s70}"(s70), "+{s69}"(s69), "+{s79}"(s79), "+{s59}"(s59), "+{v203}"(cp_)
#define B_SIN "{v[238:241]}"(sel4), "{s[40:43]}"(orr), "{s[72:75]}"(xr), "{s39}"(0x401fffffu), "{s[28:31]}"(wr), "{s33}"(wave * 36864), "{s78}"(96 * w), \
              "{s27}"(96 * w - 384), "{s[50:51]}"(xp), "{s[52:53]}"(sp), "{s54}"(CLANE), "{s55}"(CROW & 0x0F0F0F0Fu), "{s[34:35]}"(0x0001000100010001ull), \
              "{s[60:61]}"(0x2222222222222222ull), "{s[62:63]}"(0x4444444444444444ull), "{s[64:65]}"(0x8888888888888888ull), \
              "{s66}"(ldsb + w * 8192), "{s71}"((ldsb + X_OFF) ^ (ldsb + X1_OFF))
#define B_SCLOB "s83", "s84", "s26", "s77", "s68", "s36", "s37", "s38", "s57", "s58", "s49", "vcc", "scc", "m0", "memory"

    if (wave < 4) {
        LANE_SETUP
        const int dW = X_OFF + l15 * 384 + (((2 * w + (g >> 1)) ^ sw) << 4) + 8 * (g & 1);
        const int vB = V_OFF + w * 4608 + 1024 + lane * 8, rsR = RS_BASE + l15 * 4;
        {
            const unsigned rsb = ldsb + RS_BASE;
            const unsigned pE_ = fE - X_OFF, aO_ = ldsb + dW + (O_OFF - X_OFF), sel_ = (l15 & 3) ? 0u : 1u, aVB_ = ldsb + vB;
            unsigned pST_ = lane * 16, aPLw_ = rsb + lane * 2, aRSr_ = ldsb + rsR, aRSw_ = rsb + lane * 4;
            unsigned g16_ = (l15 & 3) ? ldsb + AT_OFF + 18816 : g * 16, g8_ = (l15 & 3) ? ldsb + AT_OFF + 18816 : g * 8;
            unsigned loVL_ = ((2 * w + (l15 >> 3)) << 4) + (l15 & 7) * 2, loOL_ = g * 128;
            unsigned on = (l15 & 3) ? 0u : 0x3F803F80u;
            CONV_CONSTS(en0 >> 30)
            unsigned c0 = lane == (int)((CLANE >> (8 * (en0 >> 30))) & 0xFF) ? LX_OFF(0) - X_OFF + 32 * w : pXw_, c1 = pSTw_, c2 = lane * 16, c3 = aB_, c4 = aA_, c5 = idx_;
            unsigned c6 = se, c7 = se, c8 = se, c9 = se, s89 = FLG_OFF + (blockIdx.x & 7) * 512, s46 = wave * 36864;
            unsigned s48 = ldsb + X_OFF, s70 = 0;
            unsigned s72 = ldsb + KQL_OFF + w * 192, s73 = ldsb + OL_OFF, s74 = ldsb + KQL_OFF + 784, s77 = ldsb + OL_OFF, s76 = ldsb + KQL_OFF + 768 + w * 4;
            KARGS
            asm volatile(
            "s_load_dwordx2 s[82:83], s[84:85], 301056\n\t"
            "s_mov_b32 m0, 159104\n\t"
            "s_mov_b32 s67, 32768\n\t"
            "s_mov_b32 s60, 0x22222222\n\t"
            "s_mov_b32 s61, 0x22222222\n\t"
            "s_mov_b32 s62, 0x44444444\n\t"
            "s_mov_b32 s63, 0x44444444\n\t"
            "s_mov_b32 s64, 0x88888888\n\t"
            "s_mov_b32 s65, 0x88888888\n\t"
            "s_waitcnt vmcnt(0) lgkmcnt(0)\n\t"
            "s_add_u32 s82, s82, 1\n\t"
            "s_cmp_lg_u32 s80, 16\n\t"
            "s_cbranch_scc1 .Lwa_p0%=\n\t"
            "s_mov_b64 exec, 0x3ffff\n\t"
            "buffer_load_dwordx4 v252, s[84:87], s89 offen sc1 lds\n\t"
            "s_mov_b64 exec, -1\n\t"
            ".Lwa_p0%=:\n\t"
            "s_call_b64 vcc, wk_conv\n\t"
            "v_min_u32 v146, 0x230, v252\n\t"
            "v_lshrrev_b32 v146, 1, v146\n\t"
            "v_add_u32 v146, 159104, v146\n\t"
            "s_mov_b32 s81, 0x10000\n\t"
            "v_mov_b32 v240, v200\n\t"
            "v_mov_b32 v241, v200\n\t"
            "v_mov_b32 v242, v200\n\t"
            "v_mov_b32 v243, v200\n\t"
            "v_mov_b32 v248, v204\n\t"
            "v_mov_b32 v249, v205\n\t"
            "v_mov_b32 v250, v206\n\t"
            "v_mov_b32 v251, v207\n\t"
            "v_mov_b32 v253, v209\n\t"
            "v_mov_b32 v254, v210\n\t"
            "v_mov_b32 v255, v211\n\t"
            ".Lwa_p1%=:\n\t"
            "ds_read_b64 v[144:145], v146\n\t"
            "s_waitcnt lgkmcnt(0)\n\t"
            "v_cmp_ne_u32 vcc, s82, v144\n\t"
            "v_cmp_ne_u32 s[60:61], s83, v145\n\t"
            "s_or_b64 vcc, vcc, s[60:61]\n\t"
            "s_cbranch_scc0 .Lwa_p3%=\n\t"
            "s_sub_u32 s81, s81, 1\n\t"
            "s_cbranch_scc1 .Lwa_p3%=\n\t"
            "s_cmp_lg_u32 s80, 16\n\t"
            "s_cbranch_scc1 .Lwa_p2%=\n\t"
            "s_mov_b64 exec, 0x3ffff\n\t"
            "buffer_load_dwordx4 v252, s[84:87], s89 offen sc1 lds\n\t"
            "s_mov_b64 exec, -1\n\t"
            "s_waitcnt vmcnt(24)\n\t"
            ".Lwa_p2%=:\n\t"
            "s_sleep 3\n\t"
            "s_branch .Lwa_p1%=\n\t"
            ".Lwa_p3%=:\n\t"
            "v_mov_b32 v252, v208\n\t"
            "v_mov_b32 v144, s82\n\t"
            "v_mov_b32 v145, s83\n\t"
            "v_mov_b32 v146, 159616\n\t"
            "ds_write_b64 v146, v[144:145]\n\t"
            "s_mov_b32 s60, 0xffff0000\n\t"
            "s_mov_b32 s61, 0x0\n\t"
            "s_mov_b32 s62, 0x0\n\t"
            "s_mov_b32 s63, 0xffff\n\t"
            "s_mov_b32 s64, 0x0\n\t"
            "s_mov_b32 s65, 0xffff0000\n\t"
            "s_mov_b32 s66, 0xffffffff\n\t"
            "s_mov_b32 s67, 0x1ffff\n\t"
            "buffer_load_dwordx4 v[140:143], v248, s[84:87], s88 offen sc1\n\t"
            "s_add_u32 s49, s46, 0\n\t"
            "buffer_load_dwordx4 v[0:3], v248, s[84:87], s49 offen sc1\n\t"
            "s_add_u32 s49, s46, 6144\n\t"
            "buffer_load_dwordx4 v[24:27], v248, s[84:87], s49 offen sc1\n\t"
            "s_add_u32 s49, s46, 12288\n\t"
            "buffer_load_dwordx4 v[48:51], v248, s[84:87], s49 offen sc1\n\t"
            "s_add_u32 s49, s46, 18432\n\t"
            "buffer_load_dwordx4 v[72:75], v248, s[84:87], s49 offen sc1\n\t"
            "s_add_u32 s49, s46, 24576\n\t"
            "buffer_load_dwordx4 v[96:99], v248, s[84:87], s49 offen sc1\n\t"
            "s_add_u32 s49, s46, 30720\n\t"
            "buffer_load_dwordx4 v[120:123], v248, s[84:87], s49 offen sc1\n\t"
            "s_add_u32 s49, s46, 1024\n\t"
            "buffer_load_dwordx4 v[4:7], v248, s[84:87], s49 offen sc1\n\t"
            "s_add_u32 s49, s46, 7168\n\t"
            "buffer_load_dwordx4 v[28:31], v248, s[84:87], s49 offen sc1\n\t"
            "s_add_u32 s49, s46, 13312\n\t"
            "buffer_load_dwordx4 v[52:55], v248, s[84:87], s49 offen sc1\n\t"
            "s_add_u32 s49, s46, 19456\n\t"
            "buffer_load_dwordx4 v[76:79], v248, s[84:87], s49 offen sc1\n\t"
            "s_add_u32 s49, s46, 25600\n\t"
            "buffer_load_dwordx4 v[100:103], v248, s[84:87], s49 offen sc1\n\t"
            "s_add_u32 s49, s46, 31744\n\t"
            "buffer_load_dwordx4 v[124:127], v248, s[84:87], s49 offen sc1\n\t"
            "s_add_u32 s49, s46, 2048\n\t"
            "buffer_load_dwordx4 v[8:11], v248, s[84:87], s49 offen sc1\n\t"
            "s_add_u32 s49, s46, 8192\n\t"
            "buffer_load_dwordx4 v[32:35], v248, s[84:87], s49 offen sc1\n\t"
            "s_add_u32 s49, s46, 14336\n\t"
            "buffer_load_dwordx4 v[56:59], v248, s[84:87], s49 offen sc1\n\t"
            "s_add_u32 s49, s46, 20480\n\t"
            "buffer_load_dwordx4 v[80:83], v248, s[84:87], s49 offen sc1\n\t"
            "s_add_u32 s49, s46, 26624\n\t"
            "buffer_load_dwordx4 v[104:107], v248, s[84:87], s49 offen sc1\n\t"
            "s_add_u32 s49, s46, 32768\n\t"
            "buffer_load_dwordx4 v[128:131], v248, s[84:87], s49 offen sc1\n\t"
            "s_add_u32 s49, s46, 3072\n\t"
            "buffer_load_dwordx4 v[12:15], v248, s[84:87], s49 offen sc1\n\t"
            "s_add_u32 s49, s46, 9216\n\t"
            "buffer_load_dwordx4 v[36:39], v248, s[84:87], s49 offen sc1\n\t"
            "s_add_u32 s49, s46, 15360\n\t"
            "buffer_load_dwordx4 v[60:63], v248, s[84:87], s49 offen sc1\n\t"
            "s_add_u32 s49, s46, 21504\n\t"
            "buffer_load_dwordx4 v[84:87], v248, s[84:87], s49 offen sc1\n\t"
            "s_add_u32 s49, s46, 27648\n\t"
            "buffer_load_dwordx4 v[108:111], v248, s[84:87], s49 offen sc1\n\t"
            "s_add_u32 s49, s46, 33792\n\t"
            "buffer_load_dwordx4 v[132:135], v248, s[84:87], s49 offen sc1\n\t"
            "s_add_u32 s49, s46, 4096\n\t"
            "buffer_load_dwordx4 v[16:19], v248, s[84:87], s49 offen sc1\n\t"
            "s_add_u32 s49, s46, 10240\n\t"
            "buffer_load_dwordx4 v[40:43], v248, s[84:87], s49 offen sc1\n\t"
            "s_add_u32 s49, s46, 16384\n\t"
            "buffer_load_dwordx4 v[64:67], v248, s[84:87], s49 offen sc1\n\t"
            "s_add_u32 s49, s46, 22528\n\t"
            "buffer_load_dwordx4 v[88:91], v248, s[84:87], s49 offen sc1\n\t"
            "s_add_u32 s49, s46, 28672\n\t"
            "buffer_load_dwordx4 v[112:115], v248, s[84:87], s49 offen sc1\n\t"
            "s_add_u32 s49, s46, 34816\n\t"
            "buffer_load_dwordx4 v[136:139], v248, s[84:87], s49 offen sc1\n\t"
            "s_add_u32 s49, s46, 5120\n\t"
            "buffer_load_dwordx4 v[20:23], v248, s[84:87], s49 offen sc1\n\t"
            "s_add_u32 s49, s46, 11264\n\t"
            "buffer_load_dwordx4 v[44:47], v248, s[84:87], s49 offen sc1\n\t"
            "s_add_u32 s49, s46, 17408\n\t"
            "buffer_load_dwordx4 v[68:71], v248, s[84:87], s49 offen sc1\n\t"
            "s_add_u32 s49, s46, 23552\n\t"
            "buffer_load_dwordx4 v[92:95], v248, s[84:87], s49 offen sc1\n\t"
            "s_add_u32 s49, s46, 29696\n\t"
            "buffer_load_dwordx4 v[116:119], v248, s[84:87], s49 offen sc1\n\t"
            "s_add_u32 s49, s46, 35840\n\t"
            "buffer_load_dwordx4 v[140:143], v248, s[84:87], s49 offen sc1\n\t"
            "s_waitcnt lgkmcnt(0)\n\t"
            "s_barrier\n\t"
            "s_setprio 2\n\t"
            "v_add_u32 v235, s71, v248\n\t"
            "v_add_u32 v232, s71, v244\n\t"
            "v_xor_b32 v233, 64, v232\n\t"
            "v_subrev_u32 v239, s75, v251\n\t"
            "v_mul_u32_u24 v239, 200, v239\n\t"
            "v_lshrrev_b32 v234, 3, v255\n\t"
            "v_add3_u32 v234, v234, v239, s72\n\t"
            "ds_read_b128 v[228:231], v235 offset:12288\n\t"
            "ds_read_b128 v[144:147], v232\n\t"
            "ds_read_b128 v[148:151], v233\n\t"
            "ds_read_b128 v[204:207], v232 offset:6144\n\t"
            "ds_read_b128 v[208:211], v233 offset:6144\n\t"
            "ds_read_b128 v[152:155], v232 offset:128\n\t"
            "ds_read_b128 v[156:159], v233 offset:128\n\t"
            "ds_read_b128 v[212:215], v232 offset:6272\n\t"
            "ds_read_b128 v[224:227], v233 offset:6272\n\t"
            "ds_read_b128 v[160:163], v232 offset:256\n\t"
            "ds_read_b128 v[164:167], v233 offset:256\n\t"
            "s_waitcnt vmcnt(24)\n\t"
            "s_waitcnt lgkmcnt(10)\n\t"
            "v_add_f32 v236, v228, v230\n\t"
            "v_add_f32 v237, v229, v231\n\t"
            "s_waitcnt lgkmcnt(8)\n\t"
            "v_mfma_f32_16x16x32_bf16 v[168:171], v[0:3], v[144:147], 0\n\t"
            "ds_read_b128 v[228:231], v235 offset:13312\n\t"
            "v_lshrrev_b32 v235, 4, v255\n\t"
            "v_mfma_f32_16x16x32_bf16 v[168:171], v[4:7], v[148:151], v[168:171]\n\t"
            "v_add3_u32 v235, v235, v239, s72\n\t"
            "v_mfma_f32_16x16x32_bf16 v[176:179], v[48:51], v[144:147], 0\n\t"
            "v_mfma_f32_16x16x32_bf16 v[176:179], v[52:55], v[148:151], v[176:179]\n\t"
            "v_mfma_f32_16x16x32_bf16 v[172:175], v[24:27], v[144:147], 0\n\t"
            "v_mfma_f32_16x16x32_bf16 v[172:175], v[28:31], v[148:151], v[172:175]\n\t"
            "v_mfma_f32_16x16x32_bf16 v[180:183], v[72:75], v[144:147], 0\n\t"
            "v_mfma_f32_16x16x32_bf16 v[180:183], v[76:79], v[148:151], v[180:183]\n\t"
            "s_waitcnt lgkmcnt(0)\n\t"
            "v_add_f32 v228, v228, v230\n\t"
            "v_add_f32 v236, v236, v228\n\t"
            "v_mfma_f32_16x16x32_bf16 v[184:187], v[96:99], v[144:147], 0\n\t"
            "v_add_f32 v229, v229, v231\n\t"
            "v_mul_f32 v236, 0x3baaaaab, v236\n\t"
            "v_mfma_f32_16x16x32_bf16 v[184:187], v[100:103], v[148:151], v[184:187]\n\t"
            "v_add_f32 v237, v237, v229\n\t"
            "v_mul_f32_e64 v236, v236, -v236\n\t"
            "v_mfma_f32_16x16x32_bf16 v[188:191], v[120:123], v[144:147], 0\n\t"
            "v_fmac_f32 v236, 0x3baaaaab, v237\n\t"
            "v_max_f32 v236, 0, v236\n\t"
            "v_mfma_f32_16x16x32_bf16 v[188:191], v[124:127], v[148:151], v[188:191]\n\t"
            "v_add_f32 v236, 0x3727c5ac, v236\n\t"
            "v_rsq_f32 v236, v236\n\t"
            "v_mfma_f32_16x16x32_bf16 v[192:195], v[0:3], v[204:207], 0\n\t"
            "v_mfma_f32_16x16x32_bf16 v[192:195], v[4:7], v[208:211], v[192:195]\n\t"
            "ds_write_b32 v249, v236\n\t"
            "v_mfma_f32_16x16x32_bf16 v[196:199], v[24:27], v[204:207], 0\n\t"
            "ds_read_b32 v236, v251\n\t"
            "ds_read_b32 v237, v251 offset:64\n\t"
            "v_mfma_f32_16x16x32_bf16 v[196:199], v[28:31], v[208:211], v[196:199]\n\t"
            "v_mfma_f32_16x16x32_bf16 v[200:203], v[48:51], v[204:207], 0\n\t"
            "v_mfma_f32_16x16x32_bf16 v[200:203], v[52:55], v[208:211], v[200:203]\n\t"
            "v_mfma_f32_16x16x32_bf16 v[216:219], v[72:75], v[204:207], 0\n\t"
            "v_mfma_f32_16x16x32_bf16 v[216:219], v[76:79], v[208:211], v[216:219]\n\t"
            "v_mfma_f32_16x16x32_bf16 v[220:223], v[96:99], v[204:207], 0\n\t"
            "v_mfma_f32_16x16x32_bf16 v[220:223], v[100:103], v[208:211], v[220:223]\n\t"
            "v_mfma_f32_16x16x32_bf16 v[228:231], v[120:123], v[204:207], 0\n\t"
            "ds_read_b128 v[204:207], v232 offset:6400\n\t"
            "v_add_u32 v232, s48, v244\n\t"
            "v_mfma_f32_16x16x32_bf16 v[228:231], v[124:127], v[208:211], v[228:231]\n\t"
            "ds_read_b128 v[208:211], v233 offset:6400\n\t"
            "s_waitcnt vmcnt(12)\n\t"
            "v_mfma_f32_16x16x32_bf16 v[168:171], v[8:11], v[152:155], v[168:171]\n\t"
            "v_xor_b32 v233, 64, v232\n\t"
            "v_mfma_f32_16x16x32_bf16 v[168:171], v[12:15], v[156:159], v[168:171]\n\t"
            "v_mfma_f32_16x16x32_bf16 v[176:179], v[56:59], v[152:155], v[176:179]\n\t"
            "v_mfma_f32_16x16x32_bf16 v[176:179], v[60:63], v[156:159], v[176:179]\n\t"
            "v_mfma_f32_16x16x32_bf16 v[172:175], v[32:35], v[152:155], v[172:175]\n\t"
            "v_mfma_f32_16x16x32_bf16 v[172:175], v[36:39], v[156:159], v[172:175]\n\t"
            "v_mfma_f32_16x16x32_bf16 v[180:183], v[80:83], v[152:155], v[180:183]\n\t"
            "v_mfma_f32_16x16x32_bf16 v[180:183], v[84:87], v[156:159], v[180:183]\n\t"
            "v_mfma_f32_16x16x32_bf16 v[184:187], v[104:107], v[152:155], v[184:187]\n\t"
            "v_mfma_f32_16x16x32_bf16 v[184:187], v[108:111], v[156:159], v[184:187]\n\t"
            "v_mfma_f32_16x16x32_bf16 v[188:191], v[128:131], v[152:155], v[188:191]\n\t"
            "v_mfma_f32_16x16x32_bf16 v[188:191], v[132:135], v[156:159], v[188:191]\n\t"
            "v_mfma_f32_16x16x32_bf16 v[192:195], v[8:11], v[212:215], v[192:195]\n\t"
            "v_mfma_f32_16x16x32_bf16 v[192:195], v[12:15], v[224:227], v[192:195]\n\t"
            "v_mfma_f32_16x16x32_bf16 v[196:199], v[32:35], v[212:215], v[196:199]\n\t"
            "v_mfma_f32_16x16x32_bf16 v[196:199], v[36:39], v[224:227], v[196:199]\n\t"
            "v_mfma_f32_16x16x32_bf16 v[200:203], v[56:59], v[212:215], v[200:203]\n\t"
            "v_mfma_f32_16x16x32_bf16 v[200:203], v[60:63], v[224:227], v[200:203]\n\t"
            "v_mfma_f32_16x16x32_bf16 v[216:219], v[80:83], v[212:215], v[216:219]\n\t"
            "v_mfma_f32_16x16x32_bf16 v[216:219], v[84:87], v[224:227], v[216:219]\n\t"
            "v_mfma_f32_16x16x32_bf16 v[220:223], v[104:107], v[212:215], v[220:223]\n\t"
            "v_mfma_f32_16x16x32_bf16 v[220:223], v[108:111], v[224:227], v[220:223]\n\t"
            "v_mfma_f32_16x16x32_bf16 v[228:231], v[128:131], v[212:215], v[228:231]\n\t"
            "ds_read_b128 v[212:215], v232 offset:256\n\t"
            "v_mfma_f32_16x16x32_bf16 v[228:231], v[132:135], v[224:227], v[228:231]\n\t"
            "ds_read_b128 v[224:227], v233 offset:256\n\t"
            "s_waitcnt vmcnt(0)\n\t"
            "v_mfma_f32_16x16x32_bf16 v[168:171], v[16:19], v[160:163], v[168:171]\n\t"
            "v_mfma_f32_16x16x32_bf16 v[168:171], v[20:23], v[164:167], v[168:171]\n\t"
            "v_mfma_f32_16x16x32_bf16 v[176:179], v[64:67], v[160:163], v[176:179]\n\t"
            "v_mfma_f32_16x16x32_bf16 v[176:179], v[68:71], v[164:167], v[176:179]\n\t"
            "v_mfma_f32_16x16x32_bf16 v[180:183], v[88:91], v[160:163], v[180:183]\n\t"
            "v_mfma_f32_16x16x32_bf16 v[180:183], v[92:95], v[164:167], v[180:183]\n\t"
            "v_mfma_f32_16x16x32_bf16 v[172:175], v[40:43], v[160:163], v[172:175]\n\t"
            "v_mfma_f32_16x16x32_bf16 v[172:175], v[44:47], v[164:167], v[172:175]\n\t"
            "s_waitcnt lgkmcnt(5)\n\t"
            "s_nop 0\n\t"
            "v_mul_f32 v168, v236, v168\n\t"
            "v_mfma_f32_16x16x32_bf16 v[184:187], v[112:115], v[160:163], v[184:187]\n\t"
            "v_mul_f32 v169, v236, v169\n\t"
            "v_mul_f32 v170, v236, v170\n\t"
            "v_mfma_f32_16x16x32_bf16 v[184:187], v[116:119], v[164:167], v[184:187]\n\t"
            "v_mul_f32 v176, v236, v176\n\t"
            "v_mul_f32 v177, v236, v177\n\t"
            "v_mfma_f32_16x16x32_bf16 v[188:191], v[136:139], v[160:163], v[188:191]\n\t"
            "v_mul_f32 v171, v236, v171\n\t"
            "v_mul_f32 v178, v236, v178\n\t"
            "v_mfma_f32_16x16x32_bf16 v[188:191], v[140:143], v[164:167], v[188:191]\n\t"
            "v_mul_f32 v179, v236, v179\n\t"
            "v_cvt_pk_bf16_f32 v168, v168, v169\n\t"
            "s_waitcnt lgkmcnt(2)\n\t"
            "v_mfma_f32_16x16x32_bf16 v[192:195], v[16:19], v[204:207], v[192:195]\n\t"
            "v_cvt_pk_bf16_f32 v176, v176, v177\n\t"
            "v_cvt_pk_bf16_f32 v180, v180, v181\n\t"
            "v_mfma_f32_16x16x32_bf16 v[192:195], v[20:23], v[208:211], v[192:195]\n\t"
            "v_mul_f32 v172, v236, v172\n\t"
            "v_mul_f32 v173, v236, v173\n\t"
            "v_mfma_f32_16x16x32_bf16 v[196:199], v[40:43], v[204:207], v[196:199]\n\t"
            "v_mul_f32 v174, v236, v174\n\t"
            "v_mul_f32 v175, v236, v175\n\t"
            "v_mfma_f32_16x16x32_bf16 v[196:199], v[44:47], v[208:211], v[196:199]\n\t"
            "v_cvt_pk_bf16_f32 v169, v170, v171\n\t"
            "v_cvt_pk_bf16_f32 v177, v178, v179\n\t"
            "v_mfma_f32_16x16x32_bf16 v[200:203], v[64:67], v[204:207], v[200:203]\n\t"
            "v_cvt_pk_bf16_f32 v181, v182, v183\n\t"
            "v_cvt_pk_bf16_f32 v170, v172, v173\n\t"
            "v_mfma_f32_16x16x32_bf16 v[200:203], v[68:71], v[208:211], v[200:203]\n\t"
            "v_cvt_pk_bf16_f32 v171, v174, v175\n\t"
            "v_cvt_pk_bf16_f32 v182, v184, v185\n\t"
            "v_mfma_f32_16x16x32_bf16 v[216:219], v[88:91], v[204:207], v[216:219]\n\t"
            "v_cvt_pk_bf16_f32 v183, v186, v187\n\t"
            "v_cvt_pk_bf16_f32 v178, v188, v189\n\t"
            "v_mfma_f32_16x16x32_bf16 v[216:219], v[92:95], v[208:211], v[216:219]\n\t"
            "v_cvt_pk_bf16_f32 v179, v190, v191\n\t"
            "v_mul_f32 v236, 0x3e553b94, v236\n\t"
            "v_mfma_f32_16x16x32_bf16 v[220:223], v[112:115], v[204:207], v[220:223]\n\t"
            "v_add_u32 v184, s76, v239\n\t"
            "v_add_u32 v185, s74, v239\n\t"
            "v_mfma_f32_16x16x32_bf16 v[220:223], v[116:119], v[208:211], v[220:223]\n\t"
            "s_mov_b64 exec, -1\n\t"
            "ds_write_b128 v234, v[168:171]\n\t"
            "ds_write_b128 v234, v[180:183] offset:64\n\t"
            "ds_write_b64 v235, v[176:177] offset:128\n\t"
            "ds_write_b64 v235, v[178:179] offset:160\n\t"
            "s_mov_b64 exec, -1\n\t"
            "v_mfma_f32_16x16x16_bf16 v[172:175], v[176:177], v[178:179], 0\n\t"
            "v_mul_f32 v192, v237, v192\n\t"
            "v_mul_f32 v193, v237, v193\n\t"
            "v_mul_f32 v200, v237, v200\n\t"
            "v_mfma_f32_16x16x32_bf16 v[228:231], v[136:139], v[204:207], v[228:231]\n\t"
            "ds_read_b128 v[204:207], v232\n\t"
            "v_mul_f32 v201, v237, v201\n\t"
            "v_mfma_f32_16x16x32_bf16 v[228:231], v[140:143], v[208:211], v[228:231]\n\t"
            "ds_read_b128 v[208:211], v233\n\t"
            "v_mul_f32 v194, v237, v194\n\t"
            "v_mfma_f32_16x16x32_bf16 v[172:175], v[168:171], v[180:183], v[172:175]\n\t"
            "v_mul_f32 v195, v237, v195\n\t"
            "v_mul_f32 v202, v237, v202\n\t"
            "v_mul_f32 v203, v237, v203\n\t"
            "v_cvt_pk_bf16_f32 v192, v192, v193\n\t"
            "v_cvt_pk_bf16_f32 v200, v200, v201\n\t"
            "v_cvt_pk_bf16_f32 v216, v216, v217\n\t"
            "v_mul_f32 v196, v237, v196\n\t"
            "v_mul_f32 v197, v237, v197\n\t"
            "v_mul_f32 v198, v237, v198\n\t"
            "v_mul_f32 v199, v237, v199\n\t"
            "v_cvt_pk_bf16_f32 v193, v194, v195\n\t"
            "v_cvt_pk_bf16_f32 v201, v202, v203\n\t"
            "v_cvt_pk_bf16_f32 v217, v218, v219\n\t"
            "v_cvt_pk_bf16_f32 v194, v196, v197\n\t"
            "v_cvt_pk_bf16_f32 v195, v198, v199\n\t"
            "v_cvt_pk_bf16_f32 v218, v220, v221\n\t"
            "v_cvt_pk_bf16_f32 v219, v222, v223\n\t"
            "v_cvt_pk_bf16_f32 v202, v228, v229\n\t"
            "v_cvt_pk_bf16_f32 v203, v230, v231\n\t"
            "v_mul_f32 v237, 0x3e553b94, v237\n\t"
            "v_add_u32 v220, s76, v239\n\t"
            "v_mfma_f32_16x16x16_bf16 v[196:199], v[200:201], v[202:203], 0\n\t"
            "s_and_b32 exec_lo, s56, 0x11111111\n\t"
            "s_and_b32 exec_hi, s57, 0x11111111\n\t"
            "ds_write_b32 v184, v172 offset:0\n\t"
            "s_and_b32 exec_lo, s56, 0x22222222\n\t"
            "s_and_b32 exec_hi, s57, 0x22222222\n\t"
            "ds_write_b32 v184, v173 offset:0\n\t"
            "s_and_b32 exec_lo, s56, 0x44444444\n\t"
            "s_and_b32 exec_hi, s57, 0x44444444\n\t"
            "ds_write_b32 v184, v174 offset:0\n\t"
            "s_and_b32 exec_lo, s56, 0x88888888\n\t"
            "s_and_b32 exec_hi, s57, 0x88888888\n\t"
            "ds_write_b32 v184, v175 offset:0\n\t"
            "s_mov_b64 exec, s[56:57]\n\t"
            "ds_write_b32 v185, v236 offset:0\n\t"
            "s_mov_b64 exec, -1\n\t"
            "s_waitcnt lgkmcnt(5)\n\t"
            "v_mfma_f32_16x16x32_bf16 v[144:147], v[0:3], v[204:207], 0\n\t"
            "v_add_u32 v221, s74, v239\n\t"
            "v_mfma_f32_16x16x32_bf16 v[144:147], v[4:7], v[208:211], v[144:147]\n\t"
            "s_mov_b64 exec, s[54:55]\n\t"
            "ds_write_b128 v234, v[192:195] offset:12800\n\t"
            "ds_write_b128 v234, v[216:219] offset:12864\n\t"
            "ds_write_b64 v235, v[200:201] offset:12928\n\t"
            "ds_write_b64 v235, v[202:203] offset:12960\n\t"
            "s_mov_b64 exec, -1\n\t"
            "v_mfma_f32_16x16x32_bf16 v[148:151], v[28:31], v[208:211], 0\n\t"
            "v_add_u32 v235, s48, v248\n\t"
            "v_mfma_f32_16x16x32_bf16 v[148:151], v[24:27], v[204:207], v[148:151]\n\t"
            "ds_read_b128 v[228:231], v235 offset:18816\n\t"
            "v_mfma_f32_16x16x32_bf16 v[152:155], v[48:51], v[204:207], 0\n\t"
            "v_mfma_f32_16x16x32_bf16 v[152:155], v[52:55], v[208:211], v[152:155]\n\t"
            "v_mfma_f32_16x16x32_bf16 v[156:159], v[76:79], v[208:211], 0\n\t"
            "v_mfma_f32_16x16x32_bf16 v[156:159], v[72:75], v[204:207], v[156:159]\n\t"
            "v_mfma_f32_16x16x32_bf16 v[160:163], v[96:99], v[204:207], 0\n\t"
            "v_mfma_f32_16x16x32_bf16 v[160:163], v[100:103], v[208:211], v[160:163]\n\t"
            "v_mfma_f32_16x16x32_bf16 v[164:167], v[124:127], v[208:211], 0\n\t"
            "ds_read_b128 v[208:211], v233 offset:128\n\t"
            "s_waitcnt lgkmcnt(1)\n\t"
            "v_add_f32 v236, v228, v230\n\t"
            "v_mfma_f32_16x16x32_bf16 v[164:167], v[120:123], v[204:207], v[164:167]\n\t"
            "ds_read_b128 v[204:207], v232 offset:128\n\t"
            "v_mfma_f32_16x16x32_bf16 v[196:199], v[192:195], v[216:219], v[196:199]\n\t"
            "s_nop 7\n\t"
            "s_and_b32 exec_lo, s78, 0x11111111\n\t"
            "s_and_b32 exec_hi, s79, 0x11111111\n\t"
            "ds_write_b32 v220, v196 offset:12800\n\t"
            "s_and_b32 exec_lo, s78, 0x22222222\n\t"
            "s_and_b32 exec_hi, s79, 0x22222222\n\t"
            "ds_write_b32 v220, v197 offset:12800\n\t"
            "s_and_b32 exec_lo, s78, 0x44444444\n\t"
            "s_and_b32 exec_hi, s79, 0x44444444\n\t"
            "ds_write_b32 v220, v198 offset:12800\n\t"
            "s_and_b32 exec_lo, s78, 0x88888888\n\t"
            "s_and_b32 exec_hi, s79, 0x88888888\n\t"
            "ds_write_b32 v220, v199 offset:12800\n\t"
            "s_mov_b64 exec, s[78:79]\n\t"
            "ds_write_b32 v221, v237 offset:12800\n\t"
            "s_mov_b64 exec, -1\n\t"
            "s_waitcnt lgkmcnt(5)\n\t"
            "v_mfma_f32_16x16x32_bf16 v[144:147], v[8:11], v[204:207], v[144:147]\n\t"
            "v_add_f32 v237, v229, v231\n\t"
            "v_mfma_f32_16x16x32_bf16 v[144:147], v[12:15], v[208:211], v[144:147]\n\t"
            "ds_read_b128 v[228:231], v235 offset:19840\n\t"
            "v_mfma_f32_16x16x32_bf16 v[148:151], v[36:39], v[208:211], v[148:151]\n\t"
            "v_mfma_f32_16x16x32_bf16 v[148:151], v[32:35], v[204:207], v[148:151]\n\t"
            "v_mfma_f32_16x16x32_bf16 v[152:155], v[56:59], v[204:207], v[152:155]\n\t"
            "v_mfma_f32_16x16x32_bf16 v[152:155], v[60:63], v[208:211], v[152:155]\n\t"
            "v_mfma_f32_16x16x32_bf16 v[156:159], v[84:87], v[208:211], v[156:159]\n\t"
            "v_mfma_f32_16x16x32_bf16 v[156:159], v[80:83], v[204:207], v[156:159]\n\t"
            "v_mfma_f32_16x16x32_bf16 v[160:163], v[104:107], v[204:207], v[160:163]\n\t"
            "s_waitcnt lgkmcnt(0)\n\t"
            "v_add_f32 v228, v228, v230\n\t"
            "v_add_f32 v236, v236, v228\n\t"
            "v_mfma_f32_16x16x32_bf16 v[160:163], v[108:111], v[208:211], v[160:163]\n\t"
            "v_add_f32 v229, v229, v231\n\t"
            "v_mul_f32 v236, 0x3baaaaab, v236\n\t"
            "v_mfma_f32_16x16x32_bf16 v[164:167], v[132:135], v[208:211], v[164:167]\n\t"
            "ds_read_b128 v[208:211], v233 offset:6144\n\t"
            "v_add_f32 v237, v237, v229\n\t"
            "v_mfma_f32_16x16x32_bf16 v[164:167], v[128:131], v[204:207], v[164:167]\n\t"
            "ds_read_b128 v[204:207], v232 offset:6144\n\t"
            "v_mul_f32_e64 v236, v236, -v236\n\t"
            "v_mfma_f32_16x16x32_bf16 v[144:147], v[16:19], v[212:215], v[144:147]\n\t"
            "v_fmac_f32 v236, 0x3baaaaab, v237\n\t"
            "v_max_f32 v236, 0, v236\n\t"
            "v_mfma_f32_16x16x32_bf16 v[144:147], v[20:23], v[224:227], v[144:147]\n\t"
            "v_add_f32 v236, 0x3727c5ac, v236\n\t"
            "v_rsq_f32 v236, v236\n\t"
            "v_mfma_f32_16x16x32_bf16 v[148:151], v[44:47], v[224:227], v[148:151]\n\t"
            "v_mfma_f32_16x16x32_bf16 v[148:151], v[40:43], v[212:215], v[148:151]\n\t"
            "ds_write_b32 v249, v236\n\t"
            "v_mfma_f32_16x16x32_bf16 v[152:155], v[64:67], v[212:215], v[152:155]\n\t"
            "ds_read_b32 v236, v251\n\t"
            "ds_read_b32 v237, v251 offset:64\n\t"
            "v_mfma_f32_16x16x32_bf16 v[152:155], v[68:71], v[224:227], v[152:155]\n\t"
            "ds_read_b32 v238, v251 offset:128\n\t"
            "v_mfma_f32_16x16x32_bf16 v[156:159], v[92:95], v[224:227], v[156:159]\n\t"
            "v_mfma_f32_16x16x32_bf16 v[156:159], v[88:91], v[212:215], v[156:159]\n\t"
            "v_mfma_f32_16x16x32_bf16 v[160:163], v[112:115], v[212:215], v[160:163]\n\t"
            "v_mfma_f32_16x16x32_bf16 v[160:163], v[116:119], v[224:227], v[160:163]\n\t"
            "v_mfma_f32_16x16x32_bf16 v[164:167], v[140:143], v[224:227], v[164:167]\n\t"
            "ds_read_b128 v[224:227], v233 offset:6272\n\t"
            "v_mfma_f32_16x16x32_bf16 v[164:167], v[136:139], v[212:215], v[164:167]\n\t"
            "ds_read_b128 v[212:215], v232 offset:6272\n\t"
            "s_waitcnt lgkmcnt(4)\n\t"
            "v_mul_f32 v144, v236, v144\n\t"
            "v_mfma_f32_16x16x32_bf16 v[168:171], v[0:3], v[204:207], 0\n\t"
            "v_mul_f32 v145, v236, v145\n\t"
            "v_mul_f32 v152, v236, v152\n\t"
            "v_mfma_f32_16x16x32_bf16 v[168:171], v[4:7], v[208:211], v[168:171]\n\t"
            "v_mul_f32 v153, v236, v153\n\t"
            "v_mul_f32 v146, v236, v146\n\t"
            "v_mfma_f32_16x16x32_bf16 v[172:175], v[28:31], v[208:211], 0\n\t"
            "v_mul_f32 v147, v236, v147\n\t"
            "v_mul_f32 v154, v236, v154\n\t"
            "v_mfma_f32_16x16x32_bf16 v[172:175], v[24:27], v[204:207], v[172:175]\n\t"
            "v_mul_f32 v155, v236, v155\n\t"
            "v_cvt_pk_bf16_f32 v144, v144, v145\n\t"
            "v_mfma_f32_16x16x32_bf16 v[176:179], v[48:51], v[204:207], 0\n\t"
            "v_cvt_pk_bf16_f32 v152, v152, v153\n\t"
            "v_cvt_pk_bf16_f32 v156, v156, v157\n\t"
            "v_mfma_f32_16x16x32_bf16 v[176:179], v[52:55], v[208:211], v[176:179]\n\t"
            "v_mul_f32 v148, v236, v148\n\t"
            "v_mul_f32 v149, v236, v149\n\t"
            "v_mfma_f32_16x16x32_bf16 v[180:183], v[76:79], v[208:211], 0\n\t"
            "v_mul_f32 v150, v236, v150\n\t"
            "v_mul_f32 v151, v236, v151\n\t"
            "v_mfma_f32_16x16x32_bf16 v[180:183], v[72:75], v[204:207], v[180:183]\n\t"
            "v_cvt_pk_bf16_f32 v145, v146, v147\n\t"
            "v_cvt_pk_bf16_f32 v153, v154, v155\n\t"
            "v_mfma_f32_16x16x32_bf16 v[184:187], v[96:99], v[204:207], 0\n\t"
            "v_cvt_pk_bf16_f32 v157, v158, v159\n\t"
            "v_cvt_pk_bf16_f32 v146, v148, v149\n\t"
            "v_mfma_f32_16x16x32_bf16 v[184:187], v[100:103], v[208:211], v[184:187]\n\t"
            "v_cvt_pk_bf16_f32 v147, v150, v151\n\t"
            "v_cvt_pk_bf16_f32 v158, v160, v161\n\t"
            "v_mfma_f32_16x16x32_bf16 v[188:191], v[124:127], v[208:211], 0\n\t"
            "ds_read_b128 v[208:211], v233 offset:6400\n\t"
            "v_cvt_pk_bf16_f32 v159, v162, v163\n\t"
            "v_mfma_f32_16x16x32_bf16 v[188:191], v[120:123], v[204:207], v[188:191]\n\t"
            "ds_read_b128 v[204:207], v232 offset:6400\n\t"
            "v_cvt_pk_bf16_f32 v154, v164, v165\n\t"
            "s_waitcnt lgkmcnt(2)\n\t"
            "v_mfma_f32_16x16x32_bf16 v[168:171], v[8:11], v[212:215], v[168:171]\n\t"
            "v_cvt_pk_bf16_f32 v155, v166, v167\n\t"
            "v_mul_f32 v236, 0x3e553b94, v236\n\t"
            "v_mfma_f32_16x16x32_bf16 v[168:171], v[12:15], v[224:227], v[168:171]\n\t"
            "v_mfma_f32_16x16x32_bf16 v[172:175], v[36:39], v[224:227], v[172:175]\n\t"
            "v_mfma_f32_16x16x32_bf16 v[172:175], v[32:35], v[212:215], v[172:175]\n\t"
            "v_mfma_f32_16x16x32_bf16 v[176:179], v[56:59], v[212:215], v[176:179]\n\t"
            "v_mfma_f32_16x16x32_bf16 v[176:179], v[60:63], v[224:227], v[176:179]\n\t"
            "v_mfma_f32_16x16x32_bf16 v[180:183], v[84:87], v[224:227], v[180:183]\n\t"
            "v_mfma_f32_16x16x32_bf16 v[180:183], v[80:83], v[212:215], v[180:183]\n\t"
            "v_mfma_f32_16x16x32_bf16 v[184:187], v[104:107], v[212:215], v[184:187]\n\t"
            "v_mfma_f32_16x16x32_bf16 v[184:187], v[108:111], v[224:227], v[184:187]\n\t"
            "v_mfma_f32_16x16x32_bf16 v[188:191], v[132:135], v[224:227], v[188:191]\n\t"
            "ds_read_b128 v[224:227], v233 offset:12288\n\t"
            "v_mfma_f32_16x16x32_bf16 v[188:191], v[128:131], v[212:215], v[188:191]\n\t"
            "ds_read_b128 v[212:215], v232 offset:12288\n\t"
            "s_waitcnt lgkmcnt(2)\n\t"
            "v_mfma_f32_16x16x32_bf16 v[168:171], v[16:19], v[204:207], v[168:171]\n\t"
            "v_mfma_f32_16x16x32_bf16 v[168:171], v[20:23], v[208:211], v[168:171]\n\t"
            "v_mfma_f32_16x16x32_bf16 v[172:175], v[44:47], v[208:211], v[172:175]\n\t"
            "v_mfma_f32_16x16x32_bf16 v[172:175], v[40:43], v[204:207], v[172:175]\n\t"
            "v_mfma_f32_16x16x32_bf16 v[176:179], v[64:67], v[204:207], v[176:179]\n\t"
            "v_mfma_f32_16x16x32_bf16 v[176:179], v[68:71], v[208:211], v[176:179]\n\t"
            "v_mfma_f32_16x16x32_bf16 v[180:183], v[92:95], v[208:211], v[180:183]\n\t"
            "v_mfma_f32_16x16x32_bf16 v[180:183], v[88:91], v[204:207], v[180:183]\n\t"
            "v_mfma_f32_16x16x32_bf16 v[184:187], v[112:115], v[204:207], v[184:187]\n\t"
            "v_mfma_f32_16x16x32_bf16 v[184:187], v[116:119], v[208:211], v[184:187]\n\t"
            "v_mfma_f32_16x16x32_bf16 v[188:191], v[140:143], v[208:211], v[188:191]\n\t"
            "ds_read_b128 v[208:211], v233 offset:12416\n\t"
            "v_mfma_f32_16x16x32_bf16 v[188:191], v[136:139], v[204:207], v[188:191]\n\t"
            "ds_read_b128 v[204:207], v232 offset:12416\n\t"
            "s_waitcnt lgkmcnt(0)\n\t"
            "v_subrev_u32 v249, s75, v251\n\t"
            "v_add_u32 v249, 162816, v249\n\t"
            "s_mov_b32 s90, 2\n\t"
            "s_mov_b32 s81, 0\n\t"
            "s_mov_b32 s91, 0\n\t"
            "s_nop 7\n\t"
            ".p2align 6\n\t"
            "s_nop 0\n\t"
            "s_nop 0\n\t"
            "s_nop 0\n\t"
            ".Lwa_loop%=:\n\t"
            "s_setprio 2\n\t"
            "s_waitcnt lgkmcnt(2)\n\t"
            "v_mfma_f32_16x16x32_bf16 v[192:195], v[0:3], v[212:215], 0\n\t"
            "s_bfe_u32 s49, s70, 0x30001\n\t"
            "s_lshl_b32 s49, s49, 4\n\t"
            "s_or_b32 s73, s77, s49\n\t"
            "v_mfma_f32_16x16x32_bf16 v[192:195], v[4:7], v[224:227], v[192:195]\n\t"
            "v_mad_u32_u24 v220, v246, s72, v253\n\t"
            "v_mad_u32_u24 v216, v246, s72, v252\n\t"
            "v_mfma_f32_16x16x32_bf16 v[148:151], v[28:31], v[224:227], 0\n\t"
            "ds_read_b64 v[222:223], v220 offset:128\n\t"
            "v_mov_b32_dpp v221, v220 quad_perm:[1,0,2,3] row_mask:0xf bank_mask:0xf\n\t"
            "v_mfma_f32_16x16x32_bf16 v[148:151], v[24:27], v[212:215], v[148:151]\n\t"
            "ds_read_b64 v[220:221], v221 offset:160\n\t"
            "v_mov_b32_dpp v217, v216 quad_perm:[1,0,2,3] row_mask:0xf bank_mask:0xf\n\t"
            "v_mfma_f32_16x16x32_bf16 v[196:199], v[48:51], v[212:215], 0\n\t"
            "v_mul_f32 v176, v237, v176\n\t"
            "v_mul_f32 v177, v237, v177\n\t"
            "v_mfma_f32_16x16x32_bf16 v[196:199], v[52:55], v[224:227], v[196:199]\n\t"
            "v_mul_f32 v178, v237, v178\n\t"
            "v_mul_f32 v179, v237, v179\n\t"
            "v_mfma_f32_16x16x32_bf16 v[200:203], v[76:79], v[224:227], 0\n\t"
            "v_cvt_pk_bf16_f32 v176, v176, v177\n\t"
            "v_mul_f32 v168, v237, v168\n\t"
            "v_mfma_f32_16x16x32_bf16 v[200:203], v[72:75], v[212:215], v[200:203]\n\t"
            "v_mul_f32 v169, v237, v169\n\t"
            "v_cvt_pk_bf16_f32 v177, v178, v179\n\t"
            "v_mfma_f32_16x16x32_bf16 v[160:163], v[96:99], v[212:215], 0\n\t"
            "v_mul_f32 v170, v237, v170\n\t"
            "v_mul_f32 v171, v237, v171\n\t"
            "v_mfma_f32_16x16x32_bf16 v[160:163], v[100:103], v[224:227], v[160:163]\n\t"
            "v_cvt_pk_bf16_f32 v168, v168, v169\n\t"
            "v_cvt_pk_bf16_f32 v180, v180, v181\n\t"
            "v_mfma_f32_16x16x32_bf16 v[164:167], v[124:127], v[224:227], 0\n\t"
            "ds_read_b128 v[224:227], v233 offset:12544\n\t"
            "v_cvt_pk_bf16_f32 v178, v188, v189\n\t"
            "v_mfma_f32_16x16x32_bf16 v[164:167], v[120:123], v[212:215], v[164:167]\n\t"
            "ds_read_b128 v[212:215], v232 offset:12544\n\t"
            "v_cvt_pk_bf16_f32 v179, v190, v191\n\t"
            "s_waitcnt lgkmcnt(4)\n\t"
            "v_mfma_f32_16x16x32_bf16 v[192:195], v[8:11], v[204:207], v[192:195]\n\t"
            "v_mul_f32 v172, v237, v172\n\t"
            "v_mul_f32 v173, v237, v173\n\t"
            "v_mfma_f32_16x16x32_bf16 v[192:195], v[12:15], v[208:211], v[192:195]\n\t"
            "v_mul_f32 v174, v237, v174\n\t"
            "v_mul_f32 v175, v237, v175\n\t"
            "v_mfma_f32_16x16x32_bf16 v[196:199], v[56:59], v[204:207], v[196:199]\n\t"
            "v_cvt_pk_bf16_f32 v169, v170, v171\n\t"
            "v_cvt_pk_bf16_f32 v181, v182, v183\n\t"
            "v_mfma_f32_16x16x32_bf16 v[196:199], v[60:63], v[208:211], v[196:199]\n\t"
            "v_cvt_pk_bf16_f32 v170, v172, v173\n\t"
            "v_cvt_pk_bf16_f32 v171, v174, v175\n\t"
            "v_mfma_f32_16x16x32_bf16 v[160:163], v[104:107], v[204:207], v[160:163]\n\t"
            "v_cvt_pk_bf16_f32 v182, v184, v185\n\t"
            "v_cvt_pk_bf16_f32 v183, v186, v187\n\t"
            "v_mfma_f32_16x16x32_bf16 v[160:163], v[108:111], v[208:211], v[160:163]\n\t"
            "v_mov_b32 v230, s76\n\t"
            "v_mov_b32 v239, s74\n\t"
            "v_mfma_f32_16x16x32_bf16 v[148:151], v[36:39], v[208:211], v[148:151]\n\t"
            "s_add_u32 s70, s70, 1\n\t"
            "s_add_u32 s72, s72, 800\n\t"
            "s_add_u32 s74, s74, 800\n\t"
            "s_add_u32 s76, s76, 800\n\t"
            "v_mfma_f32_16x16x32_bf16 v[148:151], v[32:35], v[204:207], v[148:151]\n\t"
            "s_add_u32 s77, s77, 384\n\t"
            "s_xor_b32 s48, s48, s47\n\t"
            "ds_read_b32 v230, v230\n\t"
            "v_mfma_f32_16x16x32_bf16 v[200:203], v[84:87], v[208:211], v[200:203]\n\t"
            "ds_read_b32 v239, v239\n\t"
            "v_mul_f32 v237, 0x3e553b94, v237\n\t"
            "v_mfma_f32_16x16x32_bf16 v[200:203], v[80:83], v[204:207], v[200:203]\n\t"
            "v_mfma_f32_16x16x32_bf16 v[164:167], v[132:135], v[208:211], v[164:167]\n\t"
            "v_mfma_f32_16x16x32_bf16 v[164:167], v[128:131], v[204:207], v[164:167]\n\t"
            "s_waitcnt lgkmcnt(2)\n\t"
            "v_mfma_f32_16x16x32_bf16 v[192:195], v[16:19], v[212:215], v[192:195]\n\t"
            "v_mfma_f32_16x16x32_bf16 v[192:195], v[20:23], v[224:227], v[192:195]\n\t"
            "v_mfma_f32_16x16x32_bf16 v[196:199], v[64:67], v[212:215], v[196:199]\n\t"
            "v_mfma_f32_16x16x32_bf16 v[196:199], v[68:71], v[224:227], v[196:199]\n\t"
            "v_mfma_f32_16x16x32_bf16 v[160:163], v[112:115], v[212:215], v[160:163]\n\t"
            "v_mfma_f32_16x16x32_bf16 v[160:163], v[116:119], v[224:227], v[160:163]\n\t"
            "v_mfma_f32_16x16x32_bf16 v[148:151], v[44:47], v[224:227], v[148:151]\n\t"
            "v_mfma_f32_16x16x32_bf16 v[148:151], v[40:43], v[212:215], v[148:151]\n\t"
            "s_nop 1\n\t"
            "v_mul_f32 v192, v238, v192\n\t"
            "v_mfma_f32_16x16x32_bf16 v[200:203], v[92:95], v[224:227], v[200:203]\n\t"
            "v_mul_f32 v193, v238, v193\n\t"
            "v_mul_f32 v194, v238, v194\n\t"
            "v_mfma_f32_16x16x32_bf16 v[200:203], v[88:91], v[212:215], v[200:203]\n\t"
            "v_mul_f32 v196, v238, v196\n\t"
            "v_mul_f32 v197, v238, v197\n\t"
            "v_mfma_f32_16x16x32_bf16 v[164:167], v[140:143], v[224:227], v[164:167]\n\t"
            "ds_read_b128 v[224:227], v217 offset:64\n\t"
            "ds_read_b128 v[216:219], v216\n\t"
            "v_mfma_f32_16x16x32_bf16 v[164:167], v[136:139], v[212:215], v[164:167]\n\t"
            "v_mul_f32 v198, v238, v198\n\t"
            "v_mul_f32 v199, v238, v199\n\t"
            "v_mfma_f32_16x16x32_bf16 v[172:175], v[220:223], v[152:155], 0\n\t"
            "s_setprio 0\n\t"
            "v_cvt_pk_bf16_f32 v196, v196, v197\n\t"
            "v_mul_f32 v195, v238, v195\n\t"
            "s_setprio 3\n\t"
            "v_mfma_f32_16x16x32_bf16 v[212:215], v[220:223], v[176:179], 0\n\t"
            "v_cvt_pk_bf16_f32 v192, v192, v193\n\t"
            "v_cvt_pk_bf16_f32 v197, v198, v199\n\t"
            "v_cvt_pk_bf16_f32 v200, v200, v201\n\t"
            "v_mul_f32 v148, v238, v148\n\t"
            "v_mfma_f32_16x16x16_bf16 v[184:187], v[152:153], v[178:179], 0\n\t"
            "v_mul_f32 v149, v238, v149\n\t"
            "v_mul_f32 v150, v238, v150\n\t"
            "v_mul_f32 v151, v238, v151\n\t"
            "v_mfma_f32_16x16x16_bf16 v[204:207], v[176:177], v[178:179], 0\n\t"
            "v_cvt_pk_bf16_f32 v193, v194, v195\n\t"
            "v_cvt_pk_bf16_f32 v201, v202, v203\n\t"
            "v_cvt_pk_bf16_f32 v194, v148, v149\n\t"
            "v_mfma_f32_16x16x16_bf16 v[188:191], v[196:197], v[178:179], 0\n\t"
            "v_cvt_pk_bf16_f32 v195, v150, v151\n\t"
            "v_cvt_pk_bf16_f32 v202, v160, v161\n\t"
            "v_cvt_pk_bf16_f32 v203, v162, v163\n\t"
            "s_waitcnt lgkmcnt(0)\n\t"
            "v_mfma_f32_16x16x32_bf16 v[172:175], v[216:219], v[156:159], v[172:175]\n\t"
            "v_cvt_pk_bf16_f32 v198, v164, v165\n\t"
            "v_cvt_pk_bf16_f32 v199, v166, v167\n\t"
            "v_mfma_f32_16x16x32_bf16 v[172:175], v[224:227], v[144:147], v[172:175]\n\t"
            "v_mul_f32 v238, 0x3e553b94, v238\n\t"
            "v_mfma_f32_16x16x16_bf16 v[148:151], v[152:153], v[154:155], 0\n\t"
            "v_mfma_f32_16x16x16_bf16 v[160:163], v[176:177], v[154:155], 0\n\t"
            "v_mfma_f32_16x16x16_bf16 v[164:167], v[196:197], v[154:155], 0\n\t"
            "v_mfma_f32_16x16x32_bf16 v[212:215], v[216:219], v[180:183], v[212:215]\n\t"
            "v_mfma_f32_16x16x32_bf16 v[212:215], v[224:227], v[168:171], v[212:215]\n\t"
            "s_nop 1\n\t"
            "v_mfma_f32_16x16x32_bf16 v[148:151], v[144:147], v[156:159], v[148:151]\n\t"
            "v_mfma_f32_16x16x32_bf16 v[184:187], v[144:147], v[180:183], v[184:187]\n\t"
            "v_mfma_f32_16x16x32_bf16 v[160:163], v[168:171], v[156:159], v[160:163]\n\t"
            "v_mfma_f32_16x16x32_bf16 v[204:207], v[168:171], v[180:183], v[204:207]\n\t"
            "v_mfma_f32_16x16x32_bf16 v[164:167], v[192:195], v[156:159], v[164:167]\n\t"
            "v_mfma_f32_16x16x32_bf16 v[188:191], v[192:195], v[180:183], v[188:191]\n\t"
            "v_cndmask_b32_e64 v235, v173, v213, s[60:61]\n\t"
            "v_mfma_f32_16x16x32_bf16 v[208:211], v[220:223], v[196:199], 0\n\t"
            "s_nop 0\n\t"
            "v_max3_f32 v228, v172, v148, v149\n\t"
            "v_max3_f32 v228, v228, v150, v151\n\t"
            "v_mfma_f32_16x16x32_bf16 v[208:211], v[216:219], v[200:203], v[208:211]\n\t"
            "v_max3_f32 v229, v212, v184, v185\n\t"
            "v_max3_f32 v229, v229, v186, v187\n\t"
            "v_mfma_f32_16x16x32_bf16 v[208:211], v[224:227], v[192:195], v[208:211]\n\t"
            "s_setprio 0\n\t"
            "v_max3_f32 v228, v228, v160, v161\n\t"
            "v_max3_f32 v229, v229, v204, v205\n\t"
            "v_max3_f32 v228, v228, v162, v163\n\t"
            "v_max3_f32 v229, v229, v206, v207\n\t"
            "v_max3_f32 v228, v228, v164, v165\n\t"
            "v_max3_f32 v229, v229, v188, v189\n\t"
            "v_max3_f32 v228, v228, v166, v167\n\t"
            "v_max3_f32 v229, v229, v190, v191\n\t"
            "v_max3_f32 v234, v173, v213, v209\n\t"
            "v_max_f32 v234, v234, v230\n\t"
            "v_permlane32_swap_b32 v228, v229\n\t"
            "v_max_f32 v228, v228, v229\n\t"
            "v_mov_b32 v231, v228\n\t"
            "v_max_f32_dpp v234, v234, v234 row_ror:8 row_mask:0xf bank_mask:0xf\n\t"
            "v_cndmask_b32_e64 v235, v235, v209, s[62:63]\n\t"
            "v_permlane16_swap_b32 v228, v231\n\t"
            "v_max_f32 v228, v228, v231\n\t"
            "v_mov_b32 v229, v228\n\t"
            "v_max_f32_dpp v234, v234, v234 row_ror:4 row_mask:0xf bank_mask:0xf\n\t"
            "v_cndmask_b32_e64 v235, v235, v230, s[64:65]\n\t"
            "v_permlane32_swap_b32 v228, v229\n\t"
            "v_mul_f32_e64 v229, -v229, v237\n\t"
            "v_fma_f32 v184, v184, v237, v229\n\t"
            "v_fma_f32 v185, v185, v237, v229\n\t"
            "v_exp_f32 v184, v184\n\t"
            "v_exp_f32 v185, v185\n\t"
            "v_fma_f32 v186, v186, v237, v229\n\t"
            "v_fma_f32 v187, v187, v237, v229\n\t"
            "v_exp_f32 v186, v186\n\t"
            "v_exp_f32 v187, v187\n\t"
            "v_fma_f32 v204, v204, v237, v229\n\t"
            "v_fma_f32 v205, v205, v237, v229\n\t"
            "v_fma_f32 v206, v206, v237, v229\n\t"
            "v_fma_f32 v207, v207, v237, v229\n\t"
            "v_exp_f32 v204, v204\n\t"
            "v_exp_f32 v205, v205\n\t"
            "v_exp_f32 v206, v206\n\t"
            "v_exp_f32 v207, v207\n\t"
            "v_cvt_pk_bf16_f32 v184, v184, v185\n\t"
            "v_mul_f32_e64 v228, -v228, v236\n\t"
            "v_cvt_pk_bf16_f32 v185, v186, v187\n\t"
            "v_fma_f32 v210, v172, v236, v228\n\t"
            "v_cvt_pk_bf16_f32 v186, v204, v205\n\t"
            "v_cvt_pk_bf16_f32 v187, v206, v207\n\t"
            "s_setprio 3\n\t"
            "v_mfma_f32_16x16x16_bf16 v[180:183], v[152:153], v[198:199], 0\n\t"
            "v_max_f32_dpp v234, v234, v234 row_ror:2 row_mask:0xf bank_mask:0xf\n\t"
            "s_mov_b64 exec, s[60:61]\n\t"
            "v_fma_f32 v210, v212, v237, v229\n\t"
            "s_mov_b64 exec, -1\n\t"
            "v_mfma_f32_16x16x16_bf16 v[204:207], v[176:177], v[198:199], 0\n\t"
            "v_max_f32_dpp v234, v234, v234 row_ror:1 row_mask:0xf bank_mask:0xf\n\t"
            "v_sub_f32 v235, v235, v234\n\t"
            "v_mul_f32 v235, v239, v235\n\t"
            "v_mfma_f32_16x16x16_bf16 v[172:175], v[196:197], v[198:199], 0\n\t"
            "v_exp_f32 v235, v235\n\t"
            "v_fma_f32 v148, v148, v236, v228\n\t"
            "v_mfma_f32_16x16x32_bf16 v[180:183], v[144:147], v[200:203], v[180:183]\n\t"
            "v_cvt_pk_bf16_f32 v235, v235, 0\n\t"
            "v_cndmask_b32_e64 v235, 0, v235, s[66:67]\n\t"
            "v_mfma_f32_16x16x32_bf16 v[204:207], v[168:171], v[200:203], v[204:207]\n\t"
            "ds_write_b16 v250, v235 offset:256\n\t"
            "v_mfma_f32_16x16x32_bf16 v[172:175], v[192:195], v[200:203], v[172:175]\n\t"
            "s_setprio 0\n\t"
            "v_fma_f32 v149, v149, v236, v228\n\t"
            "v_fma_f32 v164, v164, v236, v228\n\t"
            "v_fma_f32 v165, v165, v236, v228\n\t"
            "v_max3_f32 v230, v208, v180, v181\n\t"
            "v_max3_f32 v230, v230, v182, v183\n\t"
            "v_exp_f32 v148, v148\n\t"
            "v_max3_f32 v230, v230, v204, v205\n\t"
            "v_max3_f32 v230, v230, v206, v207\n\t"
            "v_max3_f32 v230, v230, v172, v173\n\t"
            "v_max3_f32 v230, v230, v174, v175\n\t"
            "v_mov_b32 v231, v230\n\t"
            "v_exp_f32 v149, v149\n\t"
            "v_fma_f32 v150, v150, v236, v228\n\t"
            "v_permlane32_swap_b32 v230, v231\n\t"
            "v_max_f32 v230, v230, v231\n\t"
            "v_mov_b32 v231, v230\n\t"
            "v_fma_f32 v151, v151, v236, v228\n\t"
            "v_exp_f32 v164, v164\n\t"
            "v_permlane16_swap_b32 v230, v231\n\t"
            "v_max_f32 v230, v230, v231\n\t"
            "v_mul_f32_e64 v230, -v230, v238\n\t"
            "s_mov_b64 exec, s[62:63]\n\t"
            "v_fma_f32 v210, v208, v238, v230\n\t"
            "s_mov_b64 exec, -1\n\t"
            "v_exp_f32 v165, v165\n\t"
            "v_fma_f32 v166, v166, v236, v228\n\t"
            "v_fma_f32 v167, v167, v236, v228\n\t"
            "v_exp_f32 v150, v150\n\t"
            "v_exp_f32 v151, v151\n\t"
            "v_fma_f32 v160, v160, v236, v228\n\t"
            "v_fma_f32 v161, v161, v236, v228\n\t"
            "v_fma_f32 v162, v162, v236, v228\n\t"
            "v_fma_f32 v163, v163, v236, v228\n\t"
            "v_exp_f32 v166, v166\n\t"
            "v_exp_f32 v167, v167\n\t"
            "v_exp_f32 v210, v210\n\t"
            "v_exp_f32 v160, v160\n\t"
            "v_exp_f32 v161, v161\n\t"
            "v_exp_f32 v162, v162\n\t"
            "v_exp_f32 v163, v163\n\t"
            "v_cvt_pk_bf16_f32 v148, v148, v149\n\t"
            "v_cvt_pk_bf16_f32 v164, v164, v165\n\t"
            "v_cvt_pk_bf16_f32 v149, v150, v151\n\t"
            "v_cvt_pk_bf16_f32 v165, v166, v167\n\t"
            "v_cvt_pk_bf16_f32 v210, v210, 0\n\t"
            "v_cvt_pk_bf16_f32 v150, v160, v161\n\t"
            "v_cvt_pk_bf16_f32 v151, v162, v163\n\t"
            "v_mov_b32 v167, 0\n\t"
            "v_cndmask_b32_e64 v166, 0, v210, s[58:59]\n\t"
            "v_fma_f32 v188, v188, v237, v229\n\t"
            "v_fma_f32 v189, v189, v237, v229\n\t"
            "v_fma_f32 v180, v180, v238, v230\n\t"
            "v_fma_f32 v181, v181, v238, v230\n\t"
            "v_fma_f32 v172, v172, v238, v230\n\t"
            "v_fma_f32 v173, v173, v238, v230\n\t"
            "v_exp_f32 v188, v188\n\t"
            "v_exp_f32 v189, v189\n\t"
            "v_fma_f32 v190, v190, v237, v229\n\t"
            "v_fma_f32 v191, v191, v237, v229\n\t"
            "v_exp_f32 v180, v180\n\t"
            "v_exp_f32 v181, v181\n\t"
            "v_fma_f32 v182, v182, v238, v230\n\t"
            "v_fma_f32 v183, v183, v238, v230\n\t"
            "v_exp_f32 v172, v172\n\t"
            "v_exp_f32 v173, v173\n\t"
            "v_fma_f32 v174, v174, v238, v230\n\t"
            "v_fma_f32 v175, v175, v238, v230\n\t"
            "v_exp_f32 v190, v190\n\t"
            "v_exp_f32 v191, v191\n\t"
            "v_exp_f32 v182, v182\n\t"
            "v_exp_f32 v183, v183\n\t"
            "v_fma_f32 v204, v204, v238, v230\n\t"
            "v_fma_f32 v205, v205, v238, v230\n\t"
            "v_fma_f32 v206, v206, v238, v230\n\t"
            "v_fma_f32 v207, v207, v238, v230\n\t"
            "v_exp_f32 v174, v174\n\t"
            "v_exp_f32 v175, v175\n\t"
            "v_exp_f32 v204, v204\n\t"
            "v_exp_f32 v205, v205\n\t"
            "v_exp_f32 v206, v206\n\t"
            "v_exp_f32 v207, v207\n\t"
            "v_cvt_pk_bf16_f32 v188, v188, v189\n\t"
            "v_cvt_pk_bf16_f32 v180, v180, v181\n\t"
            "v_cvt_pk_bf16_f32 v172, v172, v173\n\t"
            "v_cvt_pk_bf16_f32 v189, v190, v191\n\t"
            "v_cvt_pk_bf16_f32 v181, v182, v183\n\t"
            "v_cvt_pk_bf16_f32 v173, v174, v175\n\t"
            "v_cvt_pk_bf16_f32 v182, v204, v205\n\t"
            "v_cvt_pk_bf16_f32 v183, v206, v207\n\t"
            "v_cndmask_b32_e64 v190, 0, v210, s[60:61]\n\t"
            "v_cndmask_b32_e64 v174, 0, v210, s[62:63]\n\t"
            "v_mov_b32 v191, 0\n\t"
            "v_mov_b32 v175, 0\n\t"
            "s_cmp_ge_u32 s70, s80\n\t"
            "s_barrier\n\t"
            "s_setprio 2\n\t"
            "s_cbranch_scc1 .Lwa_h1%=\n\t"
            ".Lwa_r1%=:\n\t"
            "v_mfma_f32_16x16x32_bf16 v[200:203], v[240:243], v[148:151], 0\n\t"
            "ds_read_b64 v[176:177], v247\n\t"
            "ds_read_b64 v[196:197], v247 offset:1536\n\t"
            "v_mfma_f32_16x16x32_bf16 v[200:203], v[240:243], v[164:167], v[200:203]\n\t"
            "ds_read_b64 v[220:221], v247 offset:3072\n\t"
            "v_xor_b32 v234, s73, v254\n\t"
            "v_add_u32 v235, s45, v248\n\t"
            "v_mov_b32 v179, 0\n\t"
            "v_mov_b32 v199, 0\n\t"
            "v_mov_b32 v223, 0\n\t"
            "v_add_u32 v232, s48, v244\n\t"
            "v_xor_b32 v233, 64, v232\n\t"
            "v_rcp_f32 v201, v200\n\t"
            "v_xor_b32 v249, 0x100, v249\n\t"
            "ds_read_b128 v[168:171], v235\n\t"
            "ds_read_u16 v178, v234\n\t"
            "ds_read_b128 v[192:195], v235 offset:1536\n\t"
            "ds_read_u16 v198, v234 offset:128\n\t"
            "ds_read_b128 v[216:219], v235 offset:3072\n\t"
            "ds_read_u16 v222, v234 offset:256\n\t"
            "ds_read_b128 v[204:207], v232\n\t"
            "ds_read_b128 v[208:211], v233\n\t"
            "ds_read_b32 v236, v249\n\t"
            "ds_read_b32 v237, v249 offset:64\n\t"
            "ds_read_b32 v238, v249 offset:128\n\t"
            "v_mad_u32_u24 v234, v246, s75, v253\n\t"
            "s_waitcnt lgkmcnt(9)\n\t"
            "v_mfma_f32_16x16x32_bf16 v[224:227], v[168:171], v[148:151], 0\n\t"
            "v_mfma_f32_16x16x32_bf16 v[224:227], v[176:179], v[164:167], v[224:227]\n\t"
            "s_waitcnt lgkmcnt(7)\n\t"
            "v_mfma_f32_16x16x32_bf16 v[212:215], v[192:195], v[148:151], 0\n\t"
            "v_mfma_f32_16x16x32_bf16 v[212:215], v[196:199], v[164:167], v[212:215]\n\t"
            "s_waitcnt lgkmcnt(5)\n\t"
            "v_mfma_f32_16x16x32_bf16 v[148:151], v[216:219], v[148:151], 0\n\t"
            "v_mfma_f32_16x16x32_bf16 v[148:151], v[220:223], v[164:167], v[148:151]\n\t"
            "s_waitcnt lgkmcnt(3)\n\t"
            "v_mfma_f32_16x16x32_bf16 v[144:147], v[0:3], v[204:207], 0\n\t"
            "v_mfma_f32_16x16x32_bf16 v[144:147], v[4:7], v[208:211], v[144:147]\n\t"
            "v_mul_f32 v224, v201, v224\n\t"
            "v_mfma_f32_16x16x32_bf16 v[152:155], v[48:51], v[204:207], 0\n\t"
            "v_mul_f32 v225, v201, v225\n\t"
            "v_mul_f32 v226, v201, v226\n\t"
            "v_mfma_f32_16x16x32_bf16 v[152:155], v[52:55], v[208:211], v[152:155]\n\t"
            "v_mul_f32 v212, v201, v212\n\t"
            "v_mul_f32 v213, v201, v213\n\t"
            "v_mfma_f32_16x16x32_bf16 v[156:159], v[76:79], v[208:211], 0\n\t"
            "v_mul_f32 v227, v201, v227\n\t"
            "v_cvt_pk_bf16_f32 v224, v224, v225\n\t"
            "v_mfma_f32_16x16x32_bf16 v[156:159], v[72:75], v[204:207], v[156:159]\n\t"
            "v_mul_f32 v214, v201, v214\n\t"
            "v_mul_f32 v215, v201, v215\n\t"
            "v_mfma_f32_16x16x32_bf16 v[160:163], v[96:99], v[204:207], 0\n\t"
            "v_cvt_pk_bf16_f32 v212, v212, v213\n\t"
            "v_cvt_pk_bf16_f32 v225, v226, v227\n\t"
            "v_mfma_f32_16x16x32_bf16 v[160:163], v[100:103], v[208:211], v[160:163]\n\t"
            "v_cvt_pk_bf16_f32 v213, v214, v215\n\t"
            "v_mul_f32 v148, v201, v148\n\t"
            "v_mfma_f32_16x16x32_bf16 v[164:167], v[124:127], v[208:211], 0\n\t"
            "v_mul_f32 v149, v201, v149\n\t"
            "v_mul_f32 v150, v201, v150\n\t"
            "v_mfma_f32_16x16x32_bf16 v[164:167], v[120:123], v[204:207], v[164:167]\n\t"
            "ds_write_b64 v245, v[224:225]\n\t"
            "v_mfma_f32_16x16x32_bf16 v[224:227], v[168:171], v[184:187], 0\n\t"
            "v_mul_f32 v151, v201, v151\n\t"
            "v_cvt_pk_bf16_f32 v148, v148, v149\n\t"
            "v_mfma_f32_16x16x32_bf16 v[224:227], v[176:179], v[188:191], v[224:227]\n\t"
            "ds_write_b64 v245, v[212:213] offset:128\n\t"
            "v_mfma_f32_16x16x32_bf16 v[200:203], v[240:243], v[184:187], 0\n\t"
            "v_cvt_pk_bf16_f32 v149, v150, v151\n\t"
            "v_mfma_f32_16x16x32_bf16 v[200:203], v[240:243], v[188:191], v[200:203]\n\t"
            "ds_write_b64 v245, v[148:149] offset:256\n\t"
            "v_mfma_f32_16x16x32_bf16 v[212:215], v[192:195], v[184:187], 0\n\t"
            "v_mfma_f32_16x16x32_bf16 v[212:215], v[196:199], v[188:191], v[212:215]\n\t"
            "v_mfma_f32_16x16x32_bf16 v[184:187], v[216:219], v[184:187], 0\n\t"
            "v_mfma_f32_16x16x32_bf16 v[184:187], v[220:223], v[188:191], v[184:187]\n\t"
            "v_mfma_f32_16x16x32_bf16 v[148:151], v[28:31], v[208:211], 0\n\t"
            "ds_read_b128 v[208:211], v233 offset:128\n\t"
            "v_mfma_f32_16x16x32_bf16 v[148:151], v[24:27], v[204:207], v[148:151]\n\t"
            "ds_read_b128 v[204:207], v232 offset:128\n\t"
            "v_rcp_f32 v201, v200\n\t"
            "v_mfma_f32_16x16x32_bf16 v[188:191], v[192:195], v[180:183], 0\n\t"
            "v_mul_f32 v224, v201, v224\n\t"
            "v_mul_f32 v225, v201, v225\n\t"
            "v_mfma_f32_16x16x32_bf16 v[188:191], v[196:199], v[172:175], v[188:191]\n\t"
            "v_mul_f32 v226, v201, v226\n\t"
            "v_mul_f32 v227, v201, v227\n\t"
            "v_mul_f32 v212, v201, v212\n\t"
            "v_mul_f32 v213, v201, v213\n\t"
            "v_mul_f32 v214, v201, v214\n\t"
            "v_mul_f32 v215, v201, v215\n\t"
            "v_mul_f32 v184, v201, v184\n\t"
            "v_mul_f32 v185, v201, v185\n\t"
            "v_mul_f32 v186, v201, v186\n\t"
            "v_mul_f32 v187, v201, v187\n\t"
            "v_mfma_f32_16x16x32_bf16 v[200:203], v[240:243], v[180:183], 0\n\t"
            "v_cvt_pk_bf16_f32 v224, v224, v225\n\t"
            "v_cvt_pk_bf16_f32 v225, v226, v227\n\t"
            "v_mfma_f32_16x16x32_bf16 v[200:203], v[240:243], v[172:175], v[200:203]\n\t"
            "ds_write_b64 v245, v[224:225] offset:6144\n\t"
            "v_mfma_f32_16x16x32_bf16 v[224:227], v[168:171], v[180:183], 0\n\t"
            "v_cvt_pk_bf16_f32 v184, v184, v185\n\t"
            "v_cvt_pk_bf16_f32 v185, v186, v187\n\t"
            "v_mfma_f32_16x16x32_bf16 v[224:227], v[176:179], v[172:175], v[224:227]\n\t"
            "ds_read_b64 v[186:187], v234 offset:288\n\t"
            "ds_write_b64 v245, v[184:185] offset:6400\n\t"
            "s_waitcnt lgkmcnt(3)\n\t"
            "v_mfma_f32_16x16x32_bf16 v[144:147], v[8:11], v[204:207], v[144:147]\n\t"
            "ds_read_b64 v[184:185], v234 offset:256\n\t"
            "v_rcp_f32 v201, v200\n\t"
            "v_mfma_f32_16x16x32_bf16 v[144:147], v[12:15], v[208:211], v[144:147]\n\t"
            "v_mul_f32 v188, v201, v188\n\t"
            "v_mul_f32 v189, v201, v189\n\t"
            "v_mfma_f32_16x16x32_bf16 v[148:151], v[36:39], v[208:211], v[148:151]\n\t"
            "v_mul_f32 v190, v201, v190\n\t"
            "v_mul_f32 v191, v201, v191\n\t"
            "v_mfma_f32_16x16x32_bf16 v[148:151], v[32:35], v[204:207], v[148:151]\n\t"
            "v_cvt_pk_bf16_f32 v188, v188, v189\n\t"
            "v_cvt_pk_bf16_f32 v189, v190, v191\n\t"
            "v_mfma_f32_16x16x32_bf16 v[152:155], v[56:59], v[204:207], v[152:155]\n\t"
            "ds_read_b64 v[190:191], v234 offset:352\n\t"
            "v_mul_f32 v224, v201, v224\n\t"
            "v_mfma_f32_16x16x32_bf16 v[152:155], v[60:63], v[208:211], v[152:155]\n\t"
            "ds_write_b64 v245, v[188:189] offset:12416\n\t"
            "v_mfma_f32_16x16x32_bf16 v[156:159], v[84:87], v[208:211], v[156:159]\n\t"
            "ds_read_b64 v[188:189], v234 offset:320\n\t"
            "v_mul_f32 v225, v201, v225\n\t"
            "v_mfma_f32_16x16x32_bf16 v[156:159], v[80:83], v[204:207], v[156:159]\n\t"
            "v_cvt_pk_bf16_f32 v212, v212, v213\n\t"
            "v_mul_f32 v226, v201, v226\n\t"
            "v_mfma_f32_16x16x32_bf16 v[160:163], v[104:107], v[204:207], v[160:163]\n\t"
            "v_mul_f32 v227, v201, v227\n\t"
            "v_cvt_pk_bf16_f32 v224, v224, v225\n\t"
            "v_mfma_f32_16x16x32_bf16 v[160:163], v[108:111], v[208:211], v[160:163]\n\t"
            "v_cvt_pk_bf16_f32 v213, v214, v215\n\t"
            "v_cvt_pk_bf16_f32 v225, v226, v227\n\t"
            "v_mfma_f32_16x16x32_bf16 v[164:167], v[132:135], v[208:211], v[164:167]\n\t"
            "ds_read_b128 v[208:211], v233 offset:6144\n\t"
            "v_mfma_f32_16x16x32_bf16 v[164:167], v[128:131], v[204:207], v[164:167]\n\t"
            "ds_read_b128 v[204:207], v232 offset:6144\n\t"
            "ds_write_b64 v245, v[212:213] offset:6272\n\t"
            "v_mfma_f32_16x16x32_bf16 v[180:183], v[216:219], v[180:183], 0\n\t"
            "ds_read_b128 v[212:215], v232 offset:256\n\t"
            "v_mfma_f32_16x16x32_bf16 v[180:183], v[220:223], v[172:175], v[180:183]\n\t"
            "ds_write_b64 v245, v[224:225] offset:12288\n\t"
            "s_waitcnt lgkmcnt(5)\n\t"
            "v_mfma_f32_16x16x32_bf16 v[168:171], v[184:187], v[168:171], 0\n\t"
            "ds_read_b128 v[224:227], v233 offset:256\n\t"
            "v_mfma_f32_16x16x32_bf16 v[168:171], v[188:191], v[176:179], v[168:171]\n\t"
            "v_mfma_f32_16x16x32_bf16 v[192:195], v[184:187], v[192:195], 0\n\t"
            "v_mfma_f32_16x16x32_bf16 v[192:195], v[188:191], v[196:199], v[192:195]\n\t"
            "s_nop 0\n\t"
            "v_mul_f32 v180, v201, v180\n\t"
            "v_mfma_f32_16x16x32_bf16 v[216:219], v[184:187], v[216:219], 0\n\t"
            "v_mul_f32 v181, v201, v181\n\t"
            "v_mul_f32 v182, v201, v182\n\t"
            "v_mfma_f32_16x16x32_bf16 v[216:219], v[188:191], v[220:223], v[216:219]\n\t"
            "v_mul_f32 v183, v201, v183\n\t"
            "v_cvt_pk_bf16_f32 v180, v180, v181\n\t"
            "v_mfma_f32_16x16x32_bf16 v[200:203], v[240:243], v[184:187], 0\n\t"
            "v_cvt_pk_bf16_f32 v181, v182, v183\n\t"
            "v_mfma_f32_16x16x32_bf16 v[200:203], v[240:243], v[188:191], v[200:203]\n\t"
            "ds_write_b64 v245, v[180:181] offset:12544\n\t"
            "s_waitcnt lgkmcnt(1)\n\t"
            "v_mfma_f32_16x16x32_bf16 v[144:147], v[16:19], v[212:215], v[144:147]\n\t"
            "v_mfma_f32_16x16x32_bf16 v[144:147], v[20:23], v[224:227], v[144:147]\n\t"
            "v_mfma_f32_16x16x32_bf16 v[148:151], v[44:47], v[224:227], v[148:151]\n\t"
            "v_mfma_f32_16x16x32_bf16 v[148:151], v[40:43], v[212:215], v[148:151]\n\t"
            "v_mfma_f32_16x16x32_bf16 v[152:155], v[64:67], v[212:215], v[152:155]\n\t"
            "s_nop 0\n\t"
            "v_cndmask_b32_e64 v203, v168, v192, s[60:61]\n\t"
            "v_mfma_f32_16x16x32_bf16 v[152:155], v[68:71], v[224:227], v[152:155]\n\t"
            "v_rcp_f32 v201, v200\n\t"
            "v_mfma_f32_16x16x32_bf16 v[156:159], v[92:95], v[224:227], v[156:159]\n\t"
            "v_cndmask_b32_e64 v203, v203, v216, s[62:63]\n\t"
            "v_xad_u32 v202, s73, v254, v255\n\t"
            "v_mfma_f32_16x16x32_bf16 v[156:159], v[88:91], v[212:215], v[156:159]\n\t"
            "v_mul_f32_dpp v203, v201, v203 quad_perm:[0,0,0,0] row_mask:0xf bank_mask:0xf\n\t"
            "v_cvt_pk_bf16_f32 v203, v203, 0\n\t"
            "v_mfma_f32_16x16x32_bf16 v[160:163], v[112:115], v[212:215], v[160:163]\n\t"
            "v_mul_f32 v144, v236, v144\n\t"
            "v_mul_f32 v145, v236, v145\n\t"
            "v_mfma_f32_16x16x32_bf16 v[160:163], v[116:119], v[224:227], v[160:163]\n\t"
            "s_mov_b64 exec, s[68:69]\n\t"
            "ds_write_b16 v202, v203\n\t"
            "s_mov_b64 exec, 1\n\t"
            "ds_add_u32 v253, v246 offset:7856\n\t"
            "s_mov_b64 exec, -1\n\t"
            "v_mfma_f32_16x16x32_bf16 v[164:167], v[140:143], v[224:227], v[164:167]\n\t"
            "ds_read_b128 v[224:227], v233 offset:6272\n\t"
            "v_mul_f32 v152, v236, v152\n\t"
            "v_mfma_f32_16x16x32_bf16 v[164:167], v[136:139], v[212:215], v[164:167]\n\t"
            "ds_read_b128 v[212:215], v232 offset:6272\n\t"
            "v_mul_f32 v153, v236, v153\n\t"
            "v_mfma_f32_16x16x32_bf16 v[168:171], v[0:3], v[204:207], 0\n\t"
            "v_mul_f32 v146, v236, v146\n\t"
            "v_mul_f32 v147, v236, v147\n\t"
            "v_mfma_f32_16x16x32_bf16 v[168:171], v[4:7], v[208:211], v[168:171]\n\t"
            "v_mul_f32 v154, v236, v154\n\t"
            "v_mul_f32 v155, v236, v155\n\t"
            "v_mfma_f32_16x16x32_bf16 v[172:175], v[28:31], v[208:211], 0\n\t"
            "v_cvt_pk_bf16_f32 v144, v144, v145\n\t"
            "v_cvt_pk_bf16_f32 v152, v152, v153\n\t"
            "v_mfma_f32_16x16x32_bf16 v[172:175], v[24:27], v[204:207], v[172:175]\n\t"
            "v_cvt_pk_bf16_f32 v156, v156, v157\n\t"
            "v_mul_f32 v148, v236, v148\n\t"
            "v_mfma_f32_16x16x32_bf16 v[176:179], v[48:51], v[204:207], 0\n\t"
            "v_mul_f32 v149, v236, v149\n\t"
            "v_mul_f32 v150, v236, v150\n\t"
            "v_mfma_f32_16x16x32_bf16 v[176:179], v[52:55], v[208:211], v[176:179]\n\t"
            "v_mul_f32 v151, v236, v151\n\t"
            "v_cvt_pk_bf16_f32 v145, v146, v147\n\t"
            "v_mfma_f32_16x16x32_bf16 v[180:183], v[76:79], v[208:211], 0\n\t"
            "v_cvt_pk_bf16_f32 v153, v154, v155\n\t"
            "v_cvt_pk_bf16_f32 v157, v158, v159\n\t"
            "v_mfma_f32_16x16x32_bf16 v[180:183], v[72:75], v[204:207], v[180:183]\n\t"
            "v_cvt_pk_bf16_f32 v146, v148, v149\n\t"
            "v_cvt_pk_bf16_f32 v147, v150, v151\n\t"
            "v_mfma_f32_16x16x32_bf16 v[184:187], v[96:99], v[204:207], 0\n\t"
            "v_cvt_pk_bf16_f32 v158, v160, v161\n\t"
            "v_cvt_pk_bf16_f32 v159, v162, v163\n\t"
            "v_mfma_f32_16x16x32_bf16 v[184:187], v[100:103], v[208:211], v[184:187]\n\t"
            "v_cvt_pk_bf16_f32 v154, v164, v165\n\t"
            "v_cvt_pk_bf16_f32 v155, v166, v167\n\t"
            "v_mfma_f32_16x16x32_bf16 v[188:191], v[124:127], v[208:211], 0\n\t"
            "ds_read_b128 v[208:211], v233 offset:6400\n\t"
            "v_mul_f32 v236, 0x3e553b94, v236\n\t"
            "v_mfma_f32_16x16x32_bf16 v[188:191], v[120:123], v[204:207], v[188:191]\n\t"
            "ds_read_b128 v[204:207], v232 offset:6400\n\t"
            "s_waitcnt lgkmcnt(2)\n\t"
            "v_mfma_f32_16x16x32_bf16 v[168:171], v[8:11], v[212:215], v[168:171]\n\t"
            "v_mfma_f32_16x16x32_bf16 v[168:171], v[12:15], v[224:227], v[168:171]\n\t"
            "v_mfma_f32_16x16x32_bf16 v[172:175], v[36:39], v[224:227], v[172:175]\n\t"
            "v_mfma_f32_16x16x32_bf16 v[172:175], v[32:35], v[212:215], v[172:175]\n\t"
            "v_mfma_f32_16x16x32_bf16 v[176:179], v[56:59], v[212:215], v[176:179]\n\t"
            "v_mfma_f32_16x16x32_bf16 v[176:179], v[60:63], v[224:227], v[176:179]\n\t"
            "v_mfma_f32_16x16x32_bf16 v[180:183], v[84:87], v[224:227], v[180:183]\n\t"
            "v_mfma_f32_16x16x32_bf16 v[180:183], v[80:83], v[212:215], v[180:183]\n\t"
            "v_mfma_f32_16x16x32_bf16 v[184:187], v[104:107], v[212:215], v[184:187]\n\t"
            "v_mfma_f32_16x16x32_bf16 v[184:187], v[108:111], v[224:227], v[184:187]\n\t"
            "v_mfma_f32_16x16x32_bf16 v[188:191], v[132:135], v[224:227], v[188:191]\n\t"
            "ds_read_b128 v[224:227], v233 offset:12288\n\t"
            "v_mfma_f32_16x16x32_bf16 v[188:191], v[128:131], v[212:215], v[188:191]\n\t"
            "ds_read_b128 v[212:215], v232 offset:12288\n\t"
            "s_waitcnt lgkmcnt(2)\n\t"
            "v_mfma_f32_16x16x32_bf16 v[168:171], v[16:19], v[204:207], v[168:171]\n\t"
            "v_mfma_f32_16x16x32_bf16 v[168:171], v[20:23], v[208:211], v[168:171]\n\t"
            "v_mfma_f32_16x16x32_bf16 v[172:175], v[44:47], v[208:211], v[172:175]\n\t"
            "v_mfma_f32_16x16x32_bf16 v[172:175], v[40:43], v[204:207], v[172:175]\n\t"
            "v_mfma_f32_16x16x32_bf16 v[176:179], v[64:67], v[204:207], v[176:179]\n\t"
            "v_mfma_f32_16x16x32_bf16 v[176:179], v[68:71], v[208:211], v[176:179]\n\t"
            "v_mfma_f32_16x16x32_bf16 v[180:183], v[92:95], v[208:211], v[180:183]\n\t"
            "v_mfma_f32_16x16x32_bf16 v[180:183], v[88:91], v[204:207], v[180:183]\n\t"
            "v_mfma_f32_16x16x32_bf16 v[184:187], v[112:115], v[204:207], v[184:187]\n\t"
            "v_mfma_f32_16x16x32_bf16 v[184:187], v[116:119], v[208:211], v[184:187]\n\t"
            "v_mfma_f32_16x16x32_bf16 v[188:191], v[140:143], v[208:211], v[188:191]\n\t"
            "ds_read_b128 v[208:211], v233 offset:12416\n\t"
            "v_mfma_f32_16x16x32_bf16 v[188:191], v[136:139], v[204:207], v[188:191]\n\t"
            "ds_read_b128 v[204:207], v232 offset:12416\n\t"
            "s_cmp_eq_u32 s91, 0\n\t"
            "s_cbranch_scc1 .Lwa_loop%=\n\t"
            "s_mov_b32 s91, 0\n\t"
            "s_waitcnt vmcnt(0)\n\t"
            "s_cmp_eq_u32 s81, 0\n\t"
            "s_cbranch_scc1 .Lwa_h6%=\n\t"
            "s_mov_b32 s89, 0x10000\n\t"
            ".Lwa_h4%=:\n\t"
            "v_readfirstlane_b32 s49, v228\n\t"
            "v_readfirstlane_b32 vcc_lo, v229\n\t"
            "s_xor_b32 s49, s49, s82\n\t"
            "s_xor_b32 vcc_lo, vcc_lo, s83\n\t"
            "s_or_b32 s49, s49, vcc_lo\n\t"
            "s_cbranch_scc0 .Lwa_h5%=\n\t"
            "s_sub_u32 s89, s89, 1\n\t"
            "s_cbranch_scc1 .Lwa_h5%=\n\t"
            "s_sleep 2\n\t"
            "buffer_load_dwordx3 v[228:230], off, s[84:87], s81 offset:1216 sc1\n\t"
            "s_waitcnt vmcnt(0)\n\t"
            "s_branch .Lwa_h4%=\n\t"
            ".Lwa_h5%=:\n\t"
            "s_lshl_b32 s49, s70, 2\n\t"
            "s_add_u32 s49, s49, 163520\n\t"
            "v_mov_b32 v228, s49\n\t"
            "s_mul_i32 s49, s70, 800\n\t"
            "s_add_u32 m0, s49, 113760\n\t"
            "ds_write_b32 v228, v230 offset:12\n\t"
            "s_mov_b32 exec_hi, 0x3ffff\n\t"
            "buffer_load_dwordx4 v248, s[84:87], s81 offen sc1 lds\n\t"
            "s_mul_i32 s49, s70, 384\n\t"
            "s_add_u32 m0, s49, 130176\n\t"
            "s_add_u32 s81, s81, 800\n\t"
            "s_mov_b64 exec, 0xffffff\n\t"
            "buffer_load_dwordx4 v248, s[84:87], s81 offen sc1 lds\n\t"
            "s_mov_b64 exec, -1\n\t"
            "s_mov_b32 s81, 0\n\t"
            "s_waitcnt lgkmcnt(0)\n\t"
            ".Lwa_h6%=:\n\t"
            "s_cmp_eq_u32 s90, 0\n\t"
            "s_cbranch_scc1 .Lwa_loop%=\n\t"
            "v_readfirstlane_b32 s49, v231\n\t"
            "s_sub_u32 s90, s90, 1\n\t"
            "s_cmp_lt_u32 s49, 209\n\t"
            "s_cselect_b32 s90, s90, 0\n\t"
            "s_cbranch_scc0 .Lwa_loop%=\n\t"
            "s_mul_i32 s81, s49, 1280\n\t"
            "s_add_u32 s81, s81, 303104\n\t"
            "s_branch .Lwa_loop%=\n\t"
            ".Lwa_h1%=:\n\t"
            "s_cmp_lt_u32 s70, 20\n\t"
            "s_cbranch_scc1 .Lwa_h2%=\n\t"
            "s_lshl_b32 s49, s70, 2\n\t"
            "s_add_u32 s49, s49, 163520\n\t"
            "v_mov_b32 v228, s49\n\t"
            "ds_read_b32 v228, v228\n\t"
            "s_waitcnt lgkmcnt(0)\n\t"
            "v_readfirstlane_b32 s49, v228\n\t"
            "s_cmp_eq_u32 s49, 0x7ffffff\n\t"
            "s_cbranch_scc1 .Lwa_last%=\n\t"
            "s_branch .Lwa_r1%=\n\t"
            ".Lwa_h2%=:\n\t"
            "s_waitcnt vmcnt(0)\n\t"
            "s_or_b32 s91, s81, s90\n\t"
            "s_cmp_eq_u32 s81, 0\n\t"
            "s_cbranch_scc1 .Lwa_h3%=\n\t"
            "buffer_load_dwordx3 v[228:230], off, s[84:87], s81 offset:1216 sc1\n\t"
            ".Lwa_h3%=:\n\t"
            "s_cmp_eq_u32 s90, 0\n\t"
            "s_cbranch_scc1 .Lwa_r1%=\n\t"
            "v_mov_b32 v231, 1\n\t"
            "s_mov_b32 s49, 301184\n\t"
            "s_mov_b64 exec, 1\n\t"
            "buffer_atomic_add v231, off, s[84:87], s49 sc0\n\t"
            "s_mov_b64 exec, -1\n\t"
            "s_branch .Lwa_r1%=\n\t"
            ".Lwa_last%=:\n\t"
            "s_setprio 2\n\t"
            "s_waitcnt lgkmcnt(0)\n\t"
            "s_nop 7\n\t"
            "v_mfma_f32_16x16x32_bf16 v[156:159], v[240:243], v[184:187], 0\n\t"
            "ds_read_b64 v[176:177], v247\n\t"
            "ds_read_b64 v[196:197], v247 offset:1536\n\t"
            "v_mfma_f32_16x16x32_bf16 v[156:159], v[240:243], v[188:191], v[156:159]\n\t"
            "ds_read_b64 v[220:221], v247 offset:3072\n\t"
            "v_xor_b32 v234, s73, v254\n\t"
            "v_mfma_f32_16x16x32_bf16 v[228:231], v[240:243], v[148:151], 0\n\t"
            "ds_read_u16 v178, v234\n\t"
            "ds_read_u16 v198, v234 offset:128\n\t"
            "v_mfma_f32_16x16x32_bf16 v[228:231], v[240:243], v[164:167], v[228:231]\n\t"
            "ds_read_u16 v222, v234 offset:256\n\t"
            "v_add_u32 v235, s45, v248\n\t"
            "v_mfma_f32_16x16x32_bf16 v[208:211], v[240:243], v[180:183], 0\n\t"
            "ds_read_b128 v[168:171], v235\n\t"
            "ds_read_b128 v[192:195], v235 offset:1536\n\t"
            "v_mfma_f32_16x16x32_bf16 v[208:211], v[240:243], v[172:175], v[208:211]\n\t"
            "ds_read_b128 v[216:219], v235 offset:3072\n\t"
            "v_mov_b32 v179, 0\n\t"
            "v_mov_b32 v199, 0\n\t"
            "v_mov_b32 v223, 0\n\t"
            "v_rcp_f32 v157, v156\n\t"
            "v_mad_u32_u24 v234, v246, s75, v253\n\t"
            "v_rcp_f32 v229, v228\n\t"
            "s_nop 0\n\t"
            "v_rcp_f32 v209, v208\n\t"
            "s_waitcnt lgkmcnt(2)\n\t"
            "v_mfma_f32_16x16x32_bf16 v[144:147], v[168:171], v[184:187], 0\n\t"
            "v_mfma_f32_16x16x32_bf16 v[144:147], v[176:179], v[188:191], v[144:147]\n\t"
            "s_waitcnt lgkmcnt(1)\n\t"
            "v_mfma_f32_16x16x32_bf16 v[152:155], v[192:195], v[184:187], 0\n\t"
            "v_mfma_f32_16x16x32_bf16 v[152:155], v[196:199], v[188:191], v[152:155]\n\t"
            "s_waitcnt lgkmcnt(0)\n\t"
            "v_mfma_f32_16x16x32_bf16 v[184:187], v[216:219], v[184:187], 0\n\t"
            "v_mfma_f32_16x16x32_bf16 v[184:187], v[220:223], v[188:191], v[184:187]\n\t"
            "ds_read_b64 v[188:189], v234 offset:320\n\t"
            "ds_read_b64 v[190:191], v234 offset:352\n\t"
            "v_mfma_f32_16x16x32_bf16 v[224:227], v[168:171], v[148:151], 0\n\t"
            "v_mul_f32 v144, v157, v144\n\t"
            "v_mul_f32 v145, v157, v145\n\t"
            "v_mfma_f32_16x16x32_bf16 v[224:227], v[176:179], v[164:167], v[224:227]\n\t"
            "v_mul_f32 v152, v157, v152\n\t"
            "v_mul_f32 v153, v157, v153\n\t"
            "v_mfma_f32_16x16x32_bf16 v[212:215], v[192:195], v[148:151], 0\n\t"
            "v_mul_f32 v184, v157, v184\n\t"
            "v_mul_f32 v185, v157, v185\n\t"
            "v_mfma_f32_16x16x32_bf16 v[212:215], v[196:199], v[164:167], v[212:215]\n\t"
            "v_mul_f32 v186, v157, v186\n\t"
            "v_mul_f32 v187, v157, v187\n\t"
            "v_mfma_f32_16x16x32_bf16 v[160:163], v[168:171], v[180:183], 0\n\t"
            "v_cvt_pk_bf16_f32 v184, v184, v185\n\t"
            "v_cvt_pk_bf16_f32 v185, v186, v187\n\t"
            "v_mfma_f32_16x16x32_bf16 v[160:163], v[176:179], v[172:175], v[160:163]\n\t"
            "ds_read_b64 v[186:187], v234 offset:288\n\t"
            "ds_write_b64 v245, v[184:185] offset:6400\n\t"
            "v_mfma_f32_16x16x32_bf16 v[204:207], v[192:195], v[180:183], 0\n\t"
            "ds_read_b64 v[184:185], v234 offset:256\n\t"
            "v_mul_f32 v224, v229, v224\n\t"
            "v_mfma_f32_16x16x32_bf16 v[204:207], v[196:199], v[172:175], v[204:207]\n\t"
            "v_mul_f32 v225, v229, v225\n\t"
            "v_mul_f32 v212, v229, v212\n\t"
            "v_mfma_f32_16x16x32_bf16 v[148:151], v[216:219], v[148:151], 0\n\t"
            "v_mul_f32 v213, v229, v213\n\t"
            "v_mul_f32 v160, v209, v160\n\t"
            "v_mfma_f32_16x16x32_bf16 v[148:151], v[220:223], v[164:167], v[148:151]\n\t"
            "v_mul_f32 v161, v209, v161\n\t"
            "v_mul_f32 v226, v229, v226\n\t"
            "v_mfma_f32_16x16x32_bf16 v[180:183], v[216:219], v[180:183], 0\n\t"
            "v_mul_f32 v204, v209, v204\n\t"
            "v_mul_f32 v205, v209, v205\n\t"
            "v_mfma_f32_16x16x32_bf16 v[180:183], v[220:223], v[172:175], v[180:183]\n\t"
            "v_mul_f32 v227, v229, v227\n\t"
            "v_cvt_pk_bf16_f32 v224, v224, v225\n\t"
            "v_mul_f32 v148, v229, v148\n\t"
            "v_mul_f32 v149, v229, v149\n\t"
            "v_mul_f32 v214, v229, v214\n\t"
            "v_mul_f32 v215, v229, v215\n\t"
            "v_cvt_pk_bf16_f32 v212, v212, v213\n\t"
            "s_waitcnt lgkmcnt(0)\n\t"
            "v_mfma_f32_16x16x32_bf16 v[200:203], v[240:243], v[184:187], 0\n\t"
            "v_mul_f32 v180, v209, v180\n\t"
            "v_mul_f32 v181, v209, v181\n\t"
            "v_mfma_f32_16x16x32_bf16 v[200:203], v[240:243], v[188:191], v[200:203]\n\t"
            "v_mul_f32 v150, v229, v150\n\t"
            "v_mul_f32 v151, v229, v151\n\t"
            "v_mfma_f32_16x16x32_bf16 v[168:171], v[184:187], v[168:171], 0\n\t"
            "v_cvt_pk_bf16_f32 v148, v148, v149\n\t"
            "v_mul_f32 v146, v157, v146\n\t"
            "v_mfma_f32_16x16x32_bf16 v[168:171], v[188:191], v[176:179], v[168:171]\n\t"
            "v_mul_f32 v147, v157, v147\n\t"
            "v_cvt_pk_bf16_f32 v144, v144, v145\n\t"
            "v_mfma_f32_16x16x32_bf16 v[192:195], v[184:187], v[192:195], 0\n\t"
            "v_rcp_f32 v201, v200\n\t"
            "v_mfma_f32_16x16x32_bf16 v[192:195], v[188:191], v[196:199], v[192:195]\n\t"
            "v_mul_f32 v154, v157, v154\n\t"
            "v_mul_f32 v155, v157, v155\n\t"
            "v_mfma_f32_16x16x32_bf16 v[216:219], v[184:187], v[216:219], 0\n\t"
            "v_cvt_pk_bf16_f32 v152, v152, v153\n\t"
            "v_mul_f32 v162, v209, v162\n\t"
            "v_mfma_f32_16x16x32_bf16 v[216:219], v[188:191], v[220:223], v[216:219]\n\t"
            "v_mul_f32 v163, v209, v163\n\t"
            "v_cvt_pk_bf16_f32 v160, v160, v161\n\t"
            "v_cndmask_b32_e64 v203, v168, v192, s[60:61]\n\t"
            "v_mul_f32 v206, v209, v206\n\t"
            "v_mul_f32 v207, v209, v207\n\t"
            "v_cvt_pk_bf16_f32 v204, v204, v205\n\t"
            "v_mul_f32 v182, v209, v182\n\t"
            "v_mul_f32 v183, v209, v183\n\t"
            "v_cvt_pk_bf16_f32 v180, v180, v181\n\t"
            "v_cndmask_b32_e64 v203, v203, v216, s[62:63]\n\t"
            "v_cvt_pk_bf16_f32 v225, v226, v227\n\t"
            "v_cvt_pk_bf16_f32 v213, v214, v215\n\t"
            "v_cvt_pk_bf16_f32 v149, v150, v151\n\t"
            "v_cvt_pk_bf16_f32 v145, v146, v147\n\t"
            "v_cvt_pk_bf16_f32 v153, v154, v155\n\t"
            "v_cvt_pk_bf16_f32 v161, v162, v163\n\t"
            "v_cvt_pk_bf16_f32 v205, v206, v207\n\t"
            "v_cvt_pk_bf16_f32 v181, v182, v183\n\t"
            "v_mul_f32_dpp v203, v201, v203 quad_perm:[0,0,0,0] row_mask:0xf bank_mask:0xf\n\t"
            "v_xad_u32 v202, s73, v254, v255\n\t"
            "v_cvt_pk_bf16_f32 v203, v203, 0\n\t"
            "ds_write_b64 v245, v[224:225]\n\t"
            "ds_write_b64 v245, v[212:213] offset:128\n\t"
            "ds_write_b64 v245, v[148:149] offset:256\n\t"
            "ds_write_b64 v245, v[144:145] offset:6144\n\t"
            "ds_write_b64 v245, v[152:153] offset:6272\n\t"
            "ds_write_b64 v245, v[160:161] offset:12288\n\t"
            "ds_write_b64 v245, v[204:205] offset:12416\n\t"
            "ds_write_b64 v245, v[180:181] offset:12544\n\t"
            "s_mov_b64 exec, s[68:69]\n\t"
            "ds_write_b16 v202, v203\n\t"
            "s_mov_b64 exec, 1\n\t"
            "ds_add_u32 v253, v246 offset:7856\n\t"
            "s_mov_b64 exec, -1\n\t"
            ".Lwa_done%=:\n\t"
            "s_waitcnt lgkmcnt(0)\n\t"
            "s_nop 7\n\t"
            : "+{v204}"(pST_), "+{v205}"(aRSw_), "+{v206}"(aPLw_), "+{v207}"(aRSr_), "+{v208}"(g16_), "+{v209}"(g8_), "+{v210}"(loVL_), "+{v211}"(loOL_), "+{v200}"(on), "+{v250}"(c0), "+{v251}"(c1), "+{v252}"(c2), "+{v253}"(c3), "+{v254}"(c4), "+{v255}"(c5),
              "+{v238}"(c6), "+{v239}"(c7), "+{v240}"(c8), "+{v241}"(c9), "+{s89}"(s89), "+{s46}"(s46), "+{s48}"(s48), "+{s70}"(s70), "+{s72}"(s72), "+{s73}"(s73), "+{s74}"(s74), "+{s76}"(s76), "+{s77}"(s77)
            : "{v244}"(pE_), "{v245}"(aO_), "{v246}"(sel_), "{v247}"(aVB_), "{s47}"((ldsb + X_OFF) ^ (ldsb + X1_OFF)),
              "{s[58:59]}"(0xFFFFull), "{s75}"(rsb), "{s45}"(ldsb + V_OFF + w * 4608), "{s[68:69]}"(0xFFFFFFFFFFFFull),
              "{s[84:87]}"(wr), "{s88}"(touch), "{s80}"(wave == 0 ? 16 : 20), "{s71}"(ldsb + O_OFF), "{s[54:55]}"(mrow), "{s[56:57]}"(0xF0000F0000F0000Full),
              "{s[78:79]}"(mrow & 0xF0000F0000F0000Full)
            : "v144", "v145", "v146", "v147", "v148", "v149", "v150", "v151", "v152", "v153", "v154", "v155", "v156", "v157", "v158", "v159", "v160", "v161", "v162", "v163", "v164", "v165", "v166", "v167", "v168", "v169", "v170", "v171", "v172", "v173", "v174", "v175", "v176", "v177", "v178", "v179", "v180", "v181", "v182", "v183", "v184", "v185", "v186", "v187", "v188", "v189", "v190", "v191", "v192", "v193", "v194", "v195", "v196", "v197", "v198", "v199", "v201", "v202", "v203", "v212", "v213", "v214", "v215", "v216", "v217", "v218", "v219", "v220", "v221", "v222", "v223", "v224", "v225", "v226", "v227", "v228", "v229", "v230", "v231", "v232", "v233", "v234", "v235", "v236", "v237", "v242", "v243", "v248", "v249", "v0", "v1", "v2", "v3", "v4", "v5", "v6", "v7", "v8", "v9", "v10", "v11", "v12", "v13", "v14", "v15", "v16", "v17", "v18", "v19", "v20", "v21", "v22", "v23", "v24", "v25", "v26", "v27", "v28", "v29", "v30", "v31", "v32", "v33", "v34", "v35", "v36", "v37", "v38", "v39", "v40", "v41", "v42", "v43", "v44", "v45", "v46", "v47", "v48", "v49", "v50", "v51", "v52", "v53", "v54", "v55", "v56", "v57", "v58", "v59", "v60", "v61", "v62", "v63", "v64", "v65", "v66", "v67", "v68", "v69", "v70", "v71", "v72", "v73", "v74", "v75", "v76", "v77", "v78", "v79", "v80", "v81", "v82", "v83", "v84", "v85", "v86", "v87", "v88", "v89", "v90", "v91", "v92", "v93", "v94", "v95", "v96", "v97", "v98", "v99", "v100", "v101", "v102", "v103", "v104", "v105", "v106", "v107", "v108", "v109", "v110", "v111", "v112", "v113", "v114", "v115", "v116", "v117", "v118", "v119", "v120", "v121", "v122", "v123", "v124", "v125", "v126", "v127", "v128", "v129", "v130", "v131", "v132", "v133", "v134", "v135", "v136", "v137", "v138", "v139", "v140", "v141", "v142", "v143",  "m0", "s60", "s61", "s62", "s63", "s64", "s65", "s66", "s67", "s81", "s82", "s83", "s90", "s91", "s49", "vcc", "scc", "memory");

        }
        if (wave == 0 && blockIdx.x == 0) {
            int ln_;
            asm volatile("v_mbcnt_lo_u32_b32 %0, -1, 0\n\tv_mbcnt_hi_u32_b32 %0, -1, %0" : "=v"(ln_));
            KARGS
            unsigned long long tg;
            asm volatile("s_load_dwordx2 %0, %1, %2\n\ts_waitcnt lgkmcnt(0)" : "=s"(tg) : "s"(wb), "n"(EPO_OFF) : "memory");
            const unsigned lo = (unsigned)tg + 1u, hi = (unsigned)(tg >> 32);
            const auto wq = __builtin_amdgcn_make_buffer_rsrc((void*)wb, 0, WS_BYTES, 0x00020000);
#pragma unroll 1
            for (int it_ = 0; it_ < 100000; ++it_) {
                bool bad = false;
#pragma unroll
                for (int k = 0; k < 2; ++k) {
                    const u32x4 a_ = __builtin_amdgcn_raw_buffer_load_b128(wq, ARR_OFF + ln_ * 16, k * 1024, SC1);
                    bad |= a_[0] != lo || a_[1] != hi || a_[2] != lo || a_[3] != hi;
                }
                if (__builtin_amdgcn_ballot_w64(bad) == 0ull) break;
                __builtin_amdgcn_s_sleep(2);
            }
            if (ln_ == 0) __builtin_amdgcn_raw_buffer_store_b64((u32x2){lo, hi}, wq, EPO_OFF, 0, SC1);
        }
        asm volatile("wk_code_end:");
        __builtin_amdgcn_endpgm();
    } else if (wave < 6) {
        LANE_SETUP
        {
            const unsigned rsb = ldsb + RSX_OFF;
            const unsigned pE_ = fE - X_OFF;
            const unsigned aVAw_ = ldsb + V_OFF + w * 9216 + lane * 16, aVBw_ = ldsb + V_OFF + w * 9216 + 1024 + lane * 8;
            const unsigned pST_ = lane * 16;
            unsigned aRSw_ = rsb + 256 + lane * 4, aRSr_ = rsb + (lane & 48);
            B_SCALARS
            unsigned s44 = 4;
            asm volatile(
            "s_waitcnt lgkmcnt(0)\n\t"
            "s_mov_b32 s83, 0x10000\n\t"
            "s_mov_b32 s84, -47104\n\t"
            "s_nop 7\n\t"
            "v_mov_b32 v200, v250\n\t"
            "v_mov_b32 v201, v251\n\t"
            "v_mov_b32 v202, v253\n\t"
            "s_mov_b32 s38, s67\n\t"
            "s_mov_b32 s67, 74496\n\t"
            "ds_read_b64 v[250:251], v203\n\t"
            "ds_read_b32 v253, v203 offset:8\n\t"
            ".Lwk_b1%=:\n\t"
            "v_mov_b32 v236, 0\n\t"
            "ds_read_b32 v236, v236 offset:7864\n\t"
            "s_waitcnt lgkmcnt(0)\n\t"
            "v_readfirstlane_b32 s77, v236\n\t"
            "s_cmp_ge_u32 s77, 4\n\t"
            "s_cbranch_scc1 .Lwk_b2%=\n\t"
            "s_cmp_eq_u32 s83, 0\n\t"
            "s_cbranch_scc1 .Lwk_b2%=\n\t"
            "s_sub_u32 s83, s83, 1\n\t"
            "s_sleep 1\n\t"
            "s_branch .Lwk_b1%=\n\t"
            ".Lwk_b2%=:\n\t"
            "s_waitcnt vmcnt(0)\n\t"
            "s_call_b64 vcc, wk_conv\n\t"
            "v_mov_b32 v250, v200\n\t"
            "v_mov_b32 v251, v201\n\t"
            "v_mov_b32 v253, v202\n\t"
            "s_mov_b32 s67, s38\n\t"
            "s_setprio 3\n\t"
            "s_barrier\n\t"
            "buffer_load_dwordx4 v[140:143], v245, s[28:31], s82 offen sc1\n\t"
            "s_add_u32 s49, s33, 0\n\t"
            "buffer_load_dwordx4 v[0:3], v245, s[28:31], s49 offen sc1\n\t"
            "s_add_u32 s49, s33, 6144\n\t"
            "buffer_load_dwordx4 v[24:27], v245, s[28:31], s49 offen sc1\n\t"
            "s_add_u32 s49, s33, 12288\n\t"
            "buffer_load_dwordx4 v[48:51], v245, s[28:31], s49 offen sc1\n\t"
            "s_add_u32 s49, s33, 18432\n\t"
            "buffer_load_dwordx4 v[72:75], v245, s[28:31], s49 offen sc1\n\t"
            "s_add_u32 s49, s33, 24576\n\t"
            "buffer_load_dwordx4 v[96:99], v245, s[28:31], s49 offen sc1\n\t"
            "s_add_u32 s49, s33, 30720\n\t"
            "buffer_load_dwordx4 v[120:123], v245, s[28:31], s49 offen sc1\n\t"
            "s_add_u32 s49, s33, 1024\n\t"
            "buffer_load_dwordx4 v[4:7], v245, s[28:31], s49 offen sc1\n\t"
            "s_add_u32 s49, s33, 7168\n\t"
            "buffer_load_dwordx4 v[28:31], v245, s[28:31], s49 offen sc1\n\t"
            "s_add_u32 s49, s33, 13312\n\t"
            "buffer_load_dwordx4 v[52:55], v245, s[28:31], s49 offen sc1\n\t"
            "s_add_u32 s49, s33, 19456\n\t"
            "buffer_load_dwordx4 v[76:79], v245, s[28:31], s49 offen sc1\n\t"
            "s_add_u32 s49, s33, 25600\n\t"
            "buffer_load_dwordx4 v[100:103], v245, s[28:31], s49 offen sc1\n\t"
            "s_add_u32 s49, s33, 31744\n\t"
            "buffer_load_dwordx4 v[124:127], v245, s[28:31], s49 offen sc1\n\t"
            "s_add_u32 s49, s33, 2048\n\t"
            "buffer_load_dwordx4 v[8:11], v245, s[28:31], s49 offen sc1\n\t"
            "s_add_u32 s49, s33, 8192\n\t"
            "buffer_load_dwordx4 v[32:35], v245, s[28:31], s49 offen sc1\n\t"
            "s_add_u32 s49, s33, 14336\n\t"
            "buffer_load_dwordx4 v[56:59], v245, s[28:31], s49 offen sc1\n\t"
            "s_add_u32 s49, s33, 20480\n\t"
            "buffer_load_dwordx4 v[80:83], v245, s[28:31], s49 offen sc1\n\t"
            "s_add_u32 s49, s33, 26624\n\t"
            "buffer_load_dwordx4 v[104:107], v245, s[28:31], s49 offen sc1\n\t"
            "s_add_u32 s49, s33, 32768\n\t"
            "buffer_load_dwordx4 v[128:131], v245, s[28:31], s49 offen sc1\n\t"
            "s_add_u32 s49, s33, 3072\n\t"
            "buffer_load_dwordx4 v[12:15], v245, s[28:31], s49 offen sc1\n\t"
            "s_add_u32 s49, s33, 9216\n\t"
            "buffer_load_dwordx4 v[36:39], v245, s[28:31], s49 offen sc1\n\t"
            "s_add_u32 s49, s33, 15360\n\t"
            "buffer_load_dwordx4 v[60:63], v245, s[28:31], s49 offen sc1\n\t"
            "s_add_u32 s49, s33, 21504\n\t"
            "buffer_load_dwordx4 v[84:87], v245, s[28:31], s49 offen sc1\n\t"
            "s_add_u32 s49, s33, 27648\n\t"
            "buffer_load_dwordx4 v[108:111], v245, s[28:31], s49 offen sc1\n\t"
            "s_add_u32 s49, s33, 33792\n\t"
            "buffer_load_dwordx4 v[132:135], v245, s[28:31], s49 offen sc1\n\t"
            "s_add_u32 s49, s33, 4096\n\t"
            "buffer_load_dwordx4 v[16:19], v245, s[28:31], s49 offen sc1\n\t"
            "s_add_u32 s49, s33, 10240\n\t"
            "buffer_load_dwordx4 v[40:43], v245, s[28:31], s49 offen sc1\n\t"
            "s_add_u32 s49, s33, 16384\n\t"
            "buffer_load_dwordx4 v[64:67], v245, s[28:31], s49 offen sc1\n\t"
            "s_add_u32 s49, s33, 22528\n\t"
            "buffer_load_dwordx4 v[88:91], v245, s[28:31], s49 offen sc1\n\t"
            "s_add_u32 s49, s33, 28672\n\t"
            "buffer_load_dwordx4 v[112:115], v245, s[28:31], s49 offen sc1\n\t"
            "s_add_u32 s49, s33, 34816\n\t"
            "buffer_load_dwordx4 v[136:139], v245, s[28:31], s49 offen sc1\n\t"
            "s_add_u32 s49, s33, 5120\n\t"
            "buffer_load_dwordx4 v[20:23], v245, s[28:31], s49 offen sc1\n\t"
            "s_add_u32 s49, s33, 11264\n\t"
            "buffer_load_dwordx4 v[44:47], v245, s[28:31], s49 offen sc1\n\t"
            "s_add_u32 s49, s33, 17408\n\t"
            "buffer_load_dwordx4 v[68:71], v245, s[28:31], s49 offen sc1\n\t"
            "s_add_u32 s49, s33, 23552\n\t"
            "buffer_load_dwordx4 v[92:95], v245, s[28:31], s49 offen sc1\n\t"
            "s_add_u32 s49, s33, 29696\n\t"
            "buffer_load_dwordx4 v[116:119], v245, s[28:31], s49 offen sc1\n\t"
            "s_add_u32 s49, s33, 35840\n\t"
            "buffer_load_dwordx4 v[140:143], v245, s[28:31], s49 offen sc1\n\t"
            "ds_read_b32 v249, v252 offset:12\n\t"
            "s_waitcnt lgkmcnt(0)\n\t"
            "v_and_b32 v249, s69, v249\n\t"
            "s_mov_b32 exec_hi, 0x3fffff\n\t"
            "s_add_u32 m0, s66, 0\n\t"
            "s_add_u32 s49, s79, 0\n\t"
            "buffer_load_dwordx4 v249, s[72:75], s49 offen lds\n\t"
            "s_mov_b32 exec_hi, -1\n\t"
            "s_mov_b32 exec_hi, 0x3fffff\n\t"
            "s_add_u32 m0, s66, 872\n\t"
            "s_add_u32 s49, s47, 196224\n\t"
            "buffer_load_dwordx4 v249, s[72:75], s49 offen lds\n\t"
            "s_mov_b32 exec_hi, -1\n\t"
            "s_mov_b32 exec_hi, 0x3fffff\n\t"
            "s_add_u32 m0, s66, 1744\n\t"
            "s_add_u32 s49, s47, 392448\n\t"
            "buffer_load_dwordx4 v249, s[72:75], s49 offen lds\n\t"
            "s_mov_b32 exec_hi, -1\n\t"
            "s_mov_b32 exec_hi, 0x3fffff\n\t"
            "s_add_u32 m0, s66, 2616\n\t"
            "s_add_u32 s49, s47, 588672\n\t"
            "buffer_load_dwordx4 v249, s[72:75], s49 offen lds\n\t"
            "s_mov_b32 exec_hi, -1\n\t"
            "s_mov_b32 exec_hi, 0x3fffff\n\t"
            "s_add_u32 m0, s66, 3488\n\t"
            "s_add_u32 s49, s47, 784896\n\t"
            "buffer_load_dwordx4 v249, s[72:75], s49 offen lds\n\t"
            "s_mov_b32 exec_hi, -1\n\t"
            "s_mov_b32 exec_hi, 0x3fffff\n\t"
            "s_add_u32 m0, s66, 4360\n\t"
            "s_add_u32 s49, s47, 981120\n\t"
            "buffer_load_dwordx4 v249, s[72:75], s49 offen lds\n\t"
            "s_mov_b32 exec_hi, -1\n\t"
            "s_mov_b32 exec_hi, 0x3fffff\n\t"
            "s_add_u32 m0, s66, 5232\n\t"
            "s_add_u32 s49, s47, 1177344\n\t"
            "buffer_load_dwordx4 v249, s[72:75], s49 offen lds\n\t"
            "s_mov_b32 exec_hi, -1\n\t"
            "s_mov_b32 exec_hi, 0x3fffff\n\t"
            "s_add_u32 m0, s66, 6104\n\t"
            "s_add_u32 s49, s47, 1373568\n\t"
            "buffer_load_dwordx4 v249, s[72:75], s49 offen lds\n\t"
            "s_mov_b32 exec_hi, -1\n\t"
            "s_mov_b32 exec_hi, 0x3fffff\n\t"
            "s_add_u32 m0, s66, 6976\n\t"
            "s_add_u32 s49, s47, 1569792\n\t"
            "buffer_load_dwordx4 v249, s[72:75], s49 offen lds\n\t"
            "s_mov_b32 exec_hi, -1\n\t"
            "ds_read_b128 v[168:171], v254\n\t"
            "ds_read_b128 v[172:175], v254 offset:64\n\t"
            "v_add_u32 v180, s48, v245\n\t"
            "v_add_u32 v232, s48, v242\n\t"
            "v_xor_b32 v233, 64, v232\n\t"
            "v_xor_b32 v246, 0x100, v246\n\t"
            "ds_read_b128 v[176:179], v180 offset:18816\n\t"
            "ds_read_b128 v[180:183], v180 offset:19840\n\t"
            "ds_read_b128 v[192:195], v232\n\t"
            "ds_read_b128 v[196:199], v233\n\t"
            "ds_read_b128 v[224:227], v232 offset:128\n\t"
            "ds_read_b128 v[228:231], v233 offset:128\n\t"
            "s_waitcnt lgkmcnt(5)\n\t"
            "v_add_f32 v176, v176, v178\n\t"
            "s_waitcnt lgkmcnt(4)\n\t"
            "v_add_f32 v180, v180, v182\n\t"
            "v_add_f32 v176, v176, v180\n\t"
            "v_add_f32 v177, v177, v179\n\t"
            "v_add_f32 v181, v181, v183\n\t"
            "v_mul_f32 v176, 0x3baaaaab, v176\n\t"
            "v_add_f32 v177, v177, v181\n\t"
            "v_mul_f32_e64 v176, v176, -v176\n\t"
            "v_fmac_f32 v176, 0x3baaaaab, v177\n\t"
            "v_max_f32 v176, 0, v176\n\t"
            "v_add_f32 v176, 0x3727c5ac, v176\n\t"
            "v_rsq_f32 v176, v176\n\t"
            "s_nop 0\n\t"
            "ds_write_b32 v246, v176\n\t"
            "s_setprio 3\n\t"
            "s_waitcnt vmcnt(9)\n\t"
            "s_waitcnt lgkmcnt(3)\n\t"
            "v_mfma_f32_16x16x32_bf16 v[200:203], v[192:195], v[0:3], 0\n\t"
            "v_mfma_f32_16x16x32_bf16 v[200:203], v[196:199], v[4:7], v[200:203]\n\t"
            "v_mfma_f32_16x16x32_bf16 v[204:207], v[196:199], v[28:31], 0\n\t"
            "v_mfma_f32_16x16x32_bf16 v[204:207], v[192:195], v[24:27], v[204:207]\n\t"
            "v_mfma_f32_16x16x32_bf16 v[208:211], v[192:195], v[48:51], 0\n\t"
            "v_mfma_f32_16x16x32_bf16 v[208:211], v[196:199], v[52:55], v[208:211]\n\t"
            "v_mfma_f32_16x16x32_bf16 v[212:215], v[196:199], v[76:79], 0\n\t"
            "v_mfma_f32_16x16x32_bf16 v[212:215], v[192:195], v[72:75], v[212:215]\n\t"
            "v_mfma_f32_16x16x32_bf16 v[216:219], v[192:195], v[96:99], 0\n\t"
            "v_mfma_f32_16x16x32_bf16 v[216:219], v[196:199], v[100:103], v[216:219]\n\t"
            "v_mfma_f32_16x16x32_bf16 v[220:223], v[196:199], v[124:127], 0\n\t"
            "v_mfma_f32_16x16x32_bf16 v[220:223], v[192:195], v[120:123], v[220:223]\n\t"
            "s_waitcnt lgkmcnt(1)\n\t"
            "v_mfma_f32_16x16x32_bf16 v[200:203], v[224:227], v[8:11], v[200:203]\n\t"
            "v_mfma_f32_16x16x32_bf16 v[200:203], v[228:231], v[12:15], v[200:203]\n\t"
            "v_mfma_f32_16x16x32_bf16 v[204:207], v[228:231], v[36:39], v[204:207]\n\t"
            "v_mfma_f32_16x16x32_bf16 v[204:207], v[224:227], v[32:35], v[204:207]\n\t"
            "v_mfma_f32_16x16x32_bf16 v[208:211], v[224:227], v[56:59], v[208:211]\n\t"
            "v_mfma_f32_16x16x32_bf16 v[208:211], v[228:231], v[60:63], v[208:211]\n\t"
            "v_mfma_f32_16x16x32_bf16 v[212:215], v[228:231], v[84:87], v[212:215]\n\t"
            "v_mfma_f32_16x16x32_bf16 v[212:215], v[224:227], v[80:83], v[212:215]\n\t"
            "v_mfma_f32_16x16x32_bf16 v[216:219], v[224:227], v[104:107], v[216:219]\n\t"
            "s_waitcnt vmcnt(0)\n\t"
            "ds_read_b64 v[144:145], v253\n\t"
            "v_mfma_f32_16x16x32_bf16 v[216:219], v[228:231], v[108:111], v[216:219]\n\t"
            "ds_read_b64 v[146:147], v253 offset:96\n\t"
            "ds_read_b64 v[148:149], v253 offset:1744\n\t"
            "v_mfma_f32_16x16x32_bf16 v[220:223], v[228:231], v[132:135], v[220:223]\n\t"
            "ds_read_b128 v[228:231], v233 offset:256\n\t"
            "ds_read_b64 v[150:151], v253 offset:1840\n\t"
            "v_mfma_f32_16x16x32_bf16 v[220:223], v[224:227], v[128:131], v[220:223]\n\t"
            "ds_read_b128 v[224:227], v232 offset:256\n\t"
            "ds_read_b64 v[152:153], v253 offset:8\n\t"
            "ds_read_b64 v[154:155], v253 offset:104\n\t"
            "ds_read_b64 v[156:157], v253 offset:1752\n\t"
            "ds_read_b64 v[158:159], v253 offset:1848\n\t"
            "ds_read_b64 v[160:161], v253 offset:16\n\t"
            "ds_read_b64 v[162:163], v253 offset:112\n\t"
            "ds_read_b64 v[164:165], v253 offset:1760\n\t"
            "ds_read_b64 v[166:167], v253 offset:1856\n\t"
            "s_waitcnt lgkmcnt(0)\n\t"
            "s_nop 7\n\t"
            ".p2align 6\n\t"
            "s_nop 0\n\t"
            "s_nop 0\n\t"
            "s_nop 0\n\t"
            "s_nop 0\n\t"
            ".Lwc_loop%=:\n\t"
            "s_setprio 3\n\t"
            "v_mfma_f32_16x16x32_bf16 v[200:203], v[224:227], v[16:19], v[200:203]\n\t"
            "v_mov_b64 v[176:177], 0\n\t"
            "v_mov_b64 v[178:179], 0\n\t"
            "v_mfma_f32_16x16x32_bf16 v[200:203], v[228:231], v[20:23], v[200:203]\n\t"
            "v_mov_b64 v[180:181], 0\n\t"
            "v_mov_b64 v[182:183], 0\n\t"
            "s_waitcnt lgkmcnt(8)\n\t"
            "v_smfmac_f32_16x16x64_bf16 v[176:179], v[168:171], v[144:151], v255\n\t"
            "ds_read_b128 v[168:171], v254 offset:128\n\t"
            "s_waitcnt lgkmcnt(5)\n\t"
            "v_smfmac_f32_16x16x64_bf16 v[180:183], v[172:175], v[152:159], v255\n\t"
            "ds_read_b64 v[144:145], v253 offset:24\n\t"
            "v_mfma_f32_16x16x32_bf16 v[204:207], v[228:231], v[44:47], v[204:207]\n\t"
            "ds_read_b64 v[146:147], v253 offset:120\n\t"
            "ds_read_b64 v[148:149], v253 offset:1768\n\t"
            "v_mfma_f32_16x16x32_bf16 v[204:207], v[224:227], v[40:43], v[204:207]\n\t"
            "ds_read_b64 v[150:151], v253 offset:1864\n\t"
            "ds_read_b128 v[172:175], v254 offset:192\n\t"
            "v_mfma_f32_16x16x32_bf16 v[208:211], v[224:227], v[64:67], v[208:211]\n\t"
            "ds_read_b64 v[152:153], v253 offset:32\n\t"
            "ds_read_b64 v[154:155], v253 offset:128\n\t"
            "v_mfma_f32_16x16x32_bf16 v[208:211], v[228:231], v[68:71], v[208:211]\n\t"
            "ds_read_b64 v[156:157], v253 offset:1776\n\t"
            "ds_read_b64 v[158:159], v253 offset:1872\n\t"
            "v_mfma_f32_16x16x32_bf16 v[212:215], v[228:231], v[92:95], v[212:215]\n\t"
            "v_mov_b32 v249, s76\n\t"
            "v_mov_b64 v[184:185], 0\n\t"
            "v_mfma_f32_16x16x32_bf16 v[212:215], v[224:227], v[88:91], v[212:215]\n\t"
            "ds_read_b32 v249, v249\n\t"
            "v_mov_b64 v[186:187], 0\n\t"
            "v_mfma_f32_16x16x32_bf16 v[216:219], v[224:227], v[112:115], v[216:219]\n\t"
            "v_mov_b64 v[188:189], 0\n\t"
            "v_mov_b64 v[190:191], 0\n\t"
            "v_mfma_f32_16x16x32_bf16 v[216:219], v[228:231], v[116:119], v[216:219]\n\t"
            "v_cvt_pk_bf16_f32 v176, v176, v177\n\t"
            "v_add_u32 v236, s67, v250\n\t"
            "s_waitcnt lgkmcnt(10)\n\t"
            "v_smfmac_f32_16x16x64_bf16 v[184:187], v[168:171], v[160:167], v255\n\t"
            "ds_read_b128 v[168:171], v254 offset:256\n\t"
            "s_waitcnt lgkmcnt(6)\n\t"
            "v_smfmac_f32_16x16x64_bf16 v[188:191], v[172:175], v[144:151], v255\n\t"
            "ds_read_b64 v[160:161], v253 offset:40\n\t"
            "v_mfma_f32_16x16x32_bf16 v[220:223], v[228:231], v[140:143], v[220:223]\n\t"
            "ds_read_b64 v[162:163], v253 offset:136\n\t"
            "ds_read_b64 v[164:165], v253 offset:1784\n\t"
            "v_mfma_f32_16x16x32_bf16 v[220:223], v[224:227], v[136:139], v[220:223]\n\t"
            "s_setprio 1\n\t"
            "ds_read_b64 v[166:167], v253 offset:1880\n\t"
            "ds_read_b128 v[172:175], v254 offset:320\n\t"
            "ds_read_b64 v[144:145], v253 offset:48\n\t"
            "ds_read_b64 v[146:147], v253 offset:144\n\t"
            "ds_read_b64 v[148:149], v253 offset:1792\n\t"
            "ds_read_b64 v[150:151], v253 offset:1888\n\t"
            "s_waitcnt lgkmcnt(14)\n\t"
            "ds_read_b128 v[224:227], v232 offset:6272\n\t"
            "s_waitcnt lgkmcnt(14)\n\t"
            "ds_read_b128 v[228:231], v233 offset:6272\n\t"
            "s_waitcnt lgkmcnt(12)\n\t"
            "v_readfirstlane_b32 s26, v249\n\t"
            "v_cvt_pk_bf16_f32 v177, v178, v179\n\t"
            "v_cvt_pk_bf16_f32 v178, v180, v181\n\t"
            "s_and_b32 s68, s26, 0x7ffffff\n\t"
            "s_add_u32 s68, s68, s78\n\t"
            "s_and_b32 s69, s26, 0x38000000\n\t"
            "s_or_b32 s69, s69, s39\n\t"
            "s_bitcmp1_b32 s26, 27\n\t"
            "s_cselect_b32 s79, 2.0, s68\n\t"
            "s_setprio 3\n\t"
            "v_mfma_f32_4x4x4_16b_bf16 v[196:199], v[176:177], v[176:177], 0\n\t"
            "s_setprio 1\n\t"
            "ds_read_b32 v249, v252 offset:12\n\t"
            "s_and_b32 s57, s56, 3\n\t"
            "s_lshl_b32 s58, s57, 3\n\t"
            "s_lshr_b32 m0, s54, s58\n\t"
            "s_lshl_b32 s58, s57, 4\n\t"
            "s_lshr_b64 s[36:37], s[50:51], s58\n\t"
            "s_and_b32 s57, s36, 0xffff\n\t"
            "s_add_u32 s57, s57, s67\n\t"
            "v_writelane_b32 v236, s57, 63\n\t"
            "v_writelane_b32 v236, s59, m0\n\t"
            "v_cvt_pk_bf16_f32 v179, v182, v183\n\t"
            "v_mov_b64 v[180:181], 0\n\t"
            "v_mov_b64 v[182:183], 0\n\t"
            "s_setprio 3\n\t"
            "v_mfma_f32_16x16x32_bf16 v[192:195], v[238:241], v[176:179], 0\n\t"
            "s_lshr_b64 s[36:37], s[52:53], s58\n\t"
            "s_and_b32 s58, s36, 0xffff\n\t"
            "s_add_u32 s58, s58, s67\n\t"
            "v_mfma_f32_4x4x4_16b_bf16 v[196:199], v[178:179], v[178:179], v[196:199]\n\t"
            "ds_write_b128 v236, v[176:179]\n\t"
            "s_waitcnt lgkmcnt(8)\n\t"
            "v_smfmac_f32_16x16x64_bf16 v[180:183], v[172:175], v[160:167], v255\n\t"
            "s_setprio 1\n\t"
            "s_and_b32 s38, s52, 0xffff\n\t"
            "s_add_u32 s38, s38, s67\n\t"
            "ds_read_b128 v[172:175], v254 offset:448\n\t"
            "ds_read_b64 v[160:161], v253 offset:64\n\t"
            "ds_read_b64 v[162:163], v253 offset:160\n\t"
            "ds_read_b64 v[164:165], v253 offset:1808\n\t"
            "ds_read_b64 v[166:167], v253 offset:1904\n\t"
            "v_mov_b64 v[176:177], 0\n\t"
            "v_mov_b64 v[178:179], 0\n\t"
            "v_cvt_pk_bf16_f32 v184, v184, v185\n\t"
            "v_cvt_pk_bf16_f32 v185, v186, v187\n\t"
            "s_setprio 3\n\t"
            "v_smfmac_f32_16x16x64_bf16 v[176:179], v[168:171], v[152:159], v255\n\t"
            "ds_read_b128 v[168:171], v254 offset:384\n\t"
            "v_mfma_f32_4x4x4_16b_bf16 v[196:199], v[184:185], v[184:185], v[196:199]\n\t"
            "s_setprio 1\n\t"
            "ds_read_b64 v[152:153], v253 offset:56\n\t"
            "s_waitcnt lgkmcnt(14)\n\t"
            "ds_read_b64 v[154:155], v253 offset:152\n\t"
            "s_waitcnt lgkmcnt(14)\n\t"
            "ds_read_b64 v[156:157], v253 offset:1800\n\t"
            "s_waitcnt lgkmcnt(14)\n\t"
            "ds_read_b64 v[158:159], v253 offset:1896\n\t"
            "v_xor_b32 v237, 16, v236\n\t"
            "v_cvt_pk_bf16_f32 v186, v188, v189\n\t"
            "v_cvt_pk_bf16_f32 v187, v190, v191\n\t"
            "v_mov_b64 v[188:189], 0\n\t"
            "v_mov_b64 v[190:191], 0\n\t"
            "s_setprio 3\n\t"
            "v_mfma_f32_16x16x32_bf16 v[192:195], v[238:241], v[184:187], v[192:195]\n\t"
            "s_waitcnt lgkmcnt(14)\n\t"
            "ds_write_b128 v237, v[184:187]\n\t"
            "v_mfma_f32_4x4x4_16b_bf16 v[196:199], v[186:187], v[186:187], v[196:199]\n\t"
            "s_setprio 1\n\t"
            "v_mov_b64 v[184:185], 0\n\t"
            "v_mov_b64 v[186:187], 0\n\t"
            "v_cvt_pk_bf16_f32 v176, v176, v177\n\t"
            "v_cvt_pk_bf16_f32 v177, v178, v179\n\t"
            "v_cvt_pk_bf16_f32 v178, v180, v181\n\t"
            "v_cvt_pk_bf16_f32 v179, v182, v183\n\t"
            "s_setprio 3\n\t"
            "v_mfma_f32_4x4x4_16b_bf16 v[196:199], v[176:177], v[176:177], v[196:199]\n\t"
            "s_waitcnt lgkmcnt(14)\n\t"
            "ds_write_b128 v236, v[176:179] offset:128\n\t"
            "s_waitcnt lgkmcnt(6)\n\t"
            "v_smfmac_f32_16x16x64_bf16 v[184:187], v[168:171], v[144:151], v255\n\t"
            "ds_read_b128 v[168:171], v254 offset:512\n\t"
            "s_waitcnt lgkmcnt(3)\n\t"
            "v_smfmac_f32_16x16x64_bf16 v[188:191], v[172:175], v[152:159], v255\n\t"
            "ds_read_b64 v[144:145], v253 offset:72\n\t"
            "v_mfma_f32_16x16x32_bf16 v[192:195], v[238:241], v[176:179], v[192:195]\n\t"
            "ds_read_b64 v[146:147], v253 offset:168\n\t"
            "ds_read_b64 v[148:149], v253 offset:1816\n\t"
            "v_mfma_f32_4x4x4_16b_bf16 v[196:199], v[178:179], v[178:179], v[196:199]\n\t"
            "s_setprio 1\n\t"
            "ds_read_b64 v[150:151], v253 offset:1912\n\t"
            "ds_read_b128 v[172:175], v254 offset:576\n\t"
            "ds_read_b64 v[152:153], v253 offset:80\n\t"
            "ds_read_b64 v[154:155], v253 offset:176\n\t"
            "ds_read_b64 v[156:157], v253 offset:1824\n\t"
            "ds_read_b64 v[158:159], v253 offset:1920\n\t"
            "v_mov_b64 v[176:177], 0\n\t"
            "v_mov_b64 v[178:179], 0\n\t"
            "v_mov_b64 v[180:181], 0\n\t"
            "v_mov_b64 v[182:183], 0\n\t"
            "v_cvt_pk_bf16_f32 v184, v184, v185\n\t"
            "v_cvt_pk_bf16_f32 v185, v186, v187\n\t"
            "v_cvt_pk_bf16_f32 v186, v188, v189\n\t"
            "v_cvt_pk_bf16_f32 v187, v190, v191\n\t"
            "s_setprio 3\n\t"
            "v_mfma_f32_4x4x4_16b_bf16 v[196:199], v[184:185], v[184:185], v[196:199]\n\t"
            "ds_write_b128 v237, v[184:187] offset:128\n\t"
            "s_waitcnt lgkmcnt(10)\n\t"
            "v_smfmac_f32_16x16x64_bf16 v[176:179], v[168:171], v[160:167], v255\n\t"
            "ds_read_b128 v[168:171], v254 offset:640\n\t"
            "v_mfma_f32_16x16x32_bf16 v[192:195], v[238:241], v[184:187], v[192:195]\n\t"
            "ds_read_b64 v[160:161], v253 offset:88\n\t"
            "ds_read_b64 v[162:163], v253 offset:184\n\t"
            "s_waitcnt lgkmcnt(8)\n\t"
            "v_smfmac_f32_16x16x64_bf16 v[180:183], v[172:175], v[144:151], v255\n\t"
            "ds_read_b64 v[164:165], v253 offset:1832\n\t"
            "v_mfma_f32_4x4x4_16b_bf16 v[196:199], v[186:187], v[186:187], v[196:199]\n\t"
            "s_setprio 1\n\t"
            "ds_read_b64 v[166:167], v253 offset:1928\n\t"
            "ds_read_b128 v[172:175], v254 offset:704\n\t"
            "v_mov_b64 v[184:185], 0\n\t"
            "v_mov_b64 v[186:187], 0\n\t"
            "v_mov_b64 v[188:189], 0\n\t"
            "v_mov_b64 v[190:191], 0\n\t"
            "v_add_u32 v248, s67, v251\n\t"
            "v_writelane_b32 v248, s58, 63\n\t"
            "v_writelane_b32 v248, s38, m0\n\t"
            "v_and_b32 v249, s69, v249\n\t"
            "v_cvt_pk_bf16_f32 v176, v176, v177\n\t"
            "v_cvt_pk_bf16_f32 v177, v178, v179\n\t"
            "v_cvt_pk_bf16_f32 v178, v180, v181\n\t"
            "v_cvt_pk_bf16_f32 v179, v182, v183\n\t"
            "s_setprio 3\n\t"
            "v_mfma_f32_4x4x4_16b_bf16 v[196:199], v[176:177], v[176:177], v[196:199]\n\t"
            "ds_write_b128 v236, v[176:179] offset:256\n\t"
            "s_waitcnt lgkmcnt(6)\n\t"
            "v_smfmac_f32_16x16x64_bf16 v[184:187], v[168:171], v[152:159], v255\n\t"
            "v_mov_b32 v234, 0\n\t"
            "v_mfma_f32_4x4x4_16b_bf16 v[196:199], v[178:179], v[178:179], v[196:199]\n\t"
            "v_xor_b32 v246, 0x100, v246\n\t"
            "v_mfma_f32_16x16x32_bf16 v[192:195], v[238:241], v[176:179], v[192:195]\n\t"
            "s_waitcnt lgkmcnt(1)\n\t"
            "v_smfmac_f32_16x16x64_bf16 v[188:191], v[172:175], v[160:167], v255\n\t"
            "s_mov_b32 exec_hi, 0x3fffff\n\t"
            "s_add_u32 m0, s66, 0\n\t"
            "s_add_u32 s49, s79, 0\n\t"
            "buffer_load_dwordx4 v249, s[72:75], s49 offen lds\n\t"
            "s_add_u32 s49, s68, 195352\n\t"
            "buffer_load_dwordx4 v249, s[72:75], s49 offen offset:872 lds\n\t"
            "s_add_u32 s49, s68, 390704\n\t"
            "buffer_load_dwordx4 v249, s[72:75], s49 offen offset:1744 lds\n\t"
            "s_add_u32 s49, s68, 586056\n\t"
            "buffer_load_dwordx4 v249, s[72:75], s49 offen offset:2616 lds\n\t"
            "s_add_u32 s49, s68, 781408\n\t"
            "buffer_load_dwordx4 v249, s[72:75], s49 offen offset:3488 lds\n\t"
            "s_mov_b32 exec_hi, -1\n\t"
            "v_cvt_pk_bf16_f32 v184, v184, v185\n\t"
            "v_cvt_pk_bf16_f32 v185, v186, v187\n\t"
            "v_cvt_pk_bf16_f32 v186, v188, v189\n\t"
            "v_cvt_pk_bf16_f32 v187, v190, v191\n\t"
            "v_mfma_f32_4x4x4_16b_bf16 v[196:199], v[184:185], v[184:185], v[196:199]\n\t"
            "s_mov_b32 exec_hi, 0x3fffff\n\t"
            "s_add_u32 m0, s66, 4360\n\t"
            "s_add_u32 s49, s68, 981120\n\t"
            "buffer_load_dwordx4 v249, s[72:75], s49 offen lds\n\t"
            "s_add_u32 s49, s68, 1176472\n\t"
            "buffer_load_dwordx4 v249, s[72:75], s49 offen offset:872 lds\n\t"
            "s_add_u32 s49, s68, 1371824\n\t"
            "buffer_load_dwordx4 v249, s[72:75], s49 offen offset:1744 lds\n\t"
            "s_add_u32 s49, s68, 1567176\n\t"
            "buffer_load_dwordx4 v249, s[72:75], s49 offen offset:2616 lds\n\t"
            "s_mov_b32 exec_hi, -1\n\t"
            "v_mfma_f32_4x4x4_16b_bf16 v[196:199], v[186:187], v[186:187], v[196:199]\n\t"
            "ds_write_b128 v237, v[184:187] offset:256\n\t"
            "v_mfma_f32_16x16x32_bf16 v[192:195], v[238:241], v[184:187], v[192:195]\n\t"
            "s_setprio 1\n\t"
            "ds_read_b64 v[236:237], v247\n\t"
            "s_nop 5\n\t"
            "v_cndmask_b32_e64 v193, v196, v197, s[60:61]\n\t"
            "v_cndmask_b32_e64 v193, v193, v198, s[62:63]\n\t"
            "v_cndmask_b32_e64 v193, v193, v199, s[64:65]\n\t"
            "ds_read_b128 v[196:199], v233 offset:6144\n\t"
            "ds_write_b64 v248, v[192:193] offset:18816\n\t"
            "ds_read_b64 v[248:249], v247 offset:8\n\t"
            "ds_read_b128 v[192:195], v232 offset:6144\n\t"
            "s_mov_b64 exec, 1\n\t"
            "ds_inc_u32 v255, v238 offset:7860\n\t"
            "s_mov_b64 exec, -1\n\t"
            "s_waitcnt lgkmcnt(5)\n\t"
            "v_mul_f32 v200, v236, v200\n\t"
            "v_mul_f32 v201, v237, v201\n\t"
            "v_mul_f32 v204, v236, v204\n\t"
            "v_mul_f32 v205, v237, v205\n\t"
            "v_mul_f32 v208, v236, v208\n\t"
            "v_mul_f32 v209, v237, v209\n\t"
            "v_mul_f32 v212, v236, v212\n\t"
            "v_mul_f32 v213, v237, v213\n\t"
            "v_mul_f32 v216, v236, v216\n\t"
            "v_mul_f32 v217, v237, v217\n\t"
            "v_mul_f32 v220, v236, v220\n\t"
            "v_mul_f32 v221, v237, v221\n\t"
            "v_cvt_pk_bf16_f32 v212, v212, v213\n\t"
            "v_cvt_pk_bf16_f32 v208, v208, v209\n\t"
            "v_cvt_pk_bf16_f32 v200, v200, v201\n\t"
            "v_cvt_pk_bf16_f32 v204, v204, v205\n\t"
            "v_cvt_pk_bf16_f32 v216, v216, v217\n\t"
            "v_cvt_pk_bf16_f32 v220, v220, v221\n\t"
            "ds_read_b64 v[236:237], v247 offset:64\n\t"
            "s_waitcnt lgkmcnt(2)\n\t"
            "v_mfma_f32_16x16x32_bf16 v[144:147], v[192:195], v[0:3], 0\n\t"
            "v_mul_f32 v202, v248, v202\n\t"
            "v_mul_f32 v203, v249, v203\n\t"
            "v_mfma_f32_16x16x32_bf16 v[144:147], v[196:199], v[4:7], v[144:147]\n\t"
            "v_mul_f32 v206, v248, v206\n\t"
            "v_mul_f32 v207, v249, v207\n\t"
            "v_mfma_f32_16x16x32_bf16 v[148:151], v[196:199], v[28:31], 0\n\t"
            "v_mul_f32 v210, v248, v210\n\t"
            "v_mul_f32 v211, v249, v211\n\t"
            "v_mfma_f32_16x16x32_bf16 v[148:151], v[192:195], v[24:27], v[148:151]\n\t"
            "v_mul_f32 v214, v248, v214\n\t"
            "v_mul_f32 v215, v249, v215\n\t"
            "v_mfma_f32_16x16x32_bf16 v[168:171], v[192:195], v[48:51], 0\n\t"
            "v_mul_f32 v218, v248, v218\n\t"
            "v_mul_f32 v219, v249, v219\n\t"
            "v_mfma_f32_16x16x32_bf16 v[168:171], v[196:199], v[52:55], v[168:171]\n\t"
            "v_mul_f32 v222, v248, v222\n\t"
            "v_mul_f32 v223, v249, v223\n\t"
            "v_mfma_f32_16x16x32_bf16 v[172:175], v[196:199], v[76:79], 0\n\t"
            "ds_read_b64 v[248:249], v247 offset:72\n\t"
            "v_cvt_pk_bf16_f32 v213, v214, v215\n\t"
            "v_mfma_f32_16x16x32_bf16 v[172:175], v[192:195], v[72:75], v[172:175]\n\t"
            "v_cvt_pk_bf16_f32 v209, v210, v211\n\t"
            "v_cvt_pk_bf16_f32 v201, v202, v203\n\t"
            "v_mfma_f32_16x16x32_bf16 v[152:155], v[192:195], v[96:99], 0\n\t"
            "v_cvt_pk_bf16_f32 v205, v206, v207\n\t"
            "v_cvt_pk_bf16_f32 v217, v218, v219\n\t"
            "v_mfma_f32_16x16x32_bf16 v[152:155], v[196:199], v[100:103], v[152:155]\n\t"
            "v_cvt_pk_bf16_f32 v221, v222, v223\n\t"
            "v_mfma_f32_16x16x32_bf16 v[156:159], v[196:199], v[124:127], 0\n\t"
            "ds_read_b128 v[196:199], v233 offset:6400\n\t"
            "v_mfma_f32_16x16x32_bf16 v[156:159], v[192:195], v[120:123], v[156:159]\n\t"
            "ds_read_b128 v[192:195], v232 offset:6400\n\t"
            "v_mfma_f32_16x16x32_bf16 v[144:147], v[224:227], v[8:11], v[144:147]\n\t"
            "v_mfma_f32_16x16x32_bf16 v[144:147], v[228:231], v[12:15], v[144:147]\n\t"
            "v_mfma_f32_16x16x32_bf16 v[148:151], v[228:231], v[36:39], v[148:151]\n\t"
            "v_mfma_f32_16x16x32_bf16 v[148:151], v[224:227], v[32:35], v[148:151]\n\t"
            "v_mfma_f32_16x16x32_bf16 v[168:171], v[224:227], v[56:59], v[168:171]\n\t"
            "v_mfma_f32_16x16x32_bf16 v[168:171], v[228:231], v[60:63], v[168:171]\n\t"
            "v_mfma_f32_16x16x32_bf16 v[172:175], v[228:231], v[84:87], v[172:175]\n\t"
            "v_mfma_f32_16x16x32_bf16 v[172:175], v[224:227], v[80:83], v[172:175]\n\t"
            "v_mfma_f32_16x16x32_bf16 v[152:155], v[224:227], v[104:107], v[152:155]\n\t"
            "v_mfma_f32_16x16x32_bf16 v[152:155], v[228:231], v[108:111], v[152:155]\n\t"
            "v_mfma_f32_16x16x32_bf16 v[156:159], v[228:231], v[132:135], v[156:159]\n\t"
            "ds_read_b128 v[228:231], v233 offset:12288\n\t"
            "v_mfma_f32_16x16x32_bf16 v[156:159], v[224:227], v[128:131], v[156:159]\n\t"
            "ds_read_b128 v[224:227], v232 offset:12288\n\t"
            "s_waitcnt lgkmcnt(2)\n\t"
            "v_mfma_f32_16x16x32_bf16 v[144:147], v[192:195], v[16:19], v[144:147]\n\t"
            "v_mfma_f32_16x16x32_bf16 v[144:147], v[196:199], v[20:23], v[144:147]\n\t"
            "v_mfma_f32_16x16x32_bf16 v[148:151], v[196:199], v[44:47], v[148:151]\n\t"
            "v_mfma_f32_16x16x32_bf16 v[148:151], v[192:195], v[40:43], v[148:151]\n\t"
            "ds_read_b64 v[234:235], v234 offset:7856\n\t"
            "v_mfma_f32_16x16x32_bf16 v[168:171], v[192:195], v[64:67], v[168:171]\n\t"
            "v_mfma_f32_16x16x32_bf16 v[168:171], v[196:199], v[68:71], v[168:171]\n\t"
            "v_mfma_f32_16x16x32_bf16 v[172:175], v[196:199], v[92:95], v[172:175]\n\t"
            "s_nop 1\n\t"
            "v_mul_f32 v144, v236, v144\n\t"
            "v_mfma_f32_16x16x32_bf16 v[172:175], v[192:195], v[88:91], v[172:175]\n\t"
            "v_mul_f32 v145, v237, v145\n\t"
            "v_mul_f32 v146, v248, v146\n\t"
            "v_mfma_f32_16x16x32_bf16 v[152:155], v[192:195], v[112:115], v[152:155]\n\t"
            "v_mul_f32 v148, v236, v148\n\t"
            "v_mul_f32 v149, v237, v149\n\t"
            "v_mfma_f32_16x16x32_bf16 v[152:155], v[196:199], v[116:119], v[152:155]\n\t"
            "v_mul_f32 v168, v236, v168\n\t"
            "v_mul_f32 v169, v237, v169\n\t"
            "v_mfma_f32_16x16x32_bf16 v[156:159], v[196:199], v[140:143], v[156:159]\n\t"
            "ds_read_b128 v[196:199], v233 offset:12416\n\t"
            "v_mul_f32 v172, v236, v172\n\t"
            "v_mfma_f32_16x16x32_bf16 v[156:159], v[192:195], v[136:139], v[156:159]\n\t"
            "ds_read_b128 v[192:195], v232 offset:12416\n\t"
            "s_waitcnt lgkmcnt(2)\n\t"
            "v_readfirstlane_b32 s77, v234\n\t"
            "v_readfirstlane_b32 s38, v235\n\t"
            "s_add_u32 s77, s77, s38\n\t"
            "s_cmp_ge_u32 s77, s44\n\t"
            "s_cbranch_scc0 .Lwc_spin%=\n\t"
            ".Lwc_go%=:\n\t"
            "v_mfma_f32_16x16x32_bf16 v[160:163], v[224:227], v[0:3], 0\n\t"
            "s_add_u32 s59, s59, s84\n\t"
            "s_movk_i32 s84, 800\n\t"
            "v_mul_f32 v173, v237, v173\n\t"
            "v_mfma_f32_16x16x32_bf16 v[160:163], v[228:231], v[4:7], v[160:163]\n\t"
            "s_add_u32 s45, s46, s27\n\t"
            "s_mov_b32 s46, s47\n\t"
            "s_mov_b32 s47, s68\n\t"
            "v_mfma_f32_16x16x32_bf16 v[164:167], v[228:231], v[28:31], 0\n\t"
            "v_mul_f32 v170, v248, v170\n\t"
            "v_mul_f32 v171, v249, v171\n\t"
            "v_mfma_f32_16x16x32_bf16 v[164:167], v[224:227], v[24:27], v[164:167]\n\t"
            "v_cvt_pk_bf16_f32 v214, v172, v173\n\t"
            "v_cvt_pk_bf16_f32 v210, v168, v169\n\t"
            "v_mfma_f32_16x16x32_bf16 v[176:179], v[224:227], v[48:51], 0\n\t"
            "v_cvt_pk_bf16_f32 v211, v170, v171\n\t"
            "v_mul_f32 v174, v248, v174\n\t"
            "v_mfma_f32_16x16x32_bf16 v[176:179], v[228:231], v[52:55], v[176:179]\n\t"
            "v_mul_f32 v175, v249, v175\n\t"
            "v_add_u32 v172, s67, v245\n\t"
            "v_mfma_f32_16x16x32_bf16 v[180:183], v[228:231], v[76:79], 0\n\t"
            "ds_read_b128 v[168:171], v172 offset:18816\n\t"
            "v_cvt_pk_bf16_f32 v215, v174, v175\n\t"
            "v_mfma_f32_16x16x32_bf16 v[180:183], v[224:227], v[72:75], v[180:183]\n\t"
            "ds_read_b128 v[172:175], v172 offset:19840\n\t"
            "v_mul_f32 v152, v236, v152\n\t"
            "v_mfma_f32_16x16x32_bf16 v[184:187], v[224:227], v[96:99], 0\n\t"
            "v_mul_f32 v153, v237, v153\n\t"
            "v_mul_f32 v156, v236, v156\n\t"
            "v_mfma_f32_16x16x32_bf16 v[184:187], v[228:231], v[100:103], v[184:187]\n\t"
            "v_mul_f32 v157, v237, v157\n\t"
            "v_mul_f32 v147, v249, v147\n\t"
            "v_mfma_f32_16x16x32_bf16 v[188:191], v[228:231], v[124:127], 0\n\t"
            "ds_read_b128 v[228:231], v233 offset:12544\n\t"
            "ds_read_b64 v[236:237], v247 offset:128\n\t"
            "v_mfma_f32_16x16x32_bf16 v[188:191], v[224:227], v[120:123], v[188:191]\n\t"
            "ds_read_b128 v[224:227], v232 offset:12544\n\t"
            "v_mul_f32 v150, v248, v150\n\t"
            "s_waitcnt lgkmcnt(5)\n\t"
            "v_mfma_f32_16x16x32_bf16 v[160:163], v[192:195], v[8:11], v[160:163]\n\t"
            "v_mul_f32 v151, v249, v151\n\t"
            "v_mul_f32 v154, v248, v154\n\t"
            "v_mfma_f32_16x16x32_bf16 v[160:163], v[196:199], v[12:15], v[160:163]\n\t"
            "v_mul_f32 v155, v249, v155\n\t"
            "v_mul_f32 v158, v248, v158\n\t"
            "v_mfma_f32_16x16x32_bf16 v[164:167], v[196:199], v[36:39], v[164:167]\n\t"
            "v_mul_f32 v159, v249, v159\n\t"
            "s_waitcnt lgkmcnt(4)\n\t"
            "v_add_f32 v168, v168, v170\n\t"
            "v_mfma_f32_16x16x32_bf16 v[164:167], v[192:195], v[32:35], v[164:167]\n\t"
            "ds_read_b64 v[248:249], v247 offset:136\n\t"
            "s_waitcnt lgkmcnt(4)\n\t"
            "v_add_f32 v172, v172, v174\n\t"
            "v_mfma_f32_16x16x32_bf16 v[176:179], v[192:195], v[56:59], v[176:179]\n\t"
            "v_add_f32 v168, v168, v172\n\t"
            "v_add_f32 v169, v169, v171\n\t"
            "v_mfma_f32_16x16x32_bf16 v[176:179], v[196:199], v[60:63], v[176:179]\n\t"
            "v_add_f32 v173, v173, v175\n\t"
            "v_mul_f32 v168, 0x3baaaaab, v168\n\t"
            "v_mfma_f32_16x16x32_bf16 v[180:183], v[196:199], v[84:87], v[180:183]\n\t"
            "v_add_f32 v169, v169, v173\n\t"
            "v_mul_f32_e64 v168, v168, -v168\n\t"
            "v_mfma_f32_16x16x32_bf16 v[180:183], v[192:195], v[80:83], v[180:183]\n\t"
            "v_fmac_f32 v168, 0x3baaaaab, v169\n\t"
            "v_max_f32 v168, 0, v168\n\t"
            "v_mfma_f32_16x16x32_bf16 v[184:187], v[192:195], v[104:107], v[184:187]\n\t"
            "v_cvt_pk_bf16_f32 v202, v144, v145\n\t"
            "v_cvt_pk_bf16_f32 v203, v146, v147\n\t"
            "v_mfma_f32_16x16x32_bf16 v[184:187], v[196:199], v[108:111], v[184:187]\n\t"
            "v_cvt_pk_bf16_f32 v206, v148, v149\n\t"
            "v_cvt_pk_bf16_f32 v207, v150, v151\n\t"
            "v_mfma_f32_16x16x32_bf16 v[188:191], v[196:199], v[132:135], v[188:191]\n\t"
            "v_cvt_pk_bf16_f32 v218, v152, v153\n\t"
            "v_cvt_pk_bf16_f32 v219, v154, v155\n\t"
            "v_mfma_f32_16x16x32_bf16 v[188:191], v[192:195], v[128:131], v[188:191]\n\t"
            "v_cvt_pk_bf16_f32 v222, v156, v157\n\t"
            "v_cvt_pk_bf16_f32 v223, v158, v159\n\t"
            "s_waitcnt lgkmcnt(1)\n\t"
            "v_mfma_f32_16x16x32_bf16 v[160:163], v[224:227], v[16:19], v[160:163]\n\t"
            "v_add_f32 v168, 0x3727c5ac, v168\n\t"
            "v_rsq_f32 v168, v168\n\t"
            "v_mfma_f32_16x16x32_bf16 v[160:163], v[228:231], v[20:23], v[160:163]\n\t"
            "ds_write_b128 v243, v[200:203]\n\t"
            "v_mfma_f32_16x16x32_bf16 v[164:167], v[228:231], v[44:47], v[164:167]\n\t"
            "v_add_u32 v232, s67, v242\n\t"
            "v_xor_b32 v233, 64, v232\n\t"
            "v_mfma_f32_16x16x32_bf16 v[164:167], v[224:227], v[40:43], v[164:167]\n\t"
            "s_xor_b32 s48, s48, s71\n\t"
            "s_xor_b32 s67, s67, s71\n\t"
            "s_add_u32 s76, s76, 4\n\t"
            "s_add_u32 s44, s44, 8\n\t"
            "v_mfma_f32_16x16x32_bf16 v[176:179], v[224:227], v[64:67], v[176:179]\n\t"
            "s_lshl_b32 s56, s56, 2\n\t"
            "s_lshr_b32 s49, s26, 30\n\t"
            "s_or_b32 s56, s56, s49\n\t"
            "v_mfma_f32_16x16x32_bf16 v[176:179], v[228:231], v[68:71], v[176:179]\n\t"
            "ds_read_b128 v[192:195], v232\n\t"
            "ds_read_b128 v[196:199], v233\n\t"
            "v_mfma_f32_16x16x32_bf16 v[180:183], v[228:231], v[92:95], v[180:183]\n\t"
            "v_mul_f32 v160, v236, v160\n\t"
            "v_mul_f32 v161, v237, v161\n\t"
            "v_mfma_f32_16x16x32_bf16 v[180:183], v[224:227], v[88:91], v[180:183]\n\t"
            "v_mul_f32 v164, v236, v164\n\t"
            "v_mul_f32 v165, v237, v165\n\t"
            "v_mfma_f32_16x16x32_bf16 v[184:187], v[224:227], v[112:115], v[184:187]\n\t"
            "v_mul_f32 v176, v236, v176\n\t"
            "v_mul_f32 v177, v237, v177\n\t"
            "v_mfma_f32_16x16x32_bf16 v[184:187], v[228:231], v[116:119], v[184:187]\n\t"
            "ds_write_b128 v243, v[204:207] offset:1536\n\t"
            "v_mfma_f32_16x16x32_bf16 v[188:191], v[228:231], v[140:143], v[188:191]\n\t"
            "ds_read_b128 v[228:231], v233 offset:128\n\t"
            "v_mul_f32 v180, v236, v180\n\t"
            "v_mfma_f32_16x16x32_bf16 v[188:191], v[224:227], v[136:139], v[188:191]\n\t"
            "ds_read_b128 v[224:227], v232 offset:128\n\t"
            "v_mul_f32 v181, v237, v181\n\t"
            "s_waitcnt lgkmcnt(3)\n\t"
            "v_mfma_f32_16x16x32_bf16 v[200:203], v[192:195], v[0:3], 0\n\t"
            "v_mul_f32 v184, v236, v184\n\t"
            "v_mul_f32 v185, v237, v185\n\t"
            "v_mfma_f32_16x16x32_bf16 v[200:203], v[196:199], v[4:7], v[200:203]\n\t"
            "ds_write_b128 v243, v[208:211] offset:3072\n\t"
            "v_mfma_f32_16x16x32_bf16 v[204:207], v[196:199], v[28:31], 0\n\t"
            "v_mul_f32 v188, v236, v188\n\t"
            "v_mul_f32 v189, v237, v189\n\t"
            "v_mfma_f32_16x16x32_bf16 v[204:207], v[192:195], v[24:27], v[204:207]\n\t"
            "ds_write_b128 v243, v[212:215] offset:4608\n\t"
            "v_mfma_f32_16x16x32_bf16 v[208:211], v[192:195], v[48:51], 0\n\t"
            "v_mul_f32 v162, v248, v162\n\t"
            "v_mul_f32 v163, v249, v163\n\t"
            "v_mfma_f32_16x16x32_bf16 v[208:211], v[196:199], v[52:55], v[208:211]\n\t"
            "ds_write_b128 v243, v[216:219] offset:6144\n\t"
            "v_mfma_f32_16x16x32_bf16 v[212:215], v[196:199], v[76:79], 0\n\t"
            "v_cvt_pk_bf16_f32 v160, v160, v161\n\t"
            "v_mul_f32 v166, v248, v166\n\t"
            "v_mfma_f32_16x16x32_bf16 v[212:215], v[192:195], v[72:75], v[212:215]\n\t"
            "ds_write_b128 v243, v[220:223] offset:7680\n\t"
            "v_mfma_f32_16x16x32_bf16 v[216:219], v[192:195], v[96:99], 0\n\t"
            "v_mul_f32 v167, v249, v167\n\t"
            "v_cvt_pk_bf16_f32 v164, v164, v165\n\t"
            "v_mfma_f32_16x16x32_bf16 v[216:219], v[196:199], v[100:103], v[216:219]\n\t"
            "v_mul_f32 v178, v248, v178\n\t"
            "v_mul_f32 v179, v249, v179\n\t"
            "v_mfma_f32_16x16x32_bf16 v[220:223], v[196:199], v[124:127], 0\n\t"
            "v_cvt_pk_bf16_f32 v176, v176, v177\n\t"
            "v_mul_f32 v182, v248, v182\n\t"
            "v_mfma_f32_16x16x32_bf16 v[220:223], v[192:195], v[120:123], v[220:223]\n\t"
            "v_mul_f32 v183, v249, v183\n\t"
            "v_cvt_pk_bf16_f32 v180, v180, v181\n\t"
            "s_waitcnt lgkmcnt(4)\n\t"
            "v_mfma_f32_16x16x32_bf16 v[200:203], v[224:227], v[8:11], v[200:203]\n\t"
            "s_waitcnt vmcnt(0)\n\t"
            "v_mul_f32 v186, v248, v186\n\t"
            "v_mul_f32 v187, v249, v187\n\t"
            "v_mfma_f32_16x16x32_bf16 v[200:203], v[228:231], v[12:15], v[200:203]\n\t"
            "v_cvt_pk_bf16_f32 v184, v184, v185\n\t"
            "v_mul_f32 v190, v248, v190\n\t"
            "v_mfma_f32_16x16x32_bf16 v[204:207], v[228:231], v[36:39], v[204:207]\n\t"
            "v_mul_f32 v191, v249, v191\n\t"
            "v_cvt_pk_bf16_f32 v188, v188, v189\n\t"
            "v_mfma_f32_16x16x32_bf16 v[204:207], v[224:227], v[32:35], v[204:207]\n\t"
            "v_cvt_pk_bf16_f32 v161, v162, v163\n\t"
            "v_cvt_pk_bf16_f32 v165, v166, v167\n\t"
            "v_mfma_f32_16x16x32_bf16 v[208:211], v[224:227], v[56:59], v[208:211]\n\t"
            "v_cvt_pk_bf16_f32 v177, v178, v179\n\t"
            "v_cvt_pk_bf16_f32 v181, v182, v183\n\t"
            "v_mfma_f32_16x16x32_bf16 v[208:211], v[228:231], v[60:63], v[208:211]\n\t"
            "v_cvt_pk_bf16_f32 v185, v186, v187\n\t"
            "v_cvt_pk_bf16_f32 v189, v190, v191\n\t"
            "v_mfma_f32_16x16x32_bf16 v[212:215], v[228:231], v[84:87], v[212:215]\n\t"
            "v_xor_b32 v247, 0x100, v247\n\t"
            "v_mfma_f32_16x16x32_bf16 v[212:215], v[224:227], v[80:83], v[212:215]\n\t"
            "ds_write_b64 v244, v[160:161]\n\t"
            "v_mfma_f32_16x16x32_bf16 v[216:219], v[224:227], v[104:107], v[216:219]\n\t"
            "ds_read_b128 v[172:175], v254 offset:64\n\t"
            "ds_read_b64 v[144:145], v253\n\t"
            "v_mfma_f32_16x16x32_bf16 v[216:219], v[228:231], v[108:111], v[216:219]\n\t"
            "ds_write_b64 v244, v[164:165] offset:1536\n\t"
            "v_mfma_f32_16x16x32_bf16 v[220:223], v[228:231], v[132:135], v[220:223]\n\t"
            "ds_read_b128 v[228:231], v233 offset:256\n\t"
            "ds_read_b64 v[146:147], v253 offset:96\n\t"
            "s_setprio 3\n\t"
            "v_mfma_f32_16x16x32_bf16 v[220:223], v[224:227], v[128:131], v[220:223]\n\t"
            "ds_read_b128 v[224:227], v232 offset:256\n\t"
            "ds_write_b64 v244, v[176:177] offset:3072\n\t"
            "ds_write_b64 v244, v[180:181] offset:4608\n\t"
            "ds_write_b64 v244, v[184:185] offset:6144\n\t"
            "ds_write_b64 v244, v[188:189] offset:7680\n\t"
            "s_waitcnt lgkmcnt(14)\n\t"
            "ds_write_b32 v246, v168\n\t"
            "s_waitcnt lgkmcnt(14)\n\t"
            "ds_read_b128 v[168:171], v254\n\t"
            "s_waitcnt lgkmcnt(14)\n\t"
            "ds_read_b64 v[148:149], v253 offset:1744\n\t"
            "s_waitcnt lgkmcnt(14)\n\t"
            "ds_read_b64 v[150:151], v253 offset:1840\n\t"
            "s_waitcnt lgkmcnt(14)\n\t"
            "ds_read_b64 v[152:153], v253 offset:8\n\t"
            "s_waitcnt lgkmcnt(14)\n\t"
            "ds_read_b64 v[154:155], v253 offset:104\n\t"
            "s_waitcnt lgkmcnt(14)\n\t"
            "ds_read_b64 v[156:157], v253 offset:1752\n\t"
            "s_waitcnt lgkmcnt(14)\n\t"
            "ds_read_b64 v[158:159], v253 offset:1848\n\t"
            "s_waitcnt lgkmcnt(14)\n\t"
            "ds_read_b64 v[160:161], v253 offset:16\n\t"
            "s_waitcnt lgkmcnt(14)\n\t"
            "ds_read_b64 v[162:163], v253 offset:112\n\t"
            "s_waitcnt lgkmcnt(14)\n\t"
            "ds_read_b64 v[164:165], v253 offset:1760\n\t"
            "s_waitcnt lgkmcnt(14)\n\t"
            "ds_read_b64 v[166:167], v253 offset:1856\n\t"
            "s_waitcnt lgkmcnt(11)\n\t"
            "s_barrier\n\t"
            "s_cmp_lt_u32 s46, 0x7000000\n\t"
            "s_cbranch_scc1 .Lwc_loop%=\n\t"
            "s_branch .Lwc_end%=\n\t"
            ".Lwc_spin%=:\n\t"
            "s_cmp_eq_u32 s83, 0\n\t"
            "s_cbranch_scc1 .Lwc_go%=\n\t"
            "s_sub_u32 s83, s83, 1\n\t"
            "s_sleep 2\n\t"
            "v_mov_b32 v234, 0\n\t"
            "ds_read_b64 v[234:235], v234 offset:7856\n\t"
            "s_waitcnt lgkmcnt(0)\n\t"
            "v_readfirstlane_b32 s77, v234\n\t"
            "v_readfirstlane_b32 s38, v235\n\t"
            "s_add_u32 s77, s77, s38\n\t"
            "s_cmp_ge_u32 s77, s44\n\t"
            "s_cbranch_scc0 .Lwc_spin%=\n\t"
            "s_branch .Lwc_go%=\n\t"
            ".Lwc_end%=:\n\t"
            "s_waitcnt lgkmcnt(0)\n\t"
            "s_nop 7\n\t"
            : "+{v246}"(aRSw_), "+{v247}"(aRSr_), "+{s44}"(s44), B_SIO
            : "{v242}"(pE_), "{v243}"(aVAw_), "{v244}"(aVBw_), "{v245}"(pST_), "{v250}"(pXw_), "{v251}"(pSTw_), "{v252}"(aCA_), "{v253}"(aB_), "{v254}"(aA_), "{v255}"(idx_), B_SIN, "{s82}"(touch)
            : "v144", "v145", "v146", "v147", "v148", "v149", "v150", "v151", "v152", "v153", "v154", "v155", "v156", "v157", "v158", "v159", "v160", "v161", "v162", "v163", "v164", "v165", "v166", "v167", "v168", "v169", "v170", "v171", "v172", "v173", "v174", "v175", "v176", "v177", "v178", "v179", "v180", "v181", "v182", "v183", "v184", "v185", "v186", "v187", "v188", "v189", "v190", "v191", "v192", "v193", "v194", "v195", "v196", "v197", "v198", "v199", "v236", "v237", "v248", "v249", "v0", "v1", "v2", "v3", "v4", "v5", "v6", "v7", "v8", "v9", "v10", "v11", "v12", "v13", "v14", "v15", "v16", "v17", "v18", "v19", "v20", "v21", "v22", "v23", "v24", "v25", "v26", "v27", "v28", "v29", "v30", "v31", "v32", "v33", "v34", "v35", "v36", "v37", "v38", "v39", "v40", "v41", "v42", "v43", "v44", "v45", "v46", "v47", "v48", "v49", "v50", "v51", "v52", "v53", "v54", "v55", "v56", "v57", "v58", "v59", "v60", "v61", "v62", "v63", "v64", "v65", "v66", "v67", "v68", "v69", "v70", "v71", "v72", "v73", "v74", "v75", "v76", "v77", "v78", "v79", "v80", "v81", "v82", "v83", "v84", "v85", "v86", "v87", "v88", "v89", "v90", "v91", "v92", "v93", "v94", "v95", "v96", "v97", "v98", "v99", "v100", "v101", "v102", "v103", "v104", "v105", "v106", "v107", "v108", "v109", "v110", "v111", "v112", "v113", "v114", "v115", "v116", "v117", "v118", "v119", "v120", "v121", "v122", "v123", "v124", "v125", "v126", "v127", "v128", "v129", "v130", "v131", "v132", "v133", "v134", "v135", "v136", "v137", "v138", "v139", "v140", "v141", "v142", "v143", "v200", "v201", "v202", "v204", "v205", "v206", "v207", "v208", "v209", "v210", "v211", "v212", "v213", "v214", "v215", "v216", "v217", "v218", "v219", "v220", "v221", "v222", "v223", "v224", "v225", "v226", "v227", "v228", "v229", "v230", "v231", "v232", "v233", "v234", "v235", B_SCLOB);
        }
        {
            __builtin_amdgcn_s_setprio(3);
            int ln_;
            asm volatile("v_mbcnt_lo_u32_b32 %0, -1, 0\n\tv_mbcnt_hi_u32_b32 %0, -1, %0" : "=v"(ln_));
            const int l15_ = ln_ & 15, g_ = ln_ >> 4;
            KARGS
            const auto orsrc = __builtin_amdgcn_make_buffer_rsrc((void*)ob, 0, IMG * IMG * CH * 2, 0x00020000);
            const auto wq = __builtin_amdgcn_make_buffer_rsrc((void*)wb, 0, WS_BYTES, 0x00020000);
            bf16x8 W[36];
#define WO_LOAD(k0)                                                                                \
    SB();                                                                                          \
    _Pragma("unroll") for (int ks = (k0); ks < (k0) + 2; ++ks)                                     \
        _Pragma("unroll") for (int j = 0; j < 6; ++j)                                              \
            W[j * 6 + ks] = BC8(__builtin_amdgcn_raw_buffer_load_b128(wq, ln_ * 16, (wave + 2) * 36864 + (j * 6 + ks) * 1024, SC1)); \
    SB();
            int lE[2], nw;
            unsigned o[2];
            f32x4 acc[2][6];
#pragma unroll
            for (int lt = 0; lt < 2; ++lt) {
                WO_LOAD(2 * lt)
                int i = 16 * lt + l15_;
                const unsigned en = *(const unsigned*)(smem + WL_OFF + i * 4);
                const bool ok = en != 0x7ffffffu;
                if (lt == 1) nw = 16 + __builtin_popcount((unsigned)__builtin_amdgcn_ballot_w64(ok) & 0xffffu);
                i = ok ? i : 0;
                lE[lt] = OL_OFF + i * 384 + ((g_ ^ ((i >> 1) & 7)) << 4);
                o[lt] = ok ? (en & 0x7ffffffu) + LPOS(en >> 31) * ROWBYTES + LPOS(en >> 30) * (CH * 2) + 192 * w + 16 * g_ : OOB;
#pragma unroll
                for (int k = 0; k < 3; ++k) {
                    const int c8 = 12 * w + 4 * k + g_, ws = (c8 * 43) >> 8, r6 = c8 - 6 * ws;
                    const u32x4 r = *(const u32x4*)(smem + LX_OFF(i) + (r6 >> 1) * 128 + ((2 * ws + (r6 & 1)) << 4));
                    acc[lt][2 * k] = (f32x4){bflo(r[0]), bfhi(r[0]), bflo(r[1]), bfhi(r[1])};
                    acc[lt][2 * k + 1] = (f32x4){bflo(r[2]), bfhi(r[2]), bflo(r[3]), bfhi(r[3])};
                }
            }
            bf16x8 of[2][6];
#pragma unroll
            for (int ks = 0; ks < 6; ++ks) of[0][ks] = *(const bf16x8*)(smem + ((ks & 1) ? (lE[0] ^ 64) : lE[0]) + (ks >> 1) * 128);
            WO_LOAD(4)
#pragma unroll
            for (int i = 0; i < 12; ++i) OPQ(acc[i / 6][i % 6]);
            OPQ(lE[1]);
#pragma unroll 1
            for (int it_ = 0; it_ < 100000; ++it_) {
                unsigned n_;
                asm volatile("ds_read_b32 %0, %1\n\ts_waitcnt lgkmcnt(0)" : "=v"(n_) : "v"(ldsb + FLAG_OFF) : "memory");
                if (__builtin_amdgcn_readfirstlane(n_) >= 4 * nw) break;
                __builtin_amdgcn_s_sleep(1);
            }
#pragma unroll
            for (int ks = 0; ks < 6; ++ks) of[1][ks] = *(const bf16x8*)(smem + ((ks & 1) ? (lE[1] ^ 64) : lE[1]) + (ks >> 1) * 128);
            SB();
#pragma unroll
            for (int lt = 0; lt < 2; ++lt) {
#pragma unroll
                for (int ks = 0; ks < 6; ++ks)
#pragma unroll
                    for (int j = 0; j < 6; ++j) acc[lt][j] = MFMA32(W[j * 6 + ks], of[lt][ks], acc[lt][j]);
#pragma unroll
                for (int k = 0; k < 3; ++k) {
                    const f32x4 a = acc[lt][2 * k], b = acc[lt][2 * k + 1];
                    const u32x4 ov = {pk2(a[0], a[1]), pk2(a[2], a[3]), pk2(b[0], b[1]), pk2(b[2], b[3])};
                    __builtin_amdgcn_raw_buffer_store_b128(ov, orsrc, o[lt] + 64 * k, 0, SC1);
                }
            }
        }
        __builtin_amdgcn_endpgm();
    } else {
        LANE_SETUP
        {
            const unsigned oE_ = ldsb + fE + (O_OFF - X_OFF), oO_ = oE_ ^ 64;
            unsigned l16_ = lane * 16;
            unsigned pr[3];
#pragma unroll
            for (int k = 0; k < 3; ++k) {
                const int c8 = 12 * (w - 2) + 4 * k + g;
                const int ws = c8 / 6, r6 = c8 - 6 * ws;
                pr[k] = l15 * 384 + (r6 >> 1) * 128 + (((2 * ws + (r6 & 1)) ^ sw) << 4);
            }
            const unsigned pR0_ = pr[0], pR1_ = pr[1], pR2_ = pr[2];
            unsigned q0 = ldsb + PF_OFF + 1024 + lane * 4, q1 = ldsb + O_OFF + lane * 16, q2 = oE_, q3 = ldsb + PF_OFF + 1024 + l15 * 4;
            unsigned q4 = ldsb + OL_OFF + l15 * 384 + (((4 * (w - 2) + (g >> 1)) ^ sw) << 4) + 8 * (g & 1);
            B_SCALARS
            asm volatile(
            "s_waitcnt lgkmcnt(0)\n\t"
            "s_mov_b32 s83, 0x10000\n\t"
            "s_mov_b32 s84, -47104\n\t"
            "s_nop 7\n\t"
            "v_mov_b32 v200, v250\n\t"
            "v_mov_b32 v201, v251\n\t"
            "v_mov_b32 v202, v253\n\t"
            "s_mov_b32 s38, s67\n\t"
            "s_mov_b32 s67, 74496\n\t"
            "ds_read_b64 v[250:251], v203\n\t"
            "ds_read_b32 v253, v203 offset:8\n\t"
            ".Lwk_b1%=:\n\t"
            "v_mov_b32 v236, 0\n\t"
            "ds_read_b32 v236, v236 offset:7864\n\t"
            "s_waitcnt lgkmcnt(0)\n\t"
            "v_readfirstlane_b32 s77, v236\n\t"
            "s_cmp_ge_u32 s77, 4\n\t"
            "s_cbranch_scc1 .Lwk_b2%=\n\t"
            "s_cmp_eq_u32 s83, 0\n\t"
            "s_cbranch_scc1 .Lwk_b2%=\n\t"
            "s_sub_u32 s83, s83, 1\n\t"
            "s_sleep 1\n\t"
            "s_branch .Lwk_b1%=\n\t"
            ".Lwk_b2%=:\n\t"
            "s_waitcnt vmcnt(0)\n\t"
            "s_call_b64 vcc, wk_conv\n\t"
            "v_mov_b32 v250, v200\n\t"
            "v_mov_b32 v251, v201\n\t"
            "v_mov_b32 v253, v202\n\t"
            "s_mov_b32 s67, s38\n\t"
            "s_barrier\n\t"
            "ds_read_b64 v[236:237], v252 offset:2560\n\t"
            "s_waitcnt lgkmcnt(0)\n\t"
            "s_mov_b64 exec, 1\n\t"
            "buffer_store_dwordx2 v[236:237], off, s[28:31], s44 sc1\n\t"
            "s_mov_b64 exec, -1\n\t"
            "s_sleep 30\n\t"
            "ds_read_b32 v249, v252 offset:12\n\t"
            "s_waitcnt lgkmcnt(0)\n\t"
            "v_and_b32 v249, s69, v249\n\t"
            "s_mov_b32 exec_hi, 0x3fffff\n\t"
            "s_add_u32 m0, s66, 0\n\t"
            "s_add_u32 s49, s79, 0\n\t"
            "buffer_load_dwordx4 v249, s[72:75], s49 offen lds\n\t"
            "s_mov_b32 exec_hi, -1\n\t"
            "s_mov_b32 exec_hi, 0x3fffff\n\t"
            "s_add_u32 m0, s66, 872\n\t"
            "s_add_u32 s49, s47, 196224\n\t"
            "buffer_load_dwordx4 v249, s[72:75], s49 offen lds\n\t"
            "s_mov_b32 exec_hi, -1\n\t"
            "s_mov_b32 exec_hi, 0x3fffff\n\t"
            "s_add_u32 m0, s66, 1744\n\t"
            "s_add_u32 s49, s47, 392448\n\t"
            "buffer_load_dwordx4 v249, s[72:75], s49 offen lds\n\t"
            "s_mov_b32 exec_hi, -1\n\t"
            "s_mov_b32 exec_hi, 0x3fffff\n\t"
            "s_add_u32 m0, s66, 2616\n\t"
            "s_add_u32 s49, s47, 588672\n\t"
            "buffer_load_dwordx4 v249, s[72:75], s49 offen lds\n\t"
            "s_mov_b32 exec_hi, -1\n\t"
            "s_mov_b32 exec_hi, 0x3fffff\n\t"
            "s_add_u32 m0, s66, 3488\n\t"
            "s_add_u32 s49, s47, 784896\n\t"
            "buffer_load_dwordx4 v249, s[72:75], s49 offen lds\n\t"
            "s_mov_b32 exec_hi, -1\n\t"
            "s_mov_b32 exec_hi, 0x3fffff\n\t"
            "s_add_u32 m0, s66, 4360\n\t"
            "s_add_u32 s49, s47, 981120\n\t"
            "buffer_load_dwordx4 v249, s[72:75], s49 offen lds\n\t"
            "s_mov_b32 exec_hi, -1\n\t"
            "s_mov_b32 exec_hi, 0x3fffff\n\t"
            "s_add_u32 m0, s66, 5232\n\t"
            "s_add_u32 s49, s47, 1177344\n\t"
            "buffer_load_dwordx4 v249, s[72:75], s49 offen lds\n\t"
            "s_mov_b32 exec_hi, -1\n\t"
            "s_mov_b32 exec_hi, 0x3fffff\n\t"
            "s_add_u32 m0, s66, 6104\n\t"
            "s_add_u32 s49, s47, 1373568\n\t"
            "buffer_load_dwordx4 v249, s[72:75], s49 offen lds\n\t"
            "s_mov_b32 exec_hi, -1\n\t"
            "s_mov_b32 exec_hi, 0x3fffff\n\t"
            "s_add_u32 m0, s66, 6976\n\t"
            "s_add_u32 s49, s47, 1569792\n\t"
            "buffer_load_dwordx4 v249, s[72:75], s49 offen lds\n\t"
            "s_mov_b32 exec_hi, -1\n\t"
            "s_add_u32 s49, s33, -73728\n\t"
            "buffer_load_dwordx4 v[0:3], v247, s[28:31], s49 offen sc1\n\t"
            "s_add_u32 s49, s33, -67584\n\t"
            "buffer_load_dwordx4 v[24:27], v247, s[28:31], s49 offen sc1\n\t"
            "s_add_u32 s49, s33, -61440\n\t"
            "buffer_load_dwordx4 v[48:51], v247, s[28:31], s49 offen sc1\n\t"
            "s_add_u32 s49, s33, -55296\n\t"
            "buffer_load_dwordx4 v[72:75], v247, s[28:31], s49 offen sc1\n\t"
            "s_add_u32 s49, s33, -49152\n\t"
            "buffer_load_dwordx4 v[96:99], v247, s[28:31], s49 offen sc1\n\t"
            "s_add_u32 s49, s33, -43008\n\t"
            "buffer_load_dwordx4 v[120:123], v247, s[28:31], s49 offen sc1\n\t"
            "s_add_u32 s49, s33, -72704\n\t"
            "buffer_load_dwordx4 v[4:7], v247, s[28:31], s49 offen sc1\n\t"
            "s_add_u32 s49, s33, -66560\n\t"
            "buffer_load_dwordx4 v[28:31], v247, s[28:31], s49 offen sc1\n\t"
            "s_add_u32 s49, s33, -60416\n\t"
            "buffer_load_dwordx4 v[52:55], v247, s[28:31], s49 offen sc1\n\t"
            "s_add_u32 s49, s33, -54272\n\t"
            "buffer_load_dwordx4 v[76:79], v247, s[28:31], s49 offen sc1\n\t"
            "s_add_u32 s49, s33, -48128\n\t"
            "buffer_load_dwordx4 v[100:103], v247, s[28:31], s49 offen sc1\n\t"
            "s_add_u32 s49, s33, -41984\n\t"
            "buffer_load_dwordx4 v[124:127], v247, s[28:31], s49 offen sc1\n\t"
            "s_add_u32 s49, s33, -71680\n\t"
            "buffer_load_dwordx4 v[8:11], v247, s[28:31], s49 offen sc1\n\t"
            "s_add_u32 s49, s33, -65536\n\t"
            "buffer_load_dwordx4 v[32:35], v247, s[28:31], s49 offen sc1\n\t"
            "s_add_u32 s49, s33, -59392\n\t"
            "buffer_load_dwordx4 v[56:59], v247, s[28:31], s49 offen sc1\n\t"
            "s_add_u32 s49, s33, -53248\n\t"
            "buffer_load_dwordx4 v[80:83], v247, s[28:31], s49 offen sc1\n\t"
            "s_add_u32 s49, s33, -47104\n\t"
            "buffer_load_dwordx4 v[104:107], v247, s[28:31], s49 offen sc1\n\t"
            "s_add_u32 s49, s33, -40960\n\t"
            "buffer_load_dwordx4 v[128:131], v247, s[28:31], s49 offen sc1\n\t"
            "s_add_u32 s49, s33, -70656\n\t"
            "buffer_load_dwordx4 v[12:15], v247, s[28:31], s49 offen sc1\n\t"
            "s_add_u32 s49, s33, -64512\n\t"
            "buffer_load_dwordx4 v[36:39], v247, s[28:31], s49 offen sc1\n\t"
            "s_add_u32 s49, s33, -58368\n\t"
            "buffer_load_dwordx4 v[60:63], v247, s[28:31], s49 offen sc1\n\t"
            "s_add_u32 s49, s33, -52224\n\t"
            "buffer_load_dwordx4 v[84:87], v247, s[28:31], s49 offen sc1\n\t"
            "s_add_u32 s49, s33, -46080\n\t"
            "buffer_load_dwordx4 v[108:111], v247, s[28:31], s49 offen sc1\n\t"
            "s_add_u32 s49, s33, -39936\n\t"
            "buffer_load_dwordx4 v[132:135], v247, s[28:31], s49 offen sc1\n\t"
            "s_add_u32 s49, s33, -69632\n\t"
            "buffer_load_dwordx4 v[16:19], v247, s[28:31], s49 offen sc1\n\t"
            "s_add_u32 s49, s33, -63488\n\t"
            "buffer_load_dwordx4 v[40:43], v247, s[28:31], s49 offen sc1\n\t"
            "s_add_u32 s49, s33, -57344\n\t"
            "buffer_load_dwordx4 v[64:67], v247, s[28:31], s49 offen sc1\n\t"
            "s_add_u32 s49, s33, -51200\n\t"
            "buffer_load_dwordx4 v[88:91], v247, s[28:31], s49 offen sc1\n\t"
            "s_add_u32 s49, s33, -45056\n\t"
            "buffer_load_dwordx4 v[112:115], v247, s[28:31], s49 offen sc1\n\t"
            "s_add_u32 s49, s33, -38912\n\t"
            "buffer_load_dwordx4 v[136:139], v247, s[28:31], s49 offen sc1\n\t"
            "s_add_u32 s49, s33, -68608\n\t"
            "buffer_load_dwordx4 v[20:23], v247, s[28:31], s49 offen sc1\n\t"
            "s_add_u32 s49, s33, -62464\n\t"
            "buffer_load_dwordx4 v[44:47], v247, s[28:31], s49 offen sc1\n\t"
            "s_add_u32 s49, s33, -56320\n\t"
            "buffer_load_dwordx4 v[68:71], v247, s[28:31], s49 offen sc1\n\t"
            "s_add_u32 s49, s33, -50176\n\t"
            "buffer_load_dwordx4 v[92:95], v247, s[28:31], s49 offen sc1\n\t"
            "s_add_u32 s49, s33, -44032\n\t"
            "buffer_load_dwordx4 v[116:119], v247, s[28:31], s49 offen sc1\n\t"
            "s_add_u32 s49, s33, -37888\n\t"
            "buffer_load_dwordx4 v[140:143], v247, s[28:31], s49 offen sc1\n\t"
            "s_setprio 3\n\t"
            "ds_read_b128 v[168:171], v254\n\t"
            "ds_read_b128 v[172:175], v254 offset:64\n\t"
            "ds_read_b128 v[220:223], v229 offset:12288\n\t"
            "ds_read_b128 v[224:227], v229 offset:13312\n\t"
            "ds_read_b128 v[200:203], v232\n\t"
            "ds_read_b128 v[208:211], v232 offset:128\n\t"
            "ds_read_b128 v[216:219], v232 offset:256\n\t"
            "s_waitcnt vmcnt(36)\n\t"
            "ds_read_b64 v[144:145], v253\n\t"
            "ds_read_b64 v[146:147], v253 offset:96\n\t"
            "ds_read_b64 v[148:149], v253 offset:1744\n\t"
            "ds_read_b64 v[150:151], v253 offset:1840\n\t"
            "ds_read_b64 v[152:153], v253 offset:8\n\t"
            "ds_read_b64 v[154:155], v253 offset:104\n\t"
            "ds_read_b64 v[156:157], v253 offset:1752\n\t"
            "ds_read_b64 v[158:159], v253 offset:1848\n\t"
            "s_waitcnt lgkmcnt(14)\n\t"
            "ds_read_b64 v[160:161], v253 offset:16\n\t"
            "s_waitcnt lgkmcnt(14)\n\t"
            "ds_read_b64 v[162:163], v253 offset:112\n\t"
            "s_waitcnt lgkmcnt(14)\n\t"
            "ds_read_b64 v[164:165], v253 offset:1760\n\t"
            "s_waitcnt lgkmcnt(14)\n\t"
            "ds_read_b64 v[166:167], v253 offset:1856\n\t"
            "v_mov_b64 v[176:177], 0\n\t"
            "v_mov_b64 v[178:179], 0\n\t"
            "v_mov_b64 v[180:181], 0\n\t"
            "v_mov_b64 v[182:183], 0\n\t"
            "v_mov_b32 v249, s76\n\t"
            "v_mov_b64 v[184:185], 0\n\t"
            "v_mov_b64 v[186:187], 0\n\t"
            "v_mov_b64 v[188:189], 0\n\t"
            "v_mov_b64 v[190:191], 0\n\t"
            "v_add_u32 v236, s67, v250\n\t"
            "v_add_u32 v248, s67, v251\n\t"
            "v_xor_b32 v235, 64, v232\n\t"
            "s_nop 0\n\t"
            "s_waitcnt lgkmcnt(14)\n\t"
            "ds_read_b32 v249, v249\n\t"
            "s_waitcnt lgkmcnt(14)\n\t"
            "ds_read_b128 v[204:207], v235\n\t"
            "s_waitcnt lgkmcnt(14)\n\t"
            "ds_read_b128 v[212:215], v235 offset:128\n\t"
            "v_add_f32 v220, v220, v222\n\t"
            "v_add_f32 v224, v224, v226\n\t"
            "v_add_f32 v220, v220, v224\n\t"
            "v_add_f32 v221, v221, v223\n\t"
            "v_add_f32 v225, v225, v227\n\t"
            "s_waitcnt lgkmcnt(11)\n\t"
            "v_smfmac_f32_16x16x64_bf16 v[176:179], v[168:171], v[144:151], v255\n\t"
            "ds_read_b128 v[168:171], v254 offset:128\n\t"
            "s_waitcnt lgkmcnt(8)\n\t"
            "v_smfmac_f32_16x16x64_bf16 v[180:183], v[172:175], v[152:159], v255\n\t"
            "ds_read_b64 v[144:145], v253 offset:24\n\t"
            "ds_read_b64 v[146:147], v253 offset:120\n\t"
            "ds_read_b64 v[148:149], v253 offset:1768\n\t"
            "ds_read_b64 v[150:151], v253 offset:1864\n\t"
            "ds_read_b128 v[172:175], v254 offset:192\n\t"
            "ds_read_b64 v[152:153], v253 offset:32\n\t"
            "ds_read_b64 v[154:155], v253 offset:128\n\t"
            "s_waitcnt lgkmcnt(14)\n\t"
            "ds_read_b64 v[156:157], v253 offset:1776\n\t"
            "s_waitcnt lgkmcnt(14)\n\t"
            "ds_read_b64 v[158:159], v253 offset:1872\n\t"
            "v_cvt_pk_bf16_f32 v176, v176, v177\n\t"
            "v_cvt_pk_bf16_f32 v177, v178, v179\n\t"
            "v_cvt_pk_bf16_f32 v178, v180, v181\n\t"
            "v_cvt_pk_bf16_f32 v179, v182, v183\n\t"
            "v_mfma_f32_4x4x4_16b_bf16 v[196:199], v[176:177], v[176:177], 0\n\t"
            "v_mov_b64 v[180:181], 0\n\t"
            "v_mfma_f32_16x16x32_bf16 v[192:195], v[238:241], v[176:179], 0\n\t"
            "s_waitcnt lgkmcnt(12)\n\t"
            "v_readfirstlane_b32 s26, v249\n\t"
            "v_mov_b64 v[182:183], 0\n\t"
            "v_mfma_f32_4x4x4_16b_bf16 v[196:199], v[178:179], v[178:179], v[196:199]\n\t"
            "ds_read_b32 v249, v252 offset:12\n\t"
            "s_and_b32 s68, s26, 0x7ffffff\n\t"
            "s_add_u32 s68, s68, s78\n\t"
            "s_and_b32 s69, s26, 0x38000000\n\t"
            "s_or_b32 s69, s69, s39\n\t"
            "s_bitcmp1_b32 s26, 27\n\t"
            "s_cselect_b32 s79, 2.0, s68\n\t"
            "s_waitcnt lgkmcnt(10)\n\t"
            "v_smfmac_f32_16x16x64_bf16 v[184:187], v[168:171], v[160:167], v255\n\t"
            "s_and_b32 s57, s56, 3\n\t"
            "s_lshl_b32 s58, s57, 3\n\t"
            "s_lshr_b32 m0, s54, s58\n\t"
            "ds_read_b128 v[168:171], v254 offset:256\n\t"
            "ds_read_b64 v[160:161], v253 offset:40\n\t"
            "ds_read_b64 v[162:163], v253 offset:136\n\t"
            "ds_read_b64 v[164:165], v253 offset:1784\n\t"
            "s_waitcnt lgkmcnt(9)\n\t"
            "v_smfmac_f32_16x16x64_bf16 v[188:191], v[172:175], v[144:151], v255\n\t"
            "s_lshl_b32 s58, s57, 4\n\t"
            "s_lshr_b64 s[36:37], s[50:51], s58\n\t"
            "s_and_b32 s57, s36, 0xffff\n\t"
            "s_add_u32 s57, s57, s67\n\t"
            "ds_read_b64 v[166:167], v253 offset:1880\n\t"
            "ds_read_b128 v[172:175], v254 offset:320\n\t"
            "ds_read_b64 v[144:145], v253 offset:48\n\t"
            "ds_read_b64 v[146:147], v253 offset:144\n\t"
            "ds_read_b64 v[148:149], v253 offset:1792\n\t"
            "ds_read_b64 v[150:151], v253 offset:1888\n\t"
            "v_writelane_b32 v236, s57, 63\n\t"
            "v_writelane_b32 v236, s59, m0\n\t"
            "v_cvt_pk_bf16_f32 v184, v184, v185\n\t"
            "v_cvt_pk_bf16_f32 v185, v186, v187\n\t"
            "v_xor_b32 v237, 16, v236\n\t"
            "v_cvt_pk_bf16_f32 v186, v188, v189\n\t"
            "v_mfma_f32_4x4x4_16b_bf16 v[196:199], v[184:185], v[184:185], v[196:199]\n\t"
            "s_waitcnt lgkmcnt(14)\n\t"
            "ds_write_b128 v236, v[176:179]\n\t"
            "v_mov_b64 v[176:177], 0\n\t"
            "v_mov_b64 v[178:179], 0\n\t"
            "v_cvt_pk_bf16_f32 v187, v190, v191\n\t"
            "v_mov_b64 v[188:189], 0\n\t"
            "s_waitcnt lgkmcnt(10)\n\t"
            "v_smfmac_f32_16x16x64_bf16 v[176:179], v[168:171], v[152:159], v255\n\t"
            "s_lshr_b64 s[36:37], s[52:53], s58\n\t"
            "s_and_b32 s58, s36, 0xffff\n\t"
            "s_add_u32 s58, s58, s67\n\t"
            "v_mfma_f32_16x16x32_bf16 v[192:195], v[238:241], v[184:187], v[192:195]\n\t"
            "s_and_b32 s38, s52, 0xffff\n\t"
            "s_add_u32 s38, s38, s67\n\t"
            "ds_read_b128 v[168:171], v254 offset:384\n\t"
            "v_mfma_f32_4x4x4_16b_bf16 v[196:199], v[186:187], v[186:187], v[196:199]\n\t"
            "ds_read_b64 v[152:153], v253 offset:56\n\t"
            "s_waitcnt lgkmcnt(7)\n\t"
            "v_smfmac_f32_16x16x64_bf16 v[180:183], v[172:175], v[160:167], v255\n\t"
            "ds_read_b64 v[154:155], v253 offset:152\n\t"
            "ds_read_b64 v[156:157], v253 offset:1800\n\t"
            "ds_read_b64 v[158:159], v253 offset:1896\n\t"
            "ds_read_b128 v[172:175], v254 offset:448\n\t"
            "ds_read_b64 v[160:161], v253 offset:64\n\t"
            "ds_read_b64 v[162:163], v253 offset:160\n\t"
            "ds_read_b64 v[164:165], v253 offset:1808\n\t"
            "ds_read_b64 v[166:167], v253 offset:1904\n\t"
            "s_waitcnt lgkmcnt(14)\n\t"
            "ds_write_b128 v237, v[184:187]\n\t"
            "v_mov_b64 v[184:185], 0\n\t"
            "v_mov_b64 v[186:187], 0\n\t"
            "v_mov_b64 v[190:191], 0\n\t"
            "v_cvt_pk_bf16_f32 v176, v176, v177\n\t"
            "v_cvt_pk_bf16_f32 v177, v178, v179\n\t"
            "v_cvt_pk_bf16_f32 v178, v180, v181\n\t"
            "v_cvt_pk_bf16_f32 v179, v182, v183\n\t"
            "v_mfma_f32_4x4x4_16b_bf16 v[196:199], v[176:177], v[176:177], v[196:199]\n\t"
            "s_waitcnt lgkmcnt(14)\n\t"
            "ds_write_b128 v236, v[176:179] offset:128\n\t"
            "s_waitcnt lgkmcnt(11)\n\t"
            "v_smfmac_f32_16x16x64_bf16 v[184:187], v[168:171], v[144:151], v255\n\t"
            "ds_read_b128 v[168:171], v254 offset:512\n\t"
            "v_mfma_f32_16x16x32_bf16 v[192:195], v[238:241], v[176:179], v[192:195]\n\t"
            "ds_read_b64 v[144:145], v253 offset:72\n\t"
            "ds_read_b64 v[146:147], v253 offset:168\n\t"
            "s_waitcnt lgkmcnt(9)\n\t"
            "v_smfmac_f32_16x16x64_bf16 v[188:191], v[172:175], v[152:159], v255\n\t"
            "ds_read_b64 v[148:149], v253 offset:1816\n\t"
            "v_mfma_f32_4x4x4_16b_bf16 v[196:199], v[178:179], v[178:179], v[196:199]\n\t"
            "ds_read_b64 v[150:151], v253 offset:1912\n\t"
            "ds_read_b128 v[172:175], v254 offset:576\n\t"
            "ds_read_b64 v[152:153], v253 offset:80\n\t"
            "ds_read_b64 v[154:155], v253 offset:176\n\t"
            "ds_read_b64 v[156:157], v253 offset:1824\n\t"
            "s_waitcnt lgkmcnt(14)\n\t"
            "ds_read_b64 v[158:159], v253 offset:1920\n\t"
            "v_mov_b64 v[176:177], 0\n\t"
            "v_mov_b64 v[178:179], 0\n\t"
            "v_mov_b64 v[180:181], 0\n\t"
            "v_mov_b64 v[182:183], 0\n\t"
            "v_cvt_pk_bf16_f32 v184, v184, v185\n\t"
            "v_cvt_pk_bf16_f32 v185, v186, v187\n\t"
            "v_cvt_pk_bf16_f32 v186, v188, v189\n\t"
            "v_cvt_pk_bf16_f32 v187, v190, v191\n\t"
            "v_mfma_f32_4x4x4_16b_bf16 v[196:199], v[184:185], v[184:185], v[196:199]\n\t"
            "s_waitcnt lgkmcnt(14)\n\t"
            "ds_write_b128 v237, v[184:187] offset:128\n\t"
            "v_mfma_f32_16x16x32_bf16 v[192:195], v[238:241], v[184:187], v[192:195]\n\t"
            "v_mov_b64 v[184:185], 0\n\t"
            "v_mov_b64 v[188:189], 0\n\t"
            "s_waitcnt lgkmcnt(10)\n\t"
            "v_smfmac_f32_16x16x64_bf16 v[176:179], v[168:171], v[160:167], v255\n\t"
            "ds_read_b128 v[168:171], v254 offset:640\n\t"
            "v_mfma_f32_4x4x4_16b_bf16 v[196:199], v[186:187], v[186:187], v[196:199]\n\t"
            "ds_read_b64 v[160:161], v253 offset:88\n\t"
            "ds_read_b64 v[162:163], v253 offset:184\n\t"
            "ds_read_b64 v[164:165], v253 offset:1832\n\t"
            "s_waitcnt lgkmcnt(9)\n\t"
            "v_smfmac_f32_16x16x64_bf16 v[180:183], v[172:175], v[144:151], v255\n\t"
            "ds_read_b64 v[166:167], v253 offset:1928\n\t"
            "ds_read_b128 v[172:175], v254 offset:704\n\t"
            "v_mov_b64 v[186:187], 0\n\t"
            "v_mov_b64 v[190:191], 0\n\t"
            "v_writelane_b32 v248, s58, 63\n\t"
            "v_writelane_b32 v248, s38, m0\n\t"
            "v_and_b32 v249, s69, v249\n\t"
            "v_cvt_pk_bf16_f32 v176, v176, v177\n\t"
            "v_cvt_pk_bf16_f32 v177, v178, v179\n\t"
            "v_cvt_pk_bf16_f32 v178, v180, v181\n\t"
            "v_cvt_pk_bf16_f32 v179, v182, v183\n\t"
            "v_mfma_f32_4x4x4_16b_bf16 v[196:199], v[176:177], v[176:177], v[196:199]\n\t"
            "v_mul_f32 v220, 0x3baaaaab, v220\n\t"
            "v_mfma_f32_16x16x32_bf16 v[192:195], v[238:241], v[176:179], v[192:195]\n\t"
            "v_add_f32 v221, v221, v225\n\t"
            "v_mul_f32_e64 v220, v220, -v220\n\t"
            "v_mfma_f32_4x4x4_16b_bf16 v[196:199], v[178:179], v[178:179], v[196:199]\n\t"
            "ds_read_b128 v[224:227], v232 offset:6144\n\t"
            "v_fmac_f32 v220, 0x3baaaaab, v221\n\t"
            "v_max_f32 v220, 0, v220\n\t"
            "v_add_f32 v220, 0x3727c5ac, v220\n\t"
            "v_rsq_f32 v220, v220\n\t"
            "s_waitcnt lgkmcnt(6)\n\t"
            "v_smfmac_f32_16x16x64_bf16 v[184:187], v[168:171], v[152:159], v255\n\t"
            "ds_write_b32 v228, v220\n\t"
            "ds_read_b128 v[220:223], v235 offset:256\n\t"
            "ds_read_b32 v230, v233\n\t"
            "ds_read_b32 v231, v233 offset:64\n\t"
            "s_waitcnt lgkmcnt(5)\n\t"
            "v_smfmac_f32_16x16x64_bf16 v[188:191], v[172:175], v[160:167], v255\n\t"
            "s_mov_b32 exec_hi, 0x3fffff\n\t"
            "s_add_u32 m0, s66, 0\n\t"
            "s_add_u32 s49, s79, 0\n\t"
            "buffer_load_dwordx4 v249, s[72:75], s49 offen lds\n\t"
            "s_add_u32 s49, s68, 195352\n\t"
            "buffer_load_dwordx4 v249, s[72:75], s49 offen offset:872 lds\n\t"
            "s_add_u32 s49, s68, 390704\n\t"
            "buffer_load_dwordx4 v249, s[72:75], s49 offen offset:1744 lds\n\t"
            "s_mov_b32 exec_hi, -1\n\t"
            "s_mov_b32 exec_hi, 0x3fffff\n\t"
            "s_add_u32 m0, s66, 2616\n\t"
            "s_add_u32 s49, s68, 588672\n\t"
            "buffer_load_dwordx4 v249, s[72:75], s49 offen lds\n\t"
            "s_add_u32 s49, s68, 784024\n\t"
            "buffer_load_dwordx4 v249, s[72:75], s49 offen offset:872 lds\n\t"
            "s_add_u32 s49, s68, 979376\n\t"
            "buffer_load_dwordx4 v249, s[72:75], s49 offen offset:1744 lds\n\t"
            "s_mov_b32 exec_hi, -1\n\t"
            "s_mov_b32 exec_hi, 0x3fffff\n\t"
            "s_add_u32 m0, s66, 5232\n\t"
            "s_add_u32 s49, s68, 1177344\n\t"
            "buffer_load_dwordx4 v249, s[72:75], s49 offen lds\n\t"
            "s_add_u32 s49, s68, 1372696\n\t"
            "buffer_load_dwordx4 v249, s[72:75], s49 offen offset:872 lds\n\t"
            "s_add_u32 s49, s68, 1568048\n\t"
            "buffer_load_dwordx4 v249, s[72:75], s49 offen offset:1744 lds\n\t"
            "s_mov_b32 exec_hi, -1\n\t"
            "v_cvt_pk_bf16_f32 v184, v184, v185\n\t"
            "v_cvt_pk_bf16_f32 v185, v186, v187\n\t"
            "v_cvt_pk_bf16_f32 v186, v188, v189\n\t"
            "v_cvt_pk_bf16_f32 v187, v190, v191\n\t"
            "v_mfma_f32_4x4x4_16b_bf16 v[196:199], v[184:185], v[184:185], v[196:199]\n\t"
            "s_waitcnt vmcnt(9)\n\t"
            "v_mfma_f32_16x16x32_bf16 v[192:195], v[238:241], v[184:187], v[192:195]\n\t"
            "ds_write_b128 v236, v[176:179] offset:256\n\t"
            "v_mfma_f32_4x4x4_16b_bf16 v[196:199], v[186:187], v[186:187], v[196:199]\n\t"
            "ds_write_b128 v237, v[184:187] offset:256\n\t"
            "v_mfma_f32_16x16x32_bf16 v[144:147], v[0:3], v[200:203], 0\n\t"
            "v_xor_b32 v228, 32, v234\n\t"
            "v_add_u32 v236, s48, v244\n\t"
            "v_mfma_f32_16x16x32_bf16 v[144:147], v[4:7], v[204:207], v[144:147]\n\t"
            "v_add_u32 v237, s48, v245\n\t"
            "v_mfma_f32_16x16x32_bf16 v[148:151], v[24:27], v[200:203], 0\n\t"
            "s_nop 0\n\t"
            "v_cndmask_b32_e64 v193, v196, v197, s[60:61]\n\t"
            "v_cndmask_b32_e64 v193, v193, v198, s[62:63]\n\t"
            "v_mfma_f32_16x16x32_bf16 v[148:151], v[28:31], v[204:207], v[148:151]\n\t"
            "v_cndmask_b32_e64 v193, v193, v199, s[64:65]\n\t"
            "v_mfma_f32_16x16x32_bf16 v[168:171], v[48:51], v[200:203], 0\n\t"
            "ds_read_b128 v[196:199], v232 offset:6272\n\t"
            "v_mfma_f32_16x16x32_bf16 v[168:171], v[52:55], v[204:207], v[168:171]\n\t"
            "ds_write_b64 v248, v[192:193] offset:18816\n\t"
            "v_mfma_f32_16x16x32_bf16 v[172:175], v[72:75], v[200:203], 0\n\t"
            "ds_read_b128 v[192:195], v235 offset:6144\n\t"
            "v_add_u32 v248, s48, v246\n\t"
            "v_mfma_f32_16x16x32_bf16 v[172:175], v[76:79], v[204:207], v[172:175]\n\t"
            "s_mov_b64 exec, 1\n\t"
            "ds_inc_u32 v255, v238 offset:7860\n\t"
            "s_mov_b64 exec, -1\n\t"
            "v_mfma_f32_16x16x32_bf16 v[152:155], v[96:99], v[200:203], 0\n\t"
            "v_mfma_f32_16x16x32_bf16 v[152:155], v[100:103], v[204:207], v[152:155]\n\t"
            "v_mfma_f32_16x16x32_bf16 v[156:159], v[120:123], v[200:203], 0\n\t"
            "ds_read_b128 v[200:203], v235 offset:6272\n\t"
            "v_mfma_f32_16x16x32_bf16 v[156:159], v[124:127], v[204:207], v[156:159]\n\t"
            "ds_read_b128 v[204:207], v232 offset:6400\n\t"
            "v_mfma_f32_16x16x32_bf16 v[144:147], v[8:11], v[208:211], v[144:147]\n\t"
            "v_mfma_f32_16x16x32_bf16 v[144:147], v[12:15], v[212:215], v[144:147]\n\t"
            "v_mfma_f32_16x16x32_bf16 v[148:151], v[32:35], v[208:211], v[148:151]\n\t"
            "v_mfma_f32_16x16x32_bf16 v[148:151], v[36:39], v[212:215], v[148:151]\n\t"
            "v_mfma_f32_16x16x32_bf16 v[168:171], v[56:59], v[208:211], v[168:171]\n\t"
            "v_mfma_f32_16x16x32_bf16 v[168:171], v[60:63], v[212:215], v[168:171]\n\t"
            "v_mfma_f32_16x16x32_bf16 v[172:175], v[80:83], v[208:211], v[172:175]\n\t"
            "v_mfma_f32_16x16x32_bf16 v[172:175], v[84:87], v[212:215], v[172:175]\n\t"
            "v_mfma_f32_16x16x32_bf16 v[152:155], v[104:107], v[208:211], v[152:155]\n\t"
            "v_mfma_f32_16x16x32_bf16 v[152:155], v[108:111], v[212:215], v[152:155]\n\t"
            "v_mfma_f32_16x16x32_bf16 v[156:159], v[128:131], v[208:211], v[156:159]\n\t"
            "ds_read_b128 v[208:211], v235 offset:6400\n\t"
            "v_mfma_f32_16x16x32_bf16 v[156:159], v[132:135], v[212:215], v[156:159]\n\t"
            "ds_read_b128 v[212:215], v236 offset:6144\n\t"
            "s_waitcnt lgkmcnt(12)\n\t"
            "v_mfma_f32_16x16x32_bf16 v[144:147], v[16:19], v[216:219], v[144:147]\n\t"
            "v_mfma_f32_16x16x32_bf16 v[144:147], v[20:23], v[220:223], v[144:147]\n\t"
            "v_mfma_f32_16x16x32_bf16 v[148:151], v[40:43], v[216:219], v[148:151]\n\t"
            "v_mfma_f32_16x16x32_bf16 v[148:151], v[44:47], v[220:223], v[148:151]\n\t"
            "v_mfma_f32_16x16x32_bf16 v[168:171], v[64:67], v[216:219], v[168:171]\n\t"
            "v_mfma_f32_16x16x32_bf16 v[168:171], v[68:71], v[220:223], v[168:171]\n\t"
            "v_mfma_f32_16x16x32_bf16 v[172:175], v[88:91], v[216:219], v[172:175]\n\t"
            "v_mfma_f32_16x16x32_bf16 v[172:175], v[92:95], v[220:223], v[172:175]\n\t"
            "s_waitcnt lgkmcnt(11)\n\t"
            "s_nop 0\n\t"
            "v_mul_f32 v144, v230, v144\n\t"
            "v_mfma_f32_16x16x32_bf16 v[152:155], v[112:115], v[216:219], v[152:155]\n\t"
            "v_mul_f32 v145, v230, v145\n\t"
            "v_mul_f32 v146, v230, v146\n\t"
            "v_mfma_f32_16x16x32_bf16 v[152:155], v[116:119], v[220:223], v[152:155]\n\t"
            "v_mul_f32 v148, v230, v148\n\t"
            "v_mul_f32 v149, v230, v149\n\t"
            "v_mfma_f32_16x16x32_bf16 v[156:159], v[136:139], v[216:219], v[156:159]\n\t"
            "ds_read_b128 v[216:219], v237 offset:6144\n\t"
            "v_mul_f32 v168, v230, v168\n\t"
            "v_mfma_f32_16x16x32_bf16 v[156:159], v[140:143], v[220:223], v[156:159]\n\t"
            "ds_read_b128 v[220:223], v248 offset:6144\n\t"
            "v_mul_f32 v169, v230, v169\n\t"
            "s_waitcnt lgkmcnt(7)\n\t"
            "v_mfma_f32_16x16x32_bf16 v[160:163], v[0:3], v[224:227], 0\n\t"
            "v_mul_f32 v172, v230, v172\n\t"
            "v_mul_f32 v173, v230, v173\n\t"
            "v_mfma_f32_16x16x32_bf16 v[160:163], v[4:7], v[192:195], v[160:163]\n\t"
            "v_mul_f32 v152, v230, v152\n\t"
            "v_mul_f32 v153, v230, v153\n\t"
            "v_mfma_f32_16x16x32_bf16 v[164:167], v[24:27], v[224:227], 0\n\t"
            "v_mul_f32 v156, v230, v156\n\t"
            "v_mul_f32 v157, v230, v157\n\t"
            "v_mfma_f32_16x16x32_bf16 v[164:167], v[28:31], v[192:195], v[164:167]\n\t"
            "v_mul_f32 v147, v230, v147\n\t"
            "v_cvt_pk_bf16_f32 v144, v144, v145\n\t"
            "v_mfma_f32_16x16x32_bf16 v[176:179], v[48:51], v[224:227], 0\n\t"
            "v_mul_f32 v150, v230, v150\n\t"
            "v_mul_f32 v151, v230, v151\n\t"
            "v_mfma_f32_16x16x32_bf16 v[176:179], v[52:55], v[192:195], v[176:179]\n\t"
            "v_cvt_pk_bf16_f32 v148, v148, v149\n\t"
            "v_mul_f32 v170, v230, v170\n\t"
            "v_mfma_f32_16x16x32_bf16 v[180:183], v[72:75], v[224:227], 0\n\t"
            "v_mul_f32 v171, v230, v171\n\t"
            "v_cvt_pk_bf16_f32 v168, v168, v169\n\t"
            "v_mfma_f32_16x16x32_bf16 v[180:183], v[76:79], v[192:195], v[180:183]\n\t"
            "v_mul_f32 v174, v230, v174\n\t"
            "v_mul_f32 v175, v230, v175\n\t"
            "v_mfma_f32_16x16x32_bf16 v[184:187], v[96:99], v[224:227], 0\n\t"
            "v_cvt_pk_bf16_f32 v172, v172, v173\n\t"
            "v_mul_f32 v154, v230, v154\n\t"
            "v_mfma_f32_16x16x32_bf16 v[184:187], v[100:103], v[192:195], v[184:187]\n\t"
            "v_mul_f32 v155, v230, v155\n\t"
            "v_cvt_pk_bf16_f32 v152, v152, v153\n\t"
            "v_mfma_f32_16x16x32_bf16 v[188:191], v[120:123], v[224:227], 0\n\t"
            "ds_read_b128 v[224:227], v236 offset:12288\n\t"
            "v_mul_f32 v158, v230, v158\n\t"
            "v_mfma_f32_16x16x32_bf16 v[188:191], v[124:127], v[192:195], v[188:191]\n\t"
            "v_mul_f32 v159, v230, v159\n\t"
            "v_cvt_pk_bf16_f32 v156, v156, v157\n\t"
            "s_waitcnt lgkmcnt(6)\n\t"
            "v_mfma_f32_16x16x32_bf16 v[160:163], v[8:11], v[196:199], v[160:163]\n\t"
            "v_cvt_pk_bf16_f32 v145, v146, v147\n\t"
            "v_cvt_pk_bf16_f32 v149, v150, v151\n\t"
            "v_mfma_f32_16x16x32_bf16 v[160:163], v[12:15], v[200:203], v[160:163]\n\t"
            "v_cvt_pk_bf16_f32 v169, v170, v171\n\t"
            "v_cvt_pk_bf16_f32 v173, v174, v175\n\t"
            "v_mfma_f32_16x16x32_bf16 v[164:167], v[32:35], v[196:199], v[164:167]\n\t"
            "v_cvt_pk_bf16_f32 v153, v154, v155\n\t"
            "v_cvt_pk_bf16_f32 v157, v158, v159\n\t"
            "v_mfma_f32_16x16x32_bf16 v[164:167], v[36:39], v[200:203], v[164:167]\n\t"
            "s_mov_b64 exec, -1\n\t"
            "ds_write_b64 v234, v[144:145]\n\t"
            "ds_write_b64 v234, v[148:149] offset:128\n\t"
            "ds_write_b64 v234, v[168:169] offset:256\n\t"
            "ds_write_b64 v228, v[172:173]\n\t"
            "ds_write_b64 v228, v[152:153] offset:128\n\t"
            "ds_write_b64 v228, v[156:157] offset:256\n\t"
            "s_mov_b64 exec, -1\n\t"
            "v_mfma_f32_16x16x32_bf16 v[176:179], v[56:59], v[196:199], v[176:179]\n\t"
            "v_mfma_f32_16x16x32_bf16 v[176:179], v[60:63], v[200:203], v[176:179]\n\t"
            "s_add_u32 s49, s33, 0\n\t"
            "buffer_load_dwordx4 v[0:3], v247, s[28:31], s49 offen sc1\n\t"
            "v_mfma_f32_16x16x32_bf16 v[180:183], v[80:83], v[196:199], v[180:183]\n\t"
            "v_mfma_f32_16x16x32_bf16 v[180:183], v[84:87], v[200:203], v[180:183]\n\t"
            "s_add_u32 s49, s33, 6144\n\t"
            "buffer_load_dwordx4 v[24:27], v247, s[28:31], s49 offen sc1\n\t"
            "v_mfma_f32_16x16x32_bf16 v[184:187], v[104:107], v[196:199], v[184:187]\n\t"
            "v_mfma_f32_16x16x32_bf16 v[184:187], v[108:111], v[200:203], v[184:187]\n\t"
            "s_add_u32 s49, s33, 12288\n\t"
            "buffer_load_dwordx4 v[48:51], v247, s[28:31], s49 offen sc1\n\t"
            "v_mfma_f32_16x16x32_bf16 v[188:191], v[128:131], v[196:199], v[188:191]\n\t"
            "v_mfma_f32_16x16x32_bf16 v[188:191], v[132:135], v[200:203], v[188:191]\n\t"
            "ds_read_b128 v[200:203], v236\n\t"
            "s_add_u32 s49, s33, 18432\n\t"
            "buffer_load_dwordx4 v[72:75], v247, s[28:31], s49 offen sc1\n\t"
            "s_waitcnt lgkmcnt(11)\n\t"
            "v_mfma_f32_16x16x32_bf16 v[160:163], v[16:19], v[204:207], v[160:163]\n\t"
            "v_mfma_f32_16x16x32_bf16 v[160:163], v[20:23], v[208:211], v[160:163]\n\t"
            "s_add_u32 s49, s33, 24576\n\t"
            "buffer_load_dwordx4 v[96:99], v247, s[28:31], s49 offen sc1\n\t"
            "v_mfma_f32_16x16x32_bf16 v[164:167], v[40:43], v[204:207], v[164:167]\n\t"
            "v_mfma_f32_16x16x32_bf16 v[164:167], v[44:47], v[208:211], v[164:167]\n\t"
            "s_add_u32 s49, s33, 30720\n\t"
            "buffer_load_dwordx4 v[120:123], v247, s[28:31], s49 offen sc1\n\t"
            "v_mfma_f32_16x16x32_bf16 v[176:179], v[64:67], v[204:207], v[176:179]\n\t"
            "s_nop 0\n\t"
            "v_mul_f32 v160, v231, v160\n\t"
            "v_mul_f32 v161, v231, v161\n\t"
            "v_mfma_f32_16x16x32_bf16 v[176:179], v[68:71], v[208:211], v[176:179]\n\t"
            "v_mul_f32 v162, v231, v162\n\t"
            "v_mul_f32 v163, v231, v163\n\t"
            "v_mfma_f32_16x16x32_bf16 v[180:183], v[88:91], v[204:207], v[180:183]\n\t"
            "v_mul_f32 v164, v231, v164\n\t"
            "v_mul_f32 v165, v231, v165\n\t"
            "v_mfma_f32_16x16x32_bf16 v[180:183], v[92:95], v[208:211], v[180:183]\n\t"
            "v_cvt_pk_bf16_f32 v160, v160, v161\n\t"
            "v_mul_f32 v166, v231, v166\n\t"
            "v_mfma_f32_16x16x32_bf16 v[184:187], v[112:115], v[204:207], v[184:187]\n\t"
            "v_mul_f32 v176, v231, v176\n\t"
            "v_mul_f32 v177, v231, v177\n\t"
            "v_mfma_f32_16x16x32_bf16 v[184:187], v[116:119], v[208:211], v[184:187]\n\t"
            "v_mul_f32 v167, v231, v167\n\t"
            "v_cvt_pk_bf16_f32 v164, v164, v165\n\t"
            "v_mfma_f32_16x16x32_bf16 v[188:191], v[136:139], v[204:207], v[188:191]\n\t"
            "ds_read_b128 v[204:207], v237\n\t"
            "v_mul_f32 v180, v231, v180\n\t"
            "v_mfma_f32_16x16x32_bf16 v[188:191], v[140:143], v[208:211], v[188:191]\n\t"
            "ds_read_b128 v[208:211], v248\n\t"
            "v_mul_f32 v181, v231, v181\n\t"
            "v_mul_f32 v184, v231, v184\n\t"
            "v_mul_f32 v185, v231, v185\n\t"
            "v_mul_f32 v178, v231, v178\n\t"
            "v_mul_f32 v179, v231, v179\n\t"
            "v_cvt_pk_bf16_f32 v176, v176, v177\n\t"
            "v_mul_f32 v182, v231, v182\n\t"
            "v_mul_f32 v188, v231, v188\n\t"
            "v_mul_f32 v189, v231, v189\n\t"
            "v_mul_f32 v183, v231, v183\n\t"
            "v_cvt_pk_bf16_f32 v180, v180, v181\n\t"
            "v_mul_f32 v186, v231, v186\n\t"
            "v_mul_f32 v187, v231, v187\n\t"
            "v_cvt_pk_bf16_f32 v184, v184, v185\n\t"
            "v_mul_f32 v190, v231, v190\n\t"
            "v_mul_f32 v191, v231, v191\n\t"
            "v_cvt_pk_bf16_f32 v188, v188, v189\n\t"
            "v_cvt_pk_bf16_f32 v161, v162, v163\n\t"
            "v_cvt_pk_bf16_f32 v165, v166, v167\n\t"
            "v_cvt_pk_bf16_f32 v177, v178, v179\n\t"
            "v_cvt_pk_bf16_f32 v181, v182, v183\n\t"
            "v_cvt_pk_bf16_f32 v185, v186, v187\n\t"
            "v_cvt_pk_bf16_f32 v189, v190, v191\n\t"
            "s_waitcnt lgkmcnt(9)\n\t"
            "s_mov_b64 exec, s[80:81]\n\t"
            "ds_write_b64 v234, v[160:161] offset:6144\n\t"
            "ds_write_b64 v234, v[164:165] offset:6272\n\t"
            "ds_write_b64 v234, v[176:177] offset:6400\n\t"
            "ds_write_b64 v228, v[180:181] offset:6144\n\t"
            "ds_write_b64 v228, v[184:185] offset:6272\n\t"
            "ds_write_b64 v228, v[188:189] offset:6400\n\t"
            "s_mov_b64 exec, -1\n\t"
            "s_waitcnt lgkmcnt(14)\n\t"
            "ds_read_b128 v[228:231], v237 offset:12288\n\t"
            "s_waitcnt lgkmcnt(14)\n\t"
            "ds_read_b128 v[232:235], v248 offset:12288\n\t"
            "s_add_u32 s49, s33, 1024\n\t"
            "buffer_load_dwordx4 v[4:7], v247, s[28:31], s49 offen sc1\n\t"
            "s_add_u32 s49, s33, 7168\n\t"
            "buffer_load_dwordx4 v[28:31], v247, s[28:31], s49 offen sc1\n\t"
            "s_waitcnt lgkmcnt(0)\n\t"
            "s_barrier\n\t"
            "s_setprio 3\n\t"
            "s_add_u32 s49, s33, 13312\n\t"
            "buffer_load_dwordx4 v[52:55], v247, s[28:31], s49 offen sc1\n\t"
            "s_add_u32 s49, s33, 19456\n\t"
            "buffer_load_dwordx4 v[76:79], v247, s[28:31], s49 offen sc1\n\t"
            "s_add_u32 s49, s33, 25600\n\t"
            "buffer_load_dwordx4 v[100:103], v247, s[28:31], s49 offen sc1\n\t"
            "s_add_u32 s49, s33, 31744\n\t"
            "buffer_load_dwordx4 v[124:127], v247, s[28:31], s49 offen sc1\n\t"
            "s_add_u32 s49, s33, 2048\n\t"
            "buffer_load_dwordx4 v[8:11], v247, s[28:31], s49 offen sc1\n\t"
            "s_add_u32 s49, s33, 8192\n\t"
            "buffer_load_dwordx4 v[32:35], v247, s[28:31], s49 offen sc1\n\t"
            "s_add_u32 s49, s33, 14336\n\t"
            "buffer_load_dwordx4 v[56:59], v247, s[28:31], s49 offen sc1\n\t"
            "s_add_u32 s49, s33, 20480\n\t"
            "buffer_load_dwordx4 v[80:83], v247, s[28:31], s49 offen sc1\n\t"
            "s_add_u32 s49, s33, 26624\n\t"
            "buffer_load_dwordx4 v[104:107], v247, s[28:31], s49 offen sc1\n\t"
            "s_add_u32 s49, s33, 32768\n\t"
            "buffer_load_dwordx4 v[128:131], v247, s[28:31], s49 offen sc1\n\t"
            "s_add_u32 s49, s33, 3072\n\t"
            "buffer_load_dwordx4 v[12:15], v247, s[28:31], s49 offen sc1\n\t"
            "s_add_u32 s49, s33, 9216\n\t"
            "buffer_load_dwordx4 v[36:39], v247, s[28:31], s49 offen sc1\n\t"
            "s_add_u32 s49, s33, 15360\n\t"
            "buffer_load_dwordx4 v[60:63], v247, s[28:31], s49 offen sc1\n\t"
            "s_add_u32 s49, s33, 21504\n\t"
            "buffer_load_dwordx4 v[84:87], v247, s[28:31], s49 offen sc1\n\t"
            "s_add_u32 s49, s33, 27648\n\t"
            "buffer_load_dwordx4 v[108:111], v247, s[28:31], s49 offen sc1\n\t"
            "s_add_u32 s49, s33, 33792\n\t"
            "buffer_load_dwordx4 v[132:135], v247, s[28:31], s49 offen sc1\n\t"
            "s_add_u32 s49, s33, 4096\n\t"
            "buffer_load_dwordx4 v[16:19], v247, s[28:31], s49 offen sc1\n\t"
            "s_add_u32 s49, s33, 10240\n\t"
            "buffer_load_dwordx4 v[40:43], v247, s[28:31], s49 offen sc1\n\t"
            "s_add_u32 s49, s33, 16384\n\t"
            "buffer_load_dwordx4 v[64:67], v247, s[28:31], s49 offen sc1\n\t"
            "s_add_u32 s49, s33, 22528\n\t"
            "buffer_load_dwordx4 v[88:91], v247, s[28:31], s49 offen sc1\n\t"
            "s_add_u32 s49, s33, 28672\n\t"
            "buffer_load_dwordx4 v[112:115], v247, s[28:31], s49 offen sc1\n\t"
            "s_add_u32 s49, s33, 34816\n\t"
            "buffer_load_dwordx4 v[136:139], v247, s[28:31], s49 offen sc1\n\t"
            "s_add_u32 s49, s33, 5120\n\t"
            "buffer_load_dwordx4 v[20:23], v247, s[28:31], s49 offen sc1\n\t"
            "s_add_u32 s49, s33, 11264\n\t"
            "buffer_load_dwordx4 v[44:47], v247, s[28:31], s49 offen sc1\n\t"
            "s_add_u32 s49, s33, 17408\n\t"
            "buffer_load_dwordx4 v[68:71], v247, s[28:31], s49 offen sc1\n\t"
            "s_add_u32 s49, s33, 23552\n\t"
            "buffer_load_dwordx4 v[92:95], v247, s[28:31], s49 offen sc1\n\t"
            "s_add_u32 s49, s33, 29696\n\t"
            "buffer_load_dwordx4 v[116:119], v247, s[28:31], s49 offen sc1\n\t"
            "s_add_u32 s49, s33, 35840\n\t"
            "buffer_load_dwordx4 v[140:143], v247, s[28:31], s49 offen sc1\n\t"
            "s_add_u32 s59, s59, s84\n\t"
            "s_movk_i32 s84, 800\n\t"
            "s_add_u32 s45, s46, s27\n\t"
            "s_mov_b32 s46, s47\n\t"
            "s_mov_b32 s47, s68\n\t"
            "s_xor_b32 s48, s48, s71\n\t"
            "s_xor_b32 s67, s67, s71\n\t"
            "s_add_u32 s76, s76, 4\n\t"
            "s_add_u32 s70, s70, 4\n\t"
            "s_lshl_b32 s56, s56, 2\n\t"
            "s_lshr_b32 s49, s26, 30\n\t"
            "s_or_b32 s56, s56, s49\n\t"
            "s_cmp_eq_u32 s82, 0\n\t"
            "s_cbranch_scc1 .Lwk_nr%=\n\t"
            "s_waitcnt lgkmcnt(0)\n\t"
            "s_nop 7\n\t"
            "v_add_u32 v236, 127360, v247\n\t"
            "v_add_u32 v237, 136704, v247\n\t"
            "ds_read_b128 v[144:147], v236\n\t"
            "ds_read_b128 v[148:151], v237\n\t"
            "s_waitcnt lgkmcnt(0)\n\t"
            "s_mov_b32 exec_hi, 0x3ffff\n\t"
            "buffer_store_dwordx4 v[144:147], v247, s[28:31], s82 offen sc1\n\t"
            "s_mov_b64 exec, 0xffffff\n\t"
            "buffer_store_dwordx4 v[148:151], v247, s[28:31], s82 offen offset:800 sc1\n\t"
            "s_mov_b64 exec, -1\n\t"
            "ds_read_b128 v[152:155], v252 offset:2560\n\t"
            "s_waitcnt vmcnt(0) lgkmcnt(0)\n\t"
            "s_mov_b64 exec, 1\n\t"
            "buffer_store_dwordx4 v[152:155], off, s[28:31], s82 offset:1216 sc1\n\t"
            "s_mov_b64 exec, -1\n\t"
            "s_branch .Lwk_n2%=\n\t"
            ".Lwk_nr%=:\n\t"
            "s_waitcnt vmcnt(0)\n\t"
            ".Lwk_n2%=:\n\t"
            "s_nop 1\n\t"
            "s_nop 7\n\t"
            ".p2align 6\n\t"
            "s_nop 0\n\t"
            "s_nop 0\n\t"
            "s_nop 0\n\t"
            "s_nop 0\n\t"
            "s_nop 0\n\t"
            "s_nop 0\n\t"
            "s_nop 0\n\t"
            ".Lwb_loop%=:\n\t"
            "s_setprio 1\n\t"
            "ds_read_b128 v[168:171], v254\n\t"
            "ds_read_b128 v[172:175], v254 offset:64\n\t"
            "s_waitcnt vmcnt(9)\n\t"
            "ds_read_b64 v[144:145], v253\n\t"
            "ds_read_b64 v[146:147], v253 offset:96\n\t"
            "ds_read_b64 v[148:149], v253 offset:1744\n\t"
            "ds_read_b64 v[150:151], v253 offset:1840\n\t"
            "ds_read_b64 v[152:153], v253 offset:8\n\t"
            "ds_read_b64 v[154:155], v253 offset:104\n\t"
            "ds_read_b64 v[156:157], v253 offset:1752\n\t"
            "ds_read_b64 v[158:159], v253 offset:1848\n\t"
            "ds_read_b64 v[160:161], v253 offset:16\n\t"
            "ds_read_b64 v[162:163], v253 offset:112\n\t"
            "ds_read_b64 v[164:165], v253 offset:1760\n\t"
            "ds_read_b64 v[166:167], v253 offset:1856\n\t"
            "v_mov_b64 v[176:177], 0\n\t"
            "v_mov_b64 v[178:179], 0\n\t"
            "v_mov_b64 v[180:181], 0\n\t"
            "v_mov_b64 v[182:183], 0\n\t"
            "v_mov_b32 v249, s76\n\t"
            "v_mov_b64 v[184:185], 0\n\t"
            "v_mov_b64 v[186:187], 0\n\t"
            "v_mov_b64 v[188:189], 0\n\t"
            "v_mov_b64 v[190:191], 0\n\t"
            "v_add_u32 v236, s67, v250\n\t"
            "v_add_u32 v248, s67, v251\n\t"
            "v_mov_b32 v247, 0\n\t"
            "ds_read_b32 v249, v249\n\t"
            "s_setprio 3\n\t"
            "s_waitcnt lgkmcnt(9)\n\t"
            "v_smfmac_f32_16x16x64_bf16 v[176:179], v[168:171], v[144:151], v255\n\t"
            "ds_read_b128 v[168:171], v254 offset:128\n\t"
            "s_waitcnt lgkmcnt(6)\n\t"
            "v_smfmac_f32_16x16x64_bf16 v[180:183], v[172:175], v[152:159], v255\n\t"
            "s_setprio 1\n\t"
            "ds_read_b64 v[144:145], v253 offset:24\n\t"
            "ds_read_b64 v[146:147], v253 offset:120\n\t"
            "ds_read_b64 v[148:149], v253 offset:1768\n\t"
            "ds_read_b64 v[150:151], v253 offset:1864\n\t"
            "ds_read_b128 v[172:175], v254 offset:192\n\t"
            "ds_read_b64 v[152:153], v253 offset:32\n\t"
            "ds_read_b64 v[154:155], v253 offset:128\n\t"
            "ds_read_b64 v[156:157], v253 offset:1776\n\t"
            "ds_read_b64 v[158:159], v253 offset:1872\n\t"
            "v_cvt_pk_bf16_f32 v176, v176, v177\n\t"
            "v_cvt_pk_bf16_f32 v177, v178, v179\n\t"
            "v_cvt_pk_bf16_f32 v178, v180, v181\n\t"
            "v_cvt_pk_bf16_f32 v179, v182, v183\n\t"
            "s_setprio 3\n\t"
            "v_mfma_f32_4x4x4_16b_bf16 v[196:199], v[176:177], v[176:177], 0\n\t"
            "v_mov_b64 v[180:181], 0\n\t"
            "v_mfma_f32_16x16x32_bf16 v[192:195], v[238:241], v[176:179], 0\n\t"
            "s_waitcnt lgkmcnt(10)\n\t"
            "v_readfirstlane_b32 s26, v249\n\t"
            "v_mov_b64 v[182:183], 0\n\t"
            "v_mfma_f32_4x4x4_16b_bf16 v[196:199], v[178:179], v[178:179], v[196:199]\n\t"
            "ds_read_b32 v249, v252 offset:12\n\t"
            "s_and_b32 s68, s26, 0x7ffffff\n\t"
            "s_add_u32 s68, s68, s78\n\t"
            "s_and_b32 s69, s26, 0x38000000\n\t"
            "s_or_b32 s69, s69, s39\n\t"
            "s_bitcmp1_b32 s26, 27\n\t"
            "s_cselect_b32 s79, 2.0, s68\n\t"
            "s_waitcnt lgkmcnt(10)\n\t"
            "v_smfmac_f32_16x16x64_bf16 v[184:187], v[168:171], v[160:167], v255\n\t"
            "s_and_b32 s57, s56, 3\n\t"
            "s_lshl_b32 s58, s57, 3\n\t"
            "s_lshr_b32 m0, s54, s58\n\t"
            "ds_read_b128 v[168:171], v254 offset:256\n\t"
            "ds_read_b64 v[160:161], v253 offset:40\n\t"
            "ds_read_b64 v[162:163], v253 offset:136\n\t"
            "ds_read_b64 v[164:165], v253 offset:1784\n\t"
            "s_waitcnt lgkmcnt(9)\n\t"
            "v_smfmac_f32_16x16x64_bf16 v[188:191], v[172:175], v[144:151], v255\n\t"
            "s_setprio 1\n\t"
            "s_lshl_b32 s58, s57, 4\n\t"
            "s_lshr_b64 s[36:37], s[50:51], s58\n\t"
            "s_and_b32 s57, s36, 0xffff\n\t"
            "s_add_u32 s57, s57, s67\n\t"
            "ds_read_b64 v[166:167], v253 offset:1880\n\t"
            "ds_read_b128 v[172:175], v254 offset:320\n\t"
            "ds_read_b64 v[144:145], v253 offset:48\n\t"
            "ds_read_b64 v[146:147], v253 offset:144\n\t"
            "ds_read_b64 v[148:149], v253 offset:1792\n\t"
            "ds_read_b64 v[150:151], v253 offset:1888\n\t"
            "s_setprio 3\n\t"
            "v_writelane_b32 v236, s57, 63\n\t"
            "v_writelane_b32 v236, s59, m0\n\t"
            "v_cvt_pk_bf16_f32 v184, v184, v185\n\t"
            "v_cvt_pk_bf16_f32 v185, v186, v187\n\t"
            "v_xor_b32 v237, 16, v236\n\t"
            "v_cvt_pk_bf16_f32 v186, v188, v189\n\t"
            "v_mfma_f32_4x4x4_16b_bf16 v[196:199], v[184:185], v[184:185], v[196:199]\n\t"
            "s_waitcnt lgkmcnt(14)\n\t"
            "ds_write_b128 v236, v[176:179]\n\t"
            "v_mov_b64 v[176:177], 0\n\t"
            "v_mov_b64 v[178:179], 0\n\t"
            "v_cvt_pk_bf16_f32 v187, v190, v191\n\t"
            "v_mov_b64 v[188:189], 0\n\t"
            "s_waitcnt lgkmcnt(10)\n\t"
            "v_smfmac_f32_16x16x64_bf16 v[176:179], v[168:171], v[152:159], v255\n\t"
            "s_lshr_b64 s[36:37], s[52:53], s58\n\t"
            "s_and_b32 s58, s36, 0xffff\n\t"
            "s_add_u32 s58, s58, s67\n\t"
            "v_mfma_f32_16x16x32_bf16 v[192:195], v[238:241], v[184:187], v[192:195]\n\t"
            "s_and_b32 s38, s52, 0xffff\n\t"
            "s_add_u32 s38, s38, s67\n\t"
            "ds_read_b128 v[168:171], v254 offset:384\n\t"
            "v_mfma_f32_4x4x4_16b_bf16 v[196:199], v[186:187], v[186:187], v[196:199]\n\t"
            "ds_read_b64 v[152:153], v253 offset:56\n\t"
            "s_waitcnt lgkmcnt(7)\n\t"
            "v_smfmac_f32_16x16x64_bf16 v[180:183], v[172:175], v[160:167], v255\n\t"
            "ds_read_b64 v[154:155], v253 offset:152\n\t"
            "ds_read_b64 v[156:157], v253 offset:1800\n\t"
            "s_setprio 1\n\t"
            "ds_read_b64 v[158:159], v253 offset:1896\n\t"
            "ds_read_b128 v[172:175], v254 offset:448\n\t"
            "ds_read_b64 v[160:161], v253 offset:64\n\t"
            "ds_read_b64 v[162:163], v253 offset:160\n\t"
            "ds_read_b64 v[164:165], v253 offset:1808\n\t"
            "ds_read_b64 v[166:167], v253 offset:1904\n\t"
            "s_waitcnt lgkmcnt(14)\n\t"
            "ds_write_b128 v237, v[184:187]\n\t"
            "v_mov_b64 v[184:185], 0\n\t"
            "v_mov_b64 v[186:187], 0\n\t"
            "v_mov_b64 v[190:191], 0\n\t"
            "v_cvt_pk_bf16_f32 v176, v176, v177\n\t"
            "v_cvt_pk_bf16_f32 v177, v178, v179\n\t"
            "v_cvt_pk_bf16_f32 v178, v180, v181\n\t"
            "v_cvt_pk_bf16_f32 v179, v182, v183\n\t"
            "s_setprio 3\n\t"
            "v_mfma_f32_4x4x4_16b_bf16 v[196:199], v[176:177], v[176:177], v[196:199]\n\t"
            "s_waitcnt lgkmcnt(14)\n\t"
            "ds_write_b128 v236, v[176:179] offset:128\n\t"
            "s_waitcnt lgkmcnt(11)\n\t"
            "v_smfmac_f32_16x16x64_bf16 v[184:187], v[168:171], v[144:151], v255\n\t"
            "ds_read_b128 v[168:171], v254 offset:512\n\t"
            "v_mfma_f32_16x16x32_bf16 v[192:195], v[238:241], v[176:179], v[192:195]\n\t"
            "ds_read_b64 v[144:145], v253 offset:72\n\t"
            "ds_read_b64 v[146:147], v253 offset:168\n\t"
            "s_waitcnt lgkmcnt(9)\n\t"
            "v_smfmac_f32_16x16x64_bf16 v[188:191], v[172:175], v[152:159], v255\n\t"
            "ds_read_b64 v[148:149], v253 offset:1816\n\t"
            "v_mfma_f32_4x4x4_16b_bf16 v[196:199], v[178:179], v[178:179], v[196:199]\n\t"
            "ds_read_b64 v[150:151], v253 offset:1912\n\t"
            "ds_read_b128 v[172:175], v254 offset:576\n\t"
            "ds_read_b64 v[152:153], v253 offset:80\n\t"
            "ds_read_b64 v[154:155], v253 offset:176\n\t"
            "ds_read_b64 v[156:157], v253 offset:1824\n\t"
            "s_waitcnt lgkmcnt(14)\n\t"
            "ds_read_b64 v[158:159], v253 offset:1920\n\t"
            "v_mov_b64 v[176:177], 0\n\t"
            "v_mov_b64 v[178:179], 0\n\t"
            "v_mov_b64 v[180:181], 0\n\t"
            "v_mov_b64 v[182:183], 0\n\t"
            "v_cvt_pk_bf16_f32 v184, v184, v185\n\t"
            "v_cvt_pk_bf16_f32 v185, v186, v187\n\t"
            "v_cvt_pk_bf16_f32 v186, v188, v189\n\t"
            "v_cvt_pk_bf16_f32 v187, v190, v191\n\t"
            "v_mfma_f32_4x4x4_16b_bf16 v[196:199], v[184:185], v[184:185], v[196:199]\n\t"
            "s_waitcnt lgkmcnt(14)\n\t"
            "ds_write_b128 v237, v[184:187] offset:128\n\t"
            "v_mfma_f32_16x16x32_bf16 v[192:195], v[238:241], v[184:187], v[192:195]\n\t"
            "v_mov_b64 v[184:185], 0\n\t"
            "v_mov_b64 v[188:189], 0\n\t"
            "s_waitcnt lgkmcnt(10)\n\t"
            "v_smfmac_f32_16x16x64_bf16 v[176:179], v[168:171], v[160:167], v255\n\t"
            "ds_read_b128 v[168:171], v254 offset:640\n\t"
            "v_mfma_f32_4x4x4_16b_bf16 v[196:199], v[186:187], v[186:187], v[196:199]\n\t"
            "ds_read_b64 v[160:161], v253 offset:88\n\t"
            "ds_read_b64 v[162:163], v253 offset:184\n\t"
            "ds_read_b64 v[164:165], v253 offset:1832\n\t"
            "s_waitcnt lgkmcnt(9)\n\t"
            "v_smfmac_f32_16x16x64_bf16 v[180:183], v[172:175], v[144:151], v255\n\t"
            "s_setprio 1\n\t"
            "ds_read_b64 v[166:167], v253 offset:1928\n\t"
            "ds_read_b128 v[172:175], v254 offset:704\n\t"
            "v_mov_b64 v[186:187], 0\n\t"
            "v_mov_b64 v[190:191], 0\n\t"
            "v_writelane_b32 v248, s58, 63\n\t"
            "v_writelane_b32 v248, s38, m0\n\t"
            "v_and_b32 v249, s69, v249\n\t"
            "v_cvt_pk_bf16_f32 v176, v176, v177\n\t"
            "v_cvt_pk_bf16_f32 v177, v178, v179\n\t"
            "v_cvt_pk_bf16_f32 v178, v180, v181\n\t"
            "v_cvt_pk_bf16_f32 v179, v182, v183\n\t"
            "s_setprio 3\n\t"
            "v_mfma_f32_4x4x4_16b_bf16 v[196:199], v[176:177], v[176:177], v[196:199]\n\t"
            "v_and_b32 v151, 0xffff0000, v227\n\t"
            "v_mfma_f32_16x16x32_bf16 v[192:195], v[238:241], v[176:179], v[192:195]\n\t"
            "v_lshlrev_b32 v150, 16, v227\n\t"
            "v_and_b32 v149, 0xffff0000, v226\n\t"
            "v_mfma_f32_4x4x4_16b_bf16 v[196:199], v[178:179], v[178:179], v[196:199]\n\t"
            "v_lshlrev_b32 v148, 16, v226\n\t"
            "v_and_b32 v147, 0xffff0000, v225\n\t"
            "v_lshlrev_b32 v146, 16, v225\n\t"
            "v_and_b32 v145, 0xffff0000, v224\n\t"
            "v_lshlrev_b32 v144, 16, v224\n\t"
            "s_waitcnt lgkmcnt(5)\n\t"
            "v_smfmac_f32_16x16x64_bf16 v[184:187], v[168:171], v[152:159], v255\n\t"
            "v_and_b32 v159, 0xffff0000, v231\n\t"
            "v_lshlrev_b32 v158, 16, v231\n\t"
            "v_and_b32 v157, 0xffff0000, v230\n\t"
            "v_lshlrev_b32 v156, 16, v230\n\t"
            "v_and_b32 v155, 0xffff0000, v229\n\t"
            "v_lshlrev_b32 v154, 16, v229\n\t"
            "s_waitcnt lgkmcnt(0)\n\t"
            "v_smfmac_f32_16x16x64_bf16 v[188:191], v[172:175], v[160:167], v255\n\t"
            "s_mov_b32 exec_hi, 0x3fffff\n\t"
            "s_add_u32 m0, s66, 0\n\t"
            "s_add_u32 s49, s79, 0\n\t"
            "buffer_load_dwordx4 v249, s[72:75], s49 offen lds\n\t"
            "s_add_u32 s49, s68, 195352\n\t"
            "buffer_load_dwordx4 v249, s[72:75], s49 offen offset:872 lds\n\t"
            "s_add_u32 s49, s68, 390704\n\t"
            "buffer_load_dwordx4 v249, s[72:75], s49 offen offset:1744 lds\n\t"
            "s_mov_b32 exec_hi, -1\n\t"
            "s_mov_b32 exec_hi, 0x3fffff\n\t"
            "s_add_u32 m0, s66, 2616\n\t"
            "s_add_u32 s49, s68, 588672\n\t"
            "buffer_load_dwordx4 v249, s[72:75], s49 offen lds\n\t"
            "s_add_u32 s49, s68, 784024\n\t"
            "buffer_load_dwordx4 v249, s[72:75], s49 offen offset:872 lds\n\t"
            "s_add_u32 s49, s68, 979376\n\t"
            "buffer_load_dwordx4 v249, s[72:75], s49 offen offset:1744 lds\n\t"
            "s_mov_b32 exec_hi, -1\n\t"
            "s_mov_b32 exec_hi, 0x3fffff\n\t"
            "s_add_u32 m0, s66, 5232\n\t"
            "s_add_u32 s49, s68, 1177344\n\t"
            "buffer_load_dwordx4 v249, s[72:75], s49 offen lds\n\t"
            "s_add_u32 s49, s68, 1372696\n\t"
            "buffer_load_dwordx4 v249, s[72:75], s49 offen offset:872 lds\n\t"
            "s_add_u32 s49, s68, 1568048\n\t"
            "buffer_load_dwordx4 v249, s[72:75], s49 offen offset:1744 lds\n\t"
            "s_mov_b32 exec_hi, -1\n\t"
            "v_cvt_pk_bf16_f32 v184, v184, v185\n\t"
            "v_cvt_pk_bf16_f32 v185, v186, v187\n\t"
            "v_cvt_pk_bf16_f32 v186, v188, v189\n\t"
            "v_cvt_pk_bf16_f32 v187, v190, v191\n\t"
            "v_mfma_f32_4x4x4_16b_bf16 v[196:199], v[184:185], v[184:185], v[196:199]\n\t"
            "ds_write_b128 v237, v[184:187] offset:256\n\t"
            "v_mfma_f32_16x16x32_bf16 v[192:195], v[238:241], v[184:187], v[192:195]\n\t"
            "v_and_b32 v153, 0xffff0000, v228\n\t"
            "v_lshlrev_b32 v152, 16, v228\n\t"
            "v_mfma_f32_4x4x4_16b_bf16 v[196:199], v[186:187], v[186:187], v[196:199]\n\t"
            "s_setprio 1\n\t"
            "v_and_b32 v167, 0xffff0000, v235\n\t"
            "v_lshlrev_b32 v166, 16, v235\n\t"
            "v_and_b32 v165, 0xffff0000, v234\n\t"
            "v_lshlrev_b32 v164, 16, v234\n\t"
            "v_and_b32 v163, 0xffff0000, v233\n\t"
            "v_lshlrev_b32 v162, 16, v233\n\t"
            "v_and_b32 v161, 0xffff0000, v232\n\t"
            "v_lshlrev_b32 v160, 16, v232\n\t"
            "v_and_b32 v227, 0xffff0000, v203\n\t"
            "v_cndmask_b32_e64 v193, v196, v197, s[60:61]\n\t"
            "v_cndmask_b32_e64 v193, v193, v198, s[62:63]\n\t"
            "v_cndmask_b32_e64 v193, v193, v199, s[64:65]\n\t"
            "v_lshlrev_b32 v226, 16, v203\n\t"
            "v_and_b32 v225, 0xffff0000, v202\n\t"
            "v_lshlrev_b32 v224, 16, v202\n\t"
            "v_and_b32 v231, 0xffff0000, v207\n\t"
            "v_lshlrev_b32 v230, 16, v207\n\t"
            "v_and_b32 v229, 0xffff0000, v206\n\t"
            "v_lshlrev_b32 v228, 16, v206\n\t"
            "v_and_b32 v235, 0xffff0000, v211\n\t"
            "v_lshlrev_b32 v234, 16, v211\n\t"
            "v_and_b32 v233, 0xffff0000, v210\n\t"
            "v_lshlrev_b32 v232, 16, v210\n\t"
            "v_and_b32 v203, 0xffff0000, v201\n\t"
            "v_lshlrev_b32 v202, 16, v201\n\t"
            "v_and_b32 v207, 0xffff0000, v205\n\t"
            "v_lshlrev_b32 v206, 16, v205\n\t"
            "v_and_b32 v211, 0xffff0000, v209\n\t"
            "v_lshlrev_b32 v210, 16, v209\n\t"
            "v_and_b32 v201, 0xffff0000, v200\n\t"
            "v_and_b32 v205, 0xffff0000, v204\n\t"
            "v_and_b32 v209, 0xffff0000, v208\n\t"
            "v_lshlrev_b32 v200, 16, v200\n\t"
            "v_lshlrev_b32 v204, 16, v204\n\t"
            "v_lshlrev_b32 v208, 16, v208\n\t"
            "v_and_b32 v171, 0xffff0000, v215\n\t"
            "v_lshlrev_b32 v170, 16, v215\n\t"
            "v_and_b32 v169, 0xffff0000, v214\n\t"
            "v_lshlrev_b32 v168, 16, v214\n\t"
            "v_and_b32 v183, 0xffff0000, v223\n\t"
            "v_lshlrev_b32 v182, 16, v223\n\t"
            "v_and_b32 v181, 0xffff0000, v222\n\t"
            "v_lshlrev_b32 v180, 16, v222\n\t"
            "v_and_b32 v215, 0xffff0000, v213\n\t"
            "v_lshlrev_b32 v214, 16, v213\n\t"
            "v_and_b32 v223, 0xffff0000, v221\n\t"
            "v_lshlrev_b32 v222, 16, v221\n\t"
            "v_and_b32 v213, 0xffff0000, v212\n\t"
            "v_and_b32 v221, 0xffff0000, v220\n\t"
            "v_lshlrev_b32 v212, 16, v212\n\t"
            "v_lshlrev_b32 v220, 16, v220\n\t"
            "v_add_u32 v237, s48, v245\n\t"
            "ds_read_b32 v247, v247 offset:7856\n\t"
            "ds_read_b128 v[196:199], v252\n\t"
            "ds_write_b64 v248, v[192:193] offset:18816\n\t"
            "ds_write_b128 v236, v[176:179] offset:256\n\t"
            "v_and_b32 v179, 0xffff0000, v219\n\t"
            "v_lshlrev_b32 v178, 16, v219\n\t"
            "v_and_b32 v177, 0xffff0000, v218\n\t"
            "v_lshlrev_b32 v176, 16, v218\n\t"
            "v_and_b32 v219, 0xffff0000, v217\n\t"
            "v_lshlrev_b32 v218, 16, v217\n\t"
            "v_and_b32 v217, 0xffff0000, v216\n\t"
            "v_lshlrev_b32 v216, 16, v216\n\t"
            "v_add_u32 v236, s48, v244\n\t"
            "v_add_u32 v248, s48, v246\n\t"
            "s_mov_b64 exec, 1\n\t"
            "ds_inc_u32 v255, v238 offset:7860\n\t"
            "s_mov_b64 exec, -1\n\t"
            "s_waitcnt lgkmcnt(4)\n\t"
            "v_readfirstlane_b32 s77, v247\n\t"
            "s_cmp_ge_u32 s77, s70\n\t"
            "s_cbranch_scc0 .Lwb_spin%=\n\t"
            ".Lwb_go%=:\n\t"
            "s_bfe_u32 s57, s56, 0x20004\n\t"
            "s_lshl_b32 s58, s57, 3\n\t"
            "s_lshr_b32 s58, s55, s58\n\t"
            "s_lshl_b64 s[36:37], s[34:35], s58\n\t"
            "ds_read_b128 v[184:187], v242\n\t"
            "ds_read_b128 v[188:191], v243\n\t"
            "ds_read_b128 v[192:195], v242 offset:128\n\t"
            "ds_read_b128 v[172:175], v243 offset:128\n\t"
            "s_bitcmp1_b32 s57, 1\n\t"
            "s_cselect_b32 s58, 1177344, 0\n\t"
            "s_bitcmp1_b32 s57, 0\n\t"
            "s_cselect_b32 s57, 2304, 0\n\t"
            "s_add_u32 s57, s57, s58\n\t"
            "s_waitcnt lgkmcnt(7)\n\t"
            "s_mov_b64 exec, s[36:37]\n\t"
            "v_add_u32 v196, s57, v196\n\t"
            "s_mov_b64 exec, -1\n\t"
            "s_add_u32 s59, s59, s84\n\t"
            "s_movk_i32 s84, 800\n\t"
            "s_waitcnt lgkmcnt(2)\n\t"
            "v_mfma_f32_16x16x32_bf16 v[200:203], v[0:3], v[184:187], v[200:203]\n\t"
            "v_mfma_f32_16x16x32_bf16 v[200:203], v[4:7], v[188:191], v[200:203]\n\t"
            "v_mfma_f32_16x16x32_bf16 v[224:227], v[28:31], v[188:191], v[224:227]\n\t"
            "v_mfma_f32_16x16x32_bf16 v[224:227], v[24:27], v[184:187], v[224:227]\n\t"
            "v_mfma_f32_16x16x32_bf16 v[204:207], v[48:51], v[184:187], v[204:207]\n\t"
            "v_mfma_f32_16x16x32_bf16 v[204:207], v[52:55], v[188:191], v[204:207]\n\t"
            "v_mfma_f32_16x16x32_bf16 v[228:231], v[76:79], v[188:191], v[228:231]\n\t"
            "v_mfma_f32_16x16x32_bf16 v[228:231], v[72:75], v[184:187], v[228:231]\n\t"
            "v_mfma_f32_16x16x32_bf16 v[208:211], v[96:99], v[184:187], v[208:211]\n\t"
            "v_mfma_f32_16x16x32_bf16 v[208:211], v[100:103], v[188:191], v[208:211]\n\t"
            "v_mfma_f32_16x16x32_bf16 v[232:235], v[124:127], v[188:191], v[232:235]\n\t"
            "ds_read_b128 v[188:191], v243 offset:256\n\t"
            "v_mfma_f32_16x16x32_bf16 v[232:235], v[120:123], v[184:187], v[232:235]\n\t"
            "ds_read_b128 v[184:187], v242 offset:256\n\t"
            "s_waitcnt lgkmcnt(2)\n\t"
            "v_mfma_f32_16x16x32_bf16 v[200:203], v[8:11], v[192:195], v[200:203]\n\t"
            "v_mfma_f32_16x16x32_bf16 v[200:203], v[12:15], v[172:175], v[200:203]\n\t"
            "v_mfma_f32_16x16x32_bf16 v[224:227], v[36:39], v[172:175], v[224:227]\n\t"
            "v_mfma_f32_16x16x32_bf16 v[224:227], v[32:35], v[192:195], v[224:227]\n\t"
            "v_mfma_f32_16x16x32_bf16 v[204:207], v[56:59], v[192:195], v[204:207]\n\t"
            "v_mfma_f32_16x16x32_bf16 v[204:207], v[60:63], v[172:175], v[204:207]\n\t"
            "v_mfma_f32_16x16x32_bf16 v[228:231], v[84:87], v[172:175], v[228:231]\n\t"
            "v_mfma_f32_16x16x32_bf16 v[228:231], v[80:83], v[192:195], v[228:231]\n\t"
            "v_mfma_f32_16x16x32_bf16 v[208:211], v[104:107], v[192:195], v[208:211]\n\t"
            "v_mfma_f32_16x16x32_bf16 v[208:211], v[108:111], v[172:175], v[208:211]\n\t"
            "v_mfma_f32_16x16x32_bf16 v[232:235], v[132:135], v[172:175], v[232:235]\n\t"
            "ds_read_b128 v[172:175], v243 offset:6144\n\t"
            "v_mfma_f32_16x16x32_bf16 v[232:235], v[128:131], v[192:195], v[232:235]\n\t"
            "ds_read_b128 v[192:195], v242 offset:6144\n\t"
            "s_waitcnt lgkmcnt(2)\n\t"
            "v_mfma_f32_16x16x32_bf16 v[200:203], v[16:19], v[184:187], v[200:203]\n\t"
            "v_mfma_f32_16x16x32_bf16 v[200:203], v[20:23], v[188:191], v[200:203]\n\t"
            "v_mfma_f32_16x16x32_bf16 v[224:227], v[44:47], v[188:191], v[224:227]\n\t"
            "v_mfma_f32_16x16x32_bf16 v[224:227], v[40:43], v[184:187], v[224:227]\n\t"
            "v_mfma_f32_16x16x32_bf16 v[204:207], v[64:67], v[184:187], v[204:207]\n\t"
            "v_mfma_f32_16x16x32_bf16 v[204:207], v[68:71], v[188:191], v[204:207]\n\t"
            "v_mfma_f32_16x16x32_bf16 v[228:231], v[92:95], v[188:191], v[228:231]\n\t"
            "v_mfma_f32_16x16x32_bf16 v[228:231], v[88:91], v[184:187], v[228:231]\n\t"
            "s_nop 1\n\t"
            "v_cvt_pk_bf16_f32 v200, v200, v201\n\t"
            "v_mfma_f32_16x16x32_bf16 v[208:211], v[112:115], v[184:187], v[208:211]\n\t"
            "v_cvt_pk_bf16_f32 v201, v202, v203\n\t"
            "v_mfma_f32_16x16x32_bf16 v[208:211], v[116:119], v[188:191], v[208:211]\n\t"
            "v_cvt_pk_bf16_f32 v202, v224, v225\n\t"
            "v_cvt_pk_bf16_f32 v203, v226, v227\n\t"
            "v_mfma_f32_16x16x32_bf16 v[232:235], v[140:143], v[188:191], v[232:235]\n\t"
            "ds_read_b128 v[188:191], v243 offset:6272\n\t"
            "ds_read_b128 v[224:227], v236 offset:12288\n\t"
            "v_mfma_f32_16x16x32_bf16 v[232:235], v[136:139], v[184:187], v[232:235]\n\t"
            "ds_read_b128 v[184:187], v242 offset:6272\n\t"
            "buffer_store_dwordx4 v[200:203], v196, s[40:43], s45 offen sc1\n\t"
            "s_waitcnt lgkmcnt(3)\n\t"
            "v_mfma_f32_16x16x32_bf16 v[212:215], v[0:3], v[192:195], v[212:215]\n\t"
            "ds_read_b128 v[200:203], v236\n\t"
            "v_cvt_pk_bf16_f32 v204, v204, v205\n\t"
            "v_mfma_f32_16x16x32_bf16 v[212:215], v[4:7], v[172:175], v[212:215]\n\t"
            "v_cvt_pk_bf16_f32 v205, v206, v207\n\t"
            "v_cvt_pk_bf16_f32 v206, v228, v229\n\t"
            "v_mfma_f32_16x16x32_bf16 v[168:171], v[28:31], v[172:175], v[168:171]\n\t"
            "v_cvt_pk_bf16_f32 v207, v230, v231\n\t"
            "v_cvt_pk_bf16_f32 v208, v208, v209\n\t"
            "v_mfma_f32_16x16x32_bf16 v[168:171], v[24:27], v[192:195], v[168:171]\n\t"
            "ds_read_b128 v[228:231], v237 offset:12288\n\t"
            "buffer_store_dwordx4 v[204:207], v196, s[40:43], s45 offen offset:64 sc1\n\t"
            "v_mfma_f32_16x16x32_bf16 v[216:219], v[48:51], v[192:195], v[216:219]\n\t"
            "s_nop 0\n\t"
            "ds_read_b128 v[204:207], v237\n\t"
            "v_cvt_pk_bf16_f32 v209, v210, v211\n\t"
            "v_mfma_f32_16x16x32_bf16 v[216:219], v[52:55], v[172:175], v[216:219]\n\t"
            "v_cvt_pk_bf16_f32 v210, v232, v233\n\t"
            "v_cvt_pk_bf16_f32 v211, v234, v235\n\t"
            "v_mfma_f32_16x16x32_bf16 v[176:179], v[76:79], v[172:175], v[176:179]\n\t"
            "ds_read_b128 v[232:235], v248 offset:12288\n\t"
            "v_mfma_f32_16x16x32_bf16 v[176:179], v[72:75], v[192:195], v[176:179]\n\t"
            "buffer_store_dwordx4 v[208:211], v196, s[40:43], s45 offen offset:128 sc1\n\t"
            "v_mfma_f32_16x16x32_bf16 v[220:223], v[96:99], v[192:195], v[220:223]\n\t"
            "s_nop 0\n\t"
            "ds_read_b128 v[208:211], v248\n\t"
            "v_mfma_f32_16x16x32_bf16 v[220:223], v[100:103], v[172:175], v[220:223]\n\t"
            "v_mfma_f32_16x16x32_bf16 v[180:183], v[124:127], v[172:175], v[180:183]\n\t"
            "ds_read_b128 v[172:175], v243 offset:6400\n\t"
            "v_mfma_f32_16x16x32_bf16 v[180:183], v[120:123], v[192:195], v[180:183]\n\t"
            "ds_read_b128 v[192:195], v242 offset:6400\n\t"
            "s_waitcnt lgkmcnt(7)\n\t"
            "v_mfma_f32_16x16x32_bf16 v[212:215], v[8:11], v[184:187], v[212:215]\n\t"
            "v_mfma_f32_16x16x32_bf16 v[212:215], v[12:15], v[188:191], v[212:215]\n\t"
            "v_mfma_f32_16x16x32_bf16 v[168:171], v[36:39], v[188:191], v[168:171]\n\t"
            "v_mfma_f32_16x16x32_bf16 v[168:171], v[32:35], v[184:187], v[168:171]\n\t"
            "v_mfma_f32_16x16x32_bf16 v[216:219], v[56:59], v[184:187], v[216:219]\n\t"
            "v_mfma_f32_16x16x32_bf16 v[216:219], v[60:63], v[188:191], v[216:219]\n\t"
            "v_mfma_f32_16x16x32_bf16 v[176:179], v[84:87], v[188:191], v[176:179]\n\t"
            "v_mfma_f32_16x16x32_bf16 v[176:179], v[80:83], v[184:187], v[176:179]\n\t"
            "v_mfma_f32_16x16x32_bf16 v[220:223], v[104:107], v[184:187], v[220:223]\n\t"
            "v_mfma_f32_16x16x32_bf16 v[220:223], v[108:111], v[188:191], v[220:223]\n\t"
            "v_mfma_f32_16x16x32_bf16 v[180:183], v[132:135], v[188:191], v[180:183]\n\t"
            "ds_read_b128 v[188:191], v243 offset:12288\n\t"
            "v_mfma_f32_16x16x32_bf16 v[180:183], v[128:131], v[184:187], v[180:183]\n\t"
            "ds_read_b128 v[184:187], v242 offset:12288\n\t"
            "s_waitcnt lgkmcnt(2)\n\t"
            "v_mfma_f32_16x16x32_bf16 v[168:171], v[44:47], v[172:175], v[168:171]\n\t"
            "v_mfma_f32_16x16x32_bf16 v[168:171], v[40:43], v[192:195], v[168:171]\n\t"
            "v_mfma_f32_16x16x32_bf16 v[176:179], v[92:95], v[172:175], v[176:179]\n\t"
            "v_mfma_f32_16x16x32_bf16 v[176:179], v[88:91], v[192:195], v[176:179]\n\t"
            "v_mfma_f32_16x16x32_bf16 v[180:183], v[140:143], v[172:175], v[180:183]\n\t"
            "v_mfma_f32_16x16x32_bf16 v[180:183], v[136:139], v[192:195], v[180:183]\n\t"
            "v_mfma_f32_16x16x32_bf16 v[212:215], v[16:19], v[192:195], v[212:215]\n\t"
            "v_mfma_f32_16x16x32_bf16 v[212:215], v[20:23], v[172:175], v[212:215]\n\t"
            "s_nop 1\n\t"
            "v_cvt_pk_bf16_f32 v171, v170, v171\n\t"
            "v_mfma_f32_16x16x32_bf16 v[216:219], v[64:67], v[192:195], v[216:219]\n\t"
            "v_cvt_pk_bf16_f32 v170, v168, v169\n\t"
            "v_mfma_f32_16x16x32_bf16 v[216:219], v[68:71], v[172:175], v[216:219]\n\t"
            "v_cvt_pk_bf16_f32 v179, v178, v179\n\t"
            "v_cvt_pk_bf16_f32 v178, v176, v177\n\t"
            "v_mfma_f32_16x16x32_bf16 v[220:223], v[112:115], v[192:195], v[220:223]\n\t"
            "ds_read_b128 v[192:195], v242 offset:12416\n\t"
            "v_cvt_pk_bf16_f32 v183, v182, v183\n\t"
            "v_mfma_f32_16x16x32_bf16 v[220:223], v[116:119], v[172:175], v[220:223]\n\t"
            "ds_read_b128 v[172:175], v243 offset:12416\n\t"
            "v_cvt_pk_bf16_f32 v182, v180, v181\n\t"
            "s_waitcnt lgkmcnt(2)\n\t"
            "v_mfma_f32_16x16x32_bf16 v[144:147], v[0:3], v[184:187], v[144:147]\n\t"
            "v_cvt_pk_bf16_f32 v168, v212, v213\n\t"
            "v_cvt_pk_bf16_f32 v169, v214, v215\n\t"
            "v_mfma_f32_16x16x32_bf16 v[144:147], v[4:7], v[188:191], v[144:147]\n\t"
            "ds_read_b128 v[212:215], v236 offset:6144\n\t"
            "v_cvt_pk_bf16_f32 v176, v216, v217\n\t"
            "v_mfma_f32_16x16x32_bf16 v[148:151], v[28:31], v[188:191], v[148:151]\n\t"
            "v_cvt_pk_bf16_f32 v177, v218, v219\n\t"
            "v_cvt_pk_bf16_f32 v180, v220, v221\n\t"
            "v_mfma_f32_16x16x32_bf16 v[148:151], v[24:27], v[184:187], v[148:151]\n\t"
            "ds_read_b128 v[216:219], v237 offset:6144\n\t"
            "v_cvt_pk_bf16_f32 v181, v222, v223\n\t"
            "v_mfma_f32_16x16x32_bf16 v[152:155], v[48:51], v[184:187], v[152:155]\n\t"
            "ds_read_b128 v[220:223], v248 offset:6144\n\t"
            "v_mfma_f32_16x16x32_bf16 v[152:155], v[52:55], v[188:191], v[152:155]\n\t"
            "buffer_store_dwordx4 v[168:171], v197, s[40:43], s45 offen sc1\n\t"
            "v_mfma_f32_16x16x32_bf16 v[156:159], v[76:79], v[188:191], v[156:159]\n\t"
            "v_mfma_f32_16x16x32_bf16 v[156:159], v[72:75], v[184:187], v[156:159]\n\t"
            "buffer_store_dwordx4 v[176:179], v197, s[40:43], s45 offen offset:64 sc1\n\t"
            "v_mfma_f32_16x16x32_bf16 v[160:163], v[96:99], v[184:187], v[160:163]\n\t"
            "v_mfma_f32_16x16x32_bf16 v[160:163], v[100:103], v[188:191], v[160:163]\n\t"
            "buffer_store_dwordx4 v[180:183], v197, s[40:43], s45 offen offset:128 sc1\n\t"
            "v_mfma_f32_16x16x32_bf16 v[164:167], v[124:127], v[188:191], v[164:167]\n\t"
            "ds_read_b128 v[188:191], v243 offset:12544\n\t"
            "v_mfma_f32_16x16x32_bf16 v[164:167], v[120:123], v[184:187], v[164:167]\n\t"
            "ds_read_b128 v[184:187], v242 offset:12544\n\t"
            "s_waitcnt lgkmcnt(5)\n\t"
            "v_mfma_f32_16x16x32_bf16 v[144:147], v[8:11], v[192:195], v[144:147]\n\t"
            "v_mfma_f32_16x16x32_bf16 v[144:147], v[12:15], v[172:175], v[144:147]\n\t"
            "v_mfma_f32_16x16x32_bf16 v[148:151], v[36:39], v[172:175], v[148:151]\n\t"
            "v_mfma_f32_16x16x32_bf16 v[148:151], v[32:35], v[192:195], v[148:151]\n\t"
            "s_waitcnt lgkmcnt(0)\n\t"
            "s_barrier\n\t"
            "s_setprio 3\n\t"
            "v_mfma_f32_16x16x32_bf16 v[152:155], v[56:59], v[192:195], v[152:155]\n\t"
            "v_mfma_f32_16x16x32_bf16 v[152:155], v[60:63], v[172:175], v[152:155]\n\t"
            "v_mfma_f32_16x16x32_bf16 v[156:159], v[84:87], v[172:175], v[156:159]\n\t"
            "v_mfma_f32_16x16x32_bf16 v[156:159], v[80:83], v[192:195], v[156:159]\n\t"
            "v_mfma_f32_16x16x32_bf16 v[144:147], v[16:19], v[184:187], v[144:147]\n\t"
            "v_mfma_f32_16x16x32_bf16 v[144:147], v[20:23], v[188:191], v[144:147]\n\t"
            "v_mfma_f32_16x16x32_bf16 v[148:151], v[44:47], v[188:191], v[148:151]\n\t"
            "v_mfma_f32_16x16x32_bf16 v[148:151], v[40:43], v[184:187], v[148:151]\n\t"
            "v_mfma_f32_16x16x32_bf16 v[160:163], v[104:107], v[192:195], v[160:163]\n\t"
            "v_mfma_f32_16x16x32_bf16 v[160:163], v[108:111], v[172:175], v[160:163]\n\t"
            "v_mfma_f32_16x16x32_bf16 v[164:167], v[132:135], v[172:175], v[164:167]\n\t"
            "v_mfma_f32_16x16x32_bf16 v[164:167], v[128:131], v[192:195], v[164:167]\n\t"
            "s_nop 1\n\t"
            "v_cvt_pk_bf16_f32 v144, v144, v145\n\t"
            "v_mfma_f32_16x16x32_bf16 v[152:155], v[64:67], v[184:187], v[152:155]\n\t"
            "v_cvt_pk_bf16_f32 v145, v146, v147\n\t"
            "v_mfma_f32_16x16x32_bf16 v[152:155], v[68:71], v[188:191], v[152:155]\n\t"
            "v_cvt_pk_bf16_f32 v146, v148, v149\n\t"
            "v_cvt_pk_bf16_f32 v147, v150, v151\n\t"
            "v_mfma_f32_16x16x32_bf16 v[156:159], v[92:95], v[188:191], v[156:159]\n\t"
            "v_mfma_f32_16x16x32_bf16 v[156:159], v[88:91], v[184:187], v[156:159]\n\t"
            "buffer_store_dwordx4 v[144:147], v198, s[40:43], s45 offen sc1\n\t"
            "v_mfma_f32_16x16x32_bf16 v[160:163], v[112:115], v[184:187], v[160:163]\n\t"
            "s_nop 1\n\t"
            "v_cvt_pk_bf16_f32 v152, v152, v153\n\t"
            "v_mfma_f32_16x16x32_bf16 v[160:163], v[116:119], v[188:191], v[160:163]\n\t"
            "v_cvt_pk_bf16_f32 v153, v154, v155\n\t"
            "v_mfma_f32_16x16x32_bf16 v[164:167], v[140:143], v[188:191], v[164:167]\n\t"
            "v_cvt_pk_bf16_f32 v154, v156, v157\n\t"
            "v_mfma_f32_16x16x32_bf16 v[164:167], v[136:139], v[184:187], v[164:167]\n\t"
            "v_cvt_pk_bf16_f32 v155, v158, v159\n\t"
            "buffer_store_dwordx4 v[152:155], v198, s[40:43], s45 offen offset:64 sc1\n\t"
            "s_nop 1\n\t"
            "v_cvt_pk_bf16_f32 v160, v160, v161\n\t"
            "v_cvt_pk_bf16_f32 v161, v162, v163\n\t"
            "s_nop 1\n\t"
            "v_cvt_pk_bf16_f32 v162, v164, v165\n\t"
            "v_cvt_pk_bf16_f32 v163, v166, v167\n\t"
            "buffer_store_dwordx4 v[160:163], v198, s[40:43], s45 offen offset:128 sc1\n\t"
            "s_add_u32 s45, s46, s27\n\t"
            "s_mov_b32 s46, s47\n\t"
            "s_mov_b32 s47, s68\n\t"
            "s_xor_b32 s48, s48, s71\n\t"
            "s_xor_b32 s67, s67, s71\n\t"
            "s_add_u32 s76, s76, 4\n\t"
            "s_add_u32 s70, s70, 4\n\t"
            "s_lshl_b32 s56, s56, 2\n\t"
            "s_lshr_b32 s49, s26, 30\n\t"
            "s_or_b32 s56, s56, s49\n\t"
            "s_cmp_lt_u32 s46, 0x7000000\n\t"
            "s_cbranch_scc1 .Lwb_loop%=\n\t"
            "s_branch .Lwb_end%=\n\t"
            ".Lwb_spin%=:\n\t"
            "s_cmp_eq_u32 s83, 0\n\t"
            "s_cbranch_scc1 .Lwb_go%=\n\t"
            "s_sub_u32 s83, s83, 1\n\t"
            "s_sleep 2\n\t"
            "v_mov_b32 v247, 0\n\t"
            "ds_read_b32 v247, v247 offset:7856\n\t"
            "s_waitcnt lgkmcnt(0)\n\t"
            "v_readfirstlane_b32 s77, v247\n\t"
            "s_cmp_ge_u32 s77, s70\n\t"
            "s_cbranch_scc0 .Lwb_spin%=\n\t"
            "s_branch .Lwb_go%=\n\t"
            ".Lwb_end%=:\n\t"
            "s_waitcnt lgkmcnt(0)\n\t"
            "s_nop 7\n\t"
            "ds_read_b128 v[196:199], v252\n\t"
            "s_bfe_u32 s57, s56, 0x20004\n\t"
            "s_lshl_b32 s58, s57, 3\n\t"
            "s_lshr_b32 s58, s55, s58\n\t"
            "s_lshl_b64 s[36:37], s[34:35], s58\n\t"
            "s_bitcmp1_b32 s57, 1\n\t"
            "s_cselect_b32 s58, 1177344, 0\n\t"
            "s_bitcmp1_b32 s57, 0\n\t"
            "s_cselect_b32 s57, 2304, 0\n\t"
            "s_add_u32 s57, s57, s58\n\t"
            "v_and_b32 v151, 0xffff0000, v227\n\t"
            "v_lshlrev_b32 v150, 16, v227\n\t"
            "v_and_b32 v149, 0xffff0000, v226\n\t"
            "v_lshlrev_b32 v148, 16, v226\n\t"
            "v_and_b32 v147, 0xffff0000, v225\n\t"
            "v_lshlrev_b32 v146, 16, v225\n\t"
            "v_and_b32 v145, 0xffff0000, v224\n\t"
            "v_lshlrev_b32 v144, 16, v224\n\t"
            "v_and_b32 v159, 0xffff0000, v231\n\t"
            "v_lshlrev_b32 v158, 16, v231\n\t"
            "v_and_b32 v157, 0xffff0000, v230\n\t"
            "v_lshlrev_b32 v156, 16, v230\n\t"
            "v_and_b32 v155, 0xffff0000, v229\n\t"
            "v_lshlrev_b32 v154, 16, v229\n\t"
            "v_and_b32 v153, 0xffff0000, v228\n\t"
            "v_lshlrev_b32 v152, 16, v228\n\t"
            "v_and_b32 v167, 0xffff0000, v235\n\t"
            "v_lshlrev_b32 v166, 16, v235\n\t"
            "v_and_b32 v165, 0xffff0000, v234\n\t"
            "v_lshlrev_b32 v164, 16, v234\n\t"
            "v_and_b32 v163, 0xffff0000, v233\n\t"
            "s_waitcnt lgkmcnt(0)\n\t"
            "s_mov_b64 exec, s[36:37]\n\t"
            "v_add_u32 v196, s57, v196\n\t"
            "s_mov_b64 exec, -1\n\t"
            "v_lshlrev_b32 v162, 16, v233\n\t"
            "v_and_b32 v161, 0xffff0000, v232\n\t"
            "v_lshlrev_b32 v160, 16, v232\n\t"
            ".Lwk_t1%=:\n\t"
            "v_mov_b32 v247, 0\n\t"
            "ds_read_b32 v247, v247 offset:7856\n\t"
            "s_waitcnt lgkmcnt(0)\n\t"
            "v_readfirstlane_b32 s77, v247\n\t"
            "s_cmp_ge_u32 s77, s70\n\t"
            "s_cbranch_scc1 .Lwk_t2%=\n\t"
            "s_cmp_eq_u32 s83, 0\n\t"
            "s_cbranch_scc1 .Lwk_t2%=\n\t"
            "s_sub_u32 s83, s83, 1\n\t"
            "s_sleep 1\n\t"
            "s_branch .Lwk_t1%=\n\t"
            ".Lwk_t2%=:\n\t"
            "v_and_b32 v227, 0xffff0000, v203\n\t"
            "v_lshlrev_b32 v226, 16, v203\n\t"
            "v_and_b32 v225, 0xffff0000, v202\n\t"
            "v_lshlrev_b32 v224, 16, v202\n\t"
            "v_and_b32 v231, 0xffff0000, v207\n\t"
            "v_lshlrev_b32 v230, 16, v207\n\t"
            "v_and_b32 v229, 0xffff0000, v206\n\t"
            "v_lshlrev_b32 v228, 16, v206\n\t"
            "v_and_b32 v235, 0xffff0000, v211\n\t"
            "v_lshlrev_b32 v234, 16, v211\n\t"
            "v_and_b32 v233, 0xffff0000, v210\n\t"
            "v_lshlrev_b32 v232, 16, v210\n\t"
            "v_and_b32 v203, 0xffff0000, v201\n\t"
            "v_lshlrev_b32 v202, 16, v201\n\t"
            "v_and_b32 v207, 0xffff0000, v205\n\t"
            "v_lshlrev_b32 v206, 16, v205\n\t"
            "v_and_b32 v211, 0xffff0000, v209\n\t"
            "v_lshlrev_b32 v210, 16, v209\n\t"
            "v_and_b32 v201, 0xffff0000, v200\n\t"
            "v_and_b32 v205, 0xffff0000, v204\n\t"
            "v_and_b32 v209, 0xffff0000, v208\n\t"
            "v_lshlrev_b32 v200, 16, v200\n\t"
            "v_lshlrev_b32 v204, 16, v204\n\t"
            "v_lshlrev_b32 v208, 16, v208\n\t"
            "v_and_b32 v171, 0xffff0000, v215\n\t"
            "v_lshlrev_b32 v170, 16, v215\n\t"
            "v_and_b32 v169, 0xffff0000, v214\n\t"
            "v_lshlrev_b32 v168, 16, v214\n\t"
            "v_and_b32 v179, 0xffff0000, v219\n\t"
            "v_lshlrev_b32 v178, 16, v219\n\t"
            "v_and_b32 v177, 0xffff0000, v218\n\t"
            "v_lshlrev_b32 v176, 16, v218\n\t"
            "v_and_b32 v183, 0xffff0000, v223\n\t"
            "v_lshlrev_b32 v182, 16, v223\n\t"
            "v_and_b32 v181, 0xffff0000, v222\n\t"
            "v_lshlrev_b32 v180, 16, v222\n\t"
            "v_and_b32 v215, 0xffff0000, v213\n\t"
            "v_lshlrev_b32 v214, 16, v213\n\t"
            "v_and_b32 v219, 0xffff0000, v217\n\t"
            "v_lshlrev_b32 v218, 16, v217\n\t"
            "v_and_b32 v223, 0xffff0000, v221\n\t"
            "v_lshlrev_b32 v222, 16, v221\n\t"
            "v_and_b32 v213, 0xffff0000, v212\n\t"
            "v_and_b32 v217, 0xffff0000, v216\n\t"
            "v_and_b32 v221, 0xffff0000, v220\n\t"
            "v_lshlrev_b32 v212, 16, v212\n\t"
            "v_lshlrev_b32 v216, 16, v216\n\t"
            "v_lshlrev_b32 v220, 16, v220\n\t"
            "ds_read_b128 v[184:187], v242\n\t"
            "ds_read_b128 v[188:191], v243\n\t"
            "ds_read_b128 v[192:195], v242 offset:128\n\t"
            "ds_read_b128 v[172:175], v243 offset:128\n\t"
            "s_waitcnt lgkmcnt(2)\n\t"
            "v_mfma_f32_16x16x32_bf16 v[200:203], v[0:3], v[184:187], v[200:203]\n\t"
            "v_mfma_f32_16x16x32_bf16 v[200:203], v[4:7], v[188:191], v[200:203]\n\t"
            "v_mfma_f32_16x16x32_bf16 v[224:227], v[28:31], v[188:191], v[224:227]\n\t"
            "v_mfma_f32_16x16x32_bf16 v[224:227], v[24:27], v[184:187], v[224:227]\n\t"
            "v_mfma_f32_16x16x32_bf16 v[204:207], v[48:51], v[184:187], v[204:207]\n\t"
            "v_mfma_f32_16x16x32_bf16 v[204:207], v[52:55], v[188:191], v[204:207]\n\t"
            "v_mfma_f32_16x16x32_bf16 v[228:231], v[76:79], v[188:191], v[228:231]\n\t"
            "v_mfma_f32_16x16x32_bf16 v[228:231], v[72:75], v[184:187], v[228:231]\n\t"
            "v_mfma_f32_16x16x32_bf16 v[208:211], v[96:99], v[184:187], v[208:211]\n\t"
            "v_mfma_f32_16x16x32_bf16 v[208:211], v[100:103], v[188:191], v[208:211]\n\t"
            "v_mfma_f32_16x16x32_bf16 v[232:235], v[124:127], v[188:191], v[232:235]\n\t"
            "ds_read_b128 v[188:191], v243 offset:256\n\t"
            "v_mfma_f32_16x16x32_bf16 v[232:235], v[120:123], v[184:187], v[232:235]\n\t"
            "ds_read_b128 v[184:187], v242 offset:256\n\t"
            "s_waitcnt lgkmcnt(2)\n\t"
            "v_mfma_f32_16x16x32_bf16 v[200:203], v[8:11], v[192:195], v[200:203]\n\t"
            "v_mfma_f32_16x16x32_bf16 v[200:203], v[12:15], v[172:175], v[200:203]\n\t"
            "v_mfma_f32_16x16x32_bf16 v[224:227], v[36:39], v[172:175], v[224:227]\n\t"
            "v_mfma_f32_16x16x32_bf16 v[224:227], v[32:35], v[192:195], v[224:227]\n\t"
            "v_mfma_f32_16x16x32_bf16 v[204:207], v[56:59], v[192:195], v[204:207]\n\t"
            "v_mfma_f32_16x16x32_bf16 v[204:207], v[60:63], v[172:175], v[204:207]\n\t"
            "v_mfma_f32_16x16x32_bf16 v[228:231], v[84:87], v[172:175], v[228:231]\n\t"
            "v_mfma_f32_16x16x32_bf16 v[228:231], v[80:83], v[192:195], v[228:231]\n\t"
            "v_mfma_f32_16x16x32_bf16 v[208:211], v[104:107], v[192:195], v[208:211]\n\t"
            "v_mfma_f32_16x16x32_bf16 v[208:211], v[108:111], v[172:175], v[208:211]\n\t"
            "v_mfma_f32_16x16x32_bf16 v[232:235], v[132:135], v[172:175], v[232:235]\n\t"
            "ds_read_b128 v[172:175], v243 offset:6144\n\t"
            "v_mfma_f32_16x16x32_bf16 v[232:235], v[128:131], v[192:195], v[232:235]\n\t"
            "ds_read_b128 v[192:195], v242 offset:6144\n\t"
            "s_waitcnt lgkmcnt(2)\n\t"
            "v_mfma_f32_16x16x32_bf16 v[200:203], v[16:19], v[184:187], v[200:203]\n\t"
            "v_mfma_f32_16x16x32_bf16 v[200:203], v[20:23], v[188:191], v[200:203]\n\t"
            "v_mfma_f32_16x16x32_bf16 v[224:227], v[44:47], v[188:191], v[224:227]\n\t"
            "v_mfma_f32_16x16x32_bf16 v[224:227], v[40:43], v[184:187], v[224:227]\n\t"
            "v_mfma_f32_16x16x32_bf16 v[204:207], v[64:67], v[184:187], v[204:207]\n\t"
            "v_mfma_f32_16x16x32_bf16 v[204:207], v[68:71], v[188:191], v[204:207]\n\t"
            "v_mfma_f32_16x16x32_bf16 v[228:231], v[92:95], v[188:191], v[228:231]\n\t"
            "v_mfma_f32_16x16x32_bf16 v[228:231], v[88:91], v[184:187], v[228:231]\n\t"
            "s_nop 1\n\t"
            "v_cvt_pk_bf16_f32 v200, v200, v201\n\t"
            "v_mfma_f32_16x16x32_bf16 v[208:211], v[112:115], v[184:187], v[208:211]\n\t"
            "v_cvt_pk_bf16_f32 v201, v202, v203\n\t"
            "v_mfma_f32_16x16x32_bf16 v[208:211], v[116:119], v[188:191], v[208:211]\n\t"
            "v_cvt_pk_bf16_f32 v202, v224, v225\n\t"
            "v_cvt_pk_bf16_f32 v203, v226, v227\n\t"
            "v_mfma_f32_16x16x32_bf16 v[232:235], v[140:143], v[188:191], v[232:235]\n\t"
            "ds_read_b128 v[188:191], v243 offset:6272\n\t"
            "v_cvt_pk_bf16_f32 v204, v204, v205\n\t"
            "v_mfma_f32_16x16x32_bf16 v[232:235], v[136:139], v[184:187], v[232:235]\n\t"
            "ds_read_b128 v[184:187], v242 offset:6272\n\t"
            "buffer_store_dwordx4 v[200:203], v196, s[40:43], s45 offen sc1\n\t"
            "s_waitcnt lgkmcnt(2)\n\t"
            "v_mfma_f32_16x16x32_bf16 v[212:215], v[0:3], v[192:195], v[212:215]\n\t"
            "v_cvt_pk_bf16_f32 v205, v206, v207\n\t"
            "v_cvt_pk_bf16_f32 v206, v228, v229\n\t"
            "v_mfma_f32_16x16x32_bf16 v[212:215], v[4:7], v[172:175], v[212:215]\n\t"
            "v_cvt_pk_bf16_f32 v207, v230, v231\n\t"
            "v_cvt_pk_bf16_f32 v208, v208, v209\n\t"
            "v_mfma_f32_16x16x32_bf16 v[168:171], v[28:31], v[172:175], v[168:171]\n\t"
            "v_cvt_pk_bf16_f32 v209, v210, v211\n\t"
            "v_cvt_pk_bf16_f32 v210, v232, v233\n\t"
            "v_mfma_f32_16x16x32_bf16 v[168:171], v[24:27], v[192:195], v[168:171]\n\t"
            "buffer_store_dwordx4 v[204:207], v196, s[40:43], s45 offen offset:64 sc1\n\t"
            "v_mfma_f32_16x16x32_bf16 v[216:219], v[48:51], v[192:195], v[216:219]\n\t"
            "v_cvt_pk_bf16_f32 v211, v234, v235\n\t"
            "v_mfma_f32_16x16x32_bf16 v[216:219], v[52:55], v[172:175], v[216:219]\n\t"
            "buffer_store_dwordx4 v[208:211], v196, s[40:43], s45 offen offset:128 sc1\n\t"
            "v_mfma_f32_16x16x32_bf16 v[176:179], v[76:79], v[172:175], v[176:179]\n\t"
            "v_mfma_f32_16x16x32_bf16 v[176:179], v[72:75], v[192:195], v[176:179]\n\t"
            "v_mfma_f32_16x16x32_bf16 v[220:223], v[96:99], v[192:195], v[220:223]\n\t"
            "v_mfma_f32_16x16x32_bf16 v[220:223], v[100:103], v[172:175], v[220:223]\n\t"
            "v_mfma_f32_16x16x32_bf16 v[180:183], v[124:127], v[172:175], v[180:183]\n\t"
            "ds_read_b128 v[172:175], v243 offset:6400\n\t"
            "v_mfma_f32_16x16x32_bf16 v[180:183], v[120:123], v[192:195], v[180:183]\n\t"
            "ds_read_b128 v[192:195], v242 offset:6400\n\t"
            "s_waitcnt lgkmcnt(2)\n\t"
            "v_mfma_f32_16x16x32_bf16 v[212:215], v[8:11], v[184:187], v[212:215]\n\t"
            "v_mfma_f32_16x16x32_bf16 v[212:215], v[12:15], v[188:191], v[212:215]\n\t"
            "v_mfma_f32_16x16x32_bf16 v[168:171], v[36:39], v[188:191], v[168:171]\n\t"
            "v_mfma_f32_16x16x32_bf16 v[168:171], v[32:35], v[184:187], v[168:171]\n\t"
            "v_mfma_f32_16x16x32_bf16 v[216:219], v[56:59], v[184:187], v[216:219]\n\t"
            "v_mfma_f32_16x16x32_bf16 v[216:219], v[60:63], v[188:191], v[216:219]\n\t"
            "v_mfma_f32_16x16x32_bf16 v[176:179], v[84:87], v[188:191], v[176:179]\n\t"
            "v_mfma_f32_16x16x32_bf16 v[176:179], v[80:83], v[184:187], v[176:179]\n\t"
            "v_mfma_f32_16x16x32_bf16 v[220:223], v[104:107], v[184:187], v[220:223]\n\t"
            "v_mfma_f32_16x16x32_bf16 v[220:223], v[108:111], v[188:191], v[220:223]\n\t"
            "v_mfma_f32_16x16x32_bf16 v[180:183], v[132:135], v[188:191], v[180:183]\n\t"
            "ds_read_b128 v[188:191], v243 offset:12288\n\t"
            "v_mfma_f32_16x16x32_bf16 v[180:183], v[128:131], v[184:187], v[180:183]\n\t"
            "ds_read_b128 v[184:187], v242 offset:12288\n\t"
            "s_waitcnt lgkmcnt(2)\n\t"
            "v_mfma_f32_16x16x32_bf16 v[212:215], v[16:19], v[192:195], v[212:215]\n\t"
            "v_mfma_f32_16x16x32_bf16 v[212:215], v[20:23], v[172:175], v[212:215]\n\t"
            "v_mfma_f32_16x16x32_bf16 v[168:171], v[44:47], v[172:175], v[168:171]\n\t"
            "v_mfma_f32_16x16x32_bf16 v[168:171], v[40:43], v[192:195], v[168:171]\n\t"
            "v_mfma_f32_16x16x32_bf16 v[216:219], v[64:67], v[192:195], v[216:219]\n\t"
            "v_mfma_f32_16x16x32_bf16 v[216:219], v[68:71], v[172:175], v[216:219]\n\t"
            "v_mfma_f32_16x16x32_bf16 v[176:179], v[92:95], v[172:175], v[176:179]\n\t"
            "v_mfma_f32_16x16x32_bf16 v[176:179], v[88:91], v[192:195], v[176:179]\n\t"
            "v_mfma_f32_16x16x32_bf16 v[220:223], v[112:115], v[192:195], v[220:223]\n\t"
            "v_mfma_f32_16x16x32_bf16 v[220:223], v[116:119], v[172:175], v[220:223]\n\t"
            "s_nop 1\n\t"
            "v_cvt_pk_bf16_f32 v171, v170, v171\n\t"
            "v_mfma_f32_16x16x32_bf16 v[180:183], v[140:143], v[172:175], v[180:183]\n\t"
            "ds_read_b128 v[172:175], v243 offset:12416\n\t"
            "v_cvt_pk_bf16_f32 v170, v168, v169\n\t"
            "v_mfma_f32_16x16x32_bf16 v[180:183], v[136:139], v[192:195], v[180:183]\n\t"
            "ds_read_b128 v[192:195], v242 offset:12416\n\t"
            "v_cvt_pk_bf16_f32 v168, v212, v213\n\t"
            "s_waitcnt lgkmcnt(2)\n\t"
            "v_mfma_f32_16x16x32_bf16 v[144:147], v[0:3], v[184:187], v[144:147]\n\t"
            "v_cvt_pk_bf16_f32 v169, v214, v215\n\t"
            "v_cvt_pk_bf16_f32 v179, v178, v179\n\t"
            "v_mfma_f32_16x16x32_bf16 v[144:147], v[4:7], v[188:191], v[144:147]\n\t"
            "buffer_store_dwordx4 v[168:171], v197, s[40:43], s45 offen sc1\n\t"
            "v_mfma_f32_16x16x32_bf16 v[148:151], v[28:31], v[188:191], v[148:151]\n\t"
            "v_cvt_pk_bf16_f32 v178, v176, v177\n\t"
            "v_cvt_pk_bf16_f32 v176, v216, v217\n\t"
            "v_mfma_f32_16x16x32_bf16 v[148:151], v[24:27], v[184:187], v[148:151]\n\t"
            "v_cvt_pk_bf16_f32 v177, v218, v219\n\t"
            "v_cvt_pk_bf16_f32 v183, v182, v183\n\t"
            "v_mfma_f32_16x16x32_bf16 v[152:155], v[48:51], v[184:187], v[152:155]\n\t"
            "v_cvt_pk_bf16_f32 v182, v180, v181\n\t"
            "v_cvt_pk_bf16_f32 v180, v220, v221\n\t"
            "v_mfma_f32_16x16x32_bf16 v[152:155], v[52:55], v[188:191], v[152:155]\n\t"
            "buffer_store_dwordx4 v[176:179], v197, s[40:43], s45 offen offset:64 sc1\n\t"
            "v_mfma_f32_16x16x32_bf16 v[156:159], v[76:79], v[188:191], v[156:159]\n\t"
            "v_cvt_pk_bf16_f32 v181, v222, v223\n\t"
            "v_mfma_f32_16x16x32_bf16 v[156:159], v[72:75], v[184:187], v[156:159]\n\t"
            "buffer_store_dwordx4 v[180:183], v197, s[40:43], s45 offen offset:128 sc1\n\t"
            "v_mfma_f32_16x16x32_bf16 v[160:163], v[96:99], v[184:187], v[160:163]\n\t"
            "v_mfma_f32_16x16x32_bf16 v[160:163], v[100:103], v[188:191], v[160:163]\n\t"
            "v_mfma_f32_16x16x32_bf16 v[164:167], v[124:127], v[188:191], v[164:167]\n\t"
            "ds_read_b128 v[188:191], v243 offset:12544\n\t"
            "v_mfma_f32_16x16x32_bf16 v[164:167], v[120:123], v[184:187], v[164:167]\n\t"
            "ds_read_b128 v[184:187], v242 offset:12544\n\t"
            "s_waitcnt lgkmcnt(2)\n\t"
            "v_mfma_f32_16x16x32_bf16 v[144:147], v[8:11], v[192:195], v[144:147]\n\t"
            "v_mfma_f32_16x16x32_bf16 v[144:147], v[12:15], v[172:175], v[144:147]\n\t"
            "v_mfma_f32_16x16x32_bf16 v[148:151], v[36:39], v[172:175], v[148:151]\n\t"
            "v_mfma_f32_16x16x32_bf16 v[148:151], v[32:35], v[192:195], v[148:151]\n\t"
            "v_mfma_f32_16x16x32_bf16 v[152:155], v[56:59], v[192:195], v[152:155]\n\t"
            "v_mfma_f32_16x16x32_bf16 v[152:155], v[60:63], v[172:175], v[152:155]\n\t"
            "v_mfma_f32_16x16x32_bf16 v[156:159], v[84:87], v[172:175], v[156:159]\n\t"
            "v_mfma_f32_16x16x32_bf16 v[156:159], v[80:83], v[192:195], v[156:159]\n\t"
            "s_waitcnt lgkmcnt(0)\n\t"
            "v_mfma_f32_16x16x32_bf16 v[144:147], v[16:19], v[184:187], v[144:147]\n\t"
            "v_mfma_f32_16x16x32_bf16 v[144:147], v[20:23], v[188:191], v[144:147]\n\t"
            "v_mfma_f32_16x16x32_bf16 v[148:151], v[44:47], v[188:191], v[148:151]\n\t"
            "v_mfma_f32_16x16x32_bf16 v[148:151], v[40:43], v[184:187], v[148:151]\n\t"
            "v_mfma_f32_16x16x32_bf16 v[160:163], v[104:107], v[192:195], v[160:163]\n\t"
            "v_mfma_f32_16x16x32_bf16 v[160:163], v[108:111], v[172:175], v[160:163]\n\t"
            "v_mfma_f32_16x16x32_bf16 v[164:167], v[132:135], v[172:175], v[164:167]\n\t"
            "v_mfma_f32_16x16x32_bf16 v[164:167], v[128:131], v[192:195], v[164:167]\n\t"
            "s_nop 1\n\t"
            "v_cvt_pk_bf16_f32 v144, v144, v145\n\t"
            "v_mfma_f32_16x16x32_bf16 v[152:155], v[64:67], v[184:187], v[152:155]\n\t"
            "v_cvt_pk_bf16_f32 v145, v146, v147\n\t"
            "v_mfma_f32_16x16x32_bf16 v[152:155], v[68:71], v[188:191], v[152:155]\n\t"
            "v_cvt_pk_bf16_f32 v146, v148, v149\n\t"
            "v_cvt_pk_bf16_f32 v147, v150, v151\n\t"
            "v_mfma_f32_16x16x32_bf16 v[156:159], v[92:95], v[188:191], v[156:159]\n\t"
            "v_mfma_f32_16x16x32_bf16 v[156:159], v[88:91], v[184:187], v[156:159]\n\t"
            "buffer_store_dwordx4 v[144:147], v198, s[40:43], s45 offen sc1\n\t"
            "v_mfma_f32_16x16x32_bf16 v[160:163], v[112:115], v[184:187], v[160:163]\n\t"
            "s_nop 1\n\t"
            "v_cvt_pk_bf16_f32 v152, v152, v153\n\t"
            "v_mfma_f32_16x16x32_bf16 v[160:163], v[116:119], v[188:191], v[160:163]\n\t"
            "v_cvt_pk_bf16_f32 v153, v154, v155\n\t"
            "v_mfma_f32_16x16x32_bf16 v[164:167], v[140:143], v[188:191], v[164:167]\n\t"
            "v_cvt_pk_bf16_f32 v154, v156, v157\n\t"
            "v_mfma_f32_16x16x32_bf16 v[164:167], v[136:139], v[184:187], v[164:167]\n\t"
            "v_cvt_pk_bf16_f32 v155, v158, v159\n\t"
            "buffer_store_dwordx4 v[152:155], v198, s[40:43], s45 offen offset:64 sc1\n\t"
            "s_nop 1\n\t"
            "v_cvt_pk_bf16_f32 v160, v160, v161\n\t"
            "v_cvt_pk_bf16_f32 v161, v162, v163\n\t"
            "s_nop 1\n\t"
            "v_cvt_pk_bf16_f32 v162, v164, v165\n\t"
            "v_cvt_pk_bf16_f32 v163, v166, v167\n\t"
            "buffer_store_dwordx4 v[160:163], v198, s[40:43], s45 offen offset:128 sc1\n\t"
            "s_nop 7\n\t"
            "s_branch .Lwk_pe%=\n\t"
            "wk_conv:\n\t"
            "s_nop 7\n\t"
            "ds_read_b128 v[168:171], v254\n\t"
            "ds_read_b64 v[144:145], v253\n\t"
            "ds_read_b64 v[146:147], v253 offset:96\n\t"
            "ds_read_b64 v[148:149], v253 offset:1744\n\t"
            "ds_read_b64 v[150:151], v253 offset:1840\n\t"
            "ds_read_b128 v[172:175], v254 offset:64\n\t"
            "ds_read_b64 v[152:153], v253 offset:8\n\t"
            "ds_read_b64 v[154:155], v253 offset:104\n\t"
            "ds_read_b64 v[156:157], v253 offset:1752\n\t"
            "ds_read_b64 v[158:159], v253 offset:1848\n\t"
            "ds_read_b64 v[160:161], v253 offset:16\n\t"
            "ds_read_b64 v[162:163], v253 offset:112\n\t"
            "ds_read_b64 v[164:165], v253 offset:1760\n\t"
            "ds_read_b64 v[166:167], v253 offset:1856\n\t"
            "v_mov_b64 v[176:177], 0\n\t"
            "v_mov_b64 v[178:179], 0\n\t"
            "v_mov_b64 v[180:181], 0\n\t"
            "v_mov_b64 v[182:183], 0\n\t"
            "v_mov_b64 v[184:185], 0\n\t"
            "v_mov_b64 v[186:187], 0\n\t"
            "v_mov_b64 v[188:189], 0\n\t"
            "v_mov_b64 v[190:191], 0\n\t"
            "v_add_u32 v236, s67, v250\n\t"
            "v_xor_b32 v237, 16, v236\n\t"
            "v_add_u32 v248, s67, v251\n\t"
            "s_waitcnt lgkmcnt(9)\n\t"
            "v_smfmac_f32_16x16x64_bf16 v[176:179], v[168:171], v[144:151], v255\n\t"
            "ds_read_b128 v[168:171], v254 offset:128\n\t"
            "ds_read_b64 v[144:145], v253 offset:24\n\t"
            "s_waitcnt lgkmcnt(6)\n\t"
            "v_smfmac_f32_16x16x64_bf16 v[180:183], v[172:175], v[152:159], v255\n\t"
            "ds_read_b64 v[146:147], v253 offset:120\n\t"
            "ds_read_b64 v[148:149], v253 offset:1768\n\t"
            "ds_read_b64 v[150:151], v253 offset:1864\n\t"
            "ds_read_b128 v[172:175], v254 offset:192\n\t"
            "ds_read_b64 v[152:153], v253 offset:32\n\t"
            "ds_read_b64 v[154:155], v253 offset:128\n\t"
            "ds_read_b64 v[156:157], v253 offset:1776\n\t"
            "ds_read_b64 v[158:159], v253 offset:1872\n\t"
            "v_cvt_pk_bf16_f32 v176, v176, v177\n\t"
            "v_cvt_pk_bf16_f32 v177, v178, v179\n\t"
            "v_cvt_pk_bf16_f32 v178, v180, v181\n\t"
            "v_cvt_pk_bf16_f32 v179, v182, v183\n\t"
            "v_mfma_f32_4x4x4_16b_bf16 v[196:199], v[176:177], v[176:177], 0\n\t"
            "ds_write_b128 v236, v[176:179]\n\t"
            "v_mfma_f32_16x16x32_bf16 v[192:195], v[238:241], v[176:179], 0\n\t"
            "v_mov_b64 v[176:177], 0\n\t"
            "v_mov_b64 v[180:181], 0\n\t"
            "v_mfma_f32_4x4x4_16b_bf16 v[196:199], v[178:179], v[178:179], v[196:199]\n\t"
            "v_mov_b64 v[178:179], 0\n\t"
            "v_mov_b64 v[182:183], 0\n\t"
            "s_waitcnt lgkmcnt(10)\n\t"
            "v_smfmac_f32_16x16x64_bf16 v[184:187], v[168:171], v[160:167], v255\n\t"
            "ds_read_b128 v[168:171], v254 offset:256\n\t"
            "ds_read_b64 v[160:161], v253 offset:40\n\t"
            "ds_read_b64 v[162:163], v253 offset:136\n\t"
            "ds_read_b64 v[164:165], v253 offset:1784\n\t"
            "ds_read_b64 v[166:167], v253 offset:1880\n\t"
            "s_waitcnt lgkmcnt(10)\n\t"
            "v_smfmac_f32_16x16x64_bf16 v[188:191], v[172:175], v[144:151], v255\n\t"
            "ds_read_b128 v[172:175], v254 offset:320\n\t"
            "ds_read_b64 v[144:145], v253 offset:48\n\t"
            "ds_read_b64 v[146:147], v253 offset:144\n\t"
            "s_cbranch_scc1 .Lwk_np1%=\n\t"
            "s_mov_b64 exec, 0x3ffff\n\t"
            "buffer_load_dwordx4 v252, s[84:87], s89 offen sc1 lds\n\t"
            "s_mov_b64 exec, -1\n\t"
            ".Lwk_np1%=:\n\t"
            "ds_read_b64 v[148:149], v253 offset:1792\n\t"
            "ds_read_b64 v[150:151], v253 offset:1888\n\t"
            "v_cvt_pk_bf16_f32 v184, v184, v185\n\t"
            "v_cvt_pk_bf16_f32 v185, v186, v187\n\t"
            "s_nop 0\n\t"
            "v_cvt_pk_bf16_f32 v186, v188, v189\n\t"
            "v_cvt_pk_bf16_f32 v187, v190, v191\n\t"
            "v_mfma_f32_4x4x4_16b_bf16 v[196:199], v[184:185], v[184:185], v[196:199]\n\t"
            "s_waitcnt lgkmcnt(14)\n\t"
            "ds_write_b128 v237, v[184:187]\n\t"
            "v_mfma_f32_16x16x32_bf16 v[192:195], v[238:241], v[184:187], v[192:195]\n\t"
            "v_mov_b64 v[184:185], 0\n\t"
            "v_mov_b64 v[188:189], 0\n\t"
            "v_mfma_f32_4x4x4_16b_bf16 v[196:199], v[186:187], v[186:187], v[196:199]\n\t"
            "v_mov_b64 v[186:187], 0\n\t"
            "v_mov_b64 v[190:191], 0\n\t"
            "s_waitcnt lgkmcnt(10)\n\t"
            "v_smfmac_f32_16x16x64_bf16 v[176:179], v[168:171], v[152:159], v255\n\t"
            "ds_read_b128 v[168:171], v254 offset:384\n\t"
            "ds_read_b64 v[152:153], v253 offset:56\n\t"
            "ds_read_b64 v[154:155], v253 offset:152\n\t"
            "ds_read_b64 v[156:157], v253 offset:1800\n\t"
            "ds_read_b64 v[158:159], v253 offset:1896\n\t"
            "s_waitcnt lgkmcnt(10)\n\t"
            "v_smfmac_f32_16x16x64_bf16 v[180:183], v[172:175], v[160:167], v255\n\t"
            "ds_read_b128 v[172:175], v254 offset:448\n\t"
            "ds_read_b64 v[160:161], v253 offset:64\n\t"
            "ds_read_b64 v[162:163], v253 offset:160\n\t"
            "ds_read_b64 v[164:165], v253 offset:1808\n\t"
            "ds_read_b64 v[166:167], v253 offset:1904\n\t"
            "v_cvt_pk_bf16_f32 v176, v176, v177\n\t"
            "v_cvt_pk_bf16_f32 v177, v178, v179\n\t"
            "s_nop 0\n\t"
            "v_cvt_pk_bf16_f32 v178, v180, v181\n\t"
            "v_cvt_pk_bf16_f32 v179, v182, v183\n\t"
            "v_mfma_f32_4x4x4_16b_bf16 v[196:199], v[176:177], v[176:177], v[196:199]\n\t"
            "s_waitcnt lgkmcnt(14)\n\t"
            "ds_write_b128 v236, v[176:179] offset:128\n\t"
            "v_mfma_f32_16x16x32_bf16 v[192:195], v[238:241], v[176:179], v[192:195]\n\t"
            "v_mov_b64 v[176:177], 0\n\t"
            "v_mov_b64 v[180:181], 0\n\t"
            "v_mfma_f32_4x4x4_16b_bf16 v[196:199], v[178:179], v[178:179], v[196:199]\n\t"
            "v_mov_b64 v[178:179], 0\n\t"
            "v_mov_b64 v[182:183], 0\n\t"
            "s_waitcnt lgkmcnt(10)\n\t"
            "v_smfmac_f32_16x16x64_bf16 v[184:187], v[168:171], v[144:151], v255\n\t"
            "ds_read_b128 v[168:171], v254 offset:512\n\t"
            "ds_read_b64 v[144:145], v253 offset:72\n\t"
            "ds_read_b64 v[146:147], v253 offset:168\n\t"
            "ds_read_b64 v[148:149], v253 offset:1816\n\t"
            "ds_read_b64 v[150:151], v253 offset:1912\n\t"
            "s_waitcnt lgkmcnt(10)\n\t"
            "v_smfmac_f32_16x16x64_bf16 v[188:191], v[172:175], v[152:159], v255\n\t"
            "ds_read_b128 v[172:175], v254 offset:576\n\t"
            "ds_read_b64 v[152:153], v253 offset:80\n\t"
            "ds_read_b64 v[154:155], v253 offset:176\n\t"
            "ds_read_b64 v[156:157], v253 offset:1824\n\t"
            "ds_read_b64 v[158:159], v253 offset:1920\n\t"
            "v_cvt_pk_bf16_f32 v184, v184, v185\n\t"
            "v_cvt_pk_bf16_f32 v185, v186, v187\n\t"
            "s_cbranch_scc1 .Lwk_np0%=\n\t"
            "s_mov_b64 exec, 0x3ffff\n\t"
            "buffer_load_dwordx4 v252, s[84:87], s89 offen sc1 lds\n\t"
            "s_mov_b64 exec, -1\n\t"
            ".Lwk_np0%=:\n\t"
            "s_nop 0\n\t"
            "v_cvt_pk_bf16_f32 v186, v188, v189\n\t"
            "v_cvt_pk_bf16_f32 v187, v190, v191\n\t"
            "v_mfma_f32_4x4x4_16b_bf16 v[196:199], v[184:185], v[184:185], v[196:199]\n\t"
            "s_waitcnt lgkmcnt(14)\n\t"
            "ds_write_b128 v237, v[184:187] offset:128\n\t"
            "v_mfma_f32_16x16x32_bf16 v[192:195], v[238:241], v[184:187], v[192:195]\n\t"
            "v_mov_b64 v[184:185], 0\n\t"
            "v_mov_b64 v[188:189], 0\n\t"
            "v_mfma_f32_4x4x4_16b_bf16 v[196:199], v[186:187], v[186:187], v[196:199]\n\t"
            "v_mov_b64 v[186:187], 0\n\t"
            "v_mov_b64 v[190:191], 0\n\t"
            "s_waitcnt lgkmcnt(10)\n\t"
            "v_smfmac_f32_16x16x64_bf16 v[176:179], v[168:171], v[160:167], v255\n\t"
            "ds_read_b128 v[168:171], v254 offset:640\n\t"
            "ds_read_b64 v[160:161], v253 offset:88\n\t"
            "ds_read_b64 v[162:163], v253 offset:184\n\t"
            "ds_read_b64 v[164:165], v253 offset:1832\n\t"
            "ds_read_b64 v[166:167], v253 offset:1928\n\t"
            "s_waitcnt lgkmcnt(10)\n\t"
            "v_smfmac_f32_16x16x64_bf16 v[180:183], v[172:175], v[144:151], v255\n\t"
            "ds_read_b128 v[172:175], v254 offset:704\n\t"
            "v_cvt_pk_bf16_f32 v176, v176, v177\n\t"
            "v_cvt_pk_bf16_f32 v177, v178, v179\n\t"
            "s_nop 1\n\t"
            "v_mfma_f32_4x4x4_16b_bf16 v[196:199], v[176:177], v[176:177], v[196:199]\n\t"
            "s_nop 1\n\t"
            "v_cvt_pk_bf16_f32 v178, v180, v181\n\t"
            "v_cvt_pk_bf16_f32 v179, v182, v183\n\t"
            "s_nop 1\n\t"
            "v_mfma_f32_4x4x4_16b_bf16 v[196:199], v[178:179], v[178:179], v[196:199]\n\t"
            "ds_write_b128 v236, v[176:179] offset:256\n\t"
            "s_waitcnt lgkmcnt(6)\n\t"
            "v_smfmac_f32_16x16x64_bf16 v[184:187], v[168:171], v[152:159], v255\n\t"
            "v_mfma_f32_16x16x32_bf16 v[192:195], v[238:241], v[176:179], v[192:195]\n\t"
            "s_waitcnt lgkmcnt(1)\n\t"
            "v_smfmac_f32_16x16x64_bf16 v[188:191], v[172:175], v[160:167], v255\n\t"
            "s_nop 4\n\t"
            "v_cvt_pk_bf16_f32 v184, v184, v185\n\t"
            "v_cvt_pk_bf16_f32 v185, v186, v187\n\t"
            "s_nop 1\n\t"
            "v_mfma_f32_4x4x4_16b_bf16 v[196:199], v[184:185], v[184:185], v[196:199]\n\t"
            "v_cvt_pk_bf16_f32 v186, v188, v189\n\t"
            "v_cvt_pk_bf16_f32 v187, v190, v191\n\t"
            "s_nop 1\n\t"
            "v_mfma_f32_4x4x4_16b_bf16 v[196:199], v[186:187], v[186:187], v[196:199]\n\t"
            "ds_write_b128 v237, v[184:187] offset:256\n\t"
            "v_mfma_f32_16x16x32_bf16 v[192:195], v[238:241], v[184:187], v[192:195]\n\t"
            "s_nop 7\n\t"
            "v_cndmask_b32_e64 v193, v196, v197, s[60:61]\n\t"
            "v_cndmask_b32_e64 v193, v193, v198, s[62:63]\n\t"
            "v_cndmask_b32_e64 v193, v193, v199, s[64:65]\n\t"
            "ds_write_b64 v248, v[192:193] offset:18816\n\t"
            "s_waitcnt lgkmcnt(0)\n\t"
            "s_nop 7\n\t"
            "s_setpc_b64 vcc\n\t"
            ".Lwk_pe%=:\n\t"
            : B_SIO, "+{v247}"(l16_), "+{v228}"(q0), "+{v229}"(q1), "+{v232}"(q2), "+{v233}"(q3), "+{v234}"(q4)
            : "{v242}"(oE_), "{v243}"(oO_), "{v244}"(pR0_), "{v245}"(pR1_), "{v246}"(pR2_), "{v250}"(pXw_), "{v251}"(pSTw_), "{v252}"(aCA_), "{v253}"(aB_), "{v254}"(aA_), "{v255}"(idx_), B_SIN, "{s44}"(ARR_OFF + blockIdx.x * 8), "{s[80:81]}"(mrow), "{s82}"(wave == 6 && cnt == 21 ? REC_OFF + (start - 20 * ((blockIdx.x & 7) * 32 + xi)) * 1280 : 0)
            : "v144", "v145", "v146", "v147", "v148", "v149", "v150", "v151", "v152", "v153", "v154", "v155", "v156", "v157", "v158", "v159", "v160", "v161", "v162", "v163", "v164", "v165", "v166", "v167", "v168", "v169", "v170", "v171", "v172", "v173", "v174", "v175", "v176", "v177", "v178", "v179", "v180", "v181", "v182", "v183", "v184", "v185", "v186", "v187", "v188", "v189", "v190", "v191", "v192", "v193", "v194", "v195", "v196", "v197", "v198", "v199", "v236", "v237", "v248", "v249", "v0", "v1", "v2", "v3", "v4", "v5", "v6", "v7", "v8", "v9", "v10", "v11", "v12", "v13", "v14", "v15", "v16", "v17", "v18", "v19", "v20", "v21", "v22", "v23", "v24", "v25", "v26", "v27", "v28", "v29", "v30", "v31", "v32", "v33", "v34", "v35", "v36", "v37", "v38", "v39", "v40", "v41", "v42", "v43", "v44", "v45", "v46", "v47", "v48", "v49", "v50", "v51", "v52", "v53", "v54", "v55", "v56", "v57", "v58", "v59", "v60", "v61", "v62", "v63", "v64", "v65", "v66", "v67", "v68", "v69", "v70", "v71", "v72", "v73", "v74", "v75", "v76", "v77", "v78", "v79", "v80", "v81", "v82", "v83", "v84", "v85", "v86", "v87", "v88", "v89", "v90", "v91", "v92", "v93", "v94", "v95", "v96", "v97", "v98", "v99", "v100", "v101", "v102", "v103", "v104", "v105", "v106", "v107", "v108", "v109", "v110", "v111", "v112", "v113", "v114", "v115", "v116", "v117", "v118", "v119", "v120", "v121", "v122", "v123", "v124", "v125", "v126", "v127", "v128", "v129", "v130", "v131", "v132", "v133", "v134", "v135", "v136", "v137", "v138", "v139", "v140", "v141", "v142", "v143", "v200", "v201", "v202", "v204", "v205", "v206", "v207", "v208", "v209", "v210", "v211", "v212", "v213", "v214", "v215", "v216", "v217", "v218", "v219", "v220", "v221", "v222", "v223", "v224", "v225", "v226", "v227", "v230", "v231", "v235", B_SCLOB);
        }
        __builtin_amdgcn_endpgm();
    }
}

extern "C" void kernel_launch(void* const* d_in, const int* in_sizes, int n_in,
                              void* d_out, int out_size, void* d_ws, size_t ws_size,
                              hipStream_t stream) {
    const unsigned short* x = (const unsigned short*)d_in[0];
    const unsigned short* dw = (const unsigned short*)d_in[1];
    const unsigned short* wqkv = (const unsigned short*)d_in[2];
    const unsigned short* wo = (const unsigned short*)d_in[3];
    unsigned short* out = (unsigned short*)d_out;
    u32x4* wf = (u32x4*)d_ws;
    win_attn_kernel<<<GRID, 512, 0, stream>>>(x, dw, wqkv, wo, wf, out);
}
```

```cpp
#include <hip/hip_runtime.h>
#include <hip/hip_bf16.h>
#include <stdint.h>

typedef short bf16x8 __attribute__((ext_vector_type(8)));
typedef float f32x4 __attribute__((ext_vector_type(4)));
typedef float f32x2 __attribute__((ext_vector_type(2)));
typedef unsigned u32x2 __attribute__((ext_vector_type(2)));
typedef unsigned u32x4 __attribute__((ext_vector_type(4)));

constexpr int IMG = 511;
constexpr int CH = 192;
constexpr int NWX = 73;
constexpr int NWIN = NWX * NWX;
constexpr int GRID = 256;
constexpr int ROWBYTES = IMG * CH * 2;
constexpr int XBIAS = (IMG + 1) * CH * 2;
constexpr unsigned OOB = 0x40000000u;

constexpr int FLAG_OFF = 7856;
constexpr int X_OFF = 32768;
constexpr int X1_OFF = 53632;
constexpr int O_OFF = 74496;
constexpr int V_OFF = 92928;
constexpr int KQL_OFF = 111360;
constexpr int OL_OFF = 129024;
constexpr int AT_OFF = 137472;
constexpr int CT_OFF = AT_OFF + 19584;
constexpr int RS_OFF = CT_OFF + 4096;
constexpr int RSX_OFF = 162816;
constexpr int WL_OFF = RS_OFF + 2304 + 64;
constexpr int LDS_BYTES = WL_OFF + 128;
#define RS_BASE (RS_OFF + wave * 384)
constexpr int STL_OFF = O_OFF + 12288;
constexpr int DW_OFF = O_OFF + 8448;
constexpr int CP_OFF = X1_OFF + 16384;
#define LX_OFF(i) ((i) < 2 ? CT_OFF + 1024 + (i) * 384 : KQL_OFF + ((i) - 2) * 800)
constexpr int PF_OFF = CT_OFF + 2048;
#define LB_OFF (w < 3 ? V_OFF + w * 13952 : X1_OFF)
#define LPOS(wc) (((wc) & 1) ? 0 : 6)
constexpr unsigned CROW = 0x060a0230u;
constexpr unsigned CLANE = 0x00132C3Fu;

constexpr unsigned long long TOKMAP[6] = {0x20001c190f2a2202ull, 0x052517121f061a13ull, 0x181e08151d0b260cull, 0x1b2b2903102d0901ull, 0x04162e0e2824142cull, 0x110a072127232f0dull};
constexpr unsigned long long ROWMAP[8] = {0x08321b240f1c0006ull, 0x23310e2a09170d14ull, 0x210c2f030a273418ull, 0x1d2b11221f043739ull, 0x3234363a103e192dull, 0x291a20022c3f1605ull, 0x303a3c2628122e15ull, 0x39253d1e01070b13ull};

constexpr int WF_BYTES = 8 * 36 * 1024;
constexpr int FLS = 128;
constexpr int ARR_OFF = WF_BYTES + 4096;
constexpr int EPO_OFF = ARR_OFF + 2048;
constexpr int CTR_OFF = EPO_OFF + 128;
constexpr int REC_OFF = WF_BYTES + 8192;
constexpr int FLG_OFF = REC_OFF + 209 * 1280;
constexpr int WS_BYTES = FLG_OFF + 8 * 36 * FLS;
constexpr int SC1 = 16;

#define MFMA32(a, b, c) __builtin_amdgcn_mfma_f32_16x16x32_bf16((a), (b), (c), 0, 0, 0)
#define BC8(v) __builtin_bit_cast(bf16x8, (v))
#define BAR() asm volatile("s_waitcnt lgkmcnt(0)\n\ts_barrier" ::: "memory")
#define SB() __builtin_amdgcn_sched_barrier(0)
#define OPQ(x) asm volatile("" : "+v"(x))
#define ZERO4 ((f32x4){0.f, 0.f, 0.f, 0.f})

typedef __bf16 nbf16x2 __attribute__((ext_vector_type(2)));
__device__ __forceinline__ unsigned pk2(float a, float b) {
    f32x2 v = {a, b};
    nbf16x2 r = __builtin_convertvector(v, nbf16x2);
    return __builtin_bit_cast(unsigned, r);
}
__device__ __forceinline__ float bflo(unsigned u) { return __uint_as_float(u << 16); }
__device__ __forceinline__ float bfhi(unsigned u) { return __uint_as_float(u & 0xffff0000u); }

__device__ __forceinline__ float bf2f(unsigned short v) { return __uint_as_float((unsigned)v << 16); }
__device__ __forceinline__ unsigned byte_of(unsigned long long a, unsigned long long b, int i) {
    return (unsigned)(((i & 8) ? b : a) >> (8 * (i & 7))) & 0xFFu;
}
#define M24(a, b) ((unsigned)__mul24((int)(a), (int)(b)))
__device__ __forceinline__ void seq_to_win(int seq, int& wy, int& wx) {
    const bool last = seq >= 5110;
    const int s = last ? 35 : M24(seq, 28729) >> 22, r = seq - M24(146, s);
    const int kx = last ? M24(r, 171) >> 10 : r >> 2, pos = r - M24(last ? 6 : 4, kx);
    const bool low = last && pos >= (kx == 36 ? 2 : 4);
    wy = low ? 72 : 2 * s + (pos & 1);
    wx = 2 * kx + (low ? (kx == 36 ? 0 : pos - 4) : pos >> 1);
}
__device__ __forceinline__ int win_to_seq(int wy, int wx) {
    const int kx = wx >> 1;
    return M24(146, wy == 72 ? 35 : wy >> 1) + M24(wy >= 70 ? 6 : 4, kx) + (wy == 72 ? (kx == 36 ? 2 : 4 + (wx & 1)) : 2 * (wx & 1) + (wy & 1));
}
__device__ __forceinline__ int block_of(int wy, int wx) { return wy >= 70 ? 1295 + (wx & ~1) + (wy == 72) : M24(37, wy >> 1) + (wx >> 1); }
__device__ __forceinline__ void block_pos(int b, int& ky, int& kx) {
    const int s = M24(b, 1772) >> 16, l = b - 1295;
    kx = l >= 0 ? l >> 1 : b - M24(37, s);
    ky = l >= 0 ? 70 + 2 * (l & 1) : 2 * s;
}
__device__ __forceinline__ unsigned win_org(int wy, int wx) { return M24(wy, 7 * ROWBYTES) + M24(wx, 7 * CH * 2); }
__device__ __forceinline__ unsigned wl_entry(int wy, int wx) {
    return win_org(wy, wx) | (wy == 0 ? 0x08000000u : 0u) | (wx == 0 ? 0x10000000u : 0u) |
           (wx == NWX - 1 ? 0x20000000u : 0u) | ((unsigned)(wx & 1) << 30) | ((unsigned)(wy & 1) << 31);
}
__device__ __forceinline__ unsigned dma_off(int lane) {
    const int col = (lane * 43) >> 8;
    return (unsigned)(col * (CH * 2 - 96) + 16 * lane) | (col == 0 ? 0x10000000u : 0u) | (col == 8 ? 0x20000000u : 0u);
}

#if defined(__HIP_DEVICE_COMPILE__)
#define NOPK __attribute__((target("no-packed-fp32-ops")))
#else
#define NOPK
#endif
__global__ __launch_bounds__(512, 2) NOPK void win_attn_kernel(const unsigned short* __restrict__ x,
                                                          const unsigned short* __restrict__ dw, const unsigned short* __restrict__ wqkv,
                                                          const unsigned short* __restrict__ wo, u32x4* wf,
                                                          unsigned short* __restrict__ out) {
    __shared__ __attribute__((aligned(16))) char smem[LDS_BYTES];

    asm volatile("wk_code_begin:");
    const int tid = threadIdx.x;
    const int wave = __builtin_amdgcn_readfirstlane(tid >> 6);
    const int w = wave & 3;
    const unsigned ldsb = (unsigned)(size_t)smem;
    {
        unsigned long long pc;
        unsigned len, rel;
        asm volatile("s_getpc_b64 %0\n.Lwk_here%=:\n\ts_mov_b32 %1, wk_code_end-wk_code_begin\n\ts_mov_b32 %2, .Lwk_here%=-wk_code_begin" : "=s"(pc), "=s"(len), "=s"(rel));
        pc -= rel;
        const u32x4 cr = {(unsigned)pc, (unsigned)(pc >> 32) & 0xffffu, len, 0x00020000u};
        asm volatile("s_mov_b32 m0, %0\n\ts_nop 0\n\tbuffer_load_dwordx4 %1, %2, %3 offen lds"
                     :: "s"(ldsb + X1_OFF + 14336 + (w & 1) * 1024), "v"((tid & 63) * 16), "s"(cr), "s"((wave * 32 + (blockIdx.x >> 3)) * 1024) : "m0", "memory");
    }
    *(unsigned*)(smem + PF_OFF + tid * 4) = 0x9E3779B9u * (tid + 1);
    *(unsigned*)(smem + (tid < 192 ? AT_OFF + 18816 + tid * 4 : FLAG_OFF + (tid & 3) * 4)) = 0u;
    const auto wrsrc = __builtin_amdgcn_make_buffer_rsrc((void*)wf, 0, WS_BYTES, 0x00020000);
    unsigned tag_lo = 0, tag_hi = 0;
#define TAG_READY                                                                                  \
    {                                                                                              \
        unsigned long long tag_;                                                                   \
        asm volatile("s_load_dwordx2 %0, %1, %2\n\ts_waitcnt lgkmcnt(0)" : "=s"(tag_) : "s"(wf), "n"(EPO_OFF) : "memory"); \
        tag_lo = (unsigned)tag_ + 1u; tag_hi = (unsigned)(tag_ >> 32);                             \
    }
    const int pb = blockIdx.x;
    if (pb < 36) {
        const int lane = tid & 63, n = lane & 15, g = lane >> 4;
        const bool isO = wave >= 6;
        const int fr = isO ? 216 + 2 * pb + (wave - 6) : 6 * pb + wave;
        const int tile = fr / 6, ks = fr - 6 * tile, tw = tile / 6, mdt = tile - 6 * tw;
        const int col = isO ? 96 * (tw - 6) + 32 * (mdt >> 1) + 8 * (n >> 2) + 4 * (mdt & 1) + (n & 3)
                            : (tw < 4 ? (mdt < 3 ? 192 : -48) + 48 * tw : 96 * tw) + 16 * mdt + n;
        const int rb = isO ? 384 : 1152;
        const auto rr = __builtin_amdgcn_make_buffer_rsrc((void*)(isO ? wo : wqkv), 0, 192 * rb, 0x00020000);
        const int vo = (48 * (g >> 1) + 8 * (g & 1)) * rb + 2 * col;
        float v[8];
#pragma unroll
        for (int j = 0; j < 8; ++j) v[j] = bf2f(__builtin_amdgcn_raw_buffer_load_b16(rr, vo, (96 * (ks & 1) + 16 * (ks >> 1) + j) * rb, 0));
        TAG_READY
        float* red = (float*)smem;
        red[n * 36 + wave * 4 + g] = ((v[0] + v[1]) + (v[2] + v[3])) + ((v[4] + v[5]) + (v[6] + v[7]));
        BAR();
        float mean = 0.f;
#pragma unroll
        for (int i = 0; i < 6; ++i) {
            const f32x4 p = *(const f32x4*)(red + n * 36 + 4 * i);
            mean += (p[0] + p[1]) + (p[2] + p[3]);
        }
        mean = isO ? 0.f : mean * (1.f / 192.f);
        u32x4 o;
#pragma unroll
        for (int j = 0; j < 4; ++j) o[j] = pk2(v[2 * j] - mean, v[2 * j + 1] - mean);
        __builtin_amdgcn_raw_buffer_store_b128(o, wrsrc, lane * 16, fr * 1024, SC1);
        if (pb == 0 && tid == 448) __builtin_amdgcn_raw_buffer_store_b32(0u, wrsrc, CTR_OFF, 0, SC1);
        asm volatile("s_waitcnt vmcnt(0)" ::: "memory");
        BAR();
        if (tid < 8) __builtin_amdgcn_raw_buffer_store_b64((u32x2){tag_lo, tag_hi}, wrsrc, FLG_OFF + (tid * 36 + pb) * FLS, 0, SC1);
    }
    const int xi = blockIdx.x >> 3, nshort = (int)(0x0506060606060606ull >> (8 * (blockIdx.x & 7))) & 63;
    const int start = 672 * (blockIdx.x & 7) - ((int)(0x2a241e18120c0600ull >> (8 * (blockIdx.x & 7))) & 63) + 21 * xi - (xi < nshort ? xi : nshort);
    const int cnt = xi < nshort ? 20 : 21;

    const auto xrsrc = __builtin_amdgcn_make_buffer_rsrc((void*)((const char*)x - XBIAS), 0, IMG * IMG * CH * 2 + XBIAS, 0x00020000);
#define LANE_SETUP                                                                                 \
    int lane;                                                                                      \
    asm volatile("v_mbcnt_lo_u32_b32 %0, -1, 0\n\tv_mbcnt_hi_u32_b32 %0, -1, %0" : "=v"(lane));    \
    __builtin_assume(lane >= 0 && lane < 64);                                                      \
    const int g = lane >> 4, l15 = lane & 15, sw = l15 >> 1;                                       \
    int fE = X_OFF + l15 * 384 + ((g ^ sw) << 4);                                                  \
    OPQ(fE);

#define DMA_SLICE(en)                                                                              \
        {                                                                                          \
            const unsigned vo_ = dma_off(lane) & (0x401fffffu | ((en) & 0x38000000u));             \
            const unsigned so_ = ((en) & 0x7ffffffu) + 96 * w;                                     \
            _Pragma("unroll") for (int r = 0; r < 9; ++r) {                                        \
                unsigned m0_ = ldsb + w * 8192 + r * 872;                                          \
                unsigned sr_ = (r == 0 && ((en) & 0x08000000u)) ? OOB : so_ + r * ROWBYTES;        \
                asm volatile("s_mov_b32 m0, %0\n\ts_mov_b32 exec_hi, 0x3fffff\n\tbuffer_load_dwordx4 %1, %2, %3 offen lds\n\ts_mov_b32 exec_hi, -1" \
                             :: "s"(m0_), "v"(vo_), "s"(xrsrc), "s"(sr_) : "m0", "memory");        \
            }                                                                                      \
        }
#define XPAT(r) (((r) < 48 ? (r) : 48) * 384 + (((2 * w) ^ (((r) >> 1) & 7)) << 4))
#define SPAT(r) ((w >> 1) * 1024 + (w & 1) * 8 + (r) * 16)
#define CONV_CONSTS(var) CONV_ROWS(var) CONV_OPS
#define CONV_ROWS(var) \
            const unsigned long long rlo = g == 0 ? ROWMAP[0] : (g == 1 ? ROWMAP[2] : (g == 2 ? ROWMAP[4] : ROWMAP[6])); \
            const unsigned long long rhi = g == 0 ? ROWMAP[1] : (g == 1 ? ROWMAP[3] : (g == 2 ? ROWMAP[5] : ROWMAP[7])); \
            int xrow = byte_of(rlo, rhi, l15); \
            if (lane == (int)((CLANE >> (8 * (var))) & 0xFF)) xrow = 48; \
            if (lane == 63) xrow = (CROW >> (8 * (var))) & 0xFF; \
            const unsigned pXw_ = XPAT(xrow); \
            const unsigned pSTw_ = (w >> 1) * 1024 + (w & 1) * 8 + xrow * 16; \
            const int oy = (0x5320 >> (4 * (l15 >> 2))) & 15, ox = (0x5320 >> (4 * (l15 & 3))) & 15; \
            const unsigned aB_ = ldsb + w * 8192 + (oy + (g >> 1)) * 872 + (ox + 2 * (g & 1)) * 96;
#define CONV_OPS \
            const int dyi = g - (l15 >> 3); \
            const unsigned aA_ = ldsb + AT_OFF + (l15 & 3) * 16 + \
                                 ((dyi < 0 || dyi > 2) ? 18816 : (dyi == 0 ? 0 : (dyi == 1 ? 6208 : 12544)) + ((l15 >> 2) & 1) * 3072 + w * 768); \
            const unsigned idx_ = (l15 & 3) * 0x5555u; \
            const unsigned se = (l15 == 4 * g) ? 0x3F803F80u : 0u; \
            const u32x4 sel4 = {se, se, se, se};

    unsigned en0, en1;
    {
        LANE_SETUP
        const int px = (lane * 43) >> 8;

        int wy, wx;
        seq_to_win(start + (lane < cnt ? lane : cnt - 1), wy, wx);
        const unsigned en = wl_entry(wy, wx);
        en0 = __builtin_amdgcn_readlane(en, 0);
        en1 = __builtin_amdgcn_readlane(en, 1);
        if (wave >= 4) {
            const int bk = block_of(wy, wx);
            const int key0 = __builtin_amdgcn_readlane(bk, 0), nblk = __builtin_amdgcn_readlane(bk, 63) - key0 + 1;
            const int b = l15 < nblk ? l15 : 0;
            int ky, kx;
            block_pos(key0 + b, ky, kx);
            const unsigned org = l15 < nblk ? (win_org(ky, 2 * kx) + (5 * ROWBYTES + 5 * CH * 2 + XBIAS)) | (kx == 36 ? OOB : 0u) : OOB + 0x8000000u;
            const unsigned lo = (px & 3) * (CH * 2) + (lane - 6 * px) * 16, keep = (px & 2) ? ~0u : ~OOB;
            BAR();
#pragma unroll
            for (int p = 0; p < 4; ++p) {
                if (p < 3 || nblk > 6) {
                    const unsigned oa = __builtin_amdgcn_readlane(org, 2 * p), ob = __builtin_amdgcn_readlane(org, 2 * p + 1);
                    const unsigned vo_ = ((lane < 24 ? oa : ob) & keep) + lo;
#pragma unroll
                    for (int r = 0; r < 4; ++r) {
                        unsigned m0_ = ldsb + LB_OFF + (4 * p + r) * 872;
                        asm volatile("s_mov_b32 m0, %0\n\ts_mov_b32 exec_hi, 0xffff\n\tbuffer_load_dwordx4 %1, %2, %3 offen lds\n\ts_mov_b32 exec_hi, -1"
                                     :: "s"(m0_), "v"(vo_), "s"(xrsrc), "s"(96 * w + r * ROWBYTES) : "m0", "memory");
                    }
                }
            }
            SB();
            const int ly = ky + (g >> 1), lx = 2 * kx + (g & 1), li = win_to_seq(ly, lx) - start;
            const bool ok = ly < NWX && lx < NWX && li >= 0 && li < cnt && l15 < nblk;
            const int row = ok ? li : 47;
            unsigned* cp = (unsigned*)(smem + CP_OFF + (w * 64 + lane) * 16);
            *(u32x2*)cp = (u32x2){(unsigned)XPAT(row), SPAT(row) + (unsigned)(STL_OFF - O_OFF - 18816)};
            cp[2] = ldsb + LB_OFF + (4 * (b >> 1) + (g >> 1)) * 872 + (b & 1) * 384 + (g & 1) * 192;
        } else {
            {
                const auto dr = __builtin_amdgcn_make_buffer_rsrc((void*)dw, 0, 9 * 384, 0x00020000);
                unsigned m0_ = ldsb + DW_OFF + w * 864, vo_ = px * 288 + lane * 16;
                asm volatile("s_mov_b32 m0, %0\n\ts_mov_b32 exec_hi, 0x3fffff\n\tbuffer_load_dwordx4 %1, %2, %3 offen lds\n\ts_mov_b32 exec_hi, -1"
                             :: "s"(m0_), "v"(vo_), "s"(dr), "s"(96 * w) : "m0", "memory");
            }
            DMA_SLICE(en0)
            BAR();
            if (wave == 2) {
                u32x4 so;
#pragma unroll
                for (int e = 0; e < 3; ++e) {
                    const int tk = byte_of(TOKMAP[2 * e], TOKMAP[2 * e + 1], l15);
                    const int ty = (tk * 37) >> 8, tx = tk - 7 * ty;
                    so[e] = (unsigned)(ty * ROWBYTES + tx * (CH * 2)) + 16 * g;
                }
                so[3] = dma_off(lane);
                *(u32x4*)(smem + CT_OFF + lane * 16) = so;
            }
            if (wave == 1 && lane < 32) *(unsigned*)(smem + WL_OFF + lane * 4) = lane < 20 ? en : 0x7ffffffu;
            if (wave == 1 && lane == 20) *(unsigned*)(smem + PF_OFF + 648) = en;
            asm volatile("s_waitcnt vmcnt(9)" ::: "memory");
            if (lane < 48) {
                unsigned short dv[9];
#pragma unroll
                for (int i = 0; i < 9; ++i) dv[i] = *(const unsigned short*)(smem + DW_OFF + w * 864 + i * 96 + lane * 2);
#pragma unroll
                for (int dy = 0; dy < 3; ++dy) {
                    const unsigned a = dv[3 * dy], b2 = dv[3 * dy + 1] | (dy == 1 ? 0x3F800000u : 0u), c = dv[3 * dy + 2];
                    char* p = smem + AT_OFF + (dy == 0 ? 0 : (dy == 1 ? 6208 : 12544)) + w * 768 + lane * 16;
                    *(u32x4*)p = (u32x4){a, b2, c, 0u};
                    *(u32x4*)(p + 3072) = (u32x4){0u, a, b2, c};
                }
            }
            asm volatile("s_mov_b64 exec, 1\n\tds_add_u32 %0, %1\n\ts_mov_b64 exec, -1" :: "v"(ldsb + FLAG_OFF + 8), "v"(1) : "memory");
        }
    }
    const unsigned long long mrow = 0x0001000100010001ull * ((1u << (cnt - 16)) - 1u);
#define KARGS                                                                                      \
    unsigned long long xb, wb, ob;                                                                 \
    asm volatile("s_load_dwordx2 %0, %3, 0x0\n\ts_load_dwordx2 %1, %3, 0x20\n\ts_load_dwordx2 %2, %3, 0x28\n\ts_waitcnt lgkmcnt(0)" \
                 : "=&s"(xb), "=&s"(wb), "=&s"(ob) : "s"(__builtin_amdgcn_kernarg_segment_ptr()) : "memory"); \
    xb -= XBIAS;                                                                                   \
    const u32x4 wr = {(unsigned)wb, (unsigned)(wb >> 32) & 0xffffu, (unsigned)WS_BYTES, 0x00020000u}; \
    const u32x4 orr = {(unsigned)ob, (unsigned)(ob >> 32) & 0xffffu, (unsigned)(IMG * IMG * CH * 2), 0x00020000u}; \
    const u32x4 xr = {(unsigned)xb, (unsigned)(xb >> 32) & 0xffffu, (unsigned)(IMG * IMG * CH * 2 + XBIAS), 0x00020000u};
    const int tk_ = (xi + 4) / 6;
    const unsigned touch = wave * 36864 + ((xi + 4 - 6 * tk_) * 6 + tk_) * 1024;
#define B_SCALARS \
            CONV_CONSTS(0) \
            const unsigned aCA_ = ldsb + CT_OFF + lane * 16; \
            KARGS \
            unsigned s45 = OOB, s46 = (en0 & 0x7ffffffu) + 96 * w, s47 = (en1 & 0x7ffffffu) + 96 * w; \
            unsigned s48 = ldsb + X_OFF, s67 = ldsb + X1_OFF, s76 = ldsb + WL_OFF + 8, s70 = 0; \
            unsigned s56 = __builtin_amdgcn_readfirstlane((en0 >> 30) * 4 + (en1 >> 30)); \
            unsigned s69 = 0x401fffffu | (en1 & 0x38000000u), s79 = (en1 & 0x08000000u) ? OOB : s47, s59 = ldsb + LX_OFF(1) + 32 * w; \
            unsigned cp_ = ldsb + CP_OFF + (w * 64 + lane) * 16; \
            unsigned long long xp = 0, sp = 0; \
            _Pragma("unroll") for (int v = 0; v < 4; ++v) { \
                const unsigned r = (CROW >> (8 * v)) & 0xFF; \
                xp |= (unsigned long long)XPAT(r) << (16 * v); \
                sp |= (unsigned long long)SPAT(r) << (16 * v); \
            }
#define B_SIO "+{s45}"(s45), "+{s46}"(s46), "+{s47}"(s47), "+{s48}"(s48), "+{s67}"(s67), "+{s76}"(s76), "+{s56}"(s56), "+{s70}"(s70), "+{s69}"(s69), "+{s79}"(s79), "+{s59}"(s59), "+{v203}"(cp_)
#define B_SIN "{v[238:241]}"(sel4), "{s[40:43]}"(orr), "{s[72:75]}"(xr), "{s39}"(0x401fffffu), "{s[28:31]}"(wr), "{s33}"(wave * 36864), "{s78}"(96 * w), \
              "{s27}"(96 * w - 384), "{s[50:51]}"(xp), "{s[52:53]}"(sp), "{s54}"(CLANE), "{s55}"(CROW & 0x0F0F0F0Fu), "{s[34:35]}"(0x0001000100010001ull), \
              "{s[60:61]}"(0x2222222222222222ull), "{s[62:63]}"(0x4444444444444444ull), "{s[64:65]}"(0x8888888888888888ull), \
              "{s66}"(ldsb + w * 8192), "{s71}"((ldsb + X_OFF) ^ (ldsb + X1_OFF))
#define B_SCLOB "s83", "s84", "s26", "s77", "s68", "s36", "s37", "s38", "s57", "s58", "s49", "vcc", "scc", "m0", "memory"

    if (wave < 4) {
        LANE_SETUP
        const int dW = X_OFF + l15 * 384 + (((2 * w + (g >> 1)) ^ sw) << 4) + 8 * (g & 1);
        const int vB = V_OFF + w * 4608 + 1024 + lane * 8, rsR = RS_BASE + l15 * 4;
        {
            const unsigned rsb = ldsb + RS_BASE;
            const unsigned pE_ = fE - X_OFF, aO_ = ldsb + dW + (O_OFF - X_OFF), sel_ = (l15 & 3) ? 0u : 1u, aVB_ = ldsb + vB;
            unsigned pST_ = lane * 16, aPLw_ = rsb + lane * 2, aRSr_ = ldsb + rsR, aRSw_ = rsb + lane * 4;
            unsigned g16_ = (l15 & 3) ? ldsb + AT_OFF + 18816 : g * 16, g8_ = (l15 & 3) ? ldsb + AT_OFF + 18816 : g * 8;
            unsigned loVL_ = ((2 * w + (l15 >> 3)) << 4) + (l15 & 7) * 2, loOL_ = g * 128;
            unsigned on = (l15 & 3) ? 0u : 0x3F803F80u;
            CONV_CONSTS(en0 >> 30)
            unsigned c0 = lane == (int)((CLANE >> (8 * (en0 >> 30))) & 0xFF) ? LX_OFF(0) - X_OFF + 32 * w : pXw_, c1 = pSTw_, c2 = lane * FLS, c3 = aB_, c4 = aA_, c5 = idx_;
            unsigned c6 = se, c7 = se, c8 = se, c9 = se, s89 = FLG_OFF + (blockIdx.x & 7) * 36 * FLS, s46 = wave * 36864;
            unsigned s48 = ldsb + X_OFF, s70 = 0;
            unsigned s72 = ldsb + KQL_OFF + w * 192, s73 = ldsb + OL_OFF, s74 = ldsb + KQL_OFF + 784, s77 = ldsb + OL_OFF, s76 = ldsb + KQL_OFF + 768 + w * 4;
            KARGS
            asm volatile(
            "s_load_dwordx2 s[82:83], s[84:85], 301056\n\t"
            "s_mov_b32 m0, 159104\n\t"
            "s_mov_b32 s67, 32768\n\t"
            "s_mov_b32 s60, 0x22222222\n\t"
            "s_mov_b32 s61, 0x22222222\n\t"
            "s_mov_b32 s62, 0x44444444\n\t"
            "s_mov_b32 s63, 0x44444444\n\t"
            "s_mov_b32 s64, 0x88888888\n\t"
            "s_mov_b32 s65, 0x88888888\n\t"
            "s_waitcnt vmcnt(0) lgkmcnt(0)\n\t"
            "s_add_u32 s82, s82, 1\n\t"
            "s_cmp_lg_u32 s80, 16\n\t"
            "s_cbranch_scc1 .Lwa_p0%=\n\t"
            "s_mov_b32 exec_hi, 15\n\t"
            "buffer_load_dwordx4 v252, s[84:87], s89 offen sc1 lds\n\t"
            "s_mov_b32 exec_hi, -1\n\t"
            ".Lwa_p0%=:\n\t"
            "s_call_b64 vcc, wk_conv\n\t"
            "v_min_u32 v146, 0x230, v204\n\t"
            "v_add_u32 v146, 159104, v146\n\t"
            "s_mov_b32 s81, 0x10000\n\t"
            "v_mov_b32 v240, v200\n\t"
            "v_mov_b32 v241, v200\n\t"
            "v_mov_b32 v242, v200\n\t"
            "v_mov_b32 v243, v200\n\t"
            "v_mov_b32 v248, v204\n\t"
            "v_mov_b32 v249, v205\n\t"
            "v_mov_b32 v250, v206\n\t"
            "v_mov_b32 v251, v207\n\t"
            "v_mov_b32 v253, v209\n\t"
            "v_mov_b32 v254, v210\n\t"
            "v_mov_b32 v255, v211\n\t"
            ".Lwa_p1%=:\n\t"
            "ds_read_b64 v[144:145], v146\n\t"
            "s_waitcnt lgkmcnt(0)\n\t"
            "v_cmp_ne_u32 vcc, s82, v144\n\t"
            "v_cmp_ne_u32 s[60:61], s83, v145\n\t"
            "s_or_b64 vcc, vcc, s[60:61]\n\t"
            "s_cbranch_scc0 .Lwa_p3%=\n\t"
            "s_sub_u32 s81, s81, 1\n\t"
            "s_cbranch_scc1 .Lwa_p3%=\n\t"
            "s_cmp_lg_u32 s80, 16\n\t"
            "s_cbranch_scc1 .Lwa_p2%=\n\t"
            "s_mov_b32 exec_hi, 15\n\t"
            "buffer_load_dwordx4 v252, s[84:87], s89 offen sc1 lds\n\t"
            "s_mov_b32 exec_hi, -1\n\t"
            "s_waitcnt vmcnt(24)\n\t"
            ".Lwa_p2%=:\n\t"
            "s_sleep 3\n\t"
            "s_branch .Lwa_p1%=\n\t"
            ".Lwa_p3%=:\n\t"
            "v_mov_b32 v252, v208\n\t"
            "v_mov_b32 v144, s82\n\t"
            "v_mov_b32 v145, s83\n\t"
            "v_mov_b32 v146, 159744\n\t"
            "ds_write_b64 v146, v[144:145]\n\t"
            "s_mov_b32 s60, 0xffff0000\n\t"
            "s_mov_b32 s61, 0x0\n\t"
            "s_mov_b32 s62, 0x0\n\t"
            "s_mov_b32 s63, 0xffff\n\t"
            "s_mov_b32 s64, 0x0\n\t"
            "s_mov_b32 s65, 0xffff0000\n\t"
            "s_mov_b32 s66, 0xffffffff\n\t"
            "s_mov_b32 s67, 0x1ffff\n\t"
            "buffer_load_dwordx4 v[140:143], v248, s[84:87], s88 offen sc1\n\t"
            "s_add_u32 s49, s46, 0\n\t"
            "buffer_load_dwordx4 v[0:3], v248, s[84:87], s49 offen sc1\n\t"
            "s_add_u32 s49, s46, 6144\n\t"
            "buffer_load_dwordx4 v[24:27], v248, s[84:87], s49 offen sc1\n\t"
            "s_add_u32 s49, s46, 12288\n\t"
            "buffer_load_dwordx4 v[48:51], v248, s[84:87], s49 offen sc1\n\t"
            "s_add_u32 s49, s46, 18432\n\t"
            "buffer_load_dwordx4 v[72:75], v248, s[84:87], s49 offen sc1\n\t"
            "s_add_u32 s49, s46, 24576\n\t"
            "buffer_load_dwordx4 v[96:99], v248, s[84:87], s49 offen sc1\n\t"
            "s_add_u32 s49, s46, 30720\n\t"
            "buffer_load_dwordx4 v[120:123], v248, s[84:87], s49 offen sc1\n\t"
            "s_add_u32 s49, s46, 1024\n\t"
            "buffer_load_dwordx4 v[4:7], v248, s[84:87], s49 offen sc1\n\t"
            "s_add_u32 s49, s46, 7168\n\t"
            "buffer_load_dwordx4 v[28:31], v248, s[84:87], s49 offen sc1\n\t"
            "s_add_u32 s49, s46, 13312\n\t"
            "buffer_load_dwordx4 v[52:55], v248, s[84:87], s49 offen sc1\n\t"
            "s_add_u32 s49, s46, 19456\n\t"
            "buffer_load_dwordx4 v[76:79], v248, s[84:87], s49 offen sc1\n\t"
            "s_add_u32 s49, s46, 25600\n\t"
            "buffer_load_dwordx4 v[100:103], v248, s[84:87], s49 offen sc1\n\t"
            "s_add_u32 s49, s46, 31744\n\t"
            "buffer_load_dwordx4 v[124:127], v248, s[84:87], s49 offen sc1\n\t"
            "s_add_u32 s49, s46, 2048\n\t"
            "buffer_load_dwordx4 v[8:11], v248, s[84:87], s49 offen sc1\n\t"
            "s_add_u32 s49, s46, 8192\n\t"
            "buffer_load_dwordx4 v[32:35], v248, s[84:87], s49 offen sc1\n\t"
            "s_add_u32 s49, s46, 14336\n\t"
            "buffer_load_dwordx4 v[56:59], v248, s[84:87], s49 offen sc1\n\t"
            "s_add_u32 s49, s46, 20480\n\t"
            "buffer_load_dwordx4 v[80:83], v248, s[84:87], s49 offen sc1\n\t"
            "s_add_u32 s49, s46, 26624\n\t"
            "buffer_load_dwordx4 v[104:107], v248, s[84:87], s49 offen sc1\n\t"
            "s_add_u32 s49, s46, 32768\n\t"
            "buffer_load_dwordx4 v[128:131], v248, s[84:87], s49 offen sc1\n\t"
            "s_add_u32 s49, s46, 3072\n\t"
            "buffer_load_dwordx4 v[12:15], v248, s[84:87], s49 offen sc1\n\t"
            "s_add_u32 s49, s46, 9216\n\t"
            "buffer_load_dwordx4 v[36:39], v248, s[84:87], s49 offen sc1\n\t"
            "s_add_u32 s49, s46, 15360\n\t"
            "buffer_load_dwordx4 v[60:63], v248, s[84:87], s49 offen sc1\n\t"
            "s_add_u32 s49, s46, 21504\n\t"
            "buffer_load_dwordx4 v[84:87], v248, s[84:87], s49 offen sc1\n\t"
            "s_add_u32 s49, s46, 27648\n\t"
            "buffer_load_dwordx4 v[108:111], v248, s[84:87], s49 offen sc1\n\t"
            "s_add_u32 s49, s46, 33792\n\t"
            "buffer_load_dwordx4 v[132:135], v248, s[84:87], s49 offen sc1\n\t"
            "s_add_u32 s49, s46, 4096\n\t"
            "buffer_load_dwordx4 v[16:19], v248, s[84:87], s49 offen sc1\n\t"
            "s_add_u32 s49, s46, 10240\n\t"
            "buffer_load_dwordx4 v[40:43], v248, s[84:87], s49 offen sc1\n\t"
            "s_add_u32 s49, s46, 16384\n\t"
            "buffer_load_dwordx4 v[64:67], v248, s[84:87], s49 offen sc1\n\t"
            "s_add_u32 s49, s46, 22528\n\t"
            "buffer_load_dwordx4 v[88:91], v248, s[84:87], s49 offen sc1\n\t"
            "s_add_u32 s49, s46, 28672\n\t"
            "buffer_load_dwordx4 v[112:115], v248, s[84:87], s49 offen sc1\n\t"
            "s_add_u32 s49, s46, 34816\n\t"
            "buffer_load_dwordx4 v[136:139], v248, s[84:87], s49 offen sc1\n\t"
            "s_add_u32 s49, s46, 5120\n\t"
            "buffer_load_dwordx4 v[20:23], v248, s[84:87], s49 offen sc1\n\t"
            "s_add_u32 s49, s46, 11264\n\t"
            "buffer_load_dwordx4 v[44:47], v248, s[84:87], s49 offen sc1\n\t"
            "s_add_u32 s49, s46, 17408\n\t"
            "buffer_load_dwordx4 v[68:71], v248, s[84:87], s49 offen sc1\n\t"
            "s_add_u32 s49, s46, 23552\n\t"
            "buffer_load_dwordx4 v[92:95], v248, s[84:87], s49 offen sc1\n\t"
            "s_add_u32 s49, s46, 29696\n\t"
            "buffer_load_dwordx4 v[116:119], v248, s[84:87], s49 offen sc1\n\t"
            "s_add_u32 s49, s46, 35840\n\t"
            "buffer_load_dwordx4 v[140:143], v248, s[84:87], s49 offen sc1\n\t"
            "s_waitcnt lgkmcnt(0)\n\t"
            "s_barrier\n\t"
            "s_setprio 2\n\t"
            "v_add_u32 v235, s71, v248\n\t"
            "v_add_u32 v232, s71, v244\n\t"
            "v_xor_b32 v233, 64, v232\n\t"
            "v_subrev_u32 v239, s75, v251\n\t"
            "v_mul_u32_u24 v239, 200, v239\n\t"
            "v_lshrrev_b32 v234, 3, v255\n\t"
            "v_add3_u32 v234, v234, v239, s72\n\t"
            "ds_read_b128 v[228:231], v235 offset:12288\n\t"
            "ds_read_b128 v[144:147], v232\n\t"
            "ds_read_b128 v[148:151], v233\n\t"
            "ds_read_b128 v[204:207], v232 offset:6144\n\t"
            "ds_read_b128 v[208:211], v233 offset:6144\n\t"
            "ds_read_b128 v[152:155], v232 offset:128\n\t"
            "ds_read_b128 v[156:159], v233 offset:128\n\t"
            "ds_read_b128 v[212:215], v232 offset:6272\n\t"
            "ds_read_b128 v[224:227], v233 offset:6272\n\t"
            "ds_read_b128 v[160:163], v232 offset:256\n\t"
            "ds_read_b128 v[164:167], v233 offset:256\n\t"
            "s_waitcnt vmcnt(24)\n\t"
            "s_waitcnt lgkmcnt(10)\n\t"
            "v_add_f32 v236, v228, v230\n\t"
            "v_add_f32 v237, v229, v231\n\t"
            "s_waitcnt lgkmcnt(8)\n\t"
            "v_mfma_f32_16x16x32_bf16 v[168:171], v[0:3], v[144:147], 0\n\t"
            "ds_read_b128 v[228:231], v235 offset:13312\n\t"
            "v_lshrrev_b32 v235, 4, v255\n\t"
            "v_mfma_f32_16x16x32_bf16 v[168:171], v[4:7], v[148:151], v[168:171]\n\t"
            "v_add3_u32 v235, v235, v239, s72\n\t"
            "v_mfma_f32_16x16x32_bf16 v[176:179], v[48:51], v[144:147], 0\n\t"
            "v_mfma_f32_16x16x32_bf16 v[176:179], v[52:55], v[148:151], v[176:179]\n\t"
            "v_mfma_f32_16x16x32_bf16 v[172:175], v[24:27], v[144:147], 0\n\t"
            "v_mfma_f32_16x16x32_bf16 v[172:175], v[28:31], v[148:151], v[172:175]\n\t"
            "v_mfma_f32_16x16x32_bf16 v[180:183], v[72:75], v[144:147], 0\n\t"
            "v_mfma_f32_16x16x32_bf16 v[180:183], v[76:79], v[148:151], v[180:183]\n\t"
            "s_waitcnt lgkmcnt(0)\n\t"
            "v_add_f32 v228, v228, v230\n\t"
            "v_add_f32 v236, v236, v228\n\t"
            "v_mfma_f32_16x16x32_bf16 v[184:187], v[96:99], v[144:147], 0\n\t"
            "v_add_f32 v229, v229, v231\n\t"
            "v_mul_f32 v236, 0x3baaaaab, v236\n\t"
            "v_mfma_f32_16x16x32_bf16 v[184:187], v[100:103], v[148:151], v[184:187]\n\t"
            "v_add_f32 v237, v237, v229\n\t"
            "v_mul_f32_e64 v236, v236, -v236\n\t"
            "v_mfma_f32_16x16x32_bf16 v[188:191], v[120:123], v[144:147], 0\n\t"
            "v_fmac_f32 v236, 0x3baaaaab, v237\n\t"
            "v_max_f32 v236, 0, v236\n\t"
            "v_mfma_f32_16x16x32_bf16 v[188:191], v[124:127], v[148:151], v[188:191]\n\t"
            "v_add_f32 v236, 0x3727c5ac, v236\n\t"
            "v_rsq_f32 v236, v236\n\t"
            "v_mfma_f32_16x16x32_bf16 v[192:195], v[0:3], v[204:207], 0\n\t"
            "v_mfma_f32_16x16x32_bf16 v[192:195], v[4:7], v[208:211], v[192:195]\n\t"
            "ds_write_b32 v249, v236\n\t"
            "v_mfma_f32_16x16x32_bf16 v[196:199], v[24:27], v[204:207], 0\n\t"
            "ds_read_b32 v236, v251\n\t"
            "ds_read_b32 v237, v251 offset:64\n\t"
            "v_mfma_f32_16x16x32_bf16 v[196:199], v[28:31], v[208:211], v[196:199]\n\t"
            "v_mfma_f32_16x16x32_bf16 v[200:203], v[48:51], v[204:207], 0\n\t"
            "v_mfma_f32_16x16x32_bf16 v[200:203], v[52:55], v[208:211], v[200:203]\n\t"
            "v_mfma_f32_16x16x32_bf16 v[216:219], v[72:75], v[204:207], 0\n\t"
            "v_mfma_f32_16x16x32_bf16 v[216:219], v[76:79], v[208:211], v[216:219]\n\t"
            "v_mfma_f32_16x16x32_bf16 v[220:223], v[96:99], v[204:207], 0\n\t"
            "v_mfma_f32_16x16x32_bf16 v[220:223], v[100:103], v[208:211], v[220:223]\n\t"
            "v_mfma_f32_16x16x32_bf16 v[228:231], v[120:123], v[204:207], 0\n\t"
            "ds_read_b128 v[204:207], v232 offset:6400\n\t"
            "v_add_u32 v232, s48, v244\n\t"
            "v_mfma_f32_16x16x32_bf16 v[228:231], v[124:127], v[208:211], v[228:231]\n\t"
            "ds_read_b128 v[208:211], v233 offset:6400\n\t"
            "s_waitcnt vmcnt(12)\n\t"
            "v_mfma_f32_16x16x32_bf16 v[168:171], v[8:11], v[152:155], v[168:171]\n\t"
            "v_xor_b32 v233, 64, v232\n\t"
            "v_mfma_f32_16x16x32_bf16 v[168:171], v[12:15], v[156:159], v[168:171]\n\t"
            "v_mfma_f32_16x16x32_bf16 v[176:179], v[56:59], v[152:155], v[176:179]\n\t"
            "v_mfma_f32_16x16x32_bf16 v[176:179], v[60:63], v[156:159], v[176:179]\n\t"
            "v_mfma_f32_16x16x32_bf16 v[172:175], v[32:35], v[152:155], v[172:175]\n\t"
            "v_mfma_f32_16x16x32_bf16 v[172:175], v[36:39], v[156:159], v[172:175]\n\t"
            "v_mfma_f32_16x16x32_bf16 v[180:183], v[80:83], v[152:155], v[180:183]\n\t"
            "v_mfma_f32_16x16x32_bf16 v[180:183], v[84:87], v[156:159], v[180:183]\n\t"
            "v_mfma_f32_16x16x32_bf16 v[184:187], v[104:107], v[152:155], v[184:187]\n\t"
            "v_mfma_f32_16x16x32_bf16 v[184:187], v[108:111], v[156:159], v[184:187]\n\t"
            "v_mfma_f32_16x16x32_bf16 v[188:191], v[128:131], v[152:155], v[188:191]\n\t"
            "v_mfma_f32_16x16x32_bf16 v[188:191], v[132:135], v[156:159], v[188:191]\n\t"
            "v_mfma_f32_16x16x32_bf16 v[192:195], v[8:11], v[212:215], v[192:195]\n\t"
            "v_mfma_f32_16x16x32_bf16 v[192:195], v[12:15], v[224:227], v[192:195]\n\t"
            "v_mfma_f32_16x16x32_bf16 v[196:199], v[32:35], v[212:215], v[196:199]\n\t"
            "v_mfma_f32_16x16x32_bf16 v[196:199], v[36:39], v[224:227], v[196:199]\n\t"
            "v_mfma_f32_16x16x32_bf16 v[200:203], v[56:59], v[212:215], v[200:203]\n\t"
            "v_mfma_f32_16x16x32_bf16 v[200:203], v[60:63], v[224:227], v[200:203]\n\t"
            "v_mfma_f32_16x16x32_bf16 v[216:219], v[80:83], v[212:215], v[216:219]\n\t"
            "v_mfma_f32_16x16x32_bf16 v[216:219], v[84:87], v[224:227], v[216:219]\n\t"
            "v_mfma_f32_16x16x32_bf16 v[220:223], v[104:107], v[212:215], v[220:223]\n\t"
            "v_mfma_f32_16x16x32_bf16 v[220:223], v[108:111], v[224:227], v[220:223]\n\t"
            "v_mfma_f32_16x16x32_bf16 v[228:231], v[128:131], v[212:215], v[228:231]\n\t"
            "ds_read_b128 v[212:215], v232 offset:256\n\t"
            "v_mfma_f32_16x16x32_bf16 v[228:231], v[132:135], v[224:227], v[228:231]\n\t"
            "ds_read_b128 v[224:227], v233 offset:256\n\t"
            "s_waitcnt vmcnt(0)\n\t"
            "v_mfma_f32_16x16x32_bf16 v[168:171], v[16:19], v[160:163], v[168:171]\n\t"
            "v_mfma_f32_16x16x32_bf16 v[168:171], v[20:23], v[164:167], v[168:171]\n\t"
            "v_mfma_f32_16x16x32_bf16 v[176:179], v[64:67], v[160:163], v[176:179]\n\t"
            "v_mfma_f32_16x16x32_bf16 v[176:179], v[68:71], v[164:167], v[176:179]\n\t"
            "v_mfma_f32_16x16x32_bf16 v[180:183], v[88:91], v[160:163], v[180:183]\n\t"
            "v_mfma_f32_16x16x32_bf16 v[180:183], v[92:95], v[164:167], v[180:183]\n\t"
            "v_mfma_f32_16x16x32_bf16 v[172:175], v[40:43], v[160:163], v[172:175]\n\t"
            "v_mfma_f32_16x16x32_bf16 v[172:175], v[44:47], v[164:167], v[172:175]\n\t"
            "s_waitcnt lgkmcnt(5)\n\t"
            "s_nop 0\n\t"
            "v_mul_f32 v168, v236, v168\n\t"
            "v_mfma_f32_16x16x32_bf16 v[184:187], v[112:115], v[160:163], v[184:187]\n\t"
            "v_mul_f32 v169, v236, v169\n\t"
            "v_mul_f32 v170, v236, v170\n\t"
            "v_mfma_f32_16x16x32_bf16 v[184:187], v[116:119], v[164:167], v[184:187]\n\t"
            "v_mul_f32 v176, v236, v176\n\t"
            "v_mul_f32 v177, v236, v177\n\t"
            "v_mfma_f32_16x16x32_bf16 v[188:191], v[136:139], v[160:163], v[188:191]\n\t"
            "v_mul_f32 v171, v236, v171\n\t"
            "v_mul_f32 v178, v236, v178\n\t"
            "v_mfma_f32_16x16x32_bf16 v[188:191], v[140:143], v[164:167], v[188:191]\n\t"
            "v_mul_f32 v179, v236, v179\n\t"
            "v_cvt_pk_bf16_f32 v168, v168, v169\n\t"
            "s_waitcnt lgkmcnt(2)\n\t"
            "v_mfma_f32_16x16x32_bf16 v[192:195], v[16:19], v[204:207], v[192:195]\n\t"
            "v_cvt_pk_bf16_f32 v176, v176, v177\n\t"
            "v_cvt_pk_bf16_f32 v180, v180, v181\n\t"
            "v_mfma_f32_16x16x32_bf16 v[192:195], v[20:23], v[208:211], v[192:195]\n\t"
            "v_mul_f32 v172, v236, v172\n\t"
            "v_mul_f32 v173, v236, v173\n\t"
            "v_mfma_f32_16x16x32_bf16 v[196:199], v[40:43], v[204:207], v[196:199]\n\t"
            "v_mul_f32 v174, v236, v174\n\t"
            "v_mul_f32 v175, v236, v175\n\t"
            "v_mfma_f32_16x16x32_bf16 v[196:199], v[44:47], v[208:211], v[196:199]\n\t"
            "v_cvt_pk_bf16_f32 v169, v170, v171\n\t"
            "v_cvt_pk_bf16_f32 v177, v178, v179\n\t"
            "v_mfma_f32_16x16x32_bf16 v[200:203], v[64:67], v[204:207], v[200:203]\n\t"
            "v_cvt_pk_bf16_f32 v181, v182, v183\n\t"
            "v_cvt_pk_bf16_f32 v170, v172, v173\n\t"
            "v_mfma_f32_16x16x32_bf16 v[200:203], v[68:71], v[208:211], v[200:203]\n\t"
            "v_cvt_pk_bf16_f32 v171, v174, v175\n\t"
            "v_cvt_pk_bf16_f32 v182, v184, v185\n\t"
            "v_mfma_f32_16x16x32_bf16 v[216:219], v[88:91], v[204:207], v[216:219]\n\t"
            "v_cvt_pk_bf16_f32 v183, v186, v187\n\t"
            "v_cvt_pk_bf16_f32 v178, v188, v189\n\t"
            "v_mfma_f32_16x16x32_bf16 v[216:219], v[92:95], v[208:211], v[216:219]\n\t"
            "v_cvt_pk_bf16_f32 v179, v190, v191\n\t"
            "v_mul_f32 v236, 0x3e553b94, v236\n\t"
            "v_mfma_f32_16x16x32_bf16 v[220:223], v[112:115], v[204:207], v[220:223]\n\t"
            "v_add_u32 v184, s76, v239\n\t"
            "v_add_u32 v185, s74, v239\n\t"
            "v_mfma_f32_16x16x32_bf16 v[220:223], v[116:119], v[208:211], v[220:223]\n\t"
            "s_mov_b64 exec, -1\n\t"
            "ds_write_b128 v234, v[168:171]\n\t"
            "ds_write_b128 v234, v[180:183] offset:64\n\t"
            "ds_write_b64 v235, v[176:177] offset:128\n\t"
            "ds_write_b64 v235, v[178:179] offset:160\n\t"
            "s_mov_b64 exec, -1\n\t"
            "v_mfma_f32_16x16x16_bf16 v[172:175], v[176:177], v[178:179], 0\n\t"
            "v_mul_f32 v192, v237, v192\n\t"
            "v_mul_f32 v193, v237, v193\n\t"
            "v_mul_f32 v200, v237, v200\n\t"
            "v_mfma_f32_16x16x32_bf16 v[228:231], v[136:139], v[204:207], v[228:231]\n\t"
            "ds_read_b128 v[204:207], v232\n\t"
            "v_mul_f32 v201, v237, v201\n\t"
            "v_mfma_f32_16x16x32_bf16 v[228:231], v[140:143], v[208:211], v[228:231]\n\t"
            "ds_read_b128 v[208:211], v233\n\t"
            "v_mul_f32 v194, v237, v194\n\t"
            "v_mfma_f32_16x16x32_bf16 v[172:175], v[168:171], v[180:183], v[172:175]\n\t"
            "v_mul_f32 v195, v237, v195\n\t"
            "v_mul_f32 v202, v237, v202\n\t"
            "v_mul_f32 v203, v237, v203\n\t"
            "v_cvt_pk_bf16_f32 v192, v192, v193\n\t"
            "v_cvt_pk_bf16_f32 v200, v200, v201\n\t"
            "v_cvt_pk_bf16_f32 v216, v216, v217\n\t"
            "v_mul_f32 v196, v237, v196\n\t"
            "v_mul_f32 v197, v237, v197\n\t"
            "v_mul_f32 v198, v237, v198\n\t"
            "v_mul_f32 v199, v237, v199\n\t"
            "v_cvt_pk_bf16_f32 v193, v194, v195\n\t"
            "v_cvt_pk_bf16_f32 v201, v202, v203\n\t"
            "v_cvt_pk_bf16_f32 v217, v218, v219\n\t"
            "v_cvt_pk_bf16_f32 v194, v196, v197\n\t"
            "v_cvt_pk_bf16_f32 v195, v198, v199\n\t"
            "v_cvt_pk_bf16_f32 v218, v220, v221\n\t"
            "v_cvt_pk_bf16_f32 v219, v222, v223\n\t"
            "v_cvt_pk_bf16_f32 v202, v228, v229\n\t"
            "v_cvt_pk_bf16_f32 v203, v230, v231\n\t"
            "v_mul_f32 v237, 0x3e553b94, v237\n\t"
            "v_add_u32 v220, s76, v239\n\t"
            "v_mfma_f32_16x16x16_bf16 v[196:199], v[200:201], v[202:203], 0\n\t"
            "s_and_b32 exec_lo, s56, 0x11111111\n\t"
            "s_and_b32 exec_hi, s57, 0x11111111\n\t"
            "ds_write_b32 v184, v172 offset:0\n\t"
            "s_and_b32 exec_lo, s56, 0x22222222\n\t"
            "s_and_b32 exec_hi, s57, 0x22222222\n\t"
            "ds_write_b32 v184, v173 offset:0\n\t"
            "s_and_b32 exec_lo, s56, 0x44444444\n\t"
            "s_and_b32 exec_hi, s57, 0x44444444\n\t"
            "ds_write_b32 v184, v174 offset:0\n\t"
            "s_and_b32 exec_lo, s56, 0x88888888\n\t"
            "s_and_b32 exec_hi, s57, 0x88888888\n\t"
            "ds_write_b32 v184, v175 offset:0\n\t"
            "s_mov_b64 exec, s[56:57]\n\t"
            "ds_write_b32 v185, v236 offset:0\n\t"
            "s_mov_b64 exec, -1\n\t"
            "s_waitcnt lgkmcnt(5)\n\t"
            "v_mfma_f32_16x16x32_bf16 v[144:147], v[0:3], v[204:207], 0\n\t"
            "v_add_u32 v221, s74, v239\n\t"
            "v_mfma_f32_16x16x32_bf16 v[144:147], v[4:7], v[208:211], v[144:147]\n\t"
            "s_mov_b64 exec, s[54:55]\n\t"
            "ds_write_b128 v234, v[192:195] offset:12800\n\t"
            "ds_write_b128 v234, v[216:219] offset:12864\n\t"
            "ds_write_b64 v235, v[200:201] offset:12928\n\t"
            "ds_write_b64 v235, v[202:203] offset:12960\n\t"
            "s_mov_b64 exec, -1\n\t"
            "v_mfma_f32_16x16x32_bf16 v[148:151], v[28:31], v[208:211], 0\n\t"
            "v_add_u32 v235, s48, v248\n\t"
            "v_mfma_f32_16x16x32_bf16 v[148:151], v[24:27], v[204:207], v[148:151]\n\t"
            "ds_read_b128 v[228:231], v235 offset:18816\n\t"
            "v_mfma_f32_16x16x32_bf16 v[152:155], v[48:51], v[204:207], 0\n\t"
            "v_mfma_f32_16x16x32_bf16 v[152:155], v[52:55], v[208:211], v[152:155]\n\t"
            "v_mfma_f32_16x16x32_bf16 v[156:159], v[76:79], v[208:211], 0\n\t"
            "v_mfma_f32_16x16x32_bf16 v[156:159], v[72:75], v[204:207], v[156:159]\n\t"
            "v_mfma_f32_16x16x32_bf16 v[160:163], v[96:99], v[204:207], 0\n\t"
            "v_mfma_f32_16x16x32_bf16 v[160:163], v[100:103], v[208:211], v[160:163]\n\t"
            "v_mfma_f32_16x16x32_bf16 v[164:167], v[124:127], v[208:211], 0\n\t"
            "ds_read_b128 v[208:211], v233 offset:128\n\t"
            "s_waitcnt lgkmcnt(1)\n\t"
            "v_add_f32 v236, v228, v230\n\t"
            "v_mfma_f32_16x16x32_bf16 v[164:167], v[120:123], v[204:207], v[164:167]\n\t"
            "ds_read_b128 v[204:207], v232 offset:128\n\t"
            "v_mfma_f32_16x16x32_bf16 v[196:199], v[192:195], v[216:219], v[196:199]\n\t"
            "s_nop 7\n\t"
            "s_and_b32 exec_lo, s78, 0x11111111\n\t"
            "s_and_b32 exec_hi, s79, 0x11111111\n\t"
            "ds_write_b32 v220, v196 offset:12800\n\t"
            "s_and_b32 exec_lo, s78, 0x22222222\n\t"
            "s_and_b32 exec_hi, s79, 0x22222222\n\t"
            "ds_write_b32 v220, v197 offset:12800\n\t"
            "s_and_b32 exec_lo, s78, 0x44444444\n\t"
            "s_and_b32 exec_hi, s79, 0x44444444\n\t"
            "ds_write_b32 v220, v198 offset:12800\n\t"
            "s_and_b32 exec_lo, s78, 0x88888888\n\t"
            "s_and_b32 exec_hi, s79, 0x88888888\n\t"
            "ds_write_b32 v220, v199 offset:12800\n\t"
            "s_mov_b64 exec, s[78:79]\n\t"
            "ds_write_b32 v221, v237 offset:12800\n\t"
            "s_mov_b64 exec, -1\n\t"
            "s_waitcnt lgkmcnt(5)\n\t"
            "v_mfma_f32_16x16x32_bf16 v[144:147], v[8:11], v[204:207], v[144:147]\n\t"
            "v_add_f32 v237, v229, v231\n\t"
            "v_mfma_f32_16x16x32_bf16 v[144:147], v[12:15], v[208:211], v[144:147]\n\t"
            "ds_read_b128 v[228:231], v235 offset:19840\n\t"
            "v_mfma_f32_16x16x32_bf16 v[148:151], v[36:39], v[208:211], v[148:151]\n\t"
            "v_mfma_f32_16x16x32_bf16 v[148:151], v[32:35], v[204:207], v[148:151]\n\t"
            "v_mfma_f32_16x16x32_bf16 v[152:155], v[56:59], v[204:207], v[152:155]\n\t"
            "v_mfma_f32_16x16x32_bf16 v[152:155], v[60:63], v[208:211], v[152:155]\n\t"
            "v_mfma_f32_16x16x32_bf16 v[156:159], v[84:87], v[208:211], v[156:159]\n\t"
            "v_mfma_f32_16x16x32_bf16 v[156:159], v[80:83], v[204:207], v[156:159]\n\t"
            "v_mfma_f32_16x16x32_bf16 v[160:163], v[104:107], v[204:207], v[160:163]\n\t"
            "s_waitcnt lgkmcnt(0)\n\t"
            "v_add_f32 v228, v228, v230\n\t"
            "v_add_f32 v236, v236, v228\n\t"
            "v_mfma_f32_16x16x32_bf16 v[160:163], v[108:111], v[208:211], v[160:163]\n\t"
            "v_add_f32 v229, v229, v231\n\t"
            "v_mul_f32 v236, 0x3baaaaab, v236\n\t"
            "v_mfma_f32_16x16x32_bf16 v[164:167], v[132:135], v[208:211], v[164:167]\n\t"
            "ds_read_b128 v[208:211], v233 offset:6144\n\t"
            "v_add_f32 v237, v237, v229\n\t"
            "v_mfma_f32_16x16x32_bf16 v[164:167], v[128:131], v[204:207], v[164:167]\n\t"
            "ds_read_b128 v[204:207], v232 offset:6144\n\t"
            "v_mul_f32_e64 v236, v236, -v236\n\t"
            "v_mfma_f32_16x16x32_bf16 v[144:147], v[16:19], v[212:215], v[144:147]\n\t"
            "v_fmac_f32 v236, 0x3baaaaab, v237\n\t"
            "v_max_f32 v236, 0, v236\n\t"
            "v_mfma_f32_16x16x32_bf16 v[144:147], v[20:23], v[224:227], v[144:147]\n\t"
            "v_add_f32 v236, 0x3727c5ac, v236\n\t"
            "v_rsq_f32 v236, v236\n\t"
            "v_mfma_f32_16x16x32_bf16 v[148:151], v[44:47], v[224:227], v[148:151]\n\t"
            "v_mfma_f32_16x16x32_bf16 v[148:151], v[40:43], v[212:215], v[148:151]\n\t"
            "ds_write_b32 v249, v236\n\t"
            "v_mfma_f32_16x16x32_bf16 v[152:155], v[64:67], v[212:215], v[152:155]\n\t"
            "ds_read_b32 v236, v251\n\t"
            "ds_read_b32 v237, v251 offset:64\n\t"
            "v_mfma_f32_16x16x32_bf16 v[152:155], v[68:71], v[224:227], v[152:155]\n\t"
            "ds_read_b32 v238, v251 offset:128\n\t"
            "v_mfma_f32_16x16x32_bf16 v[156:159], v[92:95], v[224:227], v[156:159]\n\t"
            "v_mfma_f32_16x16x32_bf16 v[156:159], v[88:91], v[212:215], v[156:159]\n\t"
            "v_mfma_f32_16x16x32_bf16 v[160:163], v[112:115], v[212:215], v[160:163]\n\t"
            "v_mfma_f32_16x16x32_bf16 v[160:163], v[116:119], v[224:227], v[160:163]\n\t"
            "v_mfma_f32_16x16x32_bf16 v[164:167], v[140:143], v[224:227], v[164:167]\n\t"
            "ds_read_b128 v[224:227], v233 offset:6272\n\t"
            "v_mfma_f32_16x16x32_bf16 v[164:167], v[136:139], v[212:215], v[164:167]\n\t"
            "ds_read_b128 v[212:215], v232 offset:6272\n\t"
            "s_waitcnt lgkmcnt(4)\n\t"
            "v_mul_f32 v144, v236, v144\n\t"
            "v_mfma_f32_16x16x32_bf16 v[168:171], v[0:3], v[204:207], 0\n\t"
            "v_mul_f32 v145, v236, v145\n\t"
            "v_mul_f32 v152, v236, v152\n\t"
            "v_mfma_f32_16x16x32_bf16 v[168:171], v[4:7], v[208:211], v[168:171]\n\t"
            "v_mul_f32 v153, v236, v153\n\t"
            "v_mul_f32 v146, v236, v146\n\t"
            "v_mfma_f32_16x16x32_bf16 v[172:175], v[28:31], v[208:211], 0\n\t"
            "v_mul_f32 v147, v236, v147\n\t"
            "v_mul_f32 v154, v236, v154\n\t"
            "v_mfma_f32_16x16x32_bf16 v[172:175], v[24:27], v[204:207], v[172:175]\n\t"
            "v_mul_f32 v155, v236, v155\n\t"
            "v_cvt_pk_bf16_f32 v144, v144, v145\n\t"
            "v_mfma_f32_16x16x32_bf16 v[176:179], v[48:51], v[204:207], 0\n\t"
            "v_cvt_pk_bf16_f32 v152, v152, v153\n\t"
            "v_cvt_pk_bf16_f32 v156, v156, v157\n\t"
            "v_mfma_f32_16x16x32_bf16 v[176:179], v[52:55], v[208:211], v[176:179]\n\t"
            "v_mul_f32 v148, v236, v148\n\t"
            "v_mul_f32 v149, v236, v149\n\t"
            "v_mfma_f32_16x16x32_bf16 v[180:183], v[76:79], v[208:211], 0\n\t"
            "v_mul_f32 v150, v236, v150\n\t"
            "v_mul_f32 v151, v236, v151\n\t"
            "v_mfma_f32_16x16x32_bf16 v[180:183], v[72:75], v[204:207], v[180:183]\n\t"
            "v_cvt_pk_bf16_f32 v145, v146, v147\n\t"
            "v_cvt_pk_bf16_f32 v153, v154, v155\n\t"
            "v_mfma_f32_16x16x32_bf16 v[184:187], v[96:99], v[204:207], 0\n\t"
            "v_cvt_pk_bf16_f32 v157, v158, v159\n\t"
            "v_cvt_pk_bf16_f32 v146, v148, v149\n\t"
            "v_mfma_f32_16x16x32_bf16 v[184:187], v[100:103], v[208:211], v[184:187]\n\t"
            "v_cvt_pk_bf16_f32 v147, v150, v151\n\t"
            "v_cvt_pk_bf16_f32 v158, v160, v161\n\t"
            "v_mfma_f32_16x16x32_bf16 v[188:191], v[124:127], v[208:211], 0\n\t"
            "ds_read_b128 v[208:211], v233 offset:6400\n\t"
            "v_cvt_pk_bf16_f32 v159, v162, v163\n\t"
            "v_mfma_f32_16x16x32_bf16 v[188:191], v[120:123], v[204:207], v[188:191]\n\t"
            "ds_read_b128 v[204:207], v232 offset:6400\n\t"
            "v_cvt_pk_bf16_f32 v154, v164, v165\n\t"
            "s_waitcnt lgkmcnt(2)\n\t"
            "v_mfma_f32_16x16x32_bf16 v[168:171], v[8:11], v[212:215], v[168:171]\n\t"
            "v_cvt_pk_bf16_f32 v155, v166, v167\n\t"
            "v_mul_f32 v236, 0x3e553b94, v236\n\t"
            "v_mfma_f32_16x16x32_bf16 v[168:171], v[12:15], v[224:227], v[168:171]\n\t"
            "v_mfma_f32_16x16x32_bf16 v[172:175], v[36:39], v[224:227], v[172:175]\n\t"
            "v_mfma_f32_16x16x32_bf16 v[172:175], v[32:35], v[212:215], v[172:175]\n\t"
            "v_mfma_f32_16x16x32_bf16 v[176:179], v[56:59], v[212:215], v[176:179]\n\t"
            "v_mfma_f32_16x16x32_bf16 v[176:179], v[60:63], v[224:227], v[176:179]\n\t"
            "v_mfma_f32_16x16x32_bf16 v[180:183], v[84:87], v[224:227], v[180:183]\n\t"
            "v_mfma_f32_16x16x32_bf16 v[180:183], v[80:83], v[212:215], v[180:183]\n\t"
            "v_mfma_f32_16x16x32_bf16 v[184:187], v[104:107], v[212:215], v[184:187]\n\t"
            "v_mfma_f32_16x16x32_bf16 v[184:187], v[108:111], v[224:227], v[184:187]\n\t"
            "v_mfma_f32_16x16x32_bf16 v[188:191], v[132:135], v[224:227], v[188:191]\n\t"
            "ds_read_b128 v[224:227], v233 offset:12288\n\t"
            "v_mfma_f32_16x16x32_bf16 v[188:191], v[128:131], v[212:215], v[188:191]\n\t"
            "ds_read_b128 v[212:215], v232 offset:12288\n\t"
            "s_waitcnt lgkmcnt(2)\n\t"
            "v_mfma_f32_16x16x32_bf16 v[168:171], v[16:19], v[204:207], v[168:171]\n\t"
            "v_mfma_f32_16x16x32_bf16 v[168:171], v[20:23], v[208:211], v[168:171]\n\t"
            "v_mfma_f32_16x16x32_bf16 v[172:175], v[44:47], v[208:211], v[172:175]\n\t"
            "v_mfma_f32_16x16x32_bf16 v[172:175], v[40:43], v[204:207], v[172:175]\n\t"
            "v_mfma_f32_16x16x32_bf16 v[176:179], v[64:67], v[204:207], v[176:179]\n\t"
            "v_mfma_f32_16x16x32_bf16 v[176:179], v[68:71], v[208:211], v[176:179]\n\t"
            "v_mfma_f32_16x16x32_bf16 v[180:183], v[92:95], v[208:211], v[180:183]\n\t"
            "v_mfma_f32_16x16x32_bf16 v[180:183], v[88:91], v[204:207], v[180:183]\n\t"
            "v_mfma_f32_16x16x32_bf16 v[184:187], v[112:115], v[204:207], v[184:187]\n\t"
            "v_mfma_f32_16x16x32_bf16 v[184:187], v[116:119], v[208:211], v[184:187]\n\t"
            "v_mfma_f32_16x16x32_bf16 v[188:191], v[140:143], v[208:211], v[188:191]\n\t"
            "ds_read_b128 v[208:211], v233 offset:12416\n\t"
            "v_mfma_f32_16x16x32_bf16 v[188:191], v[136:139], v[204:207], v[188:191]\n\t"
            "ds_read_b128 v[204:207], v232 offset:12416\n\t"
            "s_waitcnt lgkmcnt(0)\n\t"
            "v_subrev_u32 v249, s75, v251\n\t"
            "v_add_u32 v249, 162816, v249\n\t"
            "s_mov_b32 s90, 2\n\t"
            "s_mov_b32 s81, 0\n\t"
            "s_mov_b32 s91, 0\n\t"
            "s_nop 7\n\t"
            ".p2align 6\n\t"
            "s_nop 0\n\t"
            "s_nop 0\n\t"
            "s_nop 0\n\t"
            ".Lwa_loop%=:\n\t"
            "s_setprio 2\n\t"
            "s_waitcnt lgkmcnt(2)\n\t"
            "v_mfma_f32_16x16x32_bf16 v[192:195], v[0:3], v[212:215], 0\n\t"
            "s_bfe_u32 s49, s70, 0x30001\n\t"
            "s_lshl_b32 s49, s49, 4\n\t"
            "s_or_b32 s73, s77, s49\n\t"
            "v_mfma_f32_16x16x32_bf16 v[192:195], v[4:7], v[224:227], v[192:195]\n\t"
            "v_mad_u32_u24 v220, v246, s72, v253\n\t"
            "v_mad_u32_u24 v216, v246, s72, v252\n\t"
            "v_mfma_f32_16x16x32_bf16 v[148:151], v[28:31], v[224:227], 0\n\t"
            "ds_read_b64 v[222:223], v220 offset:128\n\t"
            "v_mov_b32_dpp v221, v220 quad_perm:[1,0,2,3] row_mask:0xf bank_mask:0xf\n\t"
            "v_mfma_f32_16x16x32_bf16 v[148:151], v[24:27], v[212:215], v[148:151]\n\t"
            "ds_read_b64 v[220:221], v221 offset:160\n\t"
            "v_mov_b32_dpp v217, v216 quad_perm:[1,0,2,3] row_mask:0xf bank_mask:0xf\n\t"
            "v_mfma_f32_16x16x32_bf16 v[196:199], v[48:51], v[212:215], 0\n\t"
            "v_mul_f32 v176, v237, v176\n\t"
            "v_mul_f32 v177, v237, v177\n\t"
            "v_mfma_f32_16x16x32_bf16 v[196:199], v[52:55], v[224:227], v[196:199]\n\t"
            "v_mul_f32 v178, v237, v178\n\t"
            "v_mul_f32 v179, v237, v179\n\t"
            "v_mfma_f32_16x16x32_bf16 v[200:203], v[76:79], v[224:227], 0\n\t"
            "v_cvt_pk_bf16_f32 v176, v176, v177\n\t"
            "v_mul_f32 v168, v237, v168\n\t"
            "v_mfma_f32_16x16x32_bf16 v[200:203], v[72:75], v[212:215], v[200:203]\n\t"
            "v_mul_f32 v169, v237, v169\n\t"
            "v_cvt_pk_bf16_f32 v177, v178, v179\n\t"
            "v_mfma_f32_16x16x32_bf16 v[160:163], v[96:99], v[212:215], 0\n\t"
            "v_mul_f32 v170, v237, v170\n\t"
            "v_mul_f32 v171, v237, v171\n\t"
            "v_mfma_f32_16x16x32_bf16 v[160:163], v[100:103], v[224:227], v[160:163]\n\t"
            "v_cvt_pk_bf16_f32 v168, v168, v169\n\t"
            "v_cvt_pk_bf16_f32 v180, v180, v181\n\t"
            "v_mfma_f32_16x16x32_bf16 v[164:167], v[124:127], v[224:227], 0\n\t"
            "ds_read_b128 v[224:227], v233 offset:12544\n\t"
            "v_cvt_pk_bf16_f32 v178, v188, v189\n\t"
            "v_mfma_f32_16x16x32_bf16 v[164:167], v[120:123], v[212:215], v[164:167]\n\t"
            "ds_read_b128 v[212:215], v232 offset:12544\n\t"
            "v_cvt_pk_bf16_f32 v179, v190, v191\n\t"
            "s_waitcnt lgkmcnt(4)\n\t"
            "v_mfma_f32_16x16x32_bf16 v[192:195], v[8:11], v[204:207], v[192:195]\n\t"
            "v_mul_f32 v172, v237, v172\n\t"
            "v_mul_f32 v173, v237, v173\n\t"
            "v_mfma_f32_16x16x32_bf16 v[192:195], v[12:15], v[208:211], v[192:195]\n\t"
            "v_mul_f32 v174, v237, v174\n\t"
            "v_mul_f32 v175, v237, v175\n\t"
            "v_mfma_f32_16x16x32_bf16 v[196:199], v[56:59], v[204:207], v[196:199]\n\t"
            "v_cvt_pk_bf16_f32 v169, v170, v171\n\t"
            "v_cvt_pk_bf16_f32 v181, v182, v183\n\t"
            "v_mfma_f32_16x16x32_bf16 v[196:199], v[60:63], v[208:211], v[196:199]\n\t"
            "v_cvt_pk_bf16_f32 v170, v172, v173\n\t"
            "v_cvt_pk_bf16_f32 v171, v174, v175\n\t"
            "v_mfma_f32_16x16x32_bf16 v[160:163], v[104:107], v[204:207], v[160:163]\n\t"
            "v_cvt_pk_bf16_f32 v182, v184, v185\n\t"
            "v_cvt_pk_bf16_f32 v183, v186, v187\n\t"
            "v_mfma_f32_16x16x32_bf16 v[160:163], v[108:111], v[208:211], v[160:163]\n\t"
            "v_mov_b32 v230, s76\n\t"
            "v_mov_b32 v239, s74\n\t"
            "v_mfma_f32_16x16x32_bf16 v[148:151], v[36:39], v[208:211], v[148:151]\n\t"
            "s_add_u32 s70, s70, 1\n\t"
            "s_add_u32 s72, s72, 800\n\t"
            "s_add_u32 s74, s74, 800\n\t"
            "s_add_u32 s76, s76, 800\n\t"
            "v_mfma_f32_16x16x32_bf16 v[148:151], v[32:35], v[204:207], v[148:151]\n\t"
            "s_add_u32 s77, s77, 384\n\t"
            "s_xor_b32 s48, s48, s47\n\t"
            "ds_read_b32 v230, v230\n\t"
            "v_mfma_f32_16x16x32_bf16 v[200:203], v[84:87], v[208:211], v[200:203]\n\t"
            "ds_read_b32 v239, v239\n\t"
            "v_mul_f32 v237, 0x3e553b94, v237\n\t"
            "v_mfma_f32_16x16x32_bf16 v[200:203], v[80:83], v[204:207], v[200:203]\n\t"
            "v_mfma_f32_16x16x32_bf16 v[164:167], v[132:135], v[208:211], v[164:167]\n\t"
            "v_mfma_f32_16x16x32_bf16 v[164:167], v[128:131], v[204:207], v[164:167]\n\t"
            "s_waitcnt lgkmcnt(2)\n\t"
            "v_mfma_f32_16x16x32_bf16 v[192:195], v[16:19], v[212:215], v[192:195]\n\t"
            "v_mfma_f32_16x16x32_bf16 v[192:195], v[20:23], v[224:227], v[192:195]\n\t"
            "v_mfma_f32_16x16x32_bf16 v[196:199], v[64:67], v[212:215], v[196:199]\n\t"
            "v_mfma_f32_16x16x32_bf16 v[196:199], v[68:71], v[224:227], v[196:199]\n\t"
            "v_mfma_f32_16x16x32_bf16 v[160:163], v[112:115], v[212:215], v[160:163]\n\t"
            "v_mfma_f32_16x16x32_bf16 v[160:163], v[116:119], v[224:227], v[160:163]\n\t"
            "v_mfma_f32_16x16x32_bf16 v[148:151], v[44:47], v[224:227], v[148:151]\n\t"
            "v_mfma_f32_16x16x32_bf16 v[148:151], v[40:43], v[212:215], v[148:151]\n\t"
            "s_nop 1\n\t"
            "v_mul_f32 v192, v238, v192\n\t"
            "v_mfma_f32_16x16x32_bf16 v[200:203], v[92:95], v[224:227], v[200:203]\n\t"
            "v_mul_f32 v193, v238, v193\n\t"
            "v_mul_f32 v194, v238, v194\n\t"
            "v_mfma_f32_16x16x32_bf16 v[200:203], v[88:91], v[212:215], v[200:203]\n\t"
            "v_mul_f32 v196, v238, v196\n\t"
            "v_mul_f32 v197, v238, v197\n\t"
            "v_mfma_f32_16x16x32_bf16 v[164:167], v[140:143], v[224:227], v[164:167]\n\t"
            "ds_read_b128 v[224:227], v217 offset:64\n\t"
            "ds_read_b128 v[216:219], v216\n\t"
            "v_mfma_f32_16x16x32_bf16 v[164:167], v[136:139], v[212:215], v[164:167]\n\t"
            "v_mul_f32 v198, v238, v198\n\t"
            "v_mul_f32 v199, v238, v199\n\t"
            "v_mfma_f32_16x16x32_bf16 v[172:175], v[220:223], v[152:155], 0\n\t"
            "s_setprio 0\n\t"
            "v_cvt_pk_bf16_f32 v196, v196, v197\n\t"
            "v_mul_f32 v195, v238, v195\n\t"
            "s_setprio 3\n\t"
            "v_mfma_f32_16x16x32_bf16 v[212:215], v[220:223], v[176:179], 0\n\t"
            "v_cvt_pk_bf16_f32 v192, v192, v193\n\t"
            "v_cvt_pk_bf16_f32 v197, v198, v199\n\t"
            "v_cvt_pk_bf16_f32 v200, v200, v201\n\t"
            "v_mul_f32 v148, v238, v148\n\t"
            "v_mfma_f32_16x16x16_bf16 v[184:187], v[152:153], v[178:179], 0\n\t"
            "v_mul_f32 v149, v238, v149\n\t"
            "v_mul_f32 v150, v238, v150\n\t"
            "v_mul_f32 v151, v238, v151\n\t"
            "v_mfma_f32_16x16x16_bf16 v[204:207], v[176:177], v[178:179], 0\n\t"
            "v_cvt_pk_bf16_f32 v193, v194, v195\n\t"
            "v_cvt_pk_bf16_f32 v201, v202, v203\n\t"
            "v_cvt_pk_bf16_f32 v194, v148, v149\n\t"
            "v_mfma_f32_16x16x16_bf16 v[188:191], v[196:197], v[178:179], 0\n\t"
            "v_cvt_pk_bf16_f32 v195, v150, v151\n\t"
            "v_cvt_pk_bf16_f32 v202, v160, v161\n\t"
            "v_cvt_pk_bf16_f32 v203, v162, v163\n\t"
            "s_waitcnt lgkmcnt(0)\n\t"
            "v_mfma_f32_16x16x32_bf16 v[172:175], v[216:219], v[156:159], v[172:175]\n\t"
            "v_cvt_pk_bf16_f32 v198, v164, v165\n\t"
            "v_cvt_pk_bf16_f32 v199, v166, v167\n\t"
            "v_mfma_f32_16x16x32_bf16 v[172:175], v[224:227], v[144:147], v[172:175]\n\t"
            "v_mul_f32 v238, 0x3e553b94, v238\n\t"
            "v_mfma_f32_16x16x16_bf16 v[148:151], v[152:153], v[154:155], 0\n\t"
            "v_mfma_f32_16x16x16_bf16 v[160:163], v[176:177], v[154:155], 0\n\t"
            "v_mfma_f32_16x16x16_bf16 v[164:167], v[196:197], v[154:155], 0\n\t"
            "v_mfma_f32_16x16x32_bf16 v[212:215], v[216:219], v[180:183], v[212:215]\n\t"
            "v_mfma_f32_16x16x32_bf16 v[212:215], v[224:227], v[168:171], v[212:215]\n\t"
            "s_nop 1\n\t"
            "v_mfma_f32_16x16x32_bf16 v[148:151], v[144:147], v[156:159], v[148:151]\n\t"
            "v_mfma_f32_16x16x32_bf16 v[184:187], v[144:147], v[180:183], v[184:187]\n\t"
            "v_mfma_f32_16x16x32_bf16 v[160:163], v[168:171], v[156:159], v[160:163]\n\t"
            "v_mfma_f32_16x16x32_bf16 v[204:207], v[168:171], v[180:183], v[204:207]\n\t"
            "v_mfma_f32_16x16x32_bf16 v[164:167], v[192:195], v[156:159], v[164:167]\n\t"
            "v_mfma_f32_16x16x32_bf16 v[188:191], v[192:195], v[180:183], v[188:191]\n\t"
            "v_cndmask_b32_e64 v235, v173, v213, s[60:61]\n\t"
            "v_mfma_f32_16x16x32_bf16 v[208:211], v[220:223], v[196:199], 0\n\t"
            "s_nop 0\n\t"
            "v_max3_f32 v228, v172, v148, v149\n\t"
            "v_max3_f32 v228, v228, v150, v151\n\t"
            "v_mfma_f32_16x16x32_bf16 v[208:211], v[216:219], v[200:203], v[208:211]\n\t"
            "v_max3_f32 v229, v212, v184, v185\n\t"
            "v_max3_f32 v229, v229, v186, v187\n\t"
            "v_mfma_f32_16x16x32_bf16 v[208:211], v[224:227], v[192:195], v[208:211]\n\t"
            "s_setprio 0\n\t"
            "v_max3_f32 v228, v228, v160, v161\n\t"
            "v_max3_f32 v229, v229, v204, v205\n\t"
            "v_max3_f32 v228, v228, v162, v163\n\t"
            "v_max3_f32 v229, v229, v206, v207\n\t"
            "v_max3_f32 v228, v228, v164, v165\n\t"
            "v_max3_f32 v229, v229, v188, v189\n\t"
            "v_max3_f32 v228, v228, v166, v167\n\t"
            "v_max3_f32 v229, v229, v190, v191\n\t"
            "v_max3_f32 v234, v173, v213, v209\n\t"
            "v_max_f32 v234, v234, v230\n\t"
            "v_permlane32_swap_b32 v228, v229\n\t"
            "v_max_f32 v228, v228, v229\n\t"
            "v_mov_b32 v231, v228\n\t"
            "v_max_f32_dpp v234, v234, v234 row_ror:8 row_mask:0xf bank_mask:0xf\n\t"
            "v_cndmask_b32_e64 v235, v235, v209, s[62:63]\n\t"
            "v_permlane16_swap_b32 v228, v231\n\t"
            "v_max_f32 v228, v228, v231\n\t"
            "v_mov_b32 v229, v228\n\t"
            "v_max_f32_dpp v234, v234, v234 row_ror:4 row_mask:0xf bank_mask:0xf\n\t"
            "v_cndmask_b32_e64 v235, v235, v230, s[64:65]\n\t"
            "v_permlane32_swap_b32 v228, v229\n\t"
            "v_mul_f32_e64 v229, -v229, v237\n\t"
            "v_fma_f32 v184, v184, v237, v229\n\t"
            "v_fma_f32 v185, v185, v237, v229\n\t"
            "v_exp_f32 v184, v184\n\t"
            "v_exp_f32 v185, v185\n\t"
            "v_fma_f32 v186, v186, v237, v229\n\t"
            "v_fma_f32 v187, v187, v237, v229\n\t"
            "v_exp_f32 v186, v186\n\t"
            "v_exp_f32 v187, v187\n\t"
            "v_fma_f32 v204, v204, v237, v229\n\t"
            "v_fma_f32 v205, v205, v237, v229\n\t"
            "v_fma_f32 v206, v206, v237, v229\n\t"
            "v_fma_f32 v207, v207, v237, v229\n\t"
            "v_exp_f32 v204, v204\n\t"
            "v_exp_f32 v205, v205\n\t"
            "v_exp_f32 v206, v206\n\t"
            "v_exp_f32 v207, v207\n\t"
            "v_cvt_pk_bf16_f32 v184, v184, v185\n\t"
            "v_mul_f32_e64 v228, -v228, v236\n\t"
            "v_cvt_pk_bf16_f32 v185, v186, v187\n\t"
            "v_fma_f32 v210, v172, v236, v228\n\t"
            "v_cvt_pk_bf16_f32 v186, v204, v205\n\t"
            "v_cvt_pk_bf16_f32 v187, v206, v207\n\t"
            "s_setprio 3\n\t"
            "v_mfma_f32_16x16x16_bf16 v[180:183], v[152:153], v[198:199], 0\n\t"
            "v_max_f32_dpp v234, v234, v234 row_ror:2 row_mask:0xf bank_mask:0xf\n\t"
            "s_mov_b64 exec, s[60:61]\n\t"
            "v_fma_f32 v210, v212, v237, v229\n\t"
            "s_mov_b64 exec, -1\n\t"
            "v_mfma_f32_16x16x16_bf16 v[204:207], v[176:177], v[198:199], 0\n\t"
            "v_max_f32_dpp v234, v234, v234 row_ror:1 row_mask:0xf bank_mask:0xf\n\t"
            "v_sub_f32 v235, v235, v234\n\t"
            "v_mul_f32 v235, v239, v235\n\t"
            "v_mfma_f32_16x16x16_bf16 v[172:175], v[196:197], v[198:199], 0\n\t"
            "v_exp_f32 v235, v235\n\t"
            "v_fma_f32 v148, v148, v236, v228\n\t"
            "v_mfma_f32_16x16x32_bf16 v[180:183], v[144:147], v[200:203], v[180:183]\n\t"
            "v_cvt_pk_bf16_f32 v235, v235, 0\n\t"
            "v_cndmask_b32_e64 v235, 0, v235, s[66:67]\n\t"
            "v_mfma_f32_16x16x32_bf16 v[204:207], v[168:171], v[200:203], v[204:207]\n\t"
            "ds_write_b16 v250, v235 offset:256\n\t"
            "v_mfma_f32_16x16x32_bf16 v[172:175], v[192:195], v[200:203], v[172:175]\n\t"
            "s_setprio 0\n\t"
            "v_fma_f32 v149, v149, v236, v228\n\t"
            "v_fma_f32 v164, v164, v236, v228\n\t"
            "v_fma_f32 v165, v165, v236, v228\n\t"
            "v_max3_f32 v230, v208, v180, v181\n\t"
            "v_max3_f32 v230, v230, v182, v183\n\t"
            "v_exp_f32 v148, v148\n\t"
            "v_max3_f32 v230, v230, v204, v205\n\t"
            "v_max3_f32 v230, v230, v206, v207\n\t"
            "v_max3_f32 v230, v230, v172, v173\n\t"
            "v_max3_f32 v230, v230, v174, v175\n\t"
            "v_mov_b32 v231, v230\n\t"
            "v_exp_f32 v149, v149\n\t"
            "v_fma_f32 v150, v150, v236, v228\n\t"
            "v_permlane32_swap_b32 v230, v231\n\t"
            "v_max_f32 v230, v230, v231\n\t"
            "v_mov_b32 v231, v230\n\t"
            "v_fma_f32 v151, v151, v236, v228\n\t"
            "v_exp_f32 v164, v164\n\t"
            "v_permlane16_swap_b32 v230, v231\n\t"
            "v_max_f32 v230, v230, v231\n\t"
            "v_mul_f32_e64 v230, -v230, v238\n\t"
            "s_mov_b64 exec, s[62:63]\n\t"
            "v_fma_f32 v210, v208, v238, v230\n\t"
            "s_mov_b64 exec, -1\n\t"
            "v_exp_f32 v165, v165\n\t"
            "v_fma_f32 v166, v166, v236, v228\n\t"
            "v_fma_f32 v167, v167, v236, v228\n\t"
            "v_exp_f32 v150, v150\n\t"
            "v_exp_f32 v151, v151\n\t"
            "v_fma_f32 v160, v160, v236, v228\n\t"
            "v_fma_f32 v161, v161, v236, v228\n\t"
            "v_fma_f32 v162, v162, v236, v228\n\t"
            "v_fma_f32 v163, v163, v236, v228\n\t"
            "v_exp_f32 v166, v166\n\t"
            "v_exp_f32 v167, v167\n\t"
            "v_exp_f32 v210, v210\n\t"
            "v_exp_f32 v160, v160\n\t"
            "v_exp_f32 v161, v161\n\t"
            "v_exp_f32 v162, v162\n\t"
            "v_exp_f32 v163, v163\n\t"
            "v_cvt_pk_bf16_f32 v148, v148, v149\n\t"
            "v_cvt_pk_bf16_f32 v164, v164, v165\n\t"
            "v_cvt_pk_bf16_f32 v149, v150, v151\n\t"
            "v_cvt_pk_bf16_f32 v165, v166, v167\n\t"
            "v_cvt_pk_bf16_f32 v210, v210, 0\n\t"
            "v_cvt_pk_bf16_f32 v150, v160, v161\n\t"
            "v_cvt_pk_bf16_f32 v151, v162, v163\n\t"
            "v_mov_b32 v167, 0\n\t"
            "v_cndmask_b32_e64 v166, 0, v210, s[58:59]\n\t"
            "v_fma_f32 v188, v188, v237, v229\n\t"
            "v_fma_f32 v189, v189, v237, v229\n\t"
            "v_fma_f32 v180, v180, v238, v230\n\t"
            "v_fma_f32 v181, v181, v238, v230\n\t"
            "v_fma_f32 v172, v172, v238, v230\n\t"
            "v_fma_f32 v173, v173, v238, v230\n\t"
            "v_exp_f32 v188, v188\n\t"
            "v_exp_f32 v189, v189\n\t"
            "v_fma_f32 v190, v190, v237, v229\n\t"
            "v_fma_f32 v191, v191, v237, v229\n\t"
            "v_exp_f32 v180, v180\n\t"
            "v_exp_f32 v181, v181\n\t"
            "v_fma_f32 v182, v182, v238, v230\n\t"
            "v_fma_f32 v183, v183, v238, v230\n\t"
            "v_exp_f32 v172, v172\n\t"
            "v_exp_f32 v173, v173\n\t"
            "v_fma_f32 v174, v174, v238, v230\n\t"
            "v_fma_f32 v175, v175, v238, v230\n\t"
            "v_exp_f32 v190, v190\n\t"
            "v_exp_f32 v191, v191\n\t"
            "v_exp_f32 v182, v182\n\t"
            "v_exp_f32 v183, v183\n\t"
            "v_fma_f32 v204, v204, v238, v230\n\t"
            "v_fma_f32 v205, v205, v238, v230\n\t"
            "v_fma_f32 v206, v206, v238, v230\n\t"
            "v_fma_f32 v207, v207, v238, v230\n\t"
            "v_exp_f32 v174, v174\n\t"
            "v_exp_f32 v175, v175\n\t"
            "v_exp_f32 v204, v204\n\t"
            "v_exp_f32 v205, v205\n\t"
            "v_exp_f32 v206, v206\n\t"
            "v_exp_f32 v207, v207\n\t"
            "v_cvt_pk_bf16_f32 v188, v188, v189\n\t"
            "v_cvt_pk_bf16_f32 v180, v180, v181\n\t"
            "v_cvt_pk_bf16_f32 v172, v172, v173\n\t"
            "v_cvt_pk_bf16_f32 v189, v190, v191\n\t"
            "v_cvt_pk_bf16_f32 v181, v182, v183\n\t"
            "v_cvt_pk_bf16_f32 v173, v174, v175\n\t"
            "v_cvt_pk_bf16_f32 v182, v204, v205\n\t"
            "v_cvt_pk_bf16_f32 v183, v206, v207\n\t"
            "v_cndmask_b32_e64 v190, 0, v210, s[60:61]\n\t"
            "v_cndmask_b32_e64 v174, 0, v210, s[62:63]\n\t"
            "v_mov_b32 v191, 0\n\t"
            "v_mov_b32 v175, 0\n\t"
            "s_cmp_ge_u32 s70, s80\n\t"
            "s_barrier\n\t"
            "s_setprio 2\n\t"
            "s_cbranch_scc1 .Lwa_h1%=\n\t"
            ".Lwa_r1%=:\n\t"
            "v_mfma_f32_16x16x32_bf16 v[200:203], v[240:243], v[148:151], 0\n\t"
            "ds_read_b64 v[176:177], v247\n\t"
            "ds_read_b64 v[196:197], v247 offset:1536\n\t"
            "v_mfma_f32_16x16x32_bf16 v[200:203], v[240:243], v[164:167], v[200:203]\n\t"
            "ds_read_b64 v[220:221], v247 offset:3072\n\t"
            "v_xor_b32 v234, s73, v254\n\t"
            "v_add_u32 v235, s45, v248\n\t"
            "v_mov_b32 v179, 0\n\t"
            "v_mov_b32 v199, 0\n\t"
            "v_mov_b32 v223, 0\n\t"
            "v_add_u32 v232, s48, v244\n\t"
            "v_xor_b32 v233, 64, v232\n\t"
            "v_rcp_f32 v201, v200\n\t"
            "v_xor_b32 v249, 0x100, v249\n\t"
            "ds_read_b128 v[168:171], v235\n\t"
            "ds_read_u16 v178, v234\n\t"
            "ds_read_b128 v[192:195], v235 offset:1536\n\t"
            "ds_read_u16 v198, v234 offset:128\n\t"
            "ds_read_b128 v[216:219], v235 offset:3072\n\t"
            "ds_read_u16 v222, v234 offset:256\n\t"
            "ds_read_b128 v[204:207], v232\n\t"
            "ds_read_b128 v[208:211], v233\n\t"
            "ds_read_b32 v236, v249\n\t"
            "ds_read_b32 v237, v249 offset:64\n\t"
            "ds_read_b32 v238, v249 offset:128\n\t"
            "v_mad_u32_u24 v234, v246, s75, v253\n\t"
            "s_waitcnt lgkmcnt(9)\n\t"
            "v_mfma_f32_16x16x32_bf16 v[224:227], v[168:171], v[148:151], 0\n\t"
            "v_mfma_f32_16x16x32_bf16 v[224:227], v[176:179], v[164:167], v[224:227]\n\t"
            "s_waitcnt lgkmcnt(7)\n\t"
            "v_mfma_f32_16x16x32_bf16 v[212:215], v[192:195], v[148:151], 0\n\t"
            "v_mfma_f32_16x16x32_bf16 v[212:215], v[196:199], v[164:167], v[212:215]\n\t"
            "s_waitcnt lgkmcnt(5)\n\t"
            "v_mfma_f32_16x16x32_bf16 v[148:151], v[216:219], v[148:151], 0\n\t"
            "v_mfma_f32_16x16x32_bf16 v[148:151], v[220:223], v[164:167], v[148:151]\n\t"
            "s_waitcnt lgkmcnt(3)\n\t"
            "v_mfma_f32_16x16x32_bf16 v[144:147], v[0:3], v[204:207], 0\n\t"
            "v_mfma_f32_16x16x32_bf16 v[144:147], v[4:7], v[208:211], v[144:147]\n\t"
            "v_mul_f32 v224, v201, v224\n\t"
            "v_mfma_f32_16x16x32_bf16 v[152:155], v[48:51], v[204:207], 0\n\t"
            "v_mul_f32 v225, v201, v225\n\t"
            "v_mul_f32 v226, v201, v226\n\t"
            "v_mfma_f32_16x16x32_bf16 v[152:155], v[52:55], v[208:211], v[152:155]\n\t"
            "v_mul_f32 v212, v201, v212\n\t"
            "v_mul_f32 v213, v201, v213\n\t"
            "v_mfma_f32_16x16x32_bf16 v[156:159], v[76:79], v[208:211], 0\n\t"
            "v_mul_f32 v227, v201, v227\n\t"
            "v_cvt_pk_bf16_f32 v224, v224, v225\n\t"
            "v_mfma_f32_16x16x32_bf16 v[156:159], v[72:75], v[204:207], v[156:159]\n\t"
            "v_mul_f32 v214, v201, v214\n\t"
            "v_mul_f32 v215, v201, v215\n\t"
            "v_mfma_f32_16x16x32_bf16 v[160:163], v[96:99], v[204:207], 0\n\t"
            "v_cvt_pk_bf16_f32 v212, v212, v213\n\t"
            "v_cvt_pk_bf16_f32 v225, v226, v227\n\t"
            "v_mfma_f32_16x16x32_bf16 v[160:163], v[100:103], v[208:211], v[160:163]\n\t"
            "v_cvt_pk_bf16_f32 v213, v214, v215\n\t"
            "v_mul_f32 v148, v201, v148\n\t"
            "v_mfma_f32_16x16x32_bf16 v[164:167], v[124:127], v[208:211], 0\n\t"
            "v_mul_f32 v149, v201, v149\n\t"
            "v_mul_f32 v150, v201, v150\n\t"
            "v_mfma_f32_16x16x32_bf16 v[164:167], v[120:123], v[204:207], v[164:167]\n\t"
            "ds_write_b64 v245, v[224:225]\n\t"
            "v_mfma_f32_16x16x32_bf16 v[224:227], v[168:171], v[184:187], 0\n\t"
            "v_mul_f32 v151, v201, v151\n\t"
            "v_cvt_pk_bf16_f32 v148, v148, v149\n\t"
            "v_mfma_f32_16x16x32_bf16 v[224:227], v[176:179], v[188:191], v[224:227]\n\t"
            "ds_write_b64 v245, v[212:213] offset:128\n\t"
            "v_mfma_f32_16x16x32_bf16 v[200:203], v[240:243], v[184:187], 0\n\t"
            "v_cvt_pk_bf16_f32 v149, v150, v151\n\t"
            "v_mfma_f32_16x16x32_bf16 v[200:203], v[240:243], v[188:191], v[200:203]\n\t"
            "ds_write_b64 v245, v[148:149] offset:256\n\t"
            "v_mfma_f32_16x16x32_bf16 v[212:215], v[192:195], v[184:187], 0\n\t"
            "v_mfma_f32_16x16x32_bf16 v[212:215], v[196:199], v[188:191], v[212:215]\n\t"
            "v_mfma_f32_16x16x32_bf16 v[184:187], v[216:219], v[184:187], 0\n\t"
            "v_mfma_f32_16x16x32_bf16 v[184:187], v[220:223], v[188:191], v[184:187]\n\t"
            "v_mfma_f32_16x16x32_bf16 v[148:151], v[28:31], v[208:211], 0\n\t"
            "ds_read_b128 v[208:211], v233 offset:128\n\t"
            "v_mfma_f32_16x16x32_bf16 v[148:151], v[24:27], v[204:207], v[148:151]\n\t"
            "ds_read_b128 v[204:207], v232 offset:128\n\t"
            "v_rcp_f32 v201, v200\n\t"
            "v_mfma_f32_16x16x32_bf16 v[188:191], v[192:195], v[180:183], 0\n\t"
            "v_mul_f32 v224, v201, v224\n\t"
            "v_mul_f32 v225, v201, v225\n\t"
            "v_mfma_f32_16x16x32_bf16 v[188:191], v[196:199], v[172:175], v[188:191]\n\t"
            "v_mul_f32 v226, v201, v226\n\t"
            "v_mul_f32 v227, v201, v227\n\t"
            "v_mul_f32 v212, v201, v212\n\t"
            "v_mul_f32 v213, v201, v213\n\t"
            "v_mul_f32 v214, v201, v214\n\t"
            "v_mul_f32 v215, v201, v215\n\t"
            "v_mul_f32 v184, v201, v184\n\t"
            "v_mul_f32 v185, v201, v185\n\t"
            "v_mul_f32 v186, v201, v186\n\t"
            "v_mul_f32 v187, v201, v187\n\t"
            "v_mfma_f32_16x16x32_bf16 v[200:203], v[240:243], v[180:183], 0\n\t"
            "v_cvt_pk_bf16_f32 v224, v224, v225\n\t"
            "v_cvt_pk_bf16_f32 v225, v226, v227\n\t"
            "v_mfma_f32_16x16x32_bf16 v[200:203], v[240:243], v[172:175], v[200:203]\n\t"
            "ds_write_b64 v245, v[224:225] offset:6144\n\t"
            "v_mfma_f32_16x16x32_bf16 v[224:227], v[168:171], v[180:183], 0\n\t"
            "v_cvt_pk_bf16_f32 v184, v184, v185\n\t"
            "v_cvt_pk_bf16_f32 v185, v186, v187\n\t"
            "v_mfma_f32_16x16x32_bf16 v[224:227], v[176:179], v[172:175], v[224:227]\n\t"
            "ds_read_b64 v[186:187], v234 offset:288\n\t"
            "ds_write_b64 v245, v[184:185] offset:6400\n\t"
            "s_waitcnt lgkmcnt(3)\n\t"
            "v_mfma_f32_16x16x32_bf16 v[144:147], v[8:11], v[204:207], v[144:147]\n\t"
            "ds_read_b64 v[184:185], v234 offset:256\n\t"
            "v_rcp_f32 v201, v200\n\t"
            "v_mfma_f32_16x16x32_bf16 v[144:147], v[12:15], v[208:211], v[144:147]\n\t"
            "v_mul_f32 v188, v201, v188\n\t"
            "v_mul_f32 v189, v201, v189\n\t"
            "v_mfma_f32_16x16x32_bf16 v[148:151], v[36:39], v[208:211], v[148:151]\n\t"
            "v_mul_f32 v190, v201, v190\n\t"
            "v_mul_f32 v191, v201, v191\n\t"
            "v_mfma_f32_16x16x32_bf16 v[148:151], v[32:35], v[204:207], v[148:151]\n\t"
            "v_cvt_pk_bf16_f32 v188, v188, v189\n\t"
            "v_cvt_pk_bf16_f32 v189, v190, v191\n\t"
            "v_mfma_f32_16x16x32_bf16 v[152:155], v[56:59], v[204:207], v[152:155]\n\t"
            "ds_read_b64 v[190:191], v234 offset:352\n\t"
            "v_mul_f32 v224, v201, v224\n\t"
            "v_mfma_f32_16x16x32_bf16 v[152:155], v[60:63], v[208:211], v[152:155]\n\t"
            "ds_write_b64 v245, v[188:189] offset:12416\n\t"
            "v_mfma_f32_16x16x32_bf16 v[156:159], v[84:87], v[208:211], v[156:159]\n\t"
            "ds_read_b64 v[188:189], v234 offset:320\n\t"
            "v_mul_f32 v225, v201, v225\n\t"
            "v_mfma_f32_16x16x32_bf16 v[156:159], v[80:83], v[204:207], v[156:159]\n\t"
            "v_cvt_pk_bf16_f32 v212, v212, v213\n\t"
            "v_mul_f32 v226, v201, v226\n\t"
            "v_mfma_f32_16x16x32_bf16 v[160:163], v[104:107], v[204:207], v[160:163]\n\t"
            "v_mul_f32 v227, v201, v227\n\t"
            "v_cvt_pk_bf16_f32 v224, v224, v225\n\t"
            "v_mfma_f32_16x16x32_bf16 v[160:163], v[108:111], v[208:211], v[160:163]\n\t"
            "v_cvt_pk_bf16_f32 v213, v214, v215\n\t"
            "v_cvt_pk_bf16_f32 v225, v226, v227\n\t"
            "v_mfma_f32_16x16x32_bf16 v[164:167], v[132:135], v[208:211], v[164:167]\n\t"
            "ds_read_b128 v[208:211], v233 offset:6144\n\t"
            "v_mfma_f32_16x16x32_bf16 v[164:167], v[128:131], v[204:207], v[164:167]\n\t"
            "ds_read_b128 v[204:207], v232 offset:6144\n\t"
            "ds_write_b64 v245, v[212:213] offset:6272\n\t"
            "v_mfma_f32_16x16x32_bf16 v[180:183], v[216:219], v[180:183], 0\n\t"
            "ds_read_b128 v[212:215], v232 offset:256\n\t"
            "v_mfma_f32_16x16x32_bf16 v[180:183], v[220:223], v[172:175], v[180:183]\n\t"
            "ds_write_b64 v245, v[224:225] offset:12288\n\t"
            "s_waitcnt lgkmcnt(5)\n\t"
            "v_mfma_f32_16x16x32_bf16 v[168:171], v[184:187], v[168:171], 0\n\t"
            "ds_read_b128 v[224:227], v233 offset:256\n\t"
            "v_mfma_f32_16x16x32_bf16 v[168:171], v[188:191], v[176:179], v[168:171]\n\t"
            "v_mfma_f32_16x16x32_bf16 v[192:195], v[184:187], v[192:195], 0\n\t"
            "v_mfma_f32_16x16x32_bf16 v[192:195], v[188:191], v[196:199], v[192:195]\n\t"
            "s_nop 0\n\t"
            "v_mul_f32 v180, v201, v180\n\t"
            "v_mfma_f32_16x16x32_bf16 v[216:219], v[184:187], v[216:219], 0\n\t"
            "v_mul_f32 v181, v201, v181\n\t"
            "v_mul_f32 v182, v201, v182\n\t"
            "v_mfma_f32_16x16x32_bf16 v[216:219], v[188:191], v[220:223], v[216:219]\n\t"
            "v_mul_f32 v183, v201, v183\n\t"
            "v_cvt_pk_bf16_f32 v180, v180, v181\n\t"
            "v_mfma_f32_16x16x32_bf16 v[200:203], v[240:243], v[184:187], 0\n\t"
            "v_cvt_pk_bf16_f32 v181, v182, v183\n\t"
            "v_mfma_f32_16x16x32_bf16 v[200:203], v[240:243], v[188:191], v[200:203]\n\t"
            "ds_write_b64 v245, v[180:181] offset:12544\n\t"
            "s_waitcnt lgkmcnt(1)\n\t"
            "v_mfma_f32_16x16x32_bf16 v[144:147], v[16:19], v[212:215], v[144:147]\n\t"
            "v_mfma_f32_16x16x32_bf16 v[144:147], v[20:23], v[224:227], v[144:147]\n\t"
            "v_mfma_f32_16x16x32_bf16 v[148:151], v[44:47], v[224:227], v[148:151]\n\t"
            "v_mfma_f32_16x16x32_bf16 v[148:151], v[40:43], v[212:215], v[148:151]\n\t"
            "v_mfma_f32_16x16x32_bf16 v[152:155], v[64:67], v[212:215], v[152:155]\n\t"
            "s_nop 0\n\t"
            "v_cndmask_b32_e64 v203, v168, v192, s[60:61]\n\t"
            "v_mfma_f32_16x16x32_bf16 v[152:155], v[68:71], v[224:227], v[152:155]\n\t"
            "v_rcp_f32 v201, v200\n\t"
            "v_mfma_f32_16x16x32_bf16 v[156:159], v[92:95], v[224:227], v[156:159]\n\t"
            "v_cndmask_b32_e64 v203, v203, v216, s[62:63]\n\t"
            "v_xad_u32 v202, s73, v254, v255\n\t"
            "v_mfma_f32_16x16x32_bf16 v[156:159], v[88:91], v[212:215], v[156:159]\n\t"
            "v_mul_f32_dpp v203, v201, v203 quad_perm:[0,0,0,0] row_mask:0xf bank_mask:0xf\n\t"
            "v_cvt_pk_bf16_f32 v203, v203, 0\n\t"
            "v_mfma_f32_16x16x32_bf16 v[160:163], v[112:115], v[212:215], v[160:163]\n\t"
            "v_mul_f32 v144, v236, v144\n\t"
            "v_mul_f32 v145, v236, v145\n\t"
            "v_mfma_f32_16x16x32_bf16 v[160:163], v[116:119], v[224:227], v[160:163]\n\t"
            "s_mov_b64 exec, s[68:69]\n\t"
            "ds_write_b16 v202, v203\n\t"
            "s_mov_b64 exec, 1\n\t"
            "ds_add_u32 v253, v246 offset:7856\n\t"
            "s_mov_b64 exec, -1\n\t"
            "v_mfma_f32_16x16x32_bf16 v[164:167], v[140:143], v[224:227], v[164:167]\n\t"
            "ds_read_b128 v[224:227], v233 offset:6272\n\t"
            "v_mul_f32 v152, v236, v152\n\t"
            "v_mfma_f32_16x16x32_bf16 v[164:167], v[136:139], v[212:215], v[164:167]\n\t"
            "ds_read_b128 v[212:215], v232 offset:6272\n\t"
            "v_mul_f32 v153, v236, v153\n\t"
            "v_mfma_f32_16x16x32_bf16 v[168:171], v[0:3], v[204:207], 0\n\t"
            "v_mul_f32 v146, v236, v146\n\t"
            "v_mul_f32 v147, v236, v147\n\t"
            "v_mfma_f32_16x16x32_bf16 v[168:171], v[4:7], v[208:211], v[168:171]\n\t"
            "v_mul_f32 v154, v236, v154\n\t"
            "v_mul_f32 v155, v236, v155\n\t"
            "v_mfma_f32_16x16x32_bf16 v[172:175], v[28:31], v[208:211], 0\n\t"
            "v_cvt_pk_bf16_f32 v144, v144, v145\n\t"
            "v_cvt_pk_bf16_f32 v152, v152, v153\n\t"
            "v_mfma_f32_16x16x32_bf16 v[172:175], v[24:27], v[204:207], v[172:175]\n\t"
            "v_cvt_pk_bf16_f32 v156, v156, v157\n\t"
            "v_mul_f32 v148, v236, v148\n\t"
            "v_mfma_f32_16x16x32_bf16 v[176:179], v[48:51], v[204:207], 0\n\t"
            "v_mul_f32 v149, v236, v149\n\t"
            "v_mul_f32 v150, v236, v150\n\t"
            "v_mfma_f32_16x16x32_bf16 v[176:179], v[52:55], v[208:211], v[176:179]\n\t"
            "v_mul_f32 v151, v236, v151\n\t"
            "v_cvt_pk_bf16_f32 v145, v146, v147\n\t"
            "v_mfma_f32_16x16x32_bf16 v[180:183], v[76:79], v[208:211], 0\n\t"
            "v_cvt_pk_bf16_f32 v153, v154, v155\n\t"
            "v_cvt_pk_bf16_f32 v157, v158, v159\n\t"
            "v_mfma_f32_16x16x32_bf16 v[180:183], v[72:75], v[204:207], v[180:183]\n\t"
            "v_cvt_pk_bf16_f32 v146, v148, v149\n\t"
            "v_cvt_pk_bf16_f32 v147, v150, v151\n\t"
            "v_mfma_f32_16x16x32_bf16 v[184:187], v[96:99], v[204:207], 0\n\t"
            "v_cvt_pk_bf16_f32 v158, v160, v161\n\t"
            "v_cvt_pk_bf16_f32 v159, v162, v163\n\t"
            "v_mfma_f32_16x16x32_bf16 v[184:187], v[100:103], v[208:211], v[184:187]\n\t"
            "v_cvt_pk_bf16_f32 v154, v164, v165\n\t"
            "v_cvt_pk_bf16_f32 v155, v166, v167\n\t"
            "v_mfma_f32_16x16x32_bf16 v[188:191], v[124:127], v[208:211], 0\n\t"
            "ds_read_b128 v[208:211], v233 offset:6400\n\t"
            "v_mul_f32 v236, 0x3e553b94, v236\n\t"
            "v_mfma_f32_16x16x32_bf16 v[188:191], v[120:123], v[204:207], v[188:191]\n\t"
            "ds_read_b128 v[204:207], v232 offset:6400\n\t"
            "s_waitcnt lgkmcnt(2)\n\t"
            "v_mfma_f32_16x16x32_bf16 v[168:171], v[8:11], v[212:215], v[168:171]\n\t"
            "v_mfma_f32_16x16x32_bf16 v[168:171], v[12:15], v[224:227], v[168:171]\n\t"
            "v_mfma_f32_16x16x32_bf16 v[172:175], v[36:39], v[224:227], v[172:175]\n\t"
            "v_mfma_f32_16x16x32_bf16 v[172:175], v[32:35], v[212:215], v[172:175]\n\t"
            "v_mfma_f32_16x16x32_bf16 v[176:179], v[56:59], v[212:215], v[176:179]\n\t"
            "v_mfma_f32_16x16x32_bf16 v[176:179], v[60:63], v[224:227], v[176:179]\n\t"
            "v_mfma_f32_16x16x32_bf16 v[180:183], v[84:87], v[224:227], v[180:183]\n\t"
            "v_mfma_f32_16x16x32_bf16 v[180:183], v[80:83], v[212:215], v[180:183]\n\t"
            "v_mfma_f32_16x16x32_bf16 v[184:187], v[104:107], v[212:215], v[184:187]\n\t"
            "v_mfma_f32_16x16x32_bf16 v[184:187], v[108:111], v[224:227], v[184:187]\n\t"
            "v_mfma_f32_16x16x32_bf16 v[188:191], v[132:135], v[224:227], v[188:191]\n\t"
            "ds_read_b128 v[224:227], v233 offset:12288\n\t"
            "v_mfma_f32_16x16x32_bf16 v[188:191], v[128:131], v[212:215], v[188:191]\n\t"
            "ds_read_b128 v[212:215], v232 offset:12288\n\t"
            "s_waitcnt lgkmcnt(2)\n\t"
            "v_mfma_f32_16x16x32_bf16 v[168:171], v[16:19], v[204:207], v[168:171]\n\t"
            "v_mfma_f32_16x16x32_bf16 v[168:171], v[20:23], v[208:211], v[168:171]\n\t"
            "v_mfma_f32_16x16x32_bf16 v[172:175], v[44:47], v[208:211], v[172:175]\n\t"
            "v_mfma_f32_16x16x32_bf16 v[172:175], v[40:43], v[204:207], v[172:175]\n\t"
            "v_mfma_f32_16x16x32_bf16 v[176:179], v[64:67], v[204:207], v[176:179]\n\t"
            "v_mfma_f32_16x16x32_bf16 v[176:179], v[68:71], v[208:211], v[176:179]\n\t"
            "v_mfma_f32_16x16x32_bf16 v[180:183], v[92:95], v[208:211], v[180:183]\n\t"
            "v_mfma_f32_16x16x32_bf16 v[180:183], v[88:91], v[204:207], v[180:183]\n\t"
            "v_mfma_f32_16x16x32_bf16 v[184:187], v[112:115], v[204:207], v[184:187]\n\t"
            "v_mfma_f32_16x16x32_bf16 v[184:187], v[116:119], v[208:211], v[184:187]\n\t"
            "v_mfma_f32_16x16x32_bf16 v[188:191], v[140:143], v[208:211], v[188:191]\n\t"
            "ds_read_b128 v[208:211], v233 offset:12416\n\t"
            "v_mfma_f32_16x16x32_bf16 v[188:191], v[136:139], v[204:207], v[188:191]\n\t"
            "ds_read_b128 v[204:207], v232 offset:12416\n\t"
            "s_cmp_eq_u32 s91, 0\n\t"
            "s_cbranch_scc1 .Lwa_loop%=\n\t"
            "s_mov_b32 s91, 0\n\t"
            "s_waitcnt vmcnt(0)\n\t"
            "s_cmp_eq_u32 s81, 0\n\t"
            "s_cbranch_scc1 .Lwa_h6%=\n\t"
            "s_mov_b32 s89, 0x10000\n\t"
            ".Lwa_h4%=:\n\t"
            "v_readfirstlane_b32 s49, v228\n\t"
            "v_readfirstlane_b32 vcc_lo, v229\n\t"
            "s_xor_b32 s49, s49, s82\n\t"
            "s_xor_b32 vcc_lo, vcc_lo, s83\n\t"
            "s_or_b32 s49, s49, vcc_lo\n\t"
            "s_cbranch_scc0 .Lwa_h5%=\n\t"
            "s_sub_u32 s89, s89, 1\n\t"
            "s_cbranch_scc1 .Lwa_h5%=\n\t"
            "s_sleep 2\n\t"
            "buffer_load_dwordx3 v[228:230], off, s[84:87], s81 offset:1216 sc1\n\t"
            "s_waitcnt vmcnt(0)\n\t"
            "s_branch .Lwa_h4%=\n\t"
            ".Lwa_h5%=:\n\t"
            "s_lshl_b32 s49, s70, 2\n\t"
            "s_add_u32 s49, s49, 163520\n\t"
            "v_mov_b32 v228, s49\n\t"
            "s_mul_i32 s49, s70, 800\n\t"
            "s_add_u32 m0, s49, 113760\n\t"
            "ds_write_b32 v228, v230 offset:12\n\t"
            "s_mov_b32 exec_hi, 0x3ffff\n\t"
            "buffer_load_dwordx4 v248, s[84:87], s81 offen sc1 lds\n\t"
            "s_mul_i32 s49, s70, 384\n\t"
            "s_add_u32 m0, s49, 130176\n\t"
            "s_add_u32 s81, s81, 800\n\t"
            "s_mov_b64 exec, 0xffffff\n\t"
            "buffer_load_dwordx4 v248, s[84:87], s81 offen sc1 lds\n\t"
            "s_mov_b64 exec, -1\n\t"
            "s_mov_b32 s81, 0\n\t"
            "s_waitcnt lgkmcnt(0)\n\t"
            ".Lwa_h6%=:\n\t"
            "s_cmp_eq_u32 s90, 0\n\t"
            "s_cbranch_scc1 .Lwa_loop%=\n\t"
            "v_readfirstlane_b32 s49, v231\n\t"
            "s_sub_u32 s90, s90, 1\n\t"
            "s_cmp_lt_u32 s49, 209\n\t"
            "s_cselect_b32 s90, s90, 0\n\t"
            "s_cbranch_scc0 .Lwa_loop%=\n\t"
            "s_mul_i32 s81, s49, 1280\n\t"
            "s_add_u32 s81, s81, 303104\n\t"
            "s_branch .Lwa_loop%=\n\t"
            ".Lwa_h1%=:\n\t"
            "s_cmp_lt_u32 s70, 20\n\t"
            "s_cbranch_scc1 .Lwa_h2%=\n\t"
            "s_lshl_b32 s49, s70, 2\n\t"
            "s_add_u32 s49, s49, 163520\n\t"
            "v_mov_b32 v228, s49\n\t"
            "ds_read_b32 v228, v228\n\t"
            "s_waitcnt lgkmcnt(0)\n\t"
            "v_readfirstlane_b32 s49, v228\n\t"
            "s_cmp_eq_u32 s49, 0x7ffffff\n\t"
            "s_cbranch_scc1 .Lwa_last%=\n\t"
            "s_branch .Lwa_r1%=\n\t"
            ".Lwa_h2%=:\n\t"
            "s_waitcnt vmcnt(0)\n\t"
            "s_or_b32 s91, s81, s90\n\t"
            "s_cmp_eq_u32 s81, 0\n\t"
            "s_cbranch_scc1 .Lwa_h3%=\n\t"
            "buffer_load_dwordx3 v[228:230], off, s[84:87], s81 offset:1216 sc1\n\t"
            ".Lwa_h3%=:\n\t"
            "s_cmp_eq_u32 s90, 0\n\t"
            "s_cbranch_scc1 .Lwa_r1%=\n\t"
            "v_mov_b32 v231, 1\n\t"
            "s_mov_b32 s49, 301184\n\t"
            "s_mov_b64 exec, 1\n\t"
            "buffer_atomic_add v231, off, s[84:87], s49 sc0\n\t"
            "s_mov_b64 exec, -1\n\t"
            "s_branch .Lwa_r1%=\n\t"
            ".Lwa_last%=:\n\t"
            "s_setprio 2\n\t"
            "s_waitcnt lgkmcnt(0)\n\t"
            "s_nop 7\n\t"
            "v_mfma_f32_16x16x32_bf16 v[156:159], v[240:243], v[184:187], 0\n\t"
            "ds_read_b64 v[176:177], v247\n\t"
            "ds_read_b64 v[196:197], v247 offset:1536\n\t"
            "v_mfma_f32_16x16x32_bf16 v[156:159], v[240:243], v[188:191], v[156:159]\n\t"
            "ds_read_b64 v[220:221], v247 offset:3072\n\t"
            "v_xor_b32 v234, s73, v254\n\t"
            "v_mfma_f32_16x16x32_bf16 v[228:231], v[240:243], v[148:151], 0\n\t"
            "ds_read_u16 v178, v234\n\t"
            "ds_read_u16 v198, v234 offset:128\n\t"
            "v_mfma_f32_16x16x32_bf16 v[228:231], v[240:243], v[164:167], v[228:231]\n\t"
            "ds_read_u16 v222, v234 offset:256\n\t"
            "v_add_u32 v235, s45, v248\n\t"
            "v_mfma_f32_16x16x32_bf16 v[208:211], v[240:243], v[180:183], 0\n\t"
            "ds_read_b128 v[168:171], v235\n\t"
            "ds_read_b128 v[192:195], v235 offset:1536\n\t"
            "v_mfma_f32_16x16x32_bf16 v[208:211], v[240:243], v[172:175], v[208:211]\n\t"
            "ds_read_b128 v[216:219], v235 offset:3072\n\t"
            "v_mov_b32 v179, 0\n\t"
            "v_mov_b32 v199, 0\n\t"
            "v_mov_b32 v223, 0\n\t"
            "v_rcp_f32 v157, v156\n\t"
            "v_mad_u32_u24 v234, v246, s75, v253\n\t"
            "v_rcp_f32 v229, v228\n\t"
            "s_nop 0\n\t"
            "v_rcp_f32 v209, v208\n\t"
            "s_waitcnt lgkmcnt(2)\n\t"
            "v_mfma_f32_16x16x32_bf16 v[144:147], v[168:171], v[184:187], 0\n\t"
            "v_mfma_f32_16x16x32_bf16 v[144:147], v[176:179], v[188:191], v[144:147]\n\t"
            "s_waitcnt lgkmcnt(1)\n\t"
            "v_mfma_f32_16x16x32_bf16 v[152:155], v[192:195], v[184:187], 0\n\t"
            "v_mfma_f32_16x16x32_bf16 v[152:155], v[196:199], v[188:191], v[152:155]\n\t"
            "s_waitcnt lgkmcnt(0)\n\t"
            "v_mfma_f32_16x16x32_bf16 v[184:187], v[216:219], v[184:187], 0\n\t"
            "v_mfma_f32_16x16x32_bf16 v[184:187], v[220:223], v[188:191], v[184:187]\n\t"
            "ds_read_b64 v[188:189], v234 offset:320\n\t"
            "ds_read_b64 v[190:191], v234 offset:352\n\t"
            "v_mfma_f32_16x16x32_bf16 v[224:227], v[168:171], v[148:151], 0\n\t"
            "v_mul_f32 v144, v157, v144\n\t"
            "v_mul_f32 v145, v157, v145\n\t"
            "v_mfma_f32_16x16x32_bf16 v[224:227], v[176:179], v[164:167], v[224:227]\n\t"
            "v_mul_f32 v152, v157, v152\n\t"
            "v_mul_f32 v153, v157, v153\n\t"
            "v_mfma_f32_16x16x32_bf16 v[212:215], v[192:195], v[148:151], 0\n\t"
            "v_mul_f32 v184, v157, v184\n\t"
            "v_mul_f32 v185, v157, v185\n\t"
            "v_mfma_f32_16x16x32_bf16 v[212:215], v[196:199], v[164:167], v[212:215]\n\t"
            "v_mul_f32 v186, v157, v186\n\t"
            "v_mul_f32 v187, v157, v187\n\t"
            "v_mfma_f32_16x16x32_bf16 v[160:163], v[168:171], v[180:183], 0\n\t"
            "v_cvt_pk_bf16_f32 v184, v184, v185\n\t"
            "v_cvt_pk_bf16_f32 v185, v186, v187\n\t"
            "v_mfma_f32_16x16x32_bf16 v[160:163], v[176:179], v[172:175], v[160:163]\n\t"
            "ds_read_b64 v[186:187], v234 offset:288\n\t"
            "ds_write_b64 v245, v[184:185] offset:6400\n\t"
            "v_mfma_f32_16x16x32_bf16 v[204:207], v[192:195], v[180:183], 0\n\t"
            "ds_read_b64 v[184:185], v234 offset:256\n\t"
            "v_mul_f32 v224, v229, v224\n\t"
            "v_mfma_f32_16x16x32_bf16 v[204:207], v[196:199], v[172:175], v[204:207]\n\t"
            "v_mul_f32 v225, v229, v225\n\t"
            "v_mul_f32 v212, v229, v212\n\t"
            "v_mfma_f32_16x16x32_bf16 v[148:151], v[216:219], v[148:151], 0\n\t"
            "v_mul_f32 v213, v229, v213\n\t"
            "v_mul_f32 v160, v209, v160\n\t"
            "v_mfma_f32_16x16x32_bf16 v[148:151], v[220:223], v[164:167], v[148:151]\n\t"
            "v_mul_f32 v161, v209, v161\n\t"
            "v_mul_f32 v226, v229, v226\n\t"
            "v_mfma_f32_16x16x32_bf16 v[180:183], v[216:219], v[180:183], 0\n\t"
            "v_mul_f32 v204, v209, v204\n\t"
            "v_mul_f32 v205, v209, v205\n\t"
            "v_mfma_f32_16x16x32_bf16 v[180:183], v[220:223], v[172:175], v[180:183]\n\t"
            "v_mul_f32 v227, v229, v227\n\t"
            "v_cvt_pk_bf16_f32 v224, v224, v225\n\t"
            "v_mul_f32 v148, v229, v148\n\t"
            "v_mul_f32 v149, v229, v149\n\t"
            "v_mul_f32 v214, v229, v214\n\t"
            "v_mul_f32 v215, v229, v215\n\t"
            "v_cvt_pk_bf16_f32 v212, v212, v213\n\t"
            "s_waitcnt lgkmcnt(0)\n\t"
            "v_mfma_f32_16x16x32_bf16 v[200:203], v[240:243], v[184:187], 0\n\t"
            "v_mul_f32 v180, v209, v180\n\t"
            "v_mul_f32 v181, v209, v181\n\t"
            "v_mfma_f32_16x16x32_bf16 v[200:203], v[240:243], v[188:191], v[200:203]\n\t"
            "v_mul_f32 v150, v229, v150\n\t"
            "v_mul_f32 v151, v229, v151\n\t"
            "v_mfma_f32_16x16x32_bf16 v[168:171], v[184:187], v[168:171], 0\n\t"
            "v_cvt_pk_bf16_f32 v148, v148, v149\n\t"
            "v_mul_f32 v146, v157, v146\n\t"
            "v_mfma_f32_16x16x32_bf16 v[168:171], v[188:191], v[176:179], v[168:171]\n\t"
            "v_mul_f32 v147, v157, v147\n\t"
            "v_cvt_pk_bf16_f32 v144, v144, v145\n\t"
            "v_mfma_f32_16x16x32_bf16 v[192:195], v[184:187], v[192:195], 0\n\t"
            "v_rcp_f32 v201, v200\n\t"
            "v_mfma_f32_16x16x32_bf16 v[192:195], v[188:191], v[196:199], v[192:195]\n\t"
            "v_mul_f32 v154, v157, v154\n\t"
            "v_mul_f32 v155, v157, v155\n\t"
            "v_mfma_f32_16x16x32_bf16 v[216:219], v[184:187], v[216:219], 0\n\t"
            "v_cvt_pk_bf16_f32 v152, v152, v153\n\t"
            "v_mul_f32 v162, v209, v162\n\t"
            "v_mfma_f32_16x16x32_bf16 v[216:219], v[188:191], v[220:223], v[216:219]\n\t"
            "v_mul_f32 v163, v209, v163\n\t"
            "v_cvt_pk_bf16_f32 v160, v160, v161\n\t"
            "v_cndmask_b32_e64 v203, v168, v192, s[60:61]\n\t"
            "v_mul_f32 v206, v209, v206\n\t"
            "v_mul_f32 v207, v209, v207\n\t"
            "v_cvt_pk_bf16_f32 v204, v204, v205\n\t"
            "v_mul_f32 v182, v209, v182\n\t"
            "v_mul_f32 v183, v209, v183\n\t"
            "v_cvt_pk_bf16_f32 v180, v180, v181\n\t"
            "v_cndmask_b32_e64 v203, v203, v216, s[62:63]\n\t"
            "v_cvt_pk_bf16_f32 v225, v226, v227\n\t"
            "v_cvt_pk_bf16_f32 v213, v214, v215\n\t"
            "v_cvt_pk_bf16_f32 v149, v150, v151\n\t"
            "v_cvt_pk_bf16_f32 v145, v146, v147\n\t"
            "v_cvt_pk_bf16_f32 v153, v154, v155\n\t"
            "v_cvt_pk_bf16_f32 v161, v162, v163\n\t"
            "v_cvt_pk_bf16_f32 v205, v206, v207\n\t"
            "v_cvt_pk_bf16_f32 v181, v182, v183\n\t"
            "v_mul_f32_dpp v203, v201, v203 quad_perm:[0,0,0,0] row_mask:0xf bank_mask:0xf\n\t"
            "v_xad_u32 v202, s73, v254, v255\n\t"
            "v_cvt_pk_bf16_f32 v203, v203, 0\n\t"
            "ds_write_b64 v245, v[224:225]\n\t"
            "ds_write_b64 v245, v[212:213] offset:128\n\t"
            "ds_write_b64 v245, v[148:149] offset:256\n\t"
            "ds_write_b64 v245, v[144:145] offset:6144\n\t"
            "ds_write_b64 v245, v[152:153] offset:6272\n\t"
            "ds_write_b64 v245, v[160:161] offset:12288\n\t"
            "ds_write_b64 v245, v[204:205] offset:12416\n\t"
            "ds_write_b64 v245, v[180:181] offset:12544\n\t"
            "s_mov_b64 exec, s[68:69]\n\t"
            "ds_write_b16 v202, v203\n\t"
            "s_mov_b64 exec, 1\n\t"
            "ds_add_u32 v253, v246 offset:7856\n\t"
            "s_mov_b64 exec, -1\n\t"
            ".Lwa_done%=:\n\t"
            "s_waitcnt lgkmcnt(0)\n\t"
            "s_nop 7\n\t"
            : "+{v204}"(pST_), "+{v205}"(aRSw_), "+{v206}"(aPLw_), "+{v207}"(aRSr_), "+{v208}"(g16_), "+{v209}"(g8_), "+{v210}"(loVL_), "+{v211}"(loOL_), "+{v200}"(on), "+{v250}"(c0), "+{v251}"(c1), "+{v252}"(c2), "+{v253}"(c3), "+{v254}"(c4), "+{v255}"(c5),
              "+{v238}"(c6), "+{v239}"(c7), "+{v240}"(c8), "+{v241}"(c9), "+{s89}"(s89), "+{s46}"(s46), "+{s48}"(s48), "+{s70}"(s70), "+{s72}"(s72), "+{s73}"(s73), "+{s74}"(s74), "+{s76}"(s76), "+{s77}"(s77)
            : "{v244}"(pE_), "{v245}"(aO_), "{v246}"(sel_), "{v247}"(aVB_), "{s47}"((ldsb + X_OFF) ^ (ldsb + X1_OFF)),
              "{s[58:59]}"(0xFFFFull), "{s75}"(rsb), "{s45}"(ldsb + V_OFF + w * 4608), "{s[68:69]}"(0xFFFFFFFFFFFFull),
              "{s[84:87]}"(wr), "{s88}"(touch), "{s80}"(wave == 0 ? 16 : 20), "{s71}"(ldsb + O_OFF), "{s[54:55]}"(mrow), "{s[56:57]}"(0xF0000F0000F0000Full),
              "{s[78:79]}"(mrow & 0xF0000F0000F0000Full)
            : "v144", "v145", "v146", "v147", "v148", "v149", "v150", "v151", "v152", "v153", "v154", "v155", "v156", "v157", "v158", "v159", "v160", "v161", "v162", "v163", "v164", "v165", "v166", "v167", "v168", "v169", "v170", "v171", "v172", "v173", "v174", "v175", "v176", "v177", "v178", "v179", "v180", "v181", "v182", "v183", "v184", "v185", "v186", "v187", "v188", "v189", "v190", "v191", "v192", "v193", "v194", "v195", "v196", "v197", "v198", "v199", "v201", "v202", "v203", "v212", "v213", "v214", "v215", "v216", "v217", "v218", "v219", "v220", "v221", "v222", "v223", "v224", "v225", "v226", "v227", "v228", "v229", "v230", "v231", "v232", "v233", "v234", "v235", "v236", "v237", "v242", "v243", "v248", "v249", "v0", "v1", "v2", "v3", "v4", "v5", "v6", "v7", "v8", "v9", "v10", "v11", "v12", "v13", "v14", "v15", "v16", "v17", "v18", "v19", "v20", "v21", "v22", "v23", "v24", "v25", "v26", "v27", "v28", "v29", "v30", "v31", "v32", "v33", "v34", "v35", "v36", "v37", "v38", "v39", "v40", "v41", "v42", "v43", "v44", "v45", "v46", "v47", "v48", "v49", "v50", "v51", "v52", "v53", "v54", "v55", "v56", "v57", "v58", "v59", "v60", "v61", "v62", "v63", "v64", "v65", "v66", "v67", "v68", "v69", "v70", "v71", "v72", "v73", "v74", "v75", "v76", "v77", "v78", "v79", "v80", "v81", "v82", "v83", "v84", "v85", "v86", "v87", "v88", "v89", "v90", "v91", "v92", "v93", "v94", "v95", "v96", "v97", "v98", "v99", "v100", "v101", "v102", "v103", "v104", "v105", "v106", "v107", "v108", "v109", "v110", "v111", "v112", "v113", "v114", "v115", "v116", "v117", "v118", "v119", "v120", "v121", "v122", "v123", "v124", "v125", "v126", "v127", "v128", "v129", "v130", "v131", "v132", "v133", "v134", "v135", "v136", "v137", "v138", "v139", "v140", "v141", "v142", "v143",  "m0", "s60", "s61", "s62", "s63", "s64", "s65", "s66", "s67", "s81", "s82", "s83", "s90", "s91", "s49", "vcc", "scc", "memory");

        }
        if (wave == 0 && blockIdx.x == 0) {
            int ln_;
            asm volatile("v_mbcnt_lo_u32_b32 %0, -1, 0\n\tv_mbcnt_hi_u32_b32 %0, -1, %0" : "=v"(ln_));
            KARGS
            unsigned long long tg;
            asm volatile("s_load_dwordx2 %0, %1, %2\n\ts_waitcnt lgkmcnt(0)" : "=s"(tg) : "s"(wb), "n"(EPO_OFF) : "memory");
            const unsigned lo = (unsigned)tg + 1u, hi = (unsigned)(tg >> 32);
            const auto wq = __builtin_amdgcn_make_buffer_rsrc((void*)wb, 0, WS_BYTES, 0x00020000);
#pragma unroll 1
            for (int it_ = 0; it_ < 100000; ++it_) {
                bool bad = false;
#pragma unroll
                for (int k = 0; k < 2; ++k) {
                    const u32x4 a_ = __builtin_amdgcn_raw_buffer_load_b128(wq, ARR_OFF + ln_ * 16, k * 1024, SC1);
                    bad |= a_[0] != lo || a_[1] != hi || a_[2] != lo || a_[3] != hi;
                }
                if (__builtin_amdgcn_ballot_w64(bad) == 0ull) break;
                __builtin_amdgcn_s_sleep(2);
            }
            if (ln_ == 0) __builtin_amdgcn_raw_buffer_store_b64((u32x2){lo, hi}, wq, EPO_OFF, 0, SC1);
        }
        asm volatile("wk_code_end:");
        __builtin_amdgcn_endpgm();
    } else if (wave < 6) {
        LANE_SETUP
        {
            const unsigned rsb = ldsb + RSX_OFF;
            const unsigned pE_ = fE - X_OFF;
            const unsigned aVAw_ = ldsb + V_OFF + w * 9216 + lane * 16, aVBw_ = ldsb + V_OFF + w * 9216 + 1024 + lane * 8;
            const unsigned pST_ = lane * 16;
            unsigned aRSw_ = rsb + 256 + lane * 4, aRSr_ = rsb + (lane & 48);
            B_SCALARS
            unsigned s44 = 4;
            asm volatile(
            "s_waitcnt lgkmcnt(0)\n\t"
            "s_mov_b32 s83, 0x10000\n\t"
            "s_mov_b32 s84, -47104\n\t"
            "s_nop 7\n\t"
            "v_mov_b32 v200, v250\n\t"
            "v_mov_b32 v201, v251\n\t"
            "v_mov_b32 v202, v253\n\t"
            "s_mov_b32 s38, s67\n\t"
            "s_mov_b32 s67, 74496\n\t"
            "ds_read_b64 v[250:251], v203\n\t"
            "ds_read_b32 v253, v203 offset:8\n\t"
            ".Lwk_b1%=:\n\t"
            "v_mov_b32 v236, 0\n\t"
            "ds_read_b32 v236, v236 offset:7864\n\t"
            "s_waitcnt lgkmcnt(0)\n\t"
            "v_readfirstlane_b32 s77, v236\n\t"
            "s_cmp_ge_u32 s77, 4\n\t"
            "s_cbranch_scc1 .Lwk_b2%=\n\t"
            "s_cmp_eq_u32 s83, 0\n\t"
            "s_cbranch_scc1 .Lwk_b2%=\n\t"
            "s_sub_u32 s83, s83, 1\n\t"
            "s_sleep 1\n\t"
            "s_branch .Lwk_b1%=\n\t"
            ".Lwk_b2%=:\n\t"
            "s_waitcnt vmcnt(0)\n\t"
            "s_call_b64 vcc, wk_conv\n\t"
            "v_mov_b32 v250, v200\n\t"
            "v_mov_b32 v251, v201\n\t"
            "v_mov_b32 v253, v202\n\t"
            "s_mov_b32 s67, s38\n\t"
            "s_setprio 3\n\t"
            "s_barrier\n\t"
            "buffer_load_dwordx4 v[140:143], v245, s[28:31], s82 offen sc1\n\t"
            "s_add_u32 s49, s33, 0\n\t"
            "buffer_load_dwordx4 v[0:3], v245, s[28:31], s49 offen sc1\n\t"
            "s_add_u32 s49, s33, 6144\n\t"
            "buffer_load_dwordx4 v[24:27], v245, s[28:31], s49 offen sc1\n\t"
            "s_add_u32 s49, s33, 12288\n\t"
            "buffer_load_dwordx4 v[48:51], v245, s[28:31], s49 offen sc1\n\t"
            "s_add_u32 s49, s33, 18432\n\t"
            "buffer_load_dwordx4 v[72:75], v245, s[28:31], s49 offen sc1\n\t"
            "s_add_u32 s49, s33, 24576\n\t"
            "buffer_load_dwordx4 v[96:99], v245, s[28:31], s49 offen sc1\n\t"
            "s_add_u32 s49, s33, 30720\n\t"
            "buffer_load_dwordx4 v[120:123], v245, s[28:31], s49 offen sc1\n\t"
            "s_add_u32 s49, s33, 1024\n\t"
            "buffer_load_dwordx4 v[4:7], v245, s[28:31], s49 offen sc1\n\t"
            "s_add_u32 s49, s33, 7168\n\t"
            "buffer_load_dwordx4 v[28:31], v245, s[28:31], s49 offen sc1\n\t"
            "s_add_u32 s49, s33, 13312\n\t"
            "buffer_load_dwordx4 v[52:55], v245, s[28:31], s49 offen sc1\n\t"
            "s_add_u32 s49, s33, 19456\n\t"
            "buffer_load_dwordx4 v[76:79], v245, s[28:31], s49 offen sc1\n\t"
            "s_add_u32 s49, s33, 25600\n\t"
            "buffer_load_dwordx4 v[100:103], v245, s[28:31], s49 offen sc1\n\t"
            "s_add_u32 s49, s33, 31744\n\t"
            "buffer_load_dwordx4 v[124:127], v245, s[28:31], s49 offen sc1\n\t"
            "s_add_u32 s49, s33, 2048\n\t"
            "buffer_load_dwordx4 v[8:11], v245, s[28:31], s49 offen sc1\n\t"
            "s_add_u32 s49, s33, 8192\n\t"
            "buffer_load_dwordx4 v[32:35], v245, s[28:31], s49 offen sc1\n\t"
            "s_add_u32 s49, s33, 14336\n\t"
            "buffer_load_dwordx4 v[56:59], v245, s[28:31], s49 offen sc1\n\t"
            "s_add_u32 s49, s33, 20480\n\t"
            "buffer_load_dwordx4 v[80:83], v245, s[28:31], s49 offen sc1\n\t"
            "s_add_u32 s49, s33, 26624\n\t"
            "buffer_load_dwordx4 v[104:107], v245, s[28:31], s49 offen sc1\n\t"
            "s_add_u32 s49, s33, 32768\n\t"
            "buffer_load_dwordx4 v[128:131], v245, s[28:31], s49 offen sc1\n\t"
            "s_add_u32 s49, s33, 3072\n\t"
            "buffer_load_dwordx4 v[12:15], v245, s[28:31], s49 offen sc1\n\t"
            "s_add_u32 s49, s33, 9216\n\t"
            "buffer_load_dwordx4 v[36:39], v245, s[28:31], s49 offen sc1\n\t"
            "s_add_u32 s49, s33, 15360\n\t"
            "buffer_load_dwordx4 v[60:63], v245, s[28:31], s49 offen sc1\n\t"
            "s_add_u32 s49, s33, 21504\n\t"
            "buffer_load_dwordx4 v[84:87], v245, s[28:31], s49 offen sc1\n\t"
            "s_add_u32 s49, s33, 27648\n\t"
            "buffer_load_dwordx4 v[108:111], v245, s[28:31], s49 offen sc1\n\t"
            "s_add_u32 s49, s33, 33792\n\t"
            "buffer_load_dwordx4 v[132:135], v245, s[28:31], s49 offen sc1\n\t"
            "s_add_u32 s49, s33, 4096\n\t"
            "buffer_load_dwordx4 v[16:19], v245, s[28:31], s49 offen sc1\n\t"
            "s_add_u32 s49, s33, 10240\n\t"
            "buffer_load_dwordx4 v[40:43], v245, s[28:31], s49 offen sc1\n\t"
            "s_add_u32 s49, s33, 16384\n\t"
            "buffer_load_dwordx4 v[64:67], v245, s[28:31], s49 offen sc1\n\t"
            "s_add_u32 s49, s33, 22528\n\t"
            "buffer_load_dwordx4 v[88:91], v245, s[28:31], s49 offen sc1\n\t"
            "s_add_u32 s49, s33, 28672\n\t"
            "buffer_load_dwordx4 v[112:115], v245, s[28:31], s49 offen sc1\n\t"
            "s_add_u32 s49, s33, 34816\n\t"
            "buffer_load_dwordx4 v[136:139], v245, s[28:31], s49 offen sc1\n\t"
            "s_add_u32 s49, s33, 5120\n\t"
            "buffer_load_dwordx4 v[20:23], v245, s[28:31], s49 offen sc1\n\t"
            "s_add_u32 s49, s33, 11264\n\t"
            "buffer_load_dwordx4 v[44:47], v245, s[28:31], s49 offen sc1\n\t"
            "s_add_u32 s49, s33, 17408\n\t"
            "buffer_load_dwordx4 v[68:71], v245, s[28:31], s49 offen sc1\n\t"
            "s_add_u32 s49, s33, 23552\n\t"
            "buffer_load_dwordx4 v[92:95], v245, s[28:31], s49 offen sc1\n\t"
            "s_add_u32 s49, s33, 29696\n\t"
            "buffer_load_dwordx4 v[116:119], v245, s[28:31], s49 offen sc1\n\t"
            "s_add_u32 s49, s33, 35840\n\t"
            "buffer_load_dwordx4 v[140:143], v245, s[28:31], s49 offen sc1\n\t"
            "ds_read_b32 v249, v252 offset:12\n\t"
            "s_waitcnt lgkmcnt(0)\n\t"
            "v_and_b32 v249, s69, v249\n\t"
            "s_mov_b32 exec_hi, 0x3fffff\n\t"
            "s_add_u32 m0, s66, 0\n\t"
            "s_add_u32 s49, s79, 0\n\t"
            "buffer_load_dwordx4 v249, s[72:75], s49 offen lds\n\t"
            "s_mov_b32 exec_hi, -1\n\t"
            "s_mov_b32 exec_hi, 0x3fffff\n\t"
            "s_add_u32 m0, s66, 872\n\t"
            "s_add_u32 s49, s47, 196224\n\t"
            "buffer_load_dwordx4 v249, s[72:75], s49 offen lds\n\t"
            "s_mov_b32 exec_hi, -1\n\t"
            "s_mov_b32 exec_hi, 0x3fffff\n\t"
            "s_add_u32 m0, s66, 1744\n\t"
            "s_add_u32 s49, s47, 392448\n\t"
            "buffer_load_dwordx4 v249, s[72:75], s49 offen lds\n\t"
            "s_mov_b32 exec_hi, -1\n\t"
            "s_mov_b32 exec_hi, 0x3fffff\n\t"
            "s_add_u32 m0, s66, 2616\n\t"
            "s_add_u32 s49, s47, 588672\n\t"
            "buffer_load_dwordx4 v249, s[72:75], s49 offen lds\n\t"
            "s_mov_b32 exec_hi, -1\n\t"
            "s_mov_b32 exec_hi, 0x3fffff\n\t"
            "s_add_u32 m0, s66, 3488\n\t"
            "s_add_u32 s49, s47, 784896\n\t"
            "buffer_load_dwordx4 v249, s[72:75], s49 offen lds\n\t"
            "s_mov_b32 exec_hi, -1\n\t"
            "s_mov_b32 exec_hi, 0x3fffff\n\t"
            "s_add_u32 m0, s66, 4360\n\t"
            "s_add_u32 s49, s47, 981120\n\t"
            "buffer_load_dwordx4 v249, s[72:75], s49 offen lds\n\t"
            "s_mov_b32 exec_hi, -1\n\t"
            "s_mov_b32 exec_hi, 0x3fffff\n\t"
            "s_add_u32 m0, s66, 5232\n\t"
            "s_add_u32 s49, s47, 1177344\n\t"
            "buffer_load_dwordx4 v249, s[72:75], s49 offen lds\n\t"
            "s_mov_b32 exec_hi, -1\n\t"
            "s_mov_b32 exec_hi, 0x3fffff\n\t"
            "s_add_u32 m0, s66, 6104\n\t"
            "s_add_u32 s49, s47, 1373568\n\t"
            "buffer_load_dwordx4 v249, s[72:75], s49 offen lds\n\t"
            "s_mov_b32 exec_hi, -1\n\t"
            "s_mov_b32 exec_hi, 0x3fffff\n\t"
            "s_add_u32 m0, s66, 6976\n\t"
            "s_add_u32 s49, s47, 1569792\n\t"
            "buffer_load_dwordx4 v249, s[72:75], s49 offen lds\n\t"
            "s_mov_b32 exec_hi, -1\n\t"
            "ds_read_b128 v[168:171], v254\n\t"
            "ds_read_b128 v[172:175], v254 offset:64\n\t"
            "v_add_u32 v180, s48, v245\n\t"
            "v_add_u32 v232, s48, v242\n\t"
            "v_xor_b32 v233, 64, v232\n\t"
            "v_xor_b32 v246, 0x100, v246\n\t"
            "ds_read_b128 v[176:179], v180 offset:18816\n\t"
            "ds_read_b128 v[180:183], v180 offset:19840\n\t"
            "ds_read_b128 v[192:195], v232\n\t"
            "ds_read_b128 v[196:199], v233\n\t"
            "ds_read_b128 v[224:227], v232 offset:128\n\t"
            "ds_read_b128 v[228:231], v233 offset:128\n\t"
            "s_waitcnt lgkmcnt(5)\n\t"
            "v_add_f32 v176, v176, v178\n\t"
            "s_waitcnt lgkmcnt(4)\n\t"
            "v_add_f32 v180, v180, v182\n\t"
            "v_add_f32 v176, v176, v180\n\t"
            "v_add_f32 v177, v177, v179\n\t"
            "v_add_f32 v181, v181, v183\n\t"
            "v_mul_f32 v176, 0x3baaaaab, v176\n\t"
            "v_add_f32 v177, v177, v181\n\t"
            "v_mul_f32_e64 v176, v176, -v176\n\t"
            "v_fmac_f32 v176, 0x3baaaaab, v177\n\t"
            "v_max_f32 v176, 0, v176\n\t"
            "v_add_f32 v176, 0x3727c5ac, v176\n\t"
            "v_rsq_f32 v176, v176\n\t"
            "s_nop 0\n\t"
            "ds_write_b32 v246, v176\n\t"
            "s_setprio 3\n\t"
            "s_waitcnt vmcnt(9)\n\t"
            "s_waitcnt lgkmcnt(3)\n\t"
            "v_mfma_f32_16x16x32_bf16 v[200:203], v[192:195], v[0:3], 0\n\t"
            "v_mfma_f32_16x16x32_bf16 v[200:203], v[196:199], v[4:7], v[200:203]\n\t"
            "v_mfma_f32_16x16x32_bf16 v[204:207], v[196:199], v[28:31], 0\n\t"
            "v_mfma_f32_16x16x32_bf16 v[204:207], v[192:195], v[24:27], v[204:207]\n\t"
            "v_mfma_f32_16x16x32_bf16 v[208:211], v[192:195], v[48:51], 0\n\t"
            "v_mfma_f32_16x16x32_bf16 v[208:211], v[196:199], v[52:55], v[208:211]\n\t"
            "v_mfma_f32_16x16x32_bf16 v[212:215], v[196:199], v[76:79], 0\n\t"
            "v_mfma_f32_16x16x32_bf16 v[212:215], v[192:195], v[72:75], v[212:215]\n\t"
            "v_mfma_f32_16x16x32_bf16 v[216:219], v[192:195], v[96:99], 0\n\t"
            "v_mfma_f32_16x16x32_bf16 v[216:219], v[196:199], v[100:103], v[216:219]\n\t"
            "v_mfma_f32_16x16x32_bf16 v[220:223], v[196:199], v[124:127], 0\n\t"
            "v_mfma_f32_16x16x32_bf16 v[220:223], v[192:195], v[120:123], v[220:223]\n\t"
            "s_waitcnt lgkmcnt(1)\n\t"
            "v_mfma_f32_16x16x32_bf16 v[200:203], v[224:227], v[8:11], v[200:203]\n\t"
            "v_mfma_f32_16x16x32_bf16 v[200:203], v[228:231], v[12:15], v[200:203]\n\t"
            "v_mfma_f32_16x16x32_bf16 v[204:207], v[228:231], v[36:39], v[204:207]\n\t"
            "v_mfma_f32_16x16x32_bf16 v[204:207], v[224:227], v[32:35], v[204:207]\n\t"
            "v_mfma_f32_16x16x32_bf16 v[208:211], v[224:227], v[56:59], v[208:211]\n\t"
            "v_mfma_f32_16x16x32_bf16 v[208:211], v[228:231], v[60:63], v[208:211]\n\t"
            "v_mfma_f32_16x16x32_bf16 v[212:215], v[228:231], v[84:87], v[212:215]\n\t"
            "v_mfma_f32_16x16x32_bf16 v[212:215], v[224:227], v[80:83], v[212:215]\n\t"
            "v_mfma_f32_16x16x32_bf16 v[216:219], v[224:227], v[104:107], v[216:219]\n\t"
            "s_waitcnt vmcnt(0)\n\t"
            "ds_read_b64 v[144:145], v253\n\t"
            "v_mfma_f32_16x16x32_bf16 v[216:219], v[228:231], v[108:111], v[216:219]\n\t"
            "ds_read_b64 v[146:147], v253 offset:96\n\t"
            "ds_read_b64 v[148:149], v253 offset:1744\n\t"
            "v_mfma_f32_16x16x32_bf16 v[220:223], v[228:231], v[132:135], v[220:223]\n\t"
            "ds_read_b128 v[228:231], v233 offset:256\n\t"
            "ds_read_b64 v[150:151], v253 offset:1840\n\t"
            "v_mfma_f32_16x16x32_bf16 v[220:223], v[224:227], v[128:131], v[220:223]\n\t"
            "ds_read_b128 v[224:227], v232 offset:256\n\t"
            "ds_read_b64 v[152:153], v253 offset:8\n\t"
            "ds_read_b64 v[154:155], v253 offset:104\n\t"
            "ds_read_b64 v[156:157], v253 offset:1752\n\t"
            "ds_read_b64 v[158:159], v253 offset:1848\n\t"
            "ds_read_b64 v[160:161], v253 offset:16\n\t"
            "ds_read_b64 v[162:163], v253 offset:112\n\t"
            "ds_read_b64 v[164:165], v253 offset:1760\n\t"
            "ds_read_b64 v[166:167], v253 offset:1856\n\t"
            "s_waitcnt lgkmcnt(0)\n\t"
            "s_nop 7\n\t"
            ".p2align 6\n\t"
            "s_nop 0\n\t"
            "s_nop 0\n\t"
            "s_nop 0\n\t"
            "s_nop 0\n\t"
            ".Lwc_loop%=:\n\t"
            "s_setprio 3\n\t"
            "v_mfma_f32_16x16x32_bf16 v[200:203], v[224:227], v[16:19], v[200:203]\n\t"
            "v_mov_b64 v[176:177], 0\n\t"
            "v_mov_b64 v[178:179], 0\n\t"
            "v_mfma_f32_16x16x32_bf16 v[200:203], v[228:231], v[20:23], v[200:203]\n\t"
            "v_mov_b64 v[180:181], 0\n\t"
            "v_mov_b64 v[182:183], 0\n\t"
            "s_waitcnt lgkmcnt(8)\n\t"
            "v_smfmac_f32_16x16x64_bf16 v[176:179], v[168:171], v[144:151], v255\n\t"
            "ds_read_b128 v[168:171], v254 offset:128\n\t"
            "s_waitcnt lgkmcnt(5)\n\t"
            "v_smfmac_f32_16x16x64_bf16 v[180:183], v[172:175], v[152:159], v255\n\t"
            "ds_read_b64 v[144:145], v253 offset:24\n\t"
            "v_mfma_f32_16x16x32_bf16 v[204:207], v[228:231], v[44:47], v[204:207]\n\t"
            "ds_read_b64 v[146:147], v253 offset:120\n\t"
            "ds_read_b64 v[148:149], v253 offset:1768\n\t"
            "v_mfma_f32_16x16x32_bf16 v[204:207], v[224:227], v[40:43], v[204:207]\n\t"
            "ds_read_b64 v[150:151], v253 offset:1864\n\t"
            "ds_read_b128 v[172:175], v254 offset:192\n\t"
            "v_mfma_f32_16x16x32_bf16 v[208:211], v[224:227], v[64:67], v[208:211]\n\t"
            "ds_read_b64 v[152:153], v253 offset:32\n\t"
            "ds_read_b64 v[154:155], v253 offset:128\n\t"
            "v_mfma_f32_16x16x32_bf16 v[208:211], v[228:231], v[68:71], v[208:211]\n\t"
            "ds_read_b64 v[156:157], v253 offset:1776\n\t"
            "ds_read_b64 v[158:159], v253 offset:1872\n\t"
            "v_mfma_f32_16x16x32_bf16 v[212:215], v[228:231], v[92:95], v[212:215]\n\t"
            "v_mov_b32 v249, s76\n\t"
            "v_mov_b64 v[184:185], 0\n\t"
            "v_mfma_f32_16x16x32_bf16 v[212:215], v[224:227], v[88:91], v[212:215]\n\t"
            "ds_read_b32 v249, v249\n\t"
            "v_mov_b64 v[186:187], 0\n\t"
            "v_mfma_f32_16x16x32_bf16 v[216:219], v[224:227], v[112:115], v[216:219]\n\t"
            "v_mov_b64 v[188:189], 0\n\t"
            "v_mov_b64 v[190:191], 0\n\t"
            "v_mfma_f32_16x16x32_bf16 v[216:219], v[228:231], v[116:119], v[216:219]\n\t"
            "v_cvt_pk_bf16_f32 v176, v176, v177\n\t"
            "v_add_u32 v236, s67, v250\n\t"
            "s_waitcnt lgkmcnt(10)\n\t"
            "v_smfmac_f32_16x16x64_bf16 v[184:187], v[168:171], v[160:167], v255\n\t"
            "ds_read_b128 v[168:171], v254 offset:256\n\t"
            "s_waitcnt lgkmcnt(6)\n\t"
            "v_smfmac_f32_16x16x64_bf16 v[188:191], v[172:175], v[144:151], v255\n\t"
            "ds_read_b64 v[160:161], v253 offset:40\n\t"
            "v_mfma_f32_16x16x32_bf16 v[220:223], v[228:231], v[140:143], v[220:223]\n\t"
            "ds_read_b64 v[162:163], v253 offset:136\n\t"
            "ds_read_b64 v[164:165], v253 offset:1784\n\t"
            "v_mfma_f32_16x16x32_bf16 v[220:223], v[224:227], v[136:139], v[220:223]\n\t"
            "s_setprio 1\n\t"
            "ds_read_b64 v[166:167], v253 offset:1880\n\t"
            "ds_read_b128 v[172:175], v254 offset:320\n\t"
            "ds_read_b64 v[144:145], v253 offset:48\n\t"
            "ds_read_b64 v[146:147], v253 offset:144\n\t"
            "ds_read_b64 v[148:149], v253 offset:1792\n\t"
            "ds_read_b64 v[150:151], v253 offset:1888\n\t"
            "s_waitcnt lgkmcnt(14)\n\t"
            "ds_read_b128 v[224:227], v232 offset:6272\n\t"
            "s_waitcnt lgkmcnt(14)\n\t"
            "ds_read_b128 v[228:231], v233 offset:6272\n\t"
            "s_waitcnt lgkmcnt(12)\n\t"
            "v_readfirstlane_b32 s26, v249\n\t"
            "v_cvt_pk_bf16_f32 v177, v178, v179\n\t"
            "v_cvt_pk_bf16_f32 v178, v180, v181\n\t"
            "s_and_b32 s68, s26, 0x7ffffff\n\t"
            "s_add_u32 s68, s68, s78\n\t"
            "s_and_b32 s69, s26, 0x38000000\n\t"
            "s_or_b32 s69, s69, s39\n\t"
            "s_bitcmp1_b32 s26, 27\n\t"
            "s_cselect_b32 s79, 2.0, s68\n\t"
            "s_setprio 3\n\t"
            "v_mfma_f32_4x4x4_16b_bf16 v[196:199], v[176:177], v[176:177], 0\n\t"
            "s_setprio 1\n\t"
            "ds_read_b32 v249, v252 offset:12\n\t"
            "s_and_b32 s57, s56, 3\n\t"
            "s_lshl_b32 s58, s57, 3\n\t"
            "s_lshr_b32 m0, s54, s58\n\t"
            "s_lshl_b32 s58, s57, 4\n\t"
            "s_lshr_b64 s[36:37], s[50:51], s58\n\t"
            "s_and_b32 s57, s36, 0xffff\n\t"
            "s_add_u32 s57, s57, s67\n\t"
            "v_writelane_b32 v236, s57, 63\n\t"
            "v_writelane_b32 v236, s59, m0\n\t"
            "v_cvt_pk_bf16_f32 v179, v182, v183\n\t"
            "v_mov_b64 v[180:181], 0\n\t"
            "v_mov_b64 v[182:183], 0\n\t"
            "s_setprio 3\n\t"
            "v_mfma_f32_16x16x32_bf16 v[192:195], v[238:241], v[176:179], 0\n\t"
            "s_lshr_b64 s[36:37], s[52:53], s58\n\t"
            "s_and_b32 s58, s36, 0xffff\n\t"
            "s_add_u32 s58, s58, s67\n\t"
            "v_mfma_f32_4x4x4_16b_bf16 v[196:199], v[178:179], v[178:179], v[196:199]\n\t"
            "ds_write_b128 v236, v[176:179]\n\t"
            "s_waitcnt lgkmcnt(8)\n\t"
            "v_smfmac_f32_16x16x64_bf16 v[180:183], v[172:175], v[160:167], v255\n\t"
            "s_setprio 1\n\t"
            "s_and_b32 s38, s52, 0xffff\n\t"
            "s_add_u32 s38, s38, s67\n\t"
            "ds_read_b128 v[172:175], v254 offset:448\n\t"
            "ds_read_b64 v[160:161], v253 offset:64\n\t"
            "ds_read_b64 v[162:163], v253 offset:160\n\t"
            "ds_read_b64 v[164:165], v253 offset:1808\n\t"
            "ds_read_b64 v[166:167], v253 offset:1904\n\t"
            "v_mov_b64 v[176:177], 0\n\t"
            "v_mov_b64 v[178:179], 0\n\t"
            "v_cvt_pk_bf16_f32 v184, v184, v185\n\t"
            "v_cvt_pk_bf16_f32 v185, v186, v187\n\t"
            "s_setprio 3\n\t"
            "v_smfmac_f32_16x16x64_bf16 v[176:179], v[168:171], v[152:159], v255\n\t"
            "ds_read_b128 v[168:171], v254 offset:384\n\t"
            "v_mfma_f32_4x4x4_16b_bf16 v[196:199], v[184:185], v[184:185], v[196:199]\n\t"
            "s_setprio 1\n\t"
            "ds_read_b64 v[152:153], v253 offset:56\n\t"
            "s_waitcnt lgkmcnt(14)\n\t"
            "ds_read_b64 v[154:155], v253 offset:152\n\t"
            "s_waitcnt lgkmcnt(14)\n\t"
            "ds_read_b64 v[156:157], v253 offset:1800\n\t"
            "s_waitcnt lgkmcnt(14)\n\t"
            "ds_read_b64 v[158:159], v253 offset:1896\n\t"
            "v_xor_b32 v237, 16, v236\n\t"
            "v_cvt_pk_bf16_f32 v186, v188, v189\n\t"
            "v_cvt_pk_bf16_f32 v187, v190, v191\n\t"
            "v_mov_b64 v[188:189], 0\n\t"
            "v_mov_b64 v[190:191], 0\n\t"
            "s_setprio 3\n\t"
            "v_mfma_f32_16x16x32_bf16 v[192:195], v[238:241], v[184:187], v[192:195]\n\t"
            "s_waitcnt lgkmcnt(14)\n\t"
            "ds_write_b128 v237, v[184:187]\n\t"
            "v_mfma_f32_4x4x4_16b_bf16 v[196:199], v[186:187], v[186:187], v[196:199]\n\t"
            "s_setprio 1\n\t"
            "v_mov_b64 v[184:185], 0\n\t"
            "v_mov_b64 v[186:187], 0\n\t"
            "v_cvt_pk_bf16_f32 v176, v176, v177\n\t"
            "v_cvt_pk_bf16_f32 v177, v178, v179\n\t"
            "v_cvt_pk_bf16_f32 v178, v180, v181\n\t"
            "v_cvt_pk_bf16_f32 v179, v182, v183\n\t"
            "s_setprio 3\n\t"
            "v_mfma_f32_4x4x4_16b_bf16 v[196:199], v[176:177], v[176:177], v[196:199]\n\t"
            "s_waitcnt lgkmcnt(14)\n\t"
            "ds_write_b128 v236, v[176:179] offset:128\n\t"
            "s_waitcnt lgkmcnt(6)\n\t"
            "v_smfmac_f32_16x16x64_bf16 v[184:187], v[168:171], v[144:151], v255\n\t"
            "ds_read_b128 v[168:171], v254 offset:512\n\t"
            "s_waitcnt lgkmcnt(3)\n\t"
            "v_smfmac_f32_16x16x64_bf16 v[188:191], v[172:175], v[152:159], v255\n\t"
            "ds_read_b64 v[144:145], v253 offset:72\n\t"
            "v_mfma_f32_16x16x32_bf16 v[192:195], v[238:241], v[176:179], v[192:195]\n\t"
            "ds_read_b64 v[146:147], v253 offset:168\n\t"
            "ds_read_b64 v[148:149], v253 offset:1816\n\t"
            "v_mfma_f32_4x4x4_16b_bf16 v[196:199], v[178:179], v[178:179], v[196:199]\n\t"
            "s_setprio 1\n\t"
            "ds_read_b64 v[150:151], v253 offset:1912\n\t"
            "ds_read_b128 v[172:175], v254 offset:576\n\t"
            "ds_read_b64 v[152:153], v253 offset:80\n\t"
            "ds_read_b64 v[154:155], v253 offset:176\n\t"
            "ds_read_b64 v[156:157], v253 offset:1824\n\t"
            "ds_read_b64 v[158:159], v253 offset:1920\n\t"
            "v_mov_b64 v[176:177], 0\n\t"
            "v_mov_b64 v[178:179], 0\n\t"
            "v_mov_b64 v[180:181], 0\n\t"
            "v_mov_b64 v[182:183], 0\n\t"
            "v_cvt_pk_bf16_f32 v184, v184, v185\n\t"
            "v_cvt_pk_bf16_f32 v185, v186, v187\n\t"
            "v_cvt_pk_bf16_f32 v186, v188, v189\n\t"
            "v_cvt_pk_bf16_f32 v187, v190, v191\n\t"
            "s_setprio 3\n\t"
            "v_mfma_f32_4x4x4_16b_bf16 v[196:199], v[184:185], v[184:185], v[196:199]\n\t"
            "ds_write_b128 v237, v[184:187] offset:128\n\t"
            "s_waitcnt lgkmcnt(10)\n\t"
            "v_smfmac_f32_16x16x64_bf16 v[176:179], v[168:171], v[160:167], v255\n\t"
            "ds_read_b128 v[168:171], v254 offset:640\n\t"
            "v_mfma_f32_16x16x32_bf16 v[192:195], v[238:241], v[184:187], v[192:195]\n\t"
            "ds_read_b64 v[160:161], v253 offset:88\n\t"
            "ds_read_b64 v[162:163], v253 offset:184\n\t"
            "s_waitcnt lgkmcnt(8)\n\t"
            "v_smfmac_f32_16x16x64_bf16 v[180:183], v[172:175], v[144:151], v255\n\t"
            "ds_read_b64 v[164:165], v253 offset:1832\n\t"
            "v_mfma_f32_4x4x4_16b_bf16 v[196:199], v[186:187], v[186:187], v[196:199]\n\t"
            "s_setprio 1\n\t"
            "ds_read_b64 v[166:167], v253 offset:1928\n\t"
            "ds_read_b128 v[172:175], v254 offset:704\n\t"
            "v_mov_b64 v[184:185], 0\n\t"
            "v_mov_b64 v[186:187], 0\n\t"
            "v_mov_b64 v[188:189], 0\n\t"
            "v_mov_b64 v[190:191], 0\n\t"
            "v_add_u32 v248, s67, v251\n\t"
            "v_writelane_b32 v248, s58, 63\n\t"
            "v_writelane_b32 v248, s38, m0\n\t"
            "v_and_b32 v249, s69, v249\n\t"
            "v_cvt_pk_bf16_f32 v176, v176, v177\n\t"
            "v_cvt_pk_bf16_f32 v177, v178, v179\n\t"
            "v_cvt_pk_bf16_f32 v178, v180, v181\n\t"
            "v_cvt_pk_bf16_f32 v179, v182, v183\n\t"
            "s_setprio 3\n\t"
            "v_mfma_f32_4x4x4_16b_bf16 v[196:199], v[176:177], v[176:177], v[196:199]\n\t"
            "ds_write_b128 v236, v[176:179] offset:256\n\t"
            "s_waitcnt lgkmcnt(6)\n\t"
            "v_smfmac_f32_16x16x64_bf16 v[184:187], v[168:171], v[152:159], v255\n\t"
            "v_mov_b32 v234, 0\n\t"
            "v_mfma_f32_4x4x4_16b_bf16 v[196:199], v[178:179], v[178:179], v[196:199]\n\t"
            "v_xor_b32 v246, 0x100, v246\n\t"
            "v_mfma_f32_16x16x32_bf16 v[192:195], v[238:241], v[176:179], v[192:195]\n\t"
            "s_waitcnt lgkmcnt(1)\n\t"
            "v_smfmac_f32_16x16x64_bf16 v[188:191], v[172:175], v[160:167], v255\n\t"
            "s_mov_b32 exec_hi, 0x3fffff\n\t"
            "s_add_u32 m0, s66, 0\n\t"
            "s_add_u32 s49, s79, 0\n\t"
            "buffer_load_dwordx4 v249, s[72:75], s49 offen lds\n\t"
            "s_add_u32 s49, s68, 195352\n\t"
            "buffer_load_dwordx4 v249, s[72:75], s49 offen offset:872 lds\n\t"
            "s_add_u32 s49, s68, 390704\n\t"
            "buffer_load_dwordx4 v249, s[72:75], s49 offen offset:1744 lds\n\t"
            "s_add_u32 s49, s68, 586056\n\t"
            "buffer_load_dwordx4 v249, s[72:75], s49 offen offset:2616 lds\n\t"
            "s_add_u32 s49, s68, 781408\n\t"
            "buffer_load_dwordx4 v249, s[72:75], s49 offen offset:3488 lds\n\t"
            "s_mov_b32 exec_hi, -1\n\t"
            "v_cvt_pk_bf16_f32 v184, v184, v185\n\t"
            "v_cvt_pk_bf16_f32 v185, v186, v187\n\t"
            "v_cvt_pk_bf16_f32 v186, v188, v189\n\t"
            "v_cvt_pk_bf16_f32 v187, v190, v191\n\t"
            "v_mfma_f32_4x4x4_16b_bf16 v[196:199], v[184:185], v[184:185], v[196:199]\n\t"
            "s_mov_b32 exec_hi, 0x3fffff\n\t"
            "s_add_u32 m0, s66, 4360\n\t"
            "s_add_u32 s49, s68, 981120\n\t"
            "buffer_load_dwordx4 v249, s[72:75], s49 offen lds\n\t"
            "s_add_u32 s49, s68, 1176472\n\t"
            "buffer_load_dwordx4 v249, s[72:75], s49 offen offset:872 lds\n\t"
            "s_add_u32 s49, s68, 1371824\n\t"
            "buffer_load_dwordx4 v249, s[72:75], s49 offen offset:1744 lds\n\t"
            "s_add_u32 s49, s68, 1567176\n\t"
            "buffer_load_dwordx4 v249, s[72:75], s49 offen offset:2616 lds\n\t"
            "s_mov_b32 exec_hi, -1\n\t"
            "v_mfma_f32_4x4x4_16b_bf16 v[196:199], v[186:187], v[186:187], v[196:199]\n\t"
            "ds_write_b128 v237, v[184:187] offset:256\n\t"
            "v_mfma_f32_16x16x32_bf16 v[192:195], v[238:241], v[184:187], v[192:195]\n\t"
            "s_setprio 1\n\t"
            "ds_read_b64 v[236:237], v247\n\t"
            "s_nop 5\n\t"
            "v_cndmask_b32_e64 v193, v196, v197, s[60:61]\n\t"
            "v_cndmask_b32_e64 v193, v193, v198, s[62:63]\n\t"
            "v_cndmask_b32_e64 v193, v193, v199, s[64:65]\n\t"
            "ds_read_b128 v[196:199], v233 offset:6144\n\t"
            "ds_write_b64 v248, v[192:193] offset:18816\n\t"
            "ds_read_b64 v[248:249], v247 offset:8\n\t"
            "ds_read_b128 v[192:195], v232 offset:6144\n\t"
            "s_mov_b64 exec, 1\n\t"
            "ds_inc_u32 v255, v238 offset:7860\n\t"
            "s_mov_b64 exec, -1\n\t"
            "s_waitcnt lgkmcnt(5)\n\t"
            "v_mul_f32 v200, v236, v200\n\t"
            "v_mul_f32 v201, v237, v201\n\t"
            "v_mul_f32 v204, v236, v204\n\t"
            "v_mul_f32 v205, v237, v205\n\t"
            "v_mul_f32 v208, v236, v208\n\t"
            "v_mul_f32 v209, v237, v209\n\t"
            "v_mul_f32 v212, v236, v212\n\t"
            "v_mul_f32 v213, v237, v213\n\t"
            "v_mul_f32 v216, v236, v216\n\t"
            "v_mul_f32 v217, v237, v217\n\t"
            "v_mul_f32 v220, v236, v220\n\t"
            "v_mul_f32 v221, v237, v221\n\t"
            "v_cvt_pk_bf16_f32 v212, v212, v213\n\t"
            "v_cvt_pk_bf16_f32 v208, v208, v209\n\t"
            "v_cvt_pk_bf16_f32 v200, v200, v201\n\t"
            "v_cvt_pk_bf16_f32 v204, v204, v205\n\t"
            "v_cvt_pk_bf16_f32 v216, v216, v217\n\t"
            "v_cvt_pk_bf16_f32 v220, v220, v221\n\t"
            "ds_read_b64 v[236:237], v247 offset:64\n\t"
            "s_waitcnt lgkmcnt(2)\n\t"
            "v_mfma_f32_16x16x32_bf16 v[144:147], v[192:195], v[0:3], 0\n\t"
            "v_mul_f32 v202, v248, v202\n\t"
            "v_mul_f32 v203, v249, v203\n\t"
            "v_mfma_f32_16x16x32_bf16 v[144:147], v[196:199], v[4:7], v[144:147]\n\t"
            "v_mul_f32 v206, v248, v206\n\t"
            "v_mul_f32 v207, v249, v207\n\t"
            "v_mfma_f32_16x16x32_bf16 v[148:151], v[196:199], v[28:31], 0\n\t"
            "v_mul_f32 v210, v248, v210\n\t"
            "v_mul_f32 v211, v249, v211\n\t"
            "v_mfma_f32_16x16x32_bf16 v[148:151], v[192:195], v[24:27], v[148:151]\n\t"
            "v_mul_f32 v214, v248, v214\n\t"
            "v_mul_f32 v215, v249, v215\n\t"
            "v_mfma_f32_16x16x32_bf16 v[168:171], v[192:195], v[48:51], 0\n\t"
            "v_mul_f32 v218, v248, v218\n\t"
            "v_mul_f32 v219, v249, v219\n\t"
            "v_mfma_f32_16x16x32_bf16 v[168:171], v[196:199], v[52:55], v[168:171]\n\t"
            "v_mul_f32 v222, v248, v222\n\t"
            "v_mul_f32 v223, v249, v223\n\t"
            "v_mfma_f32_16x16x32_bf16 v[172:175], v[196:199], v[76:79], 0\n\t"
            "ds_read_b64 v[248:249], v247 offset:72\n\t"
            "v_cvt_pk_bf16_f32 v213, v214, v215\n\t"
            "v_mfma_f32_16x16x32_bf16 v[172:175], v[192:195], v[72:75], v[172:175]\n\t"
            "v_cvt_pk_bf16_f32 v209, v210, v211\n\t"
            "v_cvt_pk_bf16_f32 v201, v202, v203\n\t"
            "v_mfma_f32_16x16x32_bf16 v[152:155], v[192:195], v[96:99], 0\n\t"
            "v_cvt_pk_bf16_f32 v205, v206, v207\n\t"
            "v_cvt_pk_bf16_f32 v217, v218, v219\n\t"
            "v_mfma_f32_16x16x32_bf16 v[152:155], v[196:199], v[100:103], v[152:155]\n\t"
            "v_cvt_pk_bf16_f32 v221, v222, v223\n\t"
            "v_mfma_f32_16x16x32_bf16 v[156:159], v[196:199], v[124:127], 0\n\t"
            "ds_read_b128 v[196:199], v233 offset:6400\n\t"
            "v_mfma_f32_16x16x32_bf16 v[156:159], v[192:195], v[120:123], v[156:159]\n\t"
            "ds_read_b128 v[192:195], v232 offset:6400\n\t"
            "v_mfma_f32_16x16x32_bf16 v[144:147], v[224:227], v[8:11], v[144:147]\n\t"
            "v_mfma_f32_16x16x32_bf16 v[144:147], v[228:231], v[12:15], v[144:147]\n\t"
            "v_mfma_f32_16x16x32_bf16 v[148:151], v[228:231], v[36:39], v[148:151]\n\t"
            "v_mfma_f32_16x16x32_bf16 v[148:151], v[224:227], v[32:35], v[148:151]\n\t"
            "v_mfma_f32_16x16x32_bf16 v[168:171], v[224:227], v[56:59], v[168:171]\n\t"
            "v_mfma_f32_16x16x32_bf16 v[168:171], v[228:231], v[60:63], v[168:171]\n\t"
            "v_mfma_f32_16x16x32_bf16 v[172:175], v[228:231], v[84:87], v[172:175]\n\t"
            "v_mfma_f32_16x16x32_bf16 v[172:175], v[224:227], v[80:83], v[172:175]\n\t"
            "v_mfma_f32_16x16x32_bf16 v[152:155], v[224:227], v[104:107], v[152:155]\n\t"
            "v_mfma_f32_16x16x32_bf16 v[152:155], v[228:231], v[108:111], v[152:155]\n\t"
            "v_mfma_f32_16x16x32_bf16 v[156:159], v[228:231], v[132:135], v[156:159]\n\t"
            "ds_read_b128 v[228:231], v233 offset:12288\n\t"
            "v_mfma_f32_16x16x32_bf16 v[156:159], v[224:227], v[128:131], v[156:159]\n\t"
            "ds_read_b128 v[224:227], v232 offset:12288\n\t"
            "s_waitcnt lgkmcnt(2)\n\t"
            "v_mfma_f32_16x16x32_bf16 v[144:147], v[192:195], v[16:19], v[144:147]\n\t"
            "v_mfma_f32_16x16x32_bf16 v[144:147], v[196:199], v[20:23], v[144:147]\n\t"
            "v_mfma_f32_16x16x32_bf16 v[148:151], v[196:199], v[44:47], v[148:151]\n\t"
            "v_mfma_f32_16x16x32_bf16 v[148:151], v[192:195], v[40:43], v[148:151]\n\t"
            "ds_read_b64 v[234:235], v234 offset:7856\n\t"
            "v_mfma_f32_16x16x32_bf16 v[168:171], v[192:195], v[64:67], v[168:171]\n\t"
            "v_mfma_f32_16x16x32_bf16 v[168:171], v[196:199], v[68:71], v[168:171]\n\t"
            "v_mfma_f32_16x16x32_bf16 v[172:175], v[196:199], v[92:95], v[172:175]\n\t"
            "s_nop 1\n\t"
            "v_mul_f32 v144, v236, v144\n\t"
            "v_mfma_f32_16x16x32_bf16 v[172:175], v[192:195], v[88:91], v[172:175]\n\t"
            "v_mul_f32 v145, v237, v145\n\t"
            "v_mul_f32 v146, v248, v146\n\t"
            "v_mfma_f32_16x16x32_bf16 v[152:155], v[192:195], v[112:115], v[152:155]\n\t"
            "v_mul_f32 v148, v236, v148\n\t"
            "v_mul_f32 v149, v237, v149\n\t"
            "v_mfma_f32_16x16x32_bf16 v[152:155], v[196:199], v[116:119], v[152:155]\n\t"
            "v_mul_f32 v168, v236, v168\n\t"
            "v_mul_f32 v169, v237, v169\n\t"
            "v_mfma_f32_16x16x32_bf16 v[156:159], v[196:199], v[140:143], v[156:159]\n\t"
            "ds_read_b128 v[196:199], v233 offset:12416\n\t"
            "v_mul_f32 v172, v236, v172\n\t"
            "v_mfma_f32_16x16x32_bf16 v[156:159], v[192:195], v[136:139], v[156:159]\n\t"
            "ds_read_b128 v[192:195], v232 offset:12416\n\t"
            "s_waitcnt lgkmcnt(2)\n\t"
            "v_readfirstlane_b32 s77, v234\n\t"
            "v_readfirstlane_b32 s38, v235\n\t"
            "s_add_u32 s77, s77, s38\n\t"
            "s_cmp_ge_u32 s77, s44\n\t"
            "s_cbranch_scc0 .Lwc_spin%=\n\t"
            ".Lwc_go%=:\n\t"
            "v_mfma_f32_16x16x32_bf16 v[160:163], v[224:227], v[0:3], 0\n\t"
            "s_add_u32 s59, s59, s84\n\t"
            "s_movk_i32 s84, 800\n\t"
            "v_mul_f32 v173, v237, v173\n\t"
            "v_mfma_f32_16x16x32_bf16 v[160:163], v[228:231], v[4:7], v[160:163]\n\t"
            "s_add_u32 s45, s46, s27\n\t"
            "s_mov_b32 s46, s47\n\t"
            "s_mov_b32 s47, s68\n\t"
            "v_mfma_f32_16x16x32_bf16 v[164:167], v[228:231], v[28:31], 0\n\t"
            "v_mul_f32 v170, v248, v170\n\t"
            "v_mul_f32 v171, v249, v171\n\t"
            "v_mfma_f32_16x16x32_bf16 v[164:167], v[224:227], v[24:27], v[164:167]\n\t"
            "v_cvt_pk_bf16_f32 v214, v172, v173\n\t"
            "v_cvt_pk_bf16_f32 v210, v168, v169\n\t"
            "v_mfma_f32_16x16x32_bf16 v[176:179], v[224:227], v[48:51], 0\n\t"
            "v_cvt_pk_bf16_f32 v211, v170, v171\n\t"
            "v_mul_f32 v174, v248, v174\n\t"
            "v_mfma_f32_16x16x32_bf16 v[176:179], v[228:231], v[52:55], v[176:179]\n\t"
            "v_mul_f32 v175, v249, v175\n\t"
            "v_add_u32 v172, s67, v245\n\t"
            "v_mfma_f32_16x16x32_bf16 v[180:183], v[228:231], v[76:79], 0\n\t"
            "ds_read_b128 v[168:171], v172 offset:18816\n\t"
            "v_cvt_pk_bf16_f32 v215, v174, v175\n\t"
            "v_mfma_f32_16x16x32_bf16 v[180:183], v[224:227], v[72:75], v[180:183]\n\t"
            "ds_read_b128 v[172:175], v172 offset:19840\n\t"
            "v_mul_f32 v152, v236, v152\n\t"
            "v_mfma_f32_16x16x32_bf16 v[184:187], v[224:227], v[96:99], 0\n\t"
            "v_mul_f32 v153, v237, v153\n\t"
            "v_mul_f32 v156, v236, v156\n\t"
            "v_mfma_f32_16x16x32_bf16 v[184:187], v[228:231], v[100:103], v[184:187]\n\t"
            "v_mul_f32 v157, v237, v157\n\t"
            "v_mul_f32 v147, v249, v147\n\t"
            "v_mfma_f32_16x16x32_bf16 v[188:191], v[228:231], v[124:127], 0\n\t"
            "ds_read_b128 v[228:231], v233 offset:12544\n\t"
            "ds_read_b64 v[236:237], v247 offset:128\n\t"
            "v_mfma_f32_16x16x32_bf16 v[188:191], v[224:227], v[120:123], v[188:191]\n\t"
            "ds_read_b128 v[224:227], v232 offset:12544\n\t"
            "v_mul_f32 v150, v248, v150\n\t"
            "s_waitcnt lgkmcnt(5)\n\t"
            "v_mfma_f32_16x16x32_bf16 v[160:163], v[192:195], v[8:11], v[160:163]\n\t"
            "v_mul_f32 v151, v249, v151\n\t"
            "v_mul_f32 v154, v248, v154\n\t"
            "v_mfma_f32_16x16x32_bf16 v[160:163], v[196:199], v[12:15], v[160:163]\n\t"
            "v_mul_f32 v155, v249, v155\n\t"
            "v_mul_f32 v158, v248, v158\n\t"
            "v_mfma_f32_16x16x32_bf16 v[164:167], v[196:199], v[36:39], v[164:167]\n\t"
            "v_mul_f32 v159, v249, v159\n\t"
            "s_waitcnt lgkmcnt(4)\n\t"
            "v_add_f32 v168, v168, v170\n\t"
            "v_mfma_f32_16x16x32_bf16 v[164:167], v[192:195], v[32:35], v[164:167]\n\t"
            "ds_read_b64 v[248:249], v247 offset:136\n\t"
            "s_waitcnt lgkmcnt(4)\n\t"
            "v_add_f32 v172, v172, v174\n\t"
            "v_mfma_f32_16x16x32_bf16 v[176:179], v[192:195], v[56:59], v[176:179]\n\t"
            "v_add_f32 v168, v168, v172\n\t"
            "v_add_f32 v169, v169, v171\n\t"
            "v_mfma_f32_16x16x32_bf16 v[176:179], v[196:199], v[60:63], v[176:179]\n\t"
            "v_add_f32 v173, v173, v175\n\t"
            "v_mul_f32 v168, 0x3baaaaab, v168\n\t"
            "v_mfma_f32_16x16x32_bf16 v[180:183], v[196:199], v[84:87], v[180:183]\n\t"
            "v_add_f32 v169, v169, v173\n\t"
            "v_mul_f32_e64 v168, v168, -v168\n\t"
            "v_mfma_f32_16x16x32_bf16 v[180:183], v[192:195], v[80:83], v[180:183]\n\t"
            "v_fmac_f32 v168, 0x3baaaaab, v169\n\t"
            "v_max_f32 v168, 0, v168\n\t"
            "v_mfma_f32_16x16x32_bf16 v[184:187], v[192:195], v[104:107], v[184:187]\n\t"
            "v_cvt_pk_bf16_f32 v202, v144, v145\n\t"
            "v_cvt_pk_bf16_f32 v203, v146, v147\n\t"
            "v_mfma_f32_16x16x32_bf16 v[184:187], v[196:199], v[108:111], v[184:187]\n\t"
            "v_cvt_pk_bf16_f32 v206, v148, v149\n\t"
            "v_cvt_pk_bf16_f32 v207, v150, v151\n\t"
            "v_mfma_f32_16x16x32_bf16 v[188:191], v[196:199], v[132:135], v[188:191]\n\t"
            "v_cvt_pk_bf16_f32 v218, v152, v153\n\t"
            "v_cvt_pk_bf16_f32 v219, v154, v155\n\t"
            "v_mfma_f32_16x16x32_bf16 v[188:191], v[192:195], v[128:131], v[188:191]\n\t"
            "v_cvt_pk_bf16_f32 v222, v156, v157\n\t"
            "v_cvt_pk_bf16_f32 v223, v158, v159\n\t"
            "s_waitcnt lgkmcnt(1)\n\t"
            "v_mfma_f32_16x16x32_bf16 v[160:163], v[224:227], v[16:19], v[160:163]\n\t"
            "v_add_f32 v168, 0x3727c5ac, v168\n\t"
            "v_rsq_f32 v168, v168\n\t"
            "v_mfma_f32_16x16x32_bf16 v[160:163], v[228:231], v[20:23], v[160:163]\n\t"
            "ds_write_b128 v243, v[200:203]\n\t"
            "v_mfma_f32_16x16x32_bf16 v[164:167], v[228:231], v[44:47], v[164:167]\n\t"
            "v_add_u32 v232, s67, v242\n\t"
            "v_xor_b32 v233, 64, v232\n\t"
            "v_mfma_f32_16x16x32_bf16 v[164:167], v[224:227], v[40:43], v[164:167]\n\t"
            "s_xor_b32 s48, s48, s71\n\t"
            "s_xor_b32 s67, s67, s71\n\t"
            "s_add_u32 s76, s76, 4\n\t"
            "s_add_u32 s44, s44, 8\n\t"
            "v_mfma_f32_16x16x32_bf16 v[176:179], v[224:227], v[64:67], v[176:179]\n\t"
            "s_lshl_b32 s56, s56, 2\n\t"
            "s_lshr_b32 s49, s26, 30\n\t"
            "s_or_b32 s56, s56, s49\n\t"
            "v_mfma_f32_16x16x32_bf16 v[176:179], v[228:231], v[68:71], v[176:179]\n\t"
            "ds_read_b128 v[192:195], v232\n\t"
            "ds_read_b128 v[196:199], v233\n\t"
            "v_mfma_f32_16x16x32_bf16 v[180:183], v[228:231], v[92:95], v[180:183]\n\t"
            "v_mul_f32 v160, v236, v160\n\t"
            "v_mul_f32 v161, v237, v161\n\t"
            "v_mfma_f32_16x16x32_bf16 v[180:183], v[224:227], v[88:91], v[180:183]\n\t"
            "v_mul_f32 v164, v236, v164\n\t"
            "v_mul_f32 v165, v237, v165\n\t"
            "v_mfma_f32_16x16x32_bf16 v[184:187], v[224:227], v[112:115], v[184:187]\n\t"
            "v_mul_f32 v176, v236, v176\n\t"
            "v_mul_f32 v177, v237, v177\n\t"
            "v_mfma_f32_16x16x32_bf16 v[184:187], v[228:231], v[116:119], v[184:187]\n\t"
            "ds_write_b128 v243, v[204:207] offset:1536\n\t"
            "v_mfma_f32_16x16x32_bf16 v[188:191], v[228:231], v[140:143], v[188:191]\n\t"
            "ds_read_b128 v[228:231], v233 offset:128\n\t"
            "v_mul_f32 v180, v236, v180\n\t"
            "v_mfma_f32_16x16x32_bf16 v[188:191], v[224:227], v[136:139], v[188:191]\n\t"
            "ds_read_b128 v[224:227], v232 offset:128\n\t"
            "v_mul_f32 v181, v237, v181\n\t"
            "s_waitcnt lgkmcnt(3)\n\t"
            "v_mfma_f32_16x16x32_bf16 v[200:203], v[192:195], v[0:3], 0\n\t"
            "v_mul_f32 v184, v236, v184\n\t"
            "v_mul_f32 v185, v237, v185\n\t"
            "v_mfma_f32_16x16x32_bf16 v[200:203], v[196:199], v[4:7], v[200:203]\n\t"
            "ds_write_b128 v243, v[208:211] offset:3072\n\t"
            "v_mfma_f32_16x16x32_bf16 v[204:207], v[196:199], v[28:31], 0\n\t"
            "v_mul_f32 v188, v236, v188\n\t"
            "v_mul_f32 v189, v237, v189\n\t"
            "v_mfma_f32_16x16x32_bf16 v[204:207], v[192:195], v[24:27], v[204:207]\n\t"
            "ds_write_b128 v243, v[212:215] offset:4608\n\t"
            "v_mfma_f32_16x16x32_bf16 v[208:211], v[192:195], v[48:51], 0\n\t"
            "v_mul_f32 v162, v248, v162\n\t"
            "v_mul_f32 v163, v249, v163\n\t"
            "v_mfma_f32_16x16x32_bf16 v[208:211], v[196:199], v[52:55], v[208:211]\n\t"
            "ds_write_b128 v243, v[216:219] offset:6144\n\t"
            "v_mfma_f32_16x16x32_bf16 v[212:215], v[196:199], v[76:79], 0\n\t"
            "v_cvt_pk_bf16_f32 v160, v160, v161\n\t"
            "v_mul_f32 v166, v248, v166\n\t"
            "v_mfma_f32_16x16x32_bf16 v[212:215], v[192:195], v[72:75], v[212:215]\n\t"
            "ds_write_b128 v243, v[220:223] offset:7680\n\t"
            "v_mfma_f32_16x16x32_bf16 v[216:219], v[192:195], v[96:99], 0\n\t"
            "v_mul_f32 v167, v249, v167\n\t"
            "v_cvt_pk_bf16_f32 v164, v164, v165\n\t"
            "v_mfma_f32_16x16x32_bf16 v[216:219], v[196:199], v[100:103], v[216:219]\n\t"
            "v_mul_f32 v178, v248, v178\n\t"
            "v_mul_f32 v179, v249, v179\n\t"
            "v_mfma_f32_16x16x32_bf16 v[220:223], v[196:199], v[124:127], 0\n\t"
            "v_cvt_pk_bf16_f32 v176, v176, v177\n\t"
            "v_mul_f32 v182, v248, v182\n\t"
            "v_mfma_f32_16x16x32_bf16 v[220:223], v[192:195], v[120:123], v[220:223]\n\t"
            "v_mul_f32 v183, v249, v183\n\t"
            "v_cvt_pk_bf16_f32 v180, v180, v181\n\t"
            "s_waitcnt lgkmcnt(4)\n\t"
            "v_mfma_f32_16x16x32_bf16 v[200:203], v[224:227], v[8:11], v[200:203]\n\t"
            "s_waitcnt vmcnt(0)\n\t"
            "v_mul_f32 v186, v248, v186\n\t"
            "v_mul_f32 v187, v249, v187\n\t"
            "v_mfma_f32_16x16x32_bf16 v[200:203], v[228:231], v[12:15], v[200:203]\n\t"
            "v_cvt_pk_bf16_f32 v184, v184, v185\n\t"
            "v_mul_f32 v190, v248, v190\n\t"
            "v_mfma_f32_16x16x32_bf16 v[204:207], v[228:231], v[36:39], v[204:207]\n\t"
            "v_mul_f32 v191, v249, v191\n\t"
            "v_cvt_pk_bf16_f32 v188, v188, v189\n\t"
            "v_mfma_f32_16x16x32_bf16 v[204:207], v[224:227], v[32:35], v[204:207]\n\t"
            "v_cvt_pk_bf16_f32 v161, v162, v163\n\t"
            "v_cvt_pk_bf16_f32 v165, v166, v167\n\t"
            "v_mfma_f32_16x16x32_bf16 v[208:211], v[224:227], v[56:59], v[208:211]\n\t"
            "v_cvt_pk_bf16_f32 v177, v178, v179\n\t"
            "v_cvt_pk_bf16_f32 v181, v182, v183\n\t"
            "v_mfma_f32_16x16x32_bf16 v[208:211], v[228:231], v[60:63], v[208:211]\n\t"
            "v_cvt_pk_bf16_f32 v185, v186, v187\n\t"
            "v_cvt_pk_bf16_f32 v189, v190, v191\n\t"
            "v_mfma_f32_16x16x32_bf16 v[212:215], v[228:231], v[84:87], v[212:215]\n\t"
            "v_xor_b32 v247, 0x100, v247\n\t"
            "v_mfma_f32_16x16x32_bf16 v[212:215], v[224:227], v[80:83], v[212:215]\n\t"
            "ds_write_b64 v244, v[160:161]\n\t"
            "v_mfma_f32_16x16x32_bf16 v[216:219], v[224:227], v[104:107], v[216:219]\n\t"
            "ds_read_b128 v[172:175], v254 offset:64\n\t"
            "ds_read_b64 v[144:145], v253\n\t"
            "v_mfma_f32_16x16x32_bf16 v[216:219], v[228:231], v[108:111], v[216:219]\n\t"
            "ds_write_b64 v244, v[164:165] offset:1536\n\t"
            "v_mfma_f32_16x16x32_bf16 v[220:223], v[228:231], v[132:135], v[220:223]\n\t"
            "ds_read_b128 v[228:231], v233 offset:256\n\t"
            "ds_read_b64 v[146:147], v253 offset:96\n\t"
            "s_setprio 3\n\t"
            "v_mfma_f32_16x16x32_bf16 v[220:223], v[224:227], v[128:131], v[220:223]\n\t"
            "ds_read_b128 v[224:227], v232 offset:256\n\t"
            "ds_write_b64 v244, v[176:177] offset:3072\n\t"
            "ds_write_b64 v244, v[180:181] offset:4608\n\t"
            "ds_write_b64 v244, v[184:185] offset:6144\n\t"
            "ds_write_b64 v244, v[188:189] offset:7680\n\t"
            "s_waitcnt lgkmcnt(14)\n\t"
            "ds_write_b32 v246, v168\n\t"
            "s_waitcnt lgkmcnt(14)\n\t"
            "ds_read_b128 v[168:171], v254\n\t"
            "s_waitcnt lgkmcnt(14)\n\t"
            "ds_read_b64 v[148:149], v253 offset:1744\n\t"
            "s_waitcnt lgkmcnt(14)\n\t"
            "ds_read_b64 v[150:151], v253 offset:1840\n\t"
            "s_waitcnt lgkmcnt(14)\n\t"
            "ds_read_b64 v[152:153], v253 offset:8\n\t"
            "s_waitcnt lgkmcnt(14)\n\t"
            "ds_read_b64 v[154:155], v253 offset:104\n\t"
            "s_waitcnt lgkmcnt(14)\n\t"
            "ds_read_b64 v[156:157], v253 offset:1752\n\t"
            "s_waitcnt lgkmcnt(14)\n\t"
            "ds_read_b64 v[158:159], v253 offset:1848\n\t"
            "s_waitcnt lgkmcnt(14)\n\t"
            "ds_read_b64 v[160:161], v253 offset:16\n\t"
            "s_waitcnt lgkmcnt(14)\n\t"
            "ds_read_b64 v[162:163], v253 offset:112\n\t"
            "s_waitcnt lgkmcnt(14)\n\t"
            "ds_read_b64 v[164:165], v253 offset:1760\n\t"
            "s_waitcnt lgkmcnt(14)\n\t"
            "ds_read_b64 v[166:167], v253 offset:1856\n\t"
            "s_waitcnt lgkmcnt(11)\n\t"
            "s_barrier\n\t"
            "s_cmp_lt_u32 s46, 0x7000000\n\t"
            "s_cbranch_scc1 .Lwc_loop%=\n\t"
            "s_branch .Lwc_end%=\n\t"
            ".Lwc_spin%=:\n\t"
            "s_cmp_eq_u32 s83, 0\n\t"
            "s_cbranch_scc1 .Lwc_go%=\n\t"
            "s_sub_u32 s83, s83, 1\n\t"
            "s_sleep 2\n\t"
            "v_mov_b32 v234, 0\n\t"
            "ds_read_b64 v[234:235], v234 offset:7856\n\t"
            "s_waitcnt lgkmcnt(0)\n\t"
            "v_readfirstlane_b32 s77, v234\n\t"
            "v_readfirstlane_b32 s38, v235\n\t"
            "s_add_u32 s77, s77, s38\n\t"
            "s_cmp_ge_u32 s77, s44\n\t"
            "s_cbranch_scc0 .Lwc_spin%=\n\t"
            "s_branch .Lwc_go%=\n\t"
            ".Lwc_end%=:\n\t"
            "s_waitcnt lgkmcnt(0)\n\t"
            "s_nop 7\n\t"
            : "+{v246}"(aRSw_), "+{v247}"(aRSr_), "+{s44}"(s44), B_SIO
            : "{v242}"(pE_), "{v243}"(aVAw_), "{v244}"(aVBw_), "{v245}"(pST_), "{v250}"(pXw_), "{v251}"(pSTw_), "{v252}"(aCA_), "{v253}"(aB_), "{v254}"(aA_), "{v255}"(idx_), B_SIN, "{s82}"(touch)
            : "v144", "v145", "v146", "v147", "v148", "v149", "v150", "v151", "v152", "v153", "v154", "v155", "v156", "v157", "v158", "v159", "v160", "v161", "v162", "v163", "v164", "v165", "v166", "v167", "v168", "v169", "v170", "v171", "v172", "v173", "v174", "v175", "v176", "v177", "v178", "v179", "v180", "v181", "v182", "v183", "v184", "v185", "v186", "v187", "v188", "v189", "v190", "v191", "v192", "v193", "v194", "v195", "v196", "v197", "v198", "v199", "v236", "v237", "v248", "v249", "v0", "v1", "v2", "v3", "v4", "v5", "v6", "v7", "v8", "v9", "v10", "v11", "v12", "v13", "v14", "v15", "v16", "v17", "v18", "v19", "v20", "v21", "v22", "v23", "v24", "v25", "v26", "v27", "v28", "v29", "v30", "v31", "v32", "v33", "v34", "v35", "v36", "v37", "v38", "v39", "v40", "v41", "v42", "v43", "v44", "v45", "v46", "v47", "v48", "v49", "v50", "v51", "v52", "v53", "v54", "v55", "v56", "v57", "v58", "v59", "v60", "v61", "v62", "v63", "v64", "v65", "v66", "v67", "v68", "v69", "v70", "v71", "v72", "v73", "v74", "v75", "v76", "v77", "v78", "v79", "v80", "v81", "v82", "v83", "v84", "v85", "v86", "v87", "v88", "v89", "v90", "v91", "v92", "v93", "v94", "v95", "v96", "v97", "v98", "v99", "v100", "v101", "v102", "v103", "v104", "v105", "v106", "v107", "v108", "v109", "v110", "v111", "v112", "v113", "v114", "v115", "v116", "v117", "v118", "v119", "v120", "v121", "v122", "v123", "v124", "v125", "v126", "v127", "v128", "v129", "v130", "v131", "v132", "v133", "v134", "v135", "v136", "v137", "v138", "v139", "v140", "v141", "v142", "v143", "v200", "v201", "v202", "v204", "v205", "v206", "v207", "v208", "v209", "v210", "v211", "v212", "v213", "v214", "v215", "v216", "v217", "v218", "v219", "v220", "v221", "v222", "v223", "v224", "v225", "v226", "v227", "v228", "v229", "v230", "v231", "v232", "v233", "v234", "v235", B_SCLOB);
        }
        {
            __builtin_amdgcn_s_setprio(3);
            int ln_;
            asm volatile("v_mbcnt_lo_u32_b32 %0, -1, 0\n\tv_mbcnt_hi_u32_b32 %0, -1, %0" : "=v"(ln_));
            const int l15_ = ln_ & 15, g_ = ln_ >> 4;
            KARGS
            const auto orsrc = __builtin_amdgcn_make_buffer_rsrc((void*)ob, 0, IMG * IMG * CH * 2, 0x00020000);
            const auto wq = __builtin_amdgcn_make_buffer_rsrc((void*)wb, 0, WS_BYTES, 0x00020000);
            bf16x8 W[36];
#define WO_LOAD(k0)                                                                                \
    SB();                                                                                          \
    _Pragma("unroll") for (int ks = (k0); ks < (k0) + 2; ++ks)                                     \
        _Pragma("unroll") for (int j = 0; j < 6; ++j)                                              \
            W[j * 6 + ks] = BC8(__builtin_amdgcn_raw_buffer_load_b128(wq, ln_ * 16, (wave + 2) * 36864 + (j * 6 + ks) * 1024, SC1)); \
    SB();
            int lE[2], nw;
            unsigned o[2];
            f32x4 acc[2][6];
#pragma unroll
            for (int lt = 0; lt < 2; ++lt) {
                WO_LOAD(2 * lt)
                int i = 16 * lt + l15_;
                const unsigned en = *(const unsigned*)(smem + WL_OFF + i * 4);
                const bool ok = en != 0x7ffffffu;
                if (lt == 1) nw = 16 + __builtin_popcount((unsigned)__builtin_amdgcn_ballot_w64(ok) & 0xffffu);
                i = ok ? i : 0;
                lE[lt] = OL_OFF + i * 384 + ((g_ ^ ((i >> 1) & 7)) << 4);
                o[lt] = ok ? (en & 0x7ffffffu) + LPOS(en >> 31) * ROWBYTES + LPOS(en >> 30) * (CH * 2) + 192 * w + 16 * g_ : OOB;
#pragma unroll
                for (int k = 0; k < 3; ++k) {
                    const int c8 = 12 * w + 4 * k + g_, ws = (c8 * 43) >> 8, r6 = c8 - 6 * ws;
                    const u32x4 r = *(const u32x4*)(smem + LX_OFF(i) + (r6 >> 1) * 128 + ((2 * ws + (r6 & 1)) << 4));
                    acc[lt][2 * k] = (f32x4){bflo(r[0]), bfhi(r[0]), bflo(r[1]), bfhi(r[1])};
                    acc[lt][2 * k + 1] = (f32x4){bflo(r[2]), bfhi(r[2]), bflo(r[3]), bfhi(r[3])};
                }
            }
            bf16x8 of[2][6];
#pragma unroll
            for (int ks = 0; ks < 6; ++ks) of[0][ks] = *(const bf16x8*)(smem + ((ks & 1) ? (lE[0] ^ 64) : lE[0]) + (ks >> 1) * 128);
            WO_LOAD(4)
#pragma unroll
            for (int i = 0; i < 12; ++i) OPQ(acc[i / 6][i % 6]);
            OPQ(lE[1]);
#pragma unroll 1
            for (int it_ = 0; it_ < 100000; ++it_) {
                unsigned n_;
                asm volatile("ds_read_b32 %0, %1\n\ts_waitcnt lgkmcnt(0)" : "=v"(n_) : "v"(ldsb + FLAG_OFF) : "memory");
                if (__builtin_amdgcn_readfirstlane(n_) >= 4 * nw) break;
                __builtin_amdgcn_s_sleep(1);
            }
#pragma unroll
            for (int ks = 0; ks < 6; ++ks) of[1][ks] = *(const bf16x8*)(smem + ((ks & 1) ? (lE[1] ^ 64) : lE[1]) + (ks >> 1) * 128);
            SB();
#pragma unroll
            for (int lt = 0; lt < 2; ++lt) {
#pragma unroll
                for (int ks = 0; ks < 6; ++ks)
#pragma unroll
                    for (int j = 0; j < 6; ++j) acc[lt][j] = MFMA32(W[j * 6 + ks], of[lt][ks], acc[lt][j]);
#pragma unroll
                for (int k = 0; k < 3; ++k) {
                    const f32x4 a = acc[lt][2 * k], b = acc[lt][2 * k + 1];
                    const u32x4 ov = {pk2(a[0], a[1]), pk2(a[2], a[3]), pk2(b[0], b[1]), pk2(b[2], b[3])};
                    __builtin_amdgcn_raw_buffer_store_b128(ov, orsrc, o[lt] + 64 * k, 0, SC1);
                }
            }
        }
        __builtin_amdgcn_endpgm();
    } else {
        LANE_SETUP
        {
            const unsigned oE_ = ldsb + fE + (O_OFF - X_OFF), oO_ = oE_ ^ 64;
            unsigned l16_ = lane * 16;
            unsigned pr[3];
#pragma unroll
            for (int k = 0; k < 3; ++k) {
                const int c8 = 12 * (w - 2) + 4 * k + g;
                const int ws = c8 / 6, r6 = c8 - 6 * ws;
                pr[k] = l15 * 384 + (r6 >> 1) * 128 + (((2 * ws + (r6 & 1)) ^ sw) << 4);
            }
            const unsigned pR0_ = pr[0], pR1_ = pr[1], pR2_ = pr[2];
            unsigned q0 = ldsb + PF_OFF + 1024 + lane * 4, q1 = ldsb + O_OFF + lane * 16, q2 = oE_, q3 = ldsb + PF_OFF + 1024 + l15 * 4;
            unsigned q4 = ldsb + OL_OFF + l15 * 384 + (((4 * (w - 2) + (g >> 1)) ^ sw) << 4) + 8 * (g & 1);
            B_SCALARS
            asm volatile(
            "s_waitcnt lgkmcnt(0)\n\t"
            "s_mov_b32 s83, 0x10000\n\t"
            "s_mov_b32 s84, -47104\n\t"
            "s_nop 7\n\t"
            "v_mov_b32 v200, v250\n\t"
            "v_mov_b32 v201, v251\n\t"
            "v_mov_b32 v202, v253\n\t"
            "s_mov_b32 s38, s67\n\t"
            "s_mov_b32 s67, 74496\n\t"
            "ds_read_b64 v[250:251], v203\n\t"
            "ds_read_b32 v253, v203 offset:8\n\t"
            ".Lwk_b1%=:\n\t"
            "v_mov_b32 v236, 0\n\t"
            "ds_read_b32 v236, v236 offset:7864\n\t"
            "s_waitcnt lgkmcnt(0)\n\t"
            "v_readfirstlane_b32 s77, v236\n\t"
            "s_cmp_ge_u32 s77, 4\n\t"
            "s_cbranch_scc1 .Lwk_b2%=\n\t"
            "s_cmp_eq_u32 s83, 0\n\t"
            "s_cbranch_scc1 .Lwk_b2%=\n\t"
            "s_sub_u32 s83, s83, 1\n\t"
            "s_sleep 1\n\t"
            "s_branch .Lwk_b1%=\n\t"
            ".Lwk_b2%=:\n\t"
            "s_waitcnt vmcnt(0)\n\t"
            "s_call_b64 vcc, wk_conv\n\t"
            "v_mov_b32 v250, v200\n\t"
            "v_mov_b32 v251, v201\n\t"
            "v_mov_b32 v253, v202\n\t"
            "s_mov_b32 s67, s38\n\t"
            "s_barrier\n\t"
            "ds_read_b64 v[236:237], v252 offset:2688\n\t"
            "s_waitcnt lgkmcnt(0)\n\t"
            "s_mov_b64 exec, 1\n\t"
            "buffer_store_dwordx2 v[236:237], off, s[28:31], s44 sc1\n\t"
            "s_mov_b64 exec, -1\n\t"
            "s_sleep 30\n\t"
            "ds_read_b32 v249, v252 offset:12\n\t"
            "s_waitcnt lgkmcnt(0)\n\t"
            "v_and_b32 v249, s69, v249\n\t"
            "s_mov_b32 exec_hi, 0x3fffff\n\t"
            "s_add_u32 m0, s66, 0\n\t"
            "s_add_u32 s49, s79, 0\n\t"
            "buffer_load_dwordx4 v249, s[72:75], s49 offen lds\n\t"
            "s_mov_b32 exec_hi, -1\n\t"
            "s_mov_b32 exec_hi, 0x3fffff\n\t"
            "s_add_u32 m0, s66, 872\n\t"
            "s_add_u32 s49, s47, 196224\n\t"
            "buffer_load_dwordx4 v249, s[72:75], s49 offen lds\n\t"
            "s_mov_b32 exec_hi, -1\n\t"
            "s_mov_b32 exec_hi, 0x3fffff\n\t"
            "s_add_u32 m0, s66, 1744\n\t"
            "s_add_u32 s49, s47, 392448\n\t"
            "buffer_load_dwordx4 v249, s[72:75], s49 offen lds\n\t"
            "s_mov_b32 exec_hi, -1\n\t"
            "s_mov_b32 exec_hi, 0x3fffff\n\t"
            "s_add_u32 m0, s66, 2616\n\t"
            "s_add_u32 s49, s47, 588672\n\t"
            "buffer_load_dwordx4 v249, s[72:75], s49 offen lds\n\t"
            "s_mov_b32 exec_hi, -1\n\t"
            "s_mov_b32 exec_hi, 0x3fffff\n\t"
            "s_add_u32 m0, s66, 3488\n\t"
            "s_add_u32 s49, s47, 784896\n\t"
            "buffer_load_dwordx4 v249, s[72:75], s49 offen lds\n\t"
            "s_mov_b32 exec_hi, -1\n\t"
            "s_mov_b32 exec_hi, 0x3fffff\n\t"
            "s_add_u32 m0, s66, 4360\n\t"
            "s_add_u32 s49, s47, 981120\n\t"
            "buffer_load_dwordx4 v249, s[72:75], s49 offen lds\n\t"
            "s_mov_b32 exec_hi, -1\n\t"
            "s_mov_b32 exec_hi, 0x3fffff\n\t"
            "s_add_u32 m0, s66, 5232\n\t"
            "s_add_u32 s49, s47, 1177344\n\t"
            "buffer_load_dwordx4 v249, s[72:75], s49 offen lds\n\t"
            "s_mov_b32 exec_hi, -1\n\t"
            "s_mov_b32 exec_hi, 0x3fffff\n\t"
            "s_add_u32 m0, s66, 6104\n\t"
            "s_add_u32 s49, s47, 1373568\n\t"
            "buffer_load_dwordx4 v249, s[72:75], s49 offen lds\n\t"
            "s_mov_b32 exec_hi, -1\n\t"
            "s_mov_b32 exec_hi, 0x3fffff\n\t"
            "s_add_u32 m0, s66, 6976\n\t"
            "s_add_u32 s49, s47, 1569792\n\t"
            "buffer_load_dwordx4 v249, s[72:75], s49 offen lds\n\t"
            "s_mov_b32 exec_hi, -1\n\t"
            "s_add_u32 s49, s33, -73728\n\t"
            "buffer_load_dwordx4 v[0:3], v247, s[28:31], s49 offen sc1\n\t"
            "s_add_u32 s49, s33, -67584\n\t"
            "buffer_load_dwordx4 v[24:27], v247, s[28:31], s49 offen sc1\n\t"
            "s_add_u32 s49, s33, -61440\n\t"
            "buffer_load_dwordx4 v[48:51], v247, s[28:31], s49 offen sc1\n\t"
            "s_add_u32 s49, s33, -55296\n\t"
            "buffer_load_dwordx4 v[72:75], v247, s[28:31], s49 offen sc1\n\t"
            "s_add_u32 s49, s33, -49152\n\t"
            "buffer_load_dwordx4 v[96:99], v247, s[28:31], s49 offen sc1\n\t"
            "s_add_u32 s49, s33, -43008\n\t"
            "buffer_load_dwordx4 v[120:123], v247, s[28:31], s49 offen sc1\n\t"
            "s_add_u32 s49, s33, -72704\n\t"
            "buffer_load_dwordx4 v[4:7], v247, s[28:31], s49 offen sc1\n\t"
            "s_add_u32 s49, s33, -66560\n\t"
            "buffer_load_dwordx4 v[28:31], v247, s[28:31], s49 offen sc1\n\t"
            "s_add_u32 s49, s33, -60416\n\t"
            "buffer_load_dwordx4 v[52:55], v247, s[28:31], s49 offen sc1\n\t"
            "s_add_u32 s49, s33, -54272\n\t"
            "buffer_load_dwordx4 v[76:79], v247, s[28:31], s49 offen sc1\n\t"
            "s_add_u32 s49, s33, -48128\n\t"
            "buffer_load_dwordx4 v[100:103], v247, s[28:31], s49 offen sc1\n\t"
            "s_add_u32 s49, s33, -41984\n\t"
            "buffer_load_dwordx4 v[124:127], v247, s[28:31], s49 offen sc1\n\t"
            "s_add_u32 s49, s33, -71680\n\t"
            "buffer_load_dwordx4 v[8:11], v247, s[28:31], s49 offen sc1\n\t"
            "s_add_u32 s49, s33, -65536\n\t"
            "buffer_load_dwordx4 v[32:35], v247, s[28:31], s49 offen sc1\n\t"
            "s_add_u32 s49, s33, -59392\n\t"
            "buffer_load_dwordx4 v[56:59], v247, s[28:31], s49 offen sc1\n\t"
            "s_add_u32 s49, s33, -53248\n\t"
            "buffer_load_dwordx4 v[80:83], v247, s[28:31], s49 offen sc1\n\t"
            "s_add_u32 s49, s33, -47104\n\t"
            "buffer_load_dwordx4 v[104:107], v247, s[28:31], s49 offen sc1\n\t"
            "s_add_u32 s49, s33, -40960\n\t"
            "buffer_load_dwordx4 v[128:131], v247, s[28:31], s49 offen sc1\n\t"
            "s_add_u32 s49, s33, -70656\n\t"
            "buffer_load_dwordx4 v[12:15], v247, s[28:31], s49 offen sc1\n\t"
            "s_add_u32 s49, s33, -64512\n\t"
            "buffer_load_dwordx4 v[36:39], v247, s[28:31], s49 offen sc1\n\t"
            "s_add_u32 s49, s33, -58368\n\t"
            "buffer_load_dwordx4 v[60:63], v247, s[28:31], s49 offen sc1\n\t"
            "s_add_u32 s49, s33, -52224\n\t"
            "buffer_load_dwordx4 v[84:87], v247, s[28:31], s49 offen sc1\n\t"
            "s_add_u32 s49, s33, -46080\n\t"
            "buffer_load_dwordx4 v[108:111], v247, s[28:31], s49 offen sc1\n\t"
            "s_add_u32 s49, s33, -39936\n\t"
            "buffer_load_dwordx4 v[132:135], v247, s[28:31], s49 offen sc1\n\t"
            "s_add_u32 s49, s33, -69632\n\t"
            "buffer_load_dwordx4 v[16:19], v247, s[28:31], s49 offen sc1\n\t"
            "s_add_u32 s49, s33, -63488\n\t"
            "buffer_load_dwordx4 v[40:43], v247, s[28:31], s49 offen sc1\n\t"
            "s_add_u32 s49, s33, -57344\n\t"
            "buffer_load_dwordx4 v[64:67], v247, s[28:31], s49 offen sc1\n\t"
            "s_add_u32 s49, s33, -51200\n\t"
            "buffer_load_dwordx4 v[88:91], v247, s[28:31], s49 offen sc1\n\t"
            "s_add_u32 s49, s33, -45056\n\t"
            "buffer_load_dwordx4 v[112:115], v247, s[28:31], s49 offen sc1\n\t"
            "s_add_u32 s49, s33, -38912\n\t"
            "buffer_load_dwordx4 v[136:139], v247, s[28:31], s49 offen sc1\n\t"
            "s_add_u32 s49, s33, -68608\n\t"
            "buffer_load_dwordx4 v[20:23], v247, s[28:31], s49 offen sc1\n\t"
            "s_add_u32 s49, s33, -62464\n\t"
            "buffer_load_dwordx4 v[44:47], v247, s[28:31], s49 offen sc1\n\t"
            "s_add_u32 s49, s33, -56320\n\t"
            "buffer_load_dwordx4 v[68:71], v247, s[28:31], s49 offen sc1\n\t"
            "s_add_u32 s49, s33, -50176\n\t"
            "buffer_load_dwordx4 v[92:95], v247, s[28:31], s49 offen sc1\n\t"
            "s_add_u32 s49, s33, -44032\n\t"
            "buffer_load_dwordx4 v[116:119], v247, s[28:31], s49 offen sc1\n\t"
            "s_add_u32 s49, s33, -37888\n\t"
            "buffer_load_dwordx4 v[140:143], v247, s[28:31], s49 offen sc1\n\t"
            "s_setprio 3\n\t"
            "ds_read_b128 v[168:171], v254\n\t"
            "ds_read_b128 v[172:175], v254 offset:64\n\t"
            "ds_read_b128 v[220:223], v229 offset:12288\n\t"
            "ds_read_b128 v[224:227], v229 offset:13312\n\t"
            "ds_read_b128 v[200:203], v232\n\t"
            "ds_read_b128 v[208:211], v232 offset:128\n\t"
            "ds_read_b128 v[216:219], v232 offset:256\n\t"
            "s_waitcnt vmcnt(36)\n\t"
            "ds_read_b64 v[144:145], v253\n\t"
            "ds_read_b64 v[146:147], v253 offset:96\n\t"
            "ds_read_b64 v[148:149], v253 offset:1744\n\t"
            "ds_read_b64 v[150:151], v253 offset:1840\n\t"
            "ds_read_b64 v[152:153], v253 offset:8\n\t"
            "ds_read_b64 v[154:155], v253 offset:104\n\t"
            "ds_read_b64 v[156:157], v253 offset:1752\n\t"
            "ds_read_b64 v[158:159], v253 offset:1848\n\t"
            "s_waitcnt lgkmcnt(14)\n\t"
            "ds_read_b64 v[160:161], v253 offset:16\n\t"
            "s_waitcnt lgkmcnt(14)\n\t"
            "ds_read_b64 v[162:163], v253 offset:112\n\t"
            "s_waitcnt lgkmcnt(14)\n\t"
            "ds_read_b64 v[164:165], v253 offset:1760\n\t"
            "s_waitcnt lgkmcnt(14)\n\t"
            "ds_read_b64 v[166:167], v253 offset:1856\n\t"
            "v_mov_b64 v[176:177], 0\n\t"
            "v_mov_b64 v[178:179], 0\n\t"
            "v_mov_b64 v[180:181], 0\n\t"
            "v_mov_b64 v[182:183], 0\n\t"
            "v_mov_b32 v249, s76\n\t"
            "v_mov_b64 v[184:185], 0\n\t"
            "v_mov_b64 v[186:187], 0\n\t"
            "v_mov_b64 v[188:189], 0\n\t"
            "v_mov_b64 v[190:191], 0\n\t"
            "v_add_u32 v236, s67, v250\n\t"
            "v_add_u32 v248, s67, v251\n\t"
            "v_xor_b32 v235, 64, v232\n\t"
            "s_nop 0\n\t"
            "s_waitcnt lgkmcnt(14)\n\t"
            "ds_read_b32 v249, v249\n\t"
            "s_waitcnt lgkmcnt(14)\n\t"
            "ds_read_b128 v[204:207], v235\n\t"
            "s_waitcnt lgkmcnt(14)\n\t"
            "ds_read_b128 v[212:215], v235 offset:128\n\t"
            "v_add_f32 v220, v220, v222\n\t"
            "v_add_f32 v224, v224, v226\n\t"
            "v_add_f32 v220, v220, v224\n\t"
            "v_add_f32 v221, v221, v223\n\t"
            "v_add_f32 v225, v225, v227\n\t"
            "s_waitcnt lgkmcnt(11)\n\t"
            "v_smfmac_f32_16x16x64_bf16 v[176:179], v[168:171], v[144:151], v255\n\t"
            "ds_read_b128 v[168:171], v254 offset:128\n\t"
            "s_waitcnt lgkmcnt(8)\n\t"
            "v_smfmac_f32_16x16x64_bf16 v[180:183], v[172:175], v[152:159], v255\n\t"
            "ds_read_b64 v[144:145], v253 offset:24\n\t"
            "ds_read_b64 v[146:147], v253 offset:120\n\t"
            "ds_read_b64 v[148:149], v253 offset:1768\n\t"
            "ds_read_b64 v[150:151], v253 offset:1864\n\t"
            "ds_read_b128 v[172:175], v254 offset:192\n\t"
            "ds_read_b64 v[152:153], v253 offset:32\n\t"
            "ds_read_b64 v[154:155], v253 offset:128\n\t"
            "s_waitcnt lgkmcnt(14)\n\t"
            "ds_read_b64 v[156:157], v253 offset:1776\n\t"
            "s_waitcnt lgkmcnt(14)\n\t"
            "ds_read_b64 v[158:159], v253 offset:1872\n\t"
            "v_cvt_pk_bf16_f32 v176, v176, v177\n\t"
            "v_cvt_pk_bf16_f32 v177, v178, v179\n\t"
            "v_cvt_pk_bf16_f32 v178, v180, v181\n\t"
            "v_cvt_pk_bf16_f32 v179, v182, v183\n\t"
            "v_mfma_f32_4x4x4_16b_bf16 v[196:199], v[176:177], v[176:177], 0\n\t"
            "v_mov_b64 v[180:181], 0\n\t"
            "v_mfma_f32_16x16x32_bf16 v[192:195], v[238:241], v[176:179], 0\n\t"
            "s_waitcnt lgkmcnt(12)\n\t"
            "v_readfirstlane_b32 s26, v249\n\t"
            "v_mov_b64 v[182:183], 0\n\t"
            "v_mfma_f32_4x4x4_16b_bf16 v[196:199], v[178:179], v[178:179], v[196:199]\n\t"
            "ds_read_b32 v249, v252 offset:12\n\t"
            "s_and_b32 s68, s26, 0x7ffffff\n\t"
            "s_add_u32 s68, s68, s78\n\t"
            "s_and_b32 s69, s26, 0x38000000\n\t"
            "s_or_b32 s69, s69, s39\n\t"
            "s_bitcmp1_b32 s26, 27\n\t"
            "s_cselect_b32 s79, 2.0, s68\n\t"
            "s_waitcnt lgkmcnt(10)\n\t"
            "v_smfmac_f32_16x16x64_bf16 v[184:187], v[168:171], v[160:167], v255\n\t"
            "s_and_b32 s57, s56, 3\n\t"
            "s_lshl_b32 s58, s57, 3\n\t"
            "s_lshr_b32 m0, s54, s58\n\t"
            "ds_read_b128 v[168:171], v254 offset:256\n\t"
            "ds_read_b64 v[160:161], v253 offset:40\n\t"
            "ds_read_b64 v[162:163], v253 offset:136\n\t"
            "ds_read_b64 v[164:165], v253 offset:1784\n\t"
            "s_waitcnt lgkmcnt(9)\n\t"
            "v_smfmac_f32_16x16x64_bf16 v[188:191], v[172:175], v[144:151], v255\n\t"
            "s_lshl_b32 s58, s57, 4\n\t"
            "s_lshr_b64 s[36:37], s[50:51], s58\n\t"
            "s_and_b32 s57, s36, 0xffff\n\t"
            "s_add_u32 s57, s57, s67\n\t"
            "ds_read_b64 v[166:167], v253 offset:1880\n\t"
            "ds_read_b128 v[172:175], v254 offset:320\n\t"
            "ds_read_b64 v[144:145], v253 offset:48\n\t"
            "ds_read_b64 v[146:147], v253 offset:144\n\t"
            "ds_read_b64 v[148:149], v253 offset:1792\n\t"
            "ds_read_b64 v[150:151], v253 offset:1888\n\t"
            "v_writelane_b32 v236, s57, 63\n\t"
            "v_writelane_b32 v236, s59, m0\n\t"
            "v_cvt_pk_bf16_f32 v184, v184, v185\n\t"
            "v_cvt_pk_bf16_f32 v185, v186, v187\n\t"
            "v_xor_b32 v237, 16, v236\n\t"
            "v_cvt_pk_bf16_f32 v186, v188, v189\n\t"
            "v_mfma_f32_4x4x4_16b_bf16 v[196:199], v[184:185], v[184:185], v[196:199]\n\t"
            "s_waitcnt lgkmcnt(14)\n\t"
            "ds_write_b128 v236, v[176:179]\n\t"
            "v_mov_b64 v[176:177], 0\n\t"
            "v_mov_b64 v[178:179], 0\n\t"
            "v_cvt_pk_bf16_f32 v187, v190, v191\n\t"
            "v_mov_b64 v[188:189], 0\n\t"
            "s_waitcnt lgkmcnt(10)\n\t"
            "v_smfmac_f32_16x16x64_bf16 v[176:179], v[168:171], v[152:159], v255\n\t"
            "s_lshr_b64 s[36:37], s[52:53], s58\n\t"
            "s_and_b32 s58, s36, 0xffff\n\t"
            "s_add_u32 s58, s58, s67\n\t"
            "v_mfma_f32_16x16x32_bf16 v[192:195], v[238:241], v[184:187], v[192:195]\n\t"
            "s_and_b32 s38, s52, 0xffff\n\t"
            "s_add_u32 s38, s38, s67\n\t"
            "ds_read_b128 v[168:171], v254 offset:384\n\t"
            "v_mfma_f32_4x4x4_16b_bf16 v[196:199], v[186:187], v[186:187], v[196:199]\n\t"
            "ds_read_b64 v[152:153], v253 offset:56\n\t"
            "s_waitcnt lgkmcnt(7)\n\t"
            "v_smfmac_f32_16x16x64_bf16 v[180:183], v[172:175], v[160:167], v255\n\t"
            "ds_read_b64 v[154:155], v253 offset:152\n\t"
            "ds_read_b64 v[156:157], v253 offset:1800\n\t"
            "ds_read_b64 v[158:159], v253 offset:1896\n\t"
            "ds_read_b128 v[172:175], v254 offset:448\n\t"
            "ds_read_b64 v[160:161], v253 offset:64\n\t"
            "ds_read_b64 v[162:163], v253 offset:160\n\t"
            "ds_read_b64 v[164:165], v253 offset:1808\n\t"
            "ds_read_b64 v[166:167], v253 offset:1904\n\t"
            "s_waitcnt lgkmcnt(14)\n\t"
            "ds_write_b128 v237, v[184:187]\n\t"
            "v_mov_b64 v[184:185], 0\n\t"
            "v_mov_b64 v[186:187], 0\n\t"
            "v_mov_b64 v[190:191], 0\n\t"
            "v_cvt_pk_bf16_f32 v176, v176, v177\n\t"
            "v_cvt_pk_bf16_f32 v177, v178, v179\n\t"
            "v_cvt_pk_bf16_f32 v178, v180, v181\n\t"
            "v_cvt_pk_bf16_f32 v179, v182, v183\n\t"
            "v_mfma_f32_4x4x4_16b_bf16 v[196:199], v[176:177], v[176:177], v[196:199]\n\t"
            "s_waitcnt lgkmcnt(14)\n\t"
            "ds_write_b128 v236, v[176:179] offset:128\n\t"
            "s_waitcnt lgkmcnt(11)\n\t"
            "v_smfmac_f32_16x16x64_bf16 v[184:187], v[168:171], v[144:151], v255\n\t"
            "ds_read_b128 v[168:171], v254 offset:512\n\t"
            "v_mfma_f32_16x16x32_bf16 v[192:195], v[238:241], v[176:179], v[192:195]\n\t"
            "ds_read_b64 v[144:145], v253 offset:72\n\t"
            "ds_read_b64 v[146:147], v253 offset:168\n\t"
            "s_waitcnt lgkmcnt(9)\n\t"
            "v_smfmac_f32_16x16x64_bf16 v[188:191], v[172:175], v[152:159], v255\n\t"
            "ds_read_b64 v[148:149], v253 offset:1816\n\t"
            "v_mfma_f32_4x4x4_16b_bf16 v[196:199], v[178:179], v[178:179], v[196:199]\n\t"
            "ds_read_b64 v[150:151], v253 offset:1912\n\t"
            "ds_read_b128 v[172:175], v254 offset:576\n\t"
            "ds_read_b64 v[152:153], v253 offset:80\n\t"
            "ds_read_b64 v[154:155], v253 offset:176\n\t"
            "ds_read_b64 v[156:157], v253 offset:1824\n\t"
            "s_waitcnt lgkmcnt(14)\n\t"
            "ds_read_b64 v[158:159], v253 offset:1920\n\t"
            "v_mov_b64 v[176:177], 0\n\t"
            "v_mov_b64 v[178:179], 0\n\t"
            "v_mov_b64 v[180:181], 0\n\t"
            "v_mov_b64 v[182:183], 0\n\t"
            "v_cvt_pk_bf16_f32 v184, v184, v185\n\t"
            "v_cvt_pk_bf16_f32 v185, v186, v187\n\t"
            "v_cvt_pk_bf16_f32 v186, v188, v189\n\t"
            "v_cvt_pk_bf16_f32 v187, v190, v191\n\t"
            "v_mfma_f32_4x4x4_16b_bf16 v[196:199], v[184:185], v[184:185], v[196:199]\n\t"
            "s_waitcnt lgkmcnt(14)\n\t"
            "ds_write_b128 v237, v[184:187] offset:128\n\t"
            "v_mfma_f32_16x16x32_bf16 v[192:195], v[238:241], v[184:187], v[192:195]\n\t"
            "v_mov_b64 v[184:185], 0\n\t"
            "v_mov_b64 v[188:189], 0\n\t"
            "s_waitcnt lgkmcnt(10)\n\t"
            "v_smfmac_f32_16x16x64_bf16 v[176:179], v[168:171], v[160:167], v255\n\t"
            "ds_read_b128 v[168:171], v254 offset:640\n\t"
            "v_mfma_f32_4x4x4_16b_bf16 v[196:199], v[186:187], v[186:187], v[196:199]\n\t"
            "ds_read_b64 v[160:161], v253 offset:88\n\t"
            "ds_read_b64 v[162:163], v253 offset:184\n\t"
            "ds_read_b64 v[164:165], v253 offset:1832\n\t"
            "s_waitcnt lgkmcnt(9)\n\t"
            "v_smfmac_f32_16x16x64_bf16 v[180:183], v[172:175], v[144:151], v255\n\t"
            "ds_read_b64 v[166:167], v253 offset:1928\n\t"
            "ds_read_b128 v[172:175], v254 offset:704\n\t"
            "v_mov_b64 v[186:187], 0\n\t"
            "v_mov_b64 v[190:191], 0\n\t"
            "v_writelane_b32 v248, s58, 63\n\t"
            "v_writelane_b32 v248, s38, m0\n\t"
            "v_and_b32 v249, s69, v249\n\t"
            "v_cvt_pk_bf16_f32 v176, v176, v177\n\t"
            "v_cvt_pk_bf16_f32 v177, v178, v179\n\t"
            "v_cvt_pk_bf16_f32 v178, v180, v181\n\t"
            "v_cvt_pk_bf16_f32 v179, v182, v183\n\t"
            "v_mfma_f32_4x4x4_16b_bf16 v[196:199], v[176:177], v[176:177], v[196:199]\n\t"
            "v_mul_f32 v220, 0x3baaaaab, v220\n\t"
            "v_mfma_f32_16x16x32_bf16 v[192:195], v[238:241], v[176:179], v[192:195]\n\t"
            "v_add_f32 v221, v221, v225\n\t"
            "v_mul_f32_e64 v220, v220, -v220\n\t"
            "v_mfma_f32_4x4x4_16b_bf16 v[196:199], v[178:179], v[178:179], v[196:199]\n\t"
            "ds_read_b128 v[224:227], v232 offset:6144\n\t"
            "v_fmac_f32 v220, 0x3baaaaab, v221\n\t"
            "v_max_f32 v220, 0, v220\n\t"
            "v_add_f32 v220, 0x3727c5ac, v220\n\t"
            "v_rsq_f32 v220, v220\n\t"
            "s_waitcnt lgkmcnt(6)\n\t"
            "v_smfmac_f32_16x16x64_bf16 v[184:187], v[168:171], v[152:159], v255\n\t"
            "ds_write_b32 v228, v220\n\t"
            "ds_read_b128 v[220:223], v235 offset:256\n\t"
            "ds_read_b32 v230, v233\n\t"
            "ds_read_b32 v231, v233 offset:64\n\t"
            "s_waitcnt lgkmcnt(5)\n\t"
            "v_smfmac_f32_16x16x64_bf16 v[188:191], v[172:175], v[160:167], v255\n\t"
            "s_mov_b32 exec_hi, 0x3fffff\n\t"
            "s_add_u32 m0, s66, 0\n\t"
            "s_add_u32 s49, s79, 0\n\t"
            "buffer_load_dwordx4 v249, s[72:75], s49 offen lds\n\t"
            "s_add_u32 s49, s68, 195352\n\t"
            "buffer_load_dwordx4 v249, s[72:75], s49 offen offset:872 lds\n\t"
            "s_add_u32 s49, s68, 390704\n\t"
            "buffer_load_dwordx4 v249, s[72:75], s49 offen offset:1744 lds\n\t"
            "s_mov_b32 exec_hi, -1\n\t"
            "s_mov_b32 exec_hi, 0x3fffff\n\t"
            "s_add_u32 m0, s66, 2616\n\t"
            "s_add_u32 s49, s68, 588672\n\t"
            "buffer_load_dwordx4 v249, s[72:75], s49 offen lds\n\t"
            "s_add_u32 s49, s68, 784024\n\t"
            "buffer_load_dwordx4 v249, s[72:75], s49 offen offset:872 lds\n\t"
            "s_add_u32 s49, s68, 979376\n\t"
            "buffer_load_dwordx4 v249, s[72:75], s49 offen offset:1744 lds\n\t"
            "s_mov_b32 exec_hi, -1\n\t"
            "s_mov_b32 exec_hi, 0x3fffff\n\t"
            "s_add_u32 m0, s66, 5232\n\t"
            "s_add_u32 s49, s68, 1177344\n\t"
            "buffer_load_dwordx4 v249, s[72:75], s49 offen lds\n\t"
            "s_add_u32 s49, s68, 1372696\n\t"
            "buffer_load_dwordx4 v249, s[72:75], s49 offen offset:872 lds\n\t"
            "s_add_u32 s49, s68, 1568048\n\t"
            "buffer_load_dwordx4 v249, s[72:75], s49 offen offset:1744 lds\n\t"
            "s_mov_b32 exec_hi, -1\n\t"
            "v_cvt_pk_bf16_f32 v184, v184, v185\n\t"
            "v_cvt_pk_bf16_f32 v185, v186, v187\n\t"
            "v_cvt_pk_bf16_f32 v186, v188, v189\n\t"
            "v_cvt_pk_bf16_f32 v187, v190, v191\n\t"
            "v_mfma_f32_4x4x4_16b_bf16 v[196:199], v[184:185], v[184:185], v[196:199]\n\t"
            "s_waitcnt vmcnt(9)\n\t"
            "v_mfma_f32_16x16x32_bf16 v[192:195], v[238:241], v[184:187], v[192:195]\n\t"
            "ds_write_b128 v236, v[176:179] offset:256\n\t"
            "v_mfma_f32_4x4x4_16b_bf16 v[196:199], v[186:187], v[186:187], v[196:199]\n\t"
            "ds_write_b128 v237, v[184:187] offset:256\n\t"
            "v_mfma_f32_16x16x32_bf16 v[144:147], v[0:3], v[200:203], 0\n\t"
            "v_xor_b32 v228, 32, v234\n\t"
            "v_add_u32 v236, s48, v244\n\t"
            "v_mfma_f32_16x16x32_bf16 v[144:147], v[4:7], v[204:207], v[144:147]\n\t"
            "v_add_u32 v237, s48, v245\n\t"
            "v_mfma_f32_16x16x32_bf16 v[148:151], v[24:27], v[200:203], 0\n\t"
            "s_nop 0\n\t"
            "v_cndmask_b32_e64 v193, v196, v197, s[60:61]\n\t"
            "v_cndmask_b32_e64 v193, v193, v198, s[62:63]\n\t"
            "v_mfma_f32_16x16x32_bf16 v[148:151], v[28:31], v[204:207], v[148:151]\n\t"
            "v_cndmask_b32_e64 v193, v193, v199, s[64:65]\n\t"
            "v_mfma_f32_16x16x32_bf16 v[168:171], v[48:51], v[200:203], 0\n\t"
            "ds_read_b128 v[196:199], v232 offset:6272\n\t"
            "v_mfma_f32_16x16x32_bf16 v[168:171], v[52:55], v[204:207], v[168:171]\n\t"
            "ds_write_b64 v248, v[192:193] offset:18816\n\t"
            "v_mfma_f32_16x16x32_bf16 v[172:175], v[72:75], v[200:203], 0\n\t"
            "ds_read_b128 v[192:195], v235 offset:6144\n\t"
            "v_add_u32 v248, s48, v246\n\t"
            "v_mfma_f32_16x16x32_bf16 v[172:175], v[76:79], v[204:207], v[172:175]\n\t"
            "s_mov_b64 exec, 1\n\t"
            "ds_inc_u32 v255, v238 offset:7860\n\t"
            "s_mov_b64 exec, -1\n\t"
            "v_mfma_f32_16x16x32_bf16 v[152:155], v[96:99], v[200:203], 0\n\t"
            "v_mfma_f32_16x16x32_bf16 v[152:155], v[100:103], v[204:207], v[152:155]\n\t"
            "v_mfma_f32_16x16x32_bf16 v[156:159], v[120:123], v[200:203], 0\n\t"
            "ds_read_b128 v[200:203], v235 offset:6272\n\t"
            "v_mfma_f32_16x16x32_bf16 v[156:159], v[124:127], v[204:207], v[156:159]\n\t"
            "ds_read_b128 v[204:207], v232 offset:6400\n\t"
            "v_mfma_f32_16x16x32_bf16 v[144:147], v[8:11], v[208:211], v[144:147]\n\t"
            "v_mfma_f32_16x16x32_bf16 v[144:147], v[12:15], v[212:215], v[144:147]\n\t"
            "v_mfma_f32_16x16x32_bf16 v[148:151], v[32:35], v[208:211], v[148:151]\n\t"
            "v_mfma_f32_16x16x32_bf16 v[148:151], v[36:39], v[212:215], v[148:151]\n\t"
            "v_mfma_f32_16x16x32_bf16 v[168:171], v[56:59], v[208:211], v[168:171]\n\t"
            "v_mfma_f32_16x16x32_bf16 v[168:171], v[60:63], v[212:215], v[168:171]\n\t"
            "v_mfma_f32_16x16x32_bf16 v[172:175], v[80:83], v[208:211], v[172:175]\n\t"
            "v_mfma_f32_16x16x32_bf16 v[172:175], v[84:87], v[212:215], v[172:175]\n\t"
            "v_mfma_f32_16x16x32_bf16 v[152:155], v[104:107], v[208:211], v[152:155]\n\t"
            "v_mfma_f32_16x16x32_bf16 v[152:155], v[108:111], v[212:215], v[152:155]\n\t"
            "v_mfma_f32_16x16x32_bf16 v[156:159], v[128:131], v[208:211], v[156:159]\n\t"
            "ds_read_b128 v[208:211], v235 offset:6400\n\t"
            "v_mfma_f32_16x16x32_bf16 v[156:159], v[132:135], v[212:215], v[156:159]\n\t"
            "ds_read_b128 v[212:215], v236 offset:6144\n\t"
            "s_waitcnt lgkmcnt(12)\n\t"
            "v_mfma_f32_16x16x32_bf16 v[144:147], v[16:19], v[216:219], v[144:147]\n\t"
            "v_mfma_f32_16x16x32_bf16 v[144:147], v[20:23], v[220:223], v[144:147]\n\t"
            "v_mfma_f32_16x16x32_bf16 v[148:151], v[40:43], v[216:219], v[148:151]\n\t"
            "v_mfma_f32_16x16x32_bf16 v[148:151], v[44:47], v[220:223], v[148:151]\n\t"
            "v_mfma_f32_16x16x32_bf16 v[168:171], v[64:67], v[216:219], v[168:171]\n\t"
            "v_mfma_f32_16x16x32_bf16 v[168:171], v[68:71], v[220:223], v[168:171]\n\t"
            "v_mfma_f32_16x16x32_bf16 v[172:175], v[88:91], v[216:219], v[172:175]\n\t"
            "v_mfma_f32_16x16x32_bf16 v[172:175], v[92:95], v[220:223], v[172:175]\n\t"
            "s_waitcnt lgkmcnt(11)\n\t"
            "s_nop 0\n\t"
            "v_mul_f32 v144, v230, v144\n\t"
            "v_mfma_f32_16x16x32_bf16 v[152:155], v[112:115], v[216:219], v[152:155]\n\t"
            "v_mul_f32 v145, v230, v145\n\t"
            "v_mul_f32 v146, v230, v146\n\t"
            "v_mfma_f32_16x16x32_bf16 v[152:155], v[116:119], v[220:223], v[152:155]\n\t"
            "v_mul_f32 v148, v230, v148\n\t"
            "v_mul_f32 v149, v230, v149\n\t"
            "v_mfma_f32_16x16x32_bf16 v[156:159], v[136:139], v[216:219], v[156:159]\n\t"
            "ds_read_b128 v[216:219], v237 offset:6144\n\t"
            "v_mul_f32 v168, v230, v168\n\t"
            "v_mfma_f32_16x16x32_bf16 v[156:159], v[140:143], v[220:223], v[156:159]\n\t"
            "ds_read_b128 v[220:223], v248 offset:6144\n\t"
            "v_mul_f32 v169, v230, v169\n\t"
            "s_waitcnt lgkmcnt(7)\n\t"
            "v_mfma_f32_16x16x32_bf16 v[160:163], v[0:3], v[224:227], 0\n\t"
            "v_mul_f32 v172, v230, v172\n\t"
            "v_mul_f32 v173, v230, v173\n\t"
            "v_mfma_f32_16x16x32_bf16 v[160:163], v[4:7], v[192:195], v[160:163]\n\t"
            "v_mul_f32 v152, v230, v152\n\t"
            "v_mul_f32 v153, v230, v153\n\t"
            "v_mfma_f32_16x16x32_bf16 v[164:167], v[24:27], v[224:227], 0\n\t"
            "v_mul_f32 v156, v230, v156\n\t"
            "v_mul_f32 v157, v230, v157\n\t"
            "v_mfma_f32_16x16x32_bf16 v[164:167], v[28:31], v[192:195], v[164:167]\n\t"
            "v_mul_f32 v147, v230, v147\n\t"
            "v_cvt_pk_bf16_f32 v144, v144, v145\n\t"
            "v_mfma_f32_16x16x32_bf16 v[176:179], v[48:51], v[224:227], 0\n\t"
            "v_mul_f32 v150, v230, v150\n\t"
            "v_mul_f32 v151, v230, v151\n\t"
            "v_mfma_f32_16x16x32_bf16 v[176:179], v[52:55], v[192:195], v[176:179]\n\t"
            "v_cvt_pk_bf16_f32 v148, v148, v149\n\t"
            "v_mul_f32 v170, v230, v170\n\t"
            "v_mfma_f32_16x16x32_bf16 v[180:183], v[72:75], v[224:227], 0\n\t"
            "v_mul_f32 v171, v230, v171\n\t"
            "v_cvt_pk_bf16_f32 v168, v168, v169\n\t"
            "v_mfma_f32_16x16x32_bf16 v[180:183], v[76:79], v[192:195], v[180:183]\n\t"
            "v_mul_f32 v174, v230, v174\n\t"
            "v_mul_f32 v175, v230, v175\n\t"
            "v_mfma_f32_16x16x32_bf16 v[184:187], v[96:99], v[224:227], 0\n\t"
            "v_cvt_pk_bf16_f32 v172, v172, v173\n\t"
            "v_mul_f32 v154, v230, v154\n\t"
            "v_mfma_f32_16x16x32_bf16 v[184:187], v[100:103], v[192:195], v[184:187]\n\t"
            "v_mul_f32 v155, v230, v155\n\t"
            "v_cvt_pk_bf16_f32 v152, v152, v153\n\t"
            "v_mfma_f32_16x16x32_bf16 v[188:191], v[120:123], v[224:227], 0\n\t"
            "ds_read_b128 v[224:227], v236 offset:12288\n\t"
            "v_mul_f32 v158, v230, v158\n\t"
            "v_mfma_f32_16x16x32_bf16 v[188:191], v[124:127], v[192:195], v[188:191]\n\t"
            "v_mul_f32 v159, v230, v159\n\t"
            "v_cvt_pk_bf16_f32 v156, v156, v157\n\t"
            "s_waitcnt lgkmcnt(6)\n\t"
            "v_mfma_f32_16x16x32_bf16 v[160:163], v[8:11], v[196:199], v[160:163]\n\t"
            "v_cvt_pk_bf16_f32 v145, v146, v147\n\t"
            "v_cvt_pk_bf16_f32 v149, v150, v151\n\t"
            "v_mfma_f32_16x16x32_bf16 v[160:163], v[12:15], v[200:203], v[160:163]\n\t"
            "v_cvt_pk_bf16_f32 v169, v170, v171\n\t"
            "v_cvt_pk_bf16_f32 v173, v174, v175\n\t"
            "v_mfma_f32_16x16x32_bf16 v[164:167], v[32:35], v[196:199], v[164:167]\n\t"
            "v_cvt_pk_bf16_f32 v153, v154, v155\n\t"
            "v_cvt_pk_bf16_f32 v157, v158, v159\n\t"
            "v_mfma_f32_16x16x32_bf16 v[164:167], v[36:39], v[200:203], v[164:167]\n\t"
            "s_mov_b64 exec, -1\n\t"
            "ds_write_b64 v234, v[144:145]\n\t"
            "ds_write_b64 v234, v[148:149] offset:128\n\t"
            "ds_write_b64 v234, v[168:169] offset:256\n\t"
            "ds_write_b64 v228, v[172:173]\n\t"
            "ds_write_b64 v228, v[152:153] offset:128\n\t"
            "ds_write_b64 v228, v[156:157] offset:256\n\t"
            "s_mov_b64 exec, -1\n\t"
            "v_mfma_f32_16x16x32_bf16 v[176:179], v[56:59], v[196:199], v[176:179]\n\t"
            "v_mfma_f32_16x16x32_bf16 v[176:179], v[60:63], v[200:203], v[176:179]\n\t"
            "s_add_u32 s49, s33, 0\n\t"
            "buffer_load_dwordx4 v[0:3], v247, s[28:31], s49 offen sc1\n\t"
            "v_mfma_f32_16x16x32_bf16 v[180:183], v[80:83], v[196:199], v[180:183]\n\t"
            "v_mfma_f32_16x16x32_bf16 v[180:183], v[84:87], v[200:203], v[180:183]\n\t"
            "s_add_u32 s49, s33, 6144\n\t"
            "buffer_load_dwordx4 v[24:27], v247, s[28:31], s49 offen sc1\n\t"
            "v_mfma_f32_16x16x32_bf16 v[184:187], v[104:107], v[196:199], v[184:187]\n\t"
            "v_mfma_f32_16x16x32_bf16 v[184:187], v[108:111], v[200:203], v[184:187]\n\t"
            "s_add_u32 s49, s33, 12288\n\t"
            "buffer_load_dwordx4 v[48:51], v247, s[28:31], s49 offen sc1\n\t"
            "v_mfma_f32_16x16x32_bf16 v[188:191], v[128:131], v[196:199], v[188:191]\n\t"
            "v_mfma_f32_16x16x32_bf16 v[188:191], v[132:135], v[200:203], v[188:191]\n\t"
            "ds_read_b128 v[200:203], v236\n\t"
            "s_add_u32 s49, s33, 18432\n\t"
            "buffer_load_dwordx4 v[72:75], v247, s[28:31], s49 offen sc1\n\t"
            "s_waitcnt lgkmcnt(11)\n\t"
            "v_mfma_f32_16x16x32_bf16 v[160:163], v[16:19], v[204:207], v[160:163]\n\t"
            "v_mfma_f32_16x16x32_bf16 v[160:163], v[20:23], v[208:211], v[160:163]\n\t"
            "s_add_u32 s49, s33, 24576\n\t"
            "buffer_load_dwordx4 v[96:99], v247, s[28:31], s49 offen sc1\n\t"
            "v_mfma_f32_16x16x32_bf16 v[164:167], v[40:43], v[204:207], v[164:167]\n\t"
            "v_mfma_f32_16x16x32_bf16 v[164:167], v[44:47], v[208:211], v[164:167]\n\t"
            "s_add_u32 s49, s33, 30720\n\t"
            "buffer_load_dwordx4 v[120:123], v247, s[28:31], s49 offen sc1\n\t"
            "v_mfma_f32_16x16x32_bf16 v[176:179], v[64:67], v[204:207], v[176:179]\n\t"
            "s_nop 0\n\t"
            "v_mul_f32 v160, v231, v160\n\t"
            "v_mul_f32 v161, v231, v161\n\t"
            "v_mfma_f32_16x16x32_bf16 v[176:179], v[68:71], v[208:211], v[176:179]\n\t"
            "v_mul_f32 v162, v231, v162\n\t"
            "v_mul_f32 v163, v231, v163\n\t"
            "v_mfma_f32_16x16x32_bf16 v[180:183], v[88:91], v[204:207], v[180:183]\n\t"
            "v_mul_f32 v164, v231, v164\n\t"
            "v_mul_f32 v165, v231, v165\n\t"
            "v_mfma_f32_16x16x32_bf16 v[180:183], v[92:95], v[208:211], v[180:183]\n\t"
            "v_cvt_pk_bf16_f32 v160, v160, v161\n\t"
            "v_mul_f32 v166, v231, v166\n\t"
            "v_mfma_f32_16x16x32_bf16 v[184:187], v[112:115], v[204:207], v[184:187]\n\t"
            "v_mul_f32 v176, v231, v176\n\t"
            "v_mul_f32 v177, v231, v177\n\t"
            "v_mfma_f32_16x16x32_bf16 v[184:187], v[116:119], v[208:211], v[184:187]\n\t"
            "v_mul_f32 v167, v231, v167\n\t"
            "v_cvt_pk_bf16_f32 v164, v164, v165\n\t"
            "v_mfma_f32_16x16x32_bf16 v[188:191], v[136:139], v[204:207], v[188:191]\n\t"
            "ds_read_b128 v[204:207], v237\n\t"
            "v_mul_f32 v180, v231, v180\n\t"
            "v_mfma_f32_16x16x32_bf16 v[188:191], v[140:143], v[208:211], v[188:191]\n\t"
            "ds_read_b128 v[208:211], v248\n\t"
            "v_mul_f32 v181, v231, v181\n\t"
            "v_mul_f32 v184, v231, v184\n\t"
            "v_mul_f32 v185, v231, v185\n\t"
            "v_mul_f32 v178, v231, v178\n\t"
            "v_mul_f32 v179, v231, v179\n\t"
            "v_cvt_pk_bf16_f32 v176, v176, v177\n\t"
            "v_mul_f32 v182, v231, v182\n\t"
            "v_mul_f32 v188, v231, v188\n\t"
            "v_mul_f32 v189, v231, v189\n\t"
            "v_mul_f32 v183, v231, v183\n\t"
            "v_cvt_pk_bf16_f32 v180, v180, v181\n\t"
            "v_mul_f32 v186, v231, v186\n\t"
            "v_mul_f32 v187, v231, v187\n\t"
            "v_cvt_pk_bf16_f32 v184, v184, v185\n\t"
            "v_mul_f32 v190, v231, v190\n\t"
            "v_mul_f32 v191, v231, v191\n\t"
            "v_cvt_pk_bf16_f32 v188, v188, v189\n\t"
            "v_cvt_pk_bf16_f32 v161, v162, v163\n\t"
            "v_cvt_pk_bf16_f32 v165, v166, v167\n\t"
            "v_cvt_pk_bf16_f32 v177, v178, v179\n\t"
            "v_cvt_pk_bf16_f32 v181, v182, v183\n\t"
            "v_cvt_pk_bf16_f32 v185, v186, v187\n\t"
            "v_cvt_pk_bf16_f32 v189, v190, v191\n\t"
            "s_waitcnt lgkmcnt(9)\n\t"
            "s_mov_b64 exec, s[80:81]\n\t"
            "ds_write_b64 v234, v[160:161] offset:6144\n\t"
            "ds_write_b64 v234, v[164:165] offset:6272\n\t"
            "ds_write_b64 v234, v[176:177] offset:6400\n\t"
            "ds_write_b64 v228, v[180:181] offset:6144\n\t"
            "ds_write_b64 v228, v[184:185] offset:6272\n\t"
            "ds_write_b64 v228, v[188:189] offset:6400\n\t"
            "s_mov_b64 exec, -1\n\t"
            "s_waitcnt lgkmcnt(14)\n\t"
            "ds_read_b128 v[228:231], v237 offset:12288\n\t"
            "s_waitcnt lgkmcnt(14)\n\t"
            "ds_read_b128 v[232:235], v248 offset:12288\n\t"
            "s_add_u32 s49, s33, 1024\n\t"
            "buffer_load_dwordx4 v[4:7], v247, s[28:31], s49 offen sc1\n\t"
            "s_add_u32 s49, s33, 7168\n\t"
            "buffer_load_dwordx4 v[28:31], v247, s[28:31], s49 offen sc1\n\t"
            "s_waitcnt lgkmcnt(0)\n\t"
            "s_barrier\n\t"
            "s_setprio 3\n\t"
            "s_add_u32 s49, s33, 13312\n\t"
            "buffer_load_dwordx4 v[52:55], v247, s[28:31], s49 offen sc1\n\t"
            "s_add_u32 s49, s33, 19456\n\t"
            "buffer_load_dwordx4 v[76:79], v247, s[28:31], s49 offen sc1\n\t"
            "s_add_u32 s49, s33, 25600\n\t"
            "buffer_load_dwordx4 v[100:103], v247, s[28:31], s49 offen sc1\n\t"
            "s_add_u32 s49, s33, 31744\n\t"
            "buffer_load_dwordx4 v[124:127], v247, s[28:31], s49 offen sc1\n\t"
            "s_add_u32 s49, s33, 2048\n\t"
            "buffer_load_dwordx4 v[8:11], v247, s[28:31], s49 offen sc1\n\t"
            "s_add_u32 s49, s33, 8192\n\t"
            "buffer_load_dwordx4 v[32:35], v247, s[28:31], s49 offen sc1\n\t"
            "s_add_u32 s49, s33, 14336\n\t"
            "buffer_load_dwordx4 v[56:59], v247, s[28:31], s49 offen sc1\n\t"
            "s_add_u32 s49, s33, 20480\n\t"
            "buffer_load_dwordx4 v[80:83], v247, s[28:31], s49 offen sc1\n\t"
            "s_add_u32 s49, s33, 26624\n\t"
            "buffer_load_dwordx4 v[104:107], v247, s[28:31], s49 offen sc1\n\t"
            "s_add_u32 s49, s33, 32768\n\t"
            "buffer_load_dwordx4 v[128:131], v247, s[28:31], s49 offen sc1\n\t"
            "s_add_u32 s49, s33, 3072\n\t"
            "buffer_load_dwordx4 v[12:15], v247, s[28:31], s49 offen sc1\n\t"
            "s_add_u32 s49, s33, 9216\n\t"
            "buffer_load_dwordx4 v[36:39], v247, s[28:31], s49 offen sc1\n\t"
            "s_add_u32 s49, s33, 15360\n\t"
            "buffer_load_dwordx4 v[60:63], v247, s[28:31], s49 offen sc1\n\t"
            "s_add_u32 s49, s33, 21504\n\t"
            "buffer_load_dwordx4 v[84:87], v247, s[28:31], s49 offen sc1\n\t"
            "s_add_u32 s49, s33, 27648\n\t"
            "buffer_load_dwordx4 v[108:111], v247, s[28:31], s49 offen sc1\n\t"
            "s_add_u32 s49, s33, 33792\n\t"
            "buffer_load_dwordx4 v[132:135], v247, s[28:31], s49 offen sc1\n\t"
            "s_add_u32 s49, s33, 4096\n\t"
            "buffer_load_dwordx4 v[16:19], v247, s[28:31], s49 offen sc1\n\t"
            "s_add_u32 s49, s33, 10240\n\t"
            "buffer_load_dwordx4 v[40:43], v247, s[28:31], s49 offen sc1\n\t"
            "s_add_u32 s49, s33, 16384\n\t"
            "buffer_load_dwordx4 v[64:67], v247, s[28:31], s49 offen sc1\n\t"
            "s_add_u32 s49, s33, 22528\n\t"
            "buffer_load_dwordx4 v[88:91], v247, s[28:31], s49 offen sc1\n\t"
            "s_add_u32 s49, s33, 28672\n\t"
            "buffer_load_dwordx4 v[112:115], v247, s[28:31], s49 offen sc1\n\t"
            "s_add_u32 s49, s33, 34816\n\t"
            "buffer_load_dwordx4 v[136:139], v247, s[28:31], s49 offen sc1\n\t"
            "s_add_u32 s49, s33, 5120\n\t"
            "buffer_load_dwordx4 v[20:23], v247, s[28:31], s49 offen sc1\n\t"
            "s_add_u32 s49, s33, 11264\n\t"
            "buffer_load_dwordx4 v[44:47], v247, s[28:31], s49 offen sc1\n\t"
            "s_add_u32 s49, s33, 17408\n\t"
            "buffer_load_dwordx4 v[68:71], v247, s[28:31], s49 offen sc1\n\t"
            "s_add_u32 s49, s33, 23552\n\t"
            "buffer_load_dwordx4 v[92:95], v247, s[28:31], s49 offen sc1\n\t"
            "s_add_u32 s49, s33, 29696\n\t"
            "buffer_load_dwordx4 v[116:119], v247, s[28:31], s49 offen sc1\n\t"
            "s_add_u32 s49, s33, 35840\n\t"
            "buffer_load_dwordx4 v[140:143], v247, s[28:31], s49 offen sc1\n\t"
            "s_add_u32 s59, s59, s84\n\t"
            "s_movk_i32 s84, 800\n\t"
            "s_add_u32 s45, s46, s27\n\t"
            "s_mov_b32 s46, s47\n\t"
            "s_mov_b32 s47, s68\n\t"
            "s_xor_b32 s48, s48, s71\n\t"
            "s_xor_b32 s67, s67, s71\n\t"
            "s_add_u32 s76, s76, 4\n\t"
            "s_add_u32 s70, s70, 4\n\t"
            "s_lshl_b32 s56, s56, 2\n\t"
            "s_lshr_b32 s49, s26, 30\n\t"
            "s_or_b32 s56, s56, s49\n\t"
            "s_cmp_eq_u32 s82, 0\n\t"
            "s_cbranch_scc1 .Lwk_nr%=\n\t"
            "s_waitcnt lgkmcnt(0)\n\t"
            "s_nop 7\n\t"
            "v_add_u32 v236, 127360, v247\n\t"
            "v_add_u32 v237, 136704, v247\n\t"
            "ds_read_b128 v[144:147], v236\n\t"
            "ds_read_b128 v[148:151], v237\n\t"
            "s_waitcnt lgkmcnt(0)\n\t"
            "s_mov_b32 exec_hi, 0x3ffff\n\t"
            "buffer_store_dwordx4 v[144:147], v247, s[28:31], s82 offen sc1\n\t"
            "s_mov_b64 exec, 0xffffff\n\t"
            "buffer_store_dwordx4 v[148:151], v247, s[28:31], s82 offen offset:800 sc1\n\t"
            "s_mov_b64 exec, -1\n\t"
            "ds_read_b128 v[152:155], v252 offset:2688\n\t"
            "s_waitcnt vmcnt(0) lgkmcnt(0)\n\t"
            "s_mov_b64 exec, 1\n\t"
            "buffer_store_dwordx4 v[152:155], off, s[28:31], s82 offset:1216 sc1\n\t"
            "s_mov_b64 exec, -1\n\t"
            "s_branch .Lwk_n2%=\n\t"
            ".Lwk_nr%=:\n\t"
            "s_waitcnt vmcnt(0)\n\t"
            ".Lwk_n2%=:\n\t"
            "s_nop 1\n\t"
            "s_nop 7\n\t"
            ".p2align 6\n\t"
            "s_nop 0\n\t"
            "s_nop 0\n\t"
            "s_nop 0\n\t"
            "s_nop 0\n\t"
            "s_nop 0\n\t"
            "s_nop 0\n\t"
            "s_nop 0\n\t"
            ".Lwb_loop%=:\n\t"
            "s_setprio 1\n\t"
            "ds_read_b128 v[168:171], v254\n\t"
            "ds_read_b128 v[172:175], v254 offset:64\n\t"
            "s_waitcnt vmcnt(9)\n\t"
            "ds_read_b64 v[144:145], v253\n\t"
            "ds_read_b64 v[146:147], v253 offset:96\n\t"
            "ds_read_b64 v[148:149], v253 offset:1744\n\t"
            "ds_read_b64 v[150:151], v253 offset:1840\n\t"
            "ds_read_b64 v[152:153], v253 offset:8\n\t"
            "ds_read_b64 v[154:155], v253 offset:104\n\t"
            "ds_read_b64 v[156:157], v253 offset:1752\n\t"
            "ds_read_b64 v[158:159], v253 offset:1848\n\t"
            "ds_read_b64 v[160:161], v253 offset:16\n\t"
            "ds_read_b64 v[162:163], v253 offset:112\n\t"
            "ds_read_b64 v[164:165], v253 offset:1760\n\t"
            "ds_read_b64 v[166:167], v253 offset:1856\n\t"
            "v_mov_b64 v[176:177], 0\n\t"
            "v_mov_b64 v[178:179], 0\n\t"
            "v_mov_b64 v[180:181], 0\n\t"
            "v_mov_b64 v[182:183], 0\n\t"
            "v_mov_b32 v249, s76\n\t"
            "v_mov_b64 v[184:185], 0\n\t"
            "v_mov_b64 v[186:187], 0\n\t"
            "v_mov_b64 v[188:189], 0\n\t"
            "v_mov_b64 v[190:191], 0\n\t"
            "v_add_u32 v236, s67, v250\n\t"
            "v_add_u32 v248, s67, v251\n\t"
            "v_mov_b32 v247, 0\n\t"
            "ds_read_b32 v249, v249\n\t"
            "s_setprio 3\n\t"
            "s_waitcnt lgkmcnt(9)\n\t"
            "v_smfmac_f32_16x16x64_bf16 v[176:179], v[168:171], v[144:151], v255\n\t"
            "ds_read_b128 v[168:171], v254 offset:128\n\t"
            "s_waitcnt lgkmcnt(6)\n\t"
            "v_smfmac_f32_16x16x64_bf16 v[180:183], v[172:175], v[152:159], v255\n\t"
            "s_setprio 1\n\t"
            "ds_read_b64 v[144:145], v253 offset:24\n\t"
            "ds_read_b64 v[146:147], v253 offset:120\n\t"
            "ds_read_b64 v[148:149], v253 offset:1768\n\t"
            "ds_read_b64 v[150:151], v253 offset:1864\n\t"
            "ds_read_b128 v[172:175], v254 offset:192\n\t"
            "ds_read_b64 v[152:153], v253 offset:32\n\t"
            "ds_read_b64 v[154:155], v253 offset:128\n\t"
            "ds_read_b64 v[156:157], v253 offset:1776\n\t"
            "ds_read_b64 v[158:159], v253 offset:1872\n\t"
            "v_cvt_pk_bf16_f32 v176, v176, v177\n\t"
            "v_cvt_pk_bf16_f32 v177, v178, v179\n\t"
            "v_cvt_pk_bf16_f32 v178, v180, v181\n\t"
            "v_cvt_pk_bf16_f32 v179, v182, v183\n\t"
            "s_setprio 3\n\t"
            "v_mfma_f32_4x4x4_16b_bf16 v[196:199], v[176:177], v[176:177], 0\n\t"
            "v_mov_b64 v[180:181], 0\n\t"
            "v_mfma_f32_16x16x32_bf16 v[192:195], v[238:241], v[176:179], 0\n\t"
            "s_waitcnt lgkmcnt(10)\n\t"
            "v_readfirstlane_b32 s26, v249\n\t"
            "v_mov_b64 v[182:183], 0\n\t"
            "v_mfma_f32_4x4x4_16b_bf16 v[196:199], v[178:179], v[178:179], v[196:199]\n\t"
            "ds_read_b32 v249, v252 offset:12\n\t"
            "s_and_b32 s68, s26, 0x7ffffff\n\t"
            "s_add_u32 s68, s68, s78\n\t"
            "s_and_b32 s69, s26, 0x38000000\n\t"
            "s_or_b32 s69, s69, s39\n\t"
            "s_bitcmp1_b32 s26, 27\n\t"
            "s_cselect_b32 s79, 2.0, s68\n\t"
            "s_waitcnt lgkmcnt(10)\n\t"
            "v_smfmac_f32_16x16x64_bf16 v[184:187], v[168:171], v[160:167], v255\n\t"
            "s_and_b32 s57, s56, 3\n\t"
            "s_lshl_b32 s58, s57, 3\n\t"
            "s_lshr_b32 m0, s54, s58\n\t"
            "ds_read_b128 v[168:171], v254 offset:256\n\t"
            "ds_read_b64 v[160:161], v253 offset:40\n\t"
            "ds_read_b64 v[162:163], v253 offset:136\n\t"
            "ds_read_b64 v[164:165], v253 offset:1784\n\t"
            "s_waitcnt lgkmcnt(9)\n\t"
            "v_smfmac_f32_16x16x64_bf16 v[188:191], v[172:175], v[144:151], v255\n\t"
            "s_setprio 1\n\t"
            "s_lshl_b32 s58, s57, 4\n\t"
            "s_lshr_b64 s[36:37], s[50:51], s58\n\t"
            "s_and_b32 s57, s36, 0xffff\n\t"
            "s_add_u32 s57, s57, s67\n\t"
            "ds_read_b64 v[166:167], v253 offset:1880\n\t"
            "ds_read_b128 v[172:175], v254 offset:320\n\t"
            "ds_read_b64 v[144:145], v253 offset:48\n\t"
            "ds_read_b64 v[146:147], v253 offset:144\n\t"
            "ds_read_b64 v[148:149], v253 offset:1792\n\t"
            "ds_read_b64 v[150:151], v253 offset:1888\n\t"
            "s_setprio 3\n\t"
            "v_writelane_b32 v236, s57, 63\n\t"
            "v_writelane_b32 v236, s59, m0\n\t"
            "v_cvt_pk_bf16_f32 v184, v184, v185\n\t"
            "v_cvt_pk_bf16_f32 v185, v186, v187\n\t"
            "v_xor_b32 v237, 16, v236\n\t"
            "v_cvt_pk_bf16_f32 v186, v188, v189\n\t"
            "v_mfma_f32_4x4x4_16b_bf16 v[196:199], v[184:185], v[184:185], v[196:199]\n\t"
            "s_waitcnt lgkmcnt(14)\n\t"
            "ds_write_b128 v236, v[176:179]\n\t"
            "v_mov_b64 v[176:177], 0\n\t"
            "v_mov_b64 v[178:179], 0\n\t"
            "v_cvt_pk_bf16_f32 v187, v190, v191\n\t"
            "v_mov_b64 v[188:189], 0\n\t"
            "s_waitcnt lgkmcnt(10)\n\t"
            "v_smfmac_f32_16x16x64_bf16 v[176:179], v[168:171], v[152:159], v255\n\t"
            "s_lshr_b64 s[36:37], s[52:53], s58\n\t"
            "s_and_b32 s58, s36, 0xffff\n\t"
            "s_add_u32 s58, s58, s67\n\t"
            "v_mfma_f32_16x16x32_bf16 v[192:195], v[238:241], v[184:187], v[192:195]\n\t"
            "s_and_b32 s38, s52, 0xffff\n\t"
            "s_add_u32 s38, s38, s67\n\t"
            "ds_read_b128 v[168:171], v254 offset:384\n\t"
            "v_mfma_f32_4x4x4_16b_bf16 v[196:199], v[186:187], v[186:187], v[196:199]\n\t"
            "ds_read_b64 v[152:153], v253 offset:56\n\t"
            "s_waitcnt lgkmcnt(7)\n\t"
            "v_smfmac_f32_16x16x64_bf16 v[180:183], v[172:175], v[160:167], v255\n\t"
            "ds_read_b64 v[154:155], v253 offset:152\n\t"
            "ds_read_b64 v[156:157], v253 offset:1800\n\t"
            "s_setprio 1\n\t"
            "ds_read_b64 v[158:159], v253 offset:1896\n\t"
            "ds_read_b128 v[172:175], v254 offset:448\n\t"
            "ds_read_b64 v[160:161], v253 offset:64\n\t"
            "ds_read_b64 v[162:163], v253 offset:160\n\t"
            "ds_read_b64 v[164:165], v253 offset:1808\n\t"
            "ds_read_b64 v[166:167], v253 offset:1904\n\t"
            "s_waitcnt lgkmcnt(14)\n\t"
            "ds_write_b128 v237, v[184:187]\n\t"
            "v_mov_b64 v[184:185], 0\n\t"
            "v_mov_b64 v[186:187], 0\n\t"
            "v_mov_b64 v[190:191], 0\n\t"
            "v_cvt_pk_bf16_f32 v176, v176, v177\n\t"
            "v_cvt_pk_bf16_f32 v177, v178, v179\n\t"
            "v_cvt_pk_bf16_f32 v178, v180, v181\n\t"
            "v_cvt_pk_bf16_f32 v179, v182, v183\n\t"
            "s_setprio 3\n\t"
            "v_mfma_f32_4x4x4_16b_bf16 v[196:199], v[176:177], v[176:177], v[196:199]\n\t"
            "s_waitcnt lgkmcnt(14)\n\t"
            "ds_write_b128 v236, v[176:179] offset:128\n\t"
            "s_waitcnt lgkmcnt(11)\n\t"
            "v_smfmac_f32_16x16x64_bf16 v[184:187], v[168:171], v[144:151], v255\n\t"
            "ds_read_b128 v[168:171], v254 offset:512\n\t"
            "v_mfma_f32_16x16x32_bf16 v[192:195], v[238:241], v[176:179], v[192:195]\n\t"
            "ds_read_b64 v[144:145], v253 offset:72\n\t"
            "ds_read_b64 v[146:147], v253 offset:168\n\t"
            "s_waitcnt lgkmcnt(9)\n\t"
            "v_smfmac_f32_16x16x64_bf16 v[188:191], v[172:175], v[152:159], v255\n\t"
            "ds_read_b64 v[148:149], v253 offset:1816\n\t"
            "v_mfma_f32_4x4x4_16b_bf16 v[196:199], v[178:179], v[178:179], v[196:199]\n\t"
            "ds_read_b64 v[150:151], v253 offset:1912\n\t"
            "ds_read_b128 v[172:175], v254 offset:576\n\t"
            "ds_read_b64 v[152:153], v253 offset:80\n\t"
            "ds_read_b64 v[154:155], v253 offset:176\n\t"
            "ds_read_b64 v[156:157], v253 offset:1824\n\t"
            "s_waitcnt lgkmcnt(14)\n\t"
            "ds_read_b64 v[158:159], v253 offset:1920\n\t"
            "v_mov_b64 v[176:177], 0\n\t"
            "v_mov_b64 v[178:179], 0\n\t"
            "v_mov_b64 v[180:181], 0\n\t"
            "v_mov_b64 v[182:183], 0\n\t"
            "v_cvt_pk_bf16_f32 v184, v184, v185\n\t"
            "v_cvt_pk_bf16_f32 v185, v186, v187\n\t"
            "v_cvt_pk_bf16_f32 v186, v188, v189\n\t"
            "v_cvt_pk_bf16_f32 v187, v190, v191\n\t"
            "v_mfma_f32_4x4x4_16b_bf16 v[196:199], v[184:185], v[184:185], v[196:199]\n\t"
            "s_waitcnt lgkmcnt(14)\n\t"
            "ds_write_b128 v237, v[184:187] offset:128\n\t"
            "v_mfma_f32_16x16x32_bf16 v[192:195], v[238:241], v[184:187], v[192:195]\n\t"
            "v_mov_b64 v[184:185], 0\n\t"
            "v_mov_b64 v[188:189], 0\n\t"
            "s_waitcnt lgkmcnt(10)\n\t"
            "v_smfmac_f32_16x16x64_bf16 v[176:179], v[168:171], v[160:167], v255\n\t"
            "ds_read_b128 v[168:171], v254 offset:640\n\t"
            "v_mfma_f32_4x4x4_16b_bf16 v[196:199], v[186:187], v[186:187], v[196:199]\n\t"
            "ds_read_b64 v[160:161], v253 offset:88\n\t"
            "ds_read_b64 v[162:163], v253 offset:184\n\t"
            "ds_read_b64 v[164:165], v253 offset:1832\n\t"
            "s_waitcnt lgkmcnt(9)\n\t"
            "v_smfmac_f32_16x16x64_bf16 v[180:183], v[172:175], v[144:151], v255\n\t"
            "s_setprio 1\n\t"
            "ds_read_b64 v[166:167], v253 offset:1928\n\t"
            "ds_read_b128 v[172:175], v254 offset:704\n\t"
            "v_mov_b64 v[186:187], 0\n\t"
            "v_mov_b64 v[190:191], 0\n\t"
            "v_writelane_b32 v248, s58, 63\n\t"
            "v_writelane_b32 v248, s38, m0\n\t"
            "v_and_b32 v249, s69, v249\n\t"
            "v_cvt_pk_bf16_f32 v176, v176, v177\n\t"
            "v_cvt_pk_bf16_f32 v177, v178, v179\n\t"
            "v_cvt_pk_bf16_f32 v178, v180, v181\n\t"
            "v_cvt_pk_bf16_f32 v179, v182, v183\n\t"
            "s_setprio 3\n\t"
            "v_mfma_f32_4x4x4_16b_bf16 v[196:199], v[176:177], v[176:177], v[196:199]\n\t"
            "v_and_b32 v151, 0xffff0000, v227\n\t"
            "v_mfma_f32_16x16x32_bf16 v[192:195], v[238:241], v[176:179], v[192:195]\n\t"
            "v_lshlrev_b32 v150, 16, v227\n\t"
            "v_and_b32 v149, 0xffff0000, v226\n\t"
            "v_mfma_f32_4x4x4_16b_bf16 v[196:199], v[178:179], v[178:179], v[196:199]\n\t"
            "v_lshlrev_b32 v148, 16, v226\n\t"
            "v_and_b32 v147, 0xffff0000, v225\n\t"
            "v_lshlrev_b32 v146, 16, v225\n\t"
            "v_and_b32 v145, 0xffff0000, v224\n\t"
            "v_lshlrev_b32 v144, 16, v224\n\t"
            "s_waitcnt lgkmcnt(5)\n\t"
            "v_smfmac_f32_16x16x64_bf16 v[184:187], v[168:171], v[152:159], v255\n\t"
            "v_and_b32 v159, 0xffff0000, v231\n\t"
            "v_lshlrev_b32 v158, 16, v231\n\t"
            "v_and_b32 v157, 0xffff0000, v230\n\t"
            "v_lshlrev_b32 v156, 16, v230\n\t"
            "v_and_b32 v155, 0xffff0000, v229\n\t"
            "v_lshlrev_b32 v154, 16, v229\n\t"
            "s_waitcnt lgkmcnt(0)\n\t"
            "v_smfmac_f32_16x16x64_bf16 v[188:191], v[172:175], v[160:167], v255\n\t"
            "s_mov_b32 exec_hi, 0x3fffff\n\t"
            "s_add_u32 m0, s66, 0\n\t"
            "s_add_u32 s49, s79, 0\n\t"
            "buffer_load_dwordx4 v249, s[72:75], s49 offen lds\n\t"
            "s_add_u32 s49, s68, 195352\n\t"
            "buffer_load_dwordx4 v249, s[72:75], s49 offen offset:872 lds\n\t"
            "s_add_u32 s49, s68, 390704\n\t"
            "buffer_load_dwordx4 v249, s[72:75], s49 offen offset:1744 lds\n\t"
            "s_mov_b32 exec_hi, -1\n\t"
            "s_mov_b32 exec_hi, 0x3fffff\n\t"
            "s_add_u32 m0, s66, 2616\n\t"
            "s_add_u32 s49, s68, 588672\n\t"
            "buffer_load_dwordx4 v249, s[72:75], s49 offen lds\n\t"
            "s_add_u32 s49, s68, 784024\n\t"
            "buffer_load_dwordx4 v249, s[72:75], s49 offen offset:872 lds\n\t"
            "s_add_u32 s49, s68, 979376\n\t"
            "buffer_load_dwordx4 v249, s[72:75], s49 offen offset:1744 lds\n\t"
            "s_mov_b32 exec_hi, -1\n\t"
            "s_mov_b32 exec_hi, 0x3fffff\n\t"
            "s_add_u32 m0, s66, 5232\n\t"
            "s_add_u32 s49, s68, 1177344\n\t"
            "buffer_load_dwordx4 v249, s[72:75], s49 offen lds\n\t"
            "s_add_u32 s49, s68, 1372696\n\t"
            "buffer_load_dwordx4 v249, s[72:75], s49 offen offset:872 lds\n\t"
            "s_add_u32 s49, s68, 1568048\n\t"
            "buffer_load_dwordx4 v249, s[72:75], s49 offen offset:1744 lds\n\t"
            "s_mov_b32 exec_hi, -1\n\t"
            "v_cvt_pk_bf16_f32 v184, v184, v185\n\t"
            "v_cvt_pk_bf16_f32 v185, v186, v187\n\t"
            "v_cvt_pk_bf16_f32 v186, v188, v189\n\t"
            "v_cvt_pk_bf16_f32 v187, v190, v191\n\t"
            "v_mfma_f32_4x4x4_16b_bf16 v[196:199], v[184:185], v[184:185], v[196:199]\n\t"
            "ds_write_b128 v237, v[184:187] offset:256\n\t"
            "v_mfma_f32_16x16x32_bf16 v[192:195], v[238:241], v[184:187], v[192:195]\n\t"
            "v_and_b32 v153, 0xffff0000, v228\n\t"
            "v_lshlrev_b32 v152, 16, v228\n\t"
            "v_mfma_f32_4x4x4_16b_bf16 v[196:199], v[186:187], v[186:187], v[196:199]\n\t"
            "s_setprio 1\n\t"
            "v_and_b32 v167, 0xffff0000, v235\n\t"
            "v_lshlrev_b32 v166, 16, v235\n\t"
            "v_and_b32 v165, 0xffff0000, v234\n\t"
            "v_lshlrev_b32 v164, 16, v234\n\t"
            "v_and_b32 v163, 0xffff0000, v233\n\t"
            "v_lshlrev_b32 v162, 16, v233\n\t"
            "v_and_b32 v161, 0xffff0000, v232\n\t"
            "v_lshlrev_b32 v160, 16, v232\n\t"
            "v_and_b32 v227, 0xffff0000, v203\n\t"
            "v_cndmask_b32_e64 v193, v196, v197, s[60:61]\n\t"
            "v_cndmask_b32_e64 v193, v193, v198, s[62:63]\n\t"
            "v_cndmask_b32_e64 v193, v193, v199, s[64:65]\n\t"
            "v_lshlrev_b32 v226, 16, v203\n\t"
            "v_and_b32 v225, 0xffff0000, v202\n\t"
            "v_lshlrev_b32 v224, 16, v202\n\t"
            "v_and_b32 v231, 0xffff0000, v207\n\t"
            "v_lshlrev_b32 v230, 16, v207\n\t"
            "v_and_b32 v229, 0xffff0000, v206\n\t"
            "v_lshlrev_b32 v228, 16, v206\n\t"
            "v_and_b32 v235, 0xffff0000, v211\n\t"
            "v_lshlrev_b32 v234, 16, v211\n\t"
            "v_and_b32 v233, 0xffff0000, v210\n\t"
            "v_lshlrev_b32 v232, 16, v210\n\t"
            "v_and_b32 v203, 0xffff0000, v201\n\t"
            "v_lshlrev_b32 v202, 16, v201\n\t"
            "v_and_b32 v207, 0xffff0000, v205\n\t"
            "v_lshlrev_b32 v206, 16, v205\n\t"
            "v_and_b32 v211, 0xffff0000, v209\n\t"
            "v_lshlrev_b32 v210, 16, v209\n\t"
            "v_and_b32 v201, 0xffff0000, v200\n\t"
            "v_and_b32 v205, 0xffff0000, v204\n\t"
            "v_and_b32 v209, 0xffff0000, v208\n\t"
            "v_lshlrev_b32 v200, 16, v200\n\t"
            "v_lshlrev_b32 v204, 16, v204\n\t"
            "v_lshlrev_b32 v208, 16, v208\n\t"
            "v_and_b32 v171, 0xffff0000, v215\n\t"
            "v_lshlrev_b32 v170, 16, v215\n\t"
            "v_and_b32 v169, 0xffff0000, v214\n\t"
            "v_lshlrev_b32 v168, 16, v214\n\t"
            "v_and_b32 v183, 0xffff0000, v223\n\t"
            "v_lshlrev_b32 v182, 16, v223\n\t"
            "v_and_b32 v181, 0xffff0000, v222\n\t"
            "v_lshlrev_b32 v180, 16, v222\n\t"
            "v_and_b32 v215, 0xffff0000, v213\n\t"
            "v_lshlrev_b32 v214, 16, v213\n\t"
            "v_and_b32 v223, 0xffff0000, v221\n\t"
            "v_lshlrev_b32 v222, 16, v221\n\t"
            "v_and_b32 v213, 0xffff0000, v212\n\t"
            "v_and_b32 v221, 0xffff0000, v220\n\t"
            "v_lshlrev_b32 v212, 16, v212\n\t"
            "v_lshlrev_b32 v220, 16, v220\n\t"
            "v_add_u32 v237, s48, v245\n\t"
            "ds_read_b32 v247, v247 offset:7856\n\t"
            "ds_read_b128 v[196:199], v252\n\t"
            "ds_write_b64 v248, v[192:193] offset:18816\n\t"
            "ds_write_b128 v236, v[176:179] offset:256\n\t"
            "v_and_b32 v179, 0xffff0000, v219\n\t"
            "v_lshlrev_b32 v178, 16, v219\n\t"
            "v_and_b32 v177, 0xffff0000, v218\n\t"
            "v_lshlrev_b32 v176, 16, v218\n\t"
            "v_and_b32 v219, 0xffff0000, v217\n\t"
            "v_lshlrev_b32 v218, 16, v217\n\t"
            "v_and_b32 v217, 0xffff0000, v216\n\t"
            "v_lshlrev_b32 v216, 16, v216\n\t"
            "v_add_u32 v236, s48, v244\n\t"
            "v_add_u32 v248, s48, v246\n\t"
            "s_mov_b64 exec, 1\n\t"
            "ds_inc_u32 v255, v238 offset:7860\n\t"
            "s_mov_b64 exec, -1\n\t"
            "s_waitcnt lgkmcnt(4)\n\t"
            "v_readfirstlane_b32 s77, v247\n\t"
            "s_cmp_ge_u32 s77, s70\n\t"
            "s_cbranch_scc0 .Lwb_spin%=\n\t"
            ".Lwb_go%=:\n\t"
            "s_bfe_u32 s57, s56, 0x20004\n\t"
            "s_lshl_b32 s58, s57, 3\n\t"
            "s_lshr_b32 s58, s55, s58\n\t"
            "s_lshl_b64 s[36:37], s[34:35], s58\n\t"
            "ds_read_b128 v[184:187], v242\n\t"
            "ds_read_b128 v[188:191], v243\n\t"
            "ds_read_b128 v[192:195], v242 offset:128\n\t"
            "ds_read_b128 v[172:175], v243 offset:128\n\t"
            "s_bitcmp1_b32 s57, 1\n\t"
            "s_cselect_b32 s58, 1177344, 0\n\t"
            "s_bitcmp1_b32 s57, 0\n\t"
            "s_cselect_b32 s57, 2304, 0\n\t"
            "s_add_u32 s57, s57, s58\n\t"
            "s_waitcnt lgkmcnt(7)\n\t"
            "s_mov_b64 exec, s[36:37]\n\t"
            "v_add_u32 v196, s57, v196\n\t"
            "s_mov_b64 exec, -1\n\t"
            "s_add_u32 s59, s59, s84\n\t"
            "s_movk_i32 s84, 800\n\t"
            "s_waitcnt lgkmcnt(2)\n\t"
            "v_mfma_f32_16x16x32_bf16 v[200:203], v[0:3], v[184:187], v[200:203]\n\t"
            "v_mfma_f32_16x16x32_bf16 v[200:203], v[4:7], v[188:191], v[200:203]\n\t"
            "v_mfma_f32_16x16x32_bf16 v[224:227], v[28:31], v[188:191], v[224:227]\n\t"
            "v_mfma_f32_16x16x32_bf16 v[224:227], v[24:27], v[184:187], v[224:227]\n\t"
            "v_mfma_f32_16x16x32_bf16 v[204:207], v[48:51], v[184:187], v[204:207]\n\t"
            "v_mfma_f32_16x16x32_bf16 v[204:207], v[52:55], v[188:191], v[204:207]\n\t"
            "v_mfma_f32_16x16x32_bf16 v[228:231], v[76:79], v[188:191], v[228:231]\n\t"
            "v_mfma_f32_16x16x32_bf16 v[228:231], v[72:75], v[184:187], v[228:231]\n\t"
            "v_mfma_f32_16x16x32_bf16 v[208:211], v[96:99], v[184:187], v[208:211]\n\t"
            "v_mfma_f32_16x16x32_bf16 v[208:211], v[100:103], v[188:191], v[208:211]\n\t"
            "v_mfma_f32_16x16x32_bf16 v[232:235], v[124:127], v[188:191], v[232:235]\n\t"
            "ds_read_b128 v[188:191], v243 offset:256\n\t"
            "v_mfma_f32_16x16x32_bf16 v[232:235], v[120:123], v[184:187], v[232:235]\n\t"
            "ds_read_b128 v[184:187], v242 offset:256\n\t"
            "s_waitcnt lgkmcnt(2)\n\t"
            "v_mfma_f32_16x16x32_bf16 v[200:203], v[8:11], v[192:195], v[200:203]\n\t"
            "v_mfma_f32_16x16x32_bf16 v[200:203], v[12:15], v[172:175], v[200:203]\n\t"
            "v_mfma_f32_16x16x32_bf16 v[224:227], v[36:39], v[172:175], v[224:227]\n\t"
            "v_mfma_f32_16x16x32_bf16 v[224:227], v[32:35], v[192:195], v[224:227]\n\t"
            "v_mfma_f32_16x16x32_bf16 v[204:207], v[56:59], v[192:195], v[204:207]\n\t"
            "v_mfma_f32_16x16x32_bf16 v[204:207], v[60:63], v[172:175], v[204:207]\n\t"
            "v_mfma_f32_16x16x32_bf16 v[228:231], v[84:87], v[172:175], v[228:231]\n\t"
            "v_mfma_f32_16x16x32_bf16 v[228:231], v[80:83], v[192:195], v[228:231]\n\t"
            "v_mfma_f32_16x16x32_bf16 v[208:211], v[104:107], v[192:195], v[208:211]\n\t"
            "v_mfma_f32_16x16x32_bf16 v[208:211], v[108:111], v[172:175], v[208:211]\n\t"
            "v_mfma_f32_16x16x32_bf16 v[232:235], v[132:135], v[172:175], v[232:235]\n\t"
            "ds_read_b128 v[172:175], v243 offset:6144\n\t"
            "v_mfma_f32_16x16x32_bf16 v[232:235], v[128:131], v[192:195], v[232:235]\n\t"
            "ds_read_b128 v[192:195], v242 offset:6144\n\t"
            "s_waitcnt lgkmcnt(2)\n\t"
            "v_mfma_f32_16x16x32_bf16 v[200:203], v[16:19], v[184:187], v[200:203]\n\t"
            "v_mfma_f32_16x16x32_bf16 v[200:203], v[20:23], v[188:191], v[200:203]\n\t"
            "v_mfma_f32_16x16x32_bf16 v[224:227], v[44:47], v[188:191], v[224:227]\n\t"
            "v_mfma_f32_16x16x32_bf16 v[224:227], v[40:43], v[184:187], v[224:227]\n\t"
            "v_mfma_f32_16x16x32_bf16 v[204:207], v[64:67], v[184:187], v[204:207]\n\t"
            "v_mfma_f32_16x16x32_bf16 v[204:207], v[68:71], v[188:191], v[204:207]\n\t"
            "v_mfma_f32_16x16x32_bf16 v[228:231], v[92:95], v[188:191], v[228:231]\n\t"
            "v_mfma_f32_16x16x32_bf16 v[228:231], v[88:91], v[184:187], v[228:231]\n\t"
            "s_nop 1\n\t"
            "v_cvt_pk_bf16_f32 v200, v200, v201\n\t"
            "v_mfma_f32_16x16x32_bf16 v[208:211], v[112:115], v[184:187], v[208:211]\n\t"
            "v_cvt_pk_bf16_f32 v201, v202, v203\n\t"
            "v_mfma_f32_16x16x32_bf16 v[208:211], v[116:119], v[188:191], v[208:211]\n\t"
            "v_cvt_pk_bf16_f32 v202, v224, v225\n\t"
            "v_cvt_pk_bf16_f32 v203, v226, v227\n\t"
            "v_mfma_f32_16x16x32_bf16 v[232:235], v[140:143], v[188:191], v[232:235]\n\t"
            "ds_read_b128 v[188:191], v243 offset:6272\n\t"
            "ds_read_b128 v[224:227], v236 offset:12288\n\t"
            "v_mfma_f32_16x16x32_bf16 v[232:235], v[136:139], v[184:187], v[232:235]\n\t"
            "ds_read_b128 v[184:187], v242 offset:6272\n\t"
            "buffer_store_dwordx4 v[200:203], v196, s[40:43], s45 offen sc1\n\t"
            "s_waitcnt lgkmcnt(3)\n\t"
            "v_mfma_f32_16x16x32_bf16 v[212:215], v[0:3], v[192:195], v[212:215]\n\t"
            "ds_read_b128 v[200:203], v236\n\t"
            "v_cvt_pk_bf16_f32 v204, v204, v205\n\t"
            "v_mfma_f32_16x16x32_bf16 v[212:215], v[4:7], v[172:175], v[212:215]\n\t"
            "v_cvt_pk_bf16_f32 v205, v206, v207\n\t"
            "v_cvt_pk_bf16_f32 v206, v228, v229\n\t"
            "v_mfma_f32_16x16x32_bf16 v[168:171], v[28:31], v[172:175], v[168:171]\n\t"
            "v_cvt_pk_bf16_f32 v207, v230, v231\n\t"
            "v_cvt_pk_bf16_f32 v208, v208, v209\n\t"
            "v_mfma_f32_16x16x32_bf16 v[168:171], v[24:27], v[192:195], v[168:171]\n\t"
            "ds_read_b128 v[228:231], v237 offset:12288\n\t"
            "buffer_store_dwordx4 v[204:207], v196, s[40:43], s45 offen offset:64 sc1\n\t"
            "v_mfma_f32_16x16x32_bf16 v[216:219], v[48:51], v[192:195], v[216:219]\n\t"
            "s_nop 0\n\t"
            "ds_read_b128 v[204:207], v237\n\t"
            "v_cvt_pk_bf16_f32 v209, v210, v211\n\t"
            "v_mfma_f32_16x16x32_bf16 v[216:219], v[52:55], v[172:175], v[216:219]\n\t"
            "v_cvt_pk_bf16_f32 v210, v232, v233\n\t"
            "v_cvt_pk_bf16_f32 v211, v234, v235\n\t"
            "v_mfma_f32_16x16x32_bf16 v[176:179], v[76:79], v[172:175], v[176:179]\n\t"
            "ds_read_b128 v[232:235], v248 offset:12288\n\t"
            "v_mfma_f32_16x16x32_bf16 v[176:179], v[72:75], v[192:195], v[176:179]\n\t"
            "buffer_store_dwordx4 v[208:211], v196, s[40:43], s45 offen offset:128 sc1\n\t"
            "v_mfma_f32_16x16x32_bf16 v[220:223], v[96:99], v[192:195], v[220:223]\n\t"
            "s_nop 0\n\t"
            "ds_read_b128 v[208:211], v248\n\t"
            "v_mfma_f32_16x16x32_bf16 v[220:223], v[100:103], v[172:175], v[220:223]\n\t"
            "v_mfma_f32_16x16x32_bf16 v[180:183], v[124:127], v[172:175], v[180:183]\n\t"
            "ds_read_b128 v[172:175], v243 offset:6400\n\t"
            "v_mfma_f32_16x16x32_bf16 v[180:183], v[120:123], v[192:195], v[180:183]\n\t"
            "ds_read_b128 v[192:195], v242 offset:6400\n\t"
            "s_waitcnt lgkmcnt(7)\n\t"
            "v_mfma_f32_16x16x32_bf16 v[212:215], v[8:11], v[184:187], v[212:215]\n\t"
            "v_mfma_f32_16x16x32_bf16 v[212:215], v[12:15], v[188:191], v[212:215]\n\t"
            "v_mfma_f32_16x16x32_bf16 v[168:171], v[36:39], v[188:191], v[168:171]\n\t"
            "v_mfma_f32_16x16x32_bf16 v[168:171], v[32:35], v[184:187], v[168:171]\n\t"
            "v_mfma_f32_16x16x32_bf16 v[216:219], v[56:59], v[184:187], v[216:219]\n\t"
            "v_mfma_f32_16x16x32_bf16 v[216:219], v[60:63], v[188:191], v[216:219]\n\t"
            "v_mfma_f32_16x16x32_bf16 v[176:179], v[84:87], v[188:191], v[176:179]\n\t"
            "v_mfma_f32_16x16x32_bf16 v[176:179], v[80:83], v[184:187], v[176:179]\n\t"
            "v_mfma_f32_16x16x32_bf16 v[220:223], v[104:107], v[184:187], v[220:223]\n\t"
            "v_mfma_f32_16x16x32_bf16 v[220:223], v[108:111], v[188:191], v[220:223]\n\t"
            "v_mfma_f32_16x16x32_bf16 v[180:183], v[132:135], v[188:191], v[180:183]\n\t"
            "ds_read_b128 v[188:191], v243 offset:12288\n\t"
            "v_mfma_f32_16x16x32_bf16 v[180:183], v[128:131], v[184:187], v[180:183]\n\t"
            "ds_read_b128 v[184:187], v242 offset:12288\n\t"
            "s_waitcnt lgkmcnt(2)\n\t"
            "v_mfma_f32_16x16x32_bf16 v[168:171], v[44:47], v[172:175], v[168:171]\n\t"
            "v_mfma_f32_16x16x32_bf16 v[168:171], v[40:43], v[192:195], v[168:171]\n\t"
            "v_mfma_f32_16x16x32_bf16 v[176:179], v[92:95], v[172:175], v[176:179]\n\t"
            "v_mfma_f32_16x16x32_bf16 v[176:179], v[88:91], v[192:195], v[176:179]\n\t"
            "v_mfma_f32_16x16x32_bf16 v[180:183], v[140:143], v[172:175], v[180:183]\n\t"
            "v_mfma_f32_16x16x32_bf16 v[180:183], v[136:139], v[192:195], v[180:183]\n\t"
            "v_mfma_f32_16x16x32_bf16 v[212:215], v[16:19], v[192:195], v[212:215]\n\t"
            "v_mfma_f32_16x16x32_bf16 v[212:215], v[20:23], v[172:175], v[212:215]\n\t"
            "s_nop 1\n\t"
            "v_cvt_pk_bf16_f32 v171, v170, v171\n\t"
            "v_mfma_f32_16x16x32_bf16 v[216:219], v[64:67], v[192:195], v[216:219]\n\t"
            "v_cvt_pk_bf16_f32 v170, v168, v169\n\t"
            "v_mfma_f32_16x16x32_bf16 v[216:219], v[68:71], v[172:175], v[216:219]\n\t"
            "v_cvt_pk_bf16_f32 v179, v178, v179\n\t"
            "v_cvt_pk_bf16_f32 v178, v176, v177\n\t"
            "v_mfma_f32_16x16x32_bf16 v[220:223], v[112:115], v[192:195], v[220:223]\n\t"
            "ds_read_b128 v[192:195], v242 offset:12416\n\t"
            "v_cvt_pk_bf16_f32 v183, v182, v183\n\t"
            "v_mfma_f32_16x16x32_bf16 v[220:223], v[116:119], v[172:175], v[220:223]\n\t"
            "ds_read_b128 v[172:175], v243 offset:12416\n\t"
            "v_cvt_pk_bf16_f32 v182, v180, v181\n\t"
            "s_waitcnt lgkmcnt(2)\n\t"
            "v_mfma_f32_16x16x32_bf16 v[144:147], v[0:3], v[184:187], v[144:147]\n\t"
            "v_cvt_pk_bf16_f32 v168, v212, v213\n\t"
            "v_cvt_pk_bf16_f32 v169, v214, v215\n\t"
            "v_mfma_f32_16x16x32_bf16 v[144:147], v[4:7], v[188:191], v[144:147]\n\t"
            "ds_read_b128 v[212:215], v236 offset:6144\n\t"
            "v_cvt_pk_bf16_f32 v176, v216, v217\n\t"
            "v_mfma_f32_16x16x32_bf16 v[148:151], v[28:31], v[188:191], v[148:151]\n\t"
            "v_cvt_pk_bf16_f32 v177, v218, v219\n\t"
            "v_cvt_pk_bf16_f32 v180, v220, v221\n\t"
            "v_mfma_f32_16x16x32_bf16 v[148:151], v[24:27], v[184:187], v[148:151]\n\t"
            "ds_read_b128 v[216:219], v237 offset:6144\n\t"
            "v_cvt_pk_bf16_f32 v181, v222, v223\n\t"
            "v_mfma_f32_16x16x32_bf16 v[152:155], v[48:51], v[184:187], v[152:155]\n\t"
            "ds_read_b128 v[220:223], v248 offset:6144\n\t"
            "v_mfma_f32_16x16x32_bf16 v[152:155], v[52:55], v[188:191], v[152:155]\n\t"
            "buffer_store_dwordx4 v[168:171], v197, s[40:43], s45 offen sc1\n\t"
            "v_mfma_f32_16x16x32_bf16 v[156:159], v[76:79], v[188:191], v[156:159]\n\t"
            "v_mfma_f32_16x16x32_bf16 v[156:159], v[72:75], v[184:187], v[156:159]\n\t"
            "buffer_store_dwordx4 v[176:179], v197, s[40:43], s45 offen offset:64 sc1\n\t"
            "v_mfma_f32_16x16x32_bf16 v[160:163], v[96:99], v[184:187], v[160:163]\n\t"
            "v_mfma_f32_16x16x32_bf16 v[160:163], v[100:103], v[188:191], v[160:163]\n\t"
            "buffer_store_dwordx4 v[180:183], v197, s[40:43], s45 offen offset:128 sc1\n\t"
            "v_mfma_f32_16x16x32_bf16 v[164:167], v[124:127], v[188:191], v[164:167]\n\t"
            "ds_read_b128 v[188:191], v243 offset:12544\n\t"
            "v_mfma_f32_16x16x32_bf16 v[164:167], v[120:123], v[184:187], v[164:167]\n\t"
            "ds_read_b128 v[184:187], v242 offset:12544\n\t"
            "s_waitcnt lgkmcnt(5)\n\t"
            "v_mfma_f32_16x16x32_bf16 v[144:147], v[8:11], v[192:195], v[144:147]\n\t"
            "v_mfma_f32_16x16x32_bf16 v[144:147], v[12:15], v[172:175], v[144:147]\n\t"
            "v_mfma_f32_16x16x32_bf16 v[148:151], v[36:39], v[172:175], v[148:151]\n\t"
            "v_mfma_f32_16x16x32_bf16 v[148:151], v[32:35], v[192:195], v[148:151]\n\t"
            "s_waitcnt lgkmcnt(0)\n\t"
            "s_barrier\n\t"
            "s_setprio 3\n\t"
            "v_mfma_f32_16x16x32_bf16 v[152:155], v[56:59], v[192:195], v[152:155]\n\t"
            "v_mfma_f32_16x16x32_bf16 v[152:155], v[60:63], v[172:175], v[152:155]\n\t"
            "v_mfma_f32_16x16x32_bf16 v[156:159], v[84:87], v[172:175], v[156:159]\n\t"
            "v_mfma_f32_16x16x32_bf16 v[156:159], v[80:83], v[192:195], v[156:159]\n\t"
            "v_mfma_f32_16x16x32_bf16 v[144:147], v[16:19], v[184:187], v[144:147]\n\t"
            "v_mfma_f32_16x16x32_bf16 v[144:147], v[20:23], v[188:191], v[144:147]\n\t"
            "v_mfma_f32_16x16x32_bf16 v[148:151], v[44:47], v[188:191], v[148:151]\n\t"
            "v_mfma_f32_16x16x32_bf16 v[148:151], v[40:43], v[184:187], v[148:151]\n\t"
            "v_mfma_f32_16x16x32_bf16 v[160:163], v[104:107], v[192:195], v[160:163]\n\t"
            "v_mfma_f32_16x16x32_bf16 v[160:163], v[108:111], v[172:175], v[160:163]\n\t"
            "v_mfma_f32_16x16x32_bf16 v[164:167], v[132:135], v[172:175], v[164:167]\n\t"
            "v_mfma_f32_16x16x32_bf16 v[164:167], v[128:131], v[192:195], v[164:167]\n\t"
            "s_nop 1\n\t"
            "v_cvt_pk_bf16_f32 v144, v144, v145\n\t"
            "v_mfma_f32_16x16x32_bf16 v[152:155], v[64:67], v[184:187], v[152:155]\n\t"
            "v_cvt_pk_bf16_f32 v145, v146, v147\n\t"
            "v_mfma_f32_16x16x32_bf16 v[152:155], v[68:71], v[188:191], v[152:155]\n\t"
            "v_cvt_pk_bf16_f32 v146, v148, v149\n\t"
            "v_cvt_pk_bf16_f32 v147, v150, v151\n\t"
            "v_mfma_f32_16x16x32_bf16 v[156:159], v[92:95], v[188:191], v[156:159]\n\t"
            "v_mfma_f32_16x16x32_bf16 v[156:159], v[88:91], v[184:187], v[156:159]\n\t"
            "buffer_store_dwordx4 v[144:147], v198, s[40:43], s45 offen sc1\n\t"
            "v_mfma_f32_16x16x32_bf16 v[160:163], v[112:115], v[184:187], v[160:163]\n\t"
            "s_nop 1\n\t"
            "v_cvt_pk_bf16_f32 v152, v152, v153\n\t"
            "v_mfma_f32_16x16x32_bf16 v[160:163], v[116:119], v[188:191], v[160:163]\n\t"
            "v_cvt_pk_bf16_f32 v153, v154, v155\n\t"
            "v_mfma_f32_16x16x32_bf16 v[164:167], v[140:143], v[188:191], v[164:167]\n\t"
            "v_cvt_pk_bf16_f32 v154, v156, v157\n\t"
            "v_mfma_f32_16x16x32_bf16 v[164:167], v[136:139], v[184:187], v[164:167]\n\t"
            "v_cvt_pk_bf16_f32 v155, v158, v159\n\t"
            "buffer_store_dwordx4 v[152:155], v198, s[40:43], s45 offen offset:64 sc1\n\t"
            "s_nop 1\n\t"
            "v_cvt_pk_bf16_f32 v160, v160, v161\n\t"
            "v_cvt_pk_bf16_f32 v161, v162, v163\n\t"
            "s_nop 1\n\t"
            "v_cvt_pk_bf16_f32 v162, v164, v165\n\t"
            "v_cvt_pk_bf16_f32 v163, v166, v167\n\t"
            "buffer_store_dwordx4 v[160:163], v198, s[40:43], s45 offen offset:128 sc1\n\t"
            "s_add_u32 s45, s46, s27\n\t"
            "s_mov_b32 s46, s47\n\t"
            "s_mov_b32 s47, s68\n\t"
            "s_xor_b32 s48, s48, s71\n\t"
            "s_xor_b32 s67, s67, s71\n\t"
            "s_add_u32 s76, s76, 4\n\t"
            "s_add_u32 s70, s70, 4\n\t"
            "s_lshl_b32 s56, s56, 2\n\t"
            "s_lshr_b32 s49, s26, 30\n\t"
            "s_or_b32 s56, s56, s49\n\t"
            "s_cmp_lt_u32 s46, 0x7000000\n\t"
            "s_cbranch_scc1 .Lwb_loop%=\n\t"
            "s_branch .Lwb_end%=\n\t"
            ".Lwb_spin%=:\n\t"
            "s_cmp_eq_u32 s83, 0\n\t"
            "s_cbranch_scc1 .Lwb_go%=\n\t"
            "s_sub_u32 s83, s83, 1\n\t"
            "s_sleep 2\n\t"
            "v_mov_b32 v247, 0\n\t"
            "ds_read_b32 v247, v247 offset:7856\n\t"
            "s_waitcnt lgkmcnt(0)\n\t"
            "v_readfirstlane_b32 s77, v247\n\t"
            "s_cmp_ge_u32 s77, s70\n\t"
            "s_cbranch_scc0 .Lwb_spin%=\n\t"
            "s_branch .Lwb_go%=\n\t"
            ".Lwb_end%=:\n\t"
            "s_waitcnt lgkmcnt(0)\n\t"
            "s_nop 7\n\t"
            "ds_read_b128 v[196:199], v252\n\t"
            "s_bfe_u32 s57, s56, 0x20004\n\t"
            "s_lshl_b32 s58, s57, 3\n\t"
            "s_lshr_b32 s58, s55, s58\n\t"
            "s_lshl_b64 s[36:37], s[34:35], s58\n\t"
            "s_bitcmp1_b32 s57, 1\n\t"
            "s_cselect_b32 s58, 1177344, 0\n\t"
            "s_bitcmp1_b32 s57, 0\n\t"
            "s_cselect_b32 s57, 2304, 0\n\t"
            "s_add_u32 s57, s57, s58\n\t"
            "v_and_b32 v151, 0xffff0000, v227\n\t"
            "v_lshlrev_b32 v150, 16, v227\n\t"
            "v_and_b32 v149, 0xffff0000, v226\n\t"
            "v_lshlrev_b32 v148, 16, v226\n\t"
            "v_and_b32 v147, 0xffff0000, v225\n\t"
            "v_lshlrev_b32 v146, 16, v225\n\t"
            "v_and_b32 v145, 0xffff0000, v224\n\t"
            "v_lshlrev_b32 v144, 16, v224\n\t"
            "v_and_b32 v159, 0xffff0000, v231\n\t"
            "v_lshlrev_b32 v158, 16, v231\n\t"
            "v_and_b32 v157, 0xffff0000, v230\n\t"
            "v_lshlrev_b32 v156, 16, v230\n\t"
            "v_and_b32 v155, 0xffff0000, v229\n\t"
            "v_lshlrev_b32 v154, 16, v229\n\t"
            "v_and_b32 v153, 0xffff0000, v228\n\t"
            "v_lshlrev_b32 v152, 16, v228\n\t"
            "v_and_b32 v167, 0xffff0000, v235\n\t"
            "v_lshlrev_b32 v166, 16, v235\n\t"
            "v_and_b32 v165, 0xffff0000, v234\n\t"
            "v_lshlrev_b32 v164, 16, v234\n\t"
            "v_and_b32 v163, 0xffff0000, v233\n\t"
            "s_waitcnt lgkmcnt(0)\n\t"
            "s_mov_b64 exec, s[36:37]\n\t"
            "v_add_u32 v196, s57, v196\n\t"
            "s_mov_b64 exec, -1\n\t"
            "v_lshlrev_b32 v162, 16, v233\n\t"
            "v_and_b32 v161, 0xffff0000, v232\n\t"
            "v_lshlrev_b32 v160, 16, v232\n\t"
            ".Lwk_t1%=:\n\t"
            "v_mov_b32 v247, 0\n\t"
            "ds_read_b32 v247, v247 offset:7856\n\t"
            "s_waitcnt lgkmcnt(0)\n\t"
            "v_readfirstlane_b32 s77, v247\n\t"
            "s_cmp_ge_u32 s77, s70\n\t"
            "s_cbranch_scc1 .Lwk_t2%=\n\t"
            "s_cmp_eq_u32 s83, 0\n\t"
            "s_cbranch_scc1 .Lwk_t2%=\n\t"
            "s_sub_u32 s83, s83, 1\n\t"
            "s_sleep 1\n\t"
            "s_branch .Lwk_t1%=\n\t"
            ".Lwk_t2%=:\n\t"
            "v_and_b32 v227, 0xffff0000, v203\n\t"
            "v_lshlrev_b32 v226, 16, v203\n\t"
            "v_and_b32 v225, 0xffff0000, v202\n\t"
            "v_lshlrev_b32 v224, 16, v202\n\t"
            "v_and_b32 v231, 0xffff0000, v207\n\t"
            "v_lshlrev_b32 v230, 16, v207\n\t"
            "v_and_b32 v229, 0xffff0000, v206\n\t"
            "v_lshlrev_b32 v228, 16, v206\n\t"
            "v_and_b32 v235, 0xffff0000, v211\n\t"
            "v_lshlrev_b32 v234, 16, v211\n\t"
            "v_and_b32 v233, 0xffff0000, v210\n\t"
            "v_lshlrev_b32 v232, 16, v210\n\t"
            "v_and_b32 v203, 0xffff0000, v201\n\t"
            "v_lshlrev_b32 v202, 16, v201\n\t"
            "v_and_b32 v207, 0xffff0000, v205\n\t"
            "v_lshlrev_b32 v206, 16, v205\n\t"
            "v_and_b32 v211, 0xffff0000, v209\n\t"
            "v_lshlrev_b32 v210, 16, v209\n\t"
            "v_and_b32 v201, 0xffff0000, v200\n\t"
            "v_and_b32 v205, 0xffff0000, v204\n\t"
            "v_and_b32 v209, 0xffff0000, v208\n\t"
            "v_lshlrev_b32 v200, 16, v200\n\t"
            "v_lshlrev_b32 v204, 16, v204\n\t"
            "v_lshlrev_b32 v208, 16, v208\n\t"
            "v_and_b32 v171, 0xffff0000, v215\n\t"
            "v_lshlrev_b32 v170, 16, v215\n\t"
            "v_and_b32 v169, 0xffff0000, v214\n\t"
            "v_lshlrev_b32 v168, 16, v214\n\t"
            "v_and_b32 v179, 0xffff0000, v219\n\t"
            "v_lshlrev_b32 v178, 16, v219\n\t"
            "v_and_b32 v177, 0xffff0000, v218\n\t"
            "v_lshlrev_b32 v176, 16, v218\n\t"
            "v_and_b32 v183, 0xffff0000, v223\n\t"
            "v_lshlrev_b32 v182, 16, v223\n\t"
            "v_and_b32 v181, 0xffff0000, v222\n\t"
            "v_lshlrev_b32 v180, 16, v222\n\t"
            "v_and_b32 v215, 0xffff0000, v213\n\t"
            "v_lshlrev_b32 v214, 16, v213\n\t"
            "v_and_b32 v219, 0xffff0000, v217\n\t"
            "v_lshlrev_b32 v218, 16, v217\n\t"
            "v_and_b32 v223, 0xffff0000, v221\n\t"
            "v_lshlrev_b32 v222, 16, v221\n\t"
            "v_and_b32 v213, 0xffff0000, v212\n\t"
            "v_and_b32 v217, 0xffff0000, v216\n\t"
            "v_and_b32 v221, 0xffff0000, v220\n\t"
            "v_lshlrev_b32 v212, 16, v212\n\t"
            "v_lshlrev_b32 v216, 16, v216\n\t"
            "v_lshlrev_b32 v220, 16, v220\n\t"
            "ds_read_b128 v[184:187], v242\n\t"
            "ds_read_b128 v[188:191], v243\n\t"
            "ds_read_b128 v[192:195], v242 offset:128\n\t"
            "ds_read_b128 v[172:175], v243 offset:128\n\t"
            "s_waitcnt lgkmcnt(2)\n\t"
            "v_mfma_f32_16x16x32_bf16 v[200:203], v[0:3], v[184:187], v[200:203]\n\t"
            "v_mfma_f32_16x16x32_bf16 v[200:203], v[4:7], v[188:191], v[200:203]\n\t"
            "v_mfma_f32_16x16x32_bf16 v[224:227], v[28:31], v[188:191], v[224:227]\n\t"
            "v_mfma_f32_16x16x32_bf16 v[224:227], v[24:27], v[184:187], v[224:227]\n\t"
            "v_mfma_f32_16x16x32_bf16 v[204:207], v[48:51], v[184:187], v[204:207]\n\t"
            "v_mfma_f32_16x16x32_bf16 v[204:207], v[52:55], v[188:191], v[204:207]\n\t"
            "v_mfma_f32_16x16x32_bf16 v[228:231], v[76:79], v[188:191], v[228:231]\n\t"
            "v_mfma_f32_16x16x32_bf16 v[228:231], v[72:75], v[184:187], v[228:231]\n\t"
            "v_mfma_f32_16x16x32_bf16 v[208:211], v[96:99], v[184:187], v[208:211]\n\t"
            "v_mfma_f32_16x16x32_bf16 v[208:211], v[100:103], v[188:191], v[208:211]\n\t"
            "v_mfma_f32_16x16x32_bf16 v[232:235], v[124:127], v[188:191], v[232:235]\n\t"
            "ds_read_b128 v[188:191], v243 offset:256\n\t"
            "v_mfma_f32_16x16x32_bf16 v[232:235], v[120:123], v[184:187], v[232:235]\n\t"
            "ds_read_b128 v[184:187], v242 offset:256\n\t"
            "s_waitcnt lgkmcnt(2)\n\t"
            "v_mfma_f32_16x16x32_bf16 v[200:203], v[8:11], v[192:195], v[200:203]\n\t"
            "v_mfma_f32_16x16x32_bf16 v[200:203], v[12:15], v[172:175], v[200:203]\n\t"
            "v_mfma_f32_16x16x32_bf16 v[224:227], v[36:39], v[172:175], v[224:227]\n\t"
            "v_mfma_f32_16x16x32_bf16 v[224:227], v[32:35], v[192:195], v[224:227]\n\t"
            "v_mfma_f32_16x16x32_bf16 v[204:207], v[56:59], v[192:195], v[204:207]\n\t"
            "v_mfma_f32_16x16x32_bf16 v[204:207], v[60:63], v[172:175], v[204:207]\n\t"
            "v_mfma_f32_16x16x32_bf16 v[228:231], v[84:87], v[172:175], v[228:231]\n\t"
            "v_mfma_f32_16x16x32_bf16 v[228:231], v[80:83], v[192:195], v[228:231]\n\t"
            "v_mfma_f32_16x16x32_bf16 v[208:211], v[104:107], v[192:195], v[208:211]\n\t"
            "v_mfma_f32_16x16x32_bf16 v[208:211], v[108:111], v[172:175], v[208:211]\n\t"
            "v_mfma_f32_16x16x32_bf16 v[232:235], v[132:135], v[172:175], v[232:235]\n\t"
            "ds_read_b128 v[172:175], v243 offset:6144\n\t"
            "v_mfma_f32_16x16x32_bf16 v[232:235], v[128:131], v[192:195], v[232:235]\n\t"
            "ds_read_b128 v[192:195], v242 offset:6144\n\t"
            "s_waitcnt lgkmcnt(2)\n\t"
            "v_mfma_f32_16x16x32_bf16 v[200:203], v[16:19], v[184:187], v[200:203]\n\t"
            "v_mfma_f32_16x16x32_bf16 v[200:203], v[20:23], v[188:191], v[200:203]\n\t"
            "v_mfma_f32_16x16x32_bf16 v[224:227], v[44:47], v[188:191], v[224:227]\n\t"
            "v_mfma_f32_16x16x32_bf16 v[224:227], v[40:43], v[184:187], v[224:227]\n\t"
            "v_mfma_f32_16x16x32_bf16 v[204:207], v[64:67], v[184:187], v[204:207]\n\t"
            "v_mfma_f32_16x16x32_bf16 v[204:207], v[68:71], v[188:191], v[204:207]\n\t"
            "v_mfma_f32_16x16x32_bf16 v[228:231], v[92:95], v[188:191], v[228:231]\n\t"
            "v_mfma_f32_16x16x32_bf16 v[228:231], v[88:91], v[184:187], v[228:231]\n\t"
            "s_nop 1\n\t"
            "v_cvt_pk_bf16_f32 v200, v200, v201\n\t"
            "v_mfma_f32_16x16x32_bf16 v[208:211], v[112:115], v[184:187], v[208:211]\n\t"
            "v_cvt_pk_bf16_f32 v201, v202, v203\n\t"
            "v_mfma_f32_16x16x32_bf16 v[208:211], v[116:119], v[188:191], v[208:211]\n\t"
            "v_cvt_pk_bf16_f32 v202, v224, v225\n\t"
            "v_cvt_pk_bf16_f32 v203, v226, v227\n\t"
            "v_mfma_f32_16x16x32_bf16 v[232:235], v[140:143], v[188:191], v[232:235]\n\t"
            "ds_read_b128 v[188:191], v243 offset:6272\n\t"
            "v_cvt_pk_bf16_f32 v204, v204, v205\n\t"
            "v_mfma_f32_16x16x32_bf16 v[232:235], v[136:139], v[184:187], v[232:235]\n\t"
            "ds_read_b128 v[184:187], v242 offset:6272\n\t"
            "buffer_store_dwordx4 v[200:203], v196, s[40:43], s45 offen sc1\n\t"
            "s_waitcnt lgkmcnt(2)\n\t"
            "v_mfma_f32_16x16x32_bf16 v[212:215], v[0:3], v[192:195], v[212:215]\n\t"
            "v_cvt_pk_bf16_f32 v205, v206, v207\n\t"
            "v_cvt_pk_bf16_f32 v206, v228, v229\n\t"
            "v_mfma_f32_16x16x32_bf16 v[212:215], v[4:7], v[172:175], v[212:215]\n\t"
            "v_cvt_pk_bf16_f32 v207, v230, v231\n\t"
            "v_cvt_pk_bf16_f32 v208, v208, v209\n\t"
            "v_mfma_f32_16x16x32_bf16 v[168:171], v[28:31], v[172:175], v[168:171]\n\t"
            "v_cvt_pk_bf16_f32 v209, v210, v211\n\t"
            "v_cvt_pk_bf16_f32 v210, v232, v233\n\t"
            "v_mfma_f32_16x16x32_bf16 v[168:171], v[24:27], v[192:195], v[168:171]\n\t"
            "buffer_store_dwordx4 v[204:207], v196, s[40:43], s45 offen offset:64 sc1\n\t"
            "v_mfma_f32_16x16x32_bf16 v[216:219], v[48:51], v[192:195], v[216:219]\n\t"
            "v_cvt_pk_bf16_f32 v211, v234, v235\n\t"
            "v_mfma_f32_16x16x32_bf16 v[216:219], v[52:55], v[172:175], v[216:219]\n\t"
            "buffer_store_dwordx4 v[208:211], v196, s[40:43], s45 offen offset:128 sc1\n\t"
            "v_mfma_f32_16x16x32_bf16 v[176:179], v[76:79], v[172:175], v[176:179]\n\t"
            "v_mfma_f32_16x16x32_bf16 v[176:179], v[72:75], v[192:195], v[176:179]\n\t"
            "v_mfma_f32_16x16x32_bf16 v[220:223], v[96:99], v[192:195], v[220:223]\n\t"
            "v_mfma_f32_16x16x32_bf16 v[220:223], v[100:103], v[172:175], v[220:223]\n\t"
            "v_mfma_f32_16x16x32_bf16 v[180:183], v[124:127], v[172:175], v[180:183]\n\t"
            "ds_read_b128 v[172:175], v243 offset:6400\n\t"
            "v_mfma_f32_16x16x32_bf16 v[180:183], v[120:123], v[192:195], v[180:183]\n\t"
            "ds_read_b128 v[192:195], v242 offset:6400\n\t"
            "s_waitcnt lgkmcnt(2)\n\t"
            "v_mfma_f32_16x16x32_bf16 v[212:215], v[8:11], v[184:187], v[212:215]\n\t"
            "v_mfma_f32_16x16x32_bf16 v[212:215], v[12:15], v[188:191], v[212:215]\n\t"
            "v_mfma_f32_16x16x32_bf16 v[168:171], v[36:39], v[188:191], v[168:171]\n\t"
            "v_mfma_f32_16x16x32_bf16 v[168:171], v[32:35], v[184:187], v[168:171]\n\t"
            "v_mfma_f32_16x16x32_bf16 v[216:219], v[56:59], v[184:187], v[216:219]\n\t"
            "v_mfma_f32_16x16x32_bf16 v[216:219], v[60:63], v[188:191], v[216:219]\n\t"
            "v_mfma_f32_16x16x32_bf16 v[176:179], v[84:87], v[188:191], v[176:179]\n\t"
            "v_mfma_f32_16x16x32_bf16 v[176:179], v[80:83], v[184:187], v[176:179]\n\t"
            "v_mfma_f32_16x16x32_bf16 v[220:223], v[104:107], v[184:187], v[220:223]\n\t"
            "v_mfma_f32_16x16x32_bf16 v[220:223], v[108:111], v[188:191], v[220:223]\n\t"
            "v_mfma_f32_16x16x32_bf16 v[180:183], v[132:135], v[188:191], v[180:183]\n\t"
            "ds_read_b128 v[188:191], v243 offset:12288\n\t"
            "v_mfma_f32_16x16x32_bf16 v[180:183], v[128:131], v[184:187], v[180:183]\n\t"
            "ds_read_b128 v[184:187], v242 offset:12288\n\t"
            "s_waitcnt lgkmcnt(2)\n\t"
            "v_mfma_f32_16x16x32_bf16 v[212:215], v[16:19], v[192:195], v[212:215]\n\t"
            "v_mfma_f32_16x16x32_bf16 v[212:215], v[20:23], v[172:175], v[212:215]\n\t"
            "v_mfma_f32_16x16x32_bf16 v[168:171], v[44:47], v[172:175], v[168:171]\n\t"
            "v_mfma_f32_16x16x32_bf16 v[168:171], v[40:43], v[192:195], v[168:171]\n\t"
            "v_mfma_f32_16x16x32_bf16 v[216:219], v[64:67], v[192:195], v[216:219]\n\t"
            "v_mfma_f32_16x16x32_bf16 v[216:219], v[68:71], v[172:175], v[216:219]\n\t"
            "v_mfma_f32_16x16x32_bf16 v[176:179], v[92:95], v[172:175], v[176:179]\n\t"
            "v_mfma_f32_16x16x32_bf16 v[176:179], v[88:91], v[192:195], v[176:179]\n\t"
            "v_mfma_f32_16x16x32_bf16 v[220:223], v[112:115], v[192:195], v[220:223]\n\t"
            "v_mfma_f32_16x16x32_bf16 v[220:223], v[116:119], v[172:175], v[220:223]\n\t"
            "s_nop 1\n\t"
            "v_cvt_pk_bf16_f32 v171, v170, v171\n\t"
            "v_mfma_f32_16x16x32_bf16 v[180:183], v[140:143], v[172:175], v[180:183]\n\t"
            "ds_read_b128 v[172:175], v243 offset:12416\n\t"
            "v_cvt_pk_bf16_f32 v170, v168, v169\n\t"
            "v_mfma_f32_16x16x32_bf16 v[180:183], v[136:139], v[192:195], v[180:183]\n\t"
            "ds_read_b128 v[192:195], v242 offset:12416\n\t"
            "v_cvt_pk_bf16_f32 v168, v212, v213\n\t"
            "s_waitcnt lgkmcnt(2)\n\t"
            "v_mfma_f32_16x16x32_bf16 v[144:147], v[0:3], v[184:187], v[144:147]\n\t"
            "v_cvt_pk_bf16_f32 v169, v214, v215\n\t"
            "v_cvt_pk_bf16_f32 v179, v178, v179\n\t"
            "v_mfma_f32_16x16x32_bf16 v[144:147], v[4:7], v[188:191], v[144:147]\n\t"
            "buffer_store_dwordx4 v[168:171], v197, s[40:43], s45 offen sc1\n\t"
            "v_mfma_f32_16x16x32_bf16 v[148:151], v[28:31], v[188:191], v[148:151]\n\t"
            "v_cvt_pk_bf16_f32 v178, v176, v177\n\t"
            "v_cvt_pk_bf16_f32 v176, v216, v217\n\t"
            "v_mfma_f32_16x16x32_bf16 v[148:151], v[24:27], v[184:187], v[148:151]\n\t"
            "v_cvt_pk_bf16_f32 v177, v218, v219\n\t"
            "v_cvt_pk_bf16_f32 v183, v182, v183\n\t"
            "v_mfma_f32_16x16x32_bf16 v[152:155], v[48:51], v[184:187], v[152:155]\n\t"
            "v_cvt_pk_bf16_f32 v182, v180, v181\n\t"
            "v_cvt_pk_bf16_f32 v180, v220, v221\n\t"
            "v_mfma_f32_16x16x32_bf16 v[152:155], v[52:55], v[188:191], v[152:155]\n\t"
            "buffer_store_dwordx4 v[176:179], v197, s[40:43], s45 offen offset:64 sc1\n\t"
            "v_mfma_f32_16x16x32_bf16 v[156:159], v[76:79], v[188:191], v[156:159]\n\t"
            "v_cvt_pk_bf16_f32 v181, v222, v223\n\t"
            "v_mfma_f32_16x16x32_bf16 v[156:159], v[72:75], v[184:187], v[156:159]\n\t"
            "buffer_store_dwordx4 v[180:183], v197, s[40:43], s45 offen offset:128 sc1\n\t"
            "v_mfma_f32_16x16x32_bf16 v[160:163], v[96:99], v[184:187], v[160:163]\n\t"
            "v_mfma_f32_16x16x32_bf16 v[160:163], v[100:103], v[188:191], v[160:163]\n\t"
            "v_mfma_f32_16x16x32_bf16 v[164:167], v[124:127], v[188:191], v[164:167]\n\t"
            "ds_read_b128 v[188:191], v243 offset:12544\n\t"
            "v_mfma_f32_16x16x32_bf16 v[164:167], v[120:123], v[184:187], v[164:167]\n\t"
            "ds_read_b128 v[184:187], v242 offset:12544\n\t"
            "s_waitcnt lgkmcnt(2)\n\t"
            "v_mfma_f32_16x16x32_bf16 v[144:147], v[8:11], v[192:195], v[144:147]\n\t"
            "v_mfma_f32_16x16x32_bf16 v[144:147], v[12:15], v[172:175], v[144:147]\n\t"
            "v_mfma_f32_16x16x32_bf16 v[148:151], v[36:39], v[172:175], v[148:151]\n\t"
            "v_mfma_f32_16x16x32_bf16 v[148:151], v[32:35], v[192:195], v[148:151]\n\t"
            "v_mfma_f32_16x16x32_bf16 v[152:155], v[56:59], v[192:195], v[152:155]\n\t"
            "v_mfma_f32_16x16x32_bf16 v[152:155], v[60:63], v[172:175], v[152:155]\n\t"
            "v_mfma_f32_16x16x32_bf16 v[156:159], v[84:87], v[172:175], v[156:159]\n\t"
            "v_mfma_f32_16x16x32_bf16 v[156:159], v[80:83], v[192:195], v[156:159]\n\t"
            "s_waitcnt lgkmcnt(0)\n\t"
            "v_mfma_f32_16x16x32_bf16 v[144:147], v[16:19], v[184:187], v[144:147]\n\t"
            "v_mfma_f32_16x16x32_bf16 v[144:147], v[20:23], v[188:191], v[144:147]\n\t"
            "v_mfma_f32_16x16x32_bf16 v[148:151], v[44:47], v[188:191], v[148:151]\n\t"
            "v_mfma_f32_16x16x32_bf16 v[148:151], v[40:43], v[184:187], v[148:151]\n\t"
            "v_mfma_f32_16x16x32_bf16 v[160:163], v[104:107], v[192:195], v[160:163]\n\t"
            "v_mfma_f32_16x16x32_bf16 v[160:163], v[108:111], v[172:175], v[160:163]\n\t"
            "v_mfma_f32_16x16x32_bf16 v[164:167], v[132:135], v[172:175], v[164:167]\n\t"
            "v_mfma_f32_16x16x32_bf16 v[164:167], v[128:131], v[192:195], v[164:167]\n\t"
            "s_nop 1\n\t"
            "v_cvt_pk_bf16_f32 v144, v144, v145\n\t"
            "v_mfma_f32_16x16x32_bf16 v[152:155], v[64:67], v[184:187], v[152:155]\n\t"
            "v_cvt_pk_bf16_f32 v145, v146, v147\n\t"
            "v_mfma_f32_16x16x32_bf16 v[152:155], v[68:71], v[188:191], v[152:155]\n\t"
            "v_cvt_pk_bf16_f32 v146, v148, v149\n\t"
            "v_cvt_pk_bf16_f32 v147, v150, v151\n\t"
            "v_mfma_f32_16x16x32_bf16 v[156:159], v[92:95], v[188:191], v[156:159]\n\t"
            "v_mfma_f32_16x16x32_bf16 v[156:159], v[88:91], v[184:187], v[156:159]\n\t"
            "buffer_store_dwordx4 v[144:147], v198, s[40:43], s45 offen sc1\n\t"
            "v_mfma_f32_16x16x32_bf16 v[160:163], v[112:115], v[184:187], v[160:163]\n\t"
            "s_nop 1\n\t"
            "v_cvt_pk_bf16_f32 v152, v152, v153\n\t"
            "v_mfma_f32_16x16x32_bf16 v[160:163], v[116:119], v[188:191], v[160:163]\n\t"
            "v_cvt_pk_bf16_f32 v153, v154, v155\n\t"
            "v_mfma_f32_16x16x32_bf16 v[164:167], v[140:143], v[188:191], v[164:167]\n\t"
            "v_cvt_pk_bf16_f32 v154, v156, v157\n\t"
            "v_mfma_f32_16x16x32_bf16 v[164:167], v[136:139], v[184:187], v[164:167]\n\t"
            "v_cvt_pk_bf16_f32 v155, v158, v159\n\t"
            "buffer_store_dwordx4 v[152:155], v198, s[40:43], s45 offen offset:64 sc1\n\t"
            "s_nop 1\n\t"
            "v_cvt_pk_bf16_f32 v160, v160, v161\n\t"
            "v_cvt_pk_bf16_f32 v161, v162, v163\n\t"
            "s_nop 1\n\t"
            "v_cvt_pk_bf16_f32 v162, v164, v165\n\t"
            "v_cvt_pk_bf16_f32 v163, v166, v167\n\t"
            "buffer_store_dwordx4 v[160:163], v198, s[40:43], s45 offen offset:128 sc1\n\t"
            "s_nop 7\n\t"
            "s_branch .Lwk_pe%=\n\t"
            "wk_conv:\n\t"
            "s_nop 7\n\t"
            "ds_read_b128 v[168:171], v254\n\t"
            "ds_read_b64 v[144:145], v253\n\t"
            "ds_read_b64 v[146:147], v253 offset:96\n\t"
            "ds_read_b64 v[148:149], v253 offset:1744\n\t"
            "ds_read_b64 v[150:151], v253 offset:1840\n\t"
            "ds_read_b128 v[172:175], v254 offset:64\n\t"
            "ds_read_b64 v[152:153], v253 offset:8\n\t"
            "ds_read_b64 v[154:155], v253 offset:104\n\t"
            "ds_read_b64 v[156:157], v253 offset:1752\n\t"
            "ds_read_b64 v[158:159], v253 offset:1848\n\t"
            "ds_read_b64 v[160:161], v253 offset:16\n\t"
            "ds_read_b64 v[162:163], v253 offset:112\n\t"
            "ds_read_b64 v[164:165], v253 offset:1760\n\t"
            "ds_read_b64 v[166:167], v253 offset:1856\n\t"
            "v_mov_b64 v[176:177], 0\n\t"
            "v_mov_b64 v[178:179], 0\n\t"
            "v_mov_b64 v[180:181], 0\n\t"
            "v_mov_b64 v[182:183], 0\n\t"
            "v_mov_b64 v[184:185], 0\n\t"
            "v_mov_b64 v[186:187], 0\n\t"
            "v_mov_b64 v[188:189], 0\n\t"
            "v_mov_b64 v[190:191], 0\n\t"
            "v_add_u32 v236, s67, v250\n\t"
            "v_xor_b32 v237, 16, v236\n\t"
            "v_add_u32 v248, s67, v251\n\t"
            "s_waitcnt lgkmcnt(9)\n\t"
            "v_smfmac_f32_16x16x64_bf16 v[176:179], v[168:171], v[144:151], v255\n\t"
            "ds_read_b128 v[168:171], v254 offset:128\n\t"
            "ds_read_b64 v[144:145], v253 offset:24\n\t"
            "s_waitcnt lgkmcnt(6)\n\t"
            "v_smfmac_f32_16x16x64_bf16 v[180:183], v[172:175], v[152:159], v255\n\t"
            "ds_read_b64 v[146:147], v253 offset:120\n\t"
            "ds_read_b64 v[148:149], v253 offset:1768\n\t"
            "ds_read_b64 v[150:151], v253 offset:1864\n\t"
            "ds_read_b128 v[172:175], v254 offset:192\n\t"
            "ds_read_b64 v[152:153], v253 offset:32\n\t"
            "ds_read_b64 v[154:155], v253 offset:128\n\t"
            "ds_read_b64 v[156:157], v253 offset:1776\n\t"
            "ds_read_b64 v[158:159], v253 offset:1872\n\t"
            "v_cvt_pk_bf16_f32 v176, v176, v177\n\t"
            "v_cvt_pk_bf16_f32 v177, v178, v179\n\t"
            "v_cvt_pk_bf16_f32 v178, v180, v181\n\t"
            "v_cvt_pk_bf16_f32 v179, v182, v183\n\t"
            "v_mfma_f32_4x4x4_16b_bf16 v[196:199], v[176:177], v[176:177], 0\n\t"
            "ds_write_b128 v236, v[176:179]\n\t"
            "v_mfma_f32_16x16x32_bf16 v[192:195], v[238:241], v[176:179], 0\n\t"
            "v_mov_b64 v[176:177], 0\n\t"
            "v_mov_b64 v[180:181], 0\n\t"
            "v_mfma_f32_4x4x4_16b_bf16 v[196:199], v[178:179], v[178:179], v[196:199]\n\t"
            "v_mov_b64 v[178:179], 0\n\t"
            "v_mov_b64 v[182:183], 0\n\t"
            "s_waitcnt lgkmcnt(10)\n\t"
            "v_smfmac_f32_16x16x64_bf16 v[184:187], v[168:171], v[160:167], v255\n\t"
            "ds_read_b128 v[168:171], v254 offset:256\n\t"
            "ds_read_b64 v[160:161], v253 offset:40\n\t"
            "ds_read_b64 v[162:163], v253 offset:136\n\t"
            "ds_read_b64 v[164:165], v253 offset:1784\n\t"
            "ds_read_b64 v[166:167], v253 offset:1880\n\t"
            "s_waitcnt lgkmcnt(10)\n\t"
            "v_smfmac_f32_16x16x64_bf16 v[188:191], v[172:175], v[144:151], v255\n\t"
            "ds_read_b128 v[172:175], v254 offset:320\n\t"
            "ds_read_b64 v[144:145], v253 offset:48\n\t"
            "ds_read_b64 v[146:147], v253 offset:144\n\t"
            "s_cbranch_scc1 .Lwk_np1%=\n\t"
            "s_mov_b32 exec_hi, 15\n\t"
            "buffer_load_dwordx4 v252, s[84:87], s89 offen sc1 lds\n\t"
            "s_mov_b32 exec_hi, -1\n\t"
            ".Lwk_np1%=:\n\t"
            "ds_read_b64 v[148:149], v253 offset:1792\n\t"
            "ds_read_b64 v[150:151], v253 offset:1888\n\t"
            "v_cvt_pk_bf16_f32 v184, v184, v185\n\t"
            "v_cvt_pk_bf16_f32 v185, v186, v187\n\t"
            "s_nop 0\n\t"
            "v_cvt_pk_bf16_f32 v186, v188, v189\n\t"
            "v_cvt_pk_bf16_f32 v187, v190, v191\n\t"
            "v_mfma_f32_4x4x4_16b_bf16 v[196:199], v[184:185], v[184:185], v[196:199]\n\t"
            "s_waitcnt lgkmcnt(14)\n\t"
            "ds_write_b128 v237, v[184:187]\n\t"
            "v_mfma_f32_16x16x32_bf16 v[192:195], v[238:241], v[184:187], v[192:195]\n\t"
            "v_mov_b64 v[184:185], 0\n\t"
            "v_mov_b64 v[188:189], 0\n\t"
            "v_mfma_f32_4x4x4_16b_bf16 v[196:199], v[186:187], v[186:187], v[196:199]\n\t"
            "v_mov_b64 v[186:187], 0\n\t"
            "v_mov_b64 v[190:191], 0\n\t"
            "s_waitcnt lgkmcnt(10)\n\t"
            "v_smfmac_f32_16x16x64_bf16 v[176:179], v[168:171], v[152:159], v255\n\t"
            "ds_read_b128 v[168:171], v254 offset:384\n\t"
            "ds_read_b64 v[152:153], v253 offset:56\n\t"
            "ds_read_b64 v[154:155], v253 offset:152\n\t"
            "ds_read_b64 v[156:157], v253 offset:1800\n\t"
            "ds_read_b64 v[158:159], v253 offset:1896\n\t"
            "s_waitcnt lgkmcnt(10)\n\t"
            "v_smfmac_f32_16x16x64_bf16 v[180:183], v[172:175], v[160:167], v255\n\t"
            "ds_read_b128 v[172:175], v254 offset:448\n\t"
            "ds_read_b64 v[160:161], v253 offset:64\n\t"
            "ds_read_b64 v[162:163], v253 offset:160\n\t"
            "ds_read_b64 v[164:165], v253 offset:1808\n\t"
            "ds_read_b64 v[166:167], v253 offset:1904\n\t"
            "v_cvt_pk_bf16_f32 v176, v176, v177\n\t"
            "v_cvt_pk_bf16_f32 v177, v178, v179\n\t"
            "s_nop 0\n\t"
            "v_cvt_pk_bf16_f32 v178, v180, v181\n\t"
            "v_cvt_pk_bf16_f32 v179, v182, v183\n\t"
            "v_mfma_f32_4x4x4_16b_bf16 v[196:199], v[176:177], v[176:177], v[196:199]\n\t"
            "s_waitcnt lgkmcnt(14)\n\t"
            "ds_write_b128 v236, v[176:179] offset:128\n\t"
            "v_mfma_f32_16x16x32_bf16 v[192:195], v[238:241], v[176:179], v[192:195]\n\t"
            "v_mov_b64 v[176:177], 0\n\t"
            "v_mov_b64 v[180:181], 0\n\t"
            "v_mfma_f32_4x4x4_16b_bf16 v[196:199], v[178:179], v[178:179], v[196:199]\n\t"
            "v_mov_b64 v[178:179], 0\n\t"
            "v_mov_b64 v[182:183], 0\n\t"
            "s_waitcnt lgkmcnt(10)\n\t"
            "v_smfmac_f32_16x16x64_bf16 v[184:187], v[168:171], v[144:151], v255\n\t"
            "ds_read_b128 v[168:171], v254 offset:512\n\t"
            "ds_read_b64 v[144:145], v253 offset:72\n\t"
            "ds_read_b64 v[146:147], v253 offset:168\n\t"
            "ds_read_b64 v[148:149], v253 offset:1816\n\t"
            "ds_read_b64 v[150:151], v253 offset:1912\n\t"
            "s_waitcnt lgkmcnt(10)\n\t"
            "v_smfmac_f32_16x16x64_bf16 v[188:191], v[172:175], v[152:159], v255\n\t"
            "ds_read_b128 v[172:175], v254 offset:576\n\t"
            "ds_read_b64 v[152:153], v253 offset:80\n\t"
            "ds_read_b64 v[154:155], v253 offset:176\n\t"
            "ds_read_b64 v[156:157], v253 offset:1824\n\t"
            "ds_read_b64 v[158:159], v253 offset:1920\n\t"
            "v_cvt_pk_bf16_f32 v184, v184, v185\n\t"
            "v_cvt_pk_bf16_f32 v185, v186, v187\n\t"
            "s_cbranch_scc1 .Lwk_np0%=\n\t"
            "s_mov_b32 exec_hi, 15\n\t"
            "buffer_load_dwordx4 v252, s[84:87], s89 offen sc1 lds\n\t"
            "s_mov_b32 exec_hi, -1\n\t"
            ".Lwk_np0%=:\n\t"
            "s_nop 0\n\t"
            "v_cvt_pk_bf16_f32 v186, v188, v189\n\t"
            "v_cvt_pk_bf16_f32 v187, v190, v191\n\t"
            "v_mfma_f32_4x4x4_16b_bf16 v[196:199], v[184:185], v[184:185], v[196:199]\n\t"
            "s_waitcnt lgkmcnt(14)\n\t"
            "ds_write_b128 v237, v[184:187] offset:128\n\t"
            "v_mfma_f32_16x16x32_bf16 v[192:195], v[238:241], v[184:187], v[192:195]\n\t"
            "v_mov_b64 v[184:185], 0\n\t"
            "v_mov_b64 v[188:189], 0\n\t"
            "v_mfma_f32_4x4x4_16b_bf16 v[196:199], v[186:187], v[186:187], v[196:199]\n\t"
            "v_mov_b64 v[186:187], 0\n\t"
            "v_mov_b64 v[190:191], 0\n\t"
            "s_waitcnt lgkmcnt(10)\n\t"
            "v_smfmac_f32_16x16x64_bf16 v[176:179], v[168:171], v[160:167], v255\n\t"
            "ds_read_b128 v[168:171], v254 offset:640\n\t"
            "ds_read_b64 v[160:161], v253 offset:88\n\t"
            "ds_read_b64 v[162:163], v253 offset:184\n\t"
            "ds_read_b64 v[164:165], v253 offset:1832\n\t"
            "ds_read_b64 v[166:167], v253 offset:1928\n\t"
            "s_waitcnt lgkmcnt(10)\n\t"
            "v_smfmac_f32_16x16x64_bf16 v[180:183], v[172:175], v[144:151], v255\n\t"
            "ds_read_b128 v[172:175], v254 offset:704\n\t"
            "v_cvt_pk_bf16_f32 v176, v176, v177\n\t"
            "v_cvt_pk_bf16_f32 v177, v178, v179\n\t"
            "s_nop 1\n\t"
            "v_mfma_f32_4x4x4_16b_bf16 v[196:199], v[176:177], v[176:177], v[196:199]\n\t"
            "s_nop 1\n\t"
            "v_cvt_pk_bf16_f32 v178, v180, v181\n\t"
            "v_cvt_pk_bf16_f32 v179, v182, v183\n\t"
            "s_nop 1\n\t"
            "v_mfma_f32_4x4x4_16b_bf16 v[196:199], v[178:179], v[178:179], v[196:199]\n\t"
            "ds_write_b128 v236, v[176:179] offset:256\n\t"
            "s_waitcnt lgkmcnt(6)\n\t"
            "v_smfmac_f32_16x16x64_bf16 v[184:187], v[168:171], v[152:159], v255\n\t"
            "v_mfma_f32_16x16x32_bf16 v[192:195], v[238:241], v[176:179], v[192:195]\n\t"
            "s_waitcnt lgkmcnt(1)\n\t"
            "v_smfmac_f32_16x16x64_bf16 v[188:191], v[172:175], v[160:167], v255\n\t"
            "s_nop 4\n\t"
            "v_cvt_pk_bf16_f32 v184, v184, v185\n\t"
            "v_cvt_pk_bf16_f32 v185, v186, v187\n\t"
            "s_nop 1\n\t"
            "v_mfma_f32_4x4x4_16b_bf16 v[196:199], v[184:185], v[184:185], v[196:199]\n\t"
            "v_cvt_pk_bf16_f32 v186, v188, v189\n\t"
            "v_cvt_pk_bf16_f32 v187, v190, v191\n\t"
            "s_nop 1\n\t"
            "v_mfma_f32_4x4x4_16b_bf16 v[196:199], v[186:187], v[186:187], v[196:199]\n\t"
            "ds_write_b128 v237, v[184:187] offset:256\n\t"
            "v_mfma_f32_16x16x32_bf16 v[192:195], v[238:241], v[184:187], v[192:195]\n\t"
            "s_nop 7\n\t"
            "v_cndmask_b32_e64 v193, v196, v197, s[60:61]\n\t"
            "v_cndmask_b32_e64 v193, v193, v198, s[62:63]\n\t"
            "v_cndmask_b32_e64 v193, v193, v199, s[64:65]\n\t"
            "ds_write_b64 v248, v[192:193] offset:18816\n\t"
            "s_waitcnt lgkmcnt(0)\n\t"
            "s_nop 7\n\t"
            "s_setpc_b64 vcc\n\t"
            ".Lwk_pe%=:\n\t"
            : B_SIO, "+{v247}"(l16_), "+{v228}"(q0), "+{v229}"(q1), "+{v232}"(q2), "+{v233}"(q3), "+{v234}"(q4)
            : "{v242}"(oE_), "{v243}"(oO_), "{v244}"(pR0_), "{v245}"(pR1_), "{v246}"(pR2_), "{v250}"(pXw_), "{v251}"(pSTw_), "{v252}"(aCA_), "{v253}"(aB_), "{v254}"(aA_), "{v255}"(idx_), B_SIN, "{s44}"(ARR_OFF + blockIdx.x * 8), "{s[80:81]}"(mrow), "{s82}"(wave == 6 && cnt == 21 ? REC_OFF + (start - 20 * ((blockIdx.x & 7) * 32 + xi)) * 1280 : 0)
            : "v144", "v145", "v146", "v147", "v148", "v149", "v150", "v151", "v152", "v153", "v154", "v155", "v156", "v157", "v158", "v159", "v160", "v161", "v162", "v163", "v164", "v165", "v166", "v167", "v168", "v169", "v170", "v171", "v172", "v173", "v174", "v175", "v176", "v177", "v178", "v179", "v180", "v181", "v182", "v183", "v184", "v185", "v186", "v187", "v188", "v189", "v190", "v191", "v192", "v193", "v194", "v195", "v196", "v197", "v198", "v199", "v236", "v237", "v248", "v249", "v0", "v1", "v2", "v3", "v4", "v5", "v6", "v7", "v8", "v9", "v10", "v11", "v12", "v13", "v14", "v15", "v16", "v17", "v18", "v19", "v20", "v21", "v22", "v23", "v24", "v25", "v26", "v27", "v28", "v29", "v30", "v31", "v32", "v33", "v34", "v35", "v36", "v37", "v38", "v39", "v40", "v41", "v42", "v43", "v44", "v45", "v46", "v47", "v48", "v49", "v50", "v51", "v52", "v53", "v54", "v55", "v56", "v57", "v58", "v59", "v60", "v61", "v62", "v63", "v64", "v65", "v66", "v67", "v68", "v69", "v70", "v71", "v72", "v73", "v74", "v75", "v76", "v77", "v78", "v79", "v80", "v81", "v82", "v83", "v84", "v85", "v86", "v87", "v88", "v89", "v90", "v91", "v92", "v93", "v94", "v95", "v96", "v97", "v98", "v99", "v100", "v101", "v102", "v103", "v104", "v105", "v106", "v107", "v108", "v109", "v110", "v111", "v112", "v113", "v114", "v115", "v116", "v117", "v118", "v119", "v120", "v121", "v122", "v123", "v124", "v125", "v126", "v127", "v128", "v129", "v130", "v131", "v132", "v133", "v134", "v135", "v136", "v137", "v138", "v139", "v140", "v141", "v142", "v143", "v200", "v201", "v202", "v204", "v205", "v206", "v207", "v208", "v209", "v210", "v211", "v212", "v213", "v214", "v215", "v216", "v217", "v218", "v219", "v220", "v221", "v222", "v223", "v224", "v225", "v226", "v227", "v230", "v231", "v235", B_SCLOB);
        }
        __builtin_amdgcn_endpgm();
    }
}

extern "C" void kernel_launch(void* const* d_in, const int* in_sizes, int n_in,
                              void* d_out, int out_size, void* d_ws, size_t ws_size,
                              hipStream_t stream) {
    const unsigned short* x = (const unsigned short*)d_in[0];
    const unsigned short* dw = (const unsigned short*)d_in[1];
    const unsigned short* wqkv = (const unsigned short*)d_in[2];
    const unsigned short* wo = (const unsigned short*)d_in[3];
    unsigned short* out = (unsigned short*)d_out;
    u32x4* wf = (u32x4*)d_ws;
    win_attn_kernel<<<GRID, 512, 0, stream>>>(x, dw, wqkv, wo, wf, out);
}
```

```cpp
#include <hip/hip_runtime.h>
#include <hip/hip_bf16.h>
#include <stdint.h>

typedef short bf16x8 __attribute__((ext_vector_type(8)));
typedef float f32x4 __attribute__((ext_vector_type(4)));
typedef float f32x2 __attribute__((ext_vector_type(2)));
typedef unsigned u32x2 __attribute__((ext_vector_type(2)));
typedef unsigned u32x4 __attribute__((ext_vector_type(4)));

constexpr int IMG = 511;
constexpr int CH = 192;
constexpr int NWX = 73;
constexpr int NWIN = NWX * NWX;
constexpr int GRID = 256;
constexpr int ROWBYTES = IMG * CH * 2;
constexpr int XBIAS = (IMG + 1) * CH * 2;
constexpr unsigned OOB = 0x40000000u;

constexpr int FLAG_OFF = 7856;
constexpr int X_OFF = 32768;
constexpr int X1_OFF = 53632;
constexpr int O_OFF = 74496;
constexpr int V_OFF = 92928;
constexpr int KQL_OFF = 111360;
constexpr int OL_OFF = 129024;
constexpr int AT_OFF = 137472;
constexpr int CT_OFF = AT_OFF + 19584;
constexpr int RS_OFF = CT_OFF + 4096;
constexpr int RSX_OFF = 162816;
constexpr int WL_OFF = RS_OFF + 2304 + 64;
constexpr int LDS_BYTES = WL_OFF + 128;
#define RS_BASE (RS_OFF + wave * 384)
constexpr int STL_OFF = O_OFF + 12288;
constexpr int DW_OFF = O_OFF + 8448;
constexpr int CP_OFF = X1_OFF + 16384;
#define LX_OFF(i) ((i) < 2 ? CT_OFF + 1024 + (i) * 384 : KQL_OFF + ((i) - 2) * 800)
constexpr int PF_OFF = CT_OFF + 2048;
#define LB_OFF (w < 3 ? V_OFF + w * 13952 : X1_OFF)
#define LPOS(wc) (((wc) & 1) ? 0 : 6)
constexpr unsigned CROW = 0x060a0230u;
constexpr unsigned CLANE = 0x00132C3Fu;

constexpr unsigned long long TOKMAP[6] = {0x20001c190f2a2202ull, 0x052517121f061a13ull, 0x181e08151d0b260cull, 0x1b2b2903102d0901ull, 0x04162e0e2824142cull, 0x110a072127232f0dull};
constexpr unsigned long long ROWMAP[8] = {0x08321b240f1c0006ull, 0x23310e2a09170d14ull, 0x210c2f030a273418ull, 0x1d2b11221f043739ull, 0x3234363a103e192dull, 0x291a20022c3f1605ull, 0x303a3c2628122e15ull, 0x39253d1e01070b13ull};

constexpr int WF_BYTES = 8 * 36 * 1024;
constexpr int FLS = 128;
constexpr int ARR_OFF = WF_BYTES + 4096;
constexpr int EPO_OFF = ARR_OFF + 2048;
constexpr int CTR_OFF = EPO_OFF + 128;
constexpr int REC_OFF = WF_BYTES + 8192;
constexpr int FLG_OFF = REC_OFF + 209 * 1280;
constexpr int WS_BYTES = FLG_OFF + 8 * 36 * FLS;
constexpr int SC1 = 16;

#define MFMA32(a, b, c) __builtin_amdgcn_mfma_f32_16x16x32_bf16((a), (b), (c), 0, 0, 0)
#define BC8(v) __builtin_bit_cast(bf16x8, (v))
#define BAR() asm volatile("s_waitcnt lgkmcnt(0)\n\ts_barrier" ::: "memory")
#define SB() __builtin_amdgcn_sched_barrier(0)
#define OPQ(x) asm volatile("" : "+v"(x))
#define ZERO4 ((f32x4){0.f, 0.f, 0.f, 0.f})

typedef __bf16 nbf16x2 __attribute__((ext_vector_type(2)));
__device__ __forceinline__ unsigned pk2(float a, float b) {
    f32x2 v = {a, b};
    nbf16x2 r = __builtin_convertvector(v, nbf16x2);
    return __builtin_bit_cast(unsigned, r);
}
__device__ __forceinline__ float bflo(unsigned u) { return __uint_as_float(u << 16); }
__device__ __forceinline__ float bfhi(unsigned u) { return __uint_as_float(u & 0xffff0000u); }

__device__ __forceinline__ float bf2f(unsigned short v) { return __uint_as_float((unsigned)v << 16); }
__device__ __forceinline__ unsigned byte_of(unsigned long long a, unsigned long long b, int i) {
    return (unsigned)(((i & 8) ? b : a) >> (8 * (i & 7))) & 0xFFu;
}
#define M24(a, b) ((unsigned)__mul24((int)(a), (int)(b)))
__device__ __forceinline__ void seq_to_win(int seq, int& wy, int& wx) {
    const bool last = seq >= 5110;
    const int s = last ? 35 : M24(seq, 28729) >> 22, r = seq - M24(146, s);
    const int kx = last ? M24(r, 171) >> 10 : r >> 2, pos = r - M24(last ? 6 : 4, kx);
    const bool low = last && pos >= (kx == 36 ? 2 : 4);
    wy = low ? 72 : 2 * s + (pos & 1);
    wx = 2 * kx + (low ? (kx == 36 ? 0 : pos - 4) : pos >> 1);
}
__device__ __forceinline__ int win_to_seq(int wy, int wx) {
    const int kx = wx >> 1;
    return M24(146, wy == 72 ? 35 : wy >> 1) + M24(wy >= 70 ? 6 : 4, kx) + (wy == 72 ? (kx == 36 ? 2 : 4 + (wx & 1)) : 2 * (wx & 1) + (wy & 1));
}
__device__ __forceinline__ int block_of(int wy, int wx) { return wy >= 70 ? 1295 + (wx & ~1) + (wy == 72) : M24(37, wy >> 1) + (wx >> 1); }
__device__ __forceinline__ void block_pos(int b, int& ky, int& kx) {
    const int s = M24(b, 1772) >> 16, l = b - 1295;
    kx = l >= 0 ? l >> 1 : b - M24(37, s);
    ky = l >= 0 ? 70 + 2 * (l & 1) : 2 * s;
}
__device__ __forceinline__ unsigned win_org(int wy, int wx) { return M24(wy, 7 * ROWBYTES) + M24(wx, 7 * CH * 2); }
__device__ __forceinline__ unsigned wl_entry(int wy, int wx) {
    return win_org(wy, wx) | (wy == 0 ? 0x08000000u : 0u) | (wx == 0 ? 0x10000000u : 0u) |
           (wx == NWX - 1 ? 0x20000000u : 0u) | ((unsigned)(wx & 1) << 30) | ((unsigned)(wy & 1) << 31);
}
__device__ __forceinline__ unsigned dma_off(int lane) {
    const int col = (lane * 43) >> 8;
    return (unsigned)(col * (CH * 2 - 96) + 16 * lane) | (col == 0 ? 0x10000000u : 0u) | (col == 8 ? 0x20000000u : 0u);
}

#if defined(__HIP_DEVICE_COMPILE__)
#define NOPK __attribute__((target("no-packed-fp32-ops")))
#else
#define NOPK
#endif
__global__ __launch_bounds__(512, 2) NOPK void win_attn_kernel(const unsigned short* __restrict__ x,
                                                          const unsigned short* __restrict__ dw, const unsigned short* __restrict__ wqkv,
                                                          const unsigned short* __restrict__ wo, u32x4* wf,
                                                          unsigned short* __restrict__ out) {
    __shared__ __attribute__((aligned(16))) char smem[LDS_BYTES];

    asm volatile("wk_code_begin:");
    const int tid = threadIdx.x;
    const int wave = __builtin_amdgcn_readfirstlane(tid >> 6);
    const int w = wave & 3;
    const unsigned ldsb = (unsigned)(size_t)smem;
    {
        unsigned long long pc;
        unsigned len, rel;
        asm volatile("s_getpc_b64 %0\n.Lwk_here%=:\n\ts_mov_b32 %1, wk_code_end-wk_code_begin\n\ts_mov_b32 %2, .Lwk_here%=-wk_code_begin" : "=s"(pc), "=s"(len), "=s"(rel));
        pc -= rel;
        const u32x4 cr = {(unsigned)pc, (unsigned)(pc >> 32) & 0xffffu, len, 0x00020000u};
        asm volatile("s_mov_b32 m0, %0\n\ts_nop 0\n\tbuffer_load_dwordx4 %1, %2, %3 offen lds"
                     :: "s"(ldsb + X1_OFF + 14336 + (w & 1) * 1024), "v"((tid & 63) * 16), "s"(cr), "s"((wave * 32 + (blockIdx.x >> 3)) * 1024) : "m0", "memory");
    }
    *(unsigned*)(smem + PF_OFF + tid * 4) = 0x9E3779B9u * (tid + 1);
    *(unsigned*)(smem + (tid < 192 ? AT_OFF + 18816 + tid * 4 : FLAG_OFF + (tid & 3) * 4)) = 0u;
    const auto wrsrc = __builtin_amdgcn_make_buffer_rsrc((void*)wf, 0, WS_BYTES, 0x00020000);
    unsigned tag_lo = 0, tag_hi = 0;
#define TAG_READY                                                                                  \
    {                                                                                              \
        unsigned long long tag_;                                                                   \
        asm volatile("s_load_dwordx2 %0, %1, %2\n\ts_waitcnt lgkmcnt(0)" : "=s"(tag_) : "s"(wf), "n"(EPO_OFF) : "memory"); \
        tag_lo = (unsigned)tag_ + 1u; tag_hi = (unsigned)(tag_ >> 32);                             \
    }
    const int pb = blockIdx.x;
    if (pb < 36) {
        const int lane = tid & 63, n = lane & 15, g = lane >> 4;
        const bool isO = wave >= 6;
        const int fr = isO ? 216 + 2 * pb + (wave - 6) : 6 * pb + wave;
        const int tile = fr / 6, ks = fr - 6 * tile, tw = tile / 6, mdt = tile - 6 * tw;
        const int col = isO ? 96 * (tw - 6) + 32 * (mdt >> 1) + 8 * (n >> 2) + 4 * (mdt & 1) + (n & 3)
                            : (tw < 4 ? (mdt < 3 ? 192 : -48) + 48 * tw : 96 * tw) + 16 * mdt + n;
        const int rb = isO ? 384 : 1152;
        const auto rr = __builtin_amdgcn_make_buffer_rsrc((void*)(isO ? wo : wqkv), 0, 192 * rb, 0x00020000);
        const int vo = (48 * (g >> 1) + 8 * (g & 1)) * rb + 2 * col;
        float v[8];
#pragma unroll
        for (int j = 0; j < 8; ++j) v[j] = bf2f(__builtin_amdgcn_raw_buffer_load_b16(rr, vo, (96 * (ks & 1) + 16 * (ks >> 1) + j) * rb, 0));
        TAG_READY
        float* red = (float*)smem;
        red[n * 36 + wave * 4 + g] = ((v[0] + v[1]) + (v[2] + v[3])) + ((v[4] + v[5]) + (v[6] + v[7]));
        BAR();
        float mean = 0.f;
#pragma unroll
        for (int i = 0; i < 6; ++i) {
            const f32x4 p = *(const f32x4*)(red + n * 36 + 4 * i);
            mean += (p[0] + p[1]) + (p[2] + p[3]);
        }
        mean = isO ? 0.f : mean * (1.f / 192.f);
        u32x4 o;
#pragma unroll
        for (int j = 0; j < 4; ++j) o[j] = pk2(v[2 * j] - mean, v[2 * j + 1] - mean);
        __builtin_amdgcn_raw_buffer_store_b128(o, wrsrc, lane * 16, fr * 1024, SC1);
        if (pb == 0 && tid == 448) __builtin_amdgcn_raw_buffer_store_b32(0u, wrsrc, CTR_OFF, 0, SC1);
        asm volatile("s_waitcnt vmcnt(0)" ::: "memory");
        BAR();
        if (tid < 8) __builtin_amdgcn_raw_buffer_store_b64((u32x2){tag_lo, tag_hi}, wrsrc, FLG_OFF + (tid * 36 + pb) * FLS, 0, SC1);
    }
    const int xi = blockIdx.x >> 3, nshort = (int)(0x0506060606060606ull >> (8 * (blockIdx.x & 7))) & 63;
    const int start = 672 * (blockIdx.x & 7) - ((int)(0x2a241e18120c0600ull >> (8 * (blockIdx.x & 7))) & 63) + 21 * xi - (xi < nshort ? xi : nshort);
    const int cnt = xi < nshort ? 20 : 21;

    const auto xrsrc = __builtin_amdgcn_make_buffer_rsrc((void*)((const char*)x - XBIAS), 0, IMG * IMG * CH * 2 + XBIAS, 0x00020000);
#define LANE_SETUP                                                                                 \
    int lane;                                                                                      \
    asm volatile("v_mbcnt_lo_u32_b32 %0, -1, 0\n\tv_mbcnt_hi_u32_b32 %0, -1, %0" : "=v"(lane));    \
    __builtin_assume(lane >= 0 && lane < 64);                                                      \
    const int g = lane >> 4, l15 = lane & 15, sw = l15 >> 1;                                       \
    int fE = X_OFF + l15 * 384 + ((g ^ sw) << 4);                                                  \
    OPQ(fE);

#define DMA_SLICE(en)                                                                              \
        {                                                                                          \
            const unsigned vo_ = dma_off(lane) & (0x401fffffu | ((en) & 0x38000000u));             \
            const unsigned so_ = ((en) & 0x7ffffffu) + 96 * w;                                     \
            _Pragma("unroll") for (int r = 0; r < 9; ++r) {                                        \
                unsigned m0_ = ldsb + w * 8192 + r * 872;                                          \
                unsigned sr_ = (r == 0 && ((en) & 0x08000000u)) ? OOB : so_ + r * ROWBYTES;        \
                asm volatile("s_mov_b32 m0, %0\n\ts_mov_b32 exec_hi, 0x3fffff\n\tbuffer_load_dwordx4 %1, %2, %3 offen lds\n\ts_mov_b32 exec_hi, -1" \
                             :: "s"(m0_), "v"(vo_), "s"(xrsrc), "s"(sr_) : "m0", "memory");        \
            }                                                                                      \
        }
#define XPAT(r) (((r) < 48 ? (r) : 48) * 384 + (((2 * w) ^ (((r) >> 1) & 7)) << 4))
#define SPAT(r) ((w >> 1) * 1024 + (w & 1) * 8 + (r) * 16)
#define CONV_CONSTS(var) CONV_ROWS(var) CONV_OPS
#define CONV_ROWS(var) \
            const unsigned long long rlo = g == 0 ? ROWMAP[0] : (g == 1 ? ROWMAP[2] : (g == 2 ? ROWMAP[4] : ROWMAP[6])); \
            const unsigned long long rhi = g == 0 ? ROWMAP[1] : (g == 1 ? ROWMAP[3] : (g == 2 ? ROWMAP[5] : ROWMAP[7])); \
            int xrow = byte_of(rlo, rhi, l15); \
            if (lane == (int)((CLANE >> (8 * (var))) & 0xFF)) xrow = 48; \
            if (lane == 63) xrow = (CROW >> (8 * (var))) & 0xFF; \
            const unsigned pXw_ = XPAT(xrow); \
            const unsigned pSTw_ = (w >> 1) * 1024 + (w & 1) * 8 + xrow * 16; \
            const int oy = (0x5320 >> (4 * (l15 >> 2))) & 15, ox = (0x5320 >> (4 * (l15 & 3))) & 15; \
            const unsigned aB_ = ldsb + w * 8192 + (oy + (g >> 1)) * 872 + (ox + 2 * (g & 1)) * 96;
#define CONV_OPS \
            const int dyi = g - (l15 >> 3); \
            const unsigned aA_ = ldsb + AT_OFF + (l15 & 3) * 16 + \
                                 ((dyi < 0 || dyi > 2) ? 18816 : (dyi == 0 ? 0 : (dyi == 1 ? 6208 : 12544)) + ((l15 >> 2) & 1) * 3072 + w * 768); \
            const unsigned idx_ = (l15 & 3) * 0x5555u; \
            const unsigned se = (l15 == 4 * g) ? 0x3F803F80u : 0u; \
            const u32x4 sel4 = {se, se, se, se};

    unsigned en0, en1;
    {
        LANE_SETUP
        const int px = (lane * 43) >> 8;

        int wy, wx;
        seq_to_win(start + (lane < cnt ? lane : cnt - 1), wy, wx);
        const unsigned en = wl_entry(wy, wx);
        en0 = __builtin_amdgcn_readlane(en, 0);
        en1 = __builtin_amdgcn_readlane(en, 1);
        if (wave >= 4) {
            const int bk = block_of(wy, wx);
            const int key0 = __builtin_amdgcn_readlane(bk, 0), nblk = __builtin_amdgcn_readlane(bk, 63) - key0 + 1;
            const int b = l15 < nblk ? l15 : 0;
            int ky, kx;
            block_pos(key0 + b, ky, kx);
            const unsigned org = l15 < nblk ? (win_org(ky, 2 * kx) + (5 * ROWBYTES + 5 * CH * 2 + XBIAS)) | (kx == 36 ? OOB : 0u) : OOB + 0x8000000u;
            const unsigned lo = (px & 3) * (CH * 2) + (lane - 6 * px) * 16, keep = (px & 2) ? ~0u : ~OOB;
            BAR();
#pragma unroll
            for (int p = 0; p < 4; ++p) {
                if (p < 3 || nblk > 6) {
                    const unsigned oa = __builtin_amdgcn_readlane(org, 2 * p), ob = __builtin_amdgcn_readlane(org, 2 * p + 1);
                    const unsigned vo_ = ((lane < 24 ? oa : ob) & keep) + lo;
#pragma unroll
                    for (int r = 0; r < 4; ++r) {
                        unsigned m0_ = ldsb + LB_OFF + (4 * p + r) * 872;
                        asm volatile("s_mov_b32 m0, %0\n\ts_mov_b32 exec_hi, 0xffff\n\tbuffer_load_dwordx4 %1, %2, %3 offen lds\n\ts_mov_b32 exec_hi, -1"
                                     :: "s"(m0_), "v"(vo_), "s"(xrsrc), "s"(96 * w + r * ROWBYTES) : "m0", "memory");
                    }
                }
            }
            SB();
            const int ly = ky + (g >> 1), lx = 2 * kx + (g & 1), li = win_to_seq(ly, lx) - start;
            const bool ok = ly < NWX && lx < NWX && li >= 0 && li < cnt && l15 < nblk;
            const int row = ok ? li : 47;
            unsigned* cp = (unsigned*)(smem + CP_OFF + (w * 64 + lane) * 16);
            *(u32x2*)cp = (u32x2){(unsigned)XPAT(row), SPAT(row) + (unsigned)(STL_OFF - O_OFF - 18816)};
            cp[2] = ldsb + LB_OFF + (4 * (b >> 1) + (g >> 1)) * 872 + (b & 1) * 384 + (g & 1) * 192;
        } else {
            {
                const auto dr = __builtin_amdgcn_make_buffer_rsrc((void*)dw, 0, 9 * 384, 0x00020000);
                unsigned m0_ = ldsb + DW_OFF + w * 864, vo_ = px * 288 + lane * 16;
                asm volatile("s_mov_b32 m0, %0\n\ts_mov_b32 exec_hi, 0x3fffff\n\tbuffer_load_dwordx4 %1, %2, %3 offen lds\n\ts_mov_b32 exec_hi, -1"
                             :: "s"(m0_), "v"(vo_), "s"(dr), "s"(96 * w) : "m0", "memory");
            }
            DMA_SLICE(en0)
            BAR();
            if (wave == 2) {
                u32x4 so;
#pragma unroll
                for (int e = 0; e < 3; ++e) {
                    const int tk = byte_of(TOKMAP[2 * e], TOKMAP[2 * e + 1], l15);
                    const int ty = (tk * 37) >> 8, tx = tk - 7 * ty;
                    so[e] = (unsigned)(ty * ROWBYTES + tx * (CH * 2)) + 16 * g;
                }
                so[3] = dma_off(lane);
                *(u32x4*)(smem + CT_OFF + lane * 16) = so;
            }
            if (wave == 1 && lane < 32) *(unsigned*)(smem + WL_OFF + lane * 4) = lane < 20 ? en : 0x7ffffffu;
            if (wave == 1 && lane == 20) *(unsigned*)(smem + PF_OFF + 648) = en;
            asm volatile("s_waitcnt vmcnt(9)" ::: "memory");
            if (lane < 48) {
                unsigned short dv[9];
#pragma unroll
                for (int i = 0; i < 9; ++i) dv[i] = *(const unsigned short*)(smem + DW_OFF + w * 864 + i * 96 + lane * 2);
#pragma unroll
                for (int dy = 0; dy < 3; ++dy) {
                    const unsigned a = dv[3 * dy], b2 = dv[3 * dy + 1] | (dy == 1 ? 0x3F800000u : 0u), c = dv[3 * dy + 2];
                    char* p = smem + AT_OFF + (dy == 0 ? 0 : (dy == 1 ? 6208 : 12544)) + w * 768 + lane * 16;
                    *(u32x4*)p = (u32x4){a, b2, c, 0u};
                    *(u32x4*)(p + 3072) = (u32x4){0u, a, b2, c};
                }
            }
            asm volatile("s_mov_b64 exec, 1\n\tds_add_u32 %0, %1\n\ts_mov_b64 exec, -1" :: "v"(ldsb + FLAG_OFF + 8), "v"(1) : "memory");
        }
    }
    const unsigned long long mrow = 0x0001000100010001ull * ((1u << (cnt - 16)) - 1u);
#define KARGS                                                                                      \
    unsigned long long xb, wb, ob;                                                                 \
    asm volatile("s_load_dwordx2 %0, %3, 0x0\n\ts_load_dwordx2 %1, %3, 0x20\n\ts_load_dwordx2 %2, %3, 0x28\n\ts_waitcnt lgkmcnt(0)" \
                 : "=&s"(xb), "=&s"(wb), "=&s"(ob) : "s"(__builtin_amdgcn_kernarg_segment_ptr()) : "memory"); \
    xb -= XBIAS;                                                                                   \
    const u32x4 wr = {(unsigned)wb, (unsigned)(wb >> 32) & 0xffffu, (unsigned)WS_BYTES, 0x00020000u}; \
    const u32x4 orr = {(unsigned)ob, (unsigned)(ob >> 32) & 0xffffu, (unsigned)(IMG * IMG * CH * 2), 0x00020000u}; \
    const u32x4 xr = {(unsigned)xb, (unsigned)(xb >> 32) & 0xffffu, (unsigned)(IMG * IMG * CH * 2 + XBIAS), 0x00020000u};
    const int tk_ = (xi + 4) / 6;
    const unsigned touch = wave * 36864 + ((xi + 4 - 6 * tk_) * 6 + tk_) * 1024;
#define B_SCALARS \
            CONV_CONSTS(0) \
            const unsigned aCA_ = ldsb + CT_OFF + lane * 16; \
            KARGS \
            unsigned s45 = OOB, s46 = (en0 & 0x7ffffffu) + 96 * w, s47 = (en1 & 0x7ffffffu) + 96 * w; \
            unsigned s48 = ldsb + X_OFF, s67 = ldsb + X1_OFF, s76 = ldsb + WL_OFF + 8, s70 = 0; \
            unsigned s56 = __builtin_amdgcn_readfirstlane((en0 >> 30) * 4 + (en1 >> 30)); \
            unsigned s69 = 0x401fffffu | (en1 & 0x38000000u), s79 = (en1 & 0x08000000u) ? OOB : s47, s59 = ldsb + LX_OFF(1) + 32 * w; \
            unsigned cp_ = ldsb + CP_OFF + (w * 64 + lane) * 16; \
            unsigned long long xp = 0, sp = 0; \
            _Pragma("unroll") for (int v = 0; v < 4; ++v) { \
                const unsigned r = (CROW >> (8 * v)) & 0xFF; \
                xp |= (unsigned long long)XPAT(r) << (16 * v); \
                sp |= (unsigned long long)SPAT(r) << (16 * v); \
            }
#define B_SIO "+{s45}"(s45), "+{s46}"(s46), "+{s47}"(s47), "+{s48}"(s48), "+{s67}"(s67), "+{s76}"(s76), "+{s56}"(s56), "+{s70}"(s70), "+{s69}"(s69), "+{s79}"(s79), "+{s59}"(s59), "+{v203}"(cp_)
#define B_SIN "{v[238:241]}"(sel4), "{s[40:43]}"(orr), "{s[72:75]}"(xr), "{s39}"(0x401fffffu), "{s[28:31]}"(wr), "{s33}"(wave * 36864), "{s78}"(96 * w), \
              "{s27}"(96 * w - 384), "{s[50:51]}"(xp), "{s[52:53]}"(sp), "{s54}"(CLANE), "{s55}"(CROW & 0x0F0F0F0Fu), "{s[34:35]}"(0x0001000100010001ull), \
              "{s[60:61]}"(0x2222222222222222ull), "{s[62:63]}"(0x4444444444444444ull), "{s[64:65]}"(0x8888888888888888ull), \
              "{s66}"(ldsb + w * 8192), "{s71}"((ldsb + X_OFF) ^ (ldsb + X1_OFF))
#define B_SCLOB "s83", "s84", "s26", "s77", "s68", "s36", "s37", "s38", "s57", "s58", "s49", "vcc", "scc", "m0", "memory"

    if (wave < 4) {
        LANE_SETUP
        const int dW = X_OFF + l15 * 384 + (((2 * w + (g >> 1)) ^ sw) << 4) + 8 * (g & 1);
        const int vB = V_OFF + w * 4608 + 1024 + lane * 8, rsR = RS_BASE + l15 * 4;
        {
            const unsigned rsb = ldsb + RS_BASE;
            const unsigned pE_ = fE - X_OFF, aO_ = ldsb + dW + (O_OFF - X_OFF), sel_ = (l15 & 3) ? 0u : 1u, aVB_ = ldsb + vB;
            unsigned pST_ = lane * 16, aPLw_ = rsb + lane * 2, aRSr_ = ldsb + rsR, aRSw_ = rsb + lane * 4;
            unsigned g16_ = (l15 & 3) ? ldsb + AT_OFF + 18816 : g * 16, g8_ = (l15 & 3) ? ldsb + AT_OFF + 18816 : g * 8;
            unsigned loVL_ = ((2 * w + (l15 >> 3)) << 4) + (l15 & 7) * 2, loOL_ = g * 128;
            unsigned on = (l15 & 3) ? 0u : 0x3F803F80u;
            CONV_CONSTS(en0 >> 30)
            unsigned c0 = lane == (int)((CLANE >> (8 * (en0 >> 30))) & 0xFF) ? LX_OFF(0) - X_OFF + 32 * w : pXw_, c1 = pSTw_, c2 = lane * FLS, c3 = aB_, c4 = aA_, c5 = idx_;
            unsigned c6 = se, c7 = se, c8 = se, c9 = se, s89 = FLG_OFF + (blockIdx.x & 7) * 36 * FLS, s46 = wave * 36864;
            unsigned s48 = ldsb + X_OFF, s70 = 0;
            unsigned s72 = ldsb + KQL_OFF + w * 192, s73 = ldsb + OL_OFF, s74 = ldsb + KQL_OFF + 784, s77 = ldsb + OL_OFF, s76 = ldsb + KQL_OFF + 768 + w * 4;
            KARGS
            asm volatile(
            "s_load_dwordx2 s[82:83], s[84:85], 301056\n\t"
            "s_mov_b32 m0, 159104\n\t"
            "s_mov_b32 s67, 32768\n\t"
            "s_mov_b32 s60, 0x22222222\n\t"
            "s_mov_b32 s61, 0x22222222\n\t"
            "s_mov_b32 s62, 0x44444444\n\t"
            "s_mov_b32 s63, 0x44444444\n\t"
            "s_mov_b32 s64, 0x88888888\n\t"
            "s_mov_b32 s65, 0x88888888\n\t"
            "s_waitcnt vmcnt(0) lgkmcnt(0)\n\t"
            "s_add_u32 s82, s82, 1\n\t"
            "s_cmp_lg_u32 s80, 16\n\t"
            "s_cbranch_scc1 .Lwa_p0%=\n\t"
            "s_mov_b32 exec_hi, 15\n\t"
            "buffer_load_dwordx4 v252, s[84:87], s89 offen sc1 lds\n\t"
            "s_mov_b32 exec_hi, -1\n\t"
            ".Lwa_p0%=:\n\t"
            "s_call_b64 vcc, wk_conv\n\t"
            "v_min_u32 v146, 0x230, v204\n\t"
            "v_add_u32 v146, 159104, v146\n\t"
            "s_mov_b32 s81, 0x10000\n\t"
            "v_mov_b32 v240, v200\n\t"
            "v_mov_b32 v241, v200\n\t"
            "v_mov_b32 v242, v200\n\t"
            "v_mov_b32 v243, v200\n\t"
            "v_mov_b32 v248, v204\n\t"
            "v_mov_b32 v249, v205\n\t"
            "v_mov_b32 v250, v206\n\t"
            "v_mov_b32 v251, v207\n\t"
            "v_mov_b32 v253, v209\n\t"
            "v_mov_b32 v254, v210\n\t"
            "v_mov_b32 v255, v211\n\t"
            ".Lwa_p1%=:\n\t"
            "ds_read_b64 v[144:145], v146\n\t"
            "s_waitcnt lgkmcnt(0)\n\t"
            "v_cmp_ne_u32 vcc, s82, v144\n\t"
            "v_cmp_ne_u32 s[60:61], s83, v145\n\t"
            "s_or_b64 vcc, vcc, s[60:61]\n\t"
            "s_cbranch_scc0 .Lwa_p3%=\n\t"
            "s_sub_u32 s81, s81, 1\n\t"
            "s_cbranch_scc1 .Lwa_p3%=\n\t"
            "s_cmp_lg_u32 s80, 16\n\t"
            "s_cbranch_scc1 .Lwa_p2%=\n\t"
            "s_mov_b32 exec_hi, 15\n\t"
            "buffer_load_dwordx4 v252, s[84:87], s89 offen sc1 lds\n\t"
            "s_mov_b32 exec_hi, -1\n\t"
            "s_waitcnt vmcnt(24)\n\t"
            ".Lwa_p2%=:\n\t"
            "s_sleep 3\n\t"
            "s_branch .Lwa_p1%=\n\t"
            ".Lwa_p3%=:\n\t"
            "v_mov_b32 v252, v208\n\t"
            "v_mov_b32 v144, s82\n\t"
            "v_mov_b32 v145, s83\n\t"
            "v_mov_b32 v146, 159744\n\t"
            "ds_write_b64 v146, v[144:145]\n\t"
            "s_mov_b32 s60, 0xffff0000\n\t"
            "s_mov_b32 s61, 0x0\n\t"
            "s_mov_b32 s62, 0x0\n\t"
            "s_mov_b32 s63, 0xffff\n\t"
            "s_mov_b32 s64, 0x0\n\t"
            "s_mov_b32 s65, 0xffff0000\n\t"
            "s_mov_b32 s66, 0xffffffff\n\t"
            "s_mov_b32 s67, 0x1ffff\n\t"
            "buffer_load_dwordx4 v[140:143], v248, s[84:87], s88 offen sc1\n\t"
            "s_add_u32 s49, s46, 0\n\t"
            "buffer_load_dwordx4 v[0:3], v248, s[84:87], s49 offen sc1\n\t"
            "s_add_u32 s49, s46, 6144\n\t"
            "buffer_load_dwordx4 v[24:27], v248, s[84:87], s49 offen sc1\n\t"
            "s_add_u32 s49, s46, 12288\n\t"
            "buffer_load_dwordx4 v[48:51], v248, s[84:87], s49 offen sc1\n\t"
            "s_add_u32 s49, s46, 18432\n\t"
            "buffer_load_dwordx4 v[72:75], v248, s[84:87], s49 offen sc1\n\t"
            "s_add_u32 s49, s46, 24576\n\t"
            "buffer_load_dwordx4 v[96:99], v248, s[84:87], s49 offen sc1\n\t"
            "s_add_u32 s49, s46, 30720\n\t"
            "buffer_load_dwordx4 v[120:123], v248, s[84:87], s49 offen sc1\n\t"
            "s_add_u32 s49, s46, 1024\n\t"
            "buffer_load_dwordx4 v[4:7], v248, s[84:87], s49 offen sc1\n\t"
            "s_add_u32 s49, s46, 7168\n\t"
            "buffer_load_dwordx4 v[28:31], v248, s[84:87], s49 offen sc1\n\t"
            "s_add_u32 s49, s46, 13312\n\t"
            "buffer_load_dwordx4 v[52:55], v248, s[84:87], s49 offen sc1\n\t"
            "s_add_u32 s49, s46, 19456\n\t"
            "buffer_load_dwordx4 v[76:79], v248, s[84:87], s49 offen sc1\n\t"
            "s_add_u32 s49, s46, 25600\n\t"
            "buffer_load_dwordx4 v[100:103], v248, s[84:87], s49 offen sc1\n\t"
            "s_add_u32 s49, s46, 31744\n\t"
            "buffer_load_dwordx4 v[124:127], v248, s[84:87], s49 offen sc1\n\t"
            "s_add_u32 s49, s46, 2048\n\t"
            "buffer_load_dwordx4 v[8:11], v248, s[84:87], s49 offen sc1\n\t"
            "s_add_u32 s49, s46, 8192\n\t"
            "buffer_load_dwordx4 v[32:35], v248, s[84:87], s49 offen sc1\n\t"
            "s_add_u32 s49, s46, 14336\n\t"
            "buffer_load_dwordx4 v[56:59], v248, s[84:87], s49 offen sc1\n\t"
            "s_add_u32 s49, s46, 20480\n\t"
            "buffer_load_dwordx4 v[80:83], v248, s[84:87], s49 offen sc1\n\t"
            "s_add_u32 s49, s46, 26624\n\t"
            "buffer_load_dwordx4 v[104:107], v248, s[84:87], s49 offen sc1\n\t"
            "s_add_u32 s49, s46, 32768\n\t"
            "buffer_load_dwordx4 v[128:131], v248, s[84:87], s49 offen sc1\n\t"
            "s_add_u32 s49, s46, 3072\n\t"
            "buffer_load_dwordx4 v[12:15], v248, s[84:87], s49 offen sc1\n\t"
            "s_add_u32 s49, s46, 9216\n\t"
            "buffer_load_dwordx4 v[36:39], v248, s[84:87], s49 offen sc1\n\t"
            "s_add_u32 s49, s46, 15360\n\t"
            "buffer_load_dwordx4 v[60:63], v248, s[84:87], s49 offen sc1\n\t"
            "s_add_u32 s49, s46, 21504\n\t"
            "buffer_load_dwordx4 v[84:87], v248, s[84:87], s49 offen sc1\n\t"
            "s_add_u32 s49, s46, 27648\n\t"
            "buffer_load_dwordx4 v[108:111], v248, s[84:87], s49 offen sc1\n\t"
            "s_add_u32 s49, s46, 33792\n\t"
            "buffer_load_dwordx4 v[132:135], v248, s[84:87], s49 offen sc1\n\t"
            "s_add_u32 s49, s46, 4096\n\t"
            "buffer_load_dwordx4 v[16:19], v248, s[84:87], s49 offen sc1\n\t"
            "s_add_u32 s49, s46, 10240\n\t"
            "buffer_load_dwordx4 v[40:43], v248, s[84:87], s49 offen sc1\n\t"
            "s_add_u32 s49, s46, 16384\n\t"
            "buffer_load_dwordx4 v[64:67], v248, s[84:87], s49 offen sc1\n\t"
            "s_add_u32 s49, s46, 22528\n\t"
            "buffer_load_dwordx4 v[88:91], v248, s[84:87], s49 offen sc1\n\t"
            "s_add_u32 s49, s46, 28672\n\t"
            "buffer_load_dwordx4 v[112:115], v248, s[84:87], s49 offen sc1\n\t"
            "s_add_u32 s49, s46, 34816\n\t"
            "buffer_load_dwordx4 v[136:139], v248, s[84:87], s49 offen sc1\n\t"
            "s_add_u32 s49, s46, 5120\n\t"
            "buffer_load_dwordx4 v[20:23], v248, s[84:87], s49 offen sc1\n\t"
            "s_add_u32 s49, s46, 11264\n\t"
            "buffer_load_dwordx4 v[44:47], v248, s[84:87], s49 offen sc1\n\t"
            "s_add_u32 s49, s46, 17408\n\t"
            "buffer_load_dwordx4 v[68:71], v248, s[84:87], s49 offen sc1\n\t"
            "s_add_u32 s49, s46, 23552\n\t"
            "buffer_load_dwordx4 v[92:95], v248, s[84:87], s49 offen sc1\n\t"
            "s_add_u32 s49, s46, 29696\n\t"
            "buffer_load_dwordx4 v[116:119], v248, s[84:87], s49 offen sc1\n\t"
            "s_add_u32 s49, s46, 35840\n\t"
            "buffer_load_dwordx4 v[140:143], v248, s[84:87], s49 offen sc1\n\t"
            "s_waitcnt lgkmcnt(0)\n\t"
            "s_barrier\n\t"
            "s_setprio 2\n\t"
            "v_add_u32 v235, s71, v248\n\t"
            "v_add_u32 v232, s71, v244\n\t"
            "v_xor_b32 v233, 64, v232\n\t"
            "v_subrev_u32 v239, s75, v251\n\t"
            "v_mul_u32_u24 v239, 200, v239\n\t"
            "v_lshrrev_b32 v234, 3, v255\n\t"
            "v_add3_u32 v234, v234, v239, s72\n\t"
            "ds_read_b128 v[228:231], v235 offset:12288\n\t"
            "ds_read_b128 v[144:147], v232\n\t"
            "ds_read_b128 v[148:151], v233\n\t"
            "ds_read_b128 v[204:207], v232 offset:6144\n\t"
            "ds_read_b128 v[208:211], v233 offset:6144\n\t"
            "ds_read_b128 v[152:155], v232 offset:128\n\t"
            "ds_read_b128 v[156:159], v233 offset:128\n\t"
            "ds_read_b128 v[212:215], v232 offset:6272\n\t"
            "ds_read_b128 v[224:227], v233 offset:6272\n\t"
            "ds_read_b128 v[160:163], v232 offset:256\n\t"
            "ds_read_b128 v[164:167], v233 offset:256\n\t"
            "s_waitcnt vmcnt(24)\n\t"
            "s_waitcnt lgkmcnt(10)\n\t"
            "v_add_f32 v236, v228, v230\n\t"
            "v_add_f32 v237, v229, v231\n\t"
            "s_waitcnt lgkmcnt(8)\n\t"
            "v_mfma_f32_16x16x32_bf16 v[168:171], v[0:3], v[144:147], 0\n\t"
            "ds_read_b128 v[228:231], v235 offset:13312\n\t"
            "v_lshrrev_b32 v235, 4, v255\n\t"
            "v_mfma_f32_16x16x32_bf16 v[168:171], v[4:7], v[148:151], v[168:171]\n\t"
            "v_add3_u32 v235, v235, v239, s72\n\t"
            "v_mfma_f32_16x16x32_bf16 v[176:179], v[48:51], v[144:147], 0\n\t"
            "v_mfma_f32_16x16x32_bf16 v[176:179], v[52:55], v[148:151], v[176:179]\n\t"
            "v_mfma_f32_16x16x32_bf16 v[172:175], v[24:27], v[144:147], 0\n\t"
            "v_mfma_f32_16x16x32_bf16 v[172:175], v[28:31], v[148:151], v[172:175]\n\t"
            "v_mfma_f32_16x16x32_bf16 v[180:183], v[72:75], v[144:147], 0\n\t"
            "v_mfma_f32_16x16x32_bf16 v[180:183], v[76:79], v[148:151], v[180:183]\n\t"
            "s_waitcnt lgkmcnt(0)\n\t"
            "v_add_f32 v228, v228, v230\n\t"
            "v_add_f32 v236, v236, v228\n\t"
            "v_mfma_f32_16x16x32_bf16 v[184:187], v[96:99], v[144:147], 0\n\t"
            "v_add_f32 v229, v229, v231\n\t"
            "v_mul_f32 v236, 0x3baaaaab, v236\n\t"
            "v_mfma_f32_16x16x32_bf16 v[184:187], v[100:103], v[148:151], v[184:187]\n\t"
            "v_add_f32 v237, v237, v229\n\t"
            "v_mul_f32_e64 v236, v236, -v236\n\t"
            "v_mfma_f32_16x16x32_bf16 v[188:191], v[120:123], v[144:147], 0\n\t"
            "v_fmac_f32 v236, 0x3baaaaab, v237\n\t"
            "v_max_f32 v236, 0, v236\n\t"
            "v_mfma_f32_16x16x32_bf16 v[188:191], v[124:127], v[148:151], v[188:191]\n\t"
            "v_add_f32 v236, 0x3727c5ac, v236\n\t"
            "v_rsq_f32 v236, v236\n\t"
            "v_mfma_f32_16x16x32_bf16 v[192:195], v[0:3], v[204:207], 0\n\t"
            "v_mfma_f32_16x16x32_bf16 v[192:195], v[4:7], v[208:211], v[192:195]\n\t"
            "ds_write_b32 v249, v236\n\t"
            "v_mfma_f32_16x16x32_bf16 v[196:199], v[24:27], v[204:207], 0\n\t"
            "ds_read_b32 v236, v251\n\t"
            "ds_read_b32 v237, v251 offset:64\n\t"
            "v_mfma_f32_16x16x32_bf16 v[196:199], v[28:31], v[208:211], v[196:199]\n\t"
            "v_mfma_f32_16x16x32_bf16 v[200:203], v[48:51], v[204:207], 0\n\t"
            "v_mfma_f32_16x16x32_bf16 v[200:203], v[52:55], v[208:211], v[200:203]\n\t"
            "v_mfma_f32_16x16x32_bf16 v[216:219], v[72:75], v[204:207], 0\n\t"
            "v_mfma_f32_16x16x32_bf16 v[216:219], v[76:79], v[208:211], v[216:219]\n\t"
            "v_mfma_f32_16x16x32_bf16 v[220:223], v[96:99], v[204:207], 0\n\t"
            "v_mfma_f32_16x16x32_bf16 v[220:223], v[100:103], v[208:211], v[220:223]\n\t"
            "v_mfma_f32_16x16x32_bf16 v[228:231], v[120:123], v[204:207], 0\n\t"
            "ds_read_b128 v[204:207], v232 offset:6400\n\t"
            "v_add_u32 v232, s48, v244\n\t"
            "v_mfma_f32_16x16x32_bf16 v[228:231], v[124:127], v[208:211], v[228:231]\n\t"
            "ds_read_b128 v[208:211], v233 offset:6400\n\t"
            "s_waitcnt vmcnt(12)\n\t"
            "v_mfma_f32_16x16x32_bf16 v[168:171], v[8:11], v[152:155], v[168:171]\n\t"
            "v_xor_b32 v233, 64, v232\n\t"
            "v_mfma_f32_16x16x32_bf16 v[168:171], v[12:15], v[156:159], v[168:171]\n\t"
            "v_mfma_f32_16x16x32_bf16 v[176:179], v[56:59], v[152:155], v[176:179]\n\t"
            "v_mfma_f32_16x16x32_bf16 v[176:179], v[60:63], v[156:159], v[176:179]\n\t"
            "v_mfma_f32_16x16x32_bf16 v[172:175], v[32:35], v[152:155], v[172:175]\n\t"
            "v_mfma_f32_16x16x32_bf16 v[172:175], v[36:39], v[156:159], v[172:175]\n\t"
            "v_mfma_f32_16x16x32_bf16 v[180:183], v[80:83], v[152:155], v[180:183]\n\t"
            "v_mfma_f32_16x16x32_bf16 v[180:183], v[84:87], v[156:159], v[180:183]\n\t"
            "v_mfma_f32_16x16x32_bf16 v[184:187], v[104:107], v[152:155], v[184:187]\n\t"
            "v_mfma_f32_16x16x32_bf16 v[184:187], v[108:111], v[156:159], v[184:187]\n\t"
            "v_mfma_f32_16x16x32_bf16 v[188:191], v[128:131], v[152:155], v[188:191]\n\t"
            "v_mfma_f32_16x16x32_bf16 v[188:191], v[132:135], v[156:159], v[188:191]\n\t"
            "v_mfma_f32_16x16x32_bf16 v[192:195], v[8:11], v[212:215], v[192:195]\n\t"
            "v_mfma_f32_16x16x32_bf16 v[192:195], v[12:15], v[224:227], v[192:195]\n\t"
            "v_mfma_f32_16x16x32_bf16 v[196:199], v[32:35], v[212:215], v[196:199]\n\t"
            "v_mfma_f32_16x16x32_bf16 v[196:199], v[36:39], v[224:227], v[196:199]\n\t"
            "v_mfma_f32_16x16x32_bf16 v[200:203], v[56:59], v[212:215], v[200:203]\n\t"
            "v_mfma_f32_16x16x32_bf16 v[200:203], v[60:63], v[224:227], v[200:203]\n\t"
            "v_mfma_f32_16x16x32_bf16 v[216:219], v[80:83], v[212:215], v[216:219]\n\t"
            "v_mfma_f32_16x16x32_bf16 v[216:219], v[84:87], v[224:227], v[216:219]\n\t"
            "v_mfma_f32_16x16x32_bf16 v[220:223], v[104:107], v[212:215], v[220:223]\n\t"
            "v_mfma_f32_16x16x32_bf16 v[220:223], v[108:111], v[224:227], v[220:223]\n\t"
            "v_mfma_f32_16x16x32_bf16 v[228:231], v[128:131], v[212:215], v[228:231]\n\t"
            "ds_read_b128 v[212:215], v232 offset:256\n\t"
            "v_mfma_f32_16x16x32_bf16 v[228:231], v[132:135], v[224:227], v[228:231]\n\t"
            "ds_read_b128 v[224:227], v233 offset:256\n\t"
            "s_waitcnt vmcnt(0)\n\t"
            "v_mfma_f32_16x16x32_bf16 v[168:171], v[16:19], v[160:163], v[168:171]\n\t"
            "v_mfma_f32_16x16x32_bf16 v[168:171], v[20:23], v[164:167], v[168:171]\n\t"
            "v_mfma_f32_16x16x32_bf16 v[176:179], v[64:67], v[160:163], v[176:179]\n\t"
            "v_mfma_f32_16x16x32_bf16 v[176:179], v[68:71], v[164:167], v[176:179]\n\t"
            "v_mfma_f32_16x16x32_bf16 v[180:183], v[88:91], v[160:163], v[180:183]\n\t"
            "v_mfma_f32_16x16x32_bf16 v[180:183], v[92:95], v[164:167], v[180:183]\n\t"
            "v_mfma_f32_16x16x32_bf16 v[172:175], v[40:43], v[160:163], v[172:175]\n\t"
            "v_mfma_f32_16x16x32_bf16 v[172:175], v[44:47], v[164:167], v[172:175]\n\t"
            "s_waitcnt lgkmcnt(5)\n\t"
            "s_nop 0\n\t"
            "v_mul_f32 v168, v236, v168\n\t"
            "v_mfma_f32_16x16x32_bf16 v[184:187], v[112:115], v[160:163], v[184:187]\n\t"
            "v_mul_f32 v169, v236, v169\n\t"
            "v_mul_f32 v170, v236, v170\n\t"
            "v_mfma_f32_16x16x32_bf16 v[184:187], v[116:119], v[164:167], v[184:187]\n\t"
            "v_mul_f32 v176, v236, v176\n\t"
            "v_mul_f32 v177, v236, v177\n\t"
            "v_mfma_f32_16x16x32_bf16 v[188:191], v[136:139], v[160:163], v[188:191]\n\t"
            "v_mul_f32 v171, v236, v171\n\t"
            "v_mul_f32 v178, v236, v178\n\t"
            "v_mfma_f32_16x16x32_bf16 v[188:191], v[140:143], v[164:167], v[188:191]\n\t"
            "v_mul_f32 v179, v236, v179\n\t"
            "v_cvt_pk_bf16_f32 v168, v168, v169\n\t"
            "s_waitcnt lgkmcnt(2)\n\t"
            "v_mfma_f32_16x16x32_bf16 v[192:195], v[16:19], v[204:207], v[192:195]\n\t"
            "v_cvt_pk_bf16_f32 v176, v176, v177\n\t"
            "v_cvt_pk_bf16_f32 v180, v180, v181\n\t"
            "v_mfma_f32_16x16x32_bf16 v[192:195], v[20:23], v[208:211], v[192:195]\n\t"
            "v_mul_f32 v172, v236, v172\n\t"
            "v_mul_f32 v173, v236, v173\n\t"
            "v_mfma_f32_16x16x32_bf16 v[196:199], v[40:43], v[204:207], v[196:199]\n\t"
            "v_mul_f32 v174, v236, v174\n\t"
            "v_mul_f32 v175, v236, v175\n\t"
            "v_mfma_f32_16x16x32_bf16 v[196:199], v[44:47], v[208:211], v[196:199]\n\t"
            "v_cvt_pk_bf16_f32 v169, v170, v171\n\t"
            "v_cvt_pk_bf16_f32 v177, v178, v179\n\t"
            "v_mfma_f32_16x16x32_bf16 v[200:203], v[64:67], v[204:207], v[200:203]\n\t"
            "v_cvt_pk_bf16_f32 v181, v182, v183\n\t"
            "v_cvt_pk_bf16_f32 v170, v172, v173\n\t"
            "v_mfma_f32_16x16x32_bf16 v[200:203], v[68:71], v[208:211], v[200:203]\n\t"
            "v_cvt_pk_bf16_f32 v171, v174, v175\n\t"
            "v_cvt_pk_bf16_f32 v182, v184, v185\n\t"
            "v_mfma_f32_16x16x32_bf16 v[216:219], v[88:91], v[204:207], v[216:219]\n\t"
            "v_cvt_pk_bf16_f32 v183, v186, v187\n\t"
            "v_cvt_pk_bf16_f32 v178, v188, v189\n\t"
            "v_mfma_f32_16x16x32_bf16 v[216:219], v[92:95], v[208:211], v[216:219]\n\t"
            "v_cvt_pk_bf16_f32 v179, v190, v191\n\t"
            "v_mul_f32 v236, 0x3e553b94, v236\n\t"
            "v_mfma_f32_16x16x32_bf16 v[220:223], v[112:115], v[204:207], v[220:223]\n\t"
            "v_add_u32 v184, s76, v239\n\t"
            "v_add_u32 v185, s74, v239\n\t"
            "v_mfma_f32_16x16x32_bf16 v[220:223], v[116:119], v[208:211], v[220:223]\n\t"
            "s_mov_b64 exec, -1\n\t"
            "ds_write_b128 v234, v[168:171]\n\t"
            "ds_write_b128 v234, v[180:183] offset:64\n\t"
            "ds_write_b64 v235, v[176:177] offset:128\n\t"
            "ds_write_b64 v235, v[178:179] offset:160\n\t"
            "s_mov_b64 exec, -1\n\t"
            "v_mfma_f32_16x16x16_bf16 v[172:175], v[176:177], v[178:179], 0\n\t"
            "v_mul_f32 v192, v237, v192\n\t"
            "v_mul_f32 v193, v237, v193\n\t"
            "v_mul_f32 v200, v237, v200\n\t"
            "v_mfma_f32_16x16x32_bf16 v[228:231], v[136:139], v[204:207], v[228:231]\n\t"
            "ds_read_b128 v[204:207], v232\n\t"
            "v_mul_f32 v201, v237, v201\n\t"
            "v_mfma_f32_16x16x32_bf16 v[228:231], v[140:143], v[208:211], v[228:231]\n\t"
            "ds_read_b128 v[208:211], v233\n\t"
            "v_mul_f32 v194, v237, v194\n\t"
            "v_mfma_f32_16x16x32_bf16 v[172:175], v[168:171], v[180:183], v[172:175]\n\t"
            "v_mul_f32 v195, v237, v195\n\t"
            "v_mul_f32 v202, v237, v202\n\t"
            "v_mul_f32 v203, v237, v203\n\t"
            "v_cvt_pk_bf16_f32 v192, v192, v193\n\t"
            "v_cvt_pk_bf16_f32 v200, v200, v201\n\t"
            "v_cvt_pk_bf16_f32 v216, v216, v217\n\t"
            "v_mul_f32 v196, v237, v196\n\t"
            "v_mul_f32 v197, v237, v197\n\t"
            "v_mul_f32 v198, v237, v198\n\t"
            "v_mul_f32 v199, v237, v199\n\t"
            "v_cvt_pk_bf16_f32 v193, v194, v195\n\t"
            "v_cvt_pk_bf16_f32 v201, v202, v203\n\t"
            "v_cvt_pk_bf16_f32 v217, v218, v219\n\t"
            "v_cvt_pk_bf16_f32 v194, v196, v197\n\t"
            "v_cvt_pk_bf16_f32 v195, v198, v199\n\t"
            "v_cvt_pk_bf16_f32 v218, v220, v221\n\t"
            "v_cvt_pk_bf16_f32 v219, v222, v223\n\t"
            "v_cvt_pk_bf16_f32 v202, v228, v229\n\t"
            "v_cvt_pk_bf16_f32 v203, v230, v231\n\t"
            "v_mul_f32 v237, 0x3e553b94, v237\n\t"
            "v_add_u32 v220, s76, v239\n\t"
            "v_mfma_f32_16x16x16_bf16 v[196:199], v[200:201], v[202:203], 0\n\t"
            "s_and_b32 exec_lo, s56, 0x11111111\n\t"
            "s_and_b32 exec_hi, s57, 0x11111111\n\t"
            "ds_write_b32 v184, v172 offset:0\n\t"
            "s_and_b32 exec_lo, s56, 0x22222222\n\t"
            "s_and_b32 exec_hi, s57, 0x22222222\n\t"
            "ds_write_b32 v184, v173 offset:0\n\t"
            "s_and_b32 exec_lo, s56, 0x44444444\n\t"
            "s_and_b32 exec_hi, s57, 0x44444444\n\t"
            "ds_write_b32 v184, v174 offset:0\n\t"
            "s_and_b32 exec_lo, s56, 0x88888888\n\t"
            "s_and_b32 exec_hi, s57, 0x88888888\n\t"
            "ds_write_b32 v184, v175 offset:0\n\t"
            "s_mov_b64 exec, s[56:57]\n\t"
            "ds_write_b32 v185, v236 offset:0\n\t"
            "s_mov_b64 exec, -1\n\t"
            "s_waitcnt lgkmcnt(5)\n\t"
            "v_mfma_f32_16x16x32_bf16 v[144:147], v[0:3], v[204:207], 0\n\t"
            "v_add_u32 v221, s74, v239\n\t"
            "v_mfma_f32_16x16x32_bf16 v[144:147], v[4:7], v[208:211], v[144:147]\n\t"
            "s_mov_b64 exec, s[54:55]\n\t"
            "ds_write_b128 v234, v[192:195] offset:12800\n\t"
            "ds_write_b128 v234, v[216:219] offset:12864\n\t"
            "ds_write_b64 v235, v[200:201] offset:12928\n\t"
            "ds_write_b64 v235, v[202:203] offset:12960\n\t"
            "s_mov_b64 exec, -1\n\t"
            "v_mfma_f32_16x16x32_bf16 v[148:151], v[28:31], v[208:211], 0\n\t"
            "v_add_u32 v235, s48, v248\n\t"
            "v_mfma_f32_16x16x32_bf16 v[148:151], v[24:27], v[204:207], v[148:151]\n\t"
            "ds_read_b128 v[228:231], v235 offset:18816\n\t"
            "v_mfma_f32_16x16x32_bf16 v[152:155], v[48:51], v[204:207], 0\n\t"
            "v_mfma_f32_16x16x32_bf16 v[152:155], v[52:55], v[208:211], v[152:155]\n\t"
            "v_mfma_f32_16x16x32_bf16 v[156:159], v[76:79], v[208:211], 0\n\t"
            "v_mfma_f32_16x16x32_bf16 v[156:159], v[72:75], v[204:207], v[156:159]\n\t"
            "v_mfma_f32_16x16x32_bf16 v[160:163], v[96:99], v[204:207], 0\n\t"
            "v_mfma_f32_16x16x32_bf16 v[160:163], v[100:103], v[208:211], v[160:163]\n\t"
            "v_mfma_f32_16x16x32_bf16 v[164:167], v[124:127], v[208:211], 0\n\t"
            "ds_read_b128 v[208:211], v233 offset:128\n\t"
            "s_waitcnt lgkmcnt(1)\n\t"
            "v_add_f32 v236, v228, v230\n\t"
            "v_mfma_f32_16x16x32_bf16 v[164:167], v[120:123], v[204:207], v[164:167]\n\t"
            "ds_read_b128 v[204:207], v232 offset:128\n\t"
            "v_mfma_f32_16x16x32_bf16 v[196:199], v[192:195], v[216:219], v[196:199]\n\t"
            "s_nop 7\n\t"
            "s_and_b32 exec_lo, s78, 0x11111111\n\t"
            "s_and_b32 exec_hi, s79, 0x11111111\n\t"
            "ds_write_b32 v220, v196 offset:12800\n\t"
            "s_and_b32 exec_lo, s78, 0x22222222\n\t"
            "s_and_b32 exec_hi, s79, 0x22222222\n\t"
            "ds_write_b32 v220, v197 offset:12800\n\t"
            "s_and_b32 exec_lo, s78, 0x44444444\n\t"
            "s_and_b32 exec_hi, s79, 0x44444444\n\t"
            "ds_write_b32 v220, v198 offset:12800\n\t"
            "s_and_b32 exec_lo, s78, 0x88888888\n\t"
            "s_and_b32 exec_hi, s79, 0x88888888\n\t"
            "ds_write_b32 v220, v199 offset:12800\n\t"
            "s_mov_b64 exec, s[78:79]\n\t"
            "ds_write_b32 v221, v237 offset:12800\n\t"
            "s_mov_b64 exec, -1\n\t"
            "s_waitcnt lgkmcnt(5)\n\t"
            "v_mfma_f32_16x16x32_bf16 v[144:147], v[8:11], v[204:207], v[144:147]\n\t"
            "v_add_f32 v237, v229, v231\n\t"
            "v_mfma_f32_16x16x32_bf16 v[144:147], v[12:15], v[208:211], v[144:147]\n\t"
            "ds_read_b128 v[228:231], v235 offset:19840\n\t"
            "v_mfma_f32_16x16x32_bf16 v[148:151], v[36:39], v[208:211], v[148:151]\n\t"
            "v_mfma_f32_16x16x32_bf16 v[148:151], v[32:35], v[204:207], v[148:151]\n\t"
            "v_mfma_f32_16x16x32_bf16 v[152:155], v[56:59], v[204:207], v[152:155]\n\t"
            "v_mfma_f32_16x16x32_bf16 v[152:155], v[60:63], v[208:211], v[152:155]\n\t"
            "v_mfma_f32_16x16x32_bf16 v[156:159], v[84:87], v[208:211], v[156:159]\n\t"
            "v_mfma_f32_16x16x32_bf16 v[156:159], v[80:83], v[204:207], v[156:159]\n\t"
            "v_mfma_f32_16x16x32_bf16 v[160:163], v[104:107], v[204:207], v[160:163]\n\t"
            "s_waitcnt lgkmcnt(0)\n\t"
            "v_add_f32 v228, v228, v230\n\t"
            "v_add_f32 v236, v236, v228\n\t"
            "v_mfma_f32_16x16x32_bf16 v[160:163], v[108:111], v[208:211], v[160:163]\n\t"
            "v_add_f32 v229, v229, v231\n\t"
            "v_mul_f32 v236, 0x3baaaaab, v236\n\t"
            "v_mfma_f32_16x16x32_bf16 v[164:167], v[132:135], v[208:211], v[164:167]\n\t"
            "ds_read_b128 v[208:211], v233 offset:6144\n\t"
            "v_add_f32 v237, v237, v229\n\t"
            "v_mfma_f32_16x16x32_bf16 v[164:167], v[128:131], v[204:207], v[164:167]\n\t"
            "ds_read_b128 v[204:207], v232 offset:6144\n\t"
            "v_mul_f32_e64 v236, v236, -v236\n\t"
            "v_mfma_f32_16x16x32_bf16 v[144:147], v[16:19], v[212:215], v[144:147]\n\t"
            "v_fmac_f32 v236, 0x3baaaaab, v237\n\t"
            "v_max_f32 v236, 0, v236\n\t"
            "v_mfma_f32_16x16x32_bf16 v[144:147], v[20:23], v[224:227], v[144:147]\n\t"
            "v_add_f32 v236, 0x3727c5ac, v236\n\t"
            "v_rsq_f32 v236, v236\n\t"
            "v_mfma_f32_16x16x32_bf16 v[148:151], v[44:47], v[224:227], v[148:151]\n\t"
            "v_mfma_f32_16x16x32_bf16 v[148:151], v[40:43], v[212:215], v[148:151]\n\t"
            "ds_write_b32 v249, v236\n\t"
            "v_mfma_f32_16x16x32_bf16 v[152:155], v[64:67], v[212:215], v[152:155]\n\t"
            "ds_read_b32 v236, v251\n\t"
            "ds_read_b32 v237, v251 offset:64\n\t"
            "v_mfma_f32_16x16x32_bf16 v[152:155], v[68:71], v[224:227], v[152:155]\n\t"
            "ds_read_b32 v238, v251 offset:128\n\t"
            "v_mfma_f32_16x16x32_bf16 v[156:159], v[92:95], v[224:227], v[156:159]\n\t"
            "v_mfma_f32_16x16x32_bf16 v[156:159], v[88:91], v[212:215], v[156:159]\n\t"
            "v_mfma_f32_16x16x32_bf16 v[160:163], v[112:115], v[212:215], v[160:163]\n\t"
            "v_mfma_f32_16x16x32_bf16 v[160:163], v[116:119], v[224:227], v[160:163]\n\t"
            "v_mfma_f32_16x16x32_bf16 v[164:167], v[140:143], v[224:227], v[164:167]\n\t"
            "ds_read_b128 v[224:227], v233 offset:6272\n\t"
            "v_mfma_f32_16x16x32_bf16 v[164:167], v[136:139], v[212:215], v[164:167]\n\t"
            "ds_read_b128 v[212:215], v232 offset:6272\n\t"
            "s_waitcnt lgkmcnt(4)\n\t"
            "v_mul_f32 v144, v236, v144\n\t"
            "v_mfma_f32_16x16x32_bf16 v[168:171], v[0:3], v[204:207], 0\n\t"
            "v_mul_f32 v145, v236, v145\n\t"
            "v_mul_f32 v152, v236, v152\n\t"
            "v_mfma_f32_16x16x32_bf16 v[168:171], v[4:7], v[208:211], v[168:171]\n\t"
            "v_mul_f32 v153, v236, v153\n\t"
            "v_mul_f32 v146, v236, v146\n\t"
            "v_mfma_f32_16x16x32_bf16 v[172:175], v[28:31], v[208:211], 0\n\t"
            "v_mul_f32 v147, v236, v147\n\t"
            "v_mul_f32 v154, v236, v154\n\t"
            "v_mfma_f32_16x16x32_bf16 v[172:175], v[24:27], v[204:207], v[172:175]\n\t"
            "v_mul_f32 v155, v236, v155\n\t"
            "v_cvt_pk_bf16_f32 v144, v144, v145\n\t"
            "v_mfma_f32_16x16x32_bf16 v[176:179], v[48:51], v[204:207], 0\n\t"
            "v_cvt_pk_bf16_f32 v152, v152, v153\n\t"
            "v_cvt_pk_bf16_f32 v156, v156, v157\n\t"
            "v_mfma_f32_16x16x32_bf16 v[176:179], v[52:55], v[208:211], v[176:179]\n\t"
            "v_mul_f32 v148, v236, v148\n\t"
            "v_mul_f32 v149, v236, v149\n\t"
            "v_mfma_f32_16x16x32_bf16 v[180:183], v[76:79], v[208:211], 0\n\t"
            "v_mul_f32 v150, v236, v150\n\t"
            "v_mul_f32 v151, v236, v151\n\t"
            "v_mfma_f32_16x16x32_bf16 v[180:183], v[72:75], v[204:207], v[180:183]\n\t"
            "v_cvt_pk_bf16_f32 v145, v146, v147\n\t"
            "v_cvt_pk_bf16_f32 v153, v154, v155\n\t"
            "v_mfma_f32_16x16x32_bf16 v[184:187], v[96:99], v[204:207], 0\n\t"
            "v_cvt_pk_bf16_f32 v157, v158, v159\n\t"
            "v_cvt_pk_bf16_f32 v146, v148, v149\n\t"
            "v_mfma_f32_16x16x32_bf16 v[184:187], v[100:103], v[208:211], v[184:187]\n\t"
            "v_cvt_pk_bf16_f32 v147, v150, v151\n\t"
            "v_cvt_pk_bf16_f32 v158, v160, v161\n\t"
            "v_mfma_f32_16x16x32_bf16 v[188:191], v[124:127], v[208:211], 0\n\t"
            "ds_read_b128 v[208:211], v233 offset:6400\n\t"
            "v_cvt_pk_bf16_f32 v159, v162, v163\n\t"
            "v_mfma_f32_16x16x32_bf16 v[188:191], v[120:123], v[204:207], v[188:191]\n\t"
            "ds_read_b128 v[204:207], v232 offset:6400\n\t"
            "v_cvt_pk_bf16_f32 v154, v164, v165\n\t"
            "s_waitcnt lgkmcnt(2)\n\t"
            "v_mfma_f32_16x16x32_bf16 v[168:171], v[8:11], v[212:215], v[168:171]\n\t"
            "v_cvt_pk_bf16_f32 v155, v166, v167\n\t"
            "v_mul_f32 v236, 0x3e553b94, v236\n\t"
            "v_mfma_f32_16x16x32_bf16 v[168:171], v[12:15], v[224:227], v[168:171]\n\t"
            "v_mfma_f32_16x16x32_bf16 v[172:175], v[36:39], v[224:227], v[172:175]\n\t"
            "v_mfma_f32_16x16x32_bf16 v[172:175], v[32:35], v[212:215], v[172:175]\n\t"
            "v_mfma_f32_16x16x32_bf16 v[176:179], v[56:59], v[212:215], v[176:179]\n\t"
            "v_mfma_f32_16x16x32_bf16 v[176:179], v[60:63], v[224:227], v[176:179]\n\t"
            "v_mfma_f32_16x16x32_bf16 v[180:183], v[84:87], v[224:227], v[180:183]\n\t"
            "v_mfma_f32_16x16x32_bf16 v[180:183], v[80:83], v[212:215], v[180:183]\n\t"
            "v_mfma_f32_16x16x32_bf16 v[184:187], v[104:107], v[212:215], v[184:187]\n\t"
            "v_mfma_f32_16x16x32_bf16 v[184:187], v[108:111], v[224:227], v[184:187]\n\t"
            "v_mfma_f32_16x16x32_bf16 v[188:191], v[132:135], v[224:227], v[188:191]\n\t"
            "ds_read_b128 v[224:227], v233 offset:12288\n\t"
            "v_mfma_f32_16x16x32_bf16 v[188:191], v[128:131], v[212:215], v[188:191]\n\t"
            "ds_read_b128 v[212:215], v232 offset:12288\n\t"
            "s_waitcnt lgkmcnt(2)\n\t"
            "v_mfma_f32_16x16x32_bf16 v[168:171], v[16:19], v[204:207], v[168:171]\n\t"
            "v_mfma_f32_16x16x32_bf16 v[168:171], v[20:23], v[208:211], v[168:171]\n\t"
            "v_mfma_f32_16x16x32_bf16 v[172:175], v[44:47], v[208:211], v[172:175]\n\t"
            "v_mfma_f32_16x16x32_bf16 v[172:175], v[40:43], v[204:207], v[172:175]\n\t"
            "v_mfma_f32_16x16x32_bf16 v[176:179], v[64:67], v[204:207], v[176:179]\n\t"
            "v_mfma_f32_16x16x32_bf16 v[176:179], v[68:71], v[208:211], v[176:179]\n\t"
            "v_mfma_f32_16x16x32_bf16 v[180:183], v[92:95], v[208:211], v[180:183]\n\t"
            "v_mfma_f32_16x16x32_bf16 v[180:183], v[88:91], v[204:207], v[180:183]\n\t"
            "v_mfma_f32_16x16x32_bf16 v[184:187], v[112:115], v[204:207], v[184:187]\n\t"
            "v_mfma_f32_16x16x32_bf16 v[184:187], v[116:119], v[208:211], v[184:187]\n\t"
            "v_mfma_f32_16x16x32_bf16 v[188:191], v[140:143], v[208:211], v[188:191]\n\t"
            "ds_read_b128 v[208:211], v233 offset:12416\n\t"
            "v_mfma_f32_16x16x32_bf16 v[188:191], v[136:139], v[204:207], v[188:191]\n\t"
            "ds_read_b128 v[204:207], v232 offset:12416\n\t"
            "s_waitcnt lgkmcnt(0)\n\t"
            "v_subrev_u32 v249, s75, v251\n\t"
            "v_add_u32 v249, 162816, v249\n\t"
            "s_mov_b32 s90, 2\n\t"
            "s_mov_b32 s81, 0\n\t"
            "s_mov_b32 s91, 0\n\t"
            "s_nop 7\n\t"
            ".p2align 6\n\t"
            "s_nop 0\n\t"
            "s_nop 0\n\t"
            "s_nop 0\n\t"
            ".Lwa_loop%=:\n\t"
            "s_setprio 2\n\t"
            "s_waitcnt lgkmcnt(2)\n\t"
            "v_mfma_f32_16x16x32_bf16 v[192:195], v[0:3], v[212:215], 0\n\t"
            "s_bfe_u32 s49, s70, 0x30001\n\t"
            "s_lshl_b32 s49, s49, 4\n\t"
            "s_or_b32 s73, s77, s49\n\t"
            "v_mfma_f32_16x16x32_bf16 v[192:195], v[4:7], v[224:227], v[192:195]\n\t"
            "v_mad_u32_u24 v220, v246, s72, v253\n\t"
            "v_mad_u32_u24 v216, v246, s72, v252\n\t"
            "v_mfma_f32_16x16x32_bf16 v[148:151], v[28:31], v[224:227], 0\n\t"
            "ds_read_b64 v[222:223], v220 offset:128\n\t"
            "v_mov_b32_dpp v221, v220 quad_perm:[1,0,2,3] row_mask:0xf bank_mask:0xf\n\t"
            "v_mfma_f32_16x16x32_bf16 v[148:151], v[24:27], v[212:215], v[148:151]\n\t"
            "ds_read_b64 v[220:221], v221 offset:160\n\t"
            "v_mov_b32_dpp v217, v216 quad_perm:[1,0,2,3] row_mask:0xf bank_mask:0xf\n\t"
            "v_mfma_f32_16x16x32_bf16 v[196:199], v[48:51], v[212:215], 0\n\t"
            "v_mul_f32 v176, v237, v176\n\t"
            "v_mul_f32 v177, v237, v177\n\t"
            "v_mfma_f32_16x16x32_bf16 v[196:199], v[52:55], v[224:227], v[196:199]\n\t"
            "v_mul_f32 v178, v237, v178\n\t"
            "v_mul_f32 v179, v237, v179\n\t"
            "v_mfma_f32_16x16x32_bf16 v[200:203], v[76:79], v[224:227], 0\n\t"
            "v_cvt_pk_bf16_f32 v176, v176, v177\n\t"
            "v_mul_f32 v168, v237, v168\n\t"
            "v_mfma_f32_16x16x32_bf16 v[200:203], v[72:75], v[212:215], v[200:203]\n\t"
            "v_mul_f32 v169, v237, v169\n\t"
            "v_cvt_pk_bf16_f32 v177, v178, v179\n\t"
            "v_mfma_f32_16x16x32_bf16 v[160:163], v[96:99], v[212:215], 0\n\t"
            "v_mul_f32 v170, v237, v170\n\t"
            "v_mul_f32 v171, v237, v171\n\t"
            "v_mfma_f32_16x16x32_bf16 v[160:163], v[100:103], v[224:227], v[160:163]\n\t"
            "v_cvt_pk_bf16_f32 v168, v168, v169\n\t"
            "v_cvt_pk_bf16_f32 v180, v180, v181\n\t"
            "v_mfma_f32_16x16x32_bf16 v[164:167], v[124:127], v[224:227], 0\n\t"
            "ds_read_b128 v[224:227], v233 offset:12544\n\t"
            "v_cvt_pk_bf16_f32 v178, v188, v189\n\t"
            "v_mfma_f32_16x16x32_bf16 v[164:167], v[120:123], v[212:215], v[164:167]\n\t"
            "ds_read_b128 v[212:215], v232 offset:12544\n\t"
            "v_cvt_pk_bf16_f32 v179, v190, v191\n\t"
            "s_waitcnt lgkmcnt(4)\n\t"
            "v_mfma_f32_16x16x32_bf16 v[192:195], v[8:11], v[204:207], v[192:195]\n\t"
            "v_mul_f32 v172, v237, v172\n\t"
            "v_mul_f32 v173, v237, v173\n\t"
            "v_mfma_f32_16x16x32_bf16 v[192:195], v[12:15], v[208:211], v[192:195]\n\t"
            "v_mul_f32 v174, v237, v174\n\t"
            "v_mul_f32 v175, v237, v175\n\t"
            "v_mfma_f32_16x16x32_bf16 v[196:199], v[56:59], v[204:207], v[196:199]\n\t"
            "v_cvt_pk_bf16_f32 v169, v170, v171\n\t"
            "v_cvt_pk_bf16_f32 v181, v182, v183\n\t"
            "v_mfma_f32_16x16x32_bf16 v[196:199], v[60:63], v[208:211], v[196:199]\n\t"
            "v_cvt_pk_bf16_f32 v170, v172, v173\n\t"
            "v_cvt_pk_bf16_f32 v171, v174, v175\n\t"
            "v_mfma_f32_16x16x32_bf16 v[160:163], v[104:107], v[204:207], v[160:163]\n\t"
            "v_cvt_pk_bf16_f32 v182, v184, v185\n\t"
            "v_cvt_pk_bf16_f32 v183, v186, v187\n\t"
            "v_mfma_f32_16x16x32_bf16 v[160:163], v[108:111], v[208:211], v[160:163]\n\t"
            "v_mov_b32 v230, s76\n\t"
            "v_mov_b32 v239, s74\n\t"
            "v_mfma_f32_16x16x32_bf16 v[148:151], v[36:39], v[208:211], v[148:151]\n\t"
            "s_add_u32 s70, s70, 1\n\t"
            "s_add_u32 s72, s72, 800\n\t"
            "s_add_u32 s74, s74, 800\n\t"
            "s_add_u32 s76, s76, 800\n\t"
            "v_mfma_f32_16x16x32_bf16 v[148:151], v[32:35], v[204:207], v[148:151]\n\t"
            "s_add_u32 s77, s77, 384\n\t"
            "s_xor_b32 s48, s48, s47\n\t"
            "ds_read_b32 v230, v230\n\t"
            "v_mfma_f32_16x16x32_bf16 v[200:203], v[84:87], v[208:211], v[200:203]\n\t"
            "ds_read_b32 v239, v239\n\t"
            "v_mul_f32 v237, 0x3e553b94, v237\n\t"
            "v_mfma_f32_16x16x32_bf16 v[200:203], v[80:83], v[204:207], v[200:203]\n\t"
            "v_mfma_f32_16x16x32_bf16 v[164:167], v[132:135], v[208:211], v[164:167]\n\t"
            "v_mfma_f32_16x16x32_bf16 v[164:167], v[128:131], v[204:207], v[164:167]\n\t"
            "s_waitcnt lgkmcnt(2)\n\t"
            "v_mfma_f32_16x16x32_bf16 v[192:195], v[16:19], v[212:215], v[192:195]\n\t"
            "v_mfma_f32_16x16x32_bf16 v[192:195], v[20:23], v[224:227], v[192:195]\n\t"
            "v_mfma_f32_16x16x32_bf16 v[196:199], v[64:67], v[212:215], v[196:199]\n\t"
            "v_mfma_f32_16x16x32_bf16 v[196:199], v[68:71], v[224:227], v[196:199]\n\t"
            "v_mfma_f32_16x16x32_bf16 v[160:163], v[112:115], v[212:215], v[160:163]\n\t"
            "v_mfma_f32_16x16x32_bf16 v[160:163], v[116:119], v[224:227], v[160:163]\n\t"
            "v_mfma_f32_16x16x32_bf16 v[148:151], v[44:47], v[224:227], v[148:151]\n\t"
            "v_mfma_f32_16x16x32_bf16 v[148:151], v[40:43], v[212:215], v[148:151]\n\t"
            "s_nop 1\n\t"
            "v_mul_f32 v192, v238, v192\n\t"
            "v_mfma_f32_16x16x32_bf16 v[200:203], v[92:95], v[224:227], v[200:203]\n\t"
            "v_mul_f32 v193, v238, v193\n\t"
            "v_mul_f32 v194, v238, v194\n\t"
            "v_mfma_f32_16x16x32_bf16 v[200:203], v[88:91], v[212:215], v[200:203]\n\t"
            "v_mul_f32 v196, v238, v196\n\t"
            "v_mul_f32 v197, v238, v197\n\t"
            "v_mfma_f32_16x16x32_bf16 v[164:167], v[140:143], v[224:227], v[164:167]\n\t"
            "ds_read_b128 v[224:227], v217 offset:64\n\t"
            "ds_read_b128 v[216:219], v216\n\t"
            "v_mfma_f32_16x16x32_bf16 v[164:167], v[136:139], v[212:215], v[164:167]\n\t"
            "v_mul_f32 v198, v238, v198\n\t"
            "v_mul_f32 v199, v238, v199\n\t"
            "v_mfma_f32_16x16x32_bf16 v[172:175], v[220:223], v[152:155], 0\n\t"
            "s_setprio 0\n\t"
            "v_cvt_pk_bf16_f32 v196, v196, v197\n\t"
            "v_mul_f32 v195, v238, v195\n\t"
            "s_setprio 3\n\t"
            "v_mfma_f32_16x16x32_bf16 v[212:215], v[220:223], v[176:179], 0\n\t"
            "v_cvt_pk_bf16_f32 v192, v192, v193\n\t"
            "v_cvt_pk_bf16_f32 v197, v198, v199\n\t"
            "v_cvt_pk_bf16_f32 v200, v200, v201\n\t"
            "v_mul_f32 v148, v238, v148\n\t"
            "v_mfma_f32_16x16x16_bf16 v[184:187], v[152:153], v[178:179], 0\n\t"
            "v_mul_f32 v149, v238, v149\n\t"
            "v_mul_f32 v150, v238, v150\n\t"
            "v_mul_f32 v151, v238, v151\n\t"
            "v_mfma_f32_16x16x16_bf16 v[204:207], v[176:177], v[178:179], 0\n\t"
            "v_cvt_pk_bf16_f32 v193, v194, v195\n\t"
            "v_cvt_pk_bf16_f32 v201, v202, v203\n\t"
            "v_cvt_pk_bf16_f32 v194, v148, v149\n\t"
            "v_mfma_f32_16x16x16_bf16 v[188:191], v[196:197], v[178:179], 0\n\t"
            "v_cvt_pk_bf16_f32 v195, v150, v151\n\t"
            "v_cvt_pk_bf16_f32 v202, v160, v161\n\t"
            "v_cvt_pk_bf16_f32 v203, v162, v163\n\t"
            "s_waitcnt lgkmcnt(0)\n\t"
            "v_mfma_f32_16x16x32_bf16 v[172:175], v[216:219], v[156:159], v[172:175]\n\t"
            "v_cvt_pk_bf16_f32 v198, v164, v165\n\t"
            "v_cvt_pk_bf16_f32 v199, v166, v167\n\t"
            "v_mfma_f32_16x16x32_bf16 v[172:175], v[224:227], v[144:147], v[172:175]\n\t"
            "v_mul_f32 v238, 0x3e553b94, v238\n\t"
            "v_mfma_f32_16x16x16_bf16 v[148:151], v[152:153], v[154:155], 0\n\t"
            "v_mfma_f32_16x16x16_bf16 v[160:163], v[176:177], v[154:155], 0\n\t"
            "v_mfma_f32_16x16x16_bf16 v[164:167], v[196:197], v[154:155], 0\n\t"
            "v_mfma_f32_16x16x32_bf16 v[212:215], v[216:219], v[180:183], v[212:215]\n\t"
            "v_mfma_f32_16x16x32_bf16 v[212:215], v[224:227], v[168:171], v[212:215]\n\t"
            "s_nop 1\n\t"
            "v_mfma_f32_16x16x32_bf16 v[148:151], v[144:147], v[156:159], v[148:151]\n\t"
            "v_mfma_f32_16x16x32_bf16 v[184:187], v[144:147], v[180:183], v[184:187]\n\t"
            "v_mfma_f32_16x16x32_bf16 v[160:163], v[168:171], v[156:159], v[160:163]\n\t"
            "v_mfma_f32_16x16x32_bf16 v[204:207], v[168:171], v[180:183], v[204:207]\n\t"
            "v_mfma_f32_16x16x32_bf16 v[164:167], v[192:195], v[156:159], v[164:167]\n\t"
            "v_mfma_f32_16x16x32_bf16 v[188:191], v[192:195], v[180:183], v[188:191]\n\t"
            "v_cndmask_b32_e64 v235, v173, v213, s[60:61]\n\t"
            "v_mfma_f32_16x16x32_bf16 v[208:211], v[220:223], v[196:199], 0\n\t"
            "s_nop 0\n\t"
            "v_max3_f32 v228, v172, v148, v149\n\t"
            "v_max3_f32 v228, v228, v150, v151\n\t"
            "v_mfma_f32_16x16x32_bf16 v[208:211], v[216:219], v[200:203], v[208:211]\n\t"
            "v_max3_f32 v229, v212, v184, v185\n\t"
            "v_max3_f32 v229, v229, v186, v187\n\t"
            "v_mfma_f32_16x16x32_bf16 v[208:211], v[224:227], v[192:195], v[208:211]\n\t"
            "s_setprio 0\n\t"
            "v_max3_f32 v228, v228, v160, v161\n\t"
            "v_max3_f32 v229, v229, v204, v205\n\t"
            "v_max3_f32 v228, v228, v162, v163\n\t"
            "v_max3_f32 v229, v229, v206, v207\n\t"
            "v_max3_f32 v228, v228, v164, v165\n\t"
            "v_max3_f32 v229, v229, v188, v189\n\t"
            "v_max3_f32 v228, v228, v166, v167\n\t"
            "v_max3_f32 v229, v229, v190, v191\n\t"
            "v_max3_f32 v234, v173, v213, v209\n\t"
            "v_max_f32 v234, v234, v230\n\t"
            "v_permlane32_swap_b32 v228, v229\n\t"
            "v_max_f32 v228, v228, v229\n\t"
            "v_mov_b32 v231, v228\n\t"
            "v_max_f32_dpp v234, v234, v234 row_ror:8 row_mask:0xf bank_mask:0xf\n\t"
            "v_cndmask_b32_e64 v235, v235, v209, s[62:63]\n\t"
            "v_permlane16_swap_b32 v228, v231\n\t"
            "v_max_f32 v228, v228, v231\n\t"
            "v_mov_b32 v229, v228\n\t"
            "v_max_f32_dpp v234, v234, v234 row_ror:4 row_mask:0xf bank_mask:0xf\n\t"
            "v_cndmask_b32_e64 v235, v235, v230, s[64:65]\n\t"
            "v_permlane32_swap_b32 v228, v229\n\t"
            "v_mul_f32_e64 v229, -v229, v237\n\t"
            "v_fma_f32 v184, v184, v237, v229\n\t"
            "v_fma_f32 v185, v185, v237, v229\n\t"
            "v_exp_f32 v184, v184\n\t"
            "v_exp_f32 v185, v185\n\t"
            "v_fma_f32 v186, v186, v237, v229\n\t"
            "v_fma_f32 v187, v187, v237, v229\n\t"
            "v_exp_f32 v186, v186\n\t"
            "v_exp_f32 v187, v187\n\t"
            "v_fma_f32 v204, v204, v237, v229\n\t"
            "v_fma_f32 v205, v205, v237, v229\n\t"
            "v_fma_f32 v206, v206, v237, v229\n\t"
            "v_fma_f32 v207, v207, v237, v229\n\t"
            "v_exp_f32 v204, v204\n\t"
            "v_exp_f32 v205, v205\n\t"
            "v_exp_f32 v206, v206\n\t"
            "v_exp_f32 v207, v207\n\t"
            "v_cvt_pk_bf16_f32 v184, v184, v185\n\t"
            "v_mul_f32_e64 v228, -v228, v236\n\t"
            "v_cvt_pk_bf16_f32 v185, v186, v187\n\t"
            "v_fma_f32 v210, v172, v236, v228\n\t"
            "v_cvt_pk_bf16_f32 v186, v204, v205\n\t"
            "v_cvt_pk_bf16_f32 v187, v206, v207\n\t"
            "s_setprio 3\n\t"
            "v_mfma_f32_16x16x16_bf16 v[180:183], v[152:153], v[198:199], 0\n\t"
            "v_max_f32_dpp v234, v234, v234 row_ror:2 row_mask:0xf bank_mask:0xf\n\t"
            "s_mov_b64 exec, s[60:61]\n\t"
            "v_fma_f32 v210, v212, v237, v229\n\t"
            "s_mov_b64 exec, -1\n\t"
            "v_mfma_f32_16x16x16_bf16 v[204:207], v[176:177], v[198:199], 0\n\t"
            "v_max_f32_dpp v234, v234, v234 row_ror:1 row_mask:0xf bank_mask:0xf\n\t"
            "v_sub_f32 v235, v235, v234\n\t"
            "v_mul_f32 v235, v239, v235\n\t"
            "v_mfma_f32_16x16x16_bf16 v[172:175], v[196:197], v[198:199], 0\n\t"
            "v_exp_f32 v235, v235\n\t"
            "v_fma_f32 v148, v148, v236, v228\n\t"
            "v_mfma_f32_16x16x32_bf16 v[180:183], v[144:147], v[200:203], v[180:183]\n\t"
            "v_cvt_pk_bf16_f32 v235, v235, 0\n\t"
            "v_cndmask_b32_e64 v235, 0, v235, s[66:67]\n\t"
            "v_mfma_f32_16x16x32_bf16 v[204:207], v[168:171], v[200:203], v[204:207]\n\t"
            "ds_write_b16 v250, v235 offset:256\n\t"
            "v_mfma_f32_16x16x32_bf16 v[172:175], v[192:195], v[200:203], v[172:175]\n\t"
            "s_setprio 0\n\t"
            "v_fma_f32 v149, v149, v236, v228\n\t"
            "v_fma_f32 v164, v164, v236, v228\n\t"
            "v_fma_f32 v165, v165, v236, v228\n\t"
            "v_max3_f32 v230, v208, v180, v181\n\t"
            "v_max3_f32 v230, v230, v182, v183\n\t"
            "v_exp_f32 v148, v148\n\t"
            "v_max3_f32 v230, v230, v204, v205\n\t"
            "v_max3_f32 v230, v230, v206, v207\n\t"
            "v_max3_f32 v230, v230, v172, v173\n\t"
            "v_max3_f32 v230, v230, v174, v175\n\t"
            "v_mov_b32 v231, v230\n\t"
            "v_exp_f32 v149, v149\n\t"
            "v_fma_f32 v150, v150, v236, v228\n\t"
            "v_permlane32_swap_b32 v230, v231\n\t"
            "v_max_f32 v230, v230, v231\n\t"
            "v_mov_b32 v231, v230\n\t"
            "v_fma_f32 v151, v151, v236, v228\n\t"
            "v_exp_f32 v164, v164\n\t"
            "v_permlane16_swap_b32 v230, v231\n\t"
            "v_max_f32 v230, v230, v231\n\t"
            "v_mul_f32_e64 v230, -v230, v238\n\t"
            "s_mov_b64 exec, s[62:63]\n\t"
            "v_fma_f32 v210, v208, v238, v230\n\t"
            "s_mov_b64 exec, -1\n\t"
            "v_exp_f32 v165, v165\n\t"
            "v_fma_f32 v166, v166, v236, v228\n\t"
            "v_fma_f32 v167, v167, v236, v228\n\t"
            "v_exp_f32 v150, v150\n\t"
            "v_exp_f32 v151, v151\n\t"
            "v_fma_f32 v160, v160, v236, v228\n\t"
            "v_fma_f32 v161, v161, v236, v228\n\t"
            "v_fma_f32 v162, v162, v236, v228\n\t"
            "v_fma_f32 v163, v163, v236, v228\n\t"
            "v_exp_f32 v166, v166\n\t"
            "v_exp_f32 v167, v167\n\t"
            "v_exp_f32 v210, v210\n\t"
            "v_exp_f32 v160, v160\n\t"
            "v_exp_f32 v161, v161\n\t"
            "v_exp_f32 v162, v162\n\t"
            "v_exp_f32 v163, v163\n\t"
            "v_cvt_pk_bf16_f32 v148, v148, v149\n\t"
            "v_cvt_pk_bf16_f32 v164, v164, v165\n\t"
            "v_cvt_pk_bf16_f32 v149, v150, v151\n\t"
            "v_cvt_pk_bf16_f32 v165, v166, v167\n\t"
            "v_cvt_pk_bf16_f32 v210, v210, 0\n\t"
            "v_cvt_pk_bf16_f32 v150, v160, v161\n\t"
            "v_cvt_pk_bf16_f32 v151, v162, v163\n\t"
            "v_mov_b32 v167, 0\n\t"
            "v_cndmask_b32_e64 v166, 0, v210, s[58:59]\n\t"
            "v_fma_f32 v188, v188, v237, v229\n\t"
            "v_fma_f32 v189, v189, v237, v229\n\t"
            "v_fma_f32 v180, v180, v238, v230\n\t"
            "v_fma_f32 v181, v181, v238, v230\n\t"
            "v_fma_f32 v172, v172, v238, v230\n\t"
            "v_fma_f32 v173, v173, v238, v230\n\t"
            "v_exp_f32 v188, v188\n\t"
            "v_exp_f32 v189, v189\n\t"
            "v_fma_f32 v190, v190, v237, v229\n\t"
            "v_fma_f32 v191, v191, v237, v229\n\t"
            "v_exp_f32 v180, v180\n\t"
            "v_exp_f32 v181, v181\n\t"
            "v_fma_f32 v182, v182, v238, v230\n\t"
            "v_fma_f32 v183, v183, v238, v230\n\t"
            "v_exp_f32 v172, v172\n\t"
            "v_exp_f32 v173, v173\n\t"
            "v_fma_f32 v174, v174, v238, v230\n\t"
            "v_fma_f32 v175, v175, v238, v230\n\t"
            "v_exp_f32 v190, v190\n\t"
            "v_exp_f32 v191, v191\n\t"
            "v_exp_f32 v182, v182\n\t"
            "v_exp_f32 v183, v183\n\t"
            "v_fma_f32 v204, v204, v238, v230\n\t"
            "v_fma_f32 v205, v205, v238, v230\n\t"
            "v_fma_f32 v206, v206, v238, v230\n\t"
            "v_fma_f32 v207, v207, v238, v230\n\t"
            "v_exp_f32 v174, v174\n\t"
            "v_exp_f32 v175, v175\n\t"
            "v_exp_f32 v204, v204\n\t"
            "v_exp_f32 v205, v205\n\t"
            "v_exp_f32 v206, v206\n\t"
            "v_exp_f32 v207, v207\n\t"
            "v_cvt_pk_bf16_f32 v188, v188, v189\n\t"
            "v_cvt_pk_bf16_f32 v180, v180, v181\n\t"
            "v_cvt_pk_bf16_f32 v172, v172, v173\n\t"
            "v_cvt_pk_bf16_f32 v189, v190, v191\n\t"
            "v_cvt_pk_bf16_f32 v181, v182, v183\n\t"
            "v_cvt_pk_bf16_f32 v173, v174, v175\n\t"
            "v_cvt_pk_bf16_f32 v182, v204, v205\n\t"
            "v_cvt_pk_bf16_f32 v183, v206, v207\n\t"
            "v_cndmask_b32_e64 v190, 0, v210, s[60:61]\n\t"
            "v_cndmask_b32_e64 v174, 0, v210, s[62:63]\n\t"
            "v_mov_b32 v191, 0\n\t"
            "v_mov_b32 v175, 0\n\t"
            "s_cmp_ge_u32 s70, s80\n\t"
            "s_barrier\n\t"
            "s_setprio 2\n\t"
            "s_cbranch_scc1 .Lwa_h1%=\n\t"
            ".Lwa_r1%=:\n\t"
            "v_mfma_f32_16x16x32_bf16 v[200:203], v[240:243], v[148:151], 0\n\t"
            "ds_read_b64 v[176:177], v247\n\t"
            "ds_read_b64 v[196:197], v247 offset:1536\n\t"
            "v_mfma_f32_16x16x32_bf16 v[200:203], v[240:243], v[164:167], v[200:203]\n\t"
            "ds_read_b64 v[220:221], v247 offset:3072\n\t"
            "v_xor_b32 v234, s73, v254\n\t"
            "v_add_u32 v235, s45, v248\n\t"
            "v_mov_b32 v179, 0\n\t"
            "v_mov_b32 v199, 0\n\t"
            "v_mov_b32 v223, 0\n\t"
            "v_add_u32 v232, s48, v244\n\t"
            "v_xor_b32 v233, 64, v232\n\t"
            "v_rcp_f32 v201, v200\n\t"
            "v_xor_b32 v249, 0x100, v249\n\t"
            "ds_read_b128 v[168:171], v235\n\t"
            "ds_read_u16 v178, v234\n\t"
            "ds_read_b128 v[192:195], v235 offset:1536\n\t"
            "ds_read_u16 v198, v234 offset:128\n\t"
            "ds_read_b128 v[216:219], v235 offset:3072\n\t"
            "ds_read_u16 v222, v234 offset:256\n\t"
            "ds_read_b128 v[204:207], v232\n\t"
            "ds_read_b128 v[208:211], v233\n\t"
            "ds_read_b32 v236, v249\n\t"
            "ds_read_b32 v237, v249 offset:64\n\t"
            "ds_read_b32 v238, v249 offset:128\n\t"
            "v_mad_u32_u24 v234, v246, s75, v253\n\t"
            "s_waitcnt lgkmcnt(9)\n\t"
            "v_mfma_f32_16x16x32_bf16 v[224:227], v[168:171], v[148:151], 0\n\t"
            "v_mfma_f32_16x16x32_bf16 v[224:227], v[176:179], v[164:167], v[224:227]\n\t"
            "s_waitcnt lgkmcnt(7)\n\t"
            "v_mfma_f32_16x16x32_bf16 v[212:215], v[192:195], v[148:151], 0\n\t"
            "v_mfma_f32_16x16x32_bf16 v[212:215], v[196:199], v[164:167], v[212:215]\n\t"
            "s_waitcnt lgkmcnt(5)\n\t"
            "v_mfma_f32_16x16x32_bf16 v[148:151], v[216:219], v[148:151], 0\n\t"
            "v_mfma_f32_16x16x32_bf16 v[148:151], v[220:223], v[164:167], v[148:151]\n\t"
            "s_waitcnt lgkmcnt(3)\n\t"
            "v_mfma_f32_16x16x32_bf16 v[144:147], v[0:3], v[204:207], 0\n\t"
            "v_mfma_f32_16x16x32_bf16 v[144:147], v[4:7], v[208:211], v[144:147]\n\t"
            "v_mul_f32 v224, v201, v224\n\t"
            "v_mfma_f32_16x16x32_bf16 v[152:155], v[48:51], v[204:207], 0\n\t"
            "v_mul_f32 v225, v201, v225\n\t"
            "v_mul_f32 v226, v201, v226\n\t"
            "v_mfma_f32_16x16x32_bf16 v[152:155], v[52:55], v[208:211], v[152:155]\n\t"
            "v_mul_f32 v212, v201, v212\n\t"
            "v_mul_f32 v213, v201, v213\n\t"
            "v_mfma_f32_16x16x32_bf16 v[156:159], v[76:79], v[208:211], 0\n\t"
            "v_mul_f32 v227, v201, v227\n\t"
            "v_cvt_pk_bf16_f32 v224, v224, v225\n\t"
            "v_mfma_f32_16x16x32_bf16 v[156:159], v[72:75], v[204:207], v[156:159]\n\t"
            "v_mul_f32 v214, v201, v214\n\t"
            "v_mul_f32 v215, v201, v215\n\t"
            "v_mfma_f32_16x16x32_bf16 v[160:163], v[96:99], v[204:207], 0\n\t"
            "v_cvt_pk_bf16_f32 v212, v212, v213\n\t"
            "v_cvt_pk_bf16_f32 v225, v226, v227\n\t"
            "v_mfma_f32_16x16x32_bf16 v[160:163], v[100:103], v[208:211], v[160:163]\n\t"
            "v_cvt_pk_bf16_f32 v213, v214, v215\n\t"
            "v_mul_f32 v148, v201, v148\n\t"
            "v_mfma_f32_16x16x32_bf16 v[164:167], v[124:127], v[208:211], 0\n\t"
            "v_mul_f32 v149, v201, v149\n\t"
            "v_mul_f32 v150, v201, v150\n\t"
            "v_mfma_f32_16x16x32_bf16 v[164:167], v[120:123], v[204:207], v[164:167]\n\t"
            "ds_write_b64 v245, v[224:225]\n\t"
            "v_mfma_f32_16x16x32_bf16 v[224:227], v[168:171], v[184:187], 0\n\t"
            "v_mul_f32 v151, v201, v151\n\t"
            "v_cvt_pk_bf16_f32 v148, v148, v149\n\t"
            "v_mfma_f32_16x16x32_bf16 v[224:227], v[176:179], v[188:191], v[224:227]\n\t"
            "ds_write_b64 v245, v[212:213] offset:128\n\t"
            "v_mfma_f32_16x16x32_bf16 v[200:203], v[240:243], v[184:187], 0\n\t"
            "v_cvt_pk_bf16_f32 v149, v150, v151\n\t"
            "v_mfma_f32_16x16x32_bf16 v[200:203], v[240:243], v[188:191], v[200:203]\n\t"
            "ds_write_b64 v245, v[148:149] offset:256\n\t"
            "v_mfma_f32_16x16x32_bf16 v[212:215], v[192:195], v[184:187], 0\n\t"
            "v_mfma_f32_16x16x32_bf16 v[212:215], v[196:199], v[188:191], v[212:215]\n\t"
            "v_mfma_f32_16x16x32_bf16 v[184:187], v[216:219], v[184:187], 0\n\t"
            "v_mfma_f32_16x16x32_bf16 v[184:187], v[220:223], v[188:191], v[184:187]\n\t"
            "v_mfma_f32_16x16x32_bf16 v[148:151], v[28:31], v[208:211], 0\n\t"
            "ds_read_b128 v[208:211], v233 offset:128\n\t"
            "v_mfma_f32_16x16x32_bf16 v[148:151], v[24:27], v[204:207], v[148:151]\n\t"
            "ds_read_b128 v[204:207], v232 offset:128\n\t"
            "v_rcp_f32 v201, v200\n\t"
            "v_mfma_f32_16x16x32_bf16 v[188:191], v[192:195], v[180:183], 0\n\t"
            "v_mul_f32 v224, v201, v224\n\t"
            "v_mul_f32 v225, v201, v225\n\t"
            "v_mfma_f32_16x16x32_bf16 v[188:191], v[196:199], v[172:175], v[188:191]\n\t"
            "v_mul_f32 v226, v201, v226\n\t"
            "v_mul_f32 v227, v201, v227\n\t"
            "v_mul_f32 v212, v201, v212\n\t"
            "v_mul_f32 v213, v201, v213\n\t"
            "v_mul_f32 v214, v201, v214\n\t"
            "v_mul_f32 v215, v201, v215\n\t"
            "v_mul_f32 v184, v201, v184\n\t"
            "v_mul_f32 v185, v201, v185\n\t"
            "v_mul_f32 v186, v201, v186\n\t"
            "v_mul_f32 v187, v201, v187\n\t"
            "v_mfma_f32_16x16x32_bf16 v[200:203], v[240:243], v[180:183], 0\n\t"
            "v_cvt_pk_bf16_f32 v224, v224, v225\n\t"
            "v_cvt_pk_bf16_f32 v225, v226, v227\n\t"
            "v_mfma_f32_16x16x32_bf16 v[200:203], v[240:243], v[172:175], v[200:203]\n\t"
            "ds_write_b64 v245, v[224:225] offset:6144\n\t"
            "v_mfma_f32_16x16x32_bf16 v[224:227], v[168:171], v[180:183], 0\n\t"
            "v_cvt_pk_bf16_f32 v184, v184, v185\n\t"
            "v_cvt_pk_bf16_f32 v185, v186, v187\n\t"
            "v_mfma_f32_16x16x32_bf16 v[224:227], v[176:179], v[172:175], v[224:227]\n\t"
            "ds_read_b64 v[186:187], v234 offset:288\n\t"
            "ds_write_b64 v245, v[184:185] offset:6400\n\t"
            "s_waitcnt lgkmcnt(3)\n\t"
            "v_mfma_f32_16x16x32_bf16 v[144:147], v[8:11], v[204:207], v[144:147]\n\t"
            "ds_read_b64 v[184:185], v234 offset:256\n\t"
            "v_rcp_f32 v201, v200\n\t"
            "v_mfma_f32_16x16x32_bf16 v[144:147], v[12:15], v[208:211], v[144:147]\n\t"
            "v_mul_f32 v188, v201, v188\n\t"
            "v_mul_f32 v189, v201, v189\n\t"
            "v_mfma_f32_16x16x32_bf16 v[148:151], v[36:39], v[208:211], v[148:151]\n\t"
            "v_mul_f32 v190, v201, v190\n\t"
            "v_mul_f32 v191, v201, v191\n\t"
            "v_mfma_f32_16x16x32_bf16 v[148:151], v[32:35], v[204:207], v[148:151]\n\t"
            "v_cvt_pk_bf16_f32 v188, v188, v189\n\t"
            "v_cvt_pk_bf16_f32 v189, v190, v191\n\t"
            "v_mfma_f32_16x16x32_bf16 v[152:155], v[56:59], v[204:207], v[152:155]\n\t"
            "ds_read_b64 v[190:191], v234 offset:352\n\t"
            "v_mul_f32 v224, v201, v224\n\t"
            "v_mfma_f32_16x16x32_bf16 v[152:155], v[60:63], v[208:211], v[152:155]\n\t"
            "ds_write_b64 v245, v[188:189] offset:12416\n\t"
            "v_mfma_f32_16x16x32_bf16 v[156:159], v[84:87], v[208:211], v[156:159]\n\t"
            "ds_read_b64 v[188:189], v234 offset:320\n\t"
            "v_mul_f32 v225, v201, v225\n\t"
            "v_mfma_f32_16x16x32_bf16 v[156:159], v[80:83], v[204:207], v[156:159]\n\t"
            "v_cvt_pk_bf16_f32 v212, v212, v213\n\t"
            "v_mul_f32 v226, v201, v226\n\t"
            "v_mfma_f32_16x16x32_bf16 v[160:163], v[104:107], v[204:207], v[160:163]\n\t"
            "v_mul_f32 v227, v201, v227\n\t"
            "v_cvt_pk_bf16_f32 v224, v224, v225\n\t"
            "v_mfma_f32_16x16x32_bf16 v[160:163], v[108:111], v[208:211], v[160:163]\n\t"
            "v_cvt_pk_bf16_f32 v213, v214, v215\n\t"
            "v_cvt_pk_bf16_f32 v225, v226, v227\n\t"
            "v_mfma_f32_16x16x32_bf16 v[164:167], v[132:135], v[208:211], v[164:167]\n\t"
            "ds_read_b128 v[208:211], v233 offset:6144\n\t"
            "v_mfma_f32_16x16x32_bf16 v[164:167], v[128:131], v[204:207], v[164:167]\n\t"
            "ds_read_b128 v[204:207], v232 offset:6144\n\t"
            "ds_write_b64 v245, v[212:213] offset:6272\n\t"
            "v_mfma_f32_16x16x32_bf16 v[180:183], v[216:219], v[180:183], 0\n\t"
            "ds_read_b128 v[212:215], v232 offset:256\n\t"
            "v_mfma_f32_16x16x32_bf16 v[180:183], v[220:223], v[172:175], v[180:183]\n\t"
            "ds_write_b64 v245, v[224:225] offset:12288\n\t"
            "s_waitcnt lgkmcnt(5)\n\t"
            "v_mfma_f32_16x16x32_bf16 v[168:171], v[184:187], v[168:171], 0\n\t"
            "ds_read_b128 v[224:227], v233 offset:256\n\t"
            "v_mfma_f32_16x16x32_bf16 v[168:171], v[188:191], v[176:179], v[168:171]\n\t"
            "v_mfma_f32_16x16x32_bf16 v[192:195], v[184:187], v[192:195], 0\n\t"
            "v_mfma_f32_16x16x32_bf16 v[192:195], v[188:191], v[196:199], v[192:195]\n\t"
            "s_nop 0\n\t"
            "v_mul_f32 v180, v201, v180\n\t"
            "v_mfma_f32_16x16x32_bf16 v[216:219], v[184:187], v[216:219], 0\n\t"
            "v_mul_f32 v181, v201, v181\n\t"
            "v_mul_f32 v182, v201, v182\n\t"
            "v_mfma_f32_16x16x32_bf16 v[216:219], v[188:191], v[220:223], v[216:219]\n\t"
            "v_mul_f32 v183, v201, v183\n\t"
            "v_cvt_pk_bf16_f32 v180, v180, v181\n\t"
            "v_mfma_f32_16x16x32_bf16 v[200:203], v[240:243], v[184:187], 0\n\t"
            "v_cvt_pk_bf16_f32 v181, v182, v183\n\t"
            "v_mfma_f32_16x16x32_bf16 v[200:203], v[240:243], v[188:191], v[200:203]\n\t"
            "ds_write_b64 v245, v[180:181] offset:12544\n\t"
            "s_waitcnt lgkmcnt(1)\n\t"
            "v_mfma_f32_16x16x32_bf16 v[144:147], v[16:19], v[212:215], v[144:147]\n\t"
            "v_mfma_f32_16x16x32_bf16 v[144:147], v[20:23], v[224:227], v[144:147]\n\t"
            "v_mfma_f32_16x16x32_bf16 v[148:151], v[44:47], v[224:227], v[148:151]\n\t"
            "v_mfma_f32_16x16x32_bf16 v[148:151], v[40:43], v[212:215], v[148:151]\n\t"
            "v_mfma_f32_16x16x32_bf16 v[152:155], v[64:67], v[212:215], v[152:155]\n\t"
            "s_nop 0\n\t"
            "v_cndmask_b32_e64 v203, v168, v192, s[60:61]\n\t"
            "v_mfma_f32_16x16x32_bf16 v[152:155], v[68:71], v[224:227], v[152:155]\n\t"
            "v_rcp_f32 v201, v200\n\t"
            "v_mfma_f32_16x16x32_bf16 v[156:159], v[92:95], v[224:227], v[156:159]\n\t"
            "v_cndmask_b32_e64 v203, v203, v216, s[62:63]\n\t"
            "v_xad_u32 v202, s73, v254, v255\n\t"
            "v_mfma_f32_16x16x32_bf16 v[156:159], v[88:91], v[212:215], v[156:159]\n\t"
            "v_mul_f32_dpp v203, v201, v203 quad_perm:[0,0,0,0] row_mask:0xf bank_mask:0xf\n\t"
            "v_cvt_pk_bf16_f32 v203, v203, 0\n\t"
            "v_mfma_f32_16x16x32_bf16 v[160:163], v[112:115], v[212:215], v[160:163]\n\t"
            "v_mul_f32 v144, v236, v144\n\t"
            "v_mul_f32 v145, v236, v145\n\t"
            "v_mfma_f32_16x16x32_bf16 v[160:163], v[116:119], v[224:227], v[160:163]\n\t"
            "s_mov_b64 exec, s[68:69]\n\t"
            "ds_write_b16 v202, v203\n\t"
            "s_mov_b64 exec, 1\n\t"
            "ds_add_u32 v253, v246 offset:7856\n\t"
            "s_mov_b64 exec, -1\n\t"
            "v_mfma_f32_16x16x32_bf16 v[164:167], v[140:143], v[224:227], v[164:167]\n\t"
            "ds_read_b128 v[224:227], v233 offset:6272\n\t"
            "v_mul_f32 v152, v236, v152\n\t"
            "v_mfma_f32_16x16x32_bf16 v[164:167], v[136:139], v[212:215], v[164:167]\n\t"
            "ds_read_b128 v[212:215], v232 offset:6272\n\t"
            "v_mul_f32 v153, v236, v153\n\t"
            "v_mfma_f32_16x16x32_bf16 v[168:171], v[0:3], v[204:207], 0\n\t"
            "v_mul_f32 v146, v236, v146\n\t"
            "v_mul_f32 v147, v236, v147\n\t"
            "v_mfma_f32_16x16x32_bf16 v[168:171], v[4:7], v[208:211], v[168:171]\n\t"
            "v_mul_f32 v154, v236, v154\n\t"
            "v_mul_f32 v155, v236, v155\n\t"
            "v_mfma_f32_16x16x32_bf16 v[172:175], v[28:31], v[208:211], 0\n\t"
            "v_cvt_pk_bf16_f32 v144, v144, v145\n\t"
            "v_cvt_pk_bf16_f32 v152, v152, v153\n\t"
            "v_mfma_f32_16x16x32_bf16 v[172:175], v[24:27], v[204:207], v[172:175]\n\t"
            "v_cvt_pk_bf16_f32 v156, v156, v157\n\t"
            "v_mul_f32 v148, v236, v148\n\t"
            "v_mfma_f32_16x16x32_bf16 v[176:179], v[48:51], v[204:207], 0\n\t"
            "v_mul_f32 v149, v236, v149\n\t"
            "v_mul_f32 v150, v236, v150\n\t"
            "v_mfma_f32_16x16x32_bf16 v[176:179], v[52:55], v[208:211], v[176:179]\n\t"
            "v_mul_f32 v151, v236, v151\n\t"
            "v_cvt_pk_bf16_f32 v145, v146, v147\n\t"
            "v_mfma_f32_16x16x32_bf16 v[180:183], v[76:79], v[208:211], 0\n\t"
            "v_cvt_pk_bf16_f32 v153, v154, v155\n\t"
            "v_cvt_pk_bf16_f32 v157, v158, v159\n\t"
            "v_mfma_f32_16x16x32_bf16 v[180:183], v[72:75], v[204:207], v[180:183]\n\t"
            "v_cvt_pk_bf16_f32 v146, v148, v149\n\t"
            "v_cvt_pk_bf16_f32 v147, v150, v151\n\t"
            "v_mfma_f32_16x16x32_bf16 v[184:187], v[96:99], v[204:207], 0\n\t"
            "v_cvt_pk_bf16_f32 v158, v160, v161\n\t"
            "v_cvt_pk_bf16_f32 v159, v162, v163\n\t"
            "v_mfma_f32_16x16x32_bf16 v[184:187], v[100:103], v[208:211], v[184:187]\n\t"
            "v_cvt_pk_bf16_f32 v154, v164, v165\n\t"
            "v_cvt_pk_bf16_f32 v155, v166, v167\n\t"
            "v_mfma_f32_16x16x32_bf16 v[188:191], v[124:127], v[208:211], 0\n\t"
            "ds_read_b128 v[208:211], v233 offset:6400\n\t"
            "v_mul_f32 v236, 0x3e553b94, v236\n\t"
            "v_mfma_f32_16x16x32_bf16 v[188:191], v[120:123], v[204:207], v[188:191]\n\t"
            "ds_read_b128 v[204:207], v232 offset:6400\n\t"
            "s_waitcnt lgkmcnt(2)\n\t"
            "v_mfma_f32_16x16x32_bf16 v[168:171], v[8:11], v[212:215], v[168:171]\n\t"
            "v_mfma_f32_16x16x32_bf16 v[168:171], v[12:15], v[224:227], v[168:171]\n\t"
            "v_mfma_f32_16x16x32_bf16 v[172:175], v[36:39], v[224:227], v[172:175]\n\t"
            "v_mfma_f32_16x16x32_bf16 v[172:175], v[32:35], v[212:215], v[172:175]\n\t"
            "v_mfma_f32_16x16x32_bf16 v[176:179], v[56:59], v[212:215], v[176:179]\n\t"
            "v_mfma_f32_16x16x32_bf16 v[176:179], v[60:63], v[224:227], v[176:179]\n\t"
            "v_mfma_f32_16x16x32_bf16 v[180:183], v[84:87], v[224:227], v[180:183]\n\t"
            "v_mfma_f32_16x16x32_bf16 v[180:183], v[80:83], v[212:215], v[180:183]\n\t"
            "v_mfma_f32_16x16x32_bf16 v[184:187], v[104:107], v[212:215], v[184:187]\n\t"
            "v_mfma_f32_16x16x32_bf16 v[184:187], v[108:111], v[224:227], v[184:187]\n\t"
            "v_mfma_f32_16x16x32_bf16 v[188:191], v[132:135], v[224:227], v[188:191]\n\t"
            "ds_read_b128 v[224:227], v233 offset:12288\n\t"
            "v_mfma_f32_16x16x32_bf16 v[188:191], v[128:131], v[212:215], v[188:191]\n\t"
            "ds_read_b128 v[212:215], v232 offset:12288\n\t"
            "s_waitcnt lgkmcnt(2)\n\t"
            "v_mfma_f32_16x16x32_bf16 v[168:171], v[16:19], v[204:207], v[168:171]\n\t"
            "v_mfma_f32_16x16x32_bf16 v[168:171], v[20:23], v[208:211], v[168:171]\n\t"
            "v_mfma_f32_16x16x32_bf16 v[172:175], v[44:47], v[208:211], v[172:175]\n\t"
            "v_mfma_f32_16x16x32_bf16 v[172:175], v[40:43], v[204:207], v[172:175]\n\t"
            "v_mfma_f32_16x16x32_bf16 v[176:179], v[64:67], v[204:207], v[176:179]\n\t"
            "v_mfma_f32_16x16x32_bf16 v[176:179], v[68:71], v[208:211], v[176:179]\n\t"
            "v_mfma_f32_16x16x32_bf16 v[180:183], v[92:95], v[208:211], v[180:183]\n\t"
            "v_mfma_f32_16x16x32_bf16 v[180:183], v[88:91], v[204:207], v[180:183]\n\t"
            "v_mfma_f32_16x16x32_bf16 v[184:187], v[112:115], v[204:207], v[184:187]\n\t"
            "v_mfma_f32_16x16x32_bf16 v[184:187], v[116:119], v[208:211], v[184:187]\n\t"
            "v_mfma_f32_16x16x32_bf16 v[188:191], v[140:143], v[208:211], v[188:191]\n\t"
            "ds_read_b128 v[208:211], v233 offset:12416\n\t"
            "v_mfma_f32_16x16x32_bf16 v[188:191], v[136:139], v[204:207], v[188:191]\n\t"
            "ds_read_b128 v[204:207], v232 offset:12416\n\t"
            "s_cmp_eq_u32 s91, 0\n\t"
            "s_cbranch_scc1 .Lwa_loop%=\n\t"
            "s_mov_b32 s91, 0\n\t"
            "s_waitcnt vmcnt(0)\n\t"
            "s_cmp_eq_u32 s81, 0\n\t"
            "s_cbranch_scc1 .Lwa_h6%=\n\t"
            "s_mov_b32 s89, 0x10000\n\t"
            ".Lwa_h4%=:\n\t"
            "v_readfirstlane_b32 s49, v228\n\t"
            "v_readfirstlane_b32 vcc_lo, v229\n\t"
            "s_xor_b32 s49, s49, s82\n\t"
            "s_xor_b32 vcc_lo, vcc_lo, s83\n\t"
            "s_or_b32 s49, s49, vcc_lo\n\t"
            "s_cbranch_scc0 .Lwa_h5%=\n\t"
            "s_sub_u32 s89, s89, 1\n\t"
            "s_cbranch_scc1 .Lwa_h5%=\n\t"
            "s_sleep 2\n\t"
            "buffer_load_dwordx3 v[228:230], off, s[84:87], s81 offset:1216 sc1\n\t"
            "s_waitcnt vmcnt(0)\n\t"
            "s_branch .Lwa_h4%=\n\t"
            ".Lwa_h5%=:\n\t"
            "s_lshl_b32 s49, s70, 2\n\t"
            "s_add_u32 s49, s49, 163520\n\t"
            "v_mov_b32 v228, s49\n\t"
            "s_mul_i32 s49, s70, 800\n\t"
            "s_add_u32 m0, s49, 113760\n\t"
            "ds_write_b32 v228, v230 offset:12\n\t"
            "s_mov_b32 exec_hi, 0x3ffff\n\t"
            "buffer_load_dwordx4 v248, s[84:87], s81 offen sc1 lds\n\t"
            "s_mul_i32 s49, s70, 384\n\t"
            "s_add_u32 m0, s49, 130176\n\t"
            "s_add_u32 s81, s81, 800\n\t"
            "s_mov_b64 exec, 0xffffff\n\t"
            "buffer_load_dwordx4 v248, s[84:87], s81 offen sc1 lds\n\t"
            "s_mov_b64 exec, -1\n\t"
            "s_mov_b32 s81, 0\n\t"
            "s_waitcnt lgkmcnt(0)\n\t"
            ".Lwa_h6%=:\n\t"
            "s_cmp_eq_u32 s90, 0\n\t"
            "s_cbranch_scc1 .Lwa_loop%=\n\t"
            "v_readfirstlane_b32 s49, v231\n\t"
            "s_sub_u32 s90, s90, 1\n\t"
            "s_cmp_lt_u32 s49, 209\n\t"
            "s_cselect_b32 s90, s90, 0\n\t"
            "s_cbranch_scc0 .Lwa_loop%=\n\t"
            "s_mul_i32 s81, s49, 1280\n\t"
            "s_add_u32 s81, s81, 303104\n\t"
            "s_branch .Lwa_loop%=\n\t"
            ".Lwa_h1%=:\n\t"
            "s_cmp_lt_u32 s70, 20\n\t"
            "s_cbranch_scc1 .Lwa_h2%=\n\t"
            "s_lshl_b32 s49, s70, 2\n\t"
            "s_add_u32 s49, s49, 163520\n\t"
            "v_mov_b32 v228, s49\n\t"
            "ds_read_b32 v228, v228\n\t"
            "s_waitcnt lgkmcnt(0)\n\t"
            "v_readfirstlane_b32 s49, v228\n\t"
            "s_cmp_eq_u32 s49, 0x7ffffff\n\t"
            "s_cbranch_scc1 .Lwa_last%=\n\t"
            "s_branch .Lwa_r1%=\n\t"
            ".Lwa_h2%=:\n\t"
            "s_waitcnt vmcnt(0)\n\t"
            "s_or_b32 s91, s81, s90\n\t"
            "s_cmp_eq_u32 s81, 0\n\t"
            "s_cbranch_scc1 .Lwa_h3%=\n\t"
            "buffer_load_dwordx3 v[228:230], off, s[84:87], s81 offset:1216 sc1\n\t"
            ".Lwa_h3%=:\n\t"
            "s_cmp_eq_u32 s90, 0\n\t"
            "s_cbranch_scc1 .Lwa_r1%=\n\t"
            "v_mov_b32 v231, 1\n\t"
            "s_mov_b32 s49, 301184\n\t"
            "s_mov_b64 exec, 1\n\t"
            "buffer_atomic_add v231, off, s[84:87], s49 sc0\n\t"
            "s_mov_b64 exec, -1\n\t"
            "s_branch .Lwa_r1%=\n\t"
            ".Lwa_last%=:\n\t"
            "s_setprio 2\n\t"
            "s_waitcnt lgkmcnt(0)\n\t"
            "s_nop 7\n\t"
            "v_mfma_f32_16x16x32_bf16 v[156:159], v[240:243], v[184:187], 0\n\t"
            "ds_read_b64 v[176:177], v247\n\t"
            "ds_read_b64 v[196:197], v247 offset:1536\n\t"
            "v_mfma_f32_16x16x32_bf16 v[156:159], v[240:243], v[188:191], v[156:159]\n\t"
            "ds_read_b64 v[220:221], v247 offset:3072\n\t"
            "v_xor_b32 v234, s73, v254\n\t"
            "v_mfma_f32_16x16x32_bf16 v[228:231], v[240:243], v[148:151], 0\n\t"
            "ds_read_u16 v178, v234\n\t"
            "ds_read_u16 v198, v234 offset:128\n\t"
            "v_mfma_f32_16x16x32_bf16 v[228:231], v[240:243], v[164:167], v[228:231]\n\t"
            "ds_read_u16 v222, v234 offset:256\n\t"
            "v_add_u32 v235, s45, v248\n\t"
            "v_mfma_f32_16x16x32_bf16 v[208:211], v[240:243], v[180:183], 0\n\t"
            "ds_read_b128 v[168:171], v235\n\t"
            "ds_read_b128 v[192:195], v235 offset:1536\n\t"
            "v_mfma_f32_16x16x32_bf16 v[208:211], v[240:243], v[172:175], v[208:211]\n\t"
            "ds_read_b128 v[216:219], v235 offset:3072\n\t"
            "v_mov_b32 v179, 0\n\t"
            "v_mov_b32 v199, 0\n\t"
            "v_mov_b32 v223, 0\n\t"
            "v_rcp_f32 v157, v156\n\t"
            "v_mad_u32_u24 v234, v246, s75, v253\n\t"
            "v_rcp_f32 v229, v228\n\t"
            "s_nop 0\n\t"
            "v_rcp_f32 v209, v208\n\t"
            "s_waitcnt lgkmcnt(2)\n\t"
            "v_mfma_f32_16x16x32_bf16 v[144:147], v[168:171], v[184:187], 0\n\t"
            "v_mfma_f32_16x16x32_bf16 v[144:147], v[176:179], v[188:191], v[144:147]\n\t"
            "s_waitcnt lgkmcnt(1)\n\t"
            "v_mfma_f32_16x16x32_bf16 v[152:155], v[192:195], v[184:187], 0\n\t"
            "v_mfma_f32_16x16x32_bf16 v[152:155], v[196:199], v[188:191], v[152:155]\n\t"
            "s_waitcnt lgkmcnt(0)\n\t"
            "v_mfma_f32_16x16x32_bf16 v[184:187], v[216:219], v[184:187], 0\n\t"
            "v_mfma_f32_16x16x32_bf16 v[184:187], v[220:223], v[188:191], v[184:187]\n\t"
            "ds_read_b64 v[188:189], v234 offset:320\n\t"
            "ds_read_b64 v[190:191], v234 offset:352\n\t"
            "v_mfma_f32_16x16x32_bf16 v[224:227], v[168:171], v[148:151], 0\n\t"
            "v_mul_f32 v144, v157, v144\n\t"
            "v_mul_f32 v145, v157, v145\n\t"
            "v_mfma_f32_16x16x32_bf16 v[224:227], v[176:179], v[164:167], v[224:227]\n\t"
            "v_mul_f32 v152, v157, v152\n\t"
            "v_mul_f32 v153, v157, v153\n\t"
            "v_mfma_f32_16x16x32_bf16 v[212:215], v[192:195], v[148:151], 0\n\t"
            "v_mul_f32 v184, v157, v184\n\t"
            "v_mul_f32 v185, v157, v185\n\t"
            "v_mfma_f32_16x16x32_bf16 v[212:215], v[196:199], v[164:167], v[212:215]\n\t"
            "v_mul_f32 v186, v157, v186\n\t"
            "v_mul_f32 v187, v157, v187\n\t"
            "v_mfma_f32_16x16x32_bf16 v[160:163], v[168:171], v[180:183], 0\n\t"
            "v_cvt_pk_bf16_f32 v184, v184, v185\n\t"
            "v_cvt_pk_bf16_f32 v185, v186, v187\n\t"
            "v_mfma_f32_16x16x32_bf16 v[160:163], v[176:179], v[172:175], v[160:163]\n\t"
            "ds_read_b64 v[186:187], v234 offset:288\n\t"
            "ds_write_b64 v245, v[184:185] offset:6400\n\t"
            "v_mfma_f32_16x16x32_bf16 v[204:207], v[192:195], v[180:183], 0\n\t"
            "ds_read_b64 v[184:185], v234 offset:256\n\t"
            "v_mul_f32 v224, v229, v224\n\t"
            "v_mfma_f32_16x16x32_bf16 v[204:207], v[196:199], v[172:175], v[204:207]\n\t"
            "v_mul_f32 v225, v229, v225\n\t"
            "v_mul_f32 v212, v229, v212\n\t"
            "v_mfma_f32_16x16x32_bf16 v[148:151], v[216:219], v[148:151], 0\n\t"
            "v_mul_f32 v213, v229, v213\n\t"
            "v_mul_f32 v160, v209, v160\n\t"
            "v_mfma_f32_16x16x32_bf16 v[148:151], v[220:223], v[164:167], v[148:151]\n\t"
            "v_mul_f32 v161, v209, v161\n\t"
            "v_mul_f32 v226, v229, v226\n\t"
            "v_mfma_f32_16x16x32_bf16 v[180:183], v[216:219], v[180:183], 0\n\t"
            "v_mul_f32 v204, v209, v204\n\t"
            "v_mul_f32 v205, v209, v205\n\t"
            "v_mfma_f32_16x16x32_bf16 v[180:183], v[220:223], v[172:175], v[180:183]\n\t"
            "v_mul_f32 v227, v229, v227\n\t"
            "v_cvt_pk_bf16_f32 v224, v224, v225\n\t"
            "v_mul_f32 v148, v229, v148\n\t"
            "v_mul_f32 v149, v229, v149\n\t"
            "v_mul_f32 v214, v229, v214\n\t"
            "v_mul_f32 v215, v229, v215\n\t"
            "v_cvt_pk_bf16_f32 v212, v212, v213\n\t"
            "s_waitcnt lgkmcnt(0)\n\t"
            "v_mfma_f32_16x16x32_bf16 v[200:203], v[240:243], v[184:187], 0\n\t"
            "v_mul_f32 v180, v209, v180\n\t"
            "v_mul_f32 v181, v209, v181\n\t"
            "v_mfma_f32_16x16x32_bf16 v[200:203], v[240:243], v[188:191], v[200:203]\n\t"
            "v_mul_f32 v150, v229, v150\n\t"
            "v_mul_f32 v151, v229, v151\n\t"
            "v_mfma_f32_16x16x32_bf16 v[168:171], v[184:187], v[168:171], 0\n\t"
            "v_cvt_pk_bf16_f32 v148, v148, v149\n\t"
            "v_mul_f32 v146, v157, v146\n\t"
            "v_mfma_f32_16x16x32_bf16 v[168:171], v[188:191], v[176:179], v[168:171]\n\t"
            "v_mul_f32 v147, v157, v147\n\t"
            "v_cvt_pk_bf16_f32 v144, v144, v145\n\t"
            "v_mfma_f32_16x16x32_bf16 v[192:195], v[184:187], v[192:195], 0\n\t"
            "v_rcp_f32 v201, v200\n\t"
            "v_mfma_f32_16x16x32_bf16 v[192:195], v[188:191], v[196:199], v[192:195]\n\t"
            "v_mul_f32 v154, v157, v154\n\t"
            "v_mul_f32 v155, v157, v155\n\t"
            "v_mfma_f32_16x16x32_bf16 v[216:219], v[184:187], v[216:219], 0\n\t"
            "v_cvt_pk_bf16_f32 v152, v152, v153\n\t"
            "v_mul_f32 v162, v209, v162\n\t"
            "v_mfma_f32_16x16x32_bf16 v[216:219], v[188:191], v[220:223], v[216:219]\n\t"
            "v_mul_f32 v163, v209, v163\n\t"
            "v_cvt_pk_bf16_f32 v160, v160, v161\n\t"
            "v_cndmask_b32_e64 v203, v168, v192, s[60:61]\n\t"
            "v_mul_f32 v206, v209, v206\n\t"
            "v_mul_f32 v207, v209, v207\n\t"
            "v_cvt_pk_bf16_f32 v204, v204, v205\n\t"
            "v_mul_f32 v182, v209, v182\n\t"
            "v_mul_f32 v183, v209, v183\n\t"
            "v_cvt_pk_bf16_f32 v180, v180, v181\n\t"
            "v_cndmask_b32_e64 v203, v203, v216, s[62:63]\n\t"
            "v_cvt_pk_bf16_f32 v225, v226, v227\n\t"
            "v_cvt_pk_bf16_f32 v213, v214, v215\n\t"
            "v_cvt_pk_bf16_f32 v149, v150, v151\n\t"
            "v_cvt_pk_bf16_f32 v145, v146, v147\n\t"
            "v_cvt_pk_bf16_f32 v153, v154, v155\n\t"
            "v_cvt_pk_bf16_f32 v161, v162, v163\n\t"
            "v_cvt_pk_bf16_f32 v205, v206, v207\n\t"
            "v_cvt_pk_bf16_f32 v181, v182, v183\n\t"
            "v_mul_f32_dpp v203, v201, v203 quad_perm:[0,0,0,0] row_mask:0xf bank_mask:0xf\n\t"
            "v_xad_u32 v202, s73, v254, v255\n\t"
            "v_cvt_pk_bf16_f32 v203, v203, 0\n\t"
            "ds_write_b64 v245, v[224:225]\n\t"
            "ds_write_b64 v245, v[212:213] offset:128\n\t"
            "ds_write_b64 v245, v[148:149] offset:256\n\t"
            "ds_write_b64 v245, v[144:145] offset:6144\n\t"
            "ds_write_b64 v245, v[152:153] offset:6272\n\t"
            "ds_write_b64 v245, v[160:161] offset:12288\n\t"
            "ds_write_b64 v245, v[204:205] offset:12416\n\t"
            "ds_write_b64 v245, v[180:181] offset:12544\n\t"
            "s_mov_b64 exec, s[68:69]\n\t"
            "ds_write_b16 v202, v203\n\t"
            "s_mov_b64 exec, 1\n\t"
            "ds_add_u32 v253, v246 offset:7856\n\t"
            "s_mov_b64 exec, -1\n\t"
            ".Lwa_done%=:\n\t"
            "s_waitcnt lgkmcnt(0)\n\t"
            "s_nop 7\n\t"
            : "+{v204}"(pST_), "+{v205}"(aRSw_), "+{v206}"(aPLw_), "+{v207}"(aRSr_), "+{v208}"(g16_), "+{v209}"(g8_), "+{v210}"(loVL_), "+{v211}"(loOL_), "+{v200}"(on), "+{v250}"(c0), "+{v251}"(c1), "+{v252}"(c2), "+{v253}"(c3), "+{v254}"(c4), "+{v255}"(c5),
              "+{v238}"(c6), "+{v239}"(c7), "+{v240}"(c8), "+{v241}"(c9), "+{s89}"(s89), "+{s46}"(s46), "+{s48}"(s48), "+{s70}"(s70), "+{s72}"(s72), "+{s73}"(s73), "+{s74}"(s74), "+{s76}"(s76), "+{s77}"(s77)
            : "{v244}"(pE_), "{v245}"(aO_), "{v246}"(sel_), "{v247}"(aVB_), "{s47}"((ldsb + X_OFF) ^ (ldsb + X1_OFF)),
              "{s[58:59]}"(0xFFFFull), "{s75}"(rsb), "{s45}"(ldsb + V_OFF + w * 4608), "{s[68:69]}"(0xFFFFFFFFFFFFull),
              "{s[84:87]}"(wr), "{s88}"(touch), "{s80}"(wave == 0 ? 16 : 20), "{s71}"(ldsb + O_OFF), "{s[54:55]}"(mrow), "{s[56:57]}"(0xF0000F0000F0000Full),
              "{s[78:79]}"(mrow & 0xF0000F0000F0000Full)
            : "v144", "v145", "v146", "v147", "v148", "v149", "v150", "v151", "v152", "v153", "v154", "v155", "v156", "v157", "v158", "v159", "v160", "v161", "v162", "v163", "v164", "v165", "v166", "v167", "v168", "v169", "v170", "v171", "v172", "v173", "v174", "v175", "v176", "v177", "v178", "v179", "v180", "v181", "v182", "v183", "v184", "v185", "v186", "v187", "v188", "v189", "v190", "v191", "v192", "v193", "v194", "v195", "v196", "v197", "v198", "v199", "v201", "v202", "v203", "v212", "v213", "v214", "v215", "v216", "v217", "v218", "v219", "v220", "v221", "v222", "v223", "v224", "v225", "v226", "v227", "v228", "v229", "v230", "v231", "v232", "v233", "v234", "v235", "v236", "v237", "v242", "v243", "v248", "v249", "v0", "v1", "v2", "v3", "v4", "v5", "v6", "v7", "v8", "v9", "v10", "v11", "v12", "v13", "v14", "v15", "v16", "v17", "v18", "v19", "v20", "v21", "v22", "v23", "v24", "v25", "v26", "v27", "v28", "v29", "v30", "v31", "v32", "v33", "v34", "v35", "v36", "v37", "v38", "v39", "v40", "v41", "v42", "v43", "v44", "v45", "v46", "v47", "v48", "v49", "v50", "v51", "v52", "v53", "v54", "v55", "v56", "v57", "v58", "v59", "v60", "v61", "v62", "v63", "v64", "v65", "v66", "v67", "v68", "v69", "v70", "v71", "v72", "v73", "v74", "v75", "v76", "v77", "v78", "v79", "v80", "v81", "v82", "v83", "v84", "v85", "v86", "v87", "v88", "v89", "v90", "v91", "v92", "v93", "v94", "v95", "v96", "v97", "v98", "v99", "v100", "v101", "v102", "v103", "v104", "v105", "v106", "v107", "v108", "v109", "v110", "v111", "v112", "v113", "v114", "v115", "v116", "v117", "v118", "v119", "v120", "v121", "v122", "v123", "v124", "v125", "v126", "v127", "v128", "v129", "v130", "v131", "v132", "v133", "v134", "v135", "v136", "v137", "v138", "v139", "v140", "v141", "v142", "v143",  "m0", "s60", "s61", "s62", "s63", "s64", "s65", "s66", "s67", "s81", "s82", "s83", "s90", "s91", "s49", "vcc", "scc", "memory");

        }
        if (wave == 0 && blockIdx.x == 0) {
            int ln_;
            asm volatile("v_mbcnt_lo_u32_b32 %0, -1, 0\n\tv_mbcnt_hi_u32_b32 %0, -1, %0" : "=v"(ln_));
            KARGS
            unsigned long long tg;
            asm volatile("s_load_dwordx2 %0, %1, %2\n\ts_waitcnt lgkmcnt(0)" : "=s"(tg) : "s"(wb), "n"(EPO_OFF) : "memory");
            const unsigned lo = (unsigned)tg + 1u, hi = (unsigned)(tg >> 32);
            const auto wq = __builtin_amdgcn_make_buffer_rsrc((void*)wb, 0, WS_BYTES, 0x00020000);
#pragma unroll 1
            for (int it_ = 0; it_ < 100000; ++it_) {
                bool bad = false;
#pragma unroll
                for (int k = 0; k < 2; ++k) {
                    const u32x4 a_ = __builtin_amdgcn_raw_buffer_load_b128(wq, ARR_OFF + ln_ * 16, k * 1024, SC1);
                    bad |= a_[0] != lo || a_[1] != hi || a_[2] != lo || a_[3] != hi;
                }
                if (__builtin_amdgcn_ballot_w64(bad) == 0ull) break;
                __builtin_amdgcn_s_sleep(2);
            }
            if (ln_ == 0) __builtin_amdgcn_raw_buffer_store_b64((u32x2){lo, hi}, wq, EPO_OFF, 0, SC1);
        }
        asm volatile("wk_code_end:");
        __builtin_amdgcn_endpgm();
    } else if (wave < 6) {
        bf16x8 W[36];
        LANE_SETUP
        {
            const unsigned rsb = ldsb + RSX_OFF;
            const unsigned pE_ = fE - X_OFF;
            const unsigned aVAw_ = ldsb + V_OFF + w * 9216 + lane * 16, aVBw_ = ldsb + V_OFF + w * 9216 + 1024 + lane * 8;
            const unsigned pST_ = lane * 16;
            unsigned aRSw_ = rsb + 256 + lane * 4, aRSr_ = rsb + (lane & 48);
            B_SCALARS
            unsigned s44 = 4;
            asm volatile(
            "s_waitcnt lgkmcnt(0)\n\t"
            "s_mov_b32 s83, 0x10000\n\t"
            "s_mov_b32 s84, -47104\n\t"
            "s_nop 7\n\t"
            "v_mov_b32 v200, v250\n\t"
            "v_mov_b32 v201, v251\n\t"
            "v_mov_b32 v202, v253\n\t"
            "s_mov_b32 s38, s67\n\t"
            "s_mov_b32 s67, 74496\n\t"
            "ds_read_b64 v[250:251], v203\n\t"
            "ds_read_b32 v253, v203 offset:8\n\t"
            ".Lwk_b1%=:\n\t"
            "v_mov_b32 v236, 0\n\t"
            "ds_read_b32 v236, v236 offset:7864\n\t"
            "s_waitcnt lgkmcnt(0)\n\t"
            "v_readfirstlane_b32 s77, v236\n\t"
            "s_cmp_ge_u32 s77, 4\n\t"
            "s_cbranch_scc1 .Lwk_b2%=\n\t"
            "s_cmp_eq_u32 s83, 0\n\t"
            "s_cbranch_scc1 .Lwk_b2%=\n\t"
            "s_sub_u32 s83, s83, 1\n\t"
            "s_sleep 1\n\t"
            "s_branch .Lwk_b1%=\n\t"
            ".Lwk_b2%=:\n\t"
            "s_waitcnt vmcnt(0)\n\t"
            "s_call_b64 vcc, wk_conv\n\t"
            "v_mov_b32 v250, v200\n\t"
            "v_mov_b32 v251, v201\n\t"
            "v_mov_b32 v253, v202\n\t"
            "s_mov_b32 s67, s38\n\t"
            "s_setprio 3\n\t"
            "s_barrier\n\t"
            "buffer_load_dwordx4 v[140:143], v245, s[28:31], s82 offen sc1\n\t"
            "s_add_u32 s49, s33, 0\n\t"
            "buffer_load_dwordx4 v[0:3], v245, s[28:31], s49 offen sc1\n\t"
            "s_add_u32 s49, s33, 6144\n\t"
            "buffer_load_dwordx4 v[24:27], v245, s[28:31], s49 offen sc1\n\t"
            "s_add_u32 s49, s33, 12288\n\t"
            "buffer_load_dwordx4 v[48:51], v245, s[28:31], s49 offen sc1\n\t"
            "s_add_u32 s49, s33, 18432\n\t"
            "buffer_load_dwordx4 v[72:75], v245, s[28:31], s49 offen sc1\n\t"
            "s_add_u32 s49, s33, 24576\n\t"
            "buffer_load_dwordx4 v[96:99], v245, s[28:31], s49 offen sc1\n\t"
            "s_add_u32 s49, s33, 30720\n\t"
            "buffer_load_dwordx4 v[120:123], v245, s[28:31], s49 offen sc1\n\t"
            "s_add_u32 s49, s33, 1024\n\t"
            "buffer_load_dwordx4 v[4:7], v245, s[28:31], s49 offen sc1\n\t"
            "s_add_u32 s49, s33, 7168\n\t"
            "buffer_load_dwordx4 v[28:31], v245, s[28:31], s49 offen sc1\n\t"
            "s_add_u32 s49, s33, 13312\n\t"
            "buffer_load_dwordx4 v[52:55], v245, s[28:31], s49 offen sc1\n\t"
            "s_add_u32 s49, s33, 19456\n\t"
            "buffer_load_dwordx4 v[76:79], v245, s[28:31], s49 offen sc1\n\t"
            "s_add_u32 s49, s33, 25600\n\t"
            "buffer_load_dwordx4 v[100:103], v245, s[28:31], s49 offen sc1\n\t"
            "s_add_u32 s49, s33, 31744\n\t"
            "buffer_load_dwordx4 v[124:127], v245, s[28:31], s49 offen sc1\n\t"
            "s_add_u32 s49, s33, 2048\n\t"
            "buffer_load_dwordx4 v[8:11], v245, s[28:31], s49 offen sc1\n\t"
            "s_add_u32 s49, s33, 8192\n\t"
            "buffer_load_dwordx4 v[32:35], v245, s[28:31], s49 offen sc1\n\t"
            "s_add_u32 s49, s33, 14336\n\t"
            "buffer_load_dwordx4 v[56:59], v245, s[28:31], s49 offen sc1\n\t"
            "s_add_u32 s49, s33, 20480\n\t"
            "buffer_load_dwordx4 v[80:83], v245, s[28:31], s49 offen sc1\n\t"
            "s_add_u32 s49, s33, 26624\n\t"
            "buffer_load_dwordx4 v[104:107], v245, s[28:31], s49 offen sc1\n\t"
            "s_add_u32 s49, s33, 32768\n\t"
            "buffer_load_dwordx4 v[128:131], v245, s[28:31], s49 offen sc1\n\t"
            "s_add_u32 s49, s33, 3072\n\t"
            "buffer_load_dwordx4 v[12:15], v245, s[28:31], s49 offen sc1\n\t"
            "s_add_u32 s49, s33, 9216\n\t"
            "buffer_load_dwordx4 v[36:39], v245, s[28:31], s49 offen sc1\n\t"
            "s_add_u32 s49, s33, 15360\n\t"
            "buffer_load_dwordx4 v[60:63], v245, s[28:31], s49 offen sc1\n\t"
            "s_add_u32 s49, s33, 21504\n\t"
            "buffer_load_dwordx4 v[84:87], v245, s[28:31], s49 offen sc1\n\t"
            "s_add_u32 s49, s33, 27648\n\t"
            "buffer_load_dwordx4 v[108:111], v245, s[28:31], s49 offen sc1\n\t"
            "s_add_u32 s49, s33, 33792\n\t"
            "buffer_load_dwordx4 v[132:135], v245, s[28:31], s49 offen sc1\n\t"
            "s_add_u32 s49, s33, 4096\n\t"
            "buffer_load_dwordx4 v[16:19], v245, s[28:31], s49 offen sc1\n\t"
            "s_add_u32 s49, s33, 10240\n\t"
            "buffer_load_dwordx4 v[40:43], v245, s[28:31], s49 offen sc1\n\t"
            "s_add_u32 s49, s33, 16384\n\t"
            "buffer_load_dwordx4 v[64:67], v245, s[28:31], s49 offen sc1\n\t"
            "s_add_u32 s49, s33, 22528\n\t"
            "buffer_load_dwordx4 v[88:91], v245, s[28:31], s49 offen sc1\n\t"
            "s_add_u32 s49, s33, 28672\n\t"
            "buffer_load_dwordx4 v[112:115], v245, s[28:31], s49 offen sc1\n\t"
            "s_add_u32 s49, s33, 34816\n\t"
            "buffer_load_dwordx4 v[136:139], v245, s[28:31], s49 offen sc1\n\t"
            "s_add_u32 s49, s33, 5120\n\t"
            "buffer_load_dwordx4 v[20:23], v245, s[28:31], s49 offen sc1\n\t"
            "s_add_u32 s49, s33, 11264\n\t"
            "buffer_load_dwordx4 v[44:47], v245, s[28:31], s49 offen sc1\n\t"
            "s_add_u32 s49, s33, 17408\n\t"
            "buffer_load_dwordx4 v[68:71], v245, s[28:31], s49 offen sc1\n\t"
            "s_add_u32 s49, s33, 23552\n\t"
            "buffer_load_dwordx4 v[92:95], v245, s[28:31], s49 offen sc1\n\t"
            "s_add_u32 s49, s33, 29696\n\t"
            "buffer_load_dwordx4 v[116:119], v245, s[28:31], s49 offen sc1\n\t"
            "s_add_u32 s49, s33, 35840\n\t"
            "buffer_load_dwordx4 v[140:143], v245, s[28:31], s49 offen sc1\n\t"
            "ds_read_b32 v249, v252 offset:12\n\t"
            "s_waitcnt lgkmcnt(0)\n\t"
            "v_and_b32 v249, s69, v249\n\t"
            "s_mov_b32 exec_hi, 0x3fffff\n\t"
            "s_add_u32 m0, s66, 0\n\t"
            "s_add_u32 s49, s79, 0\n\t"
            "buffer_load_dwordx4 v249, s[72:75], s49 offen lds\n\t"
            "s_mov_b32 exec_hi, -1\n\t"
            "s_mov_b32 exec_hi, 0x3fffff\n\t"
            "s_add_u32 m0, s66, 872\n\t"
            "s_add_u32 s49, s47, 196224\n\t"
            "buffer_load_dwordx4 v249, s[72:75], s49 offen lds\n\t"
            "s_mov_b32 exec_hi, -1\n\t"
            "s_mov_b32 exec_hi, 0x3fffff\n\t"
            "s_add_u32 m0, s66, 1744\n\t"
            "s_add_u32 s49, s47, 392448\n\t"
            "buffer_load_dwordx4 v249, s[72:75], s49 offen lds\n\t"
            "s_mov_b32 exec_hi, -1\n\t"
            "s_mov_b32 exec_hi, 0x3fffff\n\t"
            "s_add_u32 m0, s66, 2616\n\t"
            "s_add_u32 s49, s47, 588672\n\t"
            "buffer_load_dwordx4 v249, s[72:75], s49 offen lds\n\t"
            "s_mov_b32 exec_hi, -1\n\t"
            "s_mov_b32 exec_hi, 0x3fffff\n\t"
            "s_add_u32 m0, s66, 3488\n\t"
            "s_add_u32 s49, s47, 784896\n\t"
            "buffer_load_dwordx4 v249, s[72:75], s49 offen lds\n\t"
            "s_mov_b32 exec_hi, -1\n\t"
            "s_mov_b32 exec_hi, 0x3fffff\n\t"
            "s_add_u32 m0, s66, 4360\n\t"
            "s_add_u32 s49, s47, 981120\n\t"
            "buffer_load_dwordx4 v249, s[72:75], s49 offen lds\n\t"
            "s_mov_b32 exec_hi, -1\n\t"
            "s_mov_b32 exec_hi, 0x3fffff\n\t"
            "s_add_u32 m0, s66, 5232\n\t"
            "s_add_u32 s49, s47, 1177344\n\t"
            "buffer_load_dwordx4 v249, s[72:75], s49 offen lds\n\t"
            "s_mov_b32 exec_hi, -1\n\t"
            "s_mov_b32 exec_hi, 0x3fffff\n\t"
            "s_add_u32 m0, s66, 6104\n\t"
            "s_add_u32 s49, s47, 1373568\n\t"
            "buffer_load_dwordx4 v249, s[72:75], s49 offen lds\n\t"
            "s_mov_b32 exec_hi, -1\n\t"
            "s_mov_b32 exec_hi, 0x3fffff\n\t"
            "s_add_u32 m0, s66, 6976\n\t"
            "s_add_u32 s49, s47, 1569792\n\t"
            "buffer_load_dwordx4 v249, s[72:75], s49 offen lds\n\t"
            "s_mov_b32 exec_hi, -1\n\t"
            "ds_read_b128 v[168:171], v254\n\t"
            "ds_read_b128 v[172:175], v254 offset:64\n\t"
            "v_add_u32 v180, s48, v245\n\t"
            "v_add_u32 v232, s48, v242\n\t"
            "v_xor_b32 v233, 64, v232\n\t"
            "v_xor_b32 v246, 0x100, v246\n\t"
            "ds_read_b128 v[176:179], v180 offset:18816\n\t"
            "ds_read_b128 v[180:183], v180 offset:19840\n\t"
            "ds_read_b128 v[192:195], v232\n\t"
            "ds_read_b128 v[196:199], v233\n\t"
            "ds_read_b128 v[224:227], v232 offset:128\n\t"
            "ds_read_b128 v[228:231], v233 offset:128\n\t"
            "s_waitcnt lgkmcnt(5)\n\t"
            "v_add_f32 v176, v176, v178\n\t"
            "s_waitcnt lgkmcnt(4)\n\t"
            "v_add_f32 v180, v180, v182\n\t"
            "v_add_f32 v176, v176, v180\n\t"
            "v_add_f32 v177, v177, v179\n\t"
            "v_add_f32 v181, v181, v183\n\t"
            "v_mul_f32 v176, 0x3baaaaab, v176\n\t"
            "v_add_f32 v177, v177, v181\n\t"
            "v_mul_f32_e64 v176, v176, -v176\n\t"
            "v_fmac_f32 v176, 0x3baaaaab, v177\n\t"
            "v_max_f32 v176, 0, v176\n\t"
            "v_add_f32 v176, 0x3727c5ac, v176\n\t"
            "v_rsq_f32 v176, v176\n\t"
            "s_nop 0\n\t"
            "ds_write_b32 v246, v176\n\t"
            "s_setprio 3\n\t"
            "s_waitcnt vmcnt(9)\n\t"
            "s_waitcnt lgkmcnt(3)\n\t"
            "v_mfma_f32_16x16x32_bf16 v[200:203], v[192:195], v[0:3], 0\n\t"
            "v_mfma_f32_16x16x32_bf16 v[200:203], v[196:199], v[4:7], v[200:203]\n\t"
            "v_mfma_f32_16x16x32_bf16 v[204:207], v[196:199], v[28:31], 0\n\t"
            "v_mfma_f32_16x16x32_bf16 v[204:207], v[192:195], v[24:27], v[204:207]\n\t"
            "v_mfma_f32_16x16x32_bf16 v[208:211], v[192:195], v[48:51], 0\n\t"
            "v_mfma_f32_16x16x32_bf16 v[208:211], v[196:199], v[52:55], v[208:211]\n\t"
            "v_mfma_f32_16x16x32_bf16 v[212:215], v[196:199], v[76:79], 0\n\t"
            "v_mfma_f32_16x16x32_bf16 v[212:215], v[192:195], v[72:75], v[212:215]\n\t"
            "v_mfma_f32_16x16x32_bf16 v[216:219], v[192:195], v[96:99], 0\n\t"
            "v_mfma_f32_16x16x32_bf16 v[216:219], v[196:199], v[100:103], v[216:219]\n\t"
            "v_mfma_f32_16x16x32_bf16 v[220:223], v[196:199], v[124:127], 0\n\t"
            "v_mfma_f32_16x16x32_bf16 v[220:223], v[192:195], v[120:123], v[220:223]\n\t"
            "s_waitcnt lgkmcnt(1)\n\t"
            "v_mfma_f32_16x16x32_bf16 v[200:203], v[224:227], v[8:11], v[200:203]\n\t"
            "v_mfma_f32_16x16x32_bf16 v[200:203], v[228:231], v[12:15], v[200:203]\n\t"
            "v_mfma_f32_16x16x32_bf16 v[204:207], v[228:231], v[36:39], v[204:207]\n\t"
            "v_mfma_f32_16x16x32_bf16 v[204:207], v[224:227], v[32:35], v[204:207]\n\t"
            "v_mfma_f32_16x16x32_bf16 v[208:211], v[224:227], v[56:59], v[208:211]\n\t"
            "v_mfma_f32_16x16x32_bf16 v[208:211], v[228:231], v[60:63], v[208:211]\n\t"
            "v_mfma_f32_16x16x32_bf16 v[212:215], v[228:231], v[84:87], v[212:215]\n\t"
            "v_mfma_f32_16x16x32_bf16 v[212:215], v[224:227], v[80:83], v[212:215]\n\t"
            "v_mfma_f32_16x16x32_bf16 v[216:219], v[224:227], v[104:107], v[216:219]\n\t"
            "s_waitcnt vmcnt(0)\n\t"
            "ds_read_b64 v[144:145], v253\n\t"
            "v_mfma_f32_16x16x32_bf16 v[216:219], v[228:231], v[108:111], v[216:219]\n\t"
            "ds_read_b64 v[146:147], v253 offset:96\n\t"
            "ds_read_b64 v[148:149], v253 offset:1744\n\t"
            "v_mfma_f32_16x16x32_bf16 v[220:223], v[228:231], v[132:135], v[220:223]\n\t"
            "ds_read_b128 v[228:231], v233 offset:256\n\t"
            "ds_read_b64 v[150:151], v253 offset:1840\n\t"
            "v_mfma_f32_16x16x32_bf16 v[220:223], v[224:227], v[128:131], v[220:223]\n\t"
            "ds_read_b128 v[224:227], v232 offset:256\n\t"
            "ds_read_b64 v[152:153], v253 offset:8\n\t"
            "ds_read_b64 v[154:155], v253 offset:104\n\t"
            "ds_read_b64 v[156:157], v253 offset:1752\n\t"
            "ds_read_b64 v[158:159], v253 offset:1848\n\t"
            "ds_read_b64 v[160:161], v253 offset:16\n\t"
            "ds_read_b64 v[162:163], v253 offset:112\n\t"
            "ds_read_b64 v[164:165], v253 offset:1760\n\t"
            "ds_read_b64 v[166:167], v253 offset:1856\n\t"
            "s_waitcnt lgkmcnt(0)\n\t"
            "s_nop 7\n\t"
            ".p2align 6\n\t"
            "s_nop 0\n\t"
            "s_nop 0\n\t"
            "s_nop 0\n\t"
            "s_nop 0\n\t"
            ".Lwc_loop%=:\n\t"
            "s_setprio 3\n\t"
            "v_mfma_f32_16x16x32_bf16 v[200:203], v[224:227], v[16:19], v[200:203]\n\t"
            "v_mov_b64 v[176:177], 0\n\t"
            "v_mov_b64 v[178:179], 0\n\t"
            "v_mfma_f32_16x16x32_bf16 v[200:203], v[228:231], v[20:23], v[200:203]\n\t"
            "v_mov_b64 v[180:181], 0\n\t"
            "v_mov_b64 v[182:183], 0\n\t"
            "s_waitcnt lgkmcnt(8)\n\t"
            "v_smfmac_f32_16x16x64_bf16 v[176:179], v[168:171], v[144:151], v255\n\t"
            "ds_read_b128 v[168:171], v254 offset:128\n\t"
            "s_waitcnt lgkmcnt(5)\n\t"
            "v_smfmac_f32_16x16x64_bf16 v[180:183], v[172:175], v[152:159], v255\n\t"
            "ds_read_b64 v[144:145], v253 offset:24\n\t"
            "v_mfma_f32_16x16x32_bf16 v[204:207], v[228:231], v[44:47], v[204:207]\n\t"
            "ds_read_b64 v[146:147], v253 offset:120\n\t"
            "ds_read_b64 v[148:149], v253 offset:1768\n\t"
            "v_mfma_f32_16x16x32_bf16 v[204:207], v[224:227], v[40:43], v[204:207]\n\t"
            "ds_read_b64 v[150:151], v253 offset:1864\n\t"
            "ds_read_b128 v[172:175], v254 offset:192\n\t"
            "v_mfma_f32_16x16x32_bf16 v[208:211], v[224:227], v[64:67], v[208:211]\n\t"
            "ds_read_b64 v[152:153], v253 offset:32\n\t"
            "ds_read_b64 v[154:155], v253 offset:128\n\t"
            "v_mfma_f32_16x16x32_bf16 v[208:211], v[228:231], v[68:71], v[208:211]\n\t"
            "ds_read_b64 v[156:157], v253 offset:1776\n\t"
            "ds_read_b64 v[158:159], v253 offset:1872\n\t"
            "v_mfma_f32_16x16x32_bf16 v[212:215], v[228:231], v[92:95], v[212:215]\n\t"
            "v_mov_b32 v249, s76\n\t"
            "v_mov_b64 v[184:185], 0\n\t"
            "v_mfma_f32_16x16x32_bf16 v[212:215], v[224:227], v[88:91], v[212:215]\n\t"
            "ds_read_b32 v249, v249\n\t"
            "v_mov_b64 v[186:187], 0\n\t"
            "v_mfma_f32_16x16x32_bf16 v[216:219], v[224:227], v[112:115], v[216:219]\n\t"
            "v_mov_b64 v[188:189], 0\n\t"
            "v_mov_b64 v[190:191], 0\n\t"
            "v_mfma_f32_16x16x32_bf16 v[216:219], v[228:231], v[116:119], v[216:219]\n\t"
            "v_cvt_pk_bf16_f32 v176, v176, v177\n\t"
            "v_add_u32 v236, s67, v250\n\t"
            "s_waitcnt lgkmcnt(10)\n\t"
            "v_smfmac_f32_16x16x64_bf16 v[184:187], v[168:171], v[160:167], v255\n\t"
            "ds_read_b128 v[168:171], v254 offset:256\n\t"
            "s_waitcnt lgkmcnt(6)\n\t"
            "v_smfmac_f32_16x16x64_bf16 v[188:191], v[172:175], v[144:151], v255\n\t"
            "ds_read_b64 v[160:161], v253 offset:40\n\t"
            "v_mfma_f32_16x16x32_bf16 v[220:223], v[228:231], v[140:143], v[220:223]\n\t"
            "ds_read_b64 v[162:163], v253 offset:136\n\t"
            "ds_read_b64 v[164:165], v253 offset:1784\n\t"
            "v_mfma_f32_16x16x32_bf16 v[220:223], v[224:227], v[136:139], v[220:223]\n\t"
            "s_setprio 1\n\t"
            "ds_read_b64 v[166:167], v253 offset:1880\n\t"
            "ds_read_b128 v[172:175], v254 offset:320\n\t"
            "ds_read_b64 v[144:145], v253 offset:48\n\t"
            "ds_read_b64 v[146:147], v253 offset:144\n\t"
            "ds_read_b64 v[148:149], v253 offset:1792\n\t"
            "ds_read_b64 v[150:151], v253 offset:1888\n\t"
            "s_waitcnt lgkmcnt(14)\n\t"
            "ds_read_b128 v[224:227], v232 offset:6272\n\t"
            "s_waitcnt lgkmcnt(14)\n\t"
            "ds_read_b128 v[228:231], v233 offset:6272\n\t"
            "s_waitcnt lgkmcnt(12)\n\t"
            "v_readfirstlane_b32 s26, v249\n\t"
            "v_cvt_pk_bf16_f32 v177, v178, v179\n\t"
            "v_cvt_pk_bf16_f32 v178, v180, v181\n\t"
            "s_and_b32 s68, s26, 0x7ffffff\n\t"
            "s_add_u32 s68, s68, s78\n\t"
            "s_and_b32 s69, s26, 0x38000000\n\t"
            "s_or_b32 s69, s69, s39\n\t"
            "s_bitcmp1_b32 s26, 27\n\t"
            "s_cselect_b32 s79, 2.0, s68\n\t"
            "s_setprio 3\n\t"
            "v_mfma_f32_4x4x4_16b_bf16 v[196:199], v[176:177], v[176:177], 0\n\t"
            "s_setprio 1\n\t"
            "ds_read_b32 v249, v252 offset:12\n\t"
            "s_and_b32 s57, s56, 3\n\t"
            "s_lshl_b32 s58, s57, 3\n\t"
            "s_lshr_b32 m0, s54, s58\n\t"
            "s_lshl_b32 s58, s57, 4\n\t"
            "s_lshr_b64 s[36:37], s[50:51], s58\n\t"
            "s_and_b32 s57, s36, 0xffff\n\t"
            "s_add_u32 s57, s57, s67\n\t"
            "v_writelane_b32 v236, s57, 63\n\t"
            "v_writelane_b32 v236, s59, m0\n\t"
            "v_cvt_pk_bf16_f32 v179, v182, v183\n\t"
            "v_mov_b64 v[180:181], 0\n\t"
            "v_mov_b64 v[182:183], 0\n\t"
            "s_setprio 3\n\t"
            "v_mfma_f32_16x16x32_bf16 v[192:195], v[238:241], v[176:179], 0\n\t"
            "s_lshr_b64 s[36:37], s[52:53], s58\n\t"
            "s_and_b32 s58, s36, 0xffff\n\t"
            "s_add_u32 s58, s58, s67\n\t"
            "v_mfma_f32_4x4x4_16b_bf16 v[196:199], v[178:179], v[178:179], v[196:199]\n\t"
            "ds_write_b128 v236, v[176:179]\n\t"
            "s_waitcnt lgkmcnt(8)\n\t"
            "v_smfmac_f32_16x16x64_bf16 v[180:183], v[172:175], v[160:167], v255\n\t"
            "s_setprio 1\n\t"
            "s_and_b32 s38, s52, 0xffff\n\t"
            "s_add_u32 s38, s38, s67\n\t"
            "ds_read_b128 v[172:175], v254 offset:448\n\t"
            "ds_read_b64 v[160:161], v253 offset:64\n\t"
            "ds_read_b64 v[162:163], v253 offset:160\n\t"
            "ds_read_b64 v[164:165], v253 offset:1808\n\t"
            "ds_read_b64 v[166:167], v253 offset:1904\n\t"
            "v_mov_b64 v[176:177], 0\n\t"
            "v_mov_b64 v[178:179], 0\n\t"
            "v_cvt_pk_bf16_f32 v184, v184, v185\n\t"
            "v_cvt_pk_bf16_f32 v185, v186, v187\n\t"
            "s_setprio 3\n\t"
            "v_smfmac_f32_16x16x64_bf16 v[176:179], v[168:171], v[152:159], v255\n\t"
            "ds_read_b128 v[168:171], v254 offset:384\n\t"
            "v_mfma_f32_4x4x4_16b_bf16 v[196:199], v[184:185], v[184:185], v[196:199]\n\t"
            "s_setprio 1\n\t"
            "ds_read_b64 v[152:153], v253 offset:56\n\t"
            "s_waitcnt lgkmcnt(14)\n\t"
            "ds_read_b64 v[154:155], v253 offset:152\n\t"
            "s_waitcnt lgkmcnt(14)\n\t"
            "ds_read_b64 v[156:157], v253 offset:1800\n\t"
            "s_waitcnt lgkmcnt(14)\n\t"
            "ds_read_b64 v[158:159], v253 offset:1896\n\t"
            "v_xor_b32 v237, 16, v236\n\t"
            "v_cvt_pk_bf16_f32 v186, v188, v189\n\t"
            "v_cvt_pk_bf16_f32 v187, v190, v191\n\t"
            "v_mov_b64 v[188:189], 0\n\t"
            "v_mov_b64 v[190:191], 0\n\t"
            "s_setprio 3\n\t"
            "v_mfma_f32_16x16x32_bf16 v[192:195], v[238:241], v[184:187], v[192:195]\n\t"
            "s_waitcnt lgkmcnt(14)\n\t"
            "ds_write_b128 v237, v[184:187]\n\t"
            "v_mfma_f32_4x4x4_16b_bf16 v[196:199], v[186:187], v[186:187], v[196:199]\n\t"
            "s_setprio 1\n\t"
            "v_mov_b64 v[184:185], 0\n\t"
            "v_mov_b64 v[186:187], 0\n\t"
            "v_cvt_pk_bf16_f32 v176, v176, v177\n\t"
            "v_cvt_pk_bf16_f32 v177, v178, v179\n\t"
            "v_cvt_pk_bf16_f32 v178, v180, v181\n\t"
            "v_cvt_pk_bf16_f32 v179, v182, v183\n\t"
            "s_setprio 3\n\t"
            "v_mfma_f32_4x4x4_16b_bf16 v[196:199], v[176:177], v[176:177], v[196:199]\n\t"
            "s_waitcnt lgkmcnt(14)\n\t"
            "ds_write_b128 v236, v[176:179] offset:128\n\t"
            "s_waitcnt lgkmcnt(6)\n\t"
            "v_smfmac_f32_16x16x64_bf16 v[184:187], v[168:171], v[144:151], v255\n\t"
            "ds_read_b128 v[168:171], v254 offset:512\n\t"
            "s_waitcnt lgkmcnt(3)\n\t"
            "v_smfmac_f32_16x16x64_bf16 v[188:191], v[172:175], v[152:159], v255\n\t"
            "ds_read_b64 v[144:145], v253 offset:72\n\t"
            "v_mfma_f32_16x16x32_bf16 v[192:195], v[238:241], v[176:179], v[192:195]\n\t"
            "ds_read_b64 v[146:147], v253 offset:168\n\t"
            "ds_read_b64 v[148:149], v253 offset:1816\n\t"
            "v_mfma_f32_4x4x4_16b_bf16 v[196:199], v[178:179], v[178:179], v[196:199]\n\t"
            "s_setprio 1\n\t"
            "ds_read_b64 v[150:151], v253 offset:1912\n\t"
            "ds_read_b128 v[172:175], v254 offset:576\n\t"
            "ds_read_b64 v[152:153], v253 offset:80\n\t"
            "ds_read_b64 v[154:155], v253 offset:176\n\t"
            "ds_read_b64 v[156:157], v253 offset:1824\n\t"
            "ds_read_b64 v[158:159], v253 offset:1920\n\t"
            "v_mov_b64 v[176:177], 0\n\t"
            "v_mov_b64 v[178:179], 0\n\t"
            "v_mov_b64 v[180:181], 0\n\t"
            "v_mov_b64 v[182:183], 0\n\t"
            "v_cvt_pk_bf16_f32 v184, v184, v185\n\t"
            "v_cvt_pk_bf16_f32 v185, v186, v187\n\t"
            "v_cvt_pk_bf16_f32 v186, v188, v189\n\t"
            "v_cvt_pk_bf16_f32 v187, v190, v191\n\t"
            "s_setprio 3\n\t"
            "v_mfma_f32_4x4x4_16b_bf16 v[196:199], v[184:185], v[184:185], v[196:199]\n\t"
            "ds_write_b128 v237, v[184:187] offset:128\n\t"
            "s_waitcnt lgkmcnt(10)\n\t"
            "v_smfmac_f32_16x16x64_bf16 v[176:179], v[168:171], v[160:167], v255\n\t"
            "ds_read_b128 v[168:171], v254 offset:640\n\t"
            "v_mfma_f32_16x16x32_bf16 v[192:195], v[238:241], v[184:187], v[192:195]\n\t"
            "ds_read_b64 v[160:161], v253 offset:88\n\t"
            "ds_read_b64 v[162:163], v253 offset:184\n\t"
            "s_waitcnt lgkmcnt(8)\n\t"
            "v_smfmac_f32_16x16x64_bf16 v[180:183], v[172:175], v[144:151], v255\n\t"
            "ds_read_b64 v[164:165], v253 offset:1832\n\t"
            "v_mfma_f32_4x4x4_16b_bf16 v[196:199], v[186:187], v[186:187], v[196:199]\n\t"
            "s_setprio 1\n\t"
            "ds_read_b64 v[166:167], v253 offset:1928\n\t"
            "ds_read_b128 v[172:175], v254 offset:704\n\t"
            "v_mov_b64 v[184:185], 0\n\t"
            "v_mov_b64 v[186:187], 0\n\t"
            "v_mov_b64 v[188:189], 0\n\t"
            "v_mov_b64 v[190:191], 0\n\t"
            "v_add_u32 v248, s67, v251\n\t"
            "v_writelane_b32 v248, s58, 63\n\t"
            "v_writelane_b32 v248, s38, m0\n\t"
            "v_and_b32 v249, s69, v249\n\t"
            "v_cvt_pk_bf16_f32 v176, v176, v177\n\t"
            "v_cvt_pk_bf16_f32 v177, v178, v179\n\t"
            "v_cvt_pk_bf16_f32 v178, v180, v181\n\t"
            "v_cvt_pk_bf16_f32 v179, v182, v183\n\t"
            "s_setprio 3\n\t"
            "v_mfma_f32_4x4x4_16b_bf16 v[196:199], v[176:177], v[176:177], v[196:199]\n\t"
            "ds_write_b128 v236, v[176:179] offset:256\n\t"
            "s_waitcnt lgkmcnt(6)\n\t"
            "v_smfmac_f32_16x16x64_bf16 v[184:187], v[168:171], v[152:159], v255\n\t"
            "v_mov_b32 v234, 0\n\t"
            "v_mfma_f32_4x4x4_16b_bf16 v[196:199], v[178:179], v[178:179], v[196:199]\n\t"
            "v_xor_b32 v246, 0x100, v246\n\t"
            "v_mfma_f32_16x16x32_bf16 v[192:195], v[238:241], v[176:179], v[192:195]\n\t"
            "s_waitcnt lgkmcnt(1)\n\t"
            "v_smfmac_f32_16x16x64_bf16 v[188:191], v[172:175], v[160:167], v255\n\t"
            "s_mov_b32 exec_hi, 0x3fffff\n\t"
            "s_add_u32 m0, s66, 0\n\t"
            "s_add_u32 s49, s79, 0\n\t"
            "buffer_load_dwordx4 v249, s[72:75], s49 offen lds\n\t"
            "s_add_u32 s49, s68, 195352\n\t"
            "buffer_load_dwordx4 v249, s[72:75], s49 offen offset:872 lds\n\t"
            "s_add_u32 s49, s68, 390704\n\t"
            "buffer_load_dwordx4 v249, s[72:75], s49 offen offset:1744 lds\n\t"
            "s_add_u32 s49, s68, 586056\n\t"
            "buffer_load_dwordx4 v249, s[72:75], s49 offen offset:2616 lds\n\t"
            "s_add_u32 s49, s68, 781408\n\t"
            "buffer_load_dwordx4 v249, s[72:75], s49 offen offset:3488 lds\n\t"
            "s_mov_b32 exec_hi, -1\n\t"
            "v_cvt_pk_bf16_f32 v184, v184, v185\n\t"
            "v_cvt_pk_bf16_f32 v185, v186, v187\n\t"
            "v_cvt_pk_bf16_f32 v186, v188, v189\n\t"
            "v_cvt_pk_bf16_f32 v187, v190, v191\n\t"
            "v_mfma_f32_4x4x4_16b_bf16 v[196:199], v[184:185], v[184:185], v[196:199]\n\t"
            "s_mov_b32 exec_hi, 0x3fffff\n\t"
            "s_add_u32 m0, s66, 4360\n\t"
            "s_add_u32 s49, s68, 981120\n\t"
            "buffer_load_dwordx4 v249, s[72:75], s49 offen lds\n\t"
            "s_add_u32 s49, s68, 1176472\n\t"
            "buffer_load_dwordx4 v249, s[72:75], s49 offen offset:872 lds\n\t"
            "s_add_u32 s49, s68, 1371824\n\t"
            "buffer_load_dwordx4 v249, s[72:75], s49 offen offset:1744 lds\n\t"
            "s_add_u32 s49, s68, 1567176\n\t"
            "buffer_load_dwordx4 v249, s[72:75], s49 offen offset:2616 lds\n\t"
            "s_mov_b32 exec_hi, -1\n\t"
            "v_mfma_f32_4x4x4_16b_bf16 v[196:199], v[186:187], v[186:187], v[196:199]\n\t"
            "ds_write_b128 v237, v[184:187] offset:256\n\t"
            "v_mfma_f32_16x16x32_bf16 v[192:195], v[238:241], v[184:187], v[192:195]\n\t"
            "s_setprio 1\n\t"
            "ds_read_b64 v[236:237], v247\n\t"
            "s_nop 5\n\t"
            "v_cndmask_b32_e64 v193, v196, v197, s[60:61]\n\t"
            "v_cndmask_b32_e64 v193, v193, v198, s[62:63]\n\t"
            "v_cndmask_b32_e64 v193, v193, v199, s[64:65]\n\t"
            "ds_read_b128 v[196:199], v233 offset:6144\n\t"
            "ds_write_b64 v248, v[192:193] offset:18816\n\t"
            "ds_read_b64 v[248:249], v247 offset:8\n\t"
            "ds_read_b128 v[192:195], v232 offset:6144\n\t"
            "s_mov_b64 exec, 1\n\t"
            "ds_inc_u32 v255, v238 offset:7860\n\t"
            "s_mov_b64 exec, -1\n\t"
            "s_waitcnt lgkmcnt(5)\n\t"
            "v_mul_f32 v200, v236, v200\n\t"
            "v_mul_f32 v201, v237, v201\n\t"
            "v_mul_f32 v204, v236, v204\n\t"
            "v_mul_f32 v205, v237, v205\n\t"
            "v_mul_f32 v208, v236, v208\n\t"
            "v_mul_f32 v209, v237, v209\n\t"
            "v_mul_f32 v212, v236, v212\n\t"
            "v_mul_f32 v213, v237, v213\n\t"
            "v_mul_f32 v216, v236, v216\n\t"
            "v_mul_f32 v217, v237, v217\n\t"
            "v_mul_f32 v220, v236, v220\n\t"
            "v_mul_f32 v221, v237, v221\n\t"
            "v_cvt_pk_bf16_f32 v212, v212, v213\n\t"
            "v_cvt_pk_bf16_f32 v208, v208, v209\n\t"
            "v_cvt_pk_bf16_f32 v200, v200, v201\n\t"
            "v_cvt_pk_bf16_f32 v204, v204, v205\n\t"
            "v_cvt_pk_bf16_f32 v216, v216, v217\n\t"
            "v_cvt_pk_bf16_f32 v220, v220, v221\n\t"
            "ds_read_b64 v[236:237], v247 offset:64\n\t"
            "s_waitcnt lgkmcnt(2)\n\t"
            "v_mfma_f32_16x16x32_bf16 v[144:147], v[192:195], v[0:3], 0\n\t"
            "v_mul_f32 v202, v248, v202\n\t"
            "v_mul_f32 v203, v249, v203\n\t"
            "v_mfma_f32_16x16x32_bf16 v[144:147], v[196:199], v[4:7], v[144:147]\n\t"
            "v_mul_f32 v206, v248, v206\n\t"
            "v_mul_f32 v207, v249, v207\n\t"
            "v_mfma_f32_16x16x32_bf16 v[148:151], v[196:199], v[28:31], 0\n\t"
            "v_mul_f32 v210, v248, v210\n\t"
            "v_mul_f32 v211, v249, v211\n\t"
            "v_mfma_f32_16x16x32_bf16 v[148:151], v[192:195], v[24:27], v[148:151]\n\t"
            "v_mul_f32 v214, v248, v214\n\t"
            "v_mul_f32 v215, v249, v215\n\t"
            "v_mfma_f32_16x16x32_bf16 v[168:171], v[192:195], v[48:51], 0\n\t"
            "v_mul_f32 v218, v248, v218\n\t"
            "v_mul_f32 v219, v249, v219\n\t"
            "v_mfma_f32_16x16x32_bf16 v[168:171], v[196:199], v[52:55], v[168:171]\n\t"
            "v_mul_f32 v222, v248, v222\n\t"
            "v_mul_f32 v223, v249, v223\n\t"
            "v_mfma_f32_16x16x32_bf16 v[172:175], v[196:199], v[76:79], 0\n\t"
            "ds_read_b64 v[248:249], v247 offset:72\n\t"
            "v_cvt_pk_bf16_f32 v213, v214, v215\n\t"
            "v_mfma_f32_16x16x32_bf16 v[172:175], v[192:195], v[72:75], v[172:175]\n\t"
            "v_cvt_pk_bf16_f32 v209, v210, v211\n\t"
            "v_cvt_pk_bf16_f32 v201, v202, v203\n\t"
            "v_mfma_f32_16x16x32_bf16 v[152:155], v[192:195], v[96:99], 0\n\t"
            "v_cvt_pk_bf16_f32 v205, v206, v207\n\t"
            "v_cvt_pk_bf16_f32 v217, v218, v219\n\t"
            "v_mfma_f32_16x16x32_bf16 v[152:155], v[196:199], v[100:103], v[152:155]\n\t"
            "v_cvt_pk_bf16_f32 v221, v222, v223\n\t"
            "v_mfma_f32_16x16x32_bf16 v[156:159], v[196:199], v[124:127], 0\n\t"
            "ds_read_b128 v[196:199], v233 offset:6400\n\t"
            "v_mfma_f32_16x16x32_bf16 v[156:159], v[192:195], v[120:123], v[156:159]\n\t"
            "ds_read_b128 v[192:195], v232 offset:6400\n\t"
            "v_mfma_f32_16x16x32_bf16 v[144:147], v[224:227], v[8:11], v[144:147]\n\t"
            "v_mfma_f32_16x16x32_bf16 v[144:147], v[228:231], v[12:15], v[144:147]\n\t"
            "v_mfma_f32_16x16x32_bf16 v[148:151], v[228:231], v[36:39], v[148:151]\n\t"
            "v_mfma_f32_16x16x32_bf16 v[148:151], v[224:227], v[32:35], v[148:151]\n\t"
            "v_mfma_f32_16x16x32_bf16 v[168:171], v[224:227], v[56:59], v[168:171]\n\t"
            "v_mfma_f32_16x16x32_bf16 v[168:171], v[228:231], v[60:63], v[168:171]\n\t"
            "v_mfma_f32_16x16x32_bf16 v[172:175], v[228:231], v[84:87], v[172:175]\n\t"
            "v_mfma_f32_16x16x32_bf16 v[172:175], v[224:227], v[80:83], v[172:175]\n\t"
            "v_mfma_f32_16x16x32_bf16 v[152:155], v[224:227], v[104:107], v[152:155]\n\t"
            "v_mfma_f32_16x16x32_bf16 v[152:155], v[228:231], v[108:111], v[152:155]\n\t"
            "v_mfma_f32_16x16x32_bf16 v[156:159], v[228:231], v[132:135], v[156:159]\n\t"
            "ds_read_b128 v[228:231], v233 offset:12288\n\t"
            "v_mfma_f32_16x16x32_bf16 v[156:159], v[224:227], v[128:131], v[156:159]\n\t"
            "ds_read_b128 v[224:227], v232 offset:12288\n\t"
            "s_waitcnt lgkmcnt(2)\n\t"
            "v_mfma_f32_16x16x32_bf16 v[144:147], v[192:195], v[16:19], v[144:147]\n\t"
            "v_mfma_f32_16x16x32_bf16 v[144:147], v[196:199], v[20:23], v[144:147]\n\t"
            "v_mfma_f32_16x16x32_bf16 v[148:151], v[196:199], v[44:47], v[148:151]\n\t"
            "v_mfma_f32_16x16x32_bf16 v[148:151], v[192:195], v[40:43], v[148:151]\n\t"
            "ds_read_b64 v[234:235], v234 offset:7856\n\t"
            "v_mfma_f32_16x16x32_bf16 v[168:171], v[192:195], v[64:67], v[168:171]\n\t"
            "v_mfma_f32_16x16x32_bf16 v[168:171], v[196:199], v[68:71], v[168:171]\n\t"
            "v_mfma_f32_16x16x32_bf16 v[172:175], v[196:199], v[92:95], v[172:175]\n\t"
            "s_nop 1\n\t"
            "v_mul_f32 v144, v236, v144\n\t"
            "v_mfma_f32_16x16x32_bf16 v[172:175], v[192:195], v[88:91], v[172:175]\n\t"
            "v_mul_f32 v145, v237, v145\n\t"
            "v_mul_f32 v146, v248, v146\n\t"
            "v_mfma_f32_16x16x32_bf16 v[152:155], v[192:195], v[112:115], v[152:155]\n\t"
            "v_mul_f32 v148, v236, v148\n\t"
            "v_mul_f32 v149, v237, v149\n\t"
            "v_mfma_f32_16x16x32_bf16 v[152:155], v[196:199], v[116:119], v[152:155]\n\t"
            "v_mul_f32 v168, v236, v168\n\t"
            "v_mul_f32 v169, v237, v169\n\t"
            "v_mfma_f32_16x16x32_bf16 v[156:159], v[196:199], v[140:143], v[156:159]\n\t"
            "ds_read_b128 v[196:199], v233 offset:12416\n\t"
            "v_mul_f32 v172, v236, v172\n\t"
            "v_mfma_f32_16x16x32_bf16 v[156:159], v[192:195], v[136:139], v[156:159]\n\t"
            "ds_read_b128 v[192:195], v232 offset:12416\n\t"
            "s_waitcnt lgkmcnt(2)\n\t"
            "v_readfirstlane_b32 s77, v234\n\t"
            "v_readfirstlane_b32 s38, v235\n\t"
            "s_add_u32 s77, s77, s38\n\t"
            "s_cmp_ge_u32 s77, s44\n\t"
            "s_cbranch_scc0 .Lwc_spin%=\n\t"
            ".Lwc_go%=:\n\t"
            "v_mfma_f32_16x16x32_bf16 v[160:163], v[224:227], v[0:3], 0\n\t"
            "s_add_u32 s59, s59, s84\n\t"
            "s_movk_i32 s84, 800\n\t"
            "v_mul_f32 v173, v237, v173\n\t"
            "v_mfma_f32_16x16x32_bf16 v[160:163], v[228:231], v[4:7], v[160:163]\n\t"
            "s_add_u32 s45, s46, s27\n\t"
            "s_mov_b32 s46, s47\n\t"
            "s_mov_b32 s47, s68\n\t"
            "v_mfma_f32_16x16x32_bf16 v[164:167], v[228:231], v[28:31], 0\n\t"
            "v_mul_f32 v170, v248, v170\n\t"
            "v_mul_f32 v171, v249, v171\n\t"
            "v_mfma_f32_16x16x32_bf16 v[164:167], v[224:227], v[24:27], v[164:167]\n\t"
            "v_cvt_pk_bf16_f32 v214, v172, v173\n\t"
            "v_cvt_pk_bf16_f32 v210, v168, v169\n\t"
            "v_mfma_f32_16x16x32_bf16 v[176:179], v[224:227], v[48:51], 0\n\t"
            "v_cvt_pk_bf16_f32 v211, v170, v171\n\t"
            "v_mul_f32 v174, v248, v174\n\t"
            "v_mfma_f32_16x16x32_bf16 v[176:179], v[228:231], v[52:55], v[176:179]\n\t"
            "v_mul_f32 v175, v249, v175\n\t"
            "v_add_u32 v172, s67, v245\n\t"
            "v_mfma_f32_16x16x32_bf16 v[180:183], v[228:231], v[76:79], 0\n\t"
            "ds_read_b128 v[168:171], v172 offset:18816\n\t"
            "v_cvt_pk_bf16_f32 v215, v174, v175\n\t"
            "v_mfma_f32_16x16x32_bf16 v[180:183], v[224:227], v[72:75], v[180:183]\n\t"
            "ds_read_b128 v[172:175], v172 offset:19840\n\t"
            "v_mul_f32 v152, v236, v152\n\t"
            "v_mfma_f32_16x16x32_bf16 v[184:187], v[224:227], v[96:99], 0\n\t"
            "v_mul_f32 v153, v237, v153\n\t"
            "v_mul_f32 v156, v236, v156\n\t"
            "v_mfma_f32_16x16x32_bf16 v[184:187], v[228:231], v[100:103], v[184:187]\n\t"
            "v_mul_f32 v157, v237, v157\n\t"
            "v_mul_f32 v147, v249, v147\n\t"
            "v_mfma_f32_16x16x32_bf16 v[188:191], v[228:231], v[124:127], 0\n\t"
            "ds_read_b128 v[228:231], v233 offset:12544\n\t"
            "ds_read_b64 v[236:237], v247 offset:128\n\t"
            "v_mfma_f32_16x16x32_bf16 v[188:191], v[224:227], v[120:123], v[188:191]\n\t"
            "ds_read_b128 v[224:227], v232 offset:12544\n\t"
            "v_mul_f32 v150, v248, v150\n\t"
            "s_waitcnt lgkmcnt(5)\n\t"
            "v_mfma_f32_16x16x32_bf16 v[160:163], v[192:195], v[8:11], v[160:163]\n\t"
            "v_mul_f32 v151, v249, v151\n\t"
            "v_mul_f32 v154, v248, v154\n\t"
            "v_mfma_f32_16x16x32_bf16 v[160:163], v[196:199], v[12:15], v[160:163]\n\t"
            "v_mul_f32 v155, v249, v155\n\t"
            "v_mul_f32 v158, v248, v158\n\t"
            "v_mfma_f32_16x16x32_bf16 v[164:167], v[196:199], v[36:39], v[164:167]\n\t"
            "v_mul_f32 v159, v249, v159\n\t"
            "s_waitcnt lgkmcnt(4)\n\t"
            "v_add_f32 v168, v168, v170\n\t"
            "v_mfma_f32_16x16x32_bf16 v[164:167], v[192:195], v[32:35], v[164:167]\n\t"
            "ds_read_b64 v[248:249], v247 offset:136\n\t"
            "s_waitcnt lgkmcnt(4)\n\t"
            "v_add_f32 v172, v172, v174\n\t"
            "v_mfma_f32_16x16x32_bf16 v[176:179], v[192:195], v[56:59], v[176:179]\n\t"
            "v_add_f32 v168, v168, v172\n\t"
            "v_add_f32 v169, v169, v171\n\t"
            "v_mfma_f32_16x16x32_bf16 v[176:179], v[196:199], v[60:63], v[176:179]\n\t"
            "v_add_f32 v173, v173, v175\n\t"
            "v_mul_f32 v168, 0x3baaaaab, v168\n\t"
            "v_mfma_f32_16x16x32_bf16 v[180:183], v[196:199], v[84:87], v[180:183]\n\t"
            "v_add_f32 v169, v169, v173\n\t"
            "v_mul_f32_e64 v168, v168, -v168\n\t"
            "v_mfma_f32_16x16x32_bf16 v[180:183], v[192:195], v[80:83], v[180:183]\n\t"
            "v_fmac_f32 v168, 0x3baaaaab, v169\n\t"
            "v_max_f32 v168, 0, v168\n\t"
            "v_mfma_f32_16x16x32_bf16 v[184:187], v[192:195], v[104:107], v[184:187]\n\t"
            "v_cvt_pk_bf16_f32 v202, v144, v145\n\t"
            "v_cvt_pk_bf16_f32 v203, v146, v147\n\t"
            "v_mfma_f32_16x16x32_bf16 v[184:187], v[196:199], v[108:111], v[184:187]\n\t"
            "v_cvt_pk_bf16_f32 v206, v148, v149\n\t"
            "v_cvt_pk_bf16_f32 v207, v150, v151\n\t"
            "v_mfma_f32_16x16x32_bf16 v[188:191], v[196:199], v[132:135], v[188:191]\n\t"
            "v_cvt_pk_bf16_f32 v218, v152, v153\n\t"
            "v_cvt_pk_bf16_f32 v219, v154, v155\n\t"
            "v_mfma_f32_16x16x32_bf16 v[188:191], v[192:195], v[128:131], v[188:191]\n\t"
            "v_cvt_pk_bf16_f32 v222, v156, v157\n\t"
            "v_cvt_pk_bf16_f32 v223, v158, v159\n\t"
            "s_waitcnt lgkmcnt(1)\n\t"
            "v_mfma_f32_16x16x32_bf16 v[160:163], v[224:227], v[16:19], v[160:163]\n\t"
            "v_add_f32 v168, 0x3727c5ac, v168\n\t"
            "v_rsq_f32 v168, v168\n\t"
            "v_mfma_f32_16x16x32_bf16 v[160:163], v[228:231], v[20:23], v[160:163]\n\t"
            "ds_write_b128 v243, v[200:203]\n\t"
            "v_mfma_f32_16x16x32_bf16 v[164:167], v[228:231], v[44:47], v[164:167]\n\t"
            "v_add_u32 v232, s67, v242\n\t"
            "v_xor_b32 v233, 64, v232\n\t"
            "v_mfma_f32_16x16x32_bf16 v[164:167], v[224:227], v[40:43], v[164:167]\n\t"
            "s_xor_b32 s48, s48, s71\n\t"
            "s_xor_b32 s67, s67, s71\n\t"
            "s_add_u32 s76, s76, 4\n\t"
            "s_add_u32 s44, s44, 8\n\t"
            "v_mfma_f32_16x16x32_bf16 v[176:179], v[224:227], v[64:67], v[176:179]\n\t"
            "s_lshl_b32 s56, s56, 2\n\t"
            "s_lshr_b32 s49, s26, 30\n\t"
            "s_or_b32 s56, s56, s49\n\t"
            "v_mfma_f32_16x16x32_bf16 v[176:179], v[228:231], v[68:71], v[176:179]\n\t"
            "ds_read_b128 v[192:195], v232\n\t"
            "ds_read_b128 v[196:199], v233\n\t"
            "v_mfma_f32_16x16x32_bf16 v[180:183], v[228:231], v[92:95], v[180:183]\n\t"
            "v_mul_f32 v160, v236, v160\n\t"
            "v_mul_f32 v161, v237, v161\n\t"
            "v_mfma_f32_16x16x32_bf16 v[180:183], v[224:227], v[88:91], v[180:183]\n\t"
            "v_mul_f32 v164, v236, v164\n\t"
            "v_mul_f32 v165, v237, v165\n\t"
            "v_mfma_f32_16x16x32_bf16 v[184:187], v[224:227], v[112:115], v[184:187]\n\t"
            "v_mul_f32 v176, v236, v176\n\t"
            "v_mul_f32 v177, v237, v177\n\t"
            "v_mfma_f32_16x16x32_bf16 v[184:187], v[228:231], v[116:119], v[184:187]\n\t"
            "ds_write_b128 v243, v[204:207] offset:1536\n\t"
            "v_mfma_f32_16x16x32_bf16 v[188:191], v[228:231], v[140:143], v[188:191]\n\t"
            "ds_read_b128 v[228:231], v233 offset:128\n\t"
            "v_mul_f32 v180, v236, v180\n\t"
            "v_mfma_f32_16x16x32_bf16 v[188:191], v[224:227], v[136:139], v[188:191]\n\t"
            "ds_read_b128 v[224:227], v232 offset:128\n\t"
            "v_mul_f32 v181, v237, v181\n\t"
            "s_waitcnt lgkmcnt(3)\n\t"
            "v_mfma_f32_16x16x32_bf16 v[200:203], v[192:195], v[0:3], 0\n\t"
            "v_mul_f32 v184, v236, v184\n\t"
            "v_mul_f32 v185, v237, v185\n\t"
            "v_mfma_f32_16x16x32_bf16 v[200:203], v[196:199], v[4:7], v[200:203]\n\t"
            "ds_write_b128 v243, v[208:211] offset:3072\n\t"
            "v_mfma_f32_16x16x32_bf16 v[204:207], v[196:199], v[28:31], 0\n\t"
            "v_mul_f32 v188, v236, v188\n\t"
            "v_mul_f32 v189, v237, v189\n\t"
            "v_mfma_f32_16x16x32_bf16 v[204:207], v[192:195], v[24:27], v[204:207]\n\t"
            "ds_write_b128 v243, v[212:215] offset:4608\n\t"
            "v_mfma_f32_16x16x32_bf16 v[208:211], v[192:195], v[48:51], 0\n\t"
            "v_mul_f32 v162, v248, v162\n\t"
            "v_mul_f32 v163, v249, v163\n\t"
            "v_mfma_f32_16x16x32_bf16 v[208:211], v[196:199], v[52:55], v[208:211]\n\t"
            "ds_write_b128 v243, v[216:219] offset:6144\n\t"
            "v_mfma_f32_16x16x32_bf16 v[212:215], v[196:199], v[76:79], 0\n\t"
            "v_cvt_pk_bf16_f32 v160, v160, v161\n\t"
            "v_mul_f32 v166, v248, v166\n\t"
            "v_mfma_f32_16x16x32_bf16 v[212:215], v[192:195], v[72:75], v[212:215]\n\t"
            "ds_write_b128 v243, v[220:223] offset:7680\n\t"
            "v_mfma_f32_16x16x32_bf16 v[216:219], v[192:195], v[96:99], 0\n\t"
            "v_mul_f32 v167, v249, v167\n\t"
            "v_cvt_pk_bf16_f32 v164, v164, v165\n\t"
            "v_mfma_f32_16x16x32_bf16 v[216:219], v[196:199], v[100:103], v[216:219]\n\t"
            "v_mul_f32 v178, v248, v178\n\t"
            "v_mul_f32 v179, v249, v179\n\t"
            "v_mfma_f32_16x16x32_bf16 v[220:223], v[196:199], v[124:127], 0\n\t"
            "v_cvt_pk_bf16_f32 v176, v176, v177\n\t"
            "v_mul_f32 v182, v248, v182\n\t"
            "v_mfma_f32_16x16x32_bf16 v[220:223], v[192:195], v[120:123], v[220:223]\n\t"
            "v_mul_f32 v183, v249, v183\n\t"
            "v_cvt_pk_bf16_f32 v180, v180, v181\n\t"
            "s_waitcnt lgkmcnt(4)\n\t"
            "v_mfma_f32_16x16x32_bf16 v[200:203], v[224:227], v[8:11], v[200:203]\n\t"
            "s_waitcnt vmcnt(0)\n\t"
            "v_mul_f32 v186, v248, v186\n\t"
            "v_mul_f32 v187, v249, v187\n\t"
            "v_mfma_f32_16x16x32_bf16 v[200:203], v[228:231], v[12:15], v[200:203]\n\t"
            "v_cvt_pk_bf16_f32 v184, v184, v185\n\t"
            "v_mul_f32 v190, v248, v190\n\t"
            "v_mfma_f32_16x16x32_bf16 v[204:207], v[228:231], v[36:39], v[204:207]\n\t"
            "v_mul_f32 v191, v249, v191\n\t"
            "v_cvt_pk_bf16_f32 v188, v188, v189\n\t"
            "v_mfma_f32_16x16x32_bf16 v[204:207], v[224:227], v[32:35], v[204:207]\n\t"
            "v_cvt_pk_bf16_f32 v161, v162, v163\n\t"
            "v_cvt_pk_bf16_f32 v165, v166, v167\n\t"
            "v_mfma_f32_16x16x32_bf16 v[208:211], v[224:227], v[56:59], v[208:211]\n\t"
            "v_cvt_pk_bf16_f32 v177, v178, v179\n\t"
            "v_cvt_pk_bf16_f32 v181, v182, v183\n\t"
            "v_mfma_f32_16x16x32_bf16 v[208:211], v[228:231], v[60:63], v[208:211]\n\t"
            "v_cvt_pk_bf16_f32 v185, v186, v187\n\t"
            "v_cvt_pk_bf16_f32 v189, v190, v191\n\t"
            "v_mfma_f32_16x16x32_bf16 v[212:215], v[228:231], v[84:87], v[212:215]\n\t"
            "v_xor_b32 v247, 0x100, v247\n\t"
            "v_mfma_f32_16x16x32_bf16 v[212:215], v[224:227], v[80:83], v[212:215]\n\t"
            "ds_write_b64 v244, v[160:161]\n\t"
            "v_mfma_f32_16x16x32_bf16 v[216:219], v[224:227], v[104:107], v[216:219]\n\t"
            "ds_read_b128 v[172:175], v254 offset:64\n\t"
            "ds_read_b64 v[144:145], v253\n\t"
            "v_mfma_f32_16x16x32_bf16 v[216:219], v[228:231], v[108:111], v[216:219]\n\t"
            "ds_write_b64 v244, v[164:165] offset:1536\n\t"
            "v_mfma_f32_16x16x32_bf16 v[220:223], v[228:231], v[132:135], v[220:223]\n\t"
            "ds_read_b128 v[228:231], v233 offset:256\n\t"
            "ds_read_b64 v[146:147], v253 offset:96\n\t"
            "s_setprio 3\n\t"
            "v_mfma_f32_16x16x32_bf16 v[220:223], v[224:227], v[128:131], v[220:223]\n\t"
            "ds_read_b128 v[224:227], v232 offset:256\n\t"
            "ds_write_b64 v244, v[176:177] offset:3072\n\t"
            "ds_write_b64 v244, v[180:181] offset:4608\n\t"
            "ds_write_b64 v244, v[184:185] offset:6144\n\t"
            "ds_write_b64 v244, v[188:189] offset:7680\n\t"
            "s_waitcnt lgkmcnt(14)\n\t"
            "ds_write_b32 v246, v168\n\t"
            "s_waitcnt lgkmcnt(14)\n\t"
            "ds_read_b128 v[168:171], v254\n\t"
            "s_waitcnt lgkmcnt(14)\n\t"
            "ds_read_b64 v[148:149], v253 offset:1744\n\t"
            "s_waitcnt lgkmcnt(14)\n\t"
            "ds_read_b64 v[150:151], v253 offset:1840\n\t"
            "s_waitcnt lgkmcnt(14)\n\t"
            "ds_read_b64 v[152:153], v253 offset:8\n\t"
            "s_waitcnt lgkmcnt(14)\n\t"
            "ds_read_b64 v[154:155], v253 offset:104\n\t"
            "s_waitcnt lgkmcnt(14)\n\t"
            "ds_read_b64 v[156:157], v253 offset:1752\n\t"
            "s_waitcnt lgkmcnt(14)\n\t"
            "ds_read_b64 v[158:159], v253 offset:1848\n\t"
            "s_waitcnt lgkmcnt(14)\n\t"
            "ds_read_b64 v[160:161], v253 offset:16\n\t"
            "s_waitcnt lgkmcnt(14)\n\t"
            "ds_read_b64 v[162:163], v253 offset:112\n\t"
            "s_waitcnt lgkmcnt(14)\n\t"
            "ds_read_b64 v[164:165], v253 offset:1760\n\t"
            "s_waitcnt lgkmcnt(14)\n\t"
            "ds_read_b64 v[166:167], v253 offset:1856\n\t"
            "s_waitcnt lgkmcnt(11)\n\t"
            "s_barrier\n\t"
            "s_cmp_lt_u32 s47, 0x7000000\n\t"
            "s_cbranch_scc1 .Lwc_loop%=\n\t"
            "s_branch .Lwc_end%=\n\t"
            ".Lwz_spin%=:\n\t"
            "s_cmp_eq_u32 s83, 0\n\t"
            "s_cbranch_scc1 .Lwz_go%=\n\t"
            "s_sub_u32 s83, s83, 1\n\t"
            "s_sleep 2\n\t"
            "v_mov_b32 v234, 0\n\t"
            "ds_read_b64 v[234:235], v234 offset:7856\n\t"
            "s_waitcnt lgkmcnt(0)\n\t"
            "v_readfirstlane_b32 s77, v234\n\t"
            "v_readfirstlane_b32 s38, v235\n\t"
            "s_add_u32 s77, s77, s38\n\t"
            "s_cmp_ge_u32 s77, s44\n\t"
            "s_cbranch_scc0 .Lwz_spin%=\n\t"
            "s_branch .Lwz_go%=\n\t"
            ".Lwc_spin%=:\n\t"
            "s_cmp_eq_u32 s83, 0\n\t"
            "s_cbranch_scc1 .Lwc_go%=\n\t"
            "s_sub_u32 s83, s83, 1\n\t"
            "s_sleep 2\n\t"
            "v_mov_b32 v234, 0\n\t"
            "ds_read_b64 v[234:235], v234 offset:7856\n\t"
            "s_waitcnt lgkmcnt(0)\n\t"
            "v_readfirstlane_b32 s77, v234\n\t"
            "v_readfirstlane_b32 s38, v235\n\t"
            "s_add_u32 s77, s77, s38\n\t"
            "s_cmp_ge_u32 s77, s44\n\t"
            "s_cbranch_scc0 .Lwc_spin%=\n\t"
            "s_branch .Lwc_go%=\n\t"
            ".Lwc_end%=:\n\t"
            "s_waitcnt lgkmcnt(0)\n\t"
            "s_nop 7\n\t"
            "s_setprio 3\n\t"
            "s_sub_u32 s44, s44, 4\n\t"
            "v_mfma_f32_16x16x32_bf16 v[200:203], v[224:227], v[16:19], v[200:203]\n\t"
            "ds_read_b128 v[192:195], v232 offset:6144\n\t"
            "ds_read_b128 v[196:199], v233 offset:6144\n\t"
            "v_mfma_f32_16x16x32_bf16 v[200:203], v[228:231], v[20:23], v[200:203]\n\t"
            "ds_read_b64 v[236:237], v247\n\t"
            "ds_read_b64 v[248:249], v247 offset:8\n\t"
            "v_mfma_f32_16x16x32_bf16 v[204:207], v[228:231], v[44:47], v[204:207]\n\t"
            "v_mov_b32 v234, 0\n\t"
            "v_mfma_f32_16x16x32_bf16 v[204:207], v[224:227], v[40:43], v[204:207]\n\t"
            "v_mfma_f32_16x16x32_bf16 v[208:211], v[224:227], v[64:67], v[208:211]\n\t"
            "v_mfma_f32_16x16x32_bf16 v[208:211], v[228:231], v[68:71], v[208:211]\n\t"
            "v_mfma_f32_16x16x32_bf16 v[212:215], v[228:231], v[92:95], v[212:215]\n\t"
            "v_mfma_f32_16x16x32_bf16 v[212:215], v[224:227], v[88:91], v[212:215]\n\t"
            "v_mfma_f32_16x16x32_bf16 v[216:219], v[224:227], v[112:115], v[216:219]\n\t"
            "s_waitcnt lgkmcnt(1)\n\t"
            "v_mul_f32 v200, v236, v200\n\t"
            "v_mul_f32 v201, v237, v201\n\t"
            "v_mfma_f32_16x16x32_bf16 v[216:219], v[228:231], v[116:119], v[216:219]\n\t"
            "v_mul_f32 v204, v236, v204\n\t"
            "v_mul_f32 v205, v237, v205\n\t"
            "v_mfma_f32_16x16x32_bf16 v[220:223], v[228:231], v[140:143], v[220:223]\n\t"
            "ds_read_b128 v[228:231], v233 offset:6272\n\t"
            "v_mul_f32 v208, v236, v208\n\t"
            "v_mfma_f32_16x16x32_bf16 v[220:223], v[224:227], v[136:139], v[220:223]\n\t"
            "ds_read_b128 v[224:227], v232 offset:6272\n\t"
            "v_mul_f32 v209, v237, v209\n\t"
            "v_mfma_f32_16x16x32_bf16 v[144:147], v[192:195], v[0:3], 0\n\t"
            "v_mul_f32 v212, v236, v212\n\t"
            "v_mul_f32 v213, v237, v213\n\t"
            "v_mfma_f32_16x16x32_bf16 v[144:147], v[196:199], v[4:7], v[144:147]\n\t"
            "v_mul_f32 v216, v236, v216\n\t"
            "v_mul_f32 v217, v237, v217\n\t"
            "v_mfma_f32_16x16x32_bf16 v[148:151], v[196:199], v[28:31], 0\n\t"
            "v_mul_f32 v220, v236, v220\n\t"
            "v_mul_f32 v221, v237, v221\n\t"
            "v_mfma_f32_16x16x32_bf16 v[148:151], v[192:195], v[24:27], v[148:151]\n\t"
            "ds_read_b64 v[236:237], v247 offset:64\n\t"
            "s_waitcnt lgkmcnt(3)\n\t"
            "v_mul_f32 v202, v248, v202\n\t"
            "v_mfma_f32_16x16x32_bf16 v[168:171], v[192:195], v[48:51], 0\n\t"
            "v_mul_f32 v203, v249, v203\n\t"
            "v_mul_f32 v206, v248, v206\n\t"
            "v_mfma_f32_16x16x32_bf16 v[168:171], v[196:199], v[52:55], v[168:171]\n\t"
            "v_mul_f32 v207, v249, v207\n\t"
            "v_mul_f32 v210, v248, v210\n\t"
            "v_mfma_f32_16x16x32_bf16 v[172:175], v[196:199], v[76:79], 0\n\t"
            "v_mul_f32 v211, v249, v211\n\t"
            "v_mul_f32 v214, v248, v214\n\t"
            "v_mfma_f32_16x16x32_bf16 v[172:175], v[192:195], v[72:75], v[172:175]\n\t"
            "v_mul_f32 v215, v249, v215\n\t"
            "v_mul_f32 v218, v248, v218\n\t"
            "v_mfma_f32_16x16x32_bf16 v[152:155], v[192:195], v[96:99], 0\n\t"
            "v_mul_f32 v219, v249, v219\n\t"
            "v_mul_f32 v222, v248, v222\n\t"
            "v_mfma_f32_16x16x32_bf16 v[152:155], v[196:199], v[100:103], v[152:155]\n\t"
            "v_mul_f32 v223, v249, v223\n\t"
            "v_cvt_pk_bf16_f32 v200, v200, v201\n\t"
            "v_mfma_f32_16x16x32_bf16 v[156:159], v[196:199], v[124:127], 0\n\t"
            "ds_read_b128 v[196:199], v233 offset:6400\n\t"
            "ds_read_b64 v[248:249], v247 offset:72\n\t"
            "v_mfma_f32_16x16x32_bf16 v[156:159], v[192:195], v[120:123], v[156:159]\n\t"
            "ds_read_b128 v[192:195], v232 offset:6400\n\t"
            "v_cvt_pk_bf16_f32 v204, v204, v205\n\t"
            "s_waitcnt lgkmcnt(4)\n\t"
            "v_mfma_f32_16x16x32_bf16 v[144:147], v[224:227], v[8:11], v[144:147]\n\t"
            "v_cvt_pk_bf16_f32 v208, v208, v209\n\t"
            "v_cvt_pk_bf16_f32 v212, v212, v213\n\t"
            "v_mfma_f32_16x16x32_bf16 v[144:147], v[228:231], v[12:15], v[144:147]\n\t"
            "v_cvt_pk_bf16_f32 v216, v216, v217\n\t"
            "v_cvt_pk_bf16_f32 v220, v220, v221\n\t"
            "v_mfma_f32_16x16x32_bf16 v[148:151], v[228:231], v[36:39], v[148:151]\n\t"
            "v_cvt_pk_bf16_f32 v201, v202, v203\n\t"
            "v_cvt_pk_bf16_f32 v205, v206, v207\n\t"
            "v_mfma_f32_16x16x32_bf16 v[148:151], v[224:227], v[32:35], v[148:151]\n\t"
            "v_cvt_pk_bf16_f32 v209, v210, v211\n\t"
            "v_cvt_pk_bf16_f32 v213, v214, v215\n\t"
            "v_mfma_f32_16x16x32_bf16 v[168:171], v[224:227], v[56:59], v[168:171]\n\t"
            "v_cvt_pk_bf16_f32 v217, v218, v219\n\t"
            "v_cvt_pk_bf16_f32 v221, v222, v223\n\t"
            "v_mfma_f32_16x16x32_bf16 v[168:171], v[228:231], v[60:63], v[168:171]\n\t"
            "v_mfma_f32_16x16x32_bf16 v[172:175], v[228:231], v[84:87], v[172:175]\n\t"
            "v_mfma_f32_16x16x32_bf16 v[172:175], v[224:227], v[80:83], v[172:175]\n\t"
            "ds_read_b64 v[234:235], v234 offset:7856\n\t"
            "v_mfma_f32_16x16x32_bf16 v[152:155], v[224:227], v[104:107], v[152:155]\n\t"
            "v_mfma_f32_16x16x32_bf16 v[152:155], v[228:231], v[108:111], v[152:155]\n\t"
            "v_mfma_f32_16x16x32_bf16 v[156:159], v[228:231], v[132:135], v[156:159]\n\t"
            "ds_read_b128 v[228:231], v233 offset:12288\n\t"
            "v_mfma_f32_16x16x32_bf16 v[156:159], v[224:227], v[128:131], v[156:159]\n\t"
            "ds_read_b128 v[224:227], v232 offset:12288\n\t"
            "s_waitcnt lgkmcnt(3)\n\t"
            "v_mfma_f32_16x16x32_bf16 v[144:147], v[192:195], v[16:19], v[144:147]\n\t"
            "v_mfma_f32_16x16x32_bf16 v[144:147], v[196:199], v[20:23], v[144:147]\n\t"
            "v_mfma_f32_16x16x32_bf16 v[148:151], v[196:199], v[44:47], v[148:151]\n\t"
            "v_mfma_f32_16x16x32_bf16 v[148:151], v[192:195], v[40:43], v[148:151]\n\t"
            "s_waitcnt lgkmcnt(2)\n\t"
            "v_readfirstlane_b32 s77, v234\n\t"
            "v_readfirstlane_b32 s38, v235\n\t"
            "s_add_u32 s77, s77, s38\n\t"
            "s_cmp_ge_u32 s77, s44\n\t"
            "s_cbranch_scc0 .Lwz_spin%=\n\t"
            ".Lwz_go%=:\n\t"
            "v_mfma_f32_16x16x32_bf16 v[168:171], v[192:195], v[64:67], v[168:171]\n\t"
            "v_mul_f32 v144, v236, v144\n\t"
            "v_mul_f32 v145, v237, v145\n\t"
            "v_mfma_f32_16x16x32_bf16 v[168:171], v[196:199], v[68:71], v[168:171]\n\t"
            "v_mul_f32 v148, v236, v148\n\t"
            "v_mul_f32 v149, v237, v149\n\t"
            "v_mfma_f32_16x16x32_bf16 v[172:175], v[196:199], v[92:95], v[172:175]\n\t"
            "v_mul_f32 v146, v248, v146\n\t"
            "v_mul_f32 v147, v249, v147\n\t"
            "v_mfma_f32_16x16x32_bf16 v[172:175], v[192:195], v[88:91], v[172:175]\n\t"
            "v_mul_f32 v150, v248, v150\n\t"
            "v_mul_f32 v151, v249, v151\n\t"
            "v_mfma_f32_16x16x32_bf16 v[152:155], v[192:195], v[112:115], v[152:155]\n\t"
            "v_mul_f32 v168, v236, v168\n\t"
            "v_mul_f32 v169, v237, v169\n\t"
            "v_mfma_f32_16x16x32_bf16 v[152:155], v[196:199], v[116:119], v[152:155]\n\t"
            "v_mul_f32 v170, v248, v170\n\t"
            "v_mul_f32 v171, v249, v171\n\t"
            "v_mfma_f32_16x16x32_bf16 v[156:159], v[196:199], v[140:143], v[156:159]\n\t"
            "ds_read_b128 v[196:199], v233 offset:12416\n\t"
            "v_mul_f32 v172, v236, v172\n\t"
            "v_mfma_f32_16x16x32_bf16 v[156:159], v[192:195], v[136:139], v[156:159]\n\t"
            "ds_read_b128 v[192:195], v232 offset:12416\n\t"
            "v_mul_f32 v173, v237, v173\n\t"
            "s_waitcnt lgkmcnt(2)\n\t"
            "v_mfma_f32_16x16x32_bf16 v[160:163], v[224:227], v[0:3], 0\n\t"
            "v_mul_f32 v152, v236, v152\n\t"
            "v_mul_f32 v153, v237, v153\n\t"
            "v_mfma_f32_16x16x32_bf16 v[160:163], v[228:231], v[4:7], v[160:163]\n\t"
            "v_mul_f32 v174, v248, v174\n\t"
            "v_mul_f32 v175, v249, v175\n\t"
            "v_mfma_f32_16x16x32_bf16 v[164:167], v[228:231], v[28:31], 0\n\t"
            "v_mul_f32 v156, v236, v156\n\t"
            "v_mul_f32 v157, v237, v157\n\t"
            "v_mfma_f32_16x16x32_bf16 v[164:167], v[224:227], v[24:27], v[164:167]\n\t"
            "ds_read_b64 v[236:237], v247 offset:128\n\t"
            "v_mul_f32 v154, v248, v154\n\t"
            "v_mfma_f32_16x16x32_bf16 v[176:179], v[224:227], v[48:51], 0\n\t"
            "v_mul_f32 v155, v249, v155\n\t"
            "v_mul_f32 v158, v248, v158\n\t"
            "v_mfma_f32_16x16x32_bf16 v[176:179], v[228:231], v[52:55], v[176:179]\n\t"
            "v_mul_f32 v159, v249, v159\n\t"
            "v_cvt_pk_bf16_f32 v202, v144, v145\n\t"
            "v_mfma_f32_16x16x32_bf16 v[180:183], v[228:231], v[76:79], 0\n\t"
            "ds_read_b64 v[248:249], v247 offset:136\n\t"
            "v_cvt_pk_bf16_f32 v203, v146, v147\n\t"
            "v_mfma_f32_16x16x32_bf16 v[180:183], v[224:227], v[72:75], v[180:183]\n\t"
            "v_cvt_pk_bf16_f32 v206, v148, v149\n\t"
            "v_cvt_pk_bf16_f32 v207, v150, v151\n\t"
            "v_mfma_f32_16x16x32_bf16 v[184:187], v[224:227], v[96:99], 0\n\t"
            "v_cvt_pk_bf16_f32 v210, v168, v169\n\t"
            "v_cvt_pk_bf16_f32 v211, v170, v171\n\t"
            "v_mfma_f32_16x16x32_bf16 v[184:187], v[228:231], v[100:103], v[184:187]\n\t"
            "v_cvt_pk_bf16_f32 v214, v172, v173\n\t"
            "v_cvt_pk_bf16_f32 v215, v174, v175\n\t"
            "v_mfma_f32_16x16x32_bf16 v[188:191], v[228:231], v[124:127], 0\n\t"
            "ds_read_b128 v[228:231], v233 offset:12544\n\t"
            "v_cvt_pk_bf16_f32 v218, v152, v153\n\t"
            "v_mfma_f32_16x16x32_bf16 v[188:191], v[224:227], v[120:123], v[188:191]\n\t"
            "ds_read_b128 v[224:227], v232 offset:12544\n\t"
            "v_cvt_pk_bf16_f32 v219, v154, v155\n\t"
            "s_waitcnt lgkmcnt(4)\n\t"
            "v_mfma_f32_16x16x32_bf16 v[160:163], v[192:195], v[8:11], v[160:163]\n\t"
            "v_cvt_pk_bf16_f32 v222, v156, v157\n\t"
            "v_cvt_pk_bf16_f32 v223, v158, v159\n\t"
            "v_mfma_f32_16x16x32_bf16 v[160:163], v[196:199], v[12:15], v[160:163]\n\t"
            "ds_write_b128 v243, v[200:203]\n\t"
            "v_mfma_f32_16x16x32_bf16 v[164:167], v[196:199], v[36:39], v[164:167]\n\t"
            "v_mfma_f32_16x16x32_bf16 v[164:167], v[192:195], v[32:35], v[164:167]\n\t"
            "ds_write_b128 v243, v[204:207] offset:1536\n\t"
            "v_mfma_f32_16x16x32_bf16 v[176:179], v[192:195], v[56:59], v[176:179]\n\t"
            "v_mfma_f32_16x16x32_bf16 v[176:179], v[196:199], v[60:63], v[176:179]\n\t"
            "ds_write_b128 v243, v[208:211] offset:3072\n\t"
            "v_mfma_f32_16x16x32_bf16 v[180:183], v[196:199], v[84:87], v[180:183]\n\t"
            "v_mfma_f32_16x16x32_bf16 v[180:183], v[192:195], v[80:83], v[180:183]\n\t"
            "ds_write_b128 v243, v[212:215] offset:4608\n\t"
            "v_mfma_f32_16x16x32_bf16 v[184:187], v[192:195], v[104:107], v[184:187]\n\t"
            "v_mfma_f32_16x16x32_bf16 v[184:187], v[196:199], v[108:111], v[184:187]\n\t"
            "ds_write_b128 v243, v[216:219] offset:6144\n\t"
            "v_mfma_f32_16x16x32_bf16 v[188:191], v[196:199], v[132:135], v[188:191]\n\t"
            "v_mfma_f32_16x16x32_bf16 v[188:191], v[192:195], v[128:131], v[188:191]\n\t"
            "ds_write_b128 v243, v[220:223] offset:7680\n\t"
            "s_waitcnt lgkmcnt(6)\n\t"
            "v_mfma_f32_16x16x32_bf16 v[160:163], v[224:227], v[16:19], v[160:163]\n\t"
            "v_mfma_f32_16x16x32_bf16 v[160:163], v[228:231], v[20:23], v[160:163]\n\t"
            "v_mfma_f32_16x16x32_bf16 v[164:167], v[228:231], v[44:47], v[164:167]\n\t"
            "v_mfma_f32_16x16x32_bf16 v[164:167], v[224:227], v[40:43], v[164:167]\n\t"
            "v_mfma_f32_16x16x32_bf16 v[176:179], v[224:227], v[64:67], v[176:179]\n\t"
            "v_mfma_f32_16x16x32_bf16 v[176:179], v[228:231], v[68:71], v[176:179]\n\t"
            "v_mfma_f32_16x16x32_bf16 v[180:183], v[228:231], v[92:95], v[180:183]\n\t"
            "v_mfma_f32_16x16x32_bf16 v[180:183], v[224:227], v[88:91], v[180:183]\n\t"
            "s_nop 1\n\t"
            "v_mul_f32 v160, v236, v160\n\t"
            "v_mfma_f32_16x16x32_bf16 v[184:187], v[224:227], v[112:115], v[184:187]\n\t"
            "v_mul_f32 v161, v237, v161\n\t"
            "v_mul_f32 v162, v248, v162\n\t"
            "v_mfma_f32_16x16x32_bf16 v[184:187], v[228:231], v[116:119], v[184:187]\n\t"
            "v_mul_f32 v164, v236, v164\n\t"
            "v_mul_f32 v165, v237, v165\n\t"
            "v_mfma_f32_16x16x32_bf16 v[188:191], v[228:231], v[140:143], v[188:191]\n\t"
            "v_mul_f32 v176, v236, v176\n\t"
            "v_mul_f32 v177, v237, v177\n\t"
            "v_mfma_f32_16x16x32_bf16 v[188:191], v[224:227], v[136:139], v[188:191]\n\t"
            "v_mul_f32 v180, v236, v180\n\t"
            "v_mul_f32 v181, v237, v181\n\t"
            "v_mul_f32 v184, v236, v184\n\t"
            "v_mul_f32 v185, v237, v185\n\t"
            "v_mul_f32 v163, v249, v163\n\t"
            "v_cvt_pk_bf16_f32 v160, v160, v161\n\t"
            "v_mul_f32 v166, v248, v166\n\t"
            "v_mul_f32 v167, v249, v167\n\t"
            "v_mul_f32 v188, v236, v188\n\t"
            "v_mul_f32 v189, v237, v189\n\t"
            "v_cvt_pk_bf16_f32 v164, v164, v165\n\t"
            "v_mul_f32 v178, v248, v178\n\t"
            "v_mul_f32 v179, v249, v179\n\t"
            "v_cvt_pk_bf16_f32 v176, v176, v177\n\t"
            "v_mul_f32 v182, v248, v182\n\t"
            "v_mul_f32 v183, v249, v183\n\t"
            "v_cvt_pk_bf16_f32 v180, v180, v181\n\t"
            "v_mul_f32 v186, v248, v186\n\t"
            "v_mul_f32 v187, v249, v187\n\t"
            "v_cvt_pk_bf16_f32 v184, v184, v185\n\t"
            "v_mul_f32 v190, v248, v190\n\t"
            "v_mul_f32 v191, v249, v191\n\t"
            "v_cvt_pk_bf16_f32 v188, v188, v189\n\t"
            "v_cvt_pk_bf16_f32 v161, v162, v163\n\t"
            "v_cvt_pk_bf16_f32 v165, v166, v167\n\t"
            "v_cvt_pk_bf16_f32 v177, v178, v179\n\t"
            "v_cvt_pk_bf16_f32 v181, v182, v183\n\t"
            "v_cvt_pk_bf16_f32 v185, v186, v187\n\t"
            "v_cvt_pk_bf16_f32 v189, v190, v191\n\t"
            "ds_write_b64 v244, v[160:161]\n\t"
            "ds_write_b64 v244, v[164:165] offset:1536\n\t"
            "ds_write_b64 v244, v[176:177] offset:3072\n\t"
            "ds_write_b64 v244, v[180:181] offset:4608\n\t"
            "ds_write_b64 v244, v[184:185] offset:6144\n\t"
            "ds_write_b64 v244, v[188:189] offset:7680\n\t"
            "s_add_u32 s49, s33, 73728\n\t"
            "buffer_load_dwordx4 v[0:3], v245, s[28:31], s49 offen sc1\n\t"
            "s_add_u32 s49, s33, 79872\n\t"
            "buffer_load_dwordx4 v[24:27], v245, s[28:31], s49 offen sc1\n\t"
            "s_add_u32 s49, s33, 86016\n\t"
            "buffer_load_dwordx4 v[48:51], v245, s[28:31], s49 offen sc1\n\t"
            "s_add_u32 s49, s33, 92160\n\t"
            "buffer_load_dwordx4 v[72:75], v245, s[28:31], s49 offen sc1\n\t"
            "s_add_u32 s49, s33, 98304\n\t"
            "buffer_load_dwordx4 v[96:99], v245, s[28:31], s49 offen sc1\n\t"
            "s_add_u32 s49, s33, 104448\n\t"
            "buffer_load_dwordx4 v[120:123], v245, s[28:31], s49 offen sc1\n\t"
            "s_add_u32 s49, s33, 74752\n\t"
            "buffer_load_dwordx4 v[4:7], v245, s[28:31], s49 offen sc1\n\t"
            "s_add_u32 s49, s33, 80896\n\t"
            "buffer_load_dwordx4 v[28:31], v245, s[28:31], s49 offen sc1\n\t"
            "s_add_u32 s49, s33, 87040\n\t"
            "buffer_load_dwordx4 v[52:55], v245, s[28:31], s49 offen sc1\n\t"
            "s_add_u32 s49, s33, 93184\n\t"
            "buffer_load_dwordx4 v[76:79], v245, s[28:31], s49 offen sc1\n\t"
            "s_add_u32 s49, s33, 99328\n\t"
            "buffer_load_dwordx4 v[100:103], v245, s[28:31], s49 offen sc1\n\t"
            "s_add_u32 s49, s33, 105472\n\t"
            "buffer_load_dwordx4 v[124:127], v245, s[28:31], s49 offen sc1\n\t"
            "s_add_u32 s49, s33, 75776\n\t"
            "buffer_load_dwordx4 v[8:11], v245, s[28:31], s49 offen sc1\n\t"
            "s_add_u32 s49, s33, 81920\n\t"
            "buffer_load_dwordx4 v[32:35], v245, s[28:31], s49 offen sc1\n\t"
            "s_add_u32 s49, s33, 88064\n\t"
            "buffer_load_dwordx4 v[56:59], v245, s[28:31], s49 offen sc1\n\t"
            "s_add_u32 s49, s33, 94208\n\t"
            "buffer_load_dwordx4 v[80:83], v245, s[28:31], s49 offen sc1\n\t"
            "s_add_u32 s49, s33, 100352\n\t"
            "buffer_load_dwordx4 v[104:107], v245, s[28:31], s49 offen sc1\n\t"
            "s_add_u32 s49, s33, 106496\n\t"
            "buffer_load_dwordx4 v[128:131], v245, s[28:31], s49 offen sc1\n\t"
            "s_add_u32 s49, s33, 76800\n\t"
            "buffer_load_dwordx4 v[12:15], v245, s[28:31], s49 offen sc1\n\t"
            "s_add_u32 s49, s33, 82944\n\t"
            "buffer_load_dwordx4 v[36:39], v245, s[28:31], s49 offen sc1\n\t"
            "s_add_u32 s49, s33, 89088\n\t"
            "buffer_load_dwordx4 v[60:63], v245, s[28:31], s49 offen sc1\n\t"
            "s_add_u32 s49, s33, 95232\n\t"
            "buffer_load_dwordx4 v[84:87], v245, s[28:31], s49 offen sc1\n\t"
            "s_add_u32 s49, s33, 101376\n\t"
            "buffer_load_dwordx4 v[108:111], v245, s[28:31], s49 offen sc1\n\t"
            "s_add_u32 s49, s33, 107520\n\t"
            "buffer_load_dwordx4 v[132:135], v245, s[28:31], s49 offen sc1\n\t"
            "s_add_u32 s49, s33, 77824\n\t"
            "buffer_load_dwordx4 v[16:19], v245, s[28:31], s49 offen sc1\n\t"
            "s_add_u32 s49, s33, 83968\n\t"
            "buffer_load_dwordx4 v[40:43], v245, s[28:31], s49 offen sc1\n\t"
            "s_add_u32 s49, s33, 90112\n\t"
            "buffer_load_dwordx4 v[64:67], v245, s[28:31], s49 offen sc1\n\t"
            "s_add_u32 s49, s33, 96256\n\t"
            "buffer_load_dwordx4 v[88:91], v245, s[28:31], s49 offen sc1\n\t"
            "s_add_u32 s49, s33, 102400\n\t"
            "buffer_load_dwordx4 v[112:115], v245, s[28:31], s49 offen sc1\n\t"
            "s_add_u32 s49, s33, 108544\n\t"
            "buffer_load_dwordx4 v[136:139], v245, s[28:31], s49 offen sc1\n\t"
            "s_waitcnt lgkmcnt(0)\n\t"
            "s_barrier\n\t"
            "s_add_u32 s49, s33, 78848\n\t"
            "buffer_load_dwordx4 v[20:23], v245, s[28:31], s49 offen sc1\n\t"
            "s_add_u32 s49, s33, 84992\n\t"
            "buffer_load_dwordx4 v[44:47], v245, s[28:31], s49 offen sc1\n\t"
            "s_add_u32 s49, s33, 91136\n\t"
            "buffer_load_dwordx4 v[68:71], v245, s[28:31], s49 offen sc1\n\t"
            "s_add_u32 s49, s33, 97280\n\t"
            "buffer_load_dwordx4 v[92:95], v245, s[28:31], s49 offen sc1\n\t"
            "s_add_u32 s49, s33, 103424\n\t"
            "buffer_load_dwordx4 v[116:119], v245, s[28:31], s49 offen sc1\n\t"
            "s_add_u32 s49, s33, 109568\n\t"
            "buffer_load_dwordx4 v[140:143], v245, s[28:31], s49 offen sc1\n\t"
            "s_waitcnt vmcnt(0)\n\t"
            "s_nop 7\n\t"
            : "={v[0:3]}"(W[0]), "={v[4:7]}"(W[1]), "={v[8:11]}"(W[2]), "={v[12:15]}"(W[3]), "={v[16:19]}"(W[4]), "={v[20:23]}"(W[5]), "={v[24:27]}"(W[6]), "={v[28:31]}"(W[7]), "={v[32:35]}"(W[8]), "={v[36:39]}"(W[9]), "={v[40:43]}"(W[10]), "={v[44:47]}"(W[11]), "={v[48:51]}"(W[12]), "={v[52:55]}"(W[13]), "={v[56:59]}"(W[14]), "={v[60:63]}"(W[15]), "={v[64:67]}"(W[16]), "={v[68:71]}"(W[17]), "={v[72:75]}"(W[18]), "={v[76:79]}"(W[19]), "={v[80:83]}"(W[20]), "={v[84:87]}"(W[21]), "={v[88:91]}"(W[22]), "={v[92:95]}"(W[23]), "={v[96:99]}"(W[24]), "={v[100:103]}"(W[25]), "={v[104:107]}"(W[26]), "={v[108:111]}"(W[27]), "={v[112:115]}"(W[28]), "={v[116:119]}"(W[29]), "={v[120:123]}"(W[30]), "={v[124:127]}"(W[31]), "={v[128:131]}"(W[32]), "={v[132:135]}"(W[33]), "={v[136:139]}"(W[34]), "={v[140:143]}"(W[35]), "+{v246}"(aRSw_), "+{v247}"(aRSr_), "+{s44}"(s44), B_SIO
            : "{v242}"(pE_), "{v243}"(aVAw_), "{v244}"(aVBw_), "{v245}"(pST_), "{v250}"(pXw_), "{v251}"(pSTw_), "{v252}"(aCA_), "{v253}"(aB_), "{v254}"(aA_), "{v255}"(idx_), B_SIN, "{s82}"(touch)
            : "v144", "v145", "v146", "v147", "v148", "v149", "v150", "v151", "v152", "v153", "v154", "v155", "v156", "v157", "v158", "v159", "v160", "v161", "v162", "v163", "v164", "v165", "v166", "v167", "v168", "v169", "v170", "v171", "v172", "v173", "v174", "v175", "v176", "v177", "v178", "v179", "v180", "v181", "v182", "v183", "v184", "v185", "v186", "v187", "v188", "v189", "v190", "v191", "v192", "v193", "v194", "v195", "v196", "v197", "v198", "v199", "v236", "v237", "v248", "v249", "v200", "v201", "v202", "v204", "v205", "v206", "v207", "v208", "v209", "v210", "v211", "v212", "v213", "v214", "v215", "v216", "v217", "v218", "v219", "v220", "v221", "v222", "v223", "v224", "v225", "v226", "v227", "v228", "v229", "v230", "v231", "v232", "v233", "v234", "v235", B_SCLOB);
        }
        {
            __builtin_amdgcn_s_setprio(1);
            int ln_;
            asm volatile("v_mbcnt_lo_u32_b32 %0, -1, 0\n\tv_mbcnt_hi_u32_b32 %0, -1, %0" : "=v"(ln_));
            const int l15_ = ln_ & 15, g_ = ln_ >> 4;
            KARGS
            const auto orsrc = __builtin_amdgcn_make_buffer_rsrc((void*)ob, 0, IMG * IMG * CH * 2, 0x00020000);
#define L_PROJ(lt)                                                                                 \
    SB();                                                                                          \
    _Pragma("unroll") for (int ks = 0; ks < 6; ++ks)                                               \
        _Pragma("unroll") for (int j = 0; j < 6; ++j) acc[lt][j] = MFMA32(W[j * 6 + ks], of[lt][ks], acc[lt][j]); \
    _Pragma("unroll") for (int k = 0; k < 3; ++k) {                                                \
        const f32x4 a = acc[lt][2 * k], b = acc[lt][2 * k + 1];                                    \
        const u32x4 ov = {pk2(a[0], a[1]), pk2(a[2], a[3]), pk2(b[0], b[1]), pk2(b[2], b[3])};     \
        __builtin_amdgcn_raw_buffer_store_b128(ov, orsrc, o[lt] + 64 * k, 0, SC1);                 \
    }                                                                                              \
    SB();
            int lE[2], nw;
            unsigned o[2];
            f32x4 acc[2][6];
#pragma unroll
            for (int lt = 0; lt < 2; ++lt) {
                int i = 16 * lt + l15_;
                const unsigned en = *(const unsigned*)(smem + WL_OFF + i * 4);
                const bool ok = en != 0x7ffffffu;
                if (lt == 1) nw = 16 + __builtin_popcount((unsigned)__builtin_amdgcn_ballot_w64(ok) & 0xffffu);
                i = ok ? i : 0;
                lE[lt] = OL_OFF + i * 384 + ((g_ ^ ((i >> 1) & 7)) << 4);
                o[lt] = ok ? (en & 0x7ffffffu) + LPOS(en >> 31) * ROWBYTES + LPOS(en >> 30) * (CH * 2) + 192 * w + 16 * g_ : OOB;
#pragma unroll
                for (int k = 0; k < 3; ++k) {
                    const int c8 = 12 * w + 4 * k + g_, ws = (c8 * 43) >> 8, r6 = c8 - 6 * ws;
                    const u32x4 r = *(const u32x4*)(smem + LX_OFF(i) + (r6 >> 1) * 128 + ((2 * ws + (r6 & 1)) << 4));
                    acc[lt][2 * k] = (f32x4){bflo(r[0]), bfhi(r[0]), bflo(r[1]), bfhi(r[1])};
                    acc[lt][2 * k + 1] = (f32x4){bflo(r[2]), bfhi(r[2]), bflo(r[3]), bfhi(r[3])};
                }
            }
            bf16x8 of[2][6];
#pragma unroll
            for (int ks = 0; ks < 6; ++ks) of[0][ks] = *(const bf16x8*)(smem + ((ks & 1) ? (lE[0] ^ 64) : lE[0]) + (ks >> 1) * 128);
#pragma unroll
            for (int i = 0; i < 12; ++i) OPQ(acc[i / 6][i % 6]);
            OPQ(lE[1]);
            L_PROJ(0)
            if (0) {
                lE[0] = O_OFF + 12288 + l15_ * 384 + ((g_ ^ (l15_ >> 1)) << 4);
                o[0] = (*(const unsigned*)(smem + WL_OFF + (nw - 1) * 4) & 0x7ffffffu) + *(const unsigned*)(smem + CT_OFF + ln_ * 16 + 8) + 192 * w;
#pragma unroll
                for (int k = 0; k < 3; ++k) {
                    const int c8 = 12 * w + 4 * k + g_, ws = (c8 * 43) >> 8, r6 = c8 - 6 * ws;
                    const u32x4 r = *(const u32x4*)(smem + ((nw & 1) ? X_OFF : X1_OFF) + 12288 + l15_ * 384 + (r6 >> 1) * 128 + (((2 * ws + (r6 & 1)) ^ (l15_ >> 1)) << 4));
                    acc[0][2 * k] = (f32x4){bflo(r[0]), bfhi(r[0]), bflo(r[1]), bfhi(r[1])};
                    acc[0][2 * k + 1] = (f32x4){bflo(r[2]), bfhi(r[2]), bflo(r[3]), bfhi(r[3])};
                    OPQ(acc[0][2 * k]); OPQ(acc[0][2 * k + 1]);
                }
                OPQ(lE[0]); OPQ(o[0]);
            }

#pragma unroll 1
            for (int it_ = 0; it_ < 100000; ++it_) {
                unsigned n_;
                asm volatile("ds_read_b32 %0, %1\n\ts_waitcnt lgkmcnt(0)" : "=v"(n_) : "v"(ldsb + FLAG_OFF) : "memory");
                if (__builtin_amdgcn_readfirstlane(n_) >= 4 * nw) break;
                __builtin_amdgcn_s_sleep(1);
            }
#pragma unroll
            for (int ks = 0; ks < 6; ++ks) of[1][ks] = *(const bf16x8*)(smem + ((ks & 1) ? (lE[1] ^ 64) : lE[1]) + (ks >> 1) * 128);
            if (0) {
#pragma unroll
                for (int ks = 0; ks < 6; ++ks) of[0][ks] = *(const bf16x8*)(smem + ((ks & 1) ? (lE[0] ^ 64) : lE[0]) + (ks >> 1) * 128);
            }
            L_PROJ(1)
            if (0) { L_PROJ(0) }
        }
        __builtin_amdgcn_endpgm();
    } else {
        LANE_SETUP
        {
            const unsigned oE_ = ldsb + fE + (O_OFF - X_OFF), oO_ = oE_ ^ 64;
            unsigned l16_ = lane * 16;
            unsigned pr[3];
#pragma unroll
            for (int k = 0; k < 3; ++k) {
                const int c8 = 12 * (w - 2) + 4 * k + g;
                const int ws = c8 / 6, r6 = c8 - 6 * ws;
                pr[k] = l15 * 384 + (r6 >> 1) * 128 + (((2 * ws + (r6 & 1)) ^ sw) << 4);
            }
            const unsigned pR0_ = pr[0], pR1_ = pr[1], pR2_ = pr[2];
            unsigned q0 = ldsb + PF_OFF + 1024 + lane * 4, q1 = ldsb + O_OFF + lane * 16, q2 = oE_, q3 = ldsb + PF_OFF + 1024 + l15 * 4;
            unsigned q4 = ldsb + OL_OFF + l15 * 384 + (((4 * (w - 2) + (g >> 1)) ^ sw) << 4) + 8 * (g & 1);
            B_SCALARS
            asm volatile(
            "s_waitcnt lgkmcnt(0)\n\t"
            "s_mov_b32 s83, 0x10000\n\t"
            "s_mov_b32 s84, -47104\n\t"
            "s_nop 7\n\t"
            "v_mov_b32 v200, v250\n\t"
            "v_mov_b32 v201, v251\n\t"
            "v_mov_b32 v202, v253\n\t"
            "s_mov_b32 s38, s67\n\t"
            "s_mov_b32 s67, 74496\n\t"
            "ds_read_b64 v[250:251], v203\n\t"
            "ds_read_b32 v253, v203 offset:8\n\t"
            ".Lwk_b1%=:\n\t"
            "v_mov_b32 v236, 0\n\t"
            "ds_read_b32 v236, v236 offset:7864\n\t"
            "s_waitcnt lgkmcnt(0)\n\t"
            "v_readfirstlane_b32 s77, v236\n\t"
            "s_cmp_ge_u32 s77, 4\n\t"
            "s_cbranch_scc1 .Lwk_b2%=\n\t"
            "s_cmp_eq_u32 s83, 0\n\t"
            "s_cbranch_scc1 .Lwk_b2%=\n\t"
            "s_sub_u32 s83, s83, 1\n\t"
            "s_sleep 1\n\t"
            "s_branch .Lwk_b1%=\n\t"
            ".Lwk_b2%=:\n\t"
            "s_waitcnt vmcnt(0)\n\t"
            "s_call_b64 vcc, wk_conv\n\t"
            "v_mov_b32 v250, v200\n\t"
            "v_mov_b32 v251, v201\n\t"
            "v_mov_b32 v253, v202\n\t"
            "s_mov_b32 s67, s38\n\t"
            "s_barrier\n\t"
            "ds_read_b64 v[236:237], v252 offset:2688\n\t"
            "s_waitcnt lgkmcnt(0)\n\t"
            "s_mov_b64 exec, 1\n\t"
            "buffer_store_dwordx2 v[236:237], off, s[28:31], s44 sc1\n\t"
            "s_mov_b64 exec, -1\n\t"
            "s_sleep 30\n\t"
            "ds_read_b32 v249, v252 offset:12\n\t"
            "s_waitcnt lgkmcnt(0)\n\t"
            "v_and_b32 v249, s69, v249\n\t"
            "s_mov_b32 exec_hi, 0x3fffff\n\t"
            "s_add_u32 m0, s66, 0\n\t"
            "s_add_u32 s49, s79, 0\n\t"
            "buffer_load_dwordx4 v249, s[72:75], s49 offen lds\n\t"
            "s_mov_b32 exec_hi, -1\n\t"
            "s_mov_b32 exec_hi, 0x3fffff\n\t"
            "s_add_u32 m0, s66, 872\n\t"
            "s_add_u32 s49, s47, 196224\n\t"
            "buffer_load_dwordx4 v249, s[72:75], s49 offen lds\n\t"
            "s_mov_b32 exec_hi, -1\n\t"
            "s_mov_b32 exec_hi, 0x3fffff\n\t"
            "s_add_u32 m0, s66, 1744\n\t"
            "s_add_u32 s49, s47, 392448\n\t"
            "buffer_load_dwordx4 v249, s[72:75], s49 offen lds\n\t"
            "s_mov_b32 exec_hi, -1\n\t"
            "s_mov_b32 exec_hi, 0x3fffff\n\t"
            "s_add_u32 m0, s66, 2616\n\t"
            "s_add_u32 s49, s47, 588672\n\t"
            "buffer_load_dwordx4 v249, s[72:75], s49 offen lds\n\t"
            "s_mov_b32 exec_hi, -1\n\t"
            "s_mov_b32 exec_hi, 0x3fffff\n\t"
            "s_add_u32 m0, s66, 3488\n\t"
            "s_add_u32 s49, s47, 784896\n\t"
            "buffer_load_dwordx4 v249, s[72:75], s49 offen lds\n\t"
            "s_mov_b32 exec_hi, -1\n\t"
            "s_mov_b32 exec_hi, 0x3fffff\n\t"
            "s_add_u32 m0, s66, 4360\n\t"
            "s_add_u32 s49, s47, 981120\n\t"
            "buffer_load_dwordx4 v249, s[72:75], s49 offen lds\n\t"
            "s_mov_b32 exec_hi, -1\n\t"
            "s_mov_b32 exec_hi, 0x3fffff\n\t"
            "s_add_u32 m0, s66, 5232\n\t"
            "s_add_u32 s49, s47, 1177344\n\t"
            "buffer_load_dwordx4 v249, s[72:75], s49 offen lds\n\t"
            "s_mov_b32 exec_hi, -1\n\t"
            "s_mov_b32 exec_hi, 0x3fffff\n\t"
            "s_add_u32 m0, s66, 6104\n\t"
            "s_add_u32 s49, s47, 1373568\n\t"
            "buffer_load_dwordx4 v249, s[72:75], s49 offen lds\n\t"
            "s_mov_b32 exec_hi, -1\n\t"
            "s_mov_b32 exec_hi, 0x3fffff\n\t"
            "s_add_u32 m0, s66, 6976\n\t"
            "s_add_u32 s49, s47, 1569792\n\t"
            "buffer_load_dwordx4 v249, s[72:75], s49 offen lds\n\t"
            "s_mov_b32 exec_hi, -1\n\t"
            "s_add_u32 s49, s33, -73728\n\t"
            "buffer_load_dwordx4 v[0:3], v247, s[28:31], s49 offen sc1\n\t"
            "s_add_u32 s49, s33, -67584\n\t"
            "buffer_load_dwordx4 v[24:27], v247, s[28:31], s49 offen sc1\n\t"
            "s_add_u32 s49, s33, -61440\n\t"
            "buffer_load_dwordx4 v[48:51], v247, s[28:31], s49 offen sc1\n\t"
            "s_add_u32 s49, s33, -55296\n\t"
            "buffer_load_dwordx4 v[72:75], v247, s[28:31], s49 offen sc1\n\t"
            "s_add_u32 s49, s33, -49152\n\t"
            "buffer_load_dwordx4 v[96:99], v247, s[28:31], s49 offen sc1\n\t"
            "s_add_u32 s49, s33, -43008\n\t"
            "buffer_load_dwordx4 v[120:123], v247, s[28:31], s49 offen sc1\n\t"
            "s_add_u32 s49, s33, -72704\n\t"
            "buffer_load_dwordx4 v[4:7], v247, s[28:31], s49 offen sc1\n\t"
            "s_add_u32 s49, s33, -66560\n\t"
            "buffer_load_dwordx4 v[28:31], v247, s[28:31], s49 offen sc1\n\t"
            "s_add_u32 s49, s33, -60416\n\t"
            "buffer_load_dwordx4 v[52:55], v247, s[28:31], s49 offen sc1\n\t"
            "s_add_u32 s49, s33, -54272\n\t"
            "buffer_load_dwordx4 v[76:79], v247, s[28:31], s49 offen sc1\n\t"
            "s_add_u32 s49, s33, -48128\n\t"
            "buffer_load_dwordx4 v[100:103], v247, s[28:31], s49 offen sc1\n\t"
            "s_add_u32 s49, s33, -41984\n\t"
            "buffer_load_dwordx4 v[124:127], v247, s[28:31], s49 offen sc1\n\t"
            "s_add_u32 s49, s33, -71680\n\t"
            "buffer_load_dwordx4 v[8:11], v247, s[28:31], s49 offen sc1\n\t"
            "s_add_u32 s49, s33, -65536\n\t"
            "buffer_load_dwordx4 v[32:35], v247, s[28:31], s49 offen sc1\n\t"
            "s_add_u32 s49, s33, -59392\n\t"
            "buffer_load_dwordx4 v[56:59], v247, s[28:31], s49 offen sc1\n\t"
            "s_add_u32 s49, s33, -53248\n\t"
            "buffer_load_dwordx4 v[80:83], v247, s[28:31], s49 offen sc1\n\t"
            "s_add_u32 s49, s33, -47104\n\t"
            "buffer_load_dwordx4 v[104:107], v247, s[28:31], s49 offen sc1\n\t"
            "s_add_u32 s49, s33, -40960\n\t"
            "buffer_load_dwordx4 v[128:131], v247, s[28:31], s49 offen sc1\n\t"
            "s_add_u32 s49, s33, -70656\n\t"
            "buffer_load_dwordx4 v[12:15], v247, s[28:31], s49 offen sc1\n\t"
            "s_add_u32 s49, s33, -64512\n\t"
            "buffer_load_dwordx4 v[36:39], v247, s[28:31], s49 offen sc1\n\t"
            "s_add_u32 s49, s33, -58368\n\t"
            "buffer_load_dwordx4 v[60:63], v247, s[28:31], s49 offen sc1\n\t"
            "s_add_u32 s49, s33, -52224\n\t"
            "buffer_load_dwordx4 v[84:87], v247, s[28:31], s49 offen sc1\n\t"
            "s_add_u32 s49, s33, -46080\n\t"
            "buffer_load_dwordx4 v[108:111], v247, s[28:31], s49 offen sc1\n\t"
            "s_add_u32 s49, s33, -39936\n\t"
            "buffer_load_dwordx4 v[132:135], v247, s[28:31], s49 offen sc1\n\t"
            "s_add_u32 s49, s33, -69632\n\t"
            "buffer_load_dwordx4 v[16:19], v247, s[28:31], s49 offen sc1\n\t"
            "s_add_u32 s49, s33, -63488\n\t"
            "buffer_load_dwordx4 v[40:43], v247, s[28:31], s49 offen sc1\n\t"
            "s_add_u32 s49, s33, -57344\n\t"
            "buffer_load_dwordx4 v[64:67], v247, s[28:31], s49 offen sc1\n\t"
            "s_add_u32 s49, s33, -51200\n\t"
            "buffer_load_dwordx4 v[88:91], v247, s[28:31], s49 offen sc1\n\t"
            "s_add_u32 s49, s33, -45056\n\t"
            "buffer_load_dwordx4 v[112:115], v247, s[28:31], s49 offen sc1\n\t"
            "s_add_u32 s49, s33, -38912\n\t"
            "buffer_load_dwordx4 v[136:139], v247, s[28:31], s49 offen sc1\n\t"
            "s_add_u32 s49, s33, -68608\n\t"
            "buffer_load_dwordx4 v[20:23], v247, s[28:31], s49 offen sc1\n\t"
            "s_add_u32 s49, s33, -62464\n\t"
            "buffer_load_dwordx4 v[44:47], v247, s[28:31], s49 offen sc1\n\t"
            "s_add_u32 s49, s33, -56320\n\t"
            "buffer_load_dwordx4 v[68:71], v247, s[28:31], s49 offen sc1\n\t"
            "s_add_u32 s49, s33, -50176\n\t"
            "buffer_load_dwordx4 v[92:95], v247, s[28:31], s49 offen sc1\n\t"
            "s_add_u32 s49, s33, -44032\n\t"
            "buffer_load_dwordx4 v[116:119], v247, s[28:31], s49 offen sc1\n\t"
            "s_add_u32 s49, s33, -37888\n\t"
            "buffer_load_dwordx4 v[140:143], v247, s[28:31], s49 offen sc1\n\t"
            "s_setprio 3\n\t"
            "ds_read_b128 v[168:171], v254\n\t"
            "ds_read_b128 v[172:175], v254 offset:64\n\t"
            "ds_read_b128 v[220:223], v229 offset:12288\n\t"
            "ds_read_b128 v[224:227], v229 offset:13312\n\t"
            "ds_read_b128 v[200:203], v232\n\t"
            "ds_read_b128 v[208:211], v232 offset:128\n\t"
            "ds_read_b128 v[216:219], v232 offset:256\n\t"
            "s_waitcnt vmcnt(36)\n\t"
            "ds_read_b64 v[144:145], v253\n\t"
            "ds_read_b64 v[146:147], v253 offset:96\n\t"
            "ds_read_b64 v[148:149], v253 offset:1744\n\t"
            "ds_read_b64 v[150:151], v253 offset:1840\n\t"
            "ds_read_b64 v[152:153], v253 offset:8\n\t"
            "ds_read_b64 v[154:155], v253 offset:104\n\t"
            "ds_read_b64 v[156:157], v253 offset:1752\n\t"
            "ds_read_b64 v[158:159], v253 offset:1848\n\t"
            "s_waitcnt lgkmcnt(14)\n\t"
            "ds_read_b64 v[160:161], v253 offset:16\n\t"
            "s_waitcnt lgkmcnt(14)\n\t"
            "ds_read_b64 v[162:163], v253 offset:112\n\t"
            "s_waitcnt lgkmcnt(14)\n\t"
            "ds_read_b64 v[164:165], v253 offset:1760\n\t"
            "s_waitcnt lgkmcnt(14)\n\t"
            "ds_read_b64 v[166:167], v253 offset:1856\n\t"
            "v_mov_b64 v[176:177], 0\n\t"
            "v_mov_b64 v[178:179], 0\n\t"
            "v_mov_b64 v[180:181], 0\n\t"
            "v_mov_b64 v[182:183], 0\n\t"
            "v_mov_b32 v249, s76\n\t"
            "v_mov_b64 v[184:185], 0\n\t"
            "v_mov_b64 v[186:187], 0\n\t"
            "v_mov_b64 v[188:189], 0\n\t"
            "v_mov_b64 v[190:191], 0\n\t"
            "v_add_u32 v236, s67, v250\n\t"
            "v_add_u32 v248, s67, v251\n\t"
            "v_xor_b32 v235, 64, v232\n\t"
            "s_nop 0\n\t"
            "s_waitcnt lgkmcnt(14)\n\t"
            "ds_read_b32 v249, v249\n\t"
            "s_waitcnt lgkmcnt(14)\n\t"
            "ds_read_b128 v[204:207], v235\n\t"
            "s_waitcnt lgkmcnt(14)\n\t"
            "ds_read_b128 v[212:215], v235 offset:128\n\t"
            "v_add_f32 v220, v220, v222\n\t"
            "v_add_f32 v224, v224, v226\n\t"
            "v_add_f32 v220, v220, v224\n\t"
            "v_add_f32 v221, v221, v223\n\t"
            "v_add_f32 v225, v225, v227\n\t"
            "s_waitcnt lgkmcnt(11)\n\t"
            "v_smfmac_f32_16x16x64_bf16 v[176:179], v[168:171], v[144:151], v255\n\t"
            "ds_read_b128 v[168:171], v254 offset:128\n\t"
            "s_waitcnt lgkmcnt(8)\n\t"
            "v_smfmac_f32_16x16x64_bf16 v[180:183], v[172:175], v[152:159], v255\n\t"
            "ds_read_b64 v[144:145], v253 offset:24\n\t"
            "ds_read_b64 v[146:147], v253 offset:120\n\t"
            "ds_read_b64 v[148:149], v253 offset:1768\n\t"
            "ds_read_b64 v[150:151], v253 offset:1864\n\t"
            "ds_read_b128 v[172:175], v254 offset:192\n\t"
            "ds_read_b64 v[152:153], v253 offset:32\n\t"
            "ds_read_b64 v[154:155], v253 offset:128\n\t"
            "s_waitcnt lgkmcnt(14)\n\t"
            "ds_read_b64 v[156:157], v253 offset:1776\n\t"
            "s_waitcnt lgkmcnt(14)\n\t"
            "ds_read_b64 v[158:159], v253 offset:1872\n\t"
            "v_cvt_pk_bf16_f32 v176, v176, v177\n\t"
            "v_cvt_pk_bf16_f32 v177, v178, v179\n\t"
            "v_cvt_pk_bf16_f32 v178, v180, v181\n\t"
            "v_cvt_pk_bf16_f32 v179, v182, v183\n\t"
            "v_mfma_f32_4x4x4_16b_bf16 v[196:199], v[176:177], v[176:177], 0\n\t"
            "v_mov_b64 v[180:181], 0\n\t"
            "v_mfma_f32_16x16x32_bf16 v[192:195], v[238:241], v[176:179], 0\n\t"
            "s_waitcnt lgkmcnt(12)\n\t"
            "v_readfirstlane_b32 s26, v249\n\t"
            "v_mov_b64 v[182:183], 0\n\t"
            "v_mfma_f32_4x4x4_16b_bf16 v[196:199], v[178:179], v[178:179], v[196:199]\n\t"
            "ds_read_b32 v249, v252 offset:12\n\t"
            "s_and_b32 s68, s26, 0x7ffffff\n\t"
            "s_add_u32 s68, s68, s78\n\t"
            "s_and_b32 s69, s26, 0x38000000\n\t"
            "s_or_b32 s69, s69, s39\n\t"
            "s_bitcmp1_b32 s26, 27\n\t"
            "s_cselect_b32 s79, 2.0, s68\n\t"
            "s_waitcnt lgkmcnt(10)\n\t"
            "v_smfmac_f32_16x16x64_bf16 v[184:187], v[168:171], v[160:167], v255\n\t"
            "s_and_b32 s57, s56, 3\n\t"
            "s_lshl_b32 s58, s57, 3\n\t"
            "s_lshr_b32 m0, s54, s58\n\t"
            "ds_read_b128 v[168:171], v254 offset:256\n\t"
            "ds_read_b64 v[160:161], v253 offset:40\n\t"
            "ds_read_b64 v[162:163], v253 offset:136\n\t"
            "ds_read_b64 v[164:165], v253 offset:1784\n\t"
            "s_waitcnt lgkmcnt(9)\n\t"
            "v_smfmac_f32_16x16x64_bf16 v[188:191], v[172:175], v[144:151], v255\n\t"
            "s_lshl_b32 s58, s57, 4\n\t"
            "s_lshr_b64 s[36:37], s[50:51], s58\n\t"
            "s_and_b32 s57, s36, 0xffff\n\t"
            "s_add_u32 s57, s57, s67\n\t"
            "ds_read_b64 v[166:167], v253 offset:1880\n\t"
            "ds_read_b128 v[172:175], v254 offset:320\n\t"
            "ds_read_b64 v[144:145], v253 offset:48\n\t"
            "ds_read_b64 v[146:147], v253 offset:144\n\t"
            "ds_read_b64 v[148:149], v253 offset:1792\n\t"
            "ds_read_b64 v[150:151], v253 offset:1888\n\t"
            "v_writelane_b32 v236, s57, 63\n\t"
            "v_writelane_b32 v236, s59, m0\n\t"
            "v_cvt_pk_bf16_f32 v184, v184, v185\n\t"
            "v_cvt_pk_bf16_f32 v185, v186, v187\n\t"
            "v_xor_b32 v237, 16, v236\n\t"
            "v_cvt_pk_bf16_f32 v186, v188, v189\n\t"
            "v_mfma_f32_4x4x4_16b_bf16 v[196:199], v[184:185], v[184:185], v[196:199]\n\t"
            "s_waitcnt lgkmcnt(14)\n\t"
            "ds_write_b128 v236, v[176:179]\n\t"
            "v_mov_b64 v[176:177], 0\n\t"
            "v_mov_b64 v[178:179], 0\n\t"
            "v_cvt_pk_bf16_f32 v187, v190, v191\n\t"
            "v_mov_b64 v[188:189], 0\n\t"
            "s_waitcnt lgkmcnt(10)\n\t"
            "v_smfmac_f32_16x16x64_bf16 v[176:179], v[168:171], v[152:159], v255\n\t"
            "s_lshr_b64 s[36:37], s[52:53], s58\n\t"
            "s_and_b32 s58, s36, 0xffff\n\t"
            "s_add_u32 s58, s58, s67\n\t"
            "v_mfma_f32_16x16x32_bf16 v[192:195], v[238:241], v[184:187], v[192:195]\n\t"
            "s_and_b32 s38, s52, 0xffff\n\t"
            "s_add_u32 s38, s38, s67\n\t"
            "ds_read_b128 v[168:171], v254 offset:384\n\t"
            "v_mfma_f32_4x4x4_16b_bf16 v[196:199], v[186:187], v[186:187], v[196:199]\n\t"
            "ds_read_b64 v[152:153], v253 offset:56\n\t"
            "s_waitcnt lgkmcnt(7)\n\t"
            "v_smfmac_f32_16x16x64_bf16 v[180:183], v[172:175], v[160:167], v255\n\t"
            "ds_read_b64 v[154:155], v253 offset:152\n\t"
            "ds_read_b64 v[156:157], v253 offset:1800\n\t"
            "ds_read_b64 v[158:159], v253 offset:1896\n\t"
            "ds_read_b128 v[172:175], v254 offset:448\n\t"
            "ds_read_b64 v[160:161], v253 offset:64\n\t"
            "ds_read_b64 v[162:163], v253 offset:160\n\t"
            "ds_read_b64 v[164:165], v253 offset:1808\n\t"
            "ds_read_b64 v[166:167], v253 offset:1904\n\t"
            "s_waitcnt lgkmcnt(14)\n\t"
            "ds_write_b128 v237, v[184:187]\n\t"
            "v_mov_b64 v[184:185], 0\n\t"
            "v_mov_b64 v[186:187], 0\n\t"
            "v_mov_b64 v[190:191], 0\n\t"
            "v_cvt_pk_bf16_f32 v176, v176, v177\n\t"
            "v_cvt_pk_bf16_f32 v177, v178, v179\n\t"
            "v_cvt_pk_bf16_f32 v178, v180, v181\n\t"
            "v_cvt_pk_bf16_f32 v179, v182, v183\n\t"
            "v_mfma_f32_4x4x4_16b_bf16 v[196:199], v[176:177], v[176:177], v[196:199]\n\t"
            "s_waitcnt lgkmcnt(14)\n\t"
            "ds_write_b128 v236, v[176:179] offset:128\n\t"
            "s_waitcnt lgkmcnt(11)\n\t"
            "v_smfmac_f32_16x16x64_bf16 v[184:187], v[168:171], v[144:151], v255\n\t"
            "ds_read_b128 v[168:171], v254 offset:512\n\t"
            "v_mfma_f32_16x16x32_bf16 v[192:195], v[238:241], v[176:179], v[192:195]\n\t"
            "ds_read_b64 v[144:145], v253 offset:72\n\t"
            "ds_read_b64 v[146:147], v253 offset:168\n\t"
            "s_waitcnt lgkmcnt(9)\n\t"
            "v_smfmac_f32_16x16x64_bf16 v[188:191], v[172:175], v[152:159], v255\n\t"
            "ds_read_b64 v[148:149], v253 offset:1816\n\t"
            "v_mfma_f32_4x4x4_16b_bf16 v[196:199], v[178:179], v[178:179], v[196:199]\n\t"
            "ds_read_b64 v[150:151], v253 offset:1912\n\t"
            "ds_read_b128 v[172:175], v254 offset:576\n\t"
            "ds_read_b64 v[152:153], v253 offset:80\n\t"
            "ds_read_b64 v[154:155], v253 offset:176\n\t"
            "ds_read_b64 v[156:157], v253 offset:1824\n\t"
            "s_waitcnt lgkmcnt(14)\n\t"
            "ds_read_b64 v[158:159], v253 offset:1920\n\t"
            "v_mov_b64 v[176:177], 0\n\t"
            "v_mov_b64 v[178:179], 0\n\t"
            "v_mov_b64 v[180:181], 0\n\t"
            "v_mov_b64 v[182:183], 0\n\t"
            "v_cvt_pk_bf16_f32 v184, v184, v185\n\t"
            "v_cvt_pk_bf16_f32 v185, v186, v187\n\t"
            "v_cvt_pk_bf16_f32 v186, v188, v189\n\t"
            "v_cvt_pk_bf16_f32 v187, v190, v191\n\t"
            "v_mfma_f32_4x4x4_16b_bf16 v[196:199], v[184:185], v[184:185], v[196:199]\n\t"
            "s_waitcnt lgkmcnt(14)\n\t"
            "ds_write_b128 v237, v[184:187] offset:128\n\t"
            "v_mfma_f32_16x16x32_bf16 v[192:195], v[238:241], v[184:187], v[192:195]\n\t"
            "v_mov_b64 v[184:185], 0\n\t"
            "v_mov_b64 v[188:189], 0\n\t"
            "s_waitcnt lgkmcnt(10)\n\t"
            "v_smfmac_f32_16x16x64_bf16 v[176:179], v[168:171], v[160:167], v255\n\t"
            "ds_read_b128 v[168:171], v254 offset:640\n\t"
            "v_mfma_f32_4x4x4_16b_bf16 v[196:199], v[186:187], v[186:187], v[196:199]\n\t"
            "ds_read_b64 v[160:161], v253 offset:88\n\t"
            "ds_read_b64 v[162:163], v253 offset:184\n\t"
            "ds_read_b64 v[164:165], v253 offset:1832\n\t"
            "s_waitcnt lgkmcnt(9)\n\t"
            "v_smfmac_f32_16x16x64_bf16 v[180:183], v[172:175], v[144:151], v255\n\t"
            "ds_read_b64 v[166:167], v253 offset:1928\n\t"
            "ds_read_b128 v[172:175], v254 offset:704\n\t"
            "v_mov_b64 v[186:187], 0\n\t"
            "v_mov_b64 v[190:191], 0\n\t"
            "v_writelane_b32 v248, s58, 63\n\t"
            "v_writelane_b32 v248, s38, m0\n\t"
            "v_and_b32 v249, s69, v249\n\t"
            "v_cvt_pk_bf16_f32 v176, v176, v177\n\t"
            "v_cvt_pk_bf16_f32 v177, v178, v179\n\t"
            "v_cvt_pk_bf16_f32 v178, v180, v181\n\t"
            "v_cvt_pk_bf16_f32 v179, v182, v183\n\t"
            "v_mfma_f32_4x4x4_16b_bf16 v[196:199], v[176:177], v[176:177], v[196:199]\n\t"
            "v_mul_f32 v220, 0x3baaaaab, v220\n\t"
            "v_mfma_f32_16x16x32_bf16 v[192:195], v[238:241], v[176:179], v[192:195]\n\t"
            "v_add_f32 v221, v221, v225\n\t"
            "v_mul_f32_e64 v220, v220, -v220\n\t"
            "v_mfma_f32_4x4x4_16b_bf16 v[196:199], v[178:179], v[178:179], v[196:199]\n\t"
            "ds_read_b128 v[224:227], v232 offset:6144\n\t"
            "v_fmac_f32 v220, 0x3baaaaab, v221\n\t"
            "v_max_f32 v220, 0, v220\n\t"
            "v_add_f32 v220, 0x3727c5ac, v220\n\t"
            "v_rsq_f32 v220, v220\n\t"
            "s_waitcnt lgkmcnt(6)\n\t"
            "v_smfmac_f32_16x16x64_bf16 v[184:187], v[168:171], v[152:159], v255\n\t"
            "ds_write_b32 v228, v220\n\t"
            "ds_read_b128 v[220:223], v235 offset:256\n\t"
            "ds_read_b32 v230, v233\n\t"
            "ds_read_b32 v231, v233 offset:64\n\t"
            "s_waitcnt lgkmcnt(5)\n\t"
            "v_smfmac_f32_16x16x64_bf16 v[188:191], v[172:175], v[160:167], v255\n\t"
            "s_mov_b32 exec_hi, 0x3fffff\n\t"
            "s_add_u32 m0, s66, 0\n\t"
            "s_add_u32 s49, s79, 0\n\t"
            "buffer_load_dwordx4 v249, s[72:75], s49 offen lds\n\t"
            "s_add_u32 s49, s68, 195352\n\t"
            "buffer_load_dwordx4 v249, s[72:75], s49 offen offset:872 lds\n\t"
            "s_add_u32 s49, s68, 390704\n\t"
            "buffer_load_dwordx4 v249, s[72:75], s49 offen offset:1744 lds\n\t"
            "s_mov_b32 exec_hi, -1\n\t"
            "s_mov_b32 exec_hi, 0x3fffff\n\t"
            "s_add_u32 m0, s66, 2616\n\t"
            "s_add_u32 s49, s68, 588672\n\t"
            "buffer_load_dwordx4 v249, s[72:75], s49 offen lds\n\t"
            "s_add_u32 s49, s68, 784024\n\t"
            "buffer_load_dwordx4 v249, s[72:75], s49 offen offset:872 lds\n\t"
            "s_add_u32 s49, s68, 979376\n\t"
            "buffer_load_dwordx4 v249, s[72:75], s49 offen offset:1744 lds\n\t"
            "s_mov_b32 exec_hi, -1\n\t"
            "s_mov_b32 exec_hi, 0x3fffff\n\t"
            "s_add_u32 m0, s66, 5232\n\t"
            "s_add_u32 s49, s68, 1177344\n\t"
            "buffer_load_dwordx4 v249, s[72:75], s49 offen lds\n\t"
            "s_add_u32 s49, s68, 1372696\n\t"
            "buffer_load_dwordx4 v249, s[72:75], s49 offen offset:872 lds\n\t"
            "s_add_u32 s49, s68, 1568048\n\t"
            "buffer_load_dwordx4 v249, s[72:75], s49 offen offset:1744 lds\n\t"
            "s_mov_b32 exec_hi, -1\n\t"
            "v_cvt_pk_bf16_f32 v184, v184, v185\n\t"
            "v_cvt_pk_bf16_f32 v185, v186, v187\n\t"
            "v_cvt_pk_bf16_f32 v186, v188, v189\n\t"
            "v_cvt_pk_bf16_f32 v187, v190, v191\n\t"
            "v_mfma_f32_4x4x4_16b_bf16 v[196:199], v[184:185], v[184:185], v[196:199]\n\t"
            "s_waitcnt vmcnt(9)\n\t"
            "v_mfma_f32_16x16x32_bf16 v[192:195], v[238:241], v[184:187], v[192:195]\n\t"
            "ds_write_b128 v236, v[176:179] offset:256\n\t"
            "v_mfma_f32_4x4x4_16b_bf16 v[196:199], v[186:187], v[186:187], v[196:199]\n\t"
            "ds_write_b128 v237, v[184:187] offset:256\n\t"
            "v_mfma_f32_16x16x32_bf16 v[144:147], v[0:3], v[200:203], 0\n\t"
            "v_xor_b32 v228, 32, v234\n\t"
            "v_add_u32 v236, s48, v244\n\t"
            "v_mfma_f32_16x16x32_bf16 v[144:147], v[4:7], v[204:207], v[144:147]\n\t"
            "v_add_u32 v237, s48, v245\n\t"
            "v_mfma_f32_16x16x32_bf16 v[148:151], v[24:27], v[200:203], 0\n\t"
            "s_nop 0\n\t"
            "v_cndmask_b32_e64 v193, v196, v197, s[60:61]\n\t"
            "v_cndmask_b32_e64 v193, v193, v198, s[62:63]\n\t"
            "v_mfma_f32_16x16x32_bf16 v[148:151], v[28:31], v[204:207], v[148:151]\n\t"
            "v_cndmask_b32_e64 v193, v193, v199, s[64:65]\n\t"
            "v_mfma_f32_16x16x32_bf16 v[168:171], v[48:51], v[200:203], 0\n\t"
            "ds_read_b128 v[196:199], v232 offset:6272\n\t"
            "v_mfma_f32_16x16x32_bf16 v[168:171], v[52:55], v[204:207], v[168:171]\n\t"
            "ds_write_b64 v248, v[192:193] offset:18816\n\t"
            "v_mfma_f32_16x16x32_bf16 v[172:175], v[72:75], v[200:203], 0\n\t"
            "ds_read_b128 v[192:195], v235 offset:6144\n\t"
            "v_add_u32 v248, s48, v246\n\t"
            "v_mfma_f32_16x16x32_bf16 v[172:175], v[76:79], v[204:207], v[172:175]\n\t"
            "s_mov_b64 exec, 1\n\t"
            "ds_inc_u32 v255, v238 offset:7860\n\t"
            "s_mov_b64 exec, -1\n\t"
            "v_mfma_f32_16x16x32_bf16 v[152:155], v[96:99], v[200:203], 0\n\t"
            "v_mfma_f32_16x16x32_bf16 v[152:155], v[100:103], v[204:207], v[152:155]\n\t"
            "v_mfma_f32_16x16x32_bf16 v[156:159], v[120:123], v[200:203], 0\n\t"
            "ds_read_b128 v[200:203], v235 offset:6272\n\t"
            "v_mfma_f32_16x16x32_bf16 v[156:159], v[124:127], v[204:207], v[156:159]\n\t"
            "ds_read_b128 v[204:207], v232 offset:6400\n\t"
            "v_mfma_f32_16x16x32_bf16 v[144:147], v[8:11], v[208:211], v[144:147]\n\t"
            "v_mfma_f32_16x16x32_bf16 v[144:147], v[12:15], v[212:215], v[144:147]\n\t"
            "v_mfma_f32_16x16x32_bf16 v[148:151], v[32:35], v[208:211], v[148:151]\n\t"
            "v_mfma_f32_16x16x32_bf16 v[148:151], v[36:39], v[212:215], v[148:151]\n\t"
            "v_mfma_f32_16x16x32_bf16 v[168:171], v[56:59], v[208:211], v[168:171]\n\t"
            "v_mfma_f32_16x16x32_bf16 v[168:171], v[60:63], v[212:215], v[168:171]\n\t"
            "v_mfma_f32_16x16x32_bf16 v[172:175], v[80:83], v[208:211], v[172:175]\n\t"
            "v_mfma_f32_16x16x32_bf16 v[172:175], v[84:87], v[212:215], v[172:175]\n\t"
            "v_mfma_f32_16x16x32_bf16 v[152:155], v[104:107], v[208:211], v[152:155]\n\t"
            "v_mfma_f32_16x16x32_bf16 v[152:155], v[108:111], v[212:215], v[152:155]\n\t"
            "v_mfma_f32_16x16x32_bf16 v[156:159], v[128:131], v[208:211], v[156:159]\n\t"
            "ds_read_b128 v[208:211], v235 offset:6400\n\t"
            "v_mfma_f32_16x16x32_bf16 v[156:159], v[132:135], v[212:215], v[156:159]\n\t"
            "ds_read_b128 v[212:215], v236 offset:6144\n\t"
            "s_waitcnt lgkmcnt(12)\n\t"
            "v_mfma_f32_16x16x32_bf16 v[144:147], v[16:19], v[216:219], v[144:147]\n\t"
            "v_mfma_f32_16x16x32_bf16 v[144:147], v[20:23], v[220:223], v[144:147]\n\t"
            "v_mfma_f32_16x16x32_bf16 v[148:151], v[40:43], v[216:219], v[148:151]\n\t"
            "v_mfma_f32_16x16x32_bf16 v[148:151], v[44:47], v[220:223], v[148:151]\n\t"
            "v_mfma_f32_16x16x32_bf16 v[168:171], v[64:67], v[216:219], v[168:171]\n\t"
            "v_mfma_f32_16x16x32_bf16 v[168:171], v[68:71], v[220:223], v[168:171]\n\t"
            "v_mfma_f32_16x16x32_bf16 v[172:175], v[88:91], v[216:219], v[172:175]\n\t"
            "v_mfma_f32_16x16x32_bf16 v[172:175], v[92:95], v[220:223], v[172:175]\n\t"
            "s_waitcnt lgkmcnt(11)\n\t"
            "s_nop 0\n\t"
            "v_mul_f32 v144, v230, v144\n\t"
            "v_mfma_f32_16x16x32_bf16 v[152:155], v[112:115], v[216:219], v[152:155]\n\t"
            "v_mul_f32 v145, v230, v145\n\t"
            "v_mul_f32 v146, v230, v146\n\t"
            "v_mfma_f32_16x16x32_bf16 v[152:155], v[116:119], v[220:223], v[152:155]\n\t"
            "v_mul_f32 v148, v230, v148\n\t"
            "v_mul_f32 v149, v230, v149\n\t"
            "v_mfma_f32_16x16x32_bf16 v[156:159], v[136:139], v[216:219], v[156:159]\n\t"
            "ds_read_b128 v[216:219], v237 offset:6144\n\t"
            "v_mul_f32 v168, v230, v168\n\t"
            "v_mfma_f32_16x16x32_bf16 v[156:159], v[140:143], v[220:223], v[156:159]\n\t"
            "ds_read_b128 v[220:223], v248 offset:6144\n\t"
            "v_mul_f32 v169, v230, v169\n\t"
            "s_waitcnt lgkmcnt(7)\n\t"
            "v_mfma_f32_16x16x32_bf16 v[160:163], v[0:3], v[224:227], 0\n\t"
            "v_mul_f32 v172, v230, v172\n\t"
            "v_mul_f32 v173, v230, v173\n\t"
            "v_mfma_f32_16x16x32_bf16 v[160:163], v[4:7], v[192:195], v[160:163]\n\t"
            "v_mul_f32 v152, v230, v152\n\t"
            "v_mul_f32 v153, v230, v153\n\t"
            "v_mfma_f32_16x16x32_bf16 v[164:167], v[24:27], v[224:227], 0\n\t"
            "v_mul_f32 v156, v230, v156\n\t"
            "v_mul_f32 v157, v230, v157\n\t"
            "v_mfma_f32_16x16x32_bf16 v[164:167], v[28:31], v[192:195], v[164:167]\n\t"
            "v_mul_f32 v147, v230, v147\n\t"
            "v_cvt_pk_bf16_f32 v144, v144, v145\n\t"
            "v_mfma_f32_16x16x32_bf16 v[176:179], v[48:51], v[224:227], 0\n\t"
            "v_mul_f32 v150, v230, v150\n\t"
            "v_mul_f32 v151, v230, v151\n\t"
            "v_mfma_f32_16x16x32_bf16 v[176:179], v[52:55], v[192:195], v[176:179]\n\t"
            "v_cvt_pk_bf16_f32 v148, v148, v149\n\t"
            "v_mul_f32 v170, v230, v170\n\t"
            "v_mfma_f32_16x16x32_bf16 v[180:183], v[72:75], v[224:227], 0\n\t"
            "v_mul_f32 v171, v230, v171\n\t"
            "v_cvt_pk_bf16_f32 v168, v168, v169\n\t"
            "v_mfma_f32_16x16x32_bf16 v[180:183], v[76:79], v[192:195], v[180:183]\n\t"
            "v_mul_f32 v174, v230, v174\n\t"
            "v_mul_f32 v175, v230, v175\n\t"
            "v_mfma_f32_16x16x32_bf16 v[184:187], v[96:99], v[224:227], 0\n\t"
            "v_cvt_pk_bf16_f32 v172, v172, v173\n\t"
            "v_mul_f32 v154, v230, v154\n\t"
            "v_mfma_f32_16x16x32_bf16 v[184:187], v[100:103], v[192:195], v[184:187]\n\t"
            "v_mul_f32 v155, v230, v155\n\t"
            "v_cvt_pk_bf16_f32 v152, v152, v153\n\t"
            "v_mfma_f32_16x16x32_bf16 v[188:191], v[120:123], v[224:227], 0\n\t"
            "ds_read_b128 v[224:227], v236 offset:12288\n\t"
            "v_mul_f32 v158, v230, v158\n\t"
            "v_mfma_f32_16x16x32_bf16 v[188:191], v[124:127], v[192:195], v[188:191]\n\t"
            "v_mul_f32 v159, v230, v159\n\t"
            "v_cvt_pk_bf16_f32 v156, v156, v157\n\t"
            "s_waitcnt lgkmcnt(6)\n\t"
            "v_mfma_f32_16x16x32_bf16 v[160:163], v[8:11], v[196:199], v[160:163]\n\t"
            "v_cvt_pk_bf16_f32 v145, v146, v147\n\t"
            "v_cvt_pk_bf16_f32 v149, v150, v151\n\t"
            "v_mfma_f32_16x16x32_bf16 v[160:163], v[12:15], v[200:203], v[160:163]\n\t"
            "v_cvt_pk_bf16_f32 v169, v170, v171\n\t"
            "v_cvt_pk_bf16_f32 v173, v174, v175\n\t"
            "v_mfma_f32_16x16x32_bf16 v[164:167], v[32:35], v[196:199], v[164:167]\n\t"
            "v_cvt_pk_bf16_f32 v153, v154, v155\n\t"
            "v_cvt_pk_bf16_f32 v157, v158, v159\n\t"
            "v_mfma_f32_16x16x32_bf16 v[164:167], v[36:39], v[200:203], v[164:167]\n\t"
            "s_mov_b64 exec, -1\n\t"
            "ds_write_b64 v234, v[144:145]\n\t"
            "ds_write_b64 v234, v[148:149] offset:128\n\t"
            "ds_write_b64 v234, v[168:169] offset:256\n\t"
            "ds_write_b64 v228, v[172:173]\n\t"
            "ds_write_b64 v228, v[152:153] offset:128\n\t"
            "ds_write_b64 v228, v[156:157] offset:256\n\t"
            "s_mov_b64 exec, -1\n\t"
            "v_mfma_f32_16x16x32_bf16 v[176:179], v[56:59], v[196:199], v[176:179]\n\t"
            "v_mfma_f32_16x16x32_bf16 v[176:179], v[60:63], v[200:203], v[176:179]\n\t"
            "s_add_u32 s49, s33, 0\n\t"
            "buffer_load_dwordx4 v[0:3], v247, s[28:31], s49 offen sc1\n\t"
            "v_mfma_f32_16x16x32_bf16 v[180:183], v[80:83], v[196:199], v[180:183]\n\t"
            "v_mfma_f32_16x16x32_bf16 v[180:183], v[84:87], v[200:203], v[180:183]\n\t"
            "s_add_u32 s49, s33, 6144\n\t"
            "buffer_load_dwordx4 v[24:27], v247, s[28:31], s49 offen sc1\n\t"
            "v_mfma_f32_16x16x32_bf16 v[184:187], v[104:107], v[196:199], v[184:187]\n\t"
            "v_mfma_f32_16x16x32_bf16 v[184:187], v[108:111], v[200:203], v[184:187]\n\t"
            "s_add_u32 s49, s33, 12288\n\t"
            "buffer_load_dwordx4 v[48:51], v247, s[28:31], s49 offen sc1\n\t"
            "v_mfma_f32_16x16x32_bf16 v[188:191], v[128:131], v[196:199], v[188:191]\n\t"
            "v_mfma_f32_16x16x32_bf16 v[188:191], v[132:135], v[200:203], v[188:191]\n\t"
            "ds_read_b128 v[200:203], v236\n\t"
            "s_add_u32 s49, s33, 18432\n\t"
            "buffer_load_dwordx4 v[72:75], v247, s[28:31], s49 offen sc1\n\t"
            "s_waitcnt lgkmcnt(11)\n\t"
            "v_mfma_f32_16x16x32_bf16 v[160:163], v[16:19], v[204:207], v[160:163]\n\t"
            "v_mfma_f32_16x16x32_bf16 v[160:163], v[20:23], v[208:211], v[160:163]\n\t"
            "s_add_u32 s49, s33, 24576\n\t"
            "buffer_load_dwordx4 v[96:99], v247, s[28:31], s49 offen sc1\n\t"
            "v_mfma_f32_16x16x32_bf16 v[164:167], v[40:43], v[204:207], v[164:167]\n\t"
            "v_mfma_f32_16x16x32_bf16 v[164:167], v[44:47], v[208:211], v[164:167]\n\t"
            "s_add_u32 s49, s33, 30720\n\t"
            "buffer_load_dwordx4 v[120:123], v247, s[28:31], s49 offen sc1\n\t"
            "v_mfma_f32_16x16x32_bf16 v[176:179], v[64:67], v[204:207], v[176:179]\n\t"
            "s_nop 0\n\t"
            "v_mul_f32 v160, v231, v160\n\t"
            "v_mul_f32 v161, v231, v161\n\t"
            "v_mfma_f32_16x16x32_bf16 v[176:179], v[68:71], v[208:211], v[176:179]\n\t"
            "v_mul_f32 v162, v231, v162\n\t"
            "v_mul_f32 v163, v231, v163\n\t"
            "v_mfma_f32_16x16x32_bf16 v[180:183], v[88:91], v[204:207], v[180:183]\n\t"
            "v_mul_f32 v164, v231, v164\n\t"
            "v_mul_f32 v165, v231, v165\n\t"
            "v_mfma_f32_16x16x32_bf16 v[180:183], v[92:95], v[208:211], v[180:183]\n\t"
            "v_cvt_pk_bf16_f32 v160, v160, v161\n\t"
            "v_mul_f32 v166, v231, v166\n\t"
            "v_mfma_f32_16x16x32_bf16 v[184:187], v[112:115], v[204:207], v[184:187]\n\t"
            "v_mul_f32 v176, v231, v176\n\t"
            "v_mul_f32 v177, v231, v177\n\t"
            "v_mfma_f32_16x16x32_bf16 v[184:187], v[116:119], v[208:211], v[184:187]\n\t"
            "v_mul_f32 v167, v231, v167\n\t"
            "v_cvt_pk_bf16_f32 v164, v164, v165\n\t"
            "v_mfma_f32_16x16x32_bf16 v[188:191], v[136:139], v[204:207], v[188:191]\n\t"
            "ds_read_b128 v[204:207], v237\n\t"
            "v_mul_f32 v180, v231, v180\n\t"
            "v_mfma_f32_16x16x32_bf16 v[188:191], v[140:143], v[208:211], v[188:191]\n\t"
            "ds_read_b128 v[208:211], v248\n\t"
            "v_mul_f32 v181, v231, v181\n\t"
            "v_mul_f32 v184, v231, v184\n\t"
            "v_mul_f32 v185, v231, v185\n\t"
            "v_mul_f32 v178, v231, v178\n\t"
            "v_mul_f32 v179, v231, v179\n\t"
            "v_cvt_pk_bf16_f32 v176, v176, v177\n\t"
            "v_mul_f32 v182, v231, v182\n\t"
            "v_mul_f32 v188, v231, v188\n\t"
            "v_mul_f32 v189, v231, v189\n\t"
            "v_mul_f32 v183, v231, v183\n\t"
            "v_cvt_pk_bf16_f32 v180, v180, v181\n\t"
            "v_mul_f32 v186, v231, v186\n\t"
            "v_mul_f32 v187, v231, v187\n\t"
            "v_cvt_pk_bf16_f32 v184, v184, v185\n\t"
            "v_mul_f32 v190, v231, v190\n\t"
            "v_mul_f32 v191, v231, v191\n\t"
            "v_cvt_pk_bf16_f32 v188, v188, v189\n\t"
            "v_cvt_pk_bf16_f32 v161, v162, v163\n\t"
            "v_cvt_pk_bf16_f32 v165, v166, v167\n\t"
            "v_cvt_pk_bf16_f32 v177, v178, v179\n\t"
            "v_cvt_pk_bf16_f32 v181, v182, v183\n\t"
            "v_cvt_pk_bf16_f32 v185, v186, v187\n\t"
            "v_cvt_pk_bf16_f32 v189, v190, v191\n\t"
            "s_waitcnt lgkmcnt(9)\n\t"
            "s_mov_b64 exec, s[80:81]\n\t"
            "ds_write_b64 v234, v[160:161] offset:6144\n\t"
            "ds_write_b64 v234, v[164:165] offset:6272\n\t"
            "ds_write_b64 v234, v[176:177] offset:6400\n\t"
            "ds_write_b64 v228, v[180:181] offset:6144\n\t"
            "ds_write_b64 v228, v[184:185] offset:6272\n\t"
            "ds_write_b64 v228, v[188:189] offset:6400\n\t"
            "s_mov_b64 exec, -1\n\t"
            "s_waitcnt lgkmcnt(14)\n\t"
            "ds_read_b128 v[228:231], v237 offset:12288\n\t"
            "s_waitcnt lgkmcnt(14)\n\t"
            "ds_read_b128 v[232:235], v248 offset:12288\n\t"
            "s_add_u32 s49, s33, 1024\n\t"
            "buffer_load_dwordx4 v[4:7], v247, s[28:31], s49 offen sc1\n\t"
            "s_add_u32 s49, s33, 7168\n\t"
            "buffer_load_dwordx4 v[28:31], v247, s[28:31], s49 offen sc1\n\t"
            "s_waitcnt lgkmcnt(0)\n\t"
            "s_barrier\n\t"
            "s_setprio 3\n\t"
            "s_add_u32 s49, s33, 13312\n\t"
            "buffer_load_dwordx4 v[52:55], v247, s[28:31], s49 offen sc1\n\t"
            "s_add_u32 s49, s33, 19456\n\t"
            "buffer_load_dwordx4 v[76:79], v247, s[28:31], s49 offen sc1\n\t"
            "s_add_u32 s49, s33, 25600\n\t"
            "buffer_load_dwordx4 v[100:103], v247, s[28:31], s49 offen sc1\n\t"
            "s_add_u32 s49, s33, 31744\n\t"
            "buffer_load_dwordx4 v[124:127], v247, s[28:31], s49 offen sc1\n\t"
            "s_add_u32 s49, s33, 2048\n\t"
            "buffer_load_dwordx4 v[8:11], v247, s[28:31], s49 offen sc1\n\t"
            "s_add_u32 s49, s33, 8192\n\t"
            "buffer_load_dwordx4 v[32:35], v247, s[28:31], s49 offen sc1\n\t"
            "s_add_u32 s49, s33, 14336\n\t"
            "buffer_load_dwordx4 v[56:59], v247, s[28:31], s49 offen sc1\n\t"
            "s_add_u32 s49, s33, 20480\n\t"
            "buffer_load_dwordx4 v[80:83], v247, s[28:31], s49 offen sc1\n\t"
            "s_add_u32 s49, s33, 26624\n\t"
            "buffer_load_dwordx4 v[104:107], v247, s[28:31], s49 offen sc1\n\t"
            "s_add_u32 s49, s33, 32768\n\t"
            "buffer_load_dwordx4 v[128:131], v247, s[28:31], s49 offen sc1\n\t"
            "s_add_u32 s49, s33, 3072\n\t"
            "buffer_load_dwordx4 v[12:15], v247, s[28:31], s49 offen sc1\n\t"
            "s_add_u32 s49, s33, 9216\n\t"
            "buffer_load_dwordx4 v[36:39], v247, s[28:31], s49 offen sc1\n\t"
            "s_add_u32 s49, s33, 15360\n\t"
            "buffer_load_dwordx4 v[60:63], v247, s[28:31], s49 offen sc1\n\t"
            "s_add_u32 s49, s33, 21504\n\t"
            "buffer_load_dwordx4 v[84:87], v247, s[28:31], s49 offen sc1\n\t"
            "s_add_u32 s49, s33, 27648\n\t"
            "buffer_load_dwordx4 v[108:111], v247, s[28:31], s49 offen sc1\n\t"
            "s_add_u32 s49, s33, 33792\n\t"
            "buffer_load_dwordx4 v[132:135], v247, s[28:31], s49 offen sc1\n\t"
            "s_add_u32 s49, s33, 4096\n\t"
            "buffer_load_dwordx4 v[16:19], v247, s[28:31], s49 offen sc1\n\t"
            "s_add_u32 s49, s33, 10240\n\t"
            "buffer_load_dwordx4 v[40:43], v247, s[28:31], s49 offen sc1\n\t"
            "s_add_u32 s49, s33, 16384\n\t"
            "buffer_load_dwordx4 v[64:67], v247, s[28:31], s49 offen sc1\n\t"
            "s_add_u32 s49, s33, 22528\n\t"
            "buffer_load_dwordx4 v[88:91], v247, s[28:31], s49 offen sc1\n\t"
            "s_add_u32 s49, s33, 28672\n\t"
            "buffer_load_dwordx4 v[112:115], v247, s[28:31], s49 offen sc1\n\t"
            "s_add_u32 s49, s33, 34816\n\t"
            "buffer_load_dwordx4 v[136:139], v247, s[28:31], s49 offen sc1\n\t"
            "s_add_u32 s49, s33, 5120\n\t"
            "buffer_load_dwordx4 v[20:23], v247, s[28:31], s49 offen sc1\n\t"
            "s_add_u32 s49, s33, 11264\n\t"
            "buffer_load_dwordx4 v[44:47], v247, s[28:31], s49 offen sc1\n\t"
            "s_add_u32 s49, s33, 17408\n\t"
            "buffer_load_dwordx4 v[68:71], v247, s[28:31], s49 offen sc1\n\t"
            "s_add_u32 s49, s33, 23552\n\t"
            "buffer_load_dwordx4 v[92:95], v247, s[28:31], s49 offen sc1\n\t"
            "s_add_u32 s49, s33, 29696\n\t"
            "buffer_load_dwordx4 v[116:119], v247, s[28:31], s49 offen sc1\n\t"
            "s_add_u32 s49, s33, 35840\n\t"
            "buffer_load_dwordx4 v[140:143], v247, s[28:31], s49 offen sc1\n\t"
            "s_add_u32 s59, s59, s84\n\t"
            "s_movk_i32 s84, 800\n\t"
            "s_add_u32 s45, s46, s27\n\t"
            "s_mov_b32 s46, s47\n\t"
            "s_mov_b32 s47, s68\n\t"
            "s_xor_b32 s48, s48, s71\n\t"
            "s_xor_b32 s67, s67, s71\n\t"
            "s_add_u32 s76, s76, 4\n\t"
            "s_add_u32 s70, s70, 4\n\t"
            "s_lshl_b32 s56, s56, 2\n\t"
            "s_lshr_b32 s49, s26, 30\n\t"
            "s_or_b32 s56, s56, s49\n\t"
            "s_cmp_eq_u32 s82, 0\n\t"
            "s_cbranch_scc1 .Lwk_nr%=\n\t"
            "s_waitcnt lgkmcnt(0)\n\t"
            "s_nop 7\n\t"
            "v_add_u32 v236, 127360, v247\n\t"
            "v_add_u32 v237, 136704, v247\n\t"
            "ds_read_b128 v[144:147], v236\n\t"
            "ds_read_b128 v[148:151], v237\n\t"
            "s_waitcnt lgkmcnt(0)\n\t"
            "s_mov_b32 exec_hi, 0x3ffff\n\t"
            "buffer_store_dwordx4 v[144:147], v247, s[28:31], s82 offen sc1\n\t"
            "s_mov_b64 exec, 0xffffff\n\t"
            "buffer_store_dwordx4 v[148:151], v247, s[28:31], s82 offen offset:800 sc1\n\t"
            "s_mov_b64 exec, -1\n\t"
            "ds_read_b128 v[152:155], v252 offset:2688\n\t"
            "s_waitcnt vmcnt(0) lgkmcnt(0)\n\t"
            "s_mov_b64 exec, 1\n\t"
            "buffer_store_dwordx4 v[152:155], off, s[28:31], s82 offset:1216 sc1\n\t"
            "s_mov_b64 exec, -1\n\t"
            "s_branch .Lwk_n2%=\n\t"
            ".Lwk_nr%=:\n\t"
            "s_waitcnt vmcnt(0)\n\t"
            ".Lwk_n2%=:\n\t"
            "s_nop 1\n\t"
            "s_nop 7\n\t"
            ".p2align 6\n\t"
            "s_nop 0\n\t"
            "s_nop 0\n\t"
            "s_nop 0\n\t"
            "s_nop 0\n\t"
            "s_nop 0\n\t"
            "s_nop 0\n\t"
            "s_nop 0\n\t"
            ".Lwb_loop%=:\n\t"
            "s_setprio 1\n\t"
            "ds_read_b128 v[168:171], v254\n\t"
            "ds_read_b128 v[172:175], v254 offset:64\n\t"
            "s_waitcnt vmcnt(9)\n\t"
            "ds_read_b64 v[144:145], v253\n\t"
            "ds_read_b64 v[146:147], v253 offset:96\n\t"
            "ds_read_b64 v[148:149], v253 offset:1744\n\t"
            "ds_read_b64 v[150:151], v253 offset:1840\n\t"
            "ds_read_b64 v[152:153], v253 offset:8\n\t"
            "ds_read_b64 v[154:155], v253 offset:104\n\t"
            "ds_read_b64 v[156:157], v253 offset:1752\n\t"
            "ds_read_b64 v[158:159], v253 offset:1848\n\t"
            "ds_read_b64 v[160:161], v253 offset:16\n\t"
            "ds_read_b64 v[162:163], v253 offset:112\n\t"
            "ds_read_b64 v[164:165], v253 offset:1760\n\t"
            "ds_read_b64 v[166:167], v253 offset:1856\n\t"
            "v_mov_b64 v[176:177], 0\n\t"
            "v_mov_b64 v[178:179], 0\n\t"
            "v_mov_b64 v[180:181], 0\n\t"
            "v_mov_b64 v[182:183], 0\n\t"
            "v_mov_b32 v249, s76\n\t"
            "v_mov_b64 v[184:185], 0\n\t"
            "v_mov_b64 v[186:187], 0\n\t"
            "v_mov_b64 v[188:189], 0\n\t"
            "v_mov_b64 v[190:191], 0\n\t"
            "v_add_u32 v236, s67, v250\n\t"
            "v_add_u32 v248, s67, v251\n\t"
            "v_mov_b32 v247, 0\n\t"
            "ds_read_b32 v249, v249\n\t"
            "s_setprio 3\n\t"
            "s_waitcnt lgkmcnt(9)\n\t"
            "v_smfmac_f32_16x16x64_bf16 v[176:179], v[168:171], v[144:151], v255\n\t"
            "ds_read_b128 v[168:171], v254 offset:128\n\t"
            "s_waitcnt lgkmcnt(6)\n\t"
            "v_smfmac_f32_16x16x64_bf16 v[180:183], v[172:175], v[152:159], v255\n\t"
            "s_setprio 1\n\t"
            "ds_read_b64 v[144:145], v253 offset:24\n\t"
            "ds_read_b64 v[146:147], v253 offset:120\n\t"
            "ds_read_b64 v[148:149], v253 offset:1768\n\t"
            "ds_read_b64 v[150:151], v253 offset:1864\n\t"
            "ds_read_b128 v[172:175], v254 offset:192\n\t"
            "ds_read_b64 v[152:153], v253 offset:32\n\t"
            "ds_read_b64 v[154:155], v253 offset:128\n\t"
            "ds_read_b64 v[156:157], v253 offset:1776\n\t"
            "ds_read_b64 v[158:159], v253 offset:1872\n\t"
            "v_cvt_pk_bf16_f32 v176, v176, v177\n\t"
            "v_cvt_pk_bf16_f32 v177, v178, v179\n\t"
            "v_cvt_pk_bf16_f32 v178, v180, v181\n\t"
            "v_cvt_pk_bf16_f32 v179, v182, v183\n\t"
            "s_setprio 3\n\t"
            "v_mfma_f32_4x4x4_16b_bf16 v[196:199], v[176:177], v[176:177], 0\n\t"
            "v_mov_b64 v[180:181], 0\n\t"
            "v_mfma_f32_16x16x32_bf16 v[192:195], v[238:241], v[176:179], 0\n\t"
            "s_waitcnt lgkmcnt(10)\n\t"
            "v_readfirstlane_b32 s26, v249\n\t"
            "v_mov_b64 v[182:183], 0\n\t"
            "v_mfma_f32_4x4x4_16b_bf16 v[196:199], v[178:179], v[178:179], v[196:199]\n\t"
            "ds_read_b32 v249, v252 offset:12\n\t"
            "s_and_b32 s68, s26, 0x7ffffff\n\t"
            "s_add_u32 s68, s68, s78\n\t"
            "s_and_b32 s69, s26, 0x38000000\n\t"
            "s_or_b32 s69, s69, s39\n\t"
            "s_bitcmp1_b32 s26, 27\n\t"
            "s_cselect_b32 s79, 2.0, s68\n\t"
            "s_waitcnt lgkmcnt(10)\n\t"
            "v_smfmac_f32_16x16x64_bf16 v[184:187], v[168:171], v[160:167], v255\n\t"
            "s_and_b32 s57, s56, 3\n\t"
            "s_lshl_b32 s58, s57, 3\n\t"
            "s_lshr_b32 m0, s54, s58\n\t"
            "ds_read_b128 v[168:171], v254 offset:256\n\t"
            "ds_read_b64 v[160:161], v253 offset:40\n\t"
            "ds_read_b64 v[162:163], v253 offset:136\n\t"
            "ds_read_b64 v[164:165], v253 offset:1784\n\t"
            "s_waitcnt lgkmcnt(9)\n\t"
            "v_smfmac_f32_16x16x64_bf16 v[188:191], v[172:175], v[144:151], v255\n\t"
            "s_setprio 1\n\t"
            "s_lshl_b32 s58, s57, 4\n\t"
            "s_lshr_b64 s[36:37], s[50:51], s58\n\t"
            "s_and_b32 s57, s36, 0xffff\n\t"
            "s_add_u32 s57, s57, s67\n\t"
            "ds_read_b64 v[166:167], v253 offset:1880\n\t"
            "ds_read_b128 v[172:175], v254 offset:320\n\t"
            "ds_read_b64 v[144:145], v253 offset:48\n\t"
            "ds_read_b64 v[146:147], v253 offset:144\n\t"
            "ds_read_b64 v[148:149], v253 offset:1792\n\t"
            "ds_read_b64 v[150:151], v253 offset:1888\n\t"
            "s_setprio 3\n\t"
            "v_writelane_b32 v236, s57, 63\n\t"
            "v_writelane_b32 v236, s59, m0\n\t"
            "v_cvt_pk_bf16_f32 v184, v184, v185\n\t"
            "v_cvt_pk_bf16_f32 v185, v186, v187\n\t"
            "v_xor_b32 v237, 16, v236\n\t"
            "v_cvt_pk_bf16_f32 v186, v188, v189\n\t"
            "v_mfma_f32_4x4x4_16b_bf16 v[196:199], v[184:185], v[184:185], v[196:199]\n\t"
            "s_waitcnt lgkmcnt(14)\n\t"
            "ds_write_b128 v236, v[176:179]\n\t"
            "v_mov_b64 v[176:177], 0\n\t"
            "v_mov_b64 v[178:179], 0\n\t"
            "v_cvt_pk_bf16_f32 v187, v190, v191\n\t"
            "v_mov_b64 v[188:189], 0\n\t"
            "s_waitcnt lgkmcnt(10)\n\t"
            "v_smfmac_f32_16x16x64_bf16 v[176:179], v[168:171], v[152:159], v255\n\t"
            "s_lshr_b64 s[36:37], s[52:53], s58\n\t"
            "s_and_b32 s58, s36, 0xffff\n\t"
            "s_add_u32 s58, s58, s67\n\t"
            "v_mfma_f32_16x16x32_bf16 v[192:195], v[238:241], v[184:187], v[192:195]\n\t"
            "s_and_b32 s38, s52, 0xffff\n\t"
            "s_add_u32 s38, s38, s67\n\t"
            "ds_read_b128 v[168:171], v254 offset:384\n\t"
            "v_mfma_f32_4x4x4_16b_bf16 v[196:199], v[186:187], v[186:187], v[196:199]\n\t"
            "ds_read_b64 v[152:153], v253 offset:56\n\t"
            "s_waitcnt lgkmcnt(7)\n\t"
            "v_smfmac_f32_16x16x64_bf16 v[180:183], v[172:175], v[160:167], v255\n\t"
            "ds_read_b64 v[154:155], v253 offset:152\n\t"
            "ds_read_b64 v[156:157], v253 offset:1800\n\t"
            "s_setprio 1\n\t"
            "ds_read_b64 v[158:159], v253 offset:1896\n\t"
            "ds_read_b128 v[172:175], v254 offset:448\n\t"
            "ds_read_b64 v[160:161], v253 offset:64\n\t"
            "ds_read_b64 v[162:163], v253 offset:160\n\t"
            "ds_read_b64 v[164:165], v253 offset:1808\n\t"
            "ds_read_b64 v[166:167], v253 offset:1904\n\t"
            "s_waitcnt lgkmcnt(14)\n\t"
            "ds_write_b128 v237, v[184:187]\n\t"
            "v_mov_b64 v[184:185], 0\n\t"
            "v_mov_b64 v[186:187], 0\n\t"
            "v_mov_b64 v[190:191], 0\n\t"
            "v_cvt_pk_bf16_f32 v176, v176, v177\n\t"
            "v_cvt_pk_bf16_f32 v177, v178, v179\n\t"
            "v_cvt_pk_bf16_f32 v178, v180, v181\n\t"
            "v_cvt_pk_bf16_f32 v179, v182, v183\n\t"
            "s_setprio 3\n\t"
            "v_mfma_f32_4x4x4_16b_bf16 v[196:199], v[176:177], v[176:177], v[196:199]\n\t"
            "s_waitcnt lgkmcnt(14)\n\t"
            "ds_write_b128 v236, v[176:179] offset:128\n\t"
            "s_waitcnt lgkmcnt(11)\n\t"
            "v_smfmac_f32_16x16x64_bf16 v[184:187], v[168:171], v[144:151], v255\n\t"
            "ds_read_b128 v[168:171], v254 offset:512\n\t"
            "v_mfma_f32_16x16x32_bf16 v[192:195], v[238:241], v[176:179], v[192:195]\n\t"
            "ds_read_b64 v[144:145], v253 offset:72\n\t"
            "ds_read_b64 v[146:147], v253 offset:168\n\t"
            "s_waitcnt lgkmcnt(9)\n\t"
            "v_smfmac_f32_16x16x64_bf16 v[188:191], v[172:175], v[152:159], v255\n\t"
            "ds_read_b64 v[148:149], v253 offset:1816\n\t"
            "v_mfma_f32_4x4x4_16b_bf16 v[196:199], v[178:179], v[178:179], v[196:199]\n\t"
            "ds_read_b64 v[150:151], v253 offset:1912\n\t"
            "ds_read_b128 v[172:175], v254 offset:576\n\t"
            "ds_read_b64 v[152:153], v253 offset:80\n\t"
            "ds_read_b64 v[154:155], v253 offset:176\n\t"
            "ds_read_b64 v[156:157], v253 offset:1824\n\t"
            "s_waitcnt lgkmcnt(14)\n\t"
            "ds_read_b64 v[158:159], v253 offset:1920\n\t"
            "v_mov_b64 v[176:177], 0\n\t"
            "v_mov_b64 v[178:179], 0\n\t"
            "v_mov_b64 v[180:181], 0\n\t"
            "v_mov_b64 v[182:183], 0\n\t"
            "v_cvt_pk_bf16_f32 v184, v184, v185\n\t"
            "v_cvt_pk_bf16_f32 v185, v186, v187\n\t"
            "v_cvt_pk_bf16_f32 v186, v188, v189\n\t"
            "v_cvt_pk_bf16_f32 v187, v190, v191\n\t"
            "v_mfma_f32_4x4x4_16b_bf16 v[196:199], v[184:185], v[184:185], v[196:199]\n\t"
            "s_waitcnt lgkmcnt(14)\n\t"
            "ds_write_b128 v237, v[184:187] offset:128\n\t"
            "v_mfma_f32_16x16x32_bf16 v[192:195], v[238:241], v[184:187], v[192:195]\n\t"
            "v_mov_b64 v[184:185], 0\n\t"
            "v_mov_b64 v[188:189], 0\n\t"
            "s_waitcnt lgkmcnt(10)\n\t"
            "v_smfmac_f32_16x16x64_bf16 v[176:179], v[168:171], v[160:167], v255\n\t"
            "ds_read_b128 v[168:171], v254 offset:640\n\t"
            "v_mfma_f32_4x4x4_16b_bf16 v[196:199], v[186:187], v[186:187], v[196:199]\n\t"
            "ds_read_b64 v[160:161], v253 offset:88\n\t"
            "ds_read_b64 v[162:163], v253 offset:184\n\t"
            "ds_read_b64 v[164:165], v253 offset:1832\n\t"
            "s_waitcnt lgkmcnt(9)\n\t"
            "v_smfmac_f32_16x16x64_bf16 v[180:183], v[172:175], v[144:151], v255\n\t"
            "s_setprio 1\n\t"
            "ds_read_b64 v[166:167], v253 offset:1928\n\t"
            "ds_read_b128 v[172:175], v254 offset:704\n\t"
            "v_mov_b64 v[186:187], 0\n\t"
            "v_mov_b64 v[190:191], 0\n\t"
            "v_writelane_b32 v248, s58, 63\n\t"
            "v_writelane_b32 v248, s38, m0\n\t"
            "v_and_b32 v249, s69, v249\n\t"
            "v_cvt_pk_bf16_f32 v176, v176, v177\n\t"
            "v_cvt_pk_bf16_f32 v177, v178, v179\n\t"
            "v_cvt_pk_bf16_f32 v178, v180, v181\n\t"
            "v_cvt_pk_bf16_f32 v179, v182, v183\n\t"
            "s_setprio 3\n\t"
            "v_mfma_f32_4x4x4_16b_bf16 v[196:199], v[176:177], v[176:177], v[196:199]\n\t"
            "v_and_b32 v151, 0xffff0000, v227\n\t"
            "v_mfma_f32_16x16x32_bf16 v[192:195], v[238:241], v[176:179], v[192:195]\n\t"
            "v_lshlrev_b32 v150, 16, v227\n\t"
            "v_and_b32 v149, 0xffff0000, v226\n\t"
            "v_mfma_f32_4x4x4_16b_bf16 v[196:199], v[178:179], v[178:179], v[196:199]\n\t"
            "v_lshlrev_b32 v148, 16, v226\n\t"
            "v_and_b32 v147, 0xffff0000, v225\n\t"
            "v_lshlrev_b32 v146, 16, v225\n\t"
            "v_and_b32 v145, 0xffff0000, v224\n\t"
            "v_lshlrev_b32 v144, 16, v224\n\t"
            "s_waitcnt lgkmcnt(5)\n\t"
            "v_smfmac_f32_16x16x64_bf16 v[184:187], v[168:171], v[152:159], v255\n\t"
            "v_and_b32 v159, 0xffff0000, v231\n\t"
            "v_lshlrev_b32 v158, 16, v231\n\t"
            "v_and_b32 v157, 0xffff0000, v230\n\t"
            "v_lshlrev_b32 v156, 16, v230\n\t"
            "v_and_b32 v155, 0xffff0000, v229\n\t"
            "v_lshlrev_b32 v154, 16, v229\n\t"
            "s_waitcnt lgkmcnt(0)\n\t"
            "v_smfmac_f32_16x16x64_bf16 v[188:191], v[172:175], v[160:167], v255\n\t"
            "s_mov_b32 exec_hi, 0x3fffff\n\t"
            "s_add_u32 m0, s66, 0\n\t"
            "s_add_u32 s49, s79, 0\n\t"
            "buffer_load_dwordx4 v249, s[72:75], s49 offen lds\n\t"
            "s_add_u32 s49, s68, 195352\n\t"
            "buffer_load_dwordx4 v249, s[72:75], s49 offen offset:872 lds\n\t"
            "s_add_u32 s49, s68, 390704\n\t"
            "buffer_load_dwordx4 v249, s[72:75], s49 offen offset:1744 lds\n\t"
            "s_mov_b32 exec_hi, -1\n\t"
            "s_mov_b32 exec_hi, 0x3fffff\n\t"
            "s_add_u32 m0, s66, 2616\n\t"
            "s_add_u32 s49, s68, 588672\n\t"
            "buffer_load_dwordx4 v249, s[72:75], s49 offen lds\n\t"
            "s_add_u32 s49, s68, 784024\n\t"
            "buffer_load_dwordx4 v249, s[72:75], s49 offen offset:872 lds\n\t"
            "s_add_u32 s49, s68, 979376\n\t"
            "buffer_load_dwordx4 v249, s[72:75], s49 offen offset:1744 lds\n\t"
            "s_mov_b32 exec_hi, -1\n\t"
            "s_mov_b32 exec_hi, 0x3fffff\n\t"
            "s_add_u32 m0, s66, 5232\n\t"
            "s_add_u32 s49, s68, 1177344\n\t"
            "buffer_load_dwordx4 v249, s[72:75], s49 offen lds\n\t"
            "s_add_u32 s49, s68, 1372696\n\t"
            "buffer_load_dwordx4 v249, s[72:75], s49 offen offset:872 lds\n\t"
            "s_add_u32 s49, s68, 1568048\n\t"
            "buffer_load_dwordx4 v249, s[72:75], s49 offen offset:1744 lds\n\t"
            "s_mov_b32 exec_hi, -1\n\t"
            "v_cvt_pk_bf16_f32 v184, v184, v185\n\t"
            "v_cvt_pk_bf16_f32 v185, v186, v187\n\t"
            "v_cvt_pk_bf16_f32 v186, v188, v189\n\t"
            "v_cvt_pk_bf16_f32 v187, v190, v191\n\t"
            "v_mfma_f32_4x4x4_16b_bf16 v[196:199], v[184:185], v[184:185], v[196:199]\n\t"
            "ds_write_b128 v237, v[184:187] offset:256\n\t"
            "v_mfma_f32_16x16x32_bf16 v[192:195], v[238:241], v[184:187], v[192:195]\n\t"
            "v_and_b32 v153, 0xffff0000, v228\n\t"
            "v_lshlrev_b32 v152, 16, v228\n\t"
            "v_mfma_f32_4x4x4_16b_bf16 v[196:199], v[186:187], v[186:187], v[196:199]\n\t"
            "s_setprio 1\n\t"
            "v_and_b32 v167, 0xffff0000, v235\n\t"
            "v_lshlrev_b32 v166, 16, v235\n\t"
            "v_and_b32 v165, 0xffff0000, v234\n\t"
            "v_lshlrev_b32 v164, 16, v234\n\t"
            "v_and_b32 v163, 0xffff0000, v233\n\t"
            "v_lshlrev_b32 v162, 16, v233\n\t"
            "v_and_b32 v161, 0xffff0000, v232\n\t"
            "v_lshlrev_b32 v160, 16, v232\n\t"
            "v_and_b32 v227, 0xffff0000, v203\n\t"
            "v_cndmask_b32_e64 v193, v196, v197, s[60:61]\n\t"
            "v_cndmask_b32_e64 v193, v193, v198, s[62:63]\n\t"
            "v_cndmask_b32_e64 v193, v193, v199, s[64:65]\n\t"
            "v_lshlrev_b32 v226, 16, v203\n\t"
            "v_and_b32 v225, 0xffff0000, v202\n\t"
            "v_lshlrev_b32 v224, 16, v202\n\t"
            "v_and_b32 v231, 0xffff0000, v207\n\t"
            "v_lshlrev_b32 v230, 16, v207\n\t"
            "v_and_b32 v229, 0xffff0000, v206\n\t"
            "v_lshlrev_b32 v228, 16, v206\n\t"
            "v_and_b32 v235, 0xffff0000, v211\n\t"
            "v_lshlrev_b32 v234, 16, v211\n\t"
            "v_and_b32 v233, 0xffff0000, v210\n\t"
            "v_lshlrev_b32 v232, 16, v210\n\t"
            "v_and_b32 v203, 0xffff0000, v201\n\t"
            "v_lshlrev_b32 v202, 16, v201\n\t"
            "v_and_b32 v207, 0xffff0000, v205\n\t"
            "v_lshlrev_b32 v206, 16, v205\n\t"
            "v_and_b32 v211, 0xffff0000, v209\n\t"
            "v_lshlrev_b32 v210, 16, v209\n\t"
            "v_and_b32 v201, 0xffff0000, v200\n\t"
            "v_and_b32 v205, 0xffff0000, v204\n\t"
            "v_and_b32 v209, 0xffff0000, v208\n\t"
            "v_lshlrev_b32 v200, 16, v200\n\t"
            "v_lshlrev_b32 v204, 16, v204\n\t"
            "v_lshlrev_b32 v208, 16, v208\n\t"
            "v_and_b32 v171, 0xffff0000, v215\n\t"
            "v_lshlrev_b32 v170, 16, v215\n\t"
            "v_and_b32 v169, 0xffff0000, v214\n\t"
            "v_lshlrev_b32 v168, 16, v214\n\t"
            "v_and_b32 v183, 0xffff0000, v223\n\t"
            "v_lshlrev_b32 v182, 16, v223\n\t"
            "v_and_b32 v181, 0xffff0000, v222\n\t"
            "v_lshlrev_b32 v180, 16, v222\n\t"
            "v_and_b32 v215, 0xffff0000, v213\n\t"
            "v_lshlrev_b32 v214, 16, v213\n\t"
            "v_and_b32 v223, 0xffff0000, v221\n\t"
            "v_lshlrev_b32 v222, 16, v221\n\t"
            "v_and_b32 v213, 0xffff0000, v212\n\t"
            "v_and_b32 v221, 0xffff0000, v220\n\t"
            "v_lshlrev_b32 v212, 16, v212\n\t"
            "v_lshlrev_b32 v220, 16, v220\n\t"
            "v_add_u32 v237, s48, v245\n\t"
            "ds_read_b32 v247, v247 offset:7856\n\t"
            "ds_read_b128 v[196:199], v252\n\t"
            "ds_write_b64 v248, v[192:193] offset:18816\n\t"
            "ds_write_b128 v236, v[176:179] offset:256\n\t"
            "v_and_b32 v179, 0xffff0000, v219\n\t"
            "v_lshlrev_b32 v178, 16, v219\n\t"
            "v_and_b32 v177, 0xffff0000, v218\n\t"
            "v_lshlrev_b32 v176, 16, v218\n\t"
            "v_and_b32 v219, 0xffff0000, v217\n\t"
            "v_lshlrev_b32 v218, 16, v217\n\t"
            "v_and_b32 v217, 0xffff0000, v216\n\t"
            "v_lshlrev_b32 v216, 16, v216\n\t"
            "v_add_u32 v236, s48, v244\n\t"
            "v_add_u32 v248, s48, v246\n\t"
            "s_mov_b64 exec, 1\n\t"
            "ds_inc_u32 v255, v238 offset:7860\n\t"
            "s_mov_b64 exec, -1\n\t"
            "s_waitcnt lgkmcnt(4)\n\t"
            "v_readfirstlane_b32 s77, v247\n\t"
            "s_cmp_ge_u32 s77, s70\n\t"
            "s_cbranch_scc0 .Lwb_spin%=\n\t"
            ".Lwb_go%=:\n\t"
            "s_bfe_u32 s57, s56, 0x20004\n\t"
            "s_lshl_b32 s58, s57, 3\n\t"
            "s_lshr_b32 s58, s55, s58\n\t"
            "s_lshl_b64 s[36:37], s[34:35], s58\n\t"
            "ds_read_b128 v[184:187], v242\n\t"
            "ds_read_b128 v[188:191], v243\n\t"
            "ds_read_b128 v[192:195], v242 offset:128\n\t"
            "ds_read_b128 v[172:175], v243 offset:128\n\t"
            "s_bitcmp1_b32 s57, 1\n\t"
            "s_cselect_b32 s58, 1177344, 0\n\t"
            "s_bitcmp1_b32 s57, 0\n\t"
            "s_cselect_b32 s57, 2304, 0\n\t"
            "s_add_u32 s57, s57, s58\n\t"
            "s_waitcnt lgkmcnt(7)\n\t"
            "s_mov_b64 exec, s[36:37]\n\t"
            "v_add_u32 v196, s57, v196\n\t"
            "s_mov_b64 exec, -1\n\t"
            "s_add_u32 s59, s59, s84\n\t"
            "s_movk_i32 s84, 800\n\t"
            "s_waitcnt lgkmcnt(2)\n\t"
            "v_mfma_f32_16x16x32_bf16 v[200:203], v[0:3], v[184:187], v[200:203]\n\t"
            "v_mfma_f32_16x16x32_bf16 v[200:203], v[4:7], v[188:191], v[200:203]\n\t"
            "v_mfma_f32_16x16x32_bf16 v[224:227], v[28:31], v[188:191], v[224:227]\n\t"
            "v_mfma_f32_16x16x32_bf16 v[224:227], v[24:27], v[184:187], v[224:227]\n\t"
            "v_mfma_f32_16x16x32_bf16 v[204:207], v[48:51], v[184:187], v[204:207]\n\t"
            "v_mfma_f32_16x16x32_bf16 v[204:207], v[52:55], v[188:191], v[204:207]\n\t"
            "v_mfma_f32_16x16x32_bf16 v[228:231], v[76:79], v[188:191], v[228:231]\n\t"
            "v_mfma_f32_16x16x32_bf16 v[228:231], v[72:75], v[184:187], v[228:231]\n\t"
            "v_mfma_f32_16x16x32_bf16 v[208:211], v[96:99], v[184:187], v[208:211]\n\t"
            "v_mfma_f32_16x16x32_bf16 v[208:211], v[100:103], v[188:191], v[208:211]\n\t"
            "v_mfma_f32_16x16x32_bf16 v[232:235], v[124:127], v[188:191], v[232:235]\n\t"
            "ds_read_b128 v[188:191], v243 offset:256\n\t"
            "v_mfma_f32_16x16x32_bf16 v[232:235], v[120:123], v[184:187], v[232:235]\n\t"
            "ds_read_b128 v[184:187], v242 offset:256\n\t"
            "s_waitcnt lgkmcnt(2)\n\t"
            "v_mfma_f32_16x16x32_bf16 v[200:203], v[8:11], v[192:195], v[200:203]\n\t"
            "v_mfma_f32_16x16x32_bf16 v[200:203], v[12:15], v[172:175], v[200:203]\n\t"
            "v_mfma_f32_16x16x32_bf16 v[224:227], v[36:39], v[172:175], v[224:227]\n\t"
            "v_mfma_f32_16x16x32_bf16 v[224:227], v[32:35], v[192:195], v[224:227]\n\t"
            "v_mfma_f32_16x16x32_bf16 v[204:207], v[56:59], v[192:195], v[204:207]\n\t"
            "v_mfma_f32_16x16x32_bf16 v[204:207], v[60:63], v[172:175], v[204:207]\n\t"
            "v_mfma_f32_16x16x32_bf16 v[228:231], v[84:87], v[172:175], v[228:231]\n\t"
            "v_mfma_f32_16x16x32_bf16 v[228:231], v[80:83], v[192:195], v[228:231]\n\t"
            "v_mfma_f32_16x16x32_bf16 v[208:211], v[104:107], v[192:195], v[208:211]\n\t"
            "v_mfma_f32_16x16x32_bf16 v[208:211], v[108:111], v[172:175], v[208:211]\n\t"
            "v_mfma_f32_16x16x32_bf16 v[232:235], v[132:135], v[172:175], v[232:235]\n\t"
            "ds_read_b128 v[172:175], v243 offset:6144\n\t"
            "v_mfma_f32_16x16x32_bf16 v[232:235], v[128:131], v[192:195], v[232:235]\n\t"
            "ds_read_b128 v[192:195], v242 offset:6144\n\t"
            "s_waitcnt lgkmcnt(2)\n\t"
            "v_mfma_f32_16x16x32_bf16 v[200:203], v[16:19], v[184:187], v[200:203]\n\t"
            "v_mfma_f32_16x16x32_bf16 v[200:203], v[20:23], v[188:191], v[200:203]\n\t"
            "v_mfma_f32_16x16x32_bf16 v[224:227], v[44:47], v[188:191], v[224:227]\n\t"
            "v_mfma_f32_16x16x32_bf16 v[224:227], v[40:43], v[184:187], v[224:227]\n\t"
            "v_mfma_f32_16x16x32_bf16 v[204:207], v[64:67], v[184:187], v[204:207]\n\t"
            "v_mfma_f32_16x16x32_bf16 v[204:207], v[68:71], v[188:191], v[204:207]\n\t"
            "v_mfma_f32_16x16x32_bf16 v[228:231], v[92:95], v[188:191], v[228:231]\n\t"
            "v_mfma_f32_16x16x32_bf16 v[228:231], v[88:91], v[184:187], v[228:231]\n\t"
            "s_nop 1\n\t"
            "v_cvt_pk_bf16_f32 v200, v200, v201\n\t"
            "v_mfma_f32_16x16x32_bf16 v[208:211], v[112:115], v[184:187], v[208:211]\n\t"
            "v_cvt_pk_bf16_f32 v201, v202, v203\n\t"
            "v_mfma_f32_16x16x32_bf16 v[208:211], v[116:119], v[188:191], v[208:211]\n\t"
            "v_cvt_pk_bf16_f32 v202, v224, v225\n\t"
            "v_cvt_pk_bf16_f32 v203, v226, v227\n\t"
            "v_mfma_f32_16x16x32_bf16 v[232:235], v[140:143], v[188:191], v[232:235]\n\t"
            "ds_read_b128 v[188:191], v243 offset:6272\n\t"
            "ds_read_b128 v[224:227], v236 offset:12288\n\t"
            "v_mfma_f32_16x16x32_bf16 v[232:235], v[136:139], v[184:187], v[232:235]\n\t"
            "ds_read_b128 v[184:187], v242 offset:6272\n\t"
            "buffer_store_dwordx4 v[200:203], v196, s[40:43], s45 offen sc1\n\t"
            "s_waitcnt lgkmcnt(3)\n\t"
            "v_mfma_f32_16x16x32_bf16 v[212:215], v[0:3], v[192:195], v[212:215]\n\t"
            "ds_read_b128 v[200:203], v236\n\t"
            "v_cvt_pk_bf16_f32 v204, v204, v205\n\t"
            "v_mfma_f32_16x16x32_bf16 v[212:215], v[4:7], v[172:175], v[212:215]\n\t"
            "v_cvt_pk_bf16_f32 v205, v206, v207\n\t"
            "v_cvt_pk_bf16_f32 v206, v228, v229\n\t"
            "v_mfma_f32_16x16x32_bf16 v[168:171], v[28:31], v[172:175], v[168:171]\n\t"
            "v_cvt_pk_bf16_f32 v207, v230, v231\n\t"
            "v_cvt_pk_bf16_f32 v208, v208, v209\n\t"
            "v_mfma_f32_16x16x32_bf16 v[168:171], v[24:27], v[192:195], v[168:171]\n\t"
            "ds_read_b128 v[228:231], v237 offset:12288\n\t"
            "buffer_store_dwordx4 v[204:207], v196, s[40:43], s45 offen offset:64 sc1\n\t"
            "v_mfma_f32_16x16x32_bf16 v[216:219], v[48:51], v[192:195], v[216:219]\n\t"
            "s_nop 0\n\t"
            "ds_read_b128 v[204:207], v237\n\t"
            "v_cvt_pk_bf16_f32 v209, v210, v211\n\t"
            "v_mfma_f32_16x16x32_bf16 v[216:219], v[52:55], v[172:175], v[216:219]\n\t"
            "v_cvt_pk_bf16_f32 v210, v232, v233\n\t"
            "v_cvt_pk_bf16_f32 v211, v234, v235\n\t"
            "v_mfma_f32_16x16x32_bf16 v[176:179], v[76:79], v[172:175], v[176:179]\n\t"
            "ds_read_b128 v[232:235], v248 offset:12288\n\t"
            "v_mfma_f32_16x16x32_bf16 v[176:179], v[72:75], v[192:195], v[176:179]\n\t"
            "buffer_store_dwordx4 v[208:211], v196, s[40:43], s45 offen offset:128 sc1\n\t"
            "v_mfma_f32_16x16x32_bf16 v[220:223], v[96:99], v[192:195], v[220:223]\n\t"
            "s_nop 0\n\t"
            "ds_read_b128 v[208:211], v248\n\t"
            "v_mfma_f32_16x16x32_bf16 v[220:223], v[100:103], v[172:175], v[220:223]\n\t"
            "v_mfma_f32_16x16x32_bf16 v[180:183], v[124:127], v[172:175], v[180:183]\n\t"
            "ds_read_b128 v[172:175], v243 offset:6400\n\t"
            "v_mfma_f32_16x16x32_bf16 v[180:183], v[120:123], v[192:195], v[180:183]\n\t"
            "ds_read_b128 v[192:195], v242 offset:6400\n\t"
            "s_waitcnt lgkmcnt(7)\n\t"
            "v_mfma_f32_16x16x32_bf16 v[212:215], v[8:11], v[184:187], v[212:215]\n\t"
            "v_mfma_f32_16x16x32_bf16 v[212:215], v[12:15], v[188:191], v[212:215]\n\t"
            "v_mfma_f32_16x16x32_bf16 v[168:171], v[36:39], v[188:191], v[168:171]\n\t"
            "v_mfma_f32_16x16x32_bf16 v[168:171], v[32:35], v[184:187], v[168:171]\n\t"
            "v_mfma_f32_16x16x32_bf16 v[216:219], v[56:59], v[184:187], v[216:219]\n\t"
            "v_mfma_f32_16x16x32_bf16 v[216:219], v[60:63], v[188:191], v[216:219]\n\t"
            "v_mfma_f32_16x16x32_bf16 v[176:179], v[84:87], v[188:191], v[176:179]\n\t"
            "v_mfma_f32_16x16x32_bf16 v[176:179], v[80:83], v[184:187], v[176:179]\n\t"
            "v_mfma_f32_16x16x32_bf16 v[220:223], v[104:107], v[184:187], v[220:223]\n\t"
            "v_mfma_f32_16x16x32_bf16 v[220:223], v[108:111], v[188:191], v[220:223]\n\t"
            "v_mfma_f32_16x16x32_bf16 v[180:183], v[132:135], v[188:191], v[180:183]\n\t"
            "ds_read_b128 v[188:191], v243 offset:12288\n\t"
            "v_mfma_f32_16x16x32_bf16 v[180:183], v[128:131], v[184:187], v[180:183]\n\t"
            "ds_read_b128 v[184:187], v242 offset:12288\n\t"
            "s_waitcnt lgkmcnt(2)\n\t"
            "v_mfma_f32_16x16x32_bf16 v[168:171], v[44:47], v[172:175], v[168:171]\n\t"
            "v_mfma_f32_16x16x32_bf16 v[168:171], v[40:43], v[192:195], v[168:171]\n\t"
            "v_mfma_f32_16x16x32_bf16 v[176:179], v[92:95], v[172:175], v[176:179]\n\t"
            "v_mfma_f32_16x16x32_bf16 v[176:179], v[88:91], v[192:195], v[176:179]\n\t"
            "v_mfma_f32_16x16x32_bf16 v[180:183], v[140:143], v[172:175], v[180:183]\n\t"
            "v_mfma_f32_16x16x32_bf16 v[180:183], v[136:139], v[192:195], v[180:183]\n\t"
            "v_mfma_f32_16x16x32_bf16 v[212:215], v[16:19], v[192:195], v[212:215]\n\t"
            "v_mfma_f32_16x16x32_bf16 v[212:215], v[20:23], v[172:175], v[212:215]\n\t"
            "s_nop 1\n\t"
            "v_cvt_pk_bf16_f32 v171, v170, v171\n\t"
            "v_mfma_f32_16x16x32_bf16 v[216:219], v[64:67], v[192:195], v[216:219]\n\t"
            "v_cvt_pk_bf16_f32 v170, v168, v169\n\t"
            "v_mfma_f32_16x16x32_bf16 v[216:219], v[68:71], v[172:175], v[216:219]\n\t"
            "v_cvt_pk_bf16_f32 v179, v178, v179\n\t"
            "v_cvt_pk_bf16_f32 v178, v176, v177\n\t"
            "v_mfma_f32_16x16x32_bf16 v[220:223], v[112:115], v[192:195], v[220:223]\n\t"
            "ds_read_b128 v[192:195], v242 offset:12416\n\t"
            "v_cvt_pk_bf16_f32 v183, v182, v183\n\t"
            "v_mfma_f32_16x16x32_bf16 v[220:223], v[116:119], v[172:175], v[220:223]\n\t"
            "ds_read_b128 v[172:175], v243 offset:12416\n\t"
            "v_cvt_pk_bf16_f32 v182, v180, v181\n\t"
            "s_waitcnt lgkmcnt(2)\n\t"
            "v_mfma_f32_16x16x32_bf16 v[144:147], v[0:3], v[184:187], v[144:147]\n\t"
            "v_cvt_pk_bf16_f32 v168, v212, v213\n\t"
            "v_cvt_pk_bf16_f32 v169, v214, v215\n\t"
            "v_mfma_f32_16x16x32_bf16 v[144:147], v[4:7], v[188:191], v[144:147]\n\t"
            "ds_read_b128 v[212:215], v236 offset:6144\n\t"
            "v_cvt_pk_bf16_f32 v176, v216, v217\n\t"
            "v_mfma_f32_16x16x32_bf16 v[148:151], v[28:31], v[188:191], v[148:151]\n\t"
            "v_cvt_pk_bf16_f32 v177, v218, v219\n\t"
            "v_cvt_pk_bf16_f32 v180, v220, v221\n\t"
            "v_mfma_f32_16x16x32_bf16 v[148:151], v[24:27], v[184:187], v[148:151]\n\t"
            "ds_read_b128 v[216:219], v237 offset:6144\n\t"
            "v_cvt_pk_bf16_f32 v181, v222, v223\n\t"
            "v_mfma_f32_16x16x32_bf16 v[152:155], v[48:51], v[184:187], v[152:155]\n\t"
            "ds_read_b128 v[220:223], v248 offset:6144\n\t"
            "v_mfma_f32_16x16x32_bf16 v[152:155], v[52:55], v[188:191], v[152:155]\n\t"
            "buffer_store_dwordx4 v[168:171], v197, s[40:43], s45 offen sc1\n\t"
            "v_mfma_f32_16x16x32_bf16 v[156:159], v[76:79], v[188:191], v[156:159]\n\t"
            "v_mfma_f32_16x16x32_bf16 v[156:159], v[72:75], v[184:187], v[156:159]\n\t"
            "buffer_store_dwordx4 v[176:179], v197, s[40:43], s45 offen offset:64 sc1\n\t"
            "v_mfma_f32_16x16x32_bf16 v[160:163], v[96:99], v[184:187], v[160:163]\n\t"
            "v_mfma_f32_16x16x32_bf16 v[160:163], v[100:103], v[188:191], v[160:163]\n\t"
            "buffer_store_dwordx4 v[180:183], v197, s[40:43], s45 offen offset:128 sc1\n\t"
            "v_mfma_f32_16x16x32_bf16 v[164:167], v[124:127], v[188:191], v[164:167]\n\t"
            "ds_read_b128 v[188:191], v243 offset:12544\n\t"
            "v_mfma_f32_16x16x32_bf16 v[164:167], v[120:123], v[184:187], v[164:167]\n\t"
            "ds_read_b128 v[184:187], v242 offset:12544\n\t"
            "s_waitcnt lgkmcnt(5)\n\t"
            "v_mfma_f32_16x16x32_bf16 v[144:147], v[8:11], v[192:195], v[144:147]\n\t"
            "v_mfma_f32_16x16x32_bf16 v[144:147], v[12:15], v[172:175], v[144:147]\n\t"
            "v_mfma_f32_16x16x32_bf16 v[148:151], v[36:39], v[172:175], v[148:151]\n\t"
            "v_mfma_f32_16x16x32_bf16 v[148:151], v[32:35], v[192:195], v[148:151]\n\t"
            "s_waitcnt lgkmcnt(0)\n\t"
            "s_barrier\n\t"
            "s_setprio 3\n\t"
            "v_mfma_f32_16x16x32_bf16 v[152:155], v[56:59], v[192:195], v[152:155]\n\t"
            "v_mfma_f32_16x16x32_bf16 v[152:155], v[60:63], v[172:175], v[152:155]\n\t"
            "v_mfma_f32_16x16x32_bf16 v[156:159], v[84:87], v[172:175], v[156:159]\n\t"
            "v_mfma_f32_16x16x32_bf16 v[156:159], v[80:83], v[192:195], v[156:159]\n\t"
            "v_mfma_f32_16x16x32_bf16 v[144:147], v[16:19], v[184:187], v[144:147]\n\t"
            "v_mfma_f32_16x16x32_bf16 v[144:147], v[20:23], v[188:191], v[144:147]\n\t"
            "v_mfma_f32_16x16x32_bf16 v[148:151], v[44:47], v[188:191], v[148:151]\n\t"
            "v_mfma_f32_16x16x32_bf16 v[148:151], v[40:43], v[184:187], v[148:151]\n\t"
            "v_mfma_f32_16x16x32_bf16 v[160:163], v[104:107], v[192:195], v[160:163]\n\t"
            "v_mfma_f32_16x16x32_bf16 v[160:163], v[108:111], v[172:175], v[160:163]\n\t"
            "v_mfma_f32_16x16x32_bf16 v[164:167], v[132:135], v[172:175], v[164:167]\n\t"
            "v_mfma_f32_16x16x32_bf16 v[164:167], v[128:131], v[192:195], v[164:167]\n\t"
            "s_nop 1\n\t"
            "v_cvt_pk_bf16_f32 v144, v144, v145\n\t"
            "v_mfma_f32_16x16x32_bf16 v[152:155], v[64:67], v[184:187], v[152:155]\n\t"
            "v_cvt_pk_bf16_f32 v145, v146, v147\n\t"
            "v_mfma_f32_16x16x32_bf16 v[152:155], v[68:71], v[188:191], v[152:155]\n\t"
            "v_cvt_pk_bf16_f32 v146, v148, v149\n\t"
            "v_cvt_pk_bf16_f32 v147, v150, v151\n\t"
            "v_mfma_f32_16x16x32_bf16 v[156:159], v[92:95], v[188:191], v[156:159]\n\t"
            "v_mfma_f32_16x16x32_bf16 v[156:159], v[88:91], v[184:187], v[156:159]\n\t"
            "buffer_store_dwordx4 v[144:147], v198, s[40:43], s45 offen sc1\n\t"
            "v_mfma_f32_16x16x32_bf16 v[160:163], v[112:115], v[184:187], v[160:163]\n\t"
            "s_nop 1\n\t"
            "v_cvt_pk_bf16_f32 v152, v152, v153\n\t"
            "v_mfma_f32_16x16x32_bf16 v[160:163], v[116:119], v[188:191], v[160:163]\n\t"
            "v_cvt_pk_bf16_f32 v153, v154, v155\n\t"
            "v_mfma_f32_16x16x32_bf16 v[164:167], v[140:143], v[188:191], v[164:167]\n\t"
            "v_cvt_pk_bf16_f32 v154, v156, v157\n\t"
            "v_mfma_f32_16x16x32_bf16 v[164:167], v[136:139], v[184:187], v[164:167]\n\t"
            "v_cvt_pk_bf16_f32 v155, v158, v159\n\t"
            "buffer_store_dwordx4 v[152:155], v198, s[40:43], s45 offen offset:64 sc1\n\t"
            "s_nop 1\n\t"
            "v_cvt_pk_bf16_f32 v160, v160, v161\n\t"
            "v_cvt_pk_bf16_f32 v161, v162, v163\n\t"
            "s_nop 1\n\t"
            "v_cvt_pk_bf16_f32 v162, v164, v165\n\t"
            "v_cvt_pk_bf16_f32 v163, v166, v167\n\t"
            "buffer_store_dwordx4 v[160:163], v198, s[40:43], s45 offen offset:128 sc1\n\t"
            "s_add_u32 s45, s46, s27\n\t"
            "s_mov_b32 s46, s47\n\t"
            "s_mov_b32 s47, s68\n\t"
            "s_xor_b32 s48, s48, s71\n\t"
            "s_xor_b32 s67, s67, s71\n\t"
            "s_add_u32 s76, s76, 4\n\t"
            "s_add_u32 s70, s70, 4\n\t"
            "s_lshl_b32 s56, s56, 2\n\t"
            "s_lshr_b32 s49, s26, 30\n\t"
            "s_or_b32 s56, s56, s49\n\t"
            "s_cmp_lt_u32 s47, 0x7000000\n\t"
            "s_cbranch_scc1 .Lwb_loop%=\n\t"
            "s_branch .Lwb_end%=\n\t"
            ".Lwz_spin%=:\n\t"
            "s_cmp_eq_u32 s83, 0\n\t"
            "s_cbranch_scc1 .Lwz_go%=\n\t"
            "s_sub_u32 s83, s83, 1\n\t"
            "s_sleep 2\n\t"
            "v_mov_b32 v247, 0\n\t"
            "ds_read_b32 v247, v247 offset:7856\n\t"
            "s_waitcnt lgkmcnt(0)\n\t"
            "v_readfirstlane_b32 s77, v247\n\t"
            "s_cmp_ge_u32 s77, s70\n\t"
            "s_cbranch_scc0 .Lwz_spin%=\n\t"
            "s_branch .Lwz_go%=\n\t"
            ".Lwb_spin%=:\n\t"
            "s_cmp_eq_u32 s83, 0\n\t"
            "s_cbranch_scc1 .Lwb_go%=\n\t"
            "s_sub_u32 s83, s83, 1\n\t"
            "s_sleep 2\n\t"
            "v_mov_b32 v247, 0\n\t"
            "ds_read_b32 v247, v247 offset:7856\n\t"
            "s_waitcnt lgkmcnt(0)\n\t"
            "v_readfirstlane_b32 s77, v247\n\t"
            "s_cmp_ge_u32 s77, s70\n\t"
            "s_cbranch_scc0 .Lwb_spin%=\n\t"
            "s_branch .Lwb_go%=\n\t"
            ".Lwb_end%=:\n\t"
            "s_waitcnt lgkmcnt(0)\n\t"
            "s_nop 7\n\t"
            "s_setprio 1\n\t"
            "ds_read_b128 v[196:199], v252\n\t"
            "v_mov_b32 v247, 0\n\t"
            "v_and_b32 v151, 0xffff0000, v227\n\t"
            "v_lshlrev_b32 v150, 16, v227\n\t"
            "v_and_b32 v149, 0xffff0000, v226\n\t"
            "v_lshlrev_b32 v148, 16, v226\n\t"
            "v_and_b32 v147, 0xffff0000, v225\n\t"
            "v_lshlrev_b32 v146, 16, v225\n\t"
            "v_and_b32 v145, 0xffff0000, v224\n\t"
            "v_lshlrev_b32 v144, 16, v224\n\t"
            "v_and_b32 v159, 0xffff0000, v231\n\t"
            "v_lshlrev_b32 v158, 16, v231\n\t"
            "v_and_b32 v157, 0xffff0000, v230\n\t"
            "v_lshlrev_b32 v156, 16, v230\n\t"
            "v_and_b32 v155, 0xffff0000, v229\n\t"
            "v_lshlrev_b32 v154, 16, v229\n\t"
            "v_and_b32 v153, 0xffff0000, v228\n\t"
            "v_lshlrev_b32 v152, 16, v228\n\t"
            "v_and_b32 v167, 0xffff0000, v235\n\t"
            "v_lshlrev_b32 v166, 16, v235\n\t"
            "v_and_b32 v165, 0xffff0000, v234\n\t"
            "v_lshlrev_b32 v164, 16, v234\n\t"
            "v_and_b32 v163, 0xffff0000, v233\n\t"
            "v_lshlrev_b32 v162, 16, v233\n\t"
            "v_and_b32 v161, 0xffff0000, v232\n\t"
            "v_lshlrev_b32 v160, 16, v232\n\t"
            "v_and_b32 v227, 0xffff0000, v203\n\t"
            "v_lshlrev_b32 v226, 16, v203\n\t"
            "v_and_b32 v225, 0xffff0000, v202\n\t"
            "v_lshlrev_b32 v224, 16, v202\n\t"
            "v_and_b32 v231, 0xffff0000, v207\n\t"
            "v_lshlrev_b32 v230, 16, v207\n\t"
            "v_and_b32 v229, 0xffff0000, v206\n\t"
            "v_lshlrev_b32 v228, 16, v206\n\t"
            "v_and_b32 v235, 0xffff0000, v211\n\t"
            "v_lshlrev_b32 v234, 16, v211\n\t"
            "v_and_b32 v233, 0xffff0000, v210\n\t"
            "v_lshlrev_b32 v232, 16, v210\n\t"
            "v_and_b32 v203, 0xffff0000, v201\n\t"
            "v_lshlrev_b32 v202, 16, v201\n\t"
            "v_and_b32 v207, 0xffff0000, v205\n\t"
            "v_lshlrev_b32 v206, 16, v205\n\t"
            "v_and_b32 v211, 0xffff0000, v209\n\t"
            "v_lshlrev_b32 v210, 16, v209\n\t"
            "v_and_b32 v201, 0xffff0000, v200\n\t"
            "v_and_b32 v205, 0xffff0000, v204\n\t"
            "v_and_b32 v209, 0xffff0000, v208\n\t"
            "v_lshlrev_b32 v200, 16, v200\n\t"
            "v_lshlrev_b32 v204, 16, v204\n\t"
            "v_lshlrev_b32 v208, 16, v208\n\t"
            "v_and_b32 v171, 0xffff0000, v215\n\t"
            "v_lshlrev_b32 v170, 16, v215\n\t"
            "v_and_b32 v169, 0xffff0000, v214\n\t"
            "v_lshlrev_b32 v168, 16, v214\n\t"
            "v_and_b32 v179, 0xffff0000, v219\n\t"
            "v_lshlrev_b32 v178, 16, v219\n\t"
            "v_and_b32 v177, 0xffff0000, v218\n\t"
            "v_lshlrev_b32 v176, 16, v218\n\t"
            "v_and_b32 v183, 0xffff0000, v223\n\t"
            "v_lshlrev_b32 v182, 16, v223\n\t"
            "v_and_b32 v181, 0xffff0000, v222\n\t"
            "v_lshlrev_b32 v180, 16, v222\n\t"
            "v_and_b32 v215, 0xffff0000, v213\n\t"
            "v_lshlrev_b32 v214, 16, v213\n\t"
            "v_and_b32 v219, 0xffff0000, v217\n\t"
            "v_lshlrev_b32 v218, 16, v217\n\t"
            "v_and_b32 v223, 0xffff0000, v221\n\t"
            "v_lshlrev_b32 v222, 16, v221\n\t"
            "v_and_b32 v213, 0xffff0000, v212\n\t"
            "v_and_b32 v217, 0xffff0000, v216\n\t"
            "v_and_b32 v221, 0xffff0000, v220\n\t"
            "v_lshlrev_b32 v212, 16, v212\n\t"
            "v_lshlrev_b32 v216, 16, v216\n\t"
            "v_lshlrev_b32 v220, 16, v220\n\t"
            "v_add_u32 v236, s48, v244\n\t"
            "v_add_u32 v237, s48, v245\n\t"
            "v_add_u32 v248, s48, v246\n\t"
            "ds_read_b32 v247, v247 offset:7856\n\t"
            "s_waitcnt lgkmcnt(0)\n\t"
            "v_readfirstlane_b32 s77, v247\n\t"
            "s_cmp_ge_u32 s77, s70\n\t"
            "s_cbranch_scc0 .Lwz_spin%=\n\t"
            ".Lwz_go%=:\n\t"
            "s_bfe_u32 s57, s56, 0x20004\n\t"
            "s_lshl_b32 s58, s57, 3\n\t"
            "s_lshr_b32 s58, s55, s58\n\t"
            "s_lshl_b64 s[36:37], s[34:35], s58\n\t"
            "ds_read_b128 v[184:187], v242\n\t"
            "ds_read_b128 v[188:191], v243\n\t"
            "ds_read_b128 v[192:195], v242 offset:128\n\t"
            "ds_read_b128 v[172:175], v243 offset:128\n\t"
            "s_bitcmp1_b32 s57, 1\n\t"
            "s_cselect_b32 s58, 1177344, 0\n\t"
            "s_bitcmp1_b32 s57, 0\n\t"
            "s_cselect_b32 s57, 2304, 0\n\t"
            "s_add_u32 s57, s57, s58\n\t"
            "s_mov_b64 exec, s[36:37]\n\t"
            "v_add_u32 v196, s57, v196\n\t"
            "s_mov_b64 exec, -1\n\t"
            "s_add_u32 s59, s59, s84\n\t"
            "s_movk_i32 s84, 800\n\t"
            "s_waitcnt lgkmcnt(2)\n\t"
            "v_mfma_f32_16x16x32_bf16 v[200:203], v[0:3], v[184:187], v[200:203]\n\t"
            "v_mfma_f32_16x16x32_bf16 v[200:203], v[4:7], v[188:191], v[200:203]\n\t"
            "v_mfma_f32_16x16x32_bf16 v[224:227], v[28:31], v[188:191], v[224:227]\n\t"
            "v_mfma_f32_16x16x32_bf16 v[224:227], v[24:27], v[184:187], v[224:227]\n\t"
            "v_mfma_f32_16x16x32_bf16 v[204:207], v[48:51], v[184:187], v[204:207]\n\t"
            "v_mfma_f32_16x16x32_bf16 v[204:207], v[52:55], v[188:191], v[204:207]\n\t"
            "v_mfma_f32_16x16x32_bf16 v[228:231], v[76:79], v[188:191], v[228:231]\n\t"
            "v_mfma_f32_16x16x32_bf16 v[228:231], v[72:75], v[184:187], v[228:231]\n\t"
            "v_mfma_f32_16x16x32_bf16 v[208:211], v[96:99], v[184:187], v[208:211]\n\t"
            "v_mfma_f32_16x16x32_bf16 v[208:211], v[100:103], v[188:191], v[208:211]\n\t"
            "v_mfma_f32_16x16x32_bf16 v[232:235], v[124:127], v[188:191], v[232:235]\n\t"
            "ds_read_b128 v[188:191], v243 offset:256\n\t"
            "v_mfma_f32_16x16x32_bf16 v[232:235], v[120:123], v[184:187], v[232:235]\n\t"
            "ds_read_b128 v[184:187], v242 offset:256\n\t"
            "s_waitcnt lgkmcnt(2)\n\t"
            "v_mfma_f32_16x16x32_bf16 v[200:203], v[8:11], v[192:195], v[200:203]\n\t"
            "v_mfma_f32_16x16x32_bf16 v[200:203], v[12:15], v[172:175], v[200:203]\n\t"
            "v_mfma_f32_16x16x32_bf16 v[224:227], v[36:39], v[172:175], v[224:227]\n\t"
            "v_mfma_f32_16x16x32_bf16 v[224:227], v[32:35], v[192:195], v[224:227]\n\t"
            "v_mfma_f32_16x16x32_bf16 v[204:207], v[56:59], v[192:195], v[204:207]\n\t"
            "v_mfma_f32_16x16x32_bf16 v[204:207], v[60:63], v[172:175], v[204:207]\n\t"
            "v_mfma_f32_16x16x32_bf16 v[228:231], v[84:87], v[172:175], v[228:231]\n\t"
            "v_mfma_f32_16x16x32_bf16 v[228:231], v[80:83], v[192:195], v[228:231]\n\t"
            "v_mfma_f32_16x16x32_bf16 v[208:211], v[104:107], v[192:195], v[208:211]\n\t"
            "v_mfma_f32_16x16x32_bf16 v[208:211], v[108:111], v[172:175], v[208:211]\n\t"
            "v_mfma_f32_16x16x32_bf16 v[232:235], v[132:135], v[172:175], v[232:235]\n\t"
            "ds_read_b128 v[172:175], v243 offset:6144\n\t"
            "v_mfma_f32_16x16x32_bf16 v[232:235], v[128:131], v[192:195], v[232:235]\n\t"
            "ds_read_b128 v[192:195], v242 offset:6144\n\t"
            "s_waitcnt lgkmcnt(2)\n\t"
            "v_mfma_f32_16x16x32_bf16 v[200:203], v[16:19], v[184:187], v[200:203]\n\t"
            "v_mfma_f32_16x16x32_bf16 v[200:203], v[20:23], v[188:191], v[200:203]\n\t"
            "v_mfma_f32_16x16x32_bf16 v[224:227], v[44:47], v[188:191], v[224:227]\n\t"
            "v_mfma_f32_16x16x32_bf16 v[224:227], v[40:43], v[184:187], v[224:227]\n\t"
            "v_mfma_f32_16x16x32_bf16 v[204:207], v[64:67], v[184:187], v[204:207]\n\t"
            "v_mfma_f32_16x16x32_bf16 v[204:207], v[68:71], v[188:191], v[204:207]\n\t"
            "v_mfma_f32_16x16x32_bf16 v[228:231], v[92:95], v[188:191], v[228:231]\n\t"
            "v_mfma_f32_16x16x32_bf16 v[228:231], v[88:91], v[184:187], v[228:231]\n\t"
            "s_nop 1\n\t"
            "v_cvt_pk_bf16_f32 v200, v200, v201\n\t"
            "v_mfma_f32_16x16x32_bf16 v[208:211], v[112:115], v[184:187], v[208:211]\n\t"
            "v_cvt_pk_bf16_f32 v201, v202, v203\n\t"
            "v_mfma_f32_16x16x32_bf16 v[208:211], v[116:119], v[188:191], v[208:211]\n\t"
            "v_cvt_pk_bf16_f32 v202, v224, v225\n\t"
            "v_cvt_pk_bf16_f32 v203, v226, v227\n\t"
            "v_mfma_f32_16x16x32_bf16 v[232:235], v[140:143], v[188:191], v[232:235]\n\t"
            "ds_read_b128 v[188:191], v243 offset:6272\n\t"
            "ds_read_b128 v[224:227], v236 offset:12288\n\t"
            "v_mfma_f32_16x16x32_bf16 v[232:235], v[136:139], v[184:187], v[232:235]\n\t"
            "ds_read_b128 v[184:187], v242 offset:6272\n\t"
            "buffer_store_dwordx4 v[200:203], v196, s[40:43], s45 offen sc1\n\t"
            "s_waitcnt lgkmcnt(3)\n\t"
            "v_mfma_f32_16x16x32_bf16 v[212:215], v[0:3], v[192:195], v[212:215]\n\t"
            "ds_read_b128 v[200:203], v236\n\t"
            "v_cvt_pk_bf16_f32 v204, v204, v205\n\t"
            "v_mfma_f32_16x16x32_bf16 v[212:215], v[4:7], v[172:175], v[212:215]\n\t"
            "v_cvt_pk_bf16_f32 v205, v206, v207\n\t"
            "v_cvt_pk_bf16_f32 v206, v228, v229\n\t"
            "v_mfma_f32_16x16x32_bf16 v[168:171], v[28:31], v[172:175], v[168:171]\n\t"
            "v_cvt_pk_bf16_f32 v207, v230, v231\n\t"
            "v_cvt_pk_bf16_f32 v208, v208, v209\n\t"
            "v_mfma_f32_16x16x32_bf16 v[168:171], v[24:27], v[192:195], v[168:171]\n\t"
            "ds_read_b128 v[228:231], v237 offset:12288\n\t"
            "buffer_store_dwordx4 v[204:207], v196, s[40:43], s45 offen offset:64 sc1\n\t"
            "v_mfma_f32_16x16x32_bf16 v[216:219], v[48:51], v[192:195], v[216:219]\n\t"
            "s_nop 0\n\t"
            "ds_read_b128 v[204:207], v237\n\t"
            "v_cvt_pk_bf16_f32 v209, v210, v211\n\t"
            "v_mfma_f32_16x16x32_bf16 v[216:219], v[52:55], v[172:175], v[216:219]\n\t"
            "v_cvt_pk_bf16_f32 v210, v232, v233\n\t"
            "v_cvt_pk_bf16_f32 v211, v234, v235\n\t"
            "v_mfma_f32_16x16x32_bf16 v[176:179], v[76:79], v[172:175], v[176:179]\n\t"
            "ds_read_b128 v[232:235], v248 offset:12288\n\t"
            "v_mfma_f32_16x16x32_bf16 v[176:179], v[72:75], v[192:195], v[176:179]\n\t"
            "buffer_store_dwordx4 v[208:211], v196, s[40:43], s45 offen offset:128 sc1\n\t"
            "v_mfma_f32_16x16x32_bf16 v[220:223], v[96:99], v[192:195], v[220:223]\n\t"
            "s_nop 0\n\t"
            "ds_read_b128 v[208:211], v248\n\t"
            "v_mfma_f32_16x16x32_bf16 v[220:223], v[100:103], v[172:175], v[220:223]\n\t"
            "v_mfma_f32_16x16x32_bf16 v[180:183], v[124:127], v[172:175], v[180:183]\n\t"
            "ds_read_b128 v[172:175], v243 offset:6400\n\t"
            "v_mfma_f32_16x16x32_bf16 v[180:183], v[120:123], v[192:195], v[180:183]\n\t"
            "ds_read_b128 v[192:195], v242 offset:6400\n\t"
            "s_waitcnt lgkmcnt(7)\n\t"
            "v_mfma_f32_16x16x32_bf16 v[212:215], v[8:11], v[184:187], v[212:215]\n\t"
            "v_mfma_f32_16x16x32_bf16 v[212:215], v[12:15], v[188:191], v[212:215]\n\t"
            "v_mfma_f32_16x16x32_bf16 v[168:171], v[36:39], v[188:191], v[168:171]\n\t"
            "v_mfma_f32_16x16x32_bf16 v[168:171], v[32:35], v[184:187], v[168:171]\n\t"
            "v_mfma_f32_16x16x32_bf16 v[216:219], v[56:59], v[184:187], v[216:219]\n\t"
            "v_mfma_f32_16x16x32_bf16 v[216:219], v[60:63], v[188:191], v[216:219]\n\t"
            "v_mfma_f32_16x16x32_bf16 v[176:179], v[84:87], v[188:191], v[176:179]\n\t"
            "v_mfma_f32_16x16x32_bf16 v[176:179], v[80:83], v[184:187], v[176:179]\n\t"
            "v_mfma_f32_16x16x32_bf16 v[220:223], v[104:107], v[184:187], v[220:223]\n\t"
            "v_mfma_f32_16x16x32_bf16 v[220:223], v[108:111], v[188:191], v[220:223]\n\t"
            "v_mfma_f32_16x16x32_bf16 v[180:183], v[132:135], v[188:191], v[180:183]\n\t"
            "ds_read_b128 v[188:191], v243 offset:12288\n\t"
            "v_mfma_f32_16x16x32_bf16 v[180:183], v[128:131], v[184:187], v[180:183]\n\t"
            "ds_read_b128 v[184:187], v242 offset:12288\n\t"
            "s_waitcnt lgkmcnt(2)\n\t"
            "v_mfma_f32_16x16x32_bf16 v[168:171], v[44:47], v[172:175], v[168:171]\n\t"
            "v_mfma_f32_16x16x32_bf16 v[168:171], v[40:43], v[192:195], v[168:171]\n\t"
            "v_mfma_f32_16x16x32_bf16 v[176:179], v[92:95], v[172:175], v[176:179]\n\t"
            "v_mfma_f32_16x16x32_bf16 v[176:179], v[88:91], v[192:195], v[176:179]\n\t"
            "v_mfma_f32_16x16x32_bf16 v[180:183], v[140:143], v[172:175], v[180:183]\n\t"
            "v_mfma_f32_16x16x32_bf16 v[180:183], v[136:139], v[192:195], v[180:183]\n\t"
            "v_mfma_f32_16x16x32_bf16 v[212:215], v[16:19], v[192:195], v[212:215]\n\t"
            "v_mfma_f32_16x16x32_bf16 v[212:215], v[20:23], v[172:175], v[212:215]\n\t"
            "s_nop 1\n\t"
            "v_cvt_pk_bf16_f32 v171, v170, v171\n\t"
            "v_mfma_f32_16x16x32_bf16 v[216:219], v[64:67], v[192:195], v[216:219]\n\t"
            "v_cvt_pk_bf16_f32 v170, v168, v169\n\t"
            "v_mfma_f32_16x16x32_bf16 v[216:219], v[68:71], v[172:175], v[216:219]\n\t"
            "v_cvt_pk_bf16_f32 v179, v178, v179\n\t"
            "v_cvt_pk_bf16_f32 v178, v176, v177\n\t"
            "v_mfma_f32_16x16x32_bf16 v[220:223], v[112:115], v[192:195], v[220:223]\n\t"
            "ds_read_b128 v[192:195], v242 offset:12416\n\t"
            "v_cvt_pk_bf16_f32 v183, v182, v183\n\t"
            "v_mfma_f32_16x16x32_bf16 v[220:223], v[116:119], v[172:175], v[220:223]\n\t"
            "ds_read_b128 v[172:175], v243 offset:12416\n\t"
            "v_cvt_pk_bf16_f32 v182, v180, v181\n\t"
            "s_waitcnt lgkmcnt(2)\n\t"
            "v_mfma_f32_16x16x32_bf16 v[144:147], v[0:3], v[184:187], v[144:147]\n\t"
            "v_cvt_pk_bf16_f32 v168, v212, v213\n\t"
            "v_cvt_pk_bf16_f32 v169, v214, v215\n\t"
            "v_mfma_f32_16x16x32_bf16 v[144:147], v[4:7], v[188:191], v[144:147]\n\t"
            "ds_read_b128 v[212:215], v236 offset:6144\n\t"
            "v_cvt_pk_bf16_f32 v176, v216, v217\n\t"
            "v_mfma_f32_16x16x32_bf16 v[148:151], v[28:31], v[188:191], v[148:151]\n\t"
            "v_cvt_pk_bf16_f32 v177, v218, v219\n\t"
            "v_cvt_pk_bf16_f32 v180, v220, v221\n\t"
            "v_mfma_f32_16x16x32_bf16 v[148:151], v[24:27], v[184:187], v[148:151]\n\t"
            "ds_read_b128 v[216:219], v237 offset:6144\n\t"
            "v_cvt_pk_bf16_f32 v181, v222, v223\n\t"
            "v_mfma_f32_16x16x32_bf16 v[152:155], v[48:51], v[184:187], v[152:155]\n\t"
            "ds_read_b128 v[220:223], v248 offset:6144\n\t"
            "v_mfma_f32_16x16x32_bf16 v[152:155], v[52:55], v[188:191], v[152:155]\n\t"
            "buffer_store_dwordx4 v[168:171], v197, s[40:43], s45 offen sc1\n\t"
            "v_mfma_f32_16x16x32_bf16 v[156:159], v[76:79], v[188:191], v[156:159]\n\t"
            "v_mfma_f32_16x16x32_bf16 v[156:159], v[72:75], v[184:187], v[156:159]\n\t"
            "buffer_store_dwordx4 v[176:179], v197, s[40:43], s45 offen offset:64 sc1\n\t"
            "v_mfma_f32_16x16x32_bf16 v[160:163], v[96:99], v[184:187], v[160:163]\n\t"
            "v_mfma_f32_16x16x32_bf16 v[160:163], v[100:103], v[188:191], v[160:163]\n\t"
            "buffer_store_dwordx4 v[180:183], v197, s[40:43], s45 offen offset:128 sc1\n\t"
            "v_mfma_f32_16x16x32_bf16 v[164:167], v[124:127], v[188:191], v[164:167]\n\t"
            "ds_read_b128 v[188:191], v243 offset:12544\n\t"
            "v_mfma_f32_16x16x32_bf16 v[164:167], v[120:123], v[184:187], v[164:167]\n\t"
            "ds_read_b128 v[184:187], v242 offset:12544\n\t"
            "s_waitcnt lgkmcnt(5)\n\t"
            "v_mfma_f32_16x16x32_bf16 v[144:147], v[8:11], v[192:195], v[144:147]\n\t"
            "v_mfma_f32_16x16x32_bf16 v[144:147], v[12:15], v[172:175], v[144:147]\n\t"
            "v_mfma_f32_16x16x32_bf16 v[148:151], v[36:39], v[172:175], v[148:151]\n\t"
            "v_mfma_f32_16x16x32_bf16 v[148:151], v[32:35], v[192:195], v[148:151]\n\t"
            "s_waitcnt lgkmcnt(0)\n\t"
            "s_barrier\n\t"
            "s_setprio 3\n\t"
            "v_mfma_f32_16x16x32_bf16 v[152:155], v[56:59], v[192:195], v[152:155]\n\t"
            "v_mfma_f32_16x16x32_bf16 v[152:155], v[60:63], v[172:175], v[152:155]\n\t"
            "v_mfma_f32_16x16x32_bf16 v[156:159], v[84:87], v[172:175], v[156:159]\n\t"
            "v_mfma_f32_16x16x32_bf16 v[156:159], v[80:83], v[192:195], v[156:159]\n\t"
            "v_mfma_f32_16x16x32_bf16 v[144:147], v[16:19], v[184:187], v[144:147]\n\t"
            "v_mfma_f32_16x16x32_bf16 v[144:147], v[20:23], v[188:191], v[144:147]\n\t"
            "v_mfma_f32_16x16x32_bf16 v[148:151], v[44:47], v[188:191], v[148:151]\n\t"
            "v_mfma_f32_16x16x32_bf16 v[148:151], v[40:43], v[184:187], v[148:151]\n\t"
            "v_mfma_f32_16x16x32_bf16 v[160:163], v[104:107], v[192:195], v[160:163]\n\t"
            "v_mfma_f32_16x16x32_bf16 v[160:163], v[108:111], v[172:175], v[160:163]\n\t"
            "v_mfma_f32_16x16x32_bf16 v[164:167], v[132:135], v[172:175], v[164:167]\n\t"
            "v_mfma_f32_16x16x32_bf16 v[164:167], v[128:131], v[192:195], v[164:167]\n\t"
            "s_nop 1\n\t"
            "v_cvt_pk_bf16_f32 v144, v144, v145\n\t"
            "v_mfma_f32_16x16x32_bf16 v[152:155], v[64:67], v[184:187], v[152:155]\n\t"
            "v_cvt_pk_bf16_f32 v145, v146, v147\n\t"
            "v_mfma_f32_16x16x32_bf16 v[152:155], v[68:71], v[188:191], v[152:155]\n\t"
            "v_cvt_pk_bf16_f32 v146, v148, v149\n\t"
            "v_cvt_pk_bf16_f32 v147, v150, v151\n\t"
            "v_mfma_f32_16x16x32_bf16 v[156:159], v[92:95], v[188:191], v[156:159]\n\t"
            "v_mfma_f32_16x16x32_bf16 v[156:159], v[88:91], v[184:187], v[156:159]\n\t"
            "buffer_store_dwordx4 v[144:147], v198, s[40:43], s45 offen sc1\n\t"
            "v_mfma_f32_16x16x32_bf16 v[160:163], v[112:115], v[184:187], v[160:163]\n\t"
            "s_nop 1\n\t"
            "v_cvt_pk_bf16_f32 v152, v152, v153\n\t"
            "v_mfma_f32_16x16x32_bf16 v[160:163], v[116:119], v[188:191], v[160:163]\n\t"
            "v_cvt_pk_bf16_f32 v153, v154, v155\n\t"
            "v_mfma_f32_16x16x32_bf16 v[164:167], v[140:143], v[188:191], v[164:167]\n\t"
            "v_cvt_pk_bf16_f32 v154, v156, v157\n\t"
            "v_mfma_f32_16x16x32_bf16 v[164:167], v[136:139], v[184:187], v[164:167]\n\t"
            "v_cvt_pk_bf16_f32 v155, v158, v159\n\t"
            "buffer_store_dwordx4 v[152:155], v198, s[40:43], s45 offen offset:64 sc1\n\t"
            "s_nop 1\n\t"
            "v_cvt_pk_bf16_f32 v160, v160, v161\n\t"
            "v_cvt_pk_bf16_f32 v161, v162, v163\n\t"
            "s_nop 1\n\t"
            "v_cvt_pk_bf16_f32 v162, v164, v165\n\t"
            "v_cvt_pk_bf16_f32 v163, v166, v167\n\t"
            "buffer_store_dwordx4 v[160:163], v198, s[40:43], s45 offen offset:128 sc1\n\t"
            "s_add_u32 s45, s46, s27\n\t"
            "s_mov_b32 s46, s47\n\t"
            "s_mov_b32 s47, s68\n\t"
            "s_xor_b32 s48, s48, s71\n\t"
            "s_xor_b32 s67, s67, s71\n\t"
            "s_add_u32 s76, s76, 4\n\t"
            "s_add_u32 s70, s70, 4\n\t"
            "s_lshl_b32 s56, s56, 2\n\t"
            "s_lshr_b32 s49, s26, 30\n\t"
            "s_or_b32 s56, s56, s49\n\t"
            "s_nop 7\n\t"
            "ds_read_b128 v[196:199], v252\n\t"
            "s_bfe_u32 s57, s56, 0x20004\n\t"
            "s_lshl_b32 s58, s57, 3\n\t"
            "s_lshr_b32 s58, s55, s58\n\t"
            "s_lshl_b64 s[36:37], s[34:35], s58\n\t"
            "s_bitcmp1_b32 s57, 1\n\t"
            "s_cselect_b32 s58, 1177344, 0\n\t"
            "s_bitcmp1_b32 s57, 0\n\t"
            "s_cselect_b32 s57, 2304, 0\n\t"
            "s_add_u32 s57, s57, s58\n\t"
            "v_and_b32 v151, 0xffff0000, v227\n\t"
            "v_lshlrev_b32 v150, 16, v227\n\t"
            "v_and_b32 v149, 0xffff0000, v226\n\t"
            "v_lshlrev_b32 v148, 16, v226\n\t"
            "v_and_b32 v147, 0xffff0000, v225\n\t"
            "v_lshlrev_b32 v146, 16, v225\n\t"
            "v_and_b32 v145, 0xffff0000, v224\n\t"
            "v_lshlrev_b32 v144, 16, v224\n\t"
            "v_and_b32 v159, 0xffff0000, v231\n\t"
            "v_lshlrev_b32 v158, 16, v231\n\t"
            "v_and_b32 v157, 0xffff0000, v230\n\t"
            "v_lshlrev_b32 v156, 16, v230\n\t"
            "v_and_b32 v155, 0xffff0000, v229\n\t"
            "v_lshlrev_b32 v154, 16, v229\n\t"
            "v_and_b32 v153, 0xffff0000, v228\n\t"
            "v_lshlrev_b32 v152, 16, v228\n\t"
            "v_and_b32 v167, 0xffff0000, v235\n\t"
            "v_lshlrev_b32 v166, 16, v235\n\t"
            "v_and_b32 v165, 0xffff0000, v234\n\t"
            "v_lshlrev_b32 v164, 16, v234\n\t"
            "v_and_b32 v163, 0xffff0000, v233\n\t"
            "s_waitcnt lgkmcnt(0)\n\t"
            "s_mov_b64 exec, s[36:37]\n\t"
            "v_add_u32 v196, s57, v196\n\t"
            "s_mov_b64 exec, -1\n\t"
            "v_lshlrev_b32 v162, 16, v233\n\t"
            "v_and_b32 v161, 0xffff0000, v232\n\t"
            "v_lshlrev_b32 v160, 16, v232\n\t"
            ".Lwk_t1%=:\n\t"
            "v_mov_b32 v247, 0\n\t"
            "ds_read_b32 v247, v247 offset:7856\n\t"
            "s_waitcnt lgkmcnt(0)\n\t"
            "v_readfirstlane_b32 s77, v247\n\t"
            "s_cmp_ge_u32 s77, s70\n\t"
            "s_cbranch_scc1 .Lwk_t2%=\n\t"
            "s_cmp_eq_u32 s83, 0\n\t"
            "s_cbranch_scc1 .Lwk_t2%=\n\t"
            "s_sub_u32 s83, s83, 1\n\t"
            "s_sleep 1\n\t"
            "s_branch .Lwk_t1%=\n\t"
            ".Lwk_t2%=:\n\t"
            "v_and_b32 v227, 0xffff0000, v203\n\t"
            "v_lshlrev_b32 v226, 16, v203\n\t"
            "v_and_b32 v225, 0xffff0000, v202\n\t"
            "v_lshlrev_b32 v224, 16, v202\n\t"
            "v_and_b32 v231, 0xffff0000, v207\n\t"
            "v_lshlrev_b32 v230, 16, v207\n\t"
            "v_and_b32 v229, 0xffff0000, v206\n\t"
            "v_lshlrev_b32 v228, 16, v206\n\t"
            "v_and_b32 v235, 0xffff0000, v211\n\t"
            "v_lshlrev_b32 v234, 16, v211\n\t"
            "v_and_b32 v233, 0xffff0000, v210\n\t"
            "v_lshlrev_b32 v232, 16, v210\n\t"
            "v_and_b32 v203, 0xffff0000, v201\n\t"
            "v_lshlrev_b32 v202, 16, v201\n\t"
            "v_and_b32 v207, 0xffff0000, v205\n\t"
            "v_lshlrev_b32 v206, 16, v205\n\t"
            "v_and_b32 v211, 0xffff0000, v209\n\t"
            "v_lshlrev_b32 v210, 16, v209\n\t"
            "v_and_b32 v201, 0xffff0000, v200\n\t"
            "v_and_b32 v205, 0xffff0000, v204\n\t"
            "v_and_b32 v209, 0xffff0000, v208\n\t"
            "v_lshlrev_b32 v200, 16, v200\n\t"
            "v_lshlrev_b32 v204, 16, v204\n\t"
            "v_lshlrev_b32 v208, 16, v208\n\t"
            "v_and_b32 v171, 0xffff0000, v215\n\t"
            "v_lshlrev_b32 v170, 16, v215\n\t"
            "v_and_b32 v169, 0xffff0000, v214\n\t"
            "v_lshlrev_b32 v168, 16, v214\n\t"
            "v_and_b32 v179, 0xffff0000, v219\n\t"
            "v_lshlrev_b32 v178, 16, v219\n\t"
            "v_and_b32 v177, 0xffff0000, v218\n\t"
            "v_lshlrev_b32 v176, 16, v218\n\t"
            "v_and_b32 v183, 0xffff0000, v223\n\t"
            "v_lshlrev_b32 v182, 16, v223\n\t"
            "v_and_b32 v181, 0xffff0000, v222\n\t"
            "v_lshlrev_b32 v180, 16, v222\n\t"
            "v_and_b32 v215, 0xffff0000, v213\n\t"
            "v_lshlrev_b32 v214, 16, v213\n\t"
            "v_and_b32 v219, 0xffff0000, v217\n\t"
            "v_lshlrev_b32 v218, 16, v217\n\t"
            "v_and_b32 v223, 0xffff0000, v221\n\t"
            "v_lshlrev_b32 v222, 16, v221\n\t"
            "v_and_b32 v213, 0xffff0000, v212\n\t"
            "v_and_b32 v217, 0xffff0000, v216\n\t"
            "v_and_b32 v221, 0xffff0000, v220\n\t"
            "v_lshlrev_b32 v212, 16, v212\n\t"
            "v_lshlrev_b32 v216, 16, v216\n\t"
            "v_lshlrev_b32 v220, 16, v220\n\t"
            "ds_read_b128 v[184:187], v242\n\t"
            "ds_read_b128 v[188:191], v243\n\t"
            "ds_read_b128 v[192:195], v242 offset:128\n\t"
            "ds_read_b128 v[172:175], v243 offset:128\n\t"
            "s_waitcnt lgkmcnt(2)\n\t"
            "v_mfma_f32_16x16x32_bf16 v[200:203], v[0:3], v[184:187], v[200:203]\n\t"
            "v_mfma_f32_16x16x32_bf16 v[200:203], v[4:7], v[188:191], v[200:203]\n\t"
            "v_mfma_f32_16x16x32_bf16 v[224:227], v[28:31], v[188:191], v[224:227]\n\t"
            "v_mfma_f32_16x16x32_bf16 v[224:227], v[24:27], v[184:187], v[224:227]\n\t"
            "v_mfma_f32_16x16x32_bf16 v[204:207], v[48:51], v[184:187], v[204:207]\n\t"
            "v_mfma_f32_16x16x32_bf16 v[204:207], v[52:55], v[188:191], v[204:207]\n\t"
            "v_mfma_f32_16x16x32_bf16 v[228:231], v[76:79], v[188:191], v[228:231]\n\t"
            "v_mfma_f32_16x16x32_bf16 v[228:231], v[72:75], v[184:187], v[228:231]\n\t"
            "v_mfma_f32_16x16x32_bf16 v[208:211], v[96:99], v[184:187], v[208:211]\n\t"
            "v_mfma_f32_16x16x32_bf16 v[208:211], v[100:103], v[188:191], v[208:211]\n\t"
            "v_mfma_f32_16x16x32_bf16 v[232:235], v[124:127], v[188:191], v[232:235]\n\t"
            "ds_read_b128 v[188:191], v243 offset:256\n\t"
            "v_mfma_f32_16x16x32_bf16 v[232:235], v[120:123], v[184:187], v[232:235]\n\t"
            "ds_read_b128 v[184:187], v242 offset:256\n\t"
            "s_waitcnt lgkmcnt(2)\n\t"
            "v_mfma_f32_16x16x32_bf16 v[200:203], v[8:11], v[192:195], v[200:203]\n\t"
            "v_mfma_f32_16x16x32_bf16 v[200:203], v[12:15], v[172:175], v[200:203]\n\t"
            "v_mfma_f32_16x16x32_bf16 v[224:227], v[36:39], v[172:175], v[224:227]\n\t"
            "v_mfma_f32_16x16x32_bf16 v[224:227], v[32:35], v[192:195], v[224:227]\n\t"
            "v_mfma_f32_16x16x32_bf16 v[204:207], v[56:59], v[192:195], v[204:207]\n\t"
            "v_mfma_f32_16x16x32_bf16 v[204:207], v[60:63], v[172:175], v[204:207]\n\t"
            "v_mfma_f32_16x16x32_bf16 v[228:231], v[84:87], v[172:175], v[228:231]\n\t"
            "v_mfma_f32_16x16x32_bf16 v[228:231], v[80:83], v[192:195], v[228:231]\n\t"
            "v_mfma_f32_16x16x32_bf16 v[208:211], v[104:107], v[192:195], v[208:211]\n\t"
            "v_mfma_f32_16x16x32_bf16 v[208:211], v[108:111], v[172:175], v[208:211]\n\t"
            "v_mfma_f32_16x16x32_bf16 v[232:235], v[132:135], v[172:175], v[232:235]\n\t"
            "ds_read_b128 v[172:175], v243 offset:6144\n\t"
            "v_mfma_f32_16x16x32_bf16 v[232:235], v[128:131], v[192:195], v[232:235]\n\t"
            "ds_read_b128 v[192:195], v242 offset:6144\n\t"
            "s_waitcnt lgkmcnt(2)\n\t"
            "v_mfma_f32_16x16x32_bf16 v[200:203], v[16:19], v[184:187], v[200:203]\n\t"
            "v_mfma_f32_16x16x32_bf16 v[200:203], v[20:23], v[188:191], v[200:203]\n\t"
            "v_mfma_f32_16x16x32_bf16 v[224:227], v[44:47], v[188:191], v[224:227]\n\t"
            "v_mfma_f32_16x16x32_bf16 v[224:227], v[40:43], v[184:187], v[224:227]\n\t"
            "v_mfma_f32_16x16x32_bf16 v[204:207], v[64:67], v[184:187], v[204:207]\n\t"
            "v_mfma_f32_16x16x32_bf16 v[204:207], v[68:71], v[188:191], v[204:207]\n\t"
            "v_mfma_f32_16x16x32_bf16 v[228:231], v[92:95], v[188:191], v[228:231]\n\t"
            "v_mfma_f32_16x16x32_bf16 v[228:231], v[88:91], v[184:187], v[228:231]\n\t"
            "s_nop 1\n\t"
            "v_cvt_pk_bf16_f32 v200, v200, v201\n\t"
            "v_mfma_f32_16x16x32_bf16 v[208:211], v[112:115], v[184:187], v[208:211]\n\t"
            "v_cvt_pk_bf16_f32 v201, v202, v203\n\t"
            "v_mfma_f32_16x16x32_bf16 v[208:211], v[116:119], v[188:191], v[208:211]\n\t"
            "v_cvt_pk_bf16_f32 v202, v224, v225\n\t"
            "v_cvt_pk_bf16_f32 v203, v226, v227\n\t"
            "v_mfma_f32_16x16x32_bf16 v[232:235], v[140:143], v[188:191], v[232:235]\n\t"
            "ds_read_b128 v[188:191], v243 offset:6272\n\t"
            "v_cvt_pk_bf16_f32 v204, v204, v205\n\t"
            "v_mfma_f32_16x16x32_bf16 v[232:235], v[136:139], v[184:187], v[232:235]\n\t"
            "ds_read_b128 v[184:187], v242 offset:6272\n\t"
            "buffer_store_dwordx4 v[200:203], v196, s[40:43], s45 offen sc1\n\t"
            "s_waitcnt lgkmcnt(2)\n\t"
            "v_mfma_f32_16x16x32_bf16 v[212:215], v[0:3], v[192:195], v[212:215]\n\t"
            "v_cvt_pk_bf16_f32 v205, v206, v207\n\t"
            "v_cvt_pk_bf16_f32 v206, v228, v229\n\t"
            "v_mfma_f32_16x16x32_bf16 v[212:215], v[4:7], v[172:175], v[212:215]\n\t"
            "v_cvt_pk_bf16_f32 v207, v230, v231\n\t"
            "v_cvt_pk_bf16_f32 v208, v208, v209\n\t"
            "v_mfma_f32_16x16x32_bf16 v[168:171], v[28:31], v[172:175], v[168:171]\n\t"
            "v_cvt_pk_bf16_f32 v209, v210, v211\n\t"
            "v_cvt_pk_bf16_f32 v210, v232, v233\n\t"
            "v_mfma_f32_16x16x32_bf16 v[168:171], v[24:27], v[192:195], v[168:171]\n\t"
            "buffer_store_dwordx4 v[204:207], v196, s[40:43], s45 offen offset:64 sc1\n\t"
            "v_mfma_f32_16x16x32_bf16 v[216:219], v[48:51], v[192:195], v[216:219]\n\t"
            "v_cvt_pk_bf16_f32 v211, v234, v235\n\t"
            "v_mfma_f32_16x16x32_bf16 v[216:219], v[52:55], v[172:175], v[216:219]\n\t"
            "buffer_store_dwordx4 v[208:211], v196, s[40:43], s45 offen offset:128 sc1\n\t"
            "v_mfma_f32_16x16x32_bf16 v[176:179], v[76:79], v[172:175], v[176:179]\n\t"
            "v_mfma_f32_16x16x32_bf16 v[176:179], v[72:75], v[192:195], v[176:179]\n\t"
            "v_mfma_f32_16x16x32_bf16 v[220:223], v[96:99], v[192:195], v[220:223]\n\t"
            "v_mfma_f32_16x16x32_bf16 v[220:223], v[100:103], v[172:175], v[220:223]\n\t"
            "v_mfma_f32_16x16x32_bf16 v[180:183], v[124:127], v[172:175], v[180:183]\n\t"
            "ds_read_b128 v[172:175], v243 offset:6400\n\t"
            "v_mfma_f32_16x16x32_bf16 v[180:183], v[120:123], v[192:195], v[180:183]\n\t"
            "ds_read_b128 v[192:195], v242 offset:6400\n\t"
            "s_waitcnt lgkmcnt(2)\n\t"
            "v_mfma_f32_16x16x32_bf16 v[212:215], v[8:11], v[184:187], v[212:215]\n\t"
            "v_mfma_f32_16x16x32_bf16 v[212:215], v[12:15], v[188:191], v[212:215]\n\t"
            "v_mfma_f32_16x16x32_bf16 v[168:171], v[36:39], v[188:191], v[168:171]\n\t"
            "v_mfma_f32_16x16x32_bf16 v[168:171], v[32:35], v[184:187], v[168:171]\n\t"
            "v_mfma_f32_16x16x32_bf16 v[216:219], v[56:59], v[184:187], v[216:219]\n\t"
            "v_mfma_f32_16x16x32_bf16 v[216:219], v[60:63], v[188:191], v[216:219]\n\t"
            "v_mfma_f32_16x16x32_bf16 v[176:179], v[84:87], v[188:191], v[176:179]\n\t"
            "v_mfma_f32_16x16x32_bf16 v[176:179], v[80:83], v[184:187], v[176:179]\n\t"
            "v_mfma_f32_16x16x32_bf16 v[220:223], v[104:107], v[184:187], v[220:223]\n\t"
            "v_mfma_f32_16x16x32_bf16 v[220:223], v[108:111], v[188:191], v[220:223]\n\t"
            "v_mfma_f32_16x16x32_bf16 v[180:183], v[132:135], v[188:191], v[180:183]\n\t"
            "ds_read_b128 v[188:191], v243 offset:12288\n\t"
            "v_mfma_f32_16x16x32_bf16 v[180:183], v[128:131], v[184:187], v[180:183]\n\t"
            "ds_read_b128 v[184:187], v242 offset:12288\n\t"
            "s_waitcnt lgkmcnt(2)\n\t"
            "v_mfma_f32_16x16x32_bf16 v[212:215], v[16:19], v[192:195], v[212:215]\n\t"
            "v_mfma_f32_16x16x32_bf16 v[212:215], v[20:23], v[172:175], v[212:215]\n\t"
            "v_mfma_f32_16x16x32_bf16 v[168:171], v[44:47], v[172:175], v[168:171]\n\t"
            "v_mfma_f32_16x16x32_bf16 v[168:171], v[40:43], v[192:195], v[168:171]\n\t"
            "v_mfma_f32_16x16x32_bf16 v[216:219], v[64:67], v[192:195], v[216:219]\n\t"
            "v_mfma_f32_16x16x32_bf16 v[216:219], v[68:71], v[172:175], v[216:219]\n\t"
            "v_mfma_f32_16x16x32_bf16 v[176:179], v[92:95], v[172:175], v[176:179]\n\t"
            "v_mfma_f32_16x16x32_bf16 v[176:179], v[88:91], v[192:195], v[176:179]\n\t"
            "v_mfma_f32_16x16x32_bf16 v[220:223], v[112:115], v[192:195], v[220:223]\n\t"
            "v_mfma_f32_16x16x32_bf16 v[220:223], v[116:119], v[172:175], v[220:223]\n\t"
            "s_nop 1\n\t"
            "v_cvt_pk_bf16_f32 v171, v170, v171\n\t"
            "v_mfma_f32_16x16x32_bf16 v[180:183], v[140:143], v[172:175], v[180:183]\n\t"
            "ds_read_b128 v[172:175], v243 offset:12416\n\t"
            "v_cvt_pk_bf16_f32 v170, v168, v169\n\t"
            "v_mfma_f32_16x16x32_bf16 v[180:183], v[136:139], v[192:195], v[180:183]\n\t"
            "ds_read_b128 v[192:195], v242 offset:12416\n\t"
            "v_cvt_pk_bf16_f32 v168, v212, v213\n\t"
            "s_waitcnt lgkmcnt(2)\n\t"
            "v_mfma_f32_16x16x32_bf16 v[144:147], v[0:3], v[184:187], v[144:147]\n\t"
            "v_cvt_pk_bf16_f32 v169, v214, v215\n\t"
            "v_cvt_pk_bf16_f32 v179, v178, v179\n\t"
            "v_mfma_f32_16x16x32_bf16 v[144:147], v[4:7], v[188:191], v[144:147]\n\t"
            "buffer_store_dwordx4 v[168:171], v197, s[40:43], s45 offen sc1\n\t"
            "v_mfma_f32_16x16x32_bf16 v[148:151], v[28:31], v[188:191], v[148:151]\n\t"
            "v_cvt_pk_bf16_f32 v178, v176, v177\n\t"
            "v_cvt_pk_bf16_f32 v176, v216, v217\n\t"
            "v_mfma_f32_16x16x32_bf16 v[148:151], v[24:27], v[184:187], v[148:151]\n\t"
            "v_cvt_pk_bf16_f32 v177, v218, v219\n\t"
            "v_cvt_pk_bf16_f32 v183, v182, v183\n\t"
            "v_mfma_f32_16x16x32_bf16 v[152:155], v[48:51], v[184:187], v[152:155]\n\t"
            "v_cvt_pk_bf16_f32 v182, v180, v181\n\t"
            "v_cvt_pk_bf16_f32 v180, v220, v221\n\t"
            "v_mfma_f32_16x16x32_bf16 v[152:155], v[52:55], v[188:191], v[152:155]\n\t"
            "buffer_store_dwordx4 v[176:179], v197, s[40:43], s45 offen offset:64 sc1\n\t"
            "v_mfma_f32_16x16x32_bf16 v[156:159], v[76:79], v[188:191], v[156:159]\n\t"
            "v_cvt_pk_bf16_f32 v181, v222, v223\n\t"
            "v_mfma_f32_16x16x32_bf16 v[156:159], v[72:75], v[184:187], v[156:159]\n\t"
            "buffer_store_dwordx4 v[180:183], v197, s[40:43], s45 offen offset:128 sc1\n\t"
            "v_mfma_f32_16x16x32_bf16 v[160:163], v[96:99], v[184:187], v[160:163]\n\t"
            "v_mfma_f32_16x16x32_bf16 v[160:163], v[100:103], v[188:191], v[160:163]\n\t"
            "v_mfma_f32_16x16x32_bf16 v[164:167], v[124:127], v[188:191], v[164:167]\n\t"
            "ds_read_b128 v[188:191], v243 offset:12544\n\t"
            "v_mfma_f32_16x16x32_bf16 v[164:167], v[120:123], v[184:187], v[164:167]\n\t"
            "ds_read_b128 v[184:187], v242 offset:12544\n\t"
            "s_waitcnt lgkmcnt(2)\n\t"
            "v_mfma_f32_16x16x32_bf16 v[144:147], v[8:11], v[192:195], v[144:147]\n\t"
            "v_mfma_f32_16x16x32_bf16 v[144:147], v[12:15], v[172:175], v[144:147]\n\t"
            "v_mfma_f32_16x16x32_bf16 v[148:151], v[36:39], v[172:175], v[148:151]\n\t"
            "v_mfma_f32_16x16x32_bf16 v[148:151], v[32:35], v[192:195], v[148:151]\n\t"
            "v_mfma_f32_16x16x32_bf16 v[152:155], v[56:59], v[192:195], v[152:155]\n\t"
            "v_mfma_f32_16x16x32_bf16 v[152:155], v[60:63], v[172:175], v[152:155]\n\t"
            "v_mfma_f32_16x16x32_bf16 v[156:159], v[84:87], v[172:175], v[156:159]\n\t"
            "v_mfma_f32_16x16x32_bf16 v[156:159], v[80:83], v[192:195], v[156:159]\n\t"
            "s_waitcnt lgkmcnt(0)\n\t"
            "v_mfma_f32_16x16x32_bf16 v[144:147], v[16:19], v[184:187], v[144:147]\n\t"
            "v_mfma_f32_16x16x32_bf16 v[144:147], v[20:23], v[188:191], v[144:147]\n\t"
            "v_mfma_f32_16x16x32_bf16 v[148:151], v[44:47], v[188:191], v[148:151]\n\t"
            "v_mfma_f32_16x16x32_bf16 v[148:151], v[40:43], v[184:187], v[148:151]\n\t"
            "v_mfma_f32_16x16x32_bf16 v[160:163], v[104:107], v[192:195], v[160:163]\n\t"
            "v_mfma_f32_16x16x32_bf16 v[160:163], v[108:111], v[172:175], v[160:163]\n\t"
            "v_mfma_f32_16x16x32_bf16 v[164:167], v[132:135], v[172:175], v[164:167]\n\t"
            "v_mfma_f32_16x16x32_bf16 v[164:167], v[128:131], v[192:195], v[164:167]\n\t"
            "s_nop 1\n\t"
            "v_cvt_pk_bf16_f32 v144, v144, v145\n\t"
            "v_mfma_f32_16x16x32_bf16 v[152:155], v[64:67], v[184:187], v[152:155]\n\t"
            "v_cvt_pk_bf16_f32 v145, v146, v147\n\t"
            "v_mfma_f32_16x16x32_bf16 v[152:155], v[68:71], v[188:191], v[152:155]\n\t"
            "v_cvt_pk_bf16_f32 v146, v148, v149\n\t"
            "v_cvt_pk_bf16_f32 v147, v150, v151\n\t"
            "v_mfma_f32_16x16x32_bf16 v[156:159], v[92:95], v[188:191], v[156:159]\n\t"
            "v_mfma_f32_16x16x32_bf16 v[156:159], v[88:91], v[184:187], v[156:159]\n\t"
            "buffer_store_dwordx4 v[144:147], v198, s[40:43], s45 offen sc1\n\t"
            "v_mfma_f32_16x16x32_bf16 v[160:163], v[112:115], v[184:187], v[160:163]\n\t"
            "s_nop 1\n\t"
            "v_cvt_pk_bf16_f32 v152, v152, v153\n\t"
            "v_mfma_f32_16x16x32_bf16 v[160:163], v[116:119], v[188:191], v[160:163]\n\t"
            "v_cvt_pk_bf16_f32 v153, v154, v155\n\t"
            "v_mfma_f32_16x16x32_bf16 v[164:167], v[140:143], v[188:191], v[164:167]\n\t"
            "v_cvt_pk_bf16_f32 v154, v156, v157\n\t"
            "v_mfma_f32_16x16x32_bf16 v[164:167], v[136:139], v[184:187], v[164:167]\n\t"
            "v_cvt_pk_bf16_f32 v155, v158, v159\n\t"
            "buffer_store_dwordx4 v[152:155], v198, s[40:43], s45 offen offset:64 sc1\n\t"
            "s_nop 1\n\t"
            "v_cvt_pk_bf16_f32 v160, v160, v161\n\t"
            "v_cvt_pk_bf16_f32 v161, v162, v163\n\t"
            "s_nop 1\n\t"
            "v_cvt_pk_bf16_f32 v162, v164, v165\n\t"
            "v_cvt_pk_bf16_f32 v163, v166, v167\n\t"
            "buffer_store_dwordx4 v[160:163], v198, s[40:43], s45 offen offset:128 sc1\n\t"
            "s_nop 7\n\t"
            "s_branch .Lwk_pe%=\n\t"
            "wk_conv:\n\t"
            "s_nop 7\n\t"
            "ds_read_b128 v[168:171], v254\n\t"
            "ds_read_b64 v[144:145], v253\n\t"
            "ds_read_b64 v[146:147], v253 offset:96\n\t"
            "ds_read_b64 v[148:149], v253 offset:1744\n\t"
            "ds_read_b64 v[150:151], v253 offset:1840\n\t"
            "ds_read_b128 v[172:175], v254 offset:64\n\t"
            "ds_read_b64 v[152:153], v253 offset:8\n\t"
            "ds_read_b64 v[154:155], v253 offset:104\n\t"
            "ds_read_b64 v[156:157], v253 offset:1752\n\t"
            "ds_read_b64 v[158:159], v253 offset:1848\n\t"
            "ds_read_b64 v[160:161], v253 offset:16\n\t"
            "ds_read_b64 v[162:163], v253 offset:112\n\t"
            "ds_read_b64 v[164:165], v253 offset:1760\n\t"
            "ds_read_b64 v[166:167], v253 offset:1856\n\t"
            "v_mov_b64 v[176:177], 0\n\t"
            "v_mov_b64 v[178:179], 0\n\t"
            "v_mov_b64 v[180:181], 0\n\t"
            "v_mov_b64 v[182:183], 0\n\t"
            "v_mov_b64 v[184:185], 0\n\t"
            "v_mov_b64 v[186:187], 0\n\t"
            "v_mov_b64 v[188:189], 0\n\t"
            "v_mov_b64 v[190:191], 0\n\t"
            "v_add_u32 v236, s67, v250\n\t"
            "v_xor_b32 v237, 16, v236\n\t"
            "v_add_u32 v248, s67, v251\n\t"
            "s_waitcnt lgkmcnt(9)\n\t"
            "v_smfmac_f32_16x16x64_bf16 v[176:179], v[168:171], v[144:151], v255\n\t"
            "ds_read_b128 v[168:171], v254 offset:128\n\t"
            "ds_read_b64 v[144:145], v253 offset:24\n\t"
            "s_waitcnt lgkmcnt(6)\n\t"
            "v_smfmac_f32_16x16x64_bf16 v[180:183], v[172:175], v[152:159], v255\n\t"
            "ds_read_b64 v[146:147], v253 offset:120\n\t"
            "ds_read_b64 v[148:149], v253 offset:1768\n\t"
            "ds_read_b64 v[150:151], v253 offset:1864\n\t"
            "ds_read_b128 v[172:175], v254 offset:192\n\t"
            "ds_read_b64 v[152:153], v253 offset:32\n\t"
            "ds_read_b64 v[154:155], v253 offset:128\n\t"
            "ds_read_b64 v[156:157], v253 offset:1776\n\t"
            "ds_read_b64 v[158:159], v253 offset:1872\n\t"
            "v_cvt_pk_bf16_f32 v176, v176, v177\n\t"
            "v_cvt_pk_bf16_f32 v177, v178, v179\n\t"
            "v_cvt_pk_bf16_f32 v178, v180, v181\n\t"
            "v_cvt_pk_bf16_f32 v179, v182, v183\n\t"
            "v_mfma_f32_4x4x4_16b_bf16 v[196:199], v[176:177], v[176:177], 0\n\t"
            "ds_write_b128 v236, v[176:179]\n\t"
            "v_mfma_f32_16x16x32_bf16 v[192:195], v[238:241], v[176:179], 0\n\t"
            "v_mov_b64 v[176:177], 0\n\t"
            "v_mov_b64 v[180:181], 0\n\t"
            "v_mfma_f32_4x4x4_16b_bf16 v[196:199], v[178:179], v[178:179], v[196:199]\n\t"
            "v_mov_b64 v[178:179], 0\n\t"
            "v_mov_b64 v[182:183], 0\n\t"
            "s_waitcnt lgkmcnt(10)\n\t"
            "v_smfmac_f32_16x16x64_bf16 v[184:187], v[168:171], v[160:167], v255\n\t"
            "ds_read_b128 v[168:171], v254 offset:256\n\t"
            "ds_read_b64 v[160:161], v253 offset:40\n\t"
            "ds_read_b64 v[162:163], v253 offset:136\n\t"
            "ds_read_b64 v[164:165], v253 offset:1784\n\t"
            "ds_read_b64 v[166:167], v253 offset:1880\n\t"
            "s_waitcnt lgkmcnt(10)\n\t"
            "v_smfmac_f32_16x16x64_bf16 v[188:191], v[172:175], v[144:151], v255\n\t"
            "ds_read_b128 v[172:175], v254 offset:320\n\t"
            "ds_read_b64 v[144:145], v253 offset:48\n\t"
            "ds_read_b64 v[146:147], v253 offset:144\n\t"
            "s_cbranch_scc1 .Lwk_np1%=\n\t"
            "s_mov_b32 exec_hi, 15\n\t"
            "buffer_load_dwordx4 v252, s[84:87], s89 offen sc1 lds\n\t"
            "s_mov_b32 exec_hi, -1\n\t"
            ".Lwk_np1%=:\n\t"
            "ds_read_b64 v[148:149], v253 offset:1792\n\t"
            "ds_read_b64 v[150:151], v253 offset:1888\n\t"
            "v_cvt_pk_bf16_f32 v184, v184, v185\n\t"
            "v_cvt_pk_bf16_f32 v185, v186, v187\n\t"
            "s_nop 0\n\t"
            "v_cvt_pk_bf16_f32 v186, v188, v189\n\t"
            "v_cvt_pk_bf16_f32 v187, v190, v191\n\t"
            "v_mfma_f32_4x4x4_16b_bf16 v[196:199], v[184:185], v[184:185], v[196:199]\n\t"
            "s_waitcnt lgkmcnt(14)\n\t"
            "ds_write_b128 v237, v[184:187]\n\t"
            "v_mfma_f32_16x16x32_bf16 v[192:195], v[238:241], v[184:187], v[192:195]\n\t"
            "v_mov_b64 v[184:185], 0\n\t"
            "v_mov_b64 v[188:189], 0\n\t"
            "v_mfma_f32_4x4x4_16b_bf16 v[196:199], v[186:187], v[186:187], v[196:199]\n\t"
            "v_mov_b64 v[186:187], 0\n\t"
            "v_mov_b64 v[190:191], 0\n\t"
            "s_waitcnt lgkmcnt(10)\n\t"
            "v_smfmac_f32_16x16x64_bf16 v[176:179], v[168:171], v[152:159], v255\n\t"
            "ds_read_b128 v[168:171], v254 offset:384\n\t"
            "ds_read_b64 v[152:153], v253 offset:56\n\t"
            "ds_read_b64 v[154:155], v253 offset:152\n\t"
            "ds_read_b64 v[156:157], v253 offset:1800\n\t"
            "ds_read_b64 v[158:159], v253 offset:1896\n\t"
            "s_waitcnt lgkmcnt(10)\n\t"
            "v_smfmac_f32_16x16x64_bf16 v[180:183], v[172:175], v[160:167], v255\n\t"
            "ds_read_b128 v[172:175], v254 offset:448\n\t"
            "ds_read_b64 v[160:161], v253 offset:64\n\t"
            "ds_read_b64 v[162:163], v253 offset:160\n\t"
            "ds_read_b64 v[164:165], v253 offset:1808\n\t"
            "ds_read_b64 v[166:167], v253 offset:1904\n\t"
            "v_cvt_pk_bf16_f32 v176, v176, v177\n\t"
            "v_cvt_pk_bf16_f32 v177, v178, v179\n\t"
            "s_nop 0\n\t"
            "v_cvt_pk_bf16_f32 v178, v180, v181\n\t"
            "v_cvt_pk_bf16_f32 v179, v182, v183\n\t"
            "v_mfma_f32_4x4x4_16b_bf16 v[196:199], v[176:177], v[176:177], v[196:199]\n\t"
            "s_waitcnt lgkmcnt(14)\n\t"
            "ds_write_b128 v236, v[176:179] offset:128\n\t"
            "v_mfma_f32_16x16x32_bf16 v[192:195], v[238:241], v[176:179], v[192:195]\n\t"
            "v_mov_b64 v[176:177], 0\n\t"
            "v_mov_b64 v[180:181], 0\n\t"
            "v_mfma_f32_4x4x4_16b_bf16 v[196:199], v[178:179], v[178:179], v[196:199]\n\t"
            "v_mov_b64 v[178:179], 0\n\t"
            "v_mov_b64 v[182:183], 0\n\t"
            "s_waitcnt lgkmcnt(10)\n\t"
            "v_smfmac_f32_16x16x64_bf16 v[184:187], v[168:171], v[144:151], v255\n\t"
            "ds_read_b128 v[168:171], v254 offset:512\n\t"
            "ds_read_b64 v[144:145], v253 offset:72\n\t"
            "ds_read_b64 v[146:147], v253 offset:168\n\t"
            "ds_read_b64 v[148:149], v253 offset:1816\n\t"
            "ds_read_b64 v[150:151], v253 offset:1912\n\t"
            "s_waitcnt lgkmcnt(10)\n\t"
            "v_smfmac_f32_16x16x64_bf16 v[188:191], v[172:175], v[152:159], v255\n\t"
            "ds_read_b128 v[172:175], v254 offset:576\n\t"
            "ds_read_b64 v[152:153], v253 offset:80\n\t"
            "ds_read_b64 v[154:155], v253 offset:176\n\t"
            "ds_read_b64 v[156:157], v253 offset:1824\n\t"
            "ds_read_b64 v[158:159], v253 offset:1920\n\t"
            "v_cvt_pk_bf16_f32 v184, v184, v185\n\t"
            "v_cvt_pk_bf16_f32 v185, v186, v187\n\t"
            "s_cbranch_scc1 .Lwk_np0%=\n\t"
            "s_mov_b32 exec_hi, 15\n\t"
            "buffer_load_dwordx4 v252, s[84:87], s89 offen sc1 lds\n\t"
            "s_mov_b32 exec_hi, -1\n\t"
            ".Lwk_np0%=:\n\t"
            "s_nop 0\n\t"
            "v_cvt_pk_bf16_f32 v186, v188, v189\n\t"
            "v_cvt_pk_bf16_f32 v187, v190, v191\n\t"
            "v_mfma_f32_4x4x4_16b_bf16 v[196:199], v[184:185], v[184:185], v[196:199]\n\t"
            "s_waitcnt lgkmcnt(14)\n\t"
            "ds_write_b128 v237, v[184:187] offset:128\n\t"
            "v_mfma_f32_16x16x32_bf16 v[192:195], v[238:241], v[184:187], v[192:195]\n\t"
            "v_mov_b64 v[184:185], 0\n\t"
            "v_mov_b64 v[188:189], 0\n\t"
            "v_mfma_f32_4x4x4_16b_bf16 v[196:199], v[186:187], v[186:187], v[196:199]\n\t"
            "v_mov_b64 v[186:187], 0\n\t"
            "v_mov_b64 v[190:191], 0\n\t"
            "s_waitcnt lgkmcnt(10)\n\t"
            "v_smfmac_f32_16x16x64_bf16 v[176:179], v[168:171], v[160:167], v255\n\t"
            "ds_read_b128 v[168:171], v254 offset:640\n\t"
            "ds_read_b64 v[160:161], v253 offset:88\n\t"
            "ds_read_b64 v[162:163], v253 offset:184\n\t"
            "ds_read_b64 v[164:165], v253 offset:1832\n\t"
            "ds_read_b64 v[166:167], v253 offset:1928\n\t"
            "s_waitcnt lgkmcnt(10)\n\t"
            "v_smfmac_f32_16x16x64_bf16 v[180:183], v[172:175], v[144:151], v255\n\t"
            "ds_read_b128 v[172:175], v254 offset:704\n\t"
            "v_cvt_pk_bf16_f32 v176, v176, v177\n\t"
            "v_cvt_pk_bf16_f32 v177, v178, v179\n\t"
            "s_nop 1\n\t"
            "v_mfma_f32_4x4x4_16b_bf16 v[196:199], v[176:177], v[176:177], v[196:199]\n\t"
            "s_nop 1\n\t"
            "v_cvt_pk_bf16_f32 v178, v180, v181\n\t"
            "v_cvt_pk_bf16_f32 v179, v182, v183\n\t"
            "s_nop 1\n\t"
            "v_mfma_f32_4x4x4_16b_bf16 v[196:199], v[178:179], v[178:179], v[196:199]\n\t"
            "ds_write_b128 v236, v[176:179] offset:256\n\t"
            "s_waitcnt lgkmcnt(6)\n\t"
            "v_smfmac_f32_16x16x64_bf16 v[184:187], v[168:171], v[152:159], v255\n\t"
            "v_mfma_f32_16x16x32_bf16 v[192:195], v[238:241], v[176:179], v[192:195]\n\t"
            "s_waitcnt lgkmcnt(1)\n\t"
            "v_smfmac_f32_16x16x64_bf16 v[188:191], v[172:175], v[160:167], v255\n\t"
            "s_nop 4\n\t"
            "v_cvt_pk_bf16_f32 v184, v184, v185\n\t"
            "v_cvt_pk_bf16_f32 v185, v186, v187\n\t"
            "s_nop 1\n\t"
            "v_mfma_f32_4x4x4_16b_bf16 v[196:199], v[184:185], v[184:185], v[196:199]\n\t"
            "v_cvt_pk_bf16_f32 v186, v188, v189\n\t"
            "v_cvt_pk_bf16_f32 v187, v190, v191\n\t"
            "s_nop 1\n\t"
            "v_mfma_f32_4x4x4_16b_bf16 v[196:199], v[186:187], v[186:187], v[196:199]\n\t"
            "ds_write_b128 v237, v[184:187] offset:256\n\t"
            "v_mfma_f32_16x16x32_bf16 v[192:195], v[238:241], v[184:187], v[192:195]\n\t"
            "s_nop 7\n\t"
            "v_cndmask_b32_e64 v193, v196, v197, s[60:61]\n\t"
            "v_cndmask_b32_e64 v193, v193, v198, s[62:63]\n\t"
            "v_cndmask_b32_e64 v193, v193, v199, s[64:65]\n\t"
            "ds_write_b64 v248, v[192:193] offset:18816\n\t"
            "s_waitcnt lgkmcnt(0)\n\t"
            "s_nop 7\n\t"
            "s_setpc_b64 vcc\n\t"
            ".Lwk_pe%=:\n\t"
            : B_SIO, "+{v247}"(l16_), "+{v228}"(q0), "+{v229}"(q1), "+{v232}"(q2), "+{v233}"(q3), "+{v234}"(q4)
            : "{v242}"(oE_), "{v243}"(oO_), "{v244}"(pR0_), "{v245}"(pR1_), "{v246}"(pR2_), "{v250}"(pXw_), "{v251}"(pSTw_), "{v252}"(aCA_), "{v253}"(aB_), "{v254}"(aA_), "{v255}"(idx_), B_SIN, "{s44}"(ARR_OFF + blockIdx.x * 8), "{s[80:81]}"(mrow), "{s82}"(wave == 6 && cnt == 21 ? REC_OFF + (start - 20 * ((blockIdx.x & 7) * 32 + xi)) * 1280 : 0)
            : "v144", "v145", "v146", "v147", "v148", "v149", "v150", "v151", "v152", "v153", "v154", "v155", "v156", "v157", "v158", "v159", "v160", "v161", "v162", "v163", "v164", "v165", "v166", "v167", "v168", "v169", "v170", "v171", "v172", "v173", "v174", "v175", "v176", "v177", "v178", "v179", "v180", "v181", "v182", "v183", "v184", "v185", "v186", "v187", "v188", "v189", "v190", "v191", "v192", "v193", "v194", "v195", "v196", "v197", "v198", "v199", "v236", "v237", "v248", "v249", "v0", "v1", "v2", "v3", "v4", "v5", "v6", "v7", "v8", "v9", "v10", "v11", "v12", "v13", "v14", "v15", "v16", "v17", "v18", "v19", "v20", "v21", "v22", "v23", "v24", "v25", "v26", "v27", "v28", "v29", "v30", "v31", "v32", "v33", "v34", "v35", "v36", "v37", "v38", "v39", "v40", "v41", "v42", "v43", "v44", "v45", "v46", "v47", "v48", "v49", "v50", "v51", "v52", "v53", "v54", "v55", "v56", "v57", "v58", "v59", "v60", "v61", "v62", "v63", "v64", "v65", "v66", "v67", "v68", "v69", "v70", "v71", "v72", "v73", "v74", "v75", "v76", "v77", "v78", "v79", "v80", "v81", "v82", "v83", "v84", "v85", "v86", "v87", "v88", "v89", "v90", "v91", "v92", "v93", "v94", "v95", "v96", "v97", "v98", "v99", "v100", "v101", "v102", "v103", "v104", "v105", "v106", "v107", "v108", "v109", "v110", "v111", "v112", "v113", "v114", "v115", "v116", "v117", "v118", "v119", "v120", "v121", "v122", "v123", "v124", "v125", "v126", "v127", "v128", "v129", "v130", "v131", "v132", "v133", "v134", "v135", "v136", "v137", "v138", "v139", "v140", "v141", "v142", "v143", "v200", "v201", "v202", "v204", "v205", "v206", "v207", "v208", "v209", "v210", "v211", "v212", "v213", "v214", "v215", "v216", "v217", "v218", "v219", "v220", "v221", "v222", "v223", "v224", "v225", "v226", "v227", "v230", "v231", "v235", B_SCLOB);
        }
        __builtin_amdgcn_endpgm();
    }
}

extern "C" void kernel_launch(void* const* d_in, const int* in_sizes, int n_in,
                              void* d_out, int out_size, void* d_ws, size_t ws_size,
                              hipStream_t stream) {
    const unsigned short* x = (const unsigned short*)d_in[0];
    const unsigned short* dw = (const unsigned short*)d_in[1];
    const unsigned short* wqkv = (const unsigned short*)d_in[2];
    const unsigned short* wo = (const unsigned short*)d_in[3];
    unsigned short* out = (unsigned short*)d_out;
    u32x4* wf = (u32x4*)d_ws;
    win_attn_kernel<<<GRID, 512, 0, stream>>>(x, dw, wqkv, wo, wf, out);
}
```
